```python
import torch

def _softmax(x, axis=-1):
    return torch.softmax(x, dim=axis)
def _gelu(x):
    return torch.nn.functional.gelu(x, approximate="tanh")
def _silu(x):
    return torch.nn.functional.silu(x)
def _sigmoid(x):
    return torch.sigmoid(x)
def _layernorm(x, axis=-1, eps=1e-5):
    m = x.mean(dim=axis, keepdim=True)
    v = x.var(dim=axis, keepdim=True, unbiased=False)
    return (x - m) / torch.sqrt(v + eps)
def _scatter_add(tgt, idx, src, axis=0):
    return tgt.clone().index_add_(axis, idx, src)
def _segment_sum(src, seg, n):
    out = torch.zeros((n,) + src.shape[1:], dtype=src.dtype)
    return out.index_add_(0, seg, src)
def _pad(x, pw):
    flat = tuple(v for pair in reversed(pw) for v in pair)
    return torch.nn.functional.pad(x, flat)

kernel_name = 'interleave_layers__softmax_full_mha__moe_arch_mod__prompt'

def setup_inputs():

    _w = (lambda a: (lambda t: t.to(torch.bfloat16) if t.is_floating_point() else t)(torch.as_tensor(a)))
    def _A_setup():
        B, S, D = 1, 2048, 1024
        return {
            "x": _w((lambda *s: torch.randn(*s, dtype=torch.bfloat16) * 0.05)(B, S, D)),
            "wqkv": _w((lambda *s: torch.randn(*s, dtype=torch.bfloat16) * 0.05)(D, 3 * D)),
            "wo": _w((lambda *s: torch.randn(*s, dtype=torch.bfloat16) * 0.05)(D, D)),
        }
    _A_in = _A_setup()

    def _B_setup():
        B, S, D, F, C = 1, 2048, 1024, 3584, 1
        return {
            "x": _w((lambda *s: torch.randn(*s, dtype=torch.bfloat16) * 0.05)(B, S, D)),
            "w_r": _w((lambda *s: torch.randn(*s, dtype=torch.bfloat16) * 0.05)(D)),
            "w_gate": _w((lambda *s: torch.randn(*s, dtype=torch.bfloat16) * 0.05)(D, F)),
            "w_up": _w((lambda *s: torch.randn(*s, dtype=torch.bfloat16) * 0.05)(D, F)),
            "w_down": _w((lambda *s: torch.randn(*s, dtype=torch.bfloat16) * 0.05)(F, D)),
        }
    _B_in = _B_setup()

    return {
        "A_x": _A_in["x"],
        "A_wqkv": _A_in["wqkv"],
        "A_wo": _A_in["wo"],
        "B_x": _B_in["x"],
        "B_w_r": _B_in["w_r"],
        "B_w_gate": _B_in["w_gate"],
        "B_w_up": _B_in["w_up"],
        "B_w_down": _B_in["w_down"],
    }

def reference(A_x, A_wqkv, A_wo, B_x, B_w_r, B_w_gate, B_w_up, B_w_down):
    def _A_ref(x, wqkv, wo):
        B, S, D = x.shape
        H = 32; Dh = D // H
        qkv = (lambda x, *d: x.permute(*d))((x @ wqkv).reshape(B, S, 3, H, Dh), 2, 0, 3, 1, 4)
        q, k, v = qkv[0], qkv[1], qkv[2]
        scale = Dh ** -0.5
        scores = torch.einsum("bhqd,bhkd->bhqk", q, k) * scale
        p = _softmax(scores, axis=-1)
        o = torch.einsum("bhqk,bhkd->bhqd", p, v)
        return (lambda x, *d: x.permute(*d))(o, 0, 2, 1, 3).reshape(B, S, D) @ wo

    def _B_ref(x, w_r, w_gate, w_up, w_down):
        B, S, D = x.shape
        C = 1
        r = x @ w_r
        idx = torch.argsort(-r, axis=-1)[:, :C]
        idx_e = (lambda x, n, axis: torch.repeat_interleave(x, n, dim=axis))(idx.reshape(B, C, 1), D, axis=2)
        x_sel = (lambda x, i, axis: torch.take_along_dim(x, i, dim=axis))(x, idx_e, axis=1)
        g = _silu(x_sel @ w_gate)
        u = x_sel @ w_up
        y = (g * u) @ w_down
        r_sel = _sigmoid((lambda x, i, axis: torch.take_along_dim(x, i, dim=axis))(r, idx, axis=1))
        y = y * r_sel.reshape(B, C, 1)
        b_off = (torch.arange(B) * S).reshape(B, 1)
        flat_idx = (idx + b_off).reshape(B * C)
        zero = (x * 0.0).reshape(B * S, D)
        upd = _scatter_add(zero, flat_idx, y.reshape(B * C, D), axis=0)
        return x + upd.reshape(B, S, D)

    x = A_x
    x = x + _A_ref(_layernorm(x, axis=-1), A_wqkv, A_wo)
    x = x + _B_ref(_layernorm(x, axis=-1), B_w_r, B_w_gate, B_w_up, B_w_down)
    x = x + _A_ref(_layernorm(x, axis=-1), A_wqkv, A_wo)
    x = x + _B_ref(_layernorm(x, axis=-1), B_w_r, B_w_gate, B_w_up, B_w_down)
    return x
```

```cpp
#include <hip/hip_runtime.h>
#include <hip/hip_bf16.h>
#include <stdint.h>

typedef unsigned short u16;
typedef short s16x4 __attribute__((ext_vector_type(4)));
typedef short s16x8 __attribute__((ext_vector_type(8)));
typedef float f32x16 __attribute__((ext_vector_type(16)));
typedef float f32x8 __attribute__((ext_vector_type(8)));
typedef float f32x4 __attribute__((ext_vector_type(4)));
typedef float f32x2 __attribute__((ext_vector_type(2)));
typedef unsigned int u32x16 __attribute__((ext_vector_type(16)));
typedef unsigned int u32x8 __attribute__((ext_vector_type(8)));
typedef unsigned int u32x4 __attribute__((ext_vector_type(4)));
typedef unsigned int u32x2 __attribute__((ext_vector_type(2)));
typedef __bf16 bf16x2 __attribute__((ext_vector_type(2)));
typedef __bf16 bf16x8 __attribute__((ext_vector_type(8)));
typedef __bf16 bf16x16 __attribute__((ext_vector_type(16)));

#define DI __device__ __forceinline__
#define LDS_PTR(T, p) ((__attribute__((address_space(3))) T*)(p))
#define BARRIER() asm volatile("s_barrier" ::: "memory")
#define BAR_LGKM() asm volatile("s_waitcnt lgkmcnt(0)\n\ts_barrier" ::: "memory")
#define BAR_ALL() asm volatile("s_waitcnt vmcnt(0) lgkmcnt(0)\n\ts_barrier" ::: "memory")
#define DRAIN() asm volatile("s_waitcnt vmcnt(0)" ::: "memory")
#define PACE() ({ asm volatile("s_waitcnt vmcnt(3)" ::: "memory"); })
#define MFMA32(a, b, c) __builtin_amdgcn_mfma_f32_32x32x16_bf16(a, b, c, 0, 0, 0)
#define MFMA16(a, b, c) __builtin_amdgcn_mfma_f32_16x16x32_bf16(a, b, c, 0, 0, 0)
#define PIN(x) asm volatile("" : "+v"(x))
#define EX2(x) __builtin_amdgcn_exp2f(x)
#define RCP(x) __builtin_amdgcn_rcpf(x)
#define SBAR() __builtin_amdgcn_sched_barrier(0)

#define STAMPV(i)
#define STAMP_DECL
#define STAMP_OUTS
#define STAMP_PUT(i)
constexpr int D_ = 1024, F_ = 3584;

constexpr unsigned MBY = 1u << 20;
constexpr unsigned OFF_HF = 0, OFF_X1 = 4 * MBY;
constexpr unsigned OFF_KV = 8 * MBY;
constexpr unsigned OFF_OF = 24 * MBY;
constexpr unsigned OFF_FL = 32 * MBY;
constexpr unsigned FL_KV = OFF_FL;
constexpr unsigned FL_O = OFF_FL + 262144;
constexpr unsigned FL_COUNT = 4096;
constexpr unsigned OFF_CS = 33 * MBY;
constexpr unsigned OFF_SX = 34 * MBY;
constexpr unsigned SX_ST = 0;
constexpr unsigned SX_ROW = 512 * 1024;
constexpr unsigned SX_CAND = SX_ROW + 32 * 1024;
constexpr unsigned SX_YP = SX_CAND + 32 * 1024;
constexpr unsigned SX_LAYER = SX_YP + 896 * 1024;
constexpr unsigned SX_QP = 2 * SX_LAYER;
constexpr unsigned SX_BYTES = SX_QP + 512 * 1024;
constexpr unsigned WS_BYTES = 40 * MBY, OOB = 0x80000000u;
constexpr unsigned SENT = 0xFFFFFFFFu, TAG = 1u;

constexpr int NS = 9, DD = 3, SLOTB = 12288;
constexpr int L_HB = 110592, L_XB = L_HB + 2048;
constexpr int L_XCH = 122880;
constexpr int L_CAND = L_XCH + 32768;
constexpr int L_AB = L_XCH + 34816;
constexpr int L_FULL = 159744, L_FREE = L_FULL + 512;
constexpr int L_CS = L_FREE + 1024, L_PATCH = L_CS + 768;
constexpr int L_GU = L_PATCH + 768, L_YS = L_GU + 256, L_X2 = L_YS + 256;
constexpr int L_X2F = L_X2 + 128, L_X2P = L_X2F + 128;
constexpr int L_W = L_X2P + 128;
constexpr int LDS_BYTES = 163840;
static_assert(NS * SLOTB <= L_HB && L_W + 64 <= LDS_BYTES && L_AB + 2048 <= L_FULL, "LDS map");

DI int fresh_lane() { int l; asm volatile("v_mbcnt_lo_u32_b32 %0, -1, 0\n\tv_mbcnt_hi_u32_b32 %0, -1, %0" : "=v"(l)); return l; }
#define LANE_VARS const int lane = fresh_lane(), r32 = lane & 31, hi = lane >> 5, tq = (lane & 15) >> 2, tp = lane & 3, tblk = (lane >> 4) & 1, \
  wrow = 4 * hi + tq, wsw = 4 * ((wrow >> 1) & 1), wch = 2 * tblk + (tp >> 1), c24 = r32 < 24 ? r32 : 23; \
  (void)r32; (void)hi; (void)wsw; (void)wch; (void)c24
DI int fresh_s(int v) { asm volatile("" : "+s"(v)); return v; }
#define PHASE_VARS const int tm = fresh_s(tm_), tn = fresh_s(tn_), wid = fresh_s(wid_), tile = tm * 4 + wid, hh = wid & 1, kh = wid >> 1; (void)tile; (void)hh; (void)kh; LANE_VARS
DI float bf2f(u16 v) { return __uint_as_float(((unsigned)v) << 16); }
DI unsigned pack2(float lo, float hi) { f32x2 v = {lo, hi}; return __builtin_bit_cast(unsigned, __builtin_convertvector(v, bf16x2)); }
DI float rbf(float f) { return __uint_as_float(pack2(f, 0.f) << 16); }
DI float lo16(unsigned u) { return __uint_as_float(u << 16); }
DI float hi16(unsigned u) { return __uint_as_float(u & 0xffff0000u); }
DI float opq(float r) { asm("" : "+v"(r)); return r; }
DI float fadd(float a, float b) { return opq(a + b); }
DI float fmul(float a, float b) { return opq(a * b); }
DI float ffma(float a, float b, float c) { return opq(__builtin_fmaf(a, b, c)); }
DI float U2F(unsigned u) { return __uint_as_float(u); }
DI float half_swap_sum(float v) {
  auto r = __builtin_amdgcn_permlane32_swap(__float_as_uint(v), __float_as_uint(v), false, false);
  return fadd(U2F(r[0]), U2F(r[1]));
}
DI float half_swap_max(float v) {
  auto r = __builtin_amdgcn_permlane32_swap(__float_as_uint(v), __float_as_uint(v), false, false);
  return fmaxf(__uint_as_float(r[0]), __uint_as_float(r[1]));
}
template <int CTRL> DI unsigned dpp(unsigned v) { return (unsigned)__builtin_amdgcn_update_dpp(0, (int)v, CTRL, 0xf, 0xf, false); }
DI float row_swap_sum(float v) {
  auto r = __builtin_amdgcn_permlane16_swap(__float_as_uint(v), __float_as_uint(v), false, false);
  return fadd(U2F(r[0]), U2F(r[1]));
}
DI float half_sum(float v) {
  v = fadd(v, U2F(dpp<0xB1>(__float_as_uint(v)))); v = fadd(v, U2F(dpp<0x4E>(__float_as_uint(v))));
  v = fadd(v, U2F(dpp<0x141>(__float_as_uint(v)))); v = fadd(v, U2F(dpp<0x140>(__float_as_uint(v))));
  return row_swap_sum(v);
}
DI float wave_sum(float v) { return half_swap_sum(half_sum(v)); }
DI unsigned half_max_u32(unsigned v) {
  v = max(v, dpp<0xB1>(v)); v = max(v, dpp<0x4E>(v)); v = max(v, dpp<0x141>(v)); v = max(v, dpp<0x140>(v));
  auto r = __builtin_amdgcn_permlane16_swap(v, v, false, false);
  return max(r[0], r[1]);
}
DI unsigned wave_max_u32(unsigned v) {
  v = half_max_u32(v);
  auto r = __builtin_amdgcn_permlane32_swap(v, v, false, false);
  return max(r[0], r[1]);
}
DI u32x4 fo_swap(u32x4 v) {
  auto a = __builtin_amdgcn_permlane32_swap(v[0], v[2], false, false);
  auto b = __builtin_amdgcn_permlane32_swap(v[1], v[3], false, false);
  u32x4 o; o[0] = a[0]; o[1] = b[0]; o[2] = a[1]; o[3] = b[1];
  return o;
}
DI void glds16(const void* gsrc, unsigned lds_dst) {
  unsigned keep;
  asm volatile("s_mov_b32 %0, m0\n\ts_mov_b32 m0, %2\n\ts_nop 0\n\tglobal_load_lds_dwordx4 %1, off\n\ts_mov_b32 m0, %0"
               : "=&s"(keep) : "v"(gsrc), "s"(lds_dst) : "memory");
}
#define GL_ "global_load_lds_dwordx4 "
DI void glds_v4(const void* sb, const unsigned (&v)[4], unsigned m0v) {
  unsigned keep;
  asm volatile("s_mov_b32 %0, m0\n\ts_mov_b32 m0, %6\n\ts_nop 0\n\t" GL_ "%1, %5\n\t" GL_ "%2, %5 offset:1024\n\t" GL_ "%3, %5 offset:2048\n\t"
               GL_ "%4, %5 offset:3072\n\ts_mov_b32 m0, %0" : "=&s"(keep) : "v"(v[0]), "v"(v[1]), "v"(v[2]), "v"(v[3]), "s"(sb), "s"(m0v) : "memory");
}
template <int TH> DI void glds_v4t(const void* sb, const unsigned (&v)[4], unsigned m0v) {
  unsigned keep;
  asm volatile("s_mov_b32 %0, m0\n\ts_mov_b32 m0, %6\n\ts_nop 0\n\t" GL_ "%1, %5\n\ts_waitcnt vmcnt(%7)\n\t" GL_ "%2, %5 offset:1024\n\ts_waitcnt vmcnt(%7)\n\t"
               GL_ "%3, %5 offset:2048\n\ts_waitcnt vmcnt(%7)\n\t" GL_ "%4, %5 offset:3072\n\ts_waitcnt vmcnt(%7)\n\ts_mov_b32 m0, %0"
               : "=&s"(keep) : "v"(v[0]), "v"(v[1]), "v"(v[2]), "v"(v[3]), "s"(sb), "s"(m0v), "n"(TH) : "memory");
}
DI void glds_v3(const void* sb, const unsigned (&v)[3], unsigned m0v) {
  unsigned keep;
  asm volatile("s_mov_b32 %0, m0\n\ts_mov_b32 m0, %5\n\ts_nop 0\n\t" GL_ "%1, %4\n\t" GL_ "%2, %4 offset:1024\n\t" GL_ "%3, %4 offset:2048\n\t"
               "s_mov_b32 m0, %0" : "=&s"(keep) : "v"(v[0]), "v"(v[1]), "v"(v[2]), "s"(sb), "s"(m0v) : "memory");
}
DI float dot2(unsigned a, unsigned b, float c) {
  return __builtin_amdgcn_fdot2_f32_bf16(__builtin_bit_cast(bf16x2, a), __builtin_bit_cast(bf16x2, b), c, false);
}
#define CODE_ADDR(sym, out) do { unsigned long long pc_; unsigned off_; \
  asm volatile("s_getpc_b64 %0\n1:\n\ts_mov_b32 %1, " #sym "-1b" : "=s"(pc_), "=s"(off_)); out = (const char*)(pc_ + (long long)(int)off_); } while (0)
DI void st16(void* p, u32x4 v) { asm volatile("global_store_dwordx4 %0, %1, off sc0 sc1\n\ts_nop 1" :: "v"(p), "v"(v) : "memory"); }
DI void ln_scalars(float mean, float var, float& mb, float& den) { mb = rbf(mean); den = rbf(__builtin_amdgcn_sqrtf(rbf(rbf(var) + 1e-5f))); }
DI unsigned ln_pair(unsigned v, float mb, float rd) {
  const unsigned c = pack2(lo16(v) - mb, hi16(v) - mb);
  return pack2(lo16(c) * rd, hi16(c) * rd);
}
DI unsigned ln_pair_s(unsigned v, float nmb, float rd) {
  const unsigned c = pack2(fadd(lo16(v), nmb), fadd(hi16(v), nmb));
  return pack2(fmul(lo16(c), rd), fmul(hi16(c), rd));
}
template <int N, int M> DI f32x4 sum4(const u32x4 (&v)[M]) {
  f32x4 t;
#pragma unroll
  for (int e = 0; e < 4; ++e) {
    t[e] = U2F(v[0][e]);
#pragma unroll
    for (int i = 1; i < N; ++i) t[e] = fadd(t[e], U2F(v[i][e]));
  }
  return t;
}
#define PERM_LO(x, y) __builtin_amdgcn_perm(y, x, 0x05040100u)
#define PERM_HI(x, y) __builtin_amdgcn_perm(y, x, 0x07060302u)
constexpr unsigned SPIN_CAP = 1u << 15;
DI unsigned nz(unsigned v) { return min(v, SENT - 1u); }
DI unsigned nzf(float v) { return nz(__float_as_uint(v)); }
DI unsigned mx4(unsigned m, const u32x4& v) { return max(max(max(m, v[0]), v[1]), max(v[2], v[3])); }
template <int N> DI bool none_sent(const u32x4 (&v)[N]) {
  unsigned m = 0u;
#pragma unroll
  for (int i = 0; i < N; ++i) m = mx4(m, v[i]);
  return m != SENT;
}
typedef __amdgpu_buffer_rsrc_t rsrc_t;
DI u32x4 ldg(rsrc_t rs, unsigned off) { return __builtin_amdgcn_raw_buffer_load_b128(rs, (int)off, 0, 16); }
DI void stg(rsrc_t rs, unsigned off, u32x4 v) { __builtin_amdgcn_raw_buffer_store_b128(v, rs, (int)off, 0, 16); }
template <int N, int SLEEP = 1, class LD, class OKF> DI void poll_all(u32x4 (&out)[N], LD ld, OKF okf) {
  for (unsigned spins = 0;; ++spins) {
    ld(out);
    SBAR();
    if (__all(okf(out)) || spins > SPIN_CAP) break;
    asm volatile("s_sleep %0" :: "n"(SLEEP) : "memory");
  }
}
DI void lds_set(unsigned addr, unsigned v) { asm volatile("s_waitcnt lgkmcnt(0)\n\tds_write_b32 %0, %1" :: "v"(addr), "v"(v) : "memory"); }
template <int SLEEP = 1> DI void lds_wait_ge(unsigned addr, unsigned need) {
  unsigned spin = 0x100000, v;
  asm volatile("1:\n\tds_read_b32 %0, %2\n\ts_sub_u32 %1, %1, 1\n\ts_waitcnt lgkmcnt(0)\n\ts_cbranch_scc1 2f\n\tv_cmp_gt_u32 vcc, %3, %0\n\ts_cbranch_vccz 2f\n\ts_sleep %4\n\ts_branch 1b\n2:"
               : "=&v"(v), "+s"(spin) : "v"(addr), "s"(need), "n"(SLEEP) : "vcc", "scc", "memory");
}
DI s16x8 tr2(const char* p, int second) {
  s16x4 lo = __builtin_amdgcn_ds_read_tr16_b64_v4i16(LDS_PTR(s16x4, p)), hi = __builtin_amdgcn_ds_read_tr16_b64_v4i16(LDS_PTR(s16x4, p + second));
  return __builtin_shufflevector(lo, hi, 0, 1, 2, 3, 4, 5, 6, 7);
}
DI u32x16 cat4(const u32x4& a, const u32x4& b, const u32x4& c, const u32x4& d) {
  const u32x8 x = __builtin_shufflevector(a, b, 0, 1, 2, 3, 4, 5, 6, 7), y = __builtin_shufflevector(c, d, 0, 1, 2, 3, 4, 5, 6, 7);
  return __builtin_shufflevector(x, y, 0, 1, 2, 3, 4, 5, 6, 7, 8, 9, 10, 11, 12, 13, 14, 15);
}

DI void row_stats_sum(rsrc_t rs, unsigned sx, int tile, int tn, int lane, float s, float sq, float d, float& S, float& Q, float& Dd) {
  const unsigned exb = sx + SX_ST + tile * 8192 + (lane & 31) * 16;
  u32x4 o = {nzf(s), nzf(sq), nzf(d), 0u};
  stg(rs, lane < 32 ? exb + tn * 512 : OOB, o);
  __builtin_amdgcn_s_sleep(10);
  u32x4 pv[8];
  poll_all<8>(pv, [&](u32x4 (&v)[8]) {
#pragma unroll
    for (int i = 0; i < 8; ++i) v[i] = ldg(rs, exb + ((lane >> 5) * 8 + i) * 512);
  }, [&](const u32x4 (&v)[8]) { return none_sent(v); });
  S = 0.f; Q = 0.f; Dd = 0.f;
#pragma unroll
  for (int i = 0; i < 8; ++i) { S += __uint_as_float(pv[i][0]); Q += __uint_as_float(pv[i][1]); Dd += __uint_as_float(pv[i][2]); }
}
DI void row_stats_fin(char* smem, int epoch, int tile, int lane, float S, float Q, float Dd, float wsum, const u32x4 (&x1p)[4],
                      float& mean, float& var, float& mb, float& rd) {
  S = half_swap_sum(S); Q = half_swap_sum(Q); Dd = half_swap_sum(Dd);
  mean = S * (1.0f / 1024.0f); var = fmaxf(Q * (1.0f / 1024.0f) - mean * mean, 0.f);
  float den;
  ln_scalars(mean, var, mb, den);
  rd = RCP(den);
  const unsigned rb = pack2((Dd - mb * wsum) * rd, 0.f) & 0xffffu;
  const unsigned mine = (((rb & 0x8000u) ? (~rb & 0xffffu) : (rb | 0x8000u)) << 16) | (unsigned)(2047 - (tile * 32 + (lane & 31)));
  const bool best = half_max_u32(mine) == mine;
  if (best) {
    char* c = smem + L_CAND + (tile & 3) * 144;
#pragma unroll
    for (int i = 0; i < 4; ++i) *reinterpret_cast<u32x4*>(c + (2 * i + (lane >> 5)) * 16) = x1p[i];
    *reinterpret_cast<u32x4*>(c + 128) = u32x4{mine, __float_as_uint(mb), __float_as_uint(rd), 0u};
  }
  lds_set((unsigned)(uintptr_t)smem + L_W + 32 + (tile & 3) * 4, epoch);
}

DI void attn_safe(rsrc_t rs, unsigned kvoff, const u32x8& q, const u32x4& ones, f32x16& o, float& lsum, float& m) {
  const s16x8 q0 = __builtin_bit_cast(s16x8, __builtin_shufflevector(q, q, 0, 1, 2, 3)), q1 = __builtin_bit_cast(s16x8, __builtin_shufflevector(q, q, 4, 5, 6, 7));
  f32x4 l = {0.f, 0.f, 0.f, 0.f};
#pragma unroll
  for (int e = 0; e < 16; ++e) o[e] = 0.f;
  m = -INFINITY;
#pragma unroll 1
  for (int kt = 0; kt < 32; ++kt) {
    const unsigned p = kvoff + (kt >> 1) * 16384 + (kt & 1) * 4096;
    const u32x4 k0 = ldg(rs, p), k1 = ldg(rs, p + 1024), v0 = ldg(rs, p + 2048), v1 = ldg(rs, p + 3072);
    f32x16 s = {};
    s = MFMA32(__builtin_bit_cast(s16x8, k0), q0, s);
    s = MFMA32(__builtin_bit_cast(s16x8, k1), q1, s);
    float mt = fmaxf(s[0], s[1]);
#pragma unroll
    for (int e = 2; e < 16; ++e) mt = fmaxf(mt, s[e]);
    const float mn = fmaxf(m, half_swap_max(mt));
    const float alpha = EX2(m - mn);
    m = mn;
#pragma unroll
    for (int e = 0; e < 16; ++e) { s[e] = EX2(s[e] - mn); o[e] *= alpha; }
    l[0] *= alpha;
    const bf16x16 pa = __builtin_bit_cast(bf16x16, __builtin_shufflevector(s, s, 0, 1, 2, 3, 4, 5, 6, 7));
    const bf16x16 pb = __builtin_bit_cast(bf16x16, __builtin_shufflevector(s, s, 8, 9, 10, 11, 12, 13, 14, 15));
    o = __builtin_amdgcn_smfmac_f32_32x32x32_bf16(__builtin_bit_cast(bf16x8, v0), pa, o, 0xdddd, 0, 0);
    l = __builtin_amdgcn_smfmac_f32_16x16x64_bf16(__builtin_bit_cast(bf16x8, ones), pa, l, 0xdddd, 0, 0);
    o = __builtin_amdgcn_smfmac_f32_32x32x32_bf16(__builtin_bit_cast(bf16x8, v1), pb, o, 0xdddd, 0, 0);
    l = __builtin_amdgcn_smfmac_f32_16x16x64_bf16(__builtin_bit_cast(bf16x8, ones), pb, l, 0xdddd, 0, 0);
  }
  lsum = l[0];
}

extern "C" __global__ __launch_bounds__(512) void mega_kernel(const u16* __restrict__ x0, const u16* __restrict__ wqkv, const u16* __restrict__ wo,
                                                              const u16* __restrict__ w_r, const u16* __restrict__ wg, const u16* __restrict__ wu,
                                                              const u16* __restrict__ wd, char* ws, u16* __restrict__ out) {
  __shared__ __attribute__((aligned(16))) char smem[LDS_BYTES];
  const int wid = __builtin_amdgcn_readfirstlane(threadIdx.x >> 6);
  STAMPV(0);
  const int bid = blockIdx.x, xcd = bid & 7, loc = bid >> 3;
  const int tm = 8 * (xcd >> 2) + (loc >> 2), tn = 4 * (xcd & 3) + (loc & 3);
  const unsigned lbase = (unsigned)(uintptr_t)smem;
  const rsrc_t rs = __builtin_amdgcn_make_buffer_rsrc(ws, 0, (int)WS_BYTES, 0x00020000);
  volatile __attribute__((address_space(3))) unsigned* words = LDS_PTR(volatile unsigned, smem + L_W);
  if (wid >= 4) {
    auto wo_rows = [&](int g0, int g1, int gs) {
      const int lane = fresh_lane();
      unsigned wov[4];
#pragma unroll
      for (int i = 0; i < 4; ++i) wov[i] = (unsigned)((8 * i + (lane >> 3)) * (D_ * 2) + ((lane & 7) ^ (4 * ((lane >> 4) & 1))) * 16 - i * 1024);
#pragma unroll 1
      for (int g = g0; g < g1 && !0; g += gs) glds_v4t<3>(reinterpret_cast<const char*>(wo) + (size_t)((64 * tn + 32 * g) & 1023) * (D_ * 2) + tn * 128, wov, lbase + g * 4096);
    };
    auto mid_barriers = [&]() {
      BAR_ALL();
      if (__builtin_amdgcn_readfirstlane(words[0])) { BARRIER(); BARRIER(); }
      BARRIER();
    };
    if (wid < 6) {
      const int lw = wid - 4, lane = fresh_lane();
      __builtin_amdgcn_s_setprio(3);
      const char* wb = reinterpret_cast<const char*>(wqkv + (size_t)(16 * lw) * (3 * D_) + tn * 64);
      unsigned wv[3];
#pragma unroll
      for (int i = 0; i < 3; ++i) {
        const int li = lane + 64 * i, k = li / 24, cp = li % 24, c = cp ^ (4 * ((k >> 1) & 1));
        wv[i] = (unsigned)(k * (3 * D_ * 2) + (c >> 3) * (D_ * 2) + (c & 7) * 16 - i * 1024);
      }
      auto stage = [&](int s, int slot) {
        const char* p = wb + (size_t)((s + 2 * tn) & 31) * (32 * 3 * D_ * 2);
        glds_v3(p, wv, lbase + slot * SLOTB + lw * 6144);
        glds_v3(p + 8 * (3 * D_ * 2), wv, lbase + slot * SLOTB + lw * 6144 + 3072);
      };
      const unsigned fra = lbase + L_FREE + (lane & 3) * 256, fla = lbase + L_FULL + lw * 256 + lane * 4;
      const rsrc_t rw = __builtin_amdgcn_make_buffer_rsrc(const_cast<char*>(wb), 0, 0 ? 0 : 6 * MBY - (16 * lw * 3 * D_ + tn * 64) * 2, 0x00020000);
#pragma unroll 1
      for (int L = 0; L < 2; ++L) {
        const int s0 = L ? NS : 0, lead = L ? 8 : NS - 1;
        int slot = 0;
#pragma unroll 1
        for (int s = s0; s < 32; ++s) {
          if (s >= NS) lds_wait_ge(fra, 32 * L + s - lead);
          stage(s, slot);
          if (s == 0) BARRIER();
          slot = (slot == NS - 1) ? 0 : slot + 1;
          if (s >= s0 + DD) asm volatile("s_waitcnt vmcnt(%0)\n\tds_write_b32 %1, %2" :: "n"(DD * 6), "v"(fla), "v"(32 * L + s - DD + 1) : "memory");
        }
#pragma unroll
        for (int d = DD - 1; d >= 0; --d) asm volatile("s_waitcnt vmcnt(%0)\n\tds_write_b32 %1, %2" :: "n"(d * 6), "v"(fla), "v"(32 * L + 32 - d) : "memory");
        BAR_LGKM();
        lds_wait_ge<8>(fra, 32 * L + 32);
        __builtin_amdgcn_s_sleep(16);
        wo_rows(16 + lw, 30, 2);
        u32x4 pre[NS * 6];
        if (L == 0) {
#pragma unroll
          for (int s = 0; s < NS; ++s) {
#pragma unroll
            for (int i = 0; i < 6; ++i)
              pre[s * 6 + i] = __builtin_amdgcn_raw_buffer_load_b128(rw, wv[i % 3], ((s + 2 * tn) & 31) * (32 * 3 * D_ * 2) + (i / 3) * (8 * 3 * D_ * 2) + (i % 3) * 1024, 0), PACE();
          }
        } else {
#pragma unroll
          for (int i = 0; i < NS * 6; ++i) asm volatile("" : "=v"(pre[i]));
        }
        mid_barriers();
        wo_rows(30 + lw, 32, 2);
        BAR_ALL();
        if (L == 0) {
          char* d = smem + lw * 6144 + fresh_lane() * 16;
#pragma unroll
          for (int s = 0; s < NS; ++s) {
#pragma unroll
            for (int i = 0; i < 6; ++i) *reinterpret_cast<u32x4*>(d + s * SLOTB + i * 1024) = pre[s * 6 + i];
            lds_set(fla, 32 + s + 1);
          }
        }
      }
      return;
    }
    const int a = wid - 6;
    const bool ffn = loc < 28;
    const int fb = xcd * 28 + (ffn ? loc : 0);
    const int kr = loc, cg = xcd;
    const unsigned wa = lbase + L_W;
    BARRIER();
    s16x8 gf[16], uf[16];
#pragma unroll
    for (int i = 0; i < 16; ++i) asm volatile("" : "=v"(gf[i]), "=v"(uf[i]));
#pragma unroll 1
    for (int L = 0; L < 2; ++L) {
      const unsigned sx = OFF_SX + L * SX_LAYER;
      u32x4 wdf[16];
      if (L == 0) {
        LANE_VARS;
        const u32x4 sv = {SENT, SENT, SENT, SENT};
#pragma unroll
        for (int i = 0; i < 8; ++i) {
          const unsigned o = ((i * 256 + bid) * 128 + a * 64 + lane) * 16;
          stg(rs, o < SX_BYTES ? OFF_SX + o : OOB, sv);
        }
        const u16* p = wqkv + (size_t)(64 * tm + 32 * a + hi) * (3 * D_) + (c24 >> 3) * D_ + tn * 64 + (c24 & 7) * 8;
        float cs[8] = {0.f, 0.f, 0.f, 0.f, 0.f, 0.f, 0.f, 0.f};
        u32x4 v[16];
#pragma unroll
        for (int i = 0; i < 16; ++i) v[i] = *reinterpret_cast<const u32x4*>(p + (size_t)i * (2 * 3 * D_));
        SBAR();
#pragma unroll
        for (int i = 0; i < 16; ++i)
#pragma unroll
          for (int e = 0; e < 4; ++e) { cs[2 * e] = fadd(cs[2 * e], lo16(v[i][e])); cs[2 * e + 1] = fadd(cs[2 * e + 1], hi16(v[i][e])); }
        u32x4 o;
#pragma unroll
        for (int e = 0; e < 4; ++e) { const float x = half_swap_sum(cs[e]), y = half_swap_sum(cs[4 + e]); o[e] = __float_as_uint(hi ? y : x); }
        stg(rs, r32 < 24 ? OFF_CS + (tn * 32 + tm * 2 + a) * 768 + r32 * 32 + hi * 16 : OOB, o);
      }
      if (L >= 0) {
        BAR_ALL();
        LANE_VARS;
        unsigned gv[4];
#pragma unroll
        for (int i = 0; i < 4; ++i) { gv[i] = (unsigned)((32 * i + (lane >> 1)) * (F_ * 2) + (lane & 1) * 16 - i * 1024); PIN(gv[i]); }
        const rsrc_t rd = __builtin_amdgcn_make_buffer_rsrc(const_cast<u16*>(wd) + (size_t)fb * 16 * D_ + 512 * a, 0, 0 ? 0 : 32768, 0x00020000);
        unsigned l8 = 8 * tblk * (D_ * 2) + (lane & 15) * 8 + hi * 4, gp = (16 * (lane >> 5) + 4 * tblk + tq) * 32 + tp * 8 + a * 32768;
        PIN(l8); PIN(gp);
        lds_wait_ge<8>(lbase + L_FREE + (lane & 3) * 256, 32 * L + 32);
        __builtin_amdgcn_s_sleep(16);
#pragma unroll 1
        for (int i = 0; i < (ffn && L == 0 && !0 ? 8 : 0); ++i) {
          glds_v4t<1>(reinterpret_cast<const char*>(((i & 1) ? wu : wg) + (size_t)(512 * a + 128 * (i >> 1)) * F_ + fb * 16), gv, lbase + a * 32768 + (i & 1) * 16384 + (i >> 1) * 4096);
        }
        unsigned raw[64];
#pragma unroll
        for (int i = 0; i < 64; ++i) {
          raw[i] = __builtin_amdgcn_raw_buffer_load_b32(rd, l8 + (i >> 3) * 128, (i & 7) * 2048, 0);
          if ((i & 3) == 3) asm volatile("s_waitcnt vmcnt(8)" ::: "memory");
        }
        DRAIN();
        if (L == 0) {
#pragma unroll
          for (int t = 0; t < 16; ++t) { gf[t] = tr2(smem + gp + t * 1024, 256); uf[t] = tr2(smem + gp + 16384 + t * 1024, 256); }
#pragma unroll
          for (int t = 0; t < 16; ++t) { PIN(gf[t]); PIN(uf[t]); }
        }
        asm volatile("s_waitcnt lgkmcnt(0)" ::: "memory");
        wo_rows(8 * a, 8 * a + 8, 1);
        STAMPV(7 + 16 * L);
        mid_barriers();
#pragma unroll
        for (int i = 0; i < 64; ++i) PIN(raw[i]);
#pragma unroll
        for (int g = 0; g < 8; ++g) {
#pragma unroll
          for (int e = 0; e < 4; ++e) { wdf[2 * g][e] = PERM_LO(raw[8 * g + 2 * e], raw[8 * g + 2 * e + 1]); wdf[2 * g + 1][e] = PERM_HI(raw[8 * g + 2 * e], raw[8 * g + 2 * e + 1]); }
          PIN(wdf[2 * g]); PIN(wdf[2 * g + 1]);
        }
        if (L == 0) {
          LANE_VARS;
          f32x4 t[2];
#pragma unroll
          for (int c = 0; c < 2; ++c) {
            u32x4 v[8];
#pragma unroll
            for (int i = 0; i < 8; ++i) v[i] = ldg(rs, OFF_CS + (tn * 32 + 16 * c + 2 * i + hi) * 768 + a * 384 + c24 * 16);
            SBAR();
            t[c] = sum4<8>(v);
            SBAR();
          }
#pragma unroll
          for (int e = 0; e < 4; ++e) t[0][e] = half_swap_sum(fadd(t[0][e], t[1][e]));
          if (lane < 24) *reinterpret_cast<f32x4*>(smem + L_CS + a * 384 + lane * 16) = t[0];
        }
        BAR_LGKM();
      } else {
#pragma unroll
        for (int i = 0; i < 16; ++i) asm volatile("" : "=v"(wdf[i]));
      }
      const rsrc_t rs = __builtin_amdgcn_make_buffer_rsrc(ws, 0, L < 0 ? 0 : (int)WS_BYTES, 0x00020000);
      STAMPV(L < 0 ? 48 + 1 : 1 + 16 * L);
      LANE_VARS;
      unsigned cdst = lane < 8 ? sx + SX_ROW + tm * 2048 + tn * 128 + lane * 16 : sx + SX_CAND + (tn * 16 + tm) * 128;
      cdst = (a == 0 && lane < 9) ? cdst : OOB;
      PIN(cdst);
      lds_wait_ge(wa + 32 + 4 * tp, L + 1);
      unsigned key;
      {
        const unsigned k4 = *reinterpret_cast<const unsigned*>(smem + L_CAND + tp * 144 + 128);
        key = max(k4, dpp<0xB1>(k4)); key = max(key, dpp<0x4E>(key));
        const u32x4 c = *reinterpret_cast<const u32x4*>(smem + L_CAND + (((2047 - (int)(key & 2047u)) >> 5) & 3) * 144 + (lane < 8 ? lane : 8) * 16);
        const u32x4 o = {nz(c[0]), nz(c[1]), nz(c[2]), nz(c[3])};
        stg(rs, cdst, o);
        if (L < 0) key = 0u;
      }
      STAMPV(L < 0 ? 56 : 8 + 16 * L);
      __builtin_amdgcn_s_sleep(10);
      u32x4 v[2];
      poll_all<2>(v, [&](u32x4 (&t)[2]) {
        t[0] = ldg(rs, sx + SX_CAND + (tn * 16 + (lane & 15)) * 128);
        t[1] = ldg(rs, sx + SX_ROW + ((2047 - (int)(key & 2047u)) >> 7) * 2048 + a * 1024 + lane * 16);
        t[0][3] = key;
      }, [&](const u32x4 (&t)[2]) {
        const bool ok = mx4(0u, t[0]) != SENT;
        unsigned k = ok ? t[0][0] : 0u;
        k = max(k, dpp<0xB1>(k)); k = max(k, dpp<0x4E>(k)); k = max(k, dpp<0x141>(k)); k = max(k, dpp<0x140>(k));
        key = max(key, k);
        return ok & (mx4(0u, t[1]) != SENT) & (key == t[0][3]);
      });
      key = __builtin_amdgcn_readfirstlane(key);
      const int idx = 2047 - (int)(key & 2047u);
      const bool owner = (idx >> 7) == tm || L < 0;
      STAMPV(L < 0 ? 48 + 2 : 2 + 16 * L);
      {
        const float mb = __uint_as_float(__builtin_amdgcn_readlane(v[0][1], idx >> 7)), rd = __uint_as_float(__builtin_amdgcn_readlane(v[0][2], idx >> 7));
        u32x4 hb;
#pragma unroll
        for (int e = 0; e < 4; ++e) hb[e] = ln_pair_s(v[1][e], -mb, rd);
        *reinterpret_cast<u32x4*>(smem + L_HB + a * 1024 + lane * 16) = hb;
        *reinterpret_cast<u32x4*>(smem + L_XB + a * 1024 + lane * 16) = v[1];
        words[3] = (unsigned)idx;
        if (L == 0 && !owner) lds_set(wa + 4 + 4 * a, L + 1);
      }
      if (ffn) {
        f32x4 ga = {0.f, 0.f, 0.f, 0.f}, ua = {0.f, 0.f, 0.f, 0.f};
        const char* hp = smem + L_HB + a * 1024 + (lane >> 4) * 16;
        s16x8 hf[16];
#pragma unroll
        for (int t = 0; t < 6; ++t) hf[t] = *reinterpret_cast<const s16x8*>(hp + t * 64);
        __builtin_amdgcn_s_setprio(3);
#pragma unroll
        for (int t = 0; t < 16; ++t) {
          SBAR();
          ga = MFMA16(gf[t], hf[t], ga); ua = MFMA16(uf[t], hf[t], ua);
          SBAR();
          if (t + 6 < 16) hf[t + 6] = *reinterpret_cast<const s16x8*>(hp + (t + 6) * 64);
        }
        __builtin_amdgcn_s_setprio(0);
        if ((lane & 15) == 0) {
          *reinterpret_cast<f32x4*>(smem + L_GU + a * 128 + lane) = ga;
          *reinterpret_cast<f32x4*>(smem + L_GU + a * 128 + 64 + lane) = ua;
        }
        lds_set(wa + 16 + 4 * a, L + 1);
        lds_wait_ge(wa + 16 + 4 * (a ^ 1), L + 1);
        const float* gu = reinterpret_cast<const float*>(smem + L_GU) + (lane & 15);
        const float gv = rbf(fadd(gu[0], gu[32])), uv = rbf(fadd(gu[16], gu[48]));
        const float av = fmul(rbf(fmul(gv, RCP(fadd(1.0f, __expf(-gv))))), uv);
        const unsigned ap = pack2(av, U2F(dpp<0xB1>(__float_as_uint(av))));
        u32x4 af;
#pragma unroll
        for (int e = 0; e < 4; ++e) {
          const unsigned t = tblk ? (unsigned)__builtin_amdgcn_readlane(ap, 8 + 2 * e) : (unsigned)__builtin_amdgcn_readlane(ap, 2 * e);
          af[e] = ((lane >> 1) & 1) == hi ? t : 0u;
        }
        f32x4 yd[16];
        const f32x4 zero = {0.f, 0.f, 0.f, 0.f};
        SBAR();
        __builtin_amdgcn_s_setprio(3);
#pragma unroll
        for (int t = 0; t < 16; ++t) yd[t] = MFMA16(__builtin_bit_cast(s16x8, af), __builtin_bit_cast(s16x8, wdf[t]), zero);
        SBAR();
        __builtin_amdgcn_s_setprio(0);
#pragma unroll
        for (int c = 0; c < 2; ++c) {
          u32x4 o;
#pragma unroll
          for (int e = 0; e < 4; ++e) {
            const float x = tblk ? yd[8 * c + 2 + (e & 1)][e & 2] : yd[8 * c + (e & 1)][e & 2], y = tblk ? yd[8 * c + 6 + (e & 1)][e & 2] : yd[8 * c + 4 + (e & 1)][e & 2];
            o[e] = nzf(hi ? y : x);
          }
          stg(rs, sx + SX_YP + fb * 4096 + a * 2048 + c * 1024 + lane * 16, o);
        }
      }
      STAMPV(L < 0 ? 48 + 3 : 3 + 16 * L);
      if (L == 1) lds_set(wa + 4 + 4 * a, 2);
      if (L == 1 && cg != (kr & 7)) break;
      u32x4 wpf[12];
      if (L <= 0) {
        const u16* p = wqkv + (size_t)(32 * kr + 16 * hi + 4 * tblk) * (3 * D_) + (2 * cg + a) * 64 + (lane & 15) * 4;
        u32x2 v[24];
#pragma unroll
        for (int i = 0; i < 24; ++i) v[i] = *reinterpret_cast<const u32x2*>(p + (size_t)(8 * ((i >> 2) & 1) + (i & 3)) * (3 * D_) + (i >> 3) * D_);
        SBAR();
#pragma unroll
        for (int t = 0; t < 12; ++t) {
#pragma unroll
          for (int e = 0; e < 4; ++e) {
            const unsigned x = v[8 * (t >> 2) + 2 * e][(t >> 1) & 1], y = v[8 * (t >> 2) + 2 * e + 1][(t >> 1) & 1];
            wpf[t][e] = (t & 1) ? PERM_HI(x, y) : PERM_LO(x, y);
          }
          PIN(wpf[t]);
        }
      } else {
#pragma unroll
        for (int i = 0; i < 12; ++i) asm volatile("" : "=v"(wpf[i]));
        __builtin_amdgcn_s_sleep(10);
      }
      const unsigned rk = key >> 16;
      float rsig = rbf(RCP(fadd(1.0f, __expf(-lo16((rk & 0x8000u) ? (rk & 0x7fffu) : (~rk & 0xffffu))))));
      PIN(rsig);
      u32x4 pv[14];
      poll_all<14, 4>(pv, [&](u32x4 (&t)[14]) {
#pragma unroll
        for (int i = 0; i < 14; ++i) t[i] = ldg(rs, sx + SX_YP + (112 * a + 8 * i + (lane >> 3)) * 4096 + kr * 128 + (lane & 7) * 16);
      }, [&](const u32x4 (&t)[14]) { return none_sent(t); });
      STAMPV(L < 0 ? 48 + 4 : 4 + 16 * L);
      {
        f32x4 t = sum4<14>(pv);
#pragma unroll
        for (int e = 0; e < 4; ++e) {
          t[e] = fadd(t[e], U2F(dpp<0x128>(__float_as_uint(t[e]))));
          t[e] = half_swap_sum(row_swap_sum(t[e]));
        }
        if (lane < 8) *reinterpret_cast<f32x4*>(smem + L_YS + a * 128 + lane * 16) = t;
      }
      lds_set(wa + 24 + 4 * a, L + 1);
      lds_wait_ge(wa + 24 + 4 * (a ^ 1), L + 1);
      const float ys = fadd(reinterpret_cast<const float*>(smem + L_YS)[r32], reinterpret_cast<const float*>(smem + L_YS)[32 + r32]);
      const int n = kr * 32 + r32;
      const int fo = ((n >> 4) * 2 + ((n >> 2) & 1)) * 8 + ((n >> 3) & 1) * 4 + (n & 3);
      const float xv = bf2f(reinterpret_cast<const u16*>(smem + L_XB)[fo]), hv = bf2f(reinterpret_cast<const u16*>(smem + L_HB)[fo]);
      const unsigned ob = pack2(fadd(xv, rbf(fadd(hv, rbf(fmul(rbf(ys), rsig))))), 0.f) & 0xffffu;
      if (L == 1) {
        if (a == 0 && lane < 32) out[(size_t)idx * D_ + n] = (u16)ob;
        STAMPV(L < 0 ? 48 + 5 : 5 + 16 * L);
        break;
      }
      reinterpret_cast<u16*>(smem + L_X2F + a * 64)[fo & 31] = (u16)ob;
      asm volatile("s_waitcnt lgkmcnt(0)" ::: "memory");
      {
        const s16x8 xa = *reinterpret_cast<const s16x8*>(smem + L_X2F + a * 64 + (lane >> 4) * 16);
        const u32x4 xf = *reinterpret_cast<const u32x4*>(smem + L_X2F + a * 64 + tp * 16);
        f32x4 z[12];
        const f32x4 zero = {0.f, 0.f, 0.f, 0.f};
        SBAR();
        __builtin_amdgcn_s_setprio(3);
#pragma unroll
        for (int t = 0; t < 12; ++t) z[t] = MFMA16(xa, __builtin_bit_cast(s16x8, wpf[t]), zero);
        SBAR();
        __builtin_amdgcn_s_setprio(0);
        const float x2 = lo16(ob), s1 = half_sum(x2), s2 = half_sum(fmul(x2, x2));
        u32x4 o;
#pragma unroll
        for (int e = 0; e < 4; ++e) {
          const unsigned sp = lane < 52 ? (e == 0 ? __float_as_uint(s1) : __float_as_uint(s2)) : xf[e];
          const unsigned x = __float_as_uint(tblk ? z[4 + e][0] : z[e][0]), y = tblk ? sp : __float_as_uint(z[8 + e][0]);
          o[e] = nz(hi ? y : x);
        }
        const unsigned pos = lane < 24 ? lane * 16 : lane < 48 ? lane * 16 + 128 : lane < 52 ? (lane & 1) * 512 + 384 : 400 + tp * 16;
        stg(rs, lane < 56 ? OFF_SX + SX_QP + ((2 * cg + a) * 32 + kr) * 1024 + pos : OOB, o);
      }
      STAMPV(L < 0 ? 48 + 5 : 5 + 16 * L);
      if (owner) {
        __builtin_amdgcn_s_sleep(10);
        const int c25 = r32 < 25 ? r32 : 24;
        u32x4 qv[17];
        poll_all<17, 4>(qv, [&](u32x4 (&t)[17]) {
#pragma unroll
          for (int i = 0; i < 16; ++i) t[i] = ldg(rs, OFF_SX + SX_QP + (tn * 32 + 2 * i + hi) * 1024 + a * 512 + c25 * 16);
          t[16] = ldg(rs, OFF_SX + SX_QP + (tn * 34 + hi) * 1024 + 400 + tp * 16);
        }, [&](const u32x4 (&t)[17]) { return none_sent(t); });
        *reinterpret_cast<u32x4*>(smem + L_X2P + hi * 64 + tp * 16) = qv[16];
        f32x4 t = sum4<16>(qv);
#pragma unroll
        for (int e = 0; e < 4; ++e) t[e] = half_swap_sum(t[e]);
        const float mean = fmul(U2F(__builtin_amdgcn_readlane(__float_as_uint(t[0]), 24)), 1.0f / 1024.0f);
        const float var = fmaxf(ffma(-mean, mean, fmul(U2F(__builtin_amdgcn_readlane(__float_as_uint(t[1]), 24)), 1.0f / 1024.0f)), 0.f);
        float mb, den;
        ln_scalars(mean, var, mb, den);
        const float rd = RCP(den);
        if (lane < 24) {
          const f32x4 cs = *reinterpret_cast<const f32x4*>(smem + L_CS + a * 384 + lane * 16);
#pragma unroll
          for (int e = 0; e < 4; ++e) t[e] = fmul(ffma(-mb, cs[e], t[e]), rd);
          *reinterpret_cast<f32x4*>(smem + L_PATCH + a * 384 + lane * 16) = t;
        }
        lds_set(wa + 4 + 4 * a, L + 1);
      }
      STAMPV(L < 0 ? 48 + 6 : 6 + 16 * L);
    }
    return;
  }
  const int tm_ = tm, tn_ = tn, wid_ = wid;
  *reinterpret_cast<u32x2*>(smem + L_FULL + threadIdx.x * 8) = u32x2{0u, 0u};
  if (threadIdx.x < 16) words[threadIdx.x] = 0u;
  const u32x4 srd = {(unsigned)(uintptr_t)ws, (unsigned)((uintptr_t)ws >> 32) & 0xffffu, WS_BYTES, 0x00020000u};
  u32x4 x1p[4];
  float pS, pQ, pD, wsum;
  asm volatile("" : "=v"(x1p[0]), "=v"(x1p[1]), "=v"(x1p[2]), "=v"(x1p[3]), "=v"(pS), "=v"(pQ), "=v"(pD), "=v"(wsum));
#pragma unroll 1
  for (int L = 0; L < 2; ++L) {
    const unsigned sx = OFF_SX + L * SX_LAYER;
    u32x8 q[4];
    u32x16 kva, kvb;
    u32x4 res[4], wa, wb;
    u32x2 wrc[8];
    {
      f32x16 acc[6];
      u32x16 stA, stB, xr0, xr1, xr2, xr3;
      u32x4 stC;
      asm volatile("" : "={v[112:127]}"(stB), "={v[128:131]}"(stC));
      { PHASE_VARS;
      stB[8] = lane * 16 + (L ? OFF_X1 : OFF_HF) + tile * 65536;
      stB[9] = lbase + wrow * 384 + (((4 * hh + wch) ^ wsw) << 4) + 8 * (tp & 1);
      stB[10] = lbase + wrow * 384 + (((4 * (hh ^ 1) + wch) ^ wsw) << 4) + 8 * (tp & 1);
      stB[13] = lbase + L_FULL + (lane & 1) * 256;
      stB[15] = lbase + L_FREE + wid * 256 + lane * 4;
      stC[0] = 32 * L; }
      unsigned soff, slot, need = 32 * L + 1;
      if (L == 0) {
        soff = tn * 4096;
        asm volatile(
          "buffer_load_dwordx4 v[132:135], v120, s[40:43], s44 offen offset:0 sc1\n\t"
          "buffer_load_dwordx4 v[136:139], v120, s[40:43], s44 offen offset:1024 sc1\n\t"
          "buffer_load_dwordx4 v[140:143], v120, s[40:43], s44 offen offset:2048 sc1\n\t"
          "buffer_load_dwordx4 v[144:147], v120, s[40:43], s44 offen offset:3072 sc1\n\t"
          "s_add_u32 s44, s44, 0x1000\n\t"
          "s_and_b32 s44, s44, 0xffff\n\t"
          "buffer_load_dwordx4 v[148:151], v120, s[40:43], s44 offen offset:0 sc1\n\t"
          "buffer_load_dwordx4 v[152:155], v120, s[40:43], s44 offen offset:1024 sc1\n\t"
          "buffer_load_dwordx4 v[156:159], v120, s[40:43], s44 offen offset:2048 sc1\n\t"
          "buffer_load_dwordx4 v[160:163], v120, s[40:43], s44 offen offset:3072 sc1\n\t"
          "s_add_u32 s44, s44, 0x1000\n\t"
          "s_and_b32 s44, s44, 0xffff\n\t"
          "buffer_load_dwordx4 v[164:167], v120, s[40:43], s44 offen offset:0 sc1\n\t"
          "buffer_load_dwordx4 v[168:171], v120, s[40:43], s44 offen offset:1024 sc1\n\t"
          "buffer_load_dwordx4 v[172:175], v120, s[40:43], s44 offen offset:2048 sc1\n\t"
          "buffer_load_dwordx4 v[176:179], v120, s[40:43], s44 offen offset:3072 sc1\n\t"
          "s_add_u32 s44, s44, 0x1000\n\t"
          "s_and_b32 s44, s44, 0xffff\n\t"
          "buffer_load_dwordx4 v[180:183], v120, s[40:43], s44 offen offset:0 sc1\n\t"
          "buffer_load_dwordx4 v[184:187], v120, s[40:43], s44 offen offset:1024 sc1\n\t"
          "buffer_load_dwordx4 v[188:191], v120, s[40:43], s44 offen offset:2048 sc1\n\t"
          "buffer_load_dwordx4 v[192:195], v120, s[40:43], s44 offen offset:3072 sc1\n\t"
          : "={v[132:147]}"(xr0), "={v[148:163]}"(xr1), "={v[164:179]}"(xr2), "={v[180:195]}"(xr3), "+{s44}"(soff) : "{v[112:127]}"(stB), "{s[40:43]}"(srd) : "scc", "memory");
        BAR_LGKM();
      } else {
        xr0 = cat4(x1p[0], x1p[1], x1p[2], x1p[3]);
        asm volatile("" : "={v[148:163]}"(xr1), "={v[164:179]}"(xr2), "={v[180:195]}"(xr3), "={s44}"(soff));
      }
      STAMPV(1 + 16 * L);
      asm volatile(
        "v_mov_b32 v123, v121\n\t"
        "v_mov_b32 v124, v122\n\t"
        "s_mov_b64 vcc, -1\n\t"
        "s_cbranch_vccnz 10f\n\t"
        "11:\n\t"
        "s_mov_b32 s46, 12288\n\t"
        "s_add_u32 s47, s47, 1\n\t"
        "ds_read_b64_tr_b16 v[96:97], v123 offset:0\n\t"
        "ds_read_b64_tr_b16 v[98:99], v123 offset:3072\n\t"
        "ds_read_b64_tr_b16 v[100:101], v124 offset:0\n\t"
        "ds_read_b64_tr_b16 v[102:103], v124 offset:3072\n\t"
        "ds_read_b64_tr_b16 v[104:105], v123 offset:128\n\t"
        "ds_read_b64_tr_b16 v[106:107], v123 offset:3200\n\t"
        "ds_read_b64_tr_b16 v[108:109], v124 offset:128\n\t"
        "ds_read_b64_tr_b16 v[110:111], v124 offset:3200\n\t"
        "ds_read_b64_tr_b16 v[112:113], v123 offset:256\n\t"
        "ds_read_b64_tr_b16 v[114:115], v123 offset:3328\n\t"
        "ds_write_b32 v127, v128\n\t"
        "ds_read_b64_tr_b16 v[116:117], v124 offset:256\n\t"
        "ds_read_b64_tr_b16 v[118:119], v124 offset:3328\n\t"
        "s_waitcnt vmcnt(15) lgkmcnt(11)\n\t"
        "v_mfma_f32_32x32x16_bf16 v[0:15], v[96:99], v[132:135], 0\n\t"
        "ds_read_b32 v126, v125\n\t"
        "ds_read_b64_tr_b16 v[96:97], v123 offset:6144\n\t"
        "ds_read_b64_tr_b16 v[98:99], v123 offset:9216\n\t"
        "s_waitcnt lgkmcnt(12)\n\t"
        "v_mfma_f32_32x32x16_bf16 v[16:31], v[100:103], v[132:135], 0\n\t"
        "v_add_u32 v129, s46, v121\n\t"
        "ds_read_b64_tr_b16 v[100:101], v124 offset:6144\n\t"
        "ds_read_b64_tr_b16 v[102:103], v124 offset:9216\n\t"
        "s_waitcnt lgkmcnt(12)\n\t"
        "v_mfma_f32_32x32x16_bf16 v[32:47], v[104:107], v[132:135], 0\n\t"
        "ds_read_b64_tr_b16 v[104:105], v123 offset:6272\n\t"
        "ds_read_b64_tr_b16 v[106:107], v123 offset:9344\n\t"
        "s_waitcnt lgkmcnt(12)\n\t"
        "v_mfma_f32_32x32x16_bf16 v[48:63], v[108:111], v[132:135], 0\n\t"
        "v_add_u32 v130, s46, v122\n\t"
        "ds_read_b64_tr_b16 v[108:109], v124 offset:6272\n\t"
        "ds_read_b64_tr_b16 v[110:111], v124 offset:9344\n\t"
        "s_waitcnt lgkmcnt(12)\n\t"
        "v_mfma_f32_32x32x16_bf16 v[64:79], v[132:135], v[112:115], 0\n\t"
        "ds_read_b64_tr_b16 v[112:113], v123 offset:6400\n\t"
        "ds_read_b64_tr_b16 v[114:115], v123 offset:9472\n\t"
        "s_waitcnt lgkmcnt(10)\n\t"
        "v_mfma_f32_32x32x16_bf16 v[80:95], v[132:135], v[116:119], 0\n\t"
        "v_cmp_gt_u32 vcc, s47, v126\n\t"
        "ds_read_b64_tr_b16 v[116:117], v124 offset:6400\n\t"
        "ds_read_b64_tr_b16 v[118:119], v124 offset:9472\n\t"
        "s_waitcnt vmcnt(14) lgkmcnt(10)\n\t"
        "v_mfma_f32_32x32x16_bf16 v[0:15], v[96:99], v[136:139], v[0:15]\n\t"
        "s_cbranch_vccnz 12f\n\t"
        "13:\n\t"
        "ds_read_b64_tr_b16 v[96:97], v129 offset:0\n\t"
        "ds_read_b64_tr_b16 v[98:99], v129 offset:3072\n\t"
        "s_waitcnt lgkmcnt(10)\n\t"
        "v_mfma_f32_32x32x16_bf16 v[16:31], v[100:103], v[136:139], v[16:31]\n\t"
        "v_add_u32 v128, 1, v128\n\t"
        "ds_read_b64_tr_b16 v[100:101], v130 offset:0\n\t"
        "ds_read_b64_tr_b16 v[102:103], v130 offset:3072\n\t"
        "s_waitcnt lgkmcnt(10)\n\t"
        "v_mfma_f32_32x32x16_bf16 v[32:47], v[104:107], v[136:139], v[32:47]\n\t"
        "ds_read_b64_tr_b16 v[104:105], v129 offset:128\n\t"
        "ds_read_b64_tr_b16 v[106:107], v129 offset:3200\n\t"
        "s_waitcnt lgkmcnt(10)\n\t"
        "v_mfma_f32_32x32x16_bf16 v[48:63], v[108:111], v[136:139], v[48:63]\n\t"
        "s_add_u32 s46, s46, 12288\n\t"
        "s_add_u32 s47, s47, 1\n\t"
        "ds_read_b64_tr_b16 v[108:109], v130 offset:128\n\t"
        "ds_read_b64_tr_b16 v[110:111], v130 offset:3200\n\t"
        "s_waitcnt lgkmcnt(10)\n\t"
        "v_mfma_f32_32x32x16_bf16 v[64:79], v[136:139], v[112:115], v[64:79]\n\t"
        "s_cmp_eq_u32 s46, 110592\n\t"
        "s_cselect_b32 s46, 0, s46\n\t"
        "ds_read_b64_tr_b16 v[112:113], v129 offset:256\n\t"
        "ds_read_b64_tr_b16 v[114:115], v129 offset:3328\n\t"
        "s_waitcnt lgkmcnt(10)\n\t"
        "v_mfma_f32_32x32x16_bf16 v[80:95], v[136:139], v[116:119], v[80:95]\n\t"
        "ds_write_b32 v127, v128\n\t"
        "ds_read_b64_tr_b16 v[116:117], v130 offset:256\n\t"
        "ds_read_b64_tr_b16 v[118:119], v130 offset:3328\n\t"
        "s_waitcnt vmcnt(13) lgkmcnt(11)\n\t"
        "v_mfma_f32_32x32x16_bf16 v[0:15], v[96:99], v[140:143], v[0:15]\n\t"
        "ds_read_b32 v126, v125\n\t"
        "ds_read_b64_tr_b16 v[96:97], v129 offset:6144\n\t"
        "ds_read_b64_tr_b16 v[98:99], v129 offset:9216\n\t"
        "s_waitcnt lgkmcnt(12)\n\t"
        "v_mfma_f32_32x32x16_bf16 v[16:31], v[100:103], v[140:143], v[16:31]\n\t"
        "v_add_u32 v123, s46, v121\n\t"
        "ds_read_b64_tr_b16 v[100:101], v130 offset:6144\n\t"
        "ds_read_b64_tr_b16 v[102:103], v130 offset:9216\n\t"
        "s_waitcnt lgkmcnt(12)\n\t"
        "v_mfma_f32_32x32x16_bf16 v[32:47], v[104:107], v[140:143], v[32:47]\n\t"
        "ds_read_b64_tr_b16 v[104:105], v129 offset:6272\n\t"
        "ds_read_b64_tr_b16 v[106:107], v129 offset:9344\n\t"
        "s_waitcnt lgkmcnt(12)\n\t"
        "v_mfma_f32_32x32x16_bf16 v[48:63], v[108:111], v[140:143], v[48:63]\n\t"
        "v_add_u32 v124, s46, v122\n\t"
        "ds_read_b64_tr_b16 v[108:109], v130 offset:6272\n\t"
        "ds_read_b64_tr_b16 v[110:111], v130 offset:9344\n\t"
        "s_waitcnt lgkmcnt(12)\n\t"
        "v_mfma_f32_32x32x16_bf16 v[64:79], v[140:143], v[112:115], v[64:79]\n\t"
        "ds_read_b64_tr_b16 v[112:113], v129 offset:6400\n\t"
        "ds_read_b64_tr_b16 v[114:115], v129 offset:9472\n\t"
        "s_waitcnt lgkmcnt(10)\n\t"
        "v_mfma_f32_32x32x16_bf16 v[80:95], v[140:143], v[116:119], v[80:95]\n\t"
        "v_cmp_gt_u32 vcc, s47, v126\n\t"
        "ds_read_b64_tr_b16 v[116:117], v130 offset:6400\n\t"
        "ds_read_b64_tr_b16 v[118:119], v130 offset:9472\n\t"
        "s_waitcnt vmcnt(12) lgkmcnt(10)\n\t"
        "v_mfma_f32_32x32x16_bf16 v[0:15], v[96:99], v[144:147], v[0:15]\n\t"
        "s_cbranch_vccnz 14f\n\t"
        "15:\n\t"
        "ds_read_b64_tr_b16 v[96:97], v123 offset:0\n\t"
        "ds_read_b64_tr_b16 v[98:99], v123 offset:3072\n\t"
        "s_waitcnt lgkmcnt(10)\n\t"
        "v_mfma_f32_32x32x16_bf16 v[16:31], v[100:103], v[144:147], v[16:31]\n\t"
        "v_add_u32 v128, 1, v128\n\t"
        "ds_read_b64_tr_b16 v[100:101], v124 offset:0\n\t"
        "ds_read_b64_tr_b16 v[102:103], v124 offset:3072\n\t"
        "s_waitcnt lgkmcnt(10)\n\t"
        "v_mfma_f32_32x32x16_bf16 v[32:47], v[104:107], v[144:147], v[32:47]\n\t"
        "ds_read_b64_tr_b16 v[104:105], v123 offset:128\n\t"
        "ds_read_b64_tr_b16 v[106:107], v123 offset:3200\n\t"
        "s_waitcnt lgkmcnt(10)\n\t"
        "v_mfma_f32_32x32x16_bf16 v[48:63], v[108:111], v[144:147], v[48:63]\n\t"
        "s_add_u32 s46, s46, 12288\n\t"
        "s_add_u32 s47, s47, 1\n\t"
        "ds_read_b64_tr_b16 v[108:109], v124 offset:128\n\t"
        "ds_read_b64_tr_b16 v[110:111], v124 offset:3200\n\t"
        "s_waitcnt lgkmcnt(10)\n\t"
        "v_mfma_f32_32x32x16_bf16 v[64:79], v[144:147], v[112:115], v[64:79]\n\t"
        "s_cmp_eq_u32 s46, 110592\n\t"
        "s_cselect_b32 s46, 0, s46\n\t"
        "ds_read_b64_tr_b16 v[112:113], v123 offset:256\n\t"
        "ds_read_b64_tr_b16 v[114:115], v123 offset:3328\n\t"
        "s_waitcnt lgkmcnt(10)\n\t"
        "v_mfma_f32_32x32x16_bf16 v[80:95], v[144:147], v[116:119], v[80:95]\n\t"
        "ds_write_b32 v127, v128\n\t"
        "ds_read_b64_tr_b16 v[116:117], v124 offset:256\n\t"
        "ds_read_b64_tr_b16 v[118:119], v124 offset:3328\n\t"
        "s_waitcnt lgkmcnt(0)\n\t"
        "s_nop 7\n\t"
        "s_branch 9f\n\t"
        "10:\n\t"
        "s_mov_b32 s48, 0x10000\n\t"
        "8:\n\t"
        "ds_read_b32 v126, v125\n\t"
        "s_sub_u32 s48, s48, 1\n\t"
        "s_waitcnt lgkmcnt(0)\n\t"
        "s_cbranch_scc1 11b\n\t"
        "v_cmp_gt_u32 vcc, s47, v126\n\t"
        "s_cbranch_vccnz 8b\n\t"
        "s_branch 11b\n\t"
        "12:\n\t"
        "s_mov_b32 s48, 0x10000\n\t"
        "8:\n\t"
        "ds_read_b32 v126, v125\n\t"
        "s_sub_u32 s48, s48, 1\n\t"
        "s_waitcnt lgkmcnt(0)\n\t"
        "s_cbranch_scc1 13b\n\t"
        "v_cmp_gt_u32 vcc, s47, v126\n\t"
        "s_cbranch_vccnz 8b\n\t"
        "s_branch 13b\n\t"
        "14:\n\t"
        "s_mov_b32 s48, 0x10000\n\t"
        "8:\n\t"
        "ds_read_b32 v126, v125\n\t"
        "s_sub_u32 s48, s48, 1\n\t"
        "s_waitcnt lgkmcnt(0)\n\t"
        "s_cbranch_scc1 15b\n\t"
        "v_cmp_gt_u32 vcc, s47, v126\n\t"
        "s_cbranch_vccnz 8b\n\t"
        "s_branch 15b\n\t"
        "9:\n\t"
        : "={v[0:15]}"(acc[0]), "={v[16:31]}"(acc[1]), "={v[32:47]}"(acc[2]), "={v[48:63]}"(acc[3]), "={v[64:79]}"(acc[4]), "={v[80:95]}"(acc[5]),
          "={v[96:111]}"(stA), "+{v[112:127]}"(stB), "+{v[128:131]}"(stC), "={s46}"(slot), "+{s47}"(need)
        : "{v[132:147]}"(xr0) : "s48", "vcc", "scc", "memory");
      STAMPV(2 + 16 * L);
      float al = 1.f, be = 0.f;
      if (L == 1) {
        PHASE_VARS;
        DRAIN();
        float S, Q, Dd;
        row_stats_sum(rs, OFF_SX, tile, tn, lane, pS, pQ, pD, S, Q, Dd);
        soff = ((tn + 1) & 15) * 4096;
        asm volatile(
          "buffer_load_dwordx4 v[148:151], v120, s[40:43], s44 offen offset:0 sc1\n\t"
          "buffer_load_dwordx4 v[152:155], v120, s[40:43], s44 offen offset:1024 sc1\n\t"
          "buffer_load_dwordx4 v[156:159], v120, s[40:43], s44 offen offset:2048 sc1\n\t"
          "buffer_load_dwordx4 v[160:163], v120, s[40:43], s44 offen offset:3072 sc1\n\t"
          "s_add_u32 s44, s44, 0x1000\n\t"
          "s_and_b32 s44, s44, 0xffff\n\t"
          "buffer_load_dwordx4 v[164:167], v120, s[40:43], s44 offen offset:0 sc1\n\t"
          "buffer_load_dwordx4 v[168:171], v120, s[40:43], s44 offen offset:1024 sc1\n\t"
          "buffer_load_dwordx4 v[172:175], v120, s[40:43], s44 offen offset:2048 sc1\n\t"
          "buffer_load_dwordx4 v[176:179], v120, s[40:43], s44 offen offset:3072 sc1\n\t"
          "s_add_u32 s44, s44, 0x1000\n\t"
          "s_and_b32 s44, s44, 0xffff\n\t"
          "buffer_load_dwordx4 v[180:183], v120, s[40:43], s44 offen offset:0 sc1\n\t"
          "buffer_load_dwordx4 v[184:187], v120, s[40:43], s44 offen offset:1024 sc1\n\t"
          "buffer_load_dwordx4 v[188:191], v120, s[40:43], s44 offen offset:2048 sc1\n\t"
          "buffer_load_dwordx4 v[192:195], v120, s[40:43], s44 offen offset:3072 sc1\n\t"
          : "={v[148:163]}"(xr1), "={v[164:179]}"(xr2), "={v[180:195]}"(xr3), "+{s44}"(soff), "+v"(S), "+v"(Q), "+v"(Dd) : "{v[112:127]}"(stB), "{s[40:43]}"(srd) : "scc", "memory");
        float mean, var, mb1, rd1, mb2, den2;
        row_stats_fin(smem, 1, tile, lane, S, Q, Dd, wsum, x1p, mean, var, mb1, rd1);
        ln_scalars(mean + (mean - mb1) * rd1, var * (1.0f + rd1) * (1.0f + rd1), mb2, den2);
        const float rd2 = RCP(den2);
        al = (1.0f + rd1) * rd2; be = -(mb1 * rd1 + mb2) * rd2;
        reinterpret_cast<float*>(smem + L_AB)[wid * 64 + lane] = hi ? be : al;
        reinterpret_cast<float*>(smem + L_AB)[256 + wid * 64 + lane] = hi ? rd1 : mb1;
      }
      STAMPV(3 + 16 * L);
      asm volatile(
        "s_nop 0\n\t"
        "s_add_u32 s44, s44, 0x1000\n\t"
        "s_and_b32 s44, s44, 0xffff\n\t"
        "buffer_load_dwordx4 v[132:135], v120, s[40:43], s44 offen offset:0 sc1\n\t"
        "buffer_load_dwordx4 v[136:139], v120, s[40:43], s44 offen offset:1024 sc1\n\t"
        "buffer_load_dwordx4 v[140:143], v120, s[40:43], s44 offen offset:2048 sc1\n\t"
        "s_mov_b32 s45, 3\n\t"
        "1:\n\t"
        "s_waitcnt vmcnt(14) lgkmcnt(11)\n\t"
        "v_mfma_f32_32x32x16_bf16 v[0:15], v[96:99], v[148:151], v[0:15]\n\t"
        "ds_read_b32 v126, v125\n\t"
        "ds_read_b64_tr_b16 v[96:97], v123 offset:6144\n\t"
        "ds_read_b64_tr_b16 v[98:99], v123 offset:9216\n\t"
        "s_waitcnt lgkmcnt(12)\n\t"
        "v_mfma_f32_32x32x16_bf16 v[16:31], v[100:103], v[148:151], v[16:31]\n\t"
        "v_add_u32 v129, s46, v121\n\t"
        "ds_read_b64_tr_b16 v[100:101], v124 offset:6144\n\t"
        "ds_read_b64_tr_b16 v[102:103], v124 offset:9216\n\t"
        "s_waitcnt lgkmcnt(12)\n\t"
        "v_mfma_f32_32x32x16_bf16 v[32:47], v[104:107], v[148:151], v[32:47]\n\t"
        "ds_read_b64_tr_b16 v[104:105], v123 offset:6272\n\t"
        "ds_read_b64_tr_b16 v[106:107], v123 offset:9344\n\t"
        "buffer_load_dwordx4 v[144:147], v120, s[40:43], s44 offen offset:3072 sc1\n\t"
        "s_waitcnt lgkmcnt(12)\n\t"
        "v_mfma_f32_32x32x16_bf16 v[48:63], v[108:111], v[148:151], v[48:63]\n\t"
        "v_add_u32 v130, s46, v122\n\t"
        "ds_read_b64_tr_b16 v[108:109], v124 offset:6272\n\t"
        "ds_read_b64_tr_b16 v[110:111], v124 offset:9344\n\t"
        "s_waitcnt lgkmcnt(12)\n\t"
        "v_mfma_f32_32x32x16_bf16 v[64:79], v[148:151], v[112:115], v[64:79]\n\t"
        "ds_read_b64_tr_b16 v[112:113], v123 offset:6400\n\t"
        "ds_read_b64_tr_b16 v[114:115], v123 offset:9472\n\t"
        "s_waitcnt lgkmcnt(10)\n\t"
        "v_mfma_f32_32x32x16_bf16 v[80:95], v[148:151], v[116:119], v[80:95]\n\t"
        "v_cmp_gt_u32 vcc, s47, v126\n\t"
        "ds_read_b64_tr_b16 v[116:117], v124 offset:6400\n\t"
        "ds_read_b64_tr_b16 v[118:119], v124 offset:9472\n\t"
        "s_waitcnt vmcnt(14) lgkmcnt(10)\n\t"
        "v_mfma_f32_32x32x16_bf16 v[0:15], v[96:99], v[152:155], v[0:15]\n\t"
        "s_cbranch_vccnz 16f\n\t"
        "17:\n\t"
        "ds_read_b64_tr_b16 v[96:97], v129 offset:0\n\t"
        "ds_read_b64_tr_b16 v[98:99], v129 offset:3072\n\t"
        "s_waitcnt lgkmcnt(10)\n\t"
        "v_mfma_f32_32x32x16_bf16 v[16:31], v[100:103], v[152:155], v[16:31]\n\t"
        "v_add_u32 v128, 1, v128\n\t"
        "ds_read_b64_tr_b16 v[100:101], v130 offset:0\n\t"
        "ds_read_b64_tr_b16 v[102:103], v130 offset:3072\n\t"
        "s_waitcnt lgkmcnt(10)\n\t"
        "v_mfma_f32_32x32x16_bf16 v[32:47], v[104:107], v[152:155], v[32:47]\n\t"
        "ds_read_b64_tr_b16 v[104:105], v129 offset:128\n\t"
        "ds_read_b64_tr_b16 v[106:107], v129 offset:3200\n\t"
        "s_add_u32 s44, s44, 0x1000\n\t"
        "s_and_b32 s44, s44, 0xffff\n\t"
        "buffer_load_dwordx4 v[148:151], v120, s[40:43], s44 offen offset:0 sc1\n\t"
        "s_waitcnt lgkmcnt(10)\n\t"
        "v_mfma_f32_32x32x16_bf16 v[48:63], v[108:111], v[152:155], v[48:63]\n\t"
        "s_add_u32 s46, s46, 12288\n\t"
        "s_add_u32 s47, s47, 1\n\t"
        "ds_read_b64_tr_b16 v[108:109], v130 offset:128\n\t"
        "ds_read_b64_tr_b16 v[110:111], v130 offset:3200\n\t"
        "s_waitcnt lgkmcnt(10)\n\t"
        "v_mfma_f32_32x32x16_bf16 v[64:79], v[152:155], v[112:115], v[64:79]\n\t"
        "s_cmp_eq_u32 s46, 110592\n\t"
        "s_cselect_b32 s46, 0, s46\n\t"
        "ds_read_b64_tr_b16 v[112:113], v129 offset:256\n\t"
        "ds_read_b64_tr_b16 v[114:115], v129 offset:3328\n\t"
        "s_waitcnt lgkmcnt(10)\n\t"
        "v_mfma_f32_32x32x16_bf16 v[80:95], v[152:155], v[116:119], v[80:95]\n\t"
        "ds_write_b32 v127, v128\n\t"
        "ds_read_b64_tr_b16 v[116:117], v130 offset:256\n\t"
        "ds_read_b64_tr_b16 v[118:119], v130 offset:3328\n\t"
        "s_waitcnt vmcnt(14) lgkmcnt(11)\n\t"
        "v_mfma_f32_32x32x16_bf16 v[0:15], v[96:99], v[156:159], v[0:15]\n\t"
        "ds_read_b32 v126, v125\n\t"
        "ds_read_b64_tr_b16 v[96:97], v129 offset:6144\n\t"
        "ds_read_b64_tr_b16 v[98:99], v129 offset:9216\n\t"
        "s_waitcnt lgkmcnt(12)\n\t"
        "v_mfma_f32_32x32x16_bf16 v[16:31], v[100:103], v[156:159], v[16:31]\n\t"
        "v_add_u32 v123, s46, v121\n\t"
        "ds_read_b64_tr_b16 v[100:101], v130 offset:6144\n\t"
        "ds_read_b64_tr_b16 v[102:103], v130 offset:9216\n\t"
        "s_waitcnt lgkmcnt(12)\n\t"
        "v_mfma_f32_32x32x16_bf16 v[32:47], v[104:107], v[156:159], v[32:47]\n\t"
        "ds_read_b64_tr_b16 v[104:105], v129 offset:6272\n\t"
        "ds_read_b64_tr_b16 v[106:107], v129 offset:9344\n\t"
        "buffer_load_dwordx4 v[152:155], v120, s[40:43], s44 offen offset:1024 sc1\n\t"
        "s_waitcnt lgkmcnt(12)\n\t"
        "v_mfma_f32_32x32x16_bf16 v[48:63], v[108:111], v[156:159], v[48:63]\n\t"
        "v_add_u32 v124, s46, v122\n\t"
        "ds_read_b64_tr_b16 v[108:109], v130 offset:6272\n\t"
        "ds_read_b64_tr_b16 v[110:111], v130 offset:9344\n\t"
        "s_waitcnt lgkmcnt(12)\n\t"
        "v_mfma_f32_32x32x16_bf16 v[64:79], v[156:159], v[112:115], v[64:79]\n\t"
        "ds_read_b64_tr_b16 v[112:113], v129 offset:6400\n\t"
        "ds_read_b64_tr_b16 v[114:115], v129 offset:9472\n\t"
        "s_waitcnt lgkmcnt(10)\n\t"
        "v_mfma_f32_32x32x16_bf16 v[80:95], v[156:159], v[116:119], v[80:95]\n\t"
        "v_cmp_gt_u32 vcc, s47, v126\n\t"
        "ds_read_b64_tr_b16 v[116:117], v130 offset:6400\n\t"
        "ds_read_b64_tr_b16 v[118:119], v130 offset:9472\n\t"
        "s_waitcnt vmcnt(14) lgkmcnt(10)\n\t"
        "v_mfma_f32_32x32x16_bf16 v[0:15], v[96:99], v[160:163], v[0:15]\n\t"
        "s_cbranch_vccnz 18f\n\t"
        "19:\n\t"
        "ds_read_b64_tr_b16 v[96:97], v123 offset:0\n\t"
        "ds_read_b64_tr_b16 v[98:99], v123 offset:3072\n\t"
        "s_waitcnt lgkmcnt(10)\n\t"
        "v_mfma_f32_32x32x16_bf16 v[16:31], v[100:103], v[160:163], v[16:31]\n\t"
        "v_add_u32 v128, 1, v128\n\t"
        "ds_read_b64_tr_b16 v[100:101], v124 offset:0\n\t"
        "ds_read_b64_tr_b16 v[102:103], v124 offset:3072\n\t"
        "s_waitcnt lgkmcnt(10)\n\t"
        "v_mfma_f32_32x32x16_bf16 v[32:47], v[104:107], v[160:163], v[32:47]\n\t"
        "ds_read_b64_tr_b16 v[104:105], v123 offset:128\n\t"
        "ds_read_b64_tr_b16 v[106:107], v123 offset:3200\n\t"
        "buffer_load_dwordx4 v[156:159], v120, s[40:43], s44 offen offset:2048 sc1\n\t"
        "s_waitcnt lgkmcnt(10)\n\t"
        "v_mfma_f32_32x32x16_bf16 v[48:63], v[108:111], v[160:163], v[48:63]\n\t"
        "s_add_u32 s46, s46, 12288\n\t"
        "s_add_u32 s47, s47, 1\n\t"
        "ds_read_b64_tr_b16 v[108:109], v124 offset:128\n\t"
        "ds_read_b64_tr_b16 v[110:111], v124 offset:3200\n\t"
        "s_waitcnt lgkmcnt(10)\n\t"
        "v_mfma_f32_32x32x16_bf16 v[64:79], v[160:163], v[112:115], v[64:79]\n\t"
        "s_cmp_eq_u32 s46, 110592\n\t"
        "s_cselect_b32 s46, 0, s46\n\t"
        "ds_read_b64_tr_b16 v[112:113], v123 offset:256\n\t"
        "ds_read_b64_tr_b16 v[114:115], v123 offset:3328\n\t"
        "s_waitcnt lgkmcnt(10)\n\t"
        "v_mfma_f32_32x32x16_bf16 v[80:95], v[160:163], v[116:119], v[80:95]\n\t"
        "ds_write_b32 v127, v128\n\t"
        "ds_read_b64_tr_b16 v[116:117], v124 offset:256\n\t"
        "ds_read_b64_tr_b16 v[118:119], v124 offset:3328\n\t"
        "s_waitcnt vmcnt(14) lgkmcnt(11)\n\t"
        "v_mfma_f32_32x32x16_bf16 v[0:15], v[96:99], v[164:167], v[0:15]\n\t"
        "ds_read_b32 v126, v125\n\t"
        "ds_read_b64_tr_b16 v[96:97], v123 offset:6144\n\t"
        "ds_read_b64_tr_b16 v[98:99], v123 offset:9216\n\t"
        "s_waitcnt lgkmcnt(12)\n\t"
        "v_mfma_f32_32x32x16_bf16 v[16:31], v[100:103], v[164:167], v[16:31]\n\t"
        "v_add_u32 v129, s46, v121\n\t"
        "ds_read_b64_tr_b16 v[100:101], v124 offset:6144\n\t"
        "ds_read_b64_tr_b16 v[102:103], v124 offset:9216\n\t"
        "s_waitcnt lgkmcnt(12)\n\t"
        "v_mfma_f32_32x32x16_bf16 v[32:47], v[104:107], v[164:167], v[32:47]\n\t"
        "ds_read_b64_tr_b16 v[104:105], v123 offset:6272\n\t"
        "ds_read_b64_tr_b16 v[106:107], v123 offset:9344\n\t"
        "buffer_load_dwordx4 v[160:163], v120, s[40:43], s44 offen offset:3072 sc1\n\t"
        "s_waitcnt lgkmcnt(12)\n\t"
        "v_mfma_f32_32x32x16_bf16 v[48:63], v[108:111], v[164:167], v[48:63]\n\t"
        "v_add_u32 v130, s46, v122\n\t"
        "ds_read_b64_tr_b16 v[108:109], v124 offset:6272\n\t"
        "ds_read_b64_tr_b16 v[110:111], v124 offset:9344\n\t"
        "s_waitcnt lgkmcnt(12)\n\t"
        "v_mfma_f32_32x32x16_bf16 v[64:79], v[164:167], v[112:115], v[64:79]\n\t"
        "ds_read_b64_tr_b16 v[112:113], v123 offset:6400\n\t"
        "ds_read_b64_tr_b16 v[114:115], v123 offset:9472\n\t"
        "s_waitcnt lgkmcnt(10)\n\t"
        "v_mfma_f32_32x32x16_bf16 v[80:95], v[164:167], v[116:119], v[80:95]\n\t"
        "v_cmp_gt_u32 vcc, s47, v126\n\t"
        "ds_read_b64_tr_b16 v[116:117], v124 offset:6400\n\t"
        "ds_read_b64_tr_b16 v[118:119], v124 offset:9472\n\t"
        "s_waitcnt vmcnt(14) lgkmcnt(10)\n\t"
        "v_mfma_f32_32x32x16_bf16 v[0:15], v[96:99], v[168:171], v[0:15]\n\t"
        "s_cbranch_vccnz 20f\n\t"
        "21:\n\t"
        "ds_read_b64_tr_b16 v[96:97], v129 offset:0\n\t"
        "ds_read_b64_tr_b16 v[98:99], v129 offset:3072\n\t"
        "s_waitcnt lgkmcnt(10)\n\t"
        "v_mfma_f32_32x32x16_bf16 v[16:31], v[100:103], v[168:171], v[16:31]\n\t"
        "v_add_u32 v128, 1, v128\n\t"
        "ds_read_b64_tr_b16 v[100:101], v130 offset:0\n\t"
        "ds_read_b64_tr_b16 v[102:103], v130 offset:3072\n\t"
        "s_waitcnt lgkmcnt(10)\n\t"
        "v_mfma_f32_32x32x16_bf16 v[32:47], v[104:107], v[168:171], v[32:47]\n\t"
        "ds_read_b64_tr_b16 v[104:105], v129 offset:128\n\t"
        "ds_read_b64_tr_b16 v[106:107], v129 offset:3200\n\t"
        "s_add_u32 s44, s44, 0x1000\n\t"
        "s_and_b32 s44, s44, 0xffff\n\t"
        "buffer_load_dwordx4 v[164:167], v120, s[40:43], s44 offen offset:0 sc1\n\t"
        "s_waitcnt lgkmcnt(10)\n\t"
        "v_mfma_f32_32x32x16_bf16 v[48:63], v[108:111], v[168:171], v[48:63]\n\t"
        "s_add_u32 s46, s46, 12288\n\t"
        "s_add_u32 s47, s47, 1\n\t"
        "ds_read_b64_tr_b16 v[108:109], v130 offset:128\n\t"
        "ds_read_b64_tr_b16 v[110:111], v130 offset:3200\n\t"
        "s_waitcnt lgkmcnt(10)\n\t"
        "v_mfma_f32_32x32x16_bf16 v[64:79], v[168:171], v[112:115], v[64:79]\n\t"
        "s_cmp_eq_u32 s46, 110592\n\t"
        "s_cselect_b32 s46, 0, s46\n\t"
        "ds_read_b64_tr_b16 v[112:113], v129 offset:256\n\t"
        "ds_read_b64_tr_b16 v[114:115], v129 offset:3328\n\t"
        "s_waitcnt lgkmcnt(10)\n\t"
        "v_mfma_f32_32x32x16_bf16 v[80:95], v[168:171], v[116:119], v[80:95]\n\t"
        "ds_write_b32 v127, v128\n\t"
        "ds_read_b64_tr_b16 v[116:117], v130 offset:256\n\t"
        "ds_read_b64_tr_b16 v[118:119], v130 offset:3328\n\t"
        "s_waitcnt vmcnt(14) lgkmcnt(11)\n\t"
        "v_mfma_f32_32x32x16_bf16 v[0:15], v[96:99], v[172:175], v[0:15]\n\t"
        "ds_read_b32 v126, v125\n\t"
        "ds_read_b64_tr_b16 v[96:97], v129 offset:6144\n\t"
        "ds_read_b64_tr_b16 v[98:99], v129 offset:9216\n\t"
        "s_waitcnt lgkmcnt(12)\n\t"
        "v_mfma_f32_32x32x16_bf16 v[16:31], v[100:103], v[172:175], v[16:31]\n\t"
        "v_add_u32 v123, s46, v121\n\t"
        "ds_read_b64_tr_b16 v[100:101], v130 offset:6144\n\t"
        "ds_read_b64_tr_b16 v[102:103], v130 offset:9216\n\t"
        "s_waitcnt lgkmcnt(12)\n\t"
        "v_mfma_f32_32x32x16_bf16 v[32:47], v[104:107], v[172:175], v[32:47]\n\t"
        "ds_read_b64_tr_b16 v[104:105], v129 offset:6272\n\t"
        "ds_read_b64_tr_b16 v[106:107], v129 offset:9344\n\t"
        "buffer_load_dwordx4 v[168:171], v120, s[40:43], s44 offen offset:1024 sc1\n\t"
        "s_waitcnt lgkmcnt(12)\n\t"
        "v_mfma_f32_32x32x16_bf16 v[48:63], v[108:111], v[172:175], v[48:63]\n\t"
        "v_add_u32 v124, s46, v122\n\t"
        "ds_read_b64_tr_b16 v[108:109], v130 offset:6272\n\t"
        "ds_read_b64_tr_b16 v[110:111], v130 offset:9344\n\t"
        "s_waitcnt lgkmcnt(12)\n\t"
        "v_mfma_f32_32x32x16_bf16 v[64:79], v[172:175], v[112:115], v[64:79]\n\t"
        "ds_read_b64_tr_b16 v[112:113], v129 offset:6400\n\t"
        "ds_read_b64_tr_b16 v[114:115], v129 offset:9472\n\t"
        "s_waitcnt lgkmcnt(10)\n\t"
        "v_mfma_f32_32x32x16_bf16 v[80:95], v[172:175], v[116:119], v[80:95]\n\t"
        "v_cmp_gt_u32 vcc, s47, v126\n\t"
        "ds_read_b64_tr_b16 v[116:117], v130 offset:6400\n\t"
        "ds_read_b64_tr_b16 v[118:119], v130 offset:9472\n\t"
        "s_waitcnt vmcnt(14) lgkmcnt(10)\n\t"
        "v_mfma_f32_32x32x16_bf16 v[0:15], v[96:99], v[176:179], v[0:15]\n\t"
        "s_cbranch_vccnz 22f\n\t"
        "23:\n\t"
        "ds_read_b64_tr_b16 v[96:97], v123 offset:0\n\t"
        "ds_read_b64_tr_b16 v[98:99], v123 offset:3072\n\t"
        "s_waitcnt lgkmcnt(10)\n\t"
        "v_mfma_f32_32x32x16_bf16 v[16:31], v[100:103], v[176:179], v[16:31]\n\t"
        "v_add_u32 v128, 1, v128\n\t"
        "ds_read_b64_tr_b16 v[100:101], v124 offset:0\n\t"
        "ds_read_b64_tr_b16 v[102:103], v124 offset:3072\n\t"
        "s_waitcnt lgkmcnt(10)\n\t"
        "v_mfma_f32_32x32x16_bf16 v[32:47], v[104:107], v[176:179], v[32:47]\n\t"
        "ds_read_b64_tr_b16 v[104:105], v123 offset:128\n\t"
        "ds_read_b64_tr_b16 v[106:107], v123 offset:3200\n\t"
        "buffer_load_dwordx4 v[172:175], v120, s[40:43], s44 offen offset:2048 sc1\n\t"
        "s_waitcnt lgkmcnt(10)\n\t"
        "v_mfma_f32_32x32x16_bf16 v[48:63], v[108:111], v[176:179], v[48:63]\n\t"
        "s_add_u32 s46, s46, 12288\n\t"
        "s_add_u32 s47, s47, 1\n\t"
        "ds_read_b64_tr_b16 v[108:109], v124 offset:128\n\t"
        "ds_read_b64_tr_b16 v[110:111], v124 offset:3200\n\t"
        "s_waitcnt lgkmcnt(10)\n\t"
        "v_mfma_f32_32x32x16_bf16 v[64:79], v[176:179], v[112:115], v[64:79]\n\t"
        "s_cmp_eq_u32 s46, 110592\n\t"
        "s_cselect_b32 s46, 0, s46\n\t"
        "ds_read_b64_tr_b16 v[112:113], v123 offset:256\n\t"
        "ds_read_b64_tr_b16 v[114:115], v123 offset:3328\n\t"
        "s_waitcnt lgkmcnt(10)\n\t"
        "v_mfma_f32_32x32x16_bf16 v[80:95], v[176:179], v[116:119], v[80:95]\n\t"
        "ds_write_b32 v127, v128\n\t"
        "ds_read_b64_tr_b16 v[116:117], v124 offset:256\n\t"
        "ds_read_b64_tr_b16 v[118:119], v124 offset:3328\n\t"
        "s_waitcnt vmcnt(14) lgkmcnt(11)\n\t"
        "v_mfma_f32_32x32x16_bf16 v[0:15], v[96:99], v[180:183], v[0:15]\n\t"
        "ds_read_b32 v126, v125\n\t"
        "ds_read_b64_tr_b16 v[96:97], v123 offset:6144\n\t"
        "ds_read_b64_tr_b16 v[98:99], v123 offset:9216\n\t"
        "s_waitcnt lgkmcnt(12)\n\t"
        "v_mfma_f32_32x32x16_bf16 v[16:31], v[100:103], v[180:183], v[16:31]\n\t"
        "v_add_u32 v129, s46, v121\n\t"
        "ds_read_b64_tr_b16 v[100:101], v124 offset:6144\n\t"
        "ds_read_b64_tr_b16 v[102:103], v124 offset:9216\n\t"
        "s_waitcnt lgkmcnt(12)\n\t"
        "v_mfma_f32_32x32x16_bf16 v[32:47], v[104:107], v[180:183], v[32:47]\n\t"
        "ds_read_b64_tr_b16 v[104:105], v123 offset:6272\n\t"
        "ds_read_b64_tr_b16 v[106:107], v123 offset:9344\n\t"
        "buffer_load_dwordx4 v[176:179], v120, s[40:43], s44 offen offset:3072 sc1\n\t"
        "s_waitcnt lgkmcnt(12)\n\t"
        "v_mfma_f32_32x32x16_bf16 v[48:63], v[108:111], v[180:183], v[48:63]\n\t"
        "v_add_u32 v130, s46, v122\n\t"
        "ds_read_b64_tr_b16 v[108:109], v124 offset:6272\n\t"
        "ds_read_b64_tr_b16 v[110:111], v124 offset:9344\n\t"
        "s_waitcnt lgkmcnt(12)\n\t"
        "v_mfma_f32_32x32x16_bf16 v[64:79], v[180:183], v[112:115], v[64:79]\n\t"
        "ds_read_b64_tr_b16 v[112:113], v123 offset:6400\n\t"
        "ds_read_b64_tr_b16 v[114:115], v123 offset:9472\n\t"
        "s_waitcnt lgkmcnt(10)\n\t"
        "v_mfma_f32_32x32x16_bf16 v[80:95], v[180:183], v[116:119], v[80:95]\n\t"
        "v_cmp_gt_u32 vcc, s47, v126\n\t"
        "ds_read_b64_tr_b16 v[116:117], v124 offset:6400\n\t"
        "ds_read_b64_tr_b16 v[118:119], v124 offset:9472\n\t"
        "s_waitcnt vmcnt(14) lgkmcnt(10)\n\t"
        "v_mfma_f32_32x32x16_bf16 v[0:15], v[96:99], v[184:187], v[0:15]\n\t"
        "s_cbranch_vccnz 24f\n\t"
        "25:\n\t"
        "ds_read_b64_tr_b16 v[96:97], v129 offset:0\n\t"
        "ds_read_b64_tr_b16 v[98:99], v129 offset:3072\n\t"
        "s_waitcnt lgkmcnt(10)\n\t"
        "v_mfma_f32_32x32x16_bf16 v[16:31], v[100:103], v[184:187], v[16:31]\n\t"
        "v_add_u32 v128, 1, v128\n\t"
        "ds_read_b64_tr_b16 v[100:101], v130 offset:0\n\t"
        "ds_read_b64_tr_b16 v[102:103], v130 offset:3072\n\t"
        "s_waitcnt lgkmcnt(10)\n\t"
        "v_mfma_f32_32x32x16_bf16 v[32:47], v[104:107], v[184:187], v[32:47]\n\t"
        "ds_read_b64_tr_b16 v[104:105], v129 offset:128\n\t"
        "ds_read_b64_tr_b16 v[106:107], v129 offset:3200\n\t"
        "s_add_u32 s44, s44, 0x1000\n\t"
        "s_and_b32 s44, s44, 0xffff\n\t"
        "buffer_load_dwordx4 v[180:183], v120, s[40:43], s44 offen offset:0 sc1\n\t"
        "s_waitcnt lgkmcnt(10)\n\t"
        "v_mfma_f32_32x32x16_bf16 v[48:63], v[108:111], v[184:187], v[48:63]\n\t"
        "s_add_u32 s46, s46, 12288\n\t"
        "s_add_u32 s47, s47, 1\n\t"
        "ds_read_b64_tr_b16 v[108:109], v130 offset:128\n\t"
        "ds_read_b64_tr_b16 v[110:111], v130 offset:3200\n\t"
        "s_waitcnt lgkmcnt(10)\n\t"
        "v_mfma_f32_32x32x16_bf16 v[64:79], v[184:187], v[112:115], v[64:79]\n\t"
        "s_cmp_eq_u32 s46, 110592\n\t"
        "s_cselect_b32 s46, 0, s46\n\t"
        "ds_read_b64_tr_b16 v[112:113], v129 offset:256\n\t"
        "ds_read_b64_tr_b16 v[114:115], v129 offset:3328\n\t"
        "s_waitcnt lgkmcnt(10)\n\t"
        "v_mfma_f32_32x32x16_bf16 v[80:95], v[184:187], v[116:119], v[80:95]\n\t"
        "ds_write_b32 v127, v128\n\t"
        "ds_read_b64_tr_b16 v[116:117], v130 offset:256\n\t"
        "ds_read_b64_tr_b16 v[118:119], v130 offset:3328\n\t"
        "s_waitcnt vmcnt(14) lgkmcnt(11)\n\t"
        "v_mfma_f32_32x32x16_bf16 v[0:15], v[96:99], v[188:191], v[0:15]\n\t"
        "ds_read_b32 v126, v125\n\t"
        "ds_read_b64_tr_b16 v[96:97], v129 offset:6144\n\t"
        "ds_read_b64_tr_b16 v[98:99], v129 offset:9216\n\t"
        "s_waitcnt lgkmcnt(12)\n\t"
        "v_mfma_f32_32x32x16_bf16 v[16:31], v[100:103], v[188:191], v[16:31]\n\t"
        "v_add_u32 v123, s46, v121\n\t"
        "ds_read_b64_tr_b16 v[100:101], v130 offset:6144\n\t"
        "ds_read_b64_tr_b16 v[102:103], v130 offset:9216\n\t"
        "s_waitcnt lgkmcnt(12)\n\t"
        "v_mfma_f32_32x32x16_bf16 v[32:47], v[104:107], v[188:191], v[32:47]\n\t"
        "ds_read_b64_tr_b16 v[104:105], v129 offset:6272\n\t"
        "ds_read_b64_tr_b16 v[106:107], v129 offset:9344\n\t"
        "buffer_load_dwordx4 v[184:187], v120, s[40:43], s44 offen offset:1024 sc1\n\t"
        "s_waitcnt lgkmcnt(12)\n\t"
        "v_mfma_f32_32x32x16_bf16 v[48:63], v[108:111], v[188:191], v[48:63]\n\t"
        "v_add_u32 v124, s46, v122\n\t"
        "ds_read_b64_tr_b16 v[108:109], v130 offset:6272\n\t"
        "ds_read_b64_tr_b16 v[110:111], v130 offset:9344\n\t"
        "s_waitcnt lgkmcnt(12)\n\t"
        "v_mfma_f32_32x32x16_bf16 v[64:79], v[188:191], v[112:115], v[64:79]\n\t"
        "ds_read_b64_tr_b16 v[112:113], v129 offset:6400\n\t"
        "ds_read_b64_tr_b16 v[114:115], v129 offset:9472\n\t"
        "s_waitcnt lgkmcnt(10)\n\t"
        "v_mfma_f32_32x32x16_bf16 v[80:95], v[188:191], v[116:119], v[80:95]\n\t"
        "v_cmp_gt_u32 vcc, s47, v126\n\t"
        "ds_read_b64_tr_b16 v[116:117], v130 offset:6400\n\t"
        "ds_read_b64_tr_b16 v[118:119], v130 offset:9472\n\t"
        "s_waitcnt vmcnt(14) lgkmcnt(10)\n\t"
        "v_mfma_f32_32x32x16_bf16 v[0:15], v[96:99], v[192:195], v[0:15]\n\t"
        "s_cbranch_vccnz 26f\n\t"
        "27:\n\t"
        "ds_read_b64_tr_b16 v[96:97], v123 offset:0\n\t"
        "ds_read_b64_tr_b16 v[98:99], v123 offset:3072\n\t"
        "s_waitcnt lgkmcnt(10)\n\t"
        "v_mfma_f32_32x32x16_bf16 v[16:31], v[100:103], v[192:195], v[16:31]\n\t"
        "v_add_u32 v128, 1, v128\n\t"
        "ds_read_b64_tr_b16 v[100:101], v124 offset:0\n\t"
        "ds_read_b64_tr_b16 v[102:103], v124 offset:3072\n\t"
        "s_waitcnt lgkmcnt(10)\n\t"
        "v_mfma_f32_32x32x16_bf16 v[32:47], v[104:107], v[192:195], v[32:47]\n\t"
        "ds_read_b64_tr_b16 v[104:105], v123 offset:128\n\t"
        "ds_read_b64_tr_b16 v[106:107], v123 offset:3200\n\t"
        "buffer_load_dwordx4 v[188:191], v120, s[40:43], s44 offen offset:2048 sc1\n\t"
        "s_waitcnt lgkmcnt(10)\n\t"
        "v_mfma_f32_32x32x16_bf16 v[48:63], v[108:111], v[192:195], v[48:63]\n\t"
        "s_add_u32 s46, s46, 12288\n\t"
        "s_add_u32 s47, s47, 1\n\t"
        "ds_read_b64_tr_b16 v[108:109], v124 offset:128\n\t"
        "ds_read_b64_tr_b16 v[110:111], v124 offset:3200\n\t"
        "s_waitcnt lgkmcnt(10)\n\t"
        "v_mfma_f32_32x32x16_bf16 v[64:79], v[192:195], v[112:115], v[64:79]\n\t"
        "s_cmp_eq_u32 s46, 110592\n\t"
        "s_cselect_b32 s46, 0, s46\n\t"
        "ds_read_b64_tr_b16 v[112:113], v123 offset:256\n\t"
        "ds_read_b64_tr_b16 v[114:115], v123 offset:3328\n\t"
        "s_waitcnt lgkmcnt(10)\n\t"
        "v_mfma_f32_32x32x16_bf16 v[80:95], v[192:195], v[116:119], v[80:95]\n\t"
        "ds_write_b32 v127, v128\n\t"
        "ds_read_b64_tr_b16 v[116:117], v124 offset:256\n\t"
        "ds_read_b64_tr_b16 v[118:119], v124 offset:3328\n\t"
        "s_waitcnt vmcnt(14) lgkmcnt(11)\n\t"
        "v_mfma_f32_32x32x16_bf16 v[0:15], v[96:99], v[132:135], v[0:15]\n\t"
        "ds_read_b32 v126, v125\n\t"
        "ds_read_b64_tr_b16 v[96:97], v123 offset:6144\n\t"
        "ds_read_b64_tr_b16 v[98:99], v123 offset:9216\n\t"
        "s_waitcnt lgkmcnt(12)\n\t"
        "v_mfma_f32_32x32x16_bf16 v[16:31], v[100:103], v[132:135], v[16:31]\n\t"
        "v_add_u32 v129, s46, v121\n\t"
        "ds_read_b64_tr_b16 v[100:101], v124 offset:6144\n\t"
        "ds_read_b64_tr_b16 v[102:103], v124 offset:9216\n\t"
        "s_waitcnt lgkmcnt(12)\n\t"
        "v_mfma_f32_32x32x16_bf16 v[32:47], v[104:107], v[132:135], v[32:47]\n\t"
        "ds_read_b64_tr_b16 v[104:105], v123 offset:6272\n\t"
        "ds_read_b64_tr_b16 v[106:107], v123 offset:9344\n\t"
        "buffer_load_dwordx4 v[192:195], v120, s[40:43], s44 offen offset:3072 sc1\n\t"
        "s_waitcnt lgkmcnt(12)\n\t"
        "v_mfma_f32_32x32x16_bf16 v[48:63], v[108:111], v[132:135], v[48:63]\n\t"
        "v_add_u32 v130, s46, v122\n\t"
        "ds_read_b64_tr_b16 v[108:109], v124 offset:6272\n\t"
        "ds_read_b64_tr_b16 v[110:111], v124 offset:9344\n\t"
        "s_waitcnt lgkmcnt(12)\n\t"
        "v_mfma_f32_32x32x16_bf16 v[64:79], v[132:135], v[112:115], v[64:79]\n\t"
        "ds_read_b64_tr_b16 v[112:113], v123 offset:6400\n\t"
        "ds_read_b64_tr_b16 v[114:115], v123 offset:9472\n\t"
        "s_waitcnt lgkmcnt(10)\n\t"
        "v_mfma_f32_32x32x16_bf16 v[80:95], v[132:135], v[116:119], v[80:95]\n\t"
        "v_cmp_gt_u32 vcc, s47, v126\n\t"
        "ds_read_b64_tr_b16 v[116:117], v124 offset:6400\n\t"
        "ds_read_b64_tr_b16 v[118:119], v124 offset:9472\n\t"
        "s_waitcnt vmcnt(14) lgkmcnt(10)\n\t"
        "v_mfma_f32_32x32x16_bf16 v[0:15], v[96:99], v[136:139], v[0:15]\n\t"
        "s_cbranch_vccnz 28f\n\t"
        "29:\n\t"
        "ds_read_b64_tr_b16 v[96:97], v129 offset:0\n\t"
        "ds_read_b64_tr_b16 v[98:99], v129 offset:3072\n\t"
        "s_waitcnt lgkmcnt(10)\n\t"
        "v_mfma_f32_32x32x16_bf16 v[16:31], v[100:103], v[136:139], v[16:31]\n\t"
        "v_add_u32 v128, 1, v128\n\t"
        "ds_read_b64_tr_b16 v[100:101], v130 offset:0\n\t"
        "ds_read_b64_tr_b16 v[102:103], v130 offset:3072\n\t"
        "s_waitcnt lgkmcnt(10)\n\t"
        "v_mfma_f32_32x32x16_bf16 v[32:47], v[104:107], v[136:139], v[32:47]\n\t"
        "ds_read_b64_tr_b16 v[104:105], v129 offset:128\n\t"
        "ds_read_b64_tr_b16 v[106:107], v129 offset:3200\n\t"
        "s_add_u32 s44, s44, 0x1000\n\t"
        "s_and_b32 s44, s44, 0xffff\n\t"
        "buffer_load_dwordx4 v[132:135], v120, s[40:43], s44 offen offset:0 sc1\n\t"
        "s_waitcnt lgkmcnt(10)\n\t"
        "v_mfma_f32_32x32x16_bf16 v[48:63], v[108:111], v[136:139], v[48:63]\n\t"
        "s_add_u32 s46, s46, 12288\n\t"
        "s_add_u32 s47, s47, 1\n\t"
        "ds_read_b64_tr_b16 v[108:109], v130 offset:128\n\t"
        "ds_read_b64_tr_b16 v[110:111], v130 offset:3200\n\t"
        "s_waitcnt lgkmcnt(10)\n\t"
        "v_mfma_f32_32x32x16_bf16 v[64:79], v[136:139], v[112:115], v[64:79]\n\t"
        "s_cmp_eq_u32 s46, 110592\n\t"
        "s_cselect_b32 s46, 0, s46\n\t"
        "ds_read_b64_tr_b16 v[112:113], v129 offset:256\n\t"
        "ds_read_b64_tr_b16 v[114:115], v129 offset:3328\n\t"
        "s_waitcnt lgkmcnt(10)\n\t"
        "v_mfma_f32_32x32x16_bf16 v[80:95], v[136:139], v[116:119], v[80:95]\n\t"
        "ds_write_b32 v127, v128\n\t"
        "ds_read_b64_tr_b16 v[116:117], v130 offset:256\n\t"
        "ds_read_b64_tr_b16 v[118:119], v130 offset:3328\n\t"
        "s_waitcnt vmcnt(14) lgkmcnt(11)\n\t"
        "v_mfma_f32_32x32x16_bf16 v[0:15], v[96:99], v[140:143], v[0:15]\n\t"
        "ds_read_b32 v126, v125\n\t"
        "ds_read_b64_tr_b16 v[96:97], v129 offset:6144\n\t"
        "ds_read_b64_tr_b16 v[98:99], v129 offset:9216\n\t"
        "s_waitcnt lgkmcnt(12)\n\t"
        "v_mfma_f32_32x32x16_bf16 v[16:31], v[100:103], v[140:143], v[16:31]\n\t"
        "v_add_u32 v123, s46, v121\n\t"
        "ds_read_b64_tr_b16 v[100:101], v130 offset:6144\n\t"
        "ds_read_b64_tr_b16 v[102:103], v130 offset:9216\n\t"
        "s_waitcnt lgkmcnt(12)\n\t"
        "v_mfma_f32_32x32x16_bf16 v[32:47], v[104:107], v[140:143], v[32:47]\n\t"
        "ds_read_b64_tr_b16 v[104:105], v129 offset:6272\n\t"
        "ds_read_b64_tr_b16 v[106:107], v129 offset:9344\n\t"
        "buffer_load_dwordx4 v[136:139], v120, s[40:43], s44 offen offset:1024 sc1\n\t"
        "s_waitcnt lgkmcnt(12)\n\t"
        "v_mfma_f32_32x32x16_bf16 v[48:63], v[108:111], v[140:143], v[48:63]\n\t"
        "v_add_u32 v124, s46, v122\n\t"
        "ds_read_b64_tr_b16 v[108:109], v130 offset:6272\n\t"
        "ds_read_b64_tr_b16 v[110:111], v130 offset:9344\n\t"
        "s_waitcnt lgkmcnt(12)\n\t"
        "v_mfma_f32_32x32x16_bf16 v[64:79], v[140:143], v[112:115], v[64:79]\n\t"
        "ds_read_b64_tr_b16 v[112:113], v129 offset:6400\n\t"
        "ds_read_b64_tr_b16 v[114:115], v129 offset:9472\n\t"
        "s_waitcnt lgkmcnt(10)\n\t"
        "v_mfma_f32_32x32x16_bf16 v[80:95], v[140:143], v[116:119], v[80:95]\n\t"
        "v_cmp_gt_u32 vcc, s47, v126\n\t"
        "ds_read_b64_tr_b16 v[116:117], v130 offset:6400\n\t"
        "ds_read_b64_tr_b16 v[118:119], v130 offset:9472\n\t"
        "s_waitcnt vmcnt(14) lgkmcnt(10)\n\t"
        "v_mfma_f32_32x32x16_bf16 v[0:15], v[96:99], v[144:147], v[0:15]\n\t"
        "s_cbranch_vccnz 30f\n\t"
        "31:\n\t"
        "ds_read_b64_tr_b16 v[96:97], v123 offset:0\n\t"
        "ds_read_b64_tr_b16 v[98:99], v123 offset:3072\n\t"
        "s_waitcnt lgkmcnt(10)\n\t"
        "v_mfma_f32_32x32x16_bf16 v[16:31], v[100:103], v[144:147], v[16:31]\n\t"
        "v_add_u32 v128, 1, v128\n\t"
        "ds_read_b64_tr_b16 v[100:101], v124 offset:0\n\t"
        "ds_read_b64_tr_b16 v[102:103], v124 offset:3072\n\t"
        "s_waitcnt lgkmcnt(10)\n\t"
        "v_mfma_f32_32x32x16_bf16 v[32:47], v[104:107], v[144:147], v[32:47]\n\t"
        "ds_read_b64_tr_b16 v[104:105], v123 offset:128\n\t"
        "ds_read_b64_tr_b16 v[106:107], v123 offset:3200\n\t"
        "buffer_load_dwordx4 v[140:143], v120, s[40:43], s44 offen offset:2048 sc1\n\t"
        "s_waitcnt lgkmcnt(10)\n\t"
        "v_mfma_f32_32x32x16_bf16 v[48:63], v[108:111], v[144:147], v[48:63]\n\t"
        "s_add_u32 s46, s46, 12288\n\t"
        "s_add_u32 s47, s47, 1\n\t"
        "ds_read_b64_tr_b16 v[108:109], v124 offset:128\n\t"
        "ds_read_b64_tr_b16 v[110:111], v124 offset:3200\n\t"
        "s_waitcnt lgkmcnt(10)\n\t"
        "v_mfma_f32_32x32x16_bf16 v[64:79], v[144:147], v[112:115], v[64:79]\n\t"
        "s_cmp_eq_u32 s46, 110592\n\t"
        "s_cselect_b32 s46, 0, s46\n\t"
        "ds_read_b64_tr_b16 v[112:113], v123 offset:256\n\t"
        "ds_read_b64_tr_b16 v[114:115], v123 offset:3328\n\t"
        "s_waitcnt lgkmcnt(10)\n\t"
        "v_mfma_f32_32x32x16_bf16 v[80:95], v[144:147], v[116:119], v[80:95]\n\t"
        "ds_write_b32 v127, v128\n\t"
        "ds_read_b64_tr_b16 v[116:117], v124 offset:256\n\t"
        "ds_read_b64_tr_b16 v[118:119], v124 offset:3328\n\t"
        "s_sub_u32 s45, s45, 1\n\t"
        "s_cmp_lg_u32 s45, 0\n\t"
        "s_cbranch_scc1 1b\n\t"
        "s_waitcnt vmcnt(14) lgkmcnt(11)\n\t"
        "v_mfma_f32_32x32x16_bf16 v[0:15], v[96:99], v[148:151], v[0:15]\n\t"
        "ds_read_b32 v126, v125\n\t"
        "ds_read_b64_tr_b16 v[96:97], v123 offset:6144\n\t"
        "ds_read_b64_tr_b16 v[98:99], v123 offset:9216\n\t"
        "s_waitcnt lgkmcnt(12)\n\t"
        "v_mfma_f32_32x32x16_bf16 v[16:31], v[100:103], v[148:151], v[16:31]\n\t"
        "v_add_u32 v129, s46, v121\n\t"
        "ds_read_b64_tr_b16 v[100:101], v124 offset:6144\n\t"
        "ds_read_b64_tr_b16 v[102:103], v124 offset:9216\n\t"
        "s_waitcnt lgkmcnt(12)\n\t"
        "v_mfma_f32_32x32x16_bf16 v[32:47], v[104:107], v[148:151], v[32:47]\n\t"
        "ds_read_b64_tr_b16 v[104:105], v123 offset:6272\n\t"
        "ds_read_b64_tr_b16 v[106:107], v123 offset:9344\n\t"
        "s_waitcnt lgkmcnt(12)\n\t"
        "v_mfma_f32_32x32x16_bf16 v[48:63], v[108:111], v[148:151], v[48:63]\n\t"
        "v_add_u32 v130, s46, v122\n\t"
        "ds_read_b64_tr_b16 v[108:109], v124 offset:6272\n\t"
        "ds_read_b64_tr_b16 v[110:111], v124 offset:9344\n\t"
        "s_waitcnt lgkmcnt(12)\n\t"
        "v_mfma_f32_32x32x16_bf16 v[64:79], v[148:151], v[112:115], v[64:79]\n\t"
        "ds_read_b64_tr_b16 v[112:113], v123 offset:6400\n\t"
        "ds_read_b64_tr_b16 v[114:115], v123 offset:9472\n\t"
        "s_waitcnt lgkmcnt(10)\n\t"
        "v_mfma_f32_32x32x16_bf16 v[80:95], v[148:151], v[116:119], v[80:95]\n\t"
        "v_cmp_gt_u32 vcc, s47, v126\n\t"
        "ds_read_b64_tr_b16 v[116:117], v124 offset:6400\n\t"
        "ds_read_b64_tr_b16 v[118:119], v124 offset:9472\n\t"
        "s_waitcnt vmcnt(13) lgkmcnt(10)\n\t"
        "v_mfma_f32_32x32x16_bf16 v[0:15], v[96:99], v[152:155], v[0:15]\n\t"
        "s_cbranch_vccnz 64f\n\t"
        "65:\n\t"
        "ds_read_b64_tr_b16 v[96:97], v129 offset:0\n\t"
        "ds_read_b64_tr_b16 v[98:99], v129 offset:3072\n\t"
        "s_waitcnt lgkmcnt(10)\n\t"
        "v_mfma_f32_32x32x16_bf16 v[16:31], v[100:103], v[152:155], v[16:31]\n\t"
        "v_add_u32 v128, 1, v128\n\t"
        "ds_read_b64_tr_b16 v[100:101], v130 offset:0\n\t"
        "ds_read_b64_tr_b16 v[102:103], v130 offset:3072\n\t"
        "s_waitcnt lgkmcnt(10)\n\t"
        "v_mfma_f32_32x32x16_bf16 v[32:47], v[104:107], v[152:155], v[32:47]\n\t"
        "ds_read_b64_tr_b16 v[104:105], v129 offset:128\n\t"
        "ds_read_b64_tr_b16 v[106:107], v129 offset:3200\n\t"
        "s_waitcnt lgkmcnt(10)\n\t"
        "v_mfma_f32_32x32x16_bf16 v[48:63], v[108:111], v[152:155], v[48:63]\n\t"
        "s_add_u32 s46, s46, 12288\n\t"
        "s_add_u32 s47, s47, 1\n\t"
        "ds_read_b64_tr_b16 v[108:109], v130 offset:128\n\t"
        "ds_read_b64_tr_b16 v[110:111], v130 offset:3200\n\t"
        "s_waitcnt lgkmcnt(10)\n\t"
        "v_mfma_f32_32x32x16_bf16 v[64:79], v[152:155], v[112:115], v[64:79]\n\t"
        "s_cmp_eq_u32 s46, 110592\n\t"
        "s_cselect_b32 s46, 0, s46\n\t"
        "ds_read_b64_tr_b16 v[112:113], v129 offset:256\n\t"
        "ds_read_b64_tr_b16 v[114:115], v129 offset:3328\n\t"
        "s_waitcnt lgkmcnt(10)\n\t"
        "v_mfma_f32_32x32x16_bf16 v[80:95], v[152:155], v[116:119], v[80:95]\n\t"
        "ds_write_b32 v127, v128\n\t"
        "ds_read_b64_tr_b16 v[116:117], v130 offset:256\n\t"
        "ds_read_b64_tr_b16 v[118:119], v130 offset:3328\n\t"
        "s_waitcnt vmcnt(12) lgkmcnt(11)\n\t"
        "v_mfma_f32_32x32x16_bf16 v[0:15], v[96:99], v[156:159], v[0:15]\n\t"
        "ds_read_b32 v126, v125\n\t"
        "ds_read_b64_tr_b16 v[96:97], v129 offset:6144\n\t"
        "ds_read_b64_tr_b16 v[98:99], v129 offset:9216\n\t"
        "s_waitcnt lgkmcnt(12)\n\t"
        "v_mfma_f32_32x32x16_bf16 v[16:31], v[100:103], v[156:159], v[16:31]\n\t"
        "v_add_u32 v123, s46, v121\n\t"
        "ds_read_b64_tr_b16 v[100:101], v130 offset:6144\n\t"
        "ds_read_b64_tr_b16 v[102:103], v130 offset:9216\n\t"
        "s_waitcnt lgkmcnt(12)\n\t"
        "v_mfma_f32_32x32x16_bf16 v[32:47], v[104:107], v[156:159], v[32:47]\n\t"
        "ds_read_b64_tr_b16 v[104:105], v129 offset:6272\n\t"
        "ds_read_b64_tr_b16 v[106:107], v129 offset:9344\n\t"
        "s_waitcnt lgkmcnt(12)\n\t"
        "v_mfma_f32_32x32x16_bf16 v[48:63], v[108:111], v[156:159], v[48:63]\n\t"
        "v_add_u32 v124, s46, v122\n\t"
        "ds_read_b64_tr_b16 v[108:109], v130 offset:6272\n\t"
        "ds_read_b64_tr_b16 v[110:111], v130 offset:9344\n\t"
        "s_waitcnt lgkmcnt(12)\n\t"
        "v_mfma_f32_32x32x16_bf16 v[64:79], v[156:159], v[112:115], v[64:79]\n\t"
        "ds_read_b64_tr_b16 v[112:113], v129 offset:6400\n\t"
        "ds_read_b64_tr_b16 v[114:115], v129 offset:9472\n\t"
        "s_waitcnt lgkmcnt(10)\n\t"
        "v_mfma_f32_32x32x16_bf16 v[80:95], v[156:159], v[116:119], v[80:95]\n\t"
        "v_cmp_gt_u32 vcc, s47, v126\n\t"
        "ds_read_b64_tr_b16 v[116:117], v130 offset:6400\n\t"
        "ds_read_b64_tr_b16 v[118:119], v130 offset:9472\n\t"
        "s_waitcnt vmcnt(11) lgkmcnt(10)\n\t"
        "v_mfma_f32_32x32x16_bf16 v[0:15], v[96:99], v[160:163], v[0:15]\n\t"
        "s_cbranch_vccnz 66f\n\t"
        "67:\n\t"
        "ds_read_b64_tr_b16 v[96:97], v123 offset:0\n\t"
        "ds_read_b64_tr_b16 v[98:99], v123 offset:3072\n\t"
        "s_waitcnt lgkmcnt(10)\n\t"
        "v_mfma_f32_32x32x16_bf16 v[16:31], v[100:103], v[160:163], v[16:31]\n\t"
        "v_add_u32 v128, 1, v128\n\t"
        "ds_read_b64_tr_b16 v[100:101], v124 offset:0\n\t"
        "ds_read_b64_tr_b16 v[102:103], v124 offset:3072\n\t"
        "s_waitcnt lgkmcnt(10)\n\t"
        "v_mfma_f32_32x32x16_bf16 v[32:47], v[104:107], v[160:163], v[32:47]\n\t"
        "ds_read_b64_tr_b16 v[104:105], v123 offset:128\n\t"
        "ds_read_b64_tr_b16 v[106:107], v123 offset:3200\n\t"
        "s_waitcnt lgkmcnt(10)\n\t"
        "v_mfma_f32_32x32x16_bf16 v[48:63], v[108:111], v[160:163], v[48:63]\n\t"
        "s_add_u32 s46, s46, 12288\n\t"
        "s_add_u32 s47, s47, 1\n\t"
        "ds_read_b64_tr_b16 v[108:109], v124 offset:128\n\t"
        "ds_read_b64_tr_b16 v[110:111], v124 offset:3200\n\t"
        "s_waitcnt lgkmcnt(10)\n\t"
        "v_mfma_f32_32x32x16_bf16 v[64:79], v[160:163], v[112:115], v[64:79]\n\t"
        "s_cmp_eq_u32 s46, 110592\n\t"
        "s_cselect_b32 s46, 0, s46\n\t"
        "ds_read_b64_tr_b16 v[112:113], v123 offset:256\n\t"
        "ds_read_b64_tr_b16 v[114:115], v123 offset:3328\n\t"
        "s_waitcnt lgkmcnt(10)\n\t"
        "v_mfma_f32_32x32x16_bf16 v[80:95], v[160:163], v[116:119], v[80:95]\n\t"
        "ds_write_b32 v127, v128\n\t"
        "ds_read_b64_tr_b16 v[116:117], v124 offset:256\n\t"
        "ds_read_b64_tr_b16 v[118:119], v124 offset:3328\n\t"
        "s_waitcnt vmcnt(10) lgkmcnt(11)\n\t"
        "v_mfma_f32_32x32x16_bf16 v[0:15], v[96:99], v[164:167], v[0:15]\n\t"
        "ds_read_b32 v126, v125\n\t"
        "ds_read_b64_tr_b16 v[96:97], v123 offset:6144\n\t"
        "ds_read_b64_tr_b16 v[98:99], v123 offset:9216\n\t"
        "s_waitcnt lgkmcnt(12)\n\t"
        "v_mfma_f32_32x32x16_bf16 v[16:31], v[100:103], v[164:167], v[16:31]\n\t"
        "v_add_u32 v129, s46, v121\n\t"
        "ds_read_b64_tr_b16 v[100:101], v124 offset:6144\n\t"
        "ds_read_b64_tr_b16 v[102:103], v124 offset:9216\n\t"
        "s_waitcnt lgkmcnt(12)\n\t"
        "v_mfma_f32_32x32x16_bf16 v[32:47], v[104:107], v[164:167], v[32:47]\n\t"
        "ds_read_b64_tr_b16 v[104:105], v123 offset:6272\n\t"
        "ds_read_b64_tr_b16 v[106:107], v123 offset:9344\n\t"
        "s_waitcnt lgkmcnt(12)\n\t"
        "v_mfma_f32_32x32x16_bf16 v[48:63], v[108:111], v[164:167], v[48:63]\n\t"
        "v_add_u32 v130, s46, v122\n\t"
        "ds_read_b64_tr_b16 v[108:109], v124 offset:6272\n\t"
        "ds_read_b64_tr_b16 v[110:111], v124 offset:9344\n\t"
        "s_waitcnt lgkmcnt(12)\n\t"
        "v_mfma_f32_32x32x16_bf16 v[64:79], v[164:167], v[112:115], v[64:79]\n\t"
        "ds_read_b64_tr_b16 v[112:113], v123 offset:6400\n\t"
        "ds_read_b64_tr_b16 v[114:115], v123 offset:9472\n\t"
        "s_waitcnt lgkmcnt(10)\n\t"
        "v_mfma_f32_32x32x16_bf16 v[80:95], v[164:167], v[116:119], v[80:95]\n\t"
        "v_cmp_gt_u32 vcc, s47, v126\n\t"
        "ds_read_b64_tr_b16 v[116:117], v124 offset:6400\n\t"
        "ds_read_b64_tr_b16 v[118:119], v124 offset:9472\n\t"
        "s_waitcnt vmcnt(9) lgkmcnt(10)\n\t"
        "v_mfma_f32_32x32x16_bf16 v[0:15], v[96:99], v[168:171], v[0:15]\n\t"
        "s_cbranch_vccnz 68f\n\t"
        "69:\n\t"
        "ds_read_b64_tr_b16 v[96:97], v129 offset:0\n\t"
        "ds_read_b64_tr_b16 v[98:99], v129 offset:3072\n\t"
        "s_waitcnt lgkmcnt(10)\n\t"
        "v_mfma_f32_32x32x16_bf16 v[16:31], v[100:103], v[168:171], v[16:31]\n\t"
        "v_add_u32 v128, 1, v128\n\t"
        "ds_read_b64_tr_b16 v[100:101], v130 offset:0\n\t"
        "ds_read_b64_tr_b16 v[102:103], v130 offset:3072\n\t"
        "s_waitcnt lgkmcnt(10)\n\t"
        "v_mfma_f32_32x32x16_bf16 v[32:47], v[104:107], v[168:171], v[32:47]\n\t"
        "ds_read_b64_tr_b16 v[104:105], v129 offset:128\n\t"
        "ds_read_b64_tr_b16 v[106:107], v129 offset:3200\n\t"
        "s_waitcnt lgkmcnt(10)\n\t"
        "v_mfma_f32_32x32x16_bf16 v[48:63], v[108:111], v[168:171], v[48:63]\n\t"
        "s_add_u32 s46, s46, 12288\n\t"
        "s_add_u32 s47, s47, 1\n\t"
        "ds_read_b64_tr_b16 v[108:109], v130 offset:128\n\t"
        "ds_read_b64_tr_b16 v[110:111], v130 offset:3200\n\t"
        "s_waitcnt lgkmcnt(10)\n\t"
        "v_mfma_f32_32x32x16_bf16 v[64:79], v[168:171], v[112:115], v[64:79]\n\t"
        "s_cmp_eq_u32 s46, 110592\n\t"
        "s_cselect_b32 s46, 0, s46\n\t"
        "ds_read_b64_tr_b16 v[112:113], v129 offset:256\n\t"
        "ds_read_b64_tr_b16 v[114:115], v129 offset:3328\n\t"
        "s_waitcnt lgkmcnt(10)\n\t"
        "v_mfma_f32_32x32x16_bf16 v[80:95], v[168:171], v[116:119], v[80:95]\n\t"
        "ds_write_b32 v127, v128\n\t"
        "ds_read_b64_tr_b16 v[116:117], v130 offset:256\n\t"
        "ds_read_b64_tr_b16 v[118:119], v130 offset:3328\n\t"
        "s_waitcnt vmcnt(8) lgkmcnt(11)\n\t"
        "v_mfma_f32_32x32x16_bf16 v[0:15], v[96:99], v[172:175], v[0:15]\n\t"
        "ds_read_b32 v126, v125\n\t"
        "ds_read_b64_tr_b16 v[96:97], v129 offset:6144\n\t"
        "ds_read_b64_tr_b16 v[98:99], v129 offset:9216\n\t"
        "s_waitcnt lgkmcnt(12)\n\t"
        "v_mfma_f32_32x32x16_bf16 v[16:31], v[100:103], v[172:175], v[16:31]\n\t"
        "v_add_u32 v123, s46, v121\n\t"
        "ds_read_b64_tr_b16 v[100:101], v130 offset:6144\n\t"
        "ds_read_b64_tr_b16 v[102:103], v130 offset:9216\n\t"
        "s_waitcnt lgkmcnt(12)\n\t"
        "v_mfma_f32_32x32x16_bf16 v[32:47], v[104:107], v[172:175], v[32:47]\n\t"
        "ds_read_b64_tr_b16 v[104:105], v129 offset:6272\n\t"
        "ds_read_b64_tr_b16 v[106:107], v129 offset:9344\n\t"
        "s_waitcnt lgkmcnt(12)\n\t"
        "v_mfma_f32_32x32x16_bf16 v[48:63], v[108:111], v[172:175], v[48:63]\n\t"
        "v_add_u32 v124, s46, v122\n\t"
        "ds_read_b64_tr_b16 v[108:109], v130 offset:6272\n\t"
        "ds_read_b64_tr_b16 v[110:111], v130 offset:9344\n\t"
        "s_waitcnt lgkmcnt(12)\n\t"
        "v_mfma_f32_32x32x16_bf16 v[64:79], v[172:175], v[112:115], v[64:79]\n\t"
        "ds_read_b64_tr_b16 v[112:113], v129 offset:6400\n\t"
        "ds_read_b64_tr_b16 v[114:115], v129 offset:9472\n\t"
        "s_waitcnt lgkmcnt(10)\n\t"
        "v_mfma_f32_32x32x16_bf16 v[80:95], v[172:175], v[116:119], v[80:95]\n\t"
        "v_cmp_gt_u32 vcc, s47, v126\n\t"
        "ds_read_b64_tr_b16 v[116:117], v130 offset:6400\n\t"
        "ds_read_b64_tr_b16 v[118:119], v130 offset:9472\n\t"
        "s_waitcnt vmcnt(7) lgkmcnt(10)\n\t"
        "v_mfma_f32_32x32x16_bf16 v[0:15], v[96:99], v[176:179], v[0:15]\n\t"
        "s_cbranch_vccnz 70f\n\t"
        "71:\n\t"
        "ds_read_b64_tr_b16 v[96:97], v123 offset:0\n\t"
        "ds_read_b64_tr_b16 v[98:99], v123 offset:3072\n\t"
        "s_waitcnt lgkmcnt(10)\n\t"
        "v_mfma_f32_32x32x16_bf16 v[16:31], v[100:103], v[176:179], v[16:31]\n\t"
        "v_add_u32 v128, 1, v128\n\t"
        "ds_read_b64_tr_b16 v[100:101], v124 offset:0\n\t"
        "ds_read_b64_tr_b16 v[102:103], v124 offset:3072\n\t"
        "s_waitcnt lgkmcnt(10)\n\t"
        "v_mfma_f32_32x32x16_bf16 v[32:47], v[104:107], v[176:179], v[32:47]\n\t"
        "ds_read_b64_tr_b16 v[104:105], v123 offset:128\n\t"
        "ds_read_b64_tr_b16 v[106:107], v123 offset:3200\n\t"
        "s_waitcnt lgkmcnt(10)\n\t"
        "v_mfma_f32_32x32x16_bf16 v[48:63], v[108:111], v[176:179], v[48:63]\n\t"
        "s_add_u32 s46, s46, 12288\n\t"
        "s_add_u32 s47, s47, 1\n\t"
        "ds_read_b64_tr_b16 v[108:109], v124 offset:128\n\t"
        "ds_read_b64_tr_b16 v[110:111], v124 offset:3200\n\t"
        "s_waitcnt lgkmcnt(10)\n\t"
        "v_mfma_f32_32x32x16_bf16 v[64:79], v[176:179], v[112:115], v[64:79]\n\t"
        "s_cmp_eq_u32 s46, 110592\n\t"
        "s_cselect_b32 s46, 0, s46\n\t"
        "ds_read_b64_tr_b16 v[112:113], v123 offset:256\n\t"
        "ds_read_b64_tr_b16 v[114:115], v123 offset:3328\n\t"
        "s_waitcnt lgkmcnt(10)\n\t"
        "v_mfma_f32_32x32x16_bf16 v[80:95], v[176:179], v[116:119], v[80:95]\n\t"
        "ds_write_b32 v127, v128\n\t"
        "ds_read_b64_tr_b16 v[116:117], v124 offset:256\n\t"
        "ds_read_b64_tr_b16 v[118:119], v124 offset:3328\n\t"
        "s_waitcnt vmcnt(6) lgkmcnt(11)\n\t"
        "v_mfma_f32_32x32x16_bf16 v[0:15], v[96:99], v[180:183], v[0:15]\n\t"
        "ds_read_b32 v126, v125\n\t"
        "ds_read_b64_tr_b16 v[96:97], v123 offset:6144\n\t"
        "ds_read_b64_tr_b16 v[98:99], v123 offset:9216\n\t"
        "s_waitcnt lgkmcnt(12)\n\t"
        "v_mfma_f32_32x32x16_bf16 v[16:31], v[100:103], v[180:183], v[16:31]\n\t"
        "v_add_u32 v129, s46, v121\n\t"
        "ds_read_b64_tr_b16 v[100:101], v124 offset:6144\n\t"
        "ds_read_b64_tr_b16 v[102:103], v124 offset:9216\n\t"
        "s_waitcnt lgkmcnt(12)\n\t"
        "v_mfma_f32_32x32x16_bf16 v[32:47], v[104:107], v[180:183], v[32:47]\n\t"
        "ds_read_b64_tr_b16 v[104:105], v123 offset:6272\n\t"
        "ds_read_b64_tr_b16 v[106:107], v123 offset:9344\n\t"
        "s_waitcnt lgkmcnt(12)\n\t"
        "v_mfma_f32_32x32x16_bf16 v[48:63], v[108:111], v[180:183], v[48:63]\n\t"
        "v_add_u32 v130, s46, v122\n\t"
        "ds_read_b64_tr_b16 v[108:109], v124 offset:6272\n\t"
        "ds_read_b64_tr_b16 v[110:111], v124 offset:9344\n\t"
        "s_waitcnt lgkmcnt(12)\n\t"
        "v_mfma_f32_32x32x16_bf16 v[64:79], v[180:183], v[112:115], v[64:79]\n\t"
        "ds_read_b64_tr_b16 v[112:113], v123 offset:6400\n\t"
        "ds_read_b64_tr_b16 v[114:115], v123 offset:9472\n\t"
        "s_waitcnt lgkmcnt(10)\n\t"
        "v_mfma_f32_32x32x16_bf16 v[80:95], v[180:183], v[116:119], v[80:95]\n\t"
        "v_cmp_gt_u32 vcc, s47, v126\n\t"
        "ds_read_b64_tr_b16 v[116:117], v124 offset:6400\n\t"
        "ds_read_b64_tr_b16 v[118:119], v124 offset:9472\n\t"
        "s_waitcnt vmcnt(5) lgkmcnt(10)\n\t"
        "v_mfma_f32_32x32x16_bf16 v[0:15], v[96:99], v[184:187], v[0:15]\n\t"
        "s_cbranch_vccnz 72f\n\t"
        "73:\n\t"
        "ds_read_b64_tr_b16 v[96:97], v129 offset:0\n\t"
        "ds_read_b64_tr_b16 v[98:99], v129 offset:3072\n\t"
        "s_waitcnt lgkmcnt(10)\n\t"
        "v_mfma_f32_32x32x16_bf16 v[16:31], v[100:103], v[184:187], v[16:31]\n\t"
        "v_add_u32 v128, 1, v128\n\t"
        "ds_read_b64_tr_b16 v[100:101], v130 offset:0\n\t"
        "ds_read_b64_tr_b16 v[102:103], v130 offset:3072\n\t"
        "s_waitcnt lgkmcnt(10)\n\t"
        "v_mfma_f32_32x32x16_bf16 v[32:47], v[104:107], v[184:187], v[32:47]\n\t"
        "ds_read_b64_tr_b16 v[104:105], v129 offset:128\n\t"
        "ds_read_b64_tr_b16 v[106:107], v129 offset:3200\n\t"
        "s_waitcnt lgkmcnt(10)\n\t"
        "v_mfma_f32_32x32x16_bf16 v[48:63], v[108:111], v[184:187], v[48:63]\n\t"
        "s_add_u32 s46, s46, 12288\n\t"
        "s_add_u32 s47, s47, 1\n\t"
        "ds_read_b64_tr_b16 v[108:109], v130 offset:128\n\t"
        "ds_read_b64_tr_b16 v[110:111], v130 offset:3200\n\t"
        "s_waitcnt lgkmcnt(10)\n\t"
        "v_mfma_f32_32x32x16_bf16 v[64:79], v[184:187], v[112:115], v[64:79]\n\t"
        "s_cmp_eq_u32 s46, 110592\n\t"
        "s_cselect_b32 s46, 0, s46\n\t"
        "ds_read_b64_tr_b16 v[112:113], v129 offset:256\n\t"
        "ds_read_b64_tr_b16 v[114:115], v129 offset:3328\n\t"
        "s_waitcnt lgkmcnt(10)\n\t"
        "v_mfma_f32_32x32x16_bf16 v[80:95], v[184:187], v[116:119], v[80:95]\n\t"
        "ds_write_b32 v127, v128\n\t"
        "ds_read_b64_tr_b16 v[116:117], v130 offset:256\n\t"
        "ds_read_b64_tr_b16 v[118:119], v130 offset:3328\n\t"
        "s_waitcnt vmcnt(4) lgkmcnt(11)\n\t"
        "v_mfma_f32_32x32x16_bf16 v[0:15], v[96:99], v[188:191], v[0:15]\n\t"
        "ds_read_b64_tr_b16 v[96:97], v129 offset:6144\n\t"
        "ds_read_b64_tr_b16 v[98:99], v129 offset:9216\n\t"
        "s_waitcnt lgkmcnt(11)\n\t"
        "v_mfma_f32_32x32x16_bf16 v[16:31], v[100:103], v[188:191], v[16:31]\n\t"
        "ds_read_b64_tr_b16 v[100:101], v130 offset:6144\n\t"
        "ds_read_b64_tr_b16 v[102:103], v130 offset:9216\n\t"
        "s_waitcnt lgkmcnt(11)\n\t"
        "v_mfma_f32_32x32x16_bf16 v[32:47], v[104:107], v[188:191], v[32:47]\n\t"
        "ds_read_b64_tr_b16 v[104:105], v129 offset:6272\n\t"
        "ds_read_b64_tr_b16 v[106:107], v129 offset:9344\n\t"
        "s_waitcnt lgkmcnt(11)\n\t"
        "v_mfma_f32_32x32x16_bf16 v[48:63], v[108:111], v[188:191], v[48:63]\n\t"
        "ds_read_b64_tr_b16 v[108:109], v130 offset:6272\n\t"
        "ds_read_b64_tr_b16 v[110:111], v130 offset:9344\n\t"
        "s_waitcnt lgkmcnt(11)\n\t"
        "v_mfma_f32_32x32x16_bf16 v[64:79], v[188:191], v[112:115], v[64:79]\n\t"
        "ds_read_b64_tr_b16 v[112:113], v129 offset:6400\n\t"
        "ds_read_b64_tr_b16 v[114:115], v129 offset:9472\n\t"
        "s_waitcnt lgkmcnt(10)\n\t"
        "v_mfma_f32_32x32x16_bf16 v[80:95], v[188:191], v[116:119], v[80:95]\n\t"
        "ds_read_b64_tr_b16 v[116:117], v130 offset:6400\n\t"
        "ds_read_b64_tr_b16 v[118:119], v130 offset:9472\n\t"
        "s_waitcnt vmcnt(3) lgkmcnt(10)\n\t"
        "v_mfma_f32_32x32x16_bf16 v[0:15], v[96:99], v[192:195], v[0:15]\n\t"
        "s_waitcnt lgkmcnt(8)\n\t"
        "v_mfma_f32_32x32x16_bf16 v[16:31], v[100:103], v[192:195], v[16:31]\n\t"
        "s_waitcnt lgkmcnt(6)\n\t"
        "v_mfma_f32_32x32x16_bf16 v[32:47], v[104:107], v[192:195], v[32:47]\n\t"
        "s_waitcnt lgkmcnt(4)\n\t"
        "v_mfma_f32_32x32x16_bf16 v[48:63], v[108:111], v[192:195], v[48:63]\n\t"
        "s_waitcnt lgkmcnt(2)\n\t"
        "v_mfma_f32_32x32x16_bf16 v[64:79], v[192:195], v[112:115], v[64:79]\n\t"
        "s_waitcnt lgkmcnt(0)\n\t"
        "v_mfma_f32_32x32x16_bf16 v[80:95], v[192:195], v[116:119], v[80:95]\n\t"
        "s_waitcnt vmcnt(0)\n\t"
        "s_nop 10\n\t"
        "s_branch 9f\n\t"
        "16:\n\t"
        "s_mov_b32 s48, 0x10000\n\t"
        "8:\n\t"
        "ds_read_b32 v126, v125\n\t"
        "s_sub_u32 s48, s48, 1\n\t"
        "s_waitcnt lgkmcnt(0)\n\t"
        "s_cbranch_scc1 17b\n\t"
        "v_cmp_gt_u32 vcc, s47, v126\n\t"
        "s_cbranch_vccnz 8b\n\t"
        "s_branch 17b\n\t"
        "18:\n\t"
        "s_mov_b32 s48, 0x10000\n\t"
        "8:\n\t"
        "ds_read_b32 v126, v125\n\t"
        "s_sub_u32 s48, s48, 1\n\t"
        "s_waitcnt lgkmcnt(0)\n\t"
        "s_cbranch_scc1 19b\n\t"
        "v_cmp_gt_u32 vcc, s47, v126\n\t"
        "s_cbranch_vccnz 8b\n\t"
        "s_branch 19b\n\t"
        "20:\n\t"
        "s_mov_b32 s48, 0x10000\n\t"
        "8:\n\t"
        "ds_read_b32 v126, v125\n\t"
        "s_sub_u32 s48, s48, 1\n\t"
        "s_waitcnt lgkmcnt(0)\n\t"
        "s_cbranch_scc1 21b\n\t"
        "v_cmp_gt_u32 vcc, s47, v126\n\t"
        "s_cbranch_vccnz 8b\n\t"
        "s_branch 21b\n\t"
        "22:\n\t"
        "s_mov_b32 s48, 0x10000\n\t"
        "8:\n\t"
        "ds_read_b32 v126, v125\n\t"
        "s_sub_u32 s48, s48, 1\n\t"
        "s_waitcnt lgkmcnt(0)\n\t"
        "s_cbranch_scc1 23b\n\t"
        "v_cmp_gt_u32 vcc, s47, v126\n\t"
        "s_cbranch_vccnz 8b\n\t"
        "s_branch 23b\n\t"
        "24:\n\t"
        "s_mov_b32 s48, 0x10000\n\t"
        "8:\n\t"
        "ds_read_b32 v126, v125\n\t"
        "s_sub_u32 s48, s48, 1\n\t"
        "s_waitcnt lgkmcnt(0)\n\t"
        "s_cbranch_scc1 25b\n\t"
        "v_cmp_gt_u32 vcc, s47, v126\n\t"
        "s_cbranch_vccnz 8b\n\t"
        "s_branch 25b\n\t"
        "26:\n\t"
        "s_mov_b32 s48, 0x10000\n\t"
        "8:\n\t"
        "ds_read_b32 v126, v125\n\t"
        "s_sub_u32 s48, s48, 1\n\t"
        "s_waitcnt lgkmcnt(0)\n\t"
        "s_cbranch_scc1 27b\n\t"
        "v_cmp_gt_u32 vcc, s47, v126\n\t"
        "s_cbranch_vccnz 8b\n\t"
        "s_branch 27b\n\t"
        "28:\n\t"
        "s_mov_b32 s48, 0x10000\n\t"
        "8:\n\t"
        "ds_read_b32 v126, v125\n\t"
        "s_sub_u32 s48, s48, 1\n\t"
        "s_waitcnt lgkmcnt(0)\n\t"
        "s_cbranch_scc1 29b\n\t"
        "v_cmp_gt_u32 vcc, s47, v126\n\t"
        "s_cbranch_vccnz 8b\n\t"
        "s_branch 29b\n\t"
        "30:\n\t"
        "s_mov_b32 s48, 0x10000\n\t"
        "8:\n\t"
        "ds_read_b32 v126, v125\n\t"
        "s_sub_u32 s48, s48, 1\n\t"
        "s_waitcnt lgkmcnt(0)\n\t"
        "s_cbranch_scc1 31b\n\t"
        "v_cmp_gt_u32 vcc, s47, v126\n\t"
        "s_cbranch_vccnz 8b\n\t"
        "s_branch 31b\n\t"
        "64:\n\t"
        "s_mov_b32 s48, 0x10000\n\t"
        "8:\n\t"
        "ds_read_b32 v126, v125\n\t"
        "s_sub_u32 s48, s48, 1\n\t"
        "s_waitcnt lgkmcnt(0)\n\t"
        "s_cbranch_scc1 65b\n\t"
        "v_cmp_gt_u32 vcc, s47, v126\n\t"
        "s_cbranch_vccnz 8b\n\t"
        "s_branch 65b\n\t"
        "66:\n\t"
        "s_mov_b32 s48, 0x10000\n\t"
        "8:\n\t"
        "ds_read_b32 v126, v125\n\t"
        "s_sub_u32 s48, s48, 1\n\t"
        "s_waitcnt lgkmcnt(0)\n\t"
        "s_cbranch_scc1 67b\n\t"
        "v_cmp_gt_u32 vcc, s47, v126\n\t"
        "s_cbranch_vccnz 8b\n\t"
        "s_branch 67b\n\t"
        "68:\n\t"
        "s_mov_b32 s48, 0x10000\n\t"
        "8:\n\t"
        "ds_read_b32 v126, v125\n\t"
        "s_sub_u32 s48, s48, 1\n\t"
        "s_waitcnt lgkmcnt(0)\n\t"
        "s_cbranch_scc1 69b\n\t"
        "v_cmp_gt_u32 vcc, s47, v126\n\t"
        "s_cbranch_vccnz 8b\n\t"
        "s_branch 69b\n\t"
        "70:\n\t"
        "s_mov_b32 s48, 0x10000\n\t"
        "8:\n\t"
        "ds_read_b32 v126, v125\n\t"
        "s_sub_u32 s48, s48, 1\n\t"
        "s_waitcnt lgkmcnt(0)\n\t"
        "s_cbranch_scc1 71b\n\t"
        "v_cmp_gt_u32 vcc, s47, v126\n\t"
        "s_cbranch_vccnz 8b\n\t"
        "s_branch 71b\n\t"
        "72:\n\t"
        "s_mov_b32 s48, 0x10000\n\t"
        "8:\n\t"
        "ds_read_b32 v126, v125\n\t"
        "s_sub_u32 s48, s48, 1\n\t"
        "s_waitcnt lgkmcnt(0)\n\t"
        "s_cbranch_scc1 73b\n\t"
        "v_cmp_gt_u32 vcc, s47, v126\n\t"
        "s_cbranch_vccnz 8b\n\t"
        "s_branch 73b\n\t"
        "9:\n\t"
        : "+{v[0:15]}"(acc[0]), "+{v[16:31]}"(acc[1]), "+{v[32:47]}"(acc[2]), "+{v[48:63]}"(acc[3]), "+{v[64:79]}"(acc[4]), "+{v[80:95]}"(acc[5]),
          "+{v[96:111]}"(stA), "+{v[112:127]}"(stB), "+{v[128:131]}"(stC), "+{v[148:163]}"(xr1), "+{v[164:179]}"(xr2), "+{v[180:195]}"(xr3),
          "+{s44}"(soff), "+{s46}"(slot), "+{s47}"(need)
        : "{s[40:43]}"(srd) : "v132", "v133", "v134", "v135", "v136", "v137", "v138", "v139", "v140", "v141", "v142", "v143", "v144", "v145", "v146", "v147", "s45", "s48", "vcc", "scc", "memory");
      STAMPV(4 + 16 * L);
      PHASE_VARS;
      if (L == 1) {
        f32x4 av[4], bv[4], cs[4][4];
        float cv[2];
#pragma unroll
        for (int g = 0; g < 4; ++g) {
          av[g] = *reinterpret_cast<const f32x4*>(smem + L_AB + wid * 256 + (8 * g + 4 * hi) * 4);
          bv[g] = *reinterpret_cast<const f32x4*>(smem + L_AB + wid * 256 + 128 + (8 * g + 4 * hi) * 4);
        }
#pragma unroll
        for (int j = 5; j >= 0; --j) {
          const int cb = (j >> 1) * 64 + ((j & 1) ^ hh) * 32;
          if (j >= 4) cv[j - 4] = *reinterpret_cast<const float*>(smem + L_CS + (cb + r32) * 4);
          else {
#pragma unroll
            for (int g = 0; g < 4; ++g) cs[j][g] = *reinterpret_cast<const f32x4*>(smem + L_CS + (cb + 8 * g + 4 * hi) * 4);
          }
        }
        SBAR();
#pragma unroll
        for (int j = 5; j >= 0; --j)
#pragma unroll
          for (int g = 0; g < 4; ++g)
#pragma unroll
            for (int e = 0; e < 4; ++e)
              acc[j][4 * g + e] = j < 4 ? al * acc[j][4 * g + e] + be * cs[j & 3][g][e] : av[g][e] * acc[j][4 * g + e] + bv[g][e] * cv[j & 1];
        SBAR();
        lds_wait_ge(lbase + L_W + 4 + 4 * (lane & 1), 1);
        const int idx = (int)__builtin_amdgcn_readfirstlane(words[3]);
        const bool mine = (idx >> 5) == tile;
        if (mine) {
#pragma unroll
          for (int j = 0; j < 6; ++j) {
            const int cb = (j >> 1) * 64 + ((j & 1) ^ hh) * 32;
#pragma unroll
            for (int g = 0; g < 4; ++g) {
              if (j < 4) {
                const f32x4 pv = *reinterpret_cast<const f32x4*>(smem + L_PATCH + (cb + 8 * g + 4 * hi) * 4);
#pragma unroll
                for (int e = 0; e < 4; ++e) acc[j][4 * g + e] = r32 == (idx & 31) ? pv[e] : acc[j][4 * g + e];
              } else {
                const float pv = *reinterpret_cast<const float*>(smem + L_PATCH + (cb + r32) * 4);
#pragma unroll
                for (int e = 0; e < 4; ++e) acc[j][4 * g + e] = (8 * g + 4 * hi + e) == (idx & 31) ? pv : acc[j][4 * g + e];
              }
            }
          }
        }
      }
      constexpr float QS = 0.17677669529663687f * 1.4426950408889634f;
      u32x4 own[4];
#pragma unroll
      for (int j = 5; j >= 0; --j) {
        u32x4 o[2];
#pragma unroll
        for (int e = 0; e < 8; ++e) {
          unsigned w = pack2(acc[j][2 * e], acc[j][2 * e + 1]);
          if (j < 2) w = pack2(lo16(w) * QS, hi16(w) * QS);
          o[e >> 2][e & 3] = w;
        }
        if (j >= 4) { o[0] = fo_swap(o[0]); o[1] = fo_swap(o[1]); }
        const int hs = (j & 1) ^ hh;
        if (j >= 2) {
          const unsigned dst = OFF_KV + L * (8 * MBY) + ((tn * 2 + hs) * 64 + tile) * 4096 + ((j >> 1) - 1) * 2048 + lane * 16;
          stg(rs, dst, o[0]); stg(rs, dst + 1024, o[1]);
        }
        if (j >= 2 && !(j & 1)) { own[j - 2] = o[0]; own[j - 1] = o[1]; }
        else {
          char* p = smem + L_XCH + (j < 2 ? (hs * 4 + wid) * 2048 : 16384 + wid * 4096 + ((j >> 1) - 1) * 2048) + lane * 16;
          *reinterpret_cast<u32x4*>(p) = o[0]; *reinterpret_cast<u32x4*>(p + 1024) = o[1];
        }
        SBAR();
      }
      if (wid == 0) words[0] = 0u;
      BAR_LGKM();
      kva = cat4(own[0], own[1], own[2], own[3]);
      const u32x4* pk = reinterpret_cast<const u32x4*>(smem + L_XCH + 16384 + (wid ^ 1) * 4096 + lane * 16);
#pragma unroll
      for (int u = 0; u < 4; ++u) {
        const u32x4* p = reinterpret_cast<const u32x4*>(smem + L_XCH + (hh * 4 + ((2 * kh + 2 + u) & 3)) * 2048 + lane * 16);
        q[u] = __builtin_shufflevector(p[0], p[64], 0, 1, 2, 3, 4, 5, 6, 7);
      }
      kvb = cat4(pk[0], pk[64], pk[128], pk[192]);
    }
    STAMPV(5 + 16 * L);
    {
      u32x4 ones;
      unsigned kvoff;
      u32x8 fv;
      asm volatile("" : "={v[184:191]}"(fv));
      { PHASE_VARS;
        const unsigned fl = FL_KV + (L * 16 + tn) * 8192;
        const unsigned v = (lane & 3) == 0 ? 0x3F803F80u : 0u; const bool up = (lane & 4) != 0; ones[0] = ones[1] = up ? 0u : v; ones[2] = ones[3] = up ? v : 0u;
        kvoff = OFF_KV + L * (8 * MBY) + (tn * 2 + hh) * (64 * 4096) + kh * 8192 + lane * 16;
        fv[0] = lane == 0 ? fl + (kh * 32 + tm * 2 + hh) * 128 : OOB;
        fv[1] = fl + (hi ? (kh ^ 1) * 32 + tm * 2 + (lane & 1) : kh * 32 + r32) * 128;
        fv[2] = TAG; fv[3] = lbase + L_FREE + wid * 256 + lane * 4; fv[4] = 32 * L + 32; }
      f32x16 o0, o1, o2, o3, lv;
      unsigned soff = tm * 16384;
      STAMP_DECL;
      asm volatile(
        "s_nop 1\n\t"
        "v_mfma_f32_32x32x16_bf16 v[96:111], v[152:155], v[64:67], 0\n\t"
        "v_mfma_f32_32x32x16_bf16 v[96:111], v[156:159], v[68:71], v[96:111]\n\t"
        "v_mov_b32 v0, 0\n\t"
        "v_mov_b32 v1, 0\n\t"
        "v_mov_b32 v2, 0\n\t"
        "v_mov_b32 v3, 0\n\t"
        "v_mov_b32 v4, 0\n\t"
        "v_mov_b32 v5, 0\n\t"
        "v_mov_b32 v6, 0\n\t"
        "v_mov_b32 v7, 0\n\t"
        "v_mov_b32 v8, 0\n\t"
        "v_mov_b32 v9, 0\n\t"
        "v_mov_b32 v10, 0\n\t"
        "v_mov_b32 v11, 0\n\t"
        "v_mov_b32 v12, 0\n\t"
        "v_mov_b32 v13, 0\n\t"
        "v_mov_b32 v14, 0\n\t"
        "v_mov_b32 v15, 0\n\t"
        "v_mov_b32 v16, 0\n\t"
        "v_mov_b32 v17, 0\n\t"
        "v_mov_b32 v18, 0\n\t"
        "v_mov_b32 v19, 0\n\t"
        "v_mov_b32 v20, 0\n\t"
        "v_mov_b32 v21, 0\n\t"
        "v_mov_b32 v22, 0\n\t"
        "v_mov_b32 v23, 0\n\t"
        "v_mov_b32 v24, 0\n\t"
        "v_mov_b32 v25, 0\n\t"
        "v_mov_b32 v26, 0\n\t"
        "v_mov_b32 v27, 0\n\t"
        "v_mov_b32 v28, 0\n\t"
        "v_mov_b32 v29, 0\n\t"
        "v_mov_b32 v30, 0\n\t"
        "v_mov_b32 v31, 0\n\t"
        "v_mov_b32 v32, 0\n\t"
        "v_mov_b32 v33, 0\n\t"
        "v_mov_b32 v34, 0\n\t"
        "v_mov_b32 v35, 0\n\t"
        "v_mov_b32 v36, 0\n\t"
        "v_mov_b32 v37, 0\n\t"
        "v_mov_b32 v38, 0\n\t"
        "v_mov_b32 v39, 0\n\t"
        "v_mov_b32 v40, 0\n\t"
        "v_mov_b32 v41, 0\n\t"
        "v_mov_b32 v42, 0\n\t"
        "v_mov_b32 v43, 0\n\t"
        "v_mov_b32 v44, 0\n\t"
        "v_mov_b32 v45, 0\n\t"
        "v_mov_b32 v46, 0\n\t"
        "v_mov_b32 v47, 0\n\t"
        "v_mov_b32 v48, 0\n\t"
        "v_mov_b32 v49, 0\n\t"
        "v_mov_b32 v50, 0\n\t"
        "v_mov_b32 v51, 0\n\t"
        "v_mov_b32 v52, 0\n\t"
        "v_mov_b32 v53, 0\n\t"
        "v_mov_b32 v54, 0\n\t"
        "v_mov_b32 v55, 0\n\t"
        "v_mov_b32 v56, 0\n\t"
        "v_mov_b32 v57, 0\n\t"
        "v_mov_b32 v58, 0\n\t"
        "v_mov_b32 v59, 0\n\t"
        "v_mov_b32 v60, 0\n\t"
        "v_mov_b32 v61, 0\n\t"
        "v_mov_b32 v62, 0\n\t"
        "v_mov_b32 v63, 0\n\t"
        "v_mov_b32 v128, 0\n\t"
        "v_mov_b32 v129, 0\n\t"
        "v_mov_b32 v130, 0\n\t"
        "v_mov_b32 v131, 0\n\t"
        "v_mov_b32 v132, 0\n\t"
        "v_mov_b32 v133, 0\n\t"
        "v_mov_b32 v134, 0\n\t"
        "v_mov_b32 v135, 0\n\t"
        "v_mov_b32 v136, 0\n\t"
        "v_mov_b32 v137, 0\n\t"
        "v_mov_b32 v138, 0\n\t"
        "v_mov_b32 v139, 0\n\t"
        "v_mov_b32 v140, 0\n\t"
        "v_mov_b32 v141, 0\n\t"
        "v_mov_b32 v142, 0\n\t"
        "v_mov_b32 v143, 0\n\t"
        "v_exp_f32 v96, v96\n\t"
        "v_exp_f32 v97, v97\n\t"
        "v_exp_f32 v98, v98\n\t"
        "v_exp_f32 v99, v99\n\t"
        "v_exp_f32 v100, v100\n\t"
        "v_exp_f32 v101, v101\n\t"
        "v_mfma_f32_32x32x16_bf16 v[112:127], v[152:155], v[72:75], 0\n\t"
        "v_exp_f32 v102, v102\n\t"
        "v_exp_f32 v103, v103\n\t"
        "v_exp_f32 v104, v104\n\t"
        "v_mfma_f32_32x32x16_bf16 v[112:127], v[156:159], v[76:79], v[112:127]\n\t"
        "v_exp_f32 v105, v105\n\t"
        "v_exp_f32 v106, v106\n\t"
        "v_exp_f32 v107, v107\n\t"
        "v_smfmac_f32_32x32x32_bf16 v[0:15], v[160:163], v[96:103], v148\n\t"
        "v_exp_f32 v108, v108\n\t"
        "v_exp_f32 v109, v109\n\t"
        "v_exp_f32 v110, v110\n\t"
        "v_smfmac_f32_16x16x64_bf16 v[128:131], v[144:147], v[96:103], v148\n\t"
        "v_exp_f32 v111, v111\n\t"
        "v_exp_f32 v112, v112\n\t"
        "v_exp_f32 v113, v113\n\t"
        "v_smfmac_f32_32x32x32_bf16 v[0:15], v[164:167], v[104:111], v148\n\t"
        "v_exp_f32 v114, v114\n\t"
        "v_exp_f32 v115, v115\n\t"
        "v_exp_f32 v116, v116\n\t"
        "v_smfmac_f32_16x16x64_bf16 v[128:131], v[144:147], v[104:111], v148\n\t"
        "v_exp_f32 v117, v117\n\t"
        "v_mfma_f32_32x32x16_bf16 v[96:111], v[152:155], v[80:83], 0\n\t"
        "v_exp_f32 v118, v118\n\t"
        "v_exp_f32 v119, v119\n\t"
        "v_exp_f32 v120, v120\n\t"
        "v_mfma_f32_32x32x16_bf16 v[96:111], v[156:159], v[84:87], v[96:111]\n\t"
        "v_exp_f32 v121, v121\n\t"
        "v_exp_f32 v122, v122\n\t"
        "v_exp_f32 v123, v123\n\t"
        "v_smfmac_f32_32x32x32_bf16 v[16:31], v[160:163], v[112:119], v148\n\t"
        "v_exp_f32 v124, v124\n\t"
        "v_exp_f32 v125, v125\n\t"
        "v_exp_f32 v126, v126\n\t"
        "v_smfmac_f32_16x16x64_bf16 v[132:135], v[144:147], v[112:119], v148\n\t"
        "v_exp_f32 v127, v127\n\t"
        "v_exp_f32 v96, v96\n\t"
        "v_exp_f32 v97, v97\n\t"
        "v_smfmac_f32_32x32x32_bf16 v[16:31], v[164:167], v[120:127], v148\n\t"
        "v_exp_f32 v98, v98\n\t"
        "v_exp_f32 v99, v99\n\t"
        "v_exp_f32 v100, v100\n\t"
        "v_smfmac_f32_16x16x64_bf16 v[132:135], v[144:147], v[120:127], v148\n\t"
        "v_exp_f32 v101, v101\n\t"
        "v_mfma_f32_32x32x16_bf16 v[112:127], v[152:155], v[88:91], 0\n\t"
        "v_exp_f32 v102, v102\n\t"
        "v_exp_f32 v103, v103\n\t"
        "v_exp_f32 v104, v104\n\t"
        "v_mfma_f32_32x32x16_bf16 v[112:127], v[156:159], v[92:95], v[112:127]\n\t"
        "v_exp_f32 v105, v105\n\t"
        "v_exp_f32 v106, v106\n\t"
        "v_exp_f32 v107, v107\n\t"
        "v_smfmac_f32_32x32x32_bf16 v[32:47], v[160:163], v[96:103], v148\n\t"
        "v_exp_f32 v108, v108\n\t"
        "v_exp_f32 v109, v109\n\t"
        "v_exp_f32 v110, v110\n\t"
        "v_smfmac_f32_16x16x64_bf16 v[136:139], v[144:147], v[96:103], v148\n\t"
        "v_exp_f32 v111, v111\n\t"
        "v_exp_f32 v112, v112\n\t"
        "v_exp_f32 v113, v113\n\t"
        "v_smfmac_f32_32x32x32_bf16 v[32:47], v[164:167], v[104:111], v148\n\t"
        "v_exp_f32 v114, v114\n\t"
        "v_exp_f32 v115, v115\n\t"
        "v_exp_f32 v116, v116\n\t"
        "v_smfmac_f32_16x16x64_bf16 v[136:139], v[144:147], v[104:111], v148\n\t"
        "v_exp_f32 v117, v117\n\t"
        "v_mfma_f32_32x32x16_bf16 v[96:111], v[168:171], v[64:67], 0\n\t"
        "v_exp_f32 v118, v118\n\t"
        "v_exp_f32 v119, v119\n\t"
        "v_exp_f32 v120, v120\n\t"
        "v_mfma_f32_32x32x16_bf16 v[96:111], v[172:175], v[68:71], v[96:111]\n\t"
        "v_exp_f32 v121, v121\n\t"
        "v_exp_f32 v122, v122\n\t"
        "v_exp_f32 v123, v123\n\t"
        "v_smfmac_f32_32x32x32_bf16 v[48:63], v[160:163], v[112:119], v148\n\t"
        "v_exp_f32 v124, v124\n\t"
        "v_exp_f32 v125, v125\n\t"
        "v_exp_f32 v126, v126\n\t"
        "v_smfmac_f32_16x16x64_bf16 v[140:143], v[144:147], v[112:119], v148\n\t"
        "v_exp_f32 v127, v127\n\t"
        "s_waitcnt vmcnt(0)\n\t"
        "buffer_store_dword v186, v184, s[40:43], 0 offen sc1\n\t"
        "v_exp_f32 v96, v96\n\t"
        "v_exp_f32 v97, v97\n\t"
        "v_smfmac_f32_32x32x32_bf16 v[48:63], v[164:167], v[120:127], v148\n\t"
        "v_exp_f32 v98, v98\n\t"
        "v_exp_f32 v99, v99\n\t"
        "v_exp_f32 v100, v100\n\t"
        "v_smfmac_f32_16x16x64_bf16 v[140:143], v[144:147], v[120:127], v148\n\t"
        "v_exp_f32 v101, v101\n\t"
        "v_mfma_f32_32x32x16_bf16 v[112:127], v[168:171], v[72:75], 0\n\t"
        "v_exp_f32 v102, v102\n\t"
        "v_exp_f32 v103, v103\n\t"
        "v_exp_f32 v104, v104\n\t"
        "v_mfma_f32_32x32x16_bf16 v[112:127], v[172:175], v[76:79], v[112:127]\n\t"
        "v_exp_f32 v105, v105\n\t"
        "v_exp_f32 v106, v106\n\t"
        "v_exp_f32 v107, v107\n\t"
        "v_smfmac_f32_32x32x32_bf16 v[0:15], v[176:179], v[96:103], v148\n\t"
        "v_exp_f32 v108, v108\n\t"
        "v_exp_f32 v109, v109\n\t"
        "v_exp_f32 v110, v110\n\t"
        "v_smfmac_f32_16x16x64_bf16 v[128:131], v[144:147], v[96:103], v148\n\t"
        "v_exp_f32 v111, v111\n\t"
        "v_exp_f32 v112, v112\n\t"
        "v_exp_f32 v113, v113\n\t"
        "v_smfmac_f32_32x32x32_bf16 v[0:15], v[180:183], v[104:111], v148\n\t"
        "v_exp_f32 v114, v114\n\t"
        "v_exp_f32 v115, v115\n\t"
        "v_exp_f32 v116, v116\n\t"
        "v_smfmac_f32_16x16x64_bf16 v[128:131], v[144:147], v[104:111], v148\n\t"
        "v_exp_f32 v117, v117\n\t"
        "v_mfma_f32_32x32x16_bf16 v[96:111], v[168:171], v[80:83], 0\n\t"
        "v_exp_f32 v118, v118\n\t"
        "v_exp_f32 v119, v119\n\t"
        "v_exp_f32 v120, v120\n\t"
        "v_mfma_f32_32x32x16_bf16 v[96:111], v[172:175], v[84:87], v[96:111]\n\t"
        "v_exp_f32 v121, v121\n\t"
        "v_exp_f32 v122, v122\n\t"
        "v_exp_f32 v123, v123\n\t"
        "v_smfmac_f32_32x32x32_bf16 v[16:31], v[176:179], v[112:119], v148\n\t"
        "v_exp_f32 v124, v124\n\t"
        "v_exp_f32 v125, v125\n\t"
        "v_exp_f32 v126, v126\n\t"
        "v_smfmac_f32_16x16x64_bf16 v[132:135], v[144:147], v[112:119], v148\n\t"
        "v_exp_f32 v127, v127\n\t"
        "v_exp_f32 v96, v96\n\t"
        "v_exp_f32 v97, v97\n\t"
        "v_smfmac_f32_32x32x32_bf16 v[16:31], v[180:183], v[120:127], v148\n\t"
        "v_exp_f32 v98, v98\n\t"
        "v_exp_f32 v99, v99\n\t"
        "v_exp_f32 v100, v100\n\t"
        "v_smfmac_f32_16x16x64_bf16 v[132:135], v[144:147], v[120:127], v148\n\t"
        "v_exp_f32 v101, v101\n\t"
        "v_mfma_f32_32x32x16_bf16 v[112:127], v[168:171], v[88:91], 0\n\t"
        "v_exp_f32 v102, v102\n\t"
        "v_exp_f32 v103, v103\n\t"
        "v_exp_f32 v104, v104\n\t"
        "v_mfma_f32_32x32x16_bf16 v[112:127], v[172:175], v[92:95], v[112:127]\n\t"
        "v_exp_f32 v105, v105\n\t"
        "v_exp_f32 v106, v106\n\t"
        "v_exp_f32 v107, v107\n\t"
        "v_smfmac_f32_32x32x32_bf16 v[32:47], v[176:179], v[96:103], v148\n\t"
        "v_exp_f32 v108, v108\n\t"
        "v_exp_f32 v109, v109\n\t"
        "v_exp_f32 v110, v110\n\t"
        "v_smfmac_f32_16x16x64_bf16 v[136:139], v[144:147], v[96:103], v148\n\t"
        "v_exp_f32 v111, v111\n\t"
        "s_mov_b32 s45, 0x8000\n\t"
        "2:\n\t"
        "buffer_load_dword v151, v185, s[40:43], 0 offen sc1\n\t"
        "s_sub_u32 s45, s45, 1\n\t"
        "s_waitcnt vmcnt(0)\n\t"
        "s_cbranch_scc1 3f\n\t"
        "v_cmp_ne_u32 vcc, v186, v151\n\t"
        "s_cbranch_vccz 3f\n\t"
        "s_sleep 1\n\t"
        "s_branch 2b\n\t"
        "3:\n\t"
        "ds_write_b32 v187, v188\n\t"
        "s_add_u32 s44, s44, 0x4000\n\t"
        "s_and_b32 s44, s44, 0x3ffff\n\t"
        "buffer_load_dwordx4 v[184:187], v149, s[40:43], s44 offen offset:0 sc1\n\t"
        "buffer_load_dwordx4 v[188:191], v149, s[40:43], s44 offen offset:1024 sc1\n\t"
        "buffer_load_dwordx4 v[192:195], v149, s[40:43], s44 offen offset:2048 sc1\n\t"
        "buffer_load_dwordx4 v[196:199], v149, s[40:43], s44 offen offset:3072 sc1\n\t"
        "buffer_load_dwordx4 v[200:203], v150, s[40:43], s44 offen offset:0 sc1\n\t"
        "buffer_load_dwordx4 v[204:207], v150, s[40:43], s44 offen offset:1024 sc1\n\t"
        "buffer_load_dwordx4 v[208:211], v150, s[40:43], s44 offen offset:2048 sc1\n\t"
        "buffer_load_dwordx4 v[212:215], v150, s[40:43], s44 offen offset:3072 sc1\n\t"
        "s_add_u32 s44, s44, 0x4000\n\t"
        "s_and_b32 s44, s44, 0x3ffff\n\t"
        "buffer_load_dwordx4 v[152:155], v149, s[40:43], s44 offen offset:0 sc1\n\t"
        "buffer_load_dwordx4 v[156:159], v149, s[40:43], s44 offen offset:1024 sc1\n\t"
        "buffer_load_dwordx4 v[160:163], v149, s[40:43], s44 offen offset:2048 sc1\n\t"
        "buffer_load_dwordx4 v[164:167], v149, s[40:43], s44 offen offset:3072 sc1\n\t"
        "s_mov_b32 s45, 7\n\t"
        "1:\n\t"
        "v_exp_f32 v112, v112\n\t"
        "v_exp_f32 v113, v113\n\t"
        "v_smfmac_f32_32x32x32_bf16 v[32:47], v[180:183], v[104:111], v148\n\t"
        "v_exp_f32 v114, v114\n\t"
        "v_exp_f32 v115, v115\n\t"
        "v_exp_f32 v116, v116\n\t"
        "v_smfmac_f32_16x16x64_bf16 v[136:139], v[144:147], v[104:111], v148\n\t"
        "v_exp_f32 v117, v117\n\t"
        "s_waitcnt vmcnt(8)\n\t"
        "v_mfma_f32_32x32x16_bf16 v[96:111], v[184:187], v[64:67], 0\n\t"
        "v_exp_f32 v118, v118\n\t"
        "v_exp_f32 v119, v119\n\t"
        "v_exp_f32 v120, v120\n\t"
        "v_mfma_f32_32x32x16_bf16 v[96:111], v[188:191], v[68:71], v[96:111]\n\t"
        "v_exp_f32 v121, v121\n\t"
        "v_exp_f32 v122, v122\n\t"
        "v_exp_f32 v123, v123\n\t"
        "v_smfmac_f32_32x32x32_bf16 v[48:63], v[176:179], v[112:119], v148\n\t"
        "v_exp_f32 v124, v124\n\t"
        "v_exp_f32 v125, v125\n\t"
        "v_exp_f32 v126, v126\n\t"
        "v_smfmac_f32_16x16x64_bf16 v[140:143], v[144:147], v[112:119], v148\n\t"
        "buffer_load_dwordx4 v[168:171], v150, s[40:43], s44 offen offset:0 sc1\n\t"
        "v_exp_f32 v127, v127\n\t"
        "v_exp_f32 v96, v96\n\t"
        "v_exp_f32 v97, v97\n\t"
        "v_smfmac_f32_32x32x32_bf16 v[48:63], v[180:183], v[120:127], v148\n\t"
        "v_exp_f32 v98, v98\n\t"
        "v_exp_f32 v99, v99\n\t"
        "v_exp_f32 v100, v100\n\t"
        "v_smfmac_f32_16x16x64_bf16 v[140:143], v[144:147], v[120:127], v148\n\t"
        "v_exp_f32 v101, v101\n\t"
        "v_mfma_f32_32x32x16_bf16 v[112:127], v[184:187], v[72:75], 0\n\t"
        "v_exp_f32 v102, v102\n\t"
        "v_exp_f32 v103, v103\n\t"
        "v_exp_f32 v104, v104\n\t"
        "v_mfma_f32_32x32x16_bf16 v[112:127], v[188:191], v[76:79], v[112:127]\n\t"
        "v_exp_f32 v105, v105\n\t"
        "v_exp_f32 v106, v106\n\t"
        "v_exp_f32 v107, v107\n\t"
        "v_smfmac_f32_32x32x32_bf16 v[0:15], v[192:195], v[96:103], v148\n\t"
        "v_exp_f32 v108, v108\n\t"
        "v_exp_f32 v109, v109\n\t"
        "v_exp_f32 v110, v110\n\t"
        "v_smfmac_f32_16x16x64_bf16 v[128:131], v[144:147], v[96:103], v148\n\t"
        "buffer_load_dwordx4 v[172:175], v150, s[40:43], s44 offen offset:1024 sc1\n\t"
        "v_exp_f32 v111, v111\n\t"
        "v_exp_f32 v112, v112\n\t"
        "v_exp_f32 v113, v113\n\t"
        "v_smfmac_f32_32x32x32_bf16 v[0:15], v[196:199], v[104:111], v148\n\t"
        "v_exp_f32 v114, v114\n\t"
        "v_exp_f32 v115, v115\n\t"
        "v_exp_f32 v116, v116\n\t"
        "v_smfmac_f32_16x16x64_bf16 v[128:131], v[144:147], v[104:111], v148\n\t"
        "v_exp_f32 v117, v117\n\t"
        "v_mfma_f32_32x32x16_bf16 v[96:111], v[184:187], v[80:83], 0\n\t"
        "v_exp_f32 v118, v118\n\t"
        "v_exp_f32 v119, v119\n\t"
        "v_exp_f32 v120, v120\n\t"
        "v_mfma_f32_32x32x16_bf16 v[96:111], v[188:191], v[84:87], v[96:111]\n\t"
        "v_exp_f32 v121, v121\n\t"
        "v_exp_f32 v122, v122\n\t"
        "v_exp_f32 v123, v123\n\t"
        "v_smfmac_f32_32x32x32_bf16 v[16:31], v[192:195], v[112:119], v148\n\t"
        "v_exp_f32 v124, v124\n\t"
        "v_exp_f32 v125, v125\n\t"
        "v_exp_f32 v126, v126\n\t"
        "v_smfmac_f32_16x16x64_bf16 v[132:135], v[144:147], v[112:119], v148\n\t"
        "buffer_load_dwordx4 v[176:179], v150, s[40:43], s44 offen offset:2048 sc1\n\t"
        "v_exp_f32 v127, v127\n\t"
        "v_exp_f32 v96, v96\n\t"
        "v_exp_f32 v97, v97\n\t"
        "v_smfmac_f32_32x32x32_bf16 v[16:31], v[196:199], v[120:127], v148\n\t"
        "v_exp_f32 v98, v98\n\t"
        "v_exp_f32 v99, v99\n\t"
        "v_exp_f32 v100, v100\n\t"
        "v_smfmac_f32_16x16x64_bf16 v[132:135], v[144:147], v[120:127], v148\n\t"
        "v_exp_f32 v101, v101\n\t"
        "v_mfma_f32_32x32x16_bf16 v[112:127], v[184:187], v[88:91], 0\n\t"
        "v_exp_f32 v102, v102\n\t"
        "v_exp_f32 v103, v103\n\t"
        "v_exp_f32 v104, v104\n\t"
        "v_mfma_f32_32x32x16_bf16 v[112:127], v[188:191], v[92:95], v[112:127]\n\t"
        "v_exp_f32 v105, v105\n\t"
        "v_exp_f32 v106, v106\n\t"
        "v_exp_f32 v107, v107\n\t"
        "v_smfmac_f32_32x32x32_bf16 v[32:47], v[192:195], v[96:103], v148\n\t"
        "v_exp_f32 v108, v108\n\t"
        "v_exp_f32 v109, v109\n\t"
        "v_exp_f32 v110, v110\n\t"
        "v_smfmac_f32_16x16x64_bf16 v[136:139], v[144:147], v[96:103], v148\n\t"
        "buffer_load_dwordx4 v[180:183], v150, s[40:43], s44 offen offset:3072 sc1\n\t"
        "v_exp_f32 v111, v111\n\t"
        "v_exp_f32 v112, v112\n\t"
        "v_exp_f32 v113, v113\n\t"
        "v_smfmac_f32_32x32x32_bf16 v[32:47], v[196:199], v[104:111], v148\n\t"
        "s_add_u32 s44, s44, 0x4000\n\t"
        "s_and_b32 s44, s44, 0x3ffff\n\t"
        "v_exp_f32 v114, v114\n\t"
        "v_exp_f32 v115, v115\n\t"
        "v_exp_f32 v116, v116\n\t"
        "v_smfmac_f32_16x16x64_bf16 v[136:139], v[144:147], v[104:111], v148\n\t"
        "v_exp_f32 v117, v117\n\t"
        "s_waitcnt vmcnt(8)\n\t"
        "v_mfma_f32_32x32x16_bf16 v[96:111], v[200:203], v[64:67], 0\n\t"
        "v_exp_f32 v118, v118\n\t"
        "v_exp_f32 v119, v119\n\t"
        "v_exp_f32 v120, v120\n\t"
        "v_mfma_f32_32x32x16_bf16 v[96:111], v[204:207], v[68:71], v[96:111]\n\t"
        "v_exp_f32 v121, v121\n\t"
        "v_exp_f32 v122, v122\n\t"
        "v_exp_f32 v123, v123\n\t"
        "v_smfmac_f32_32x32x32_bf16 v[48:63], v[192:195], v[112:119], v148\n\t"
        "v_exp_f32 v124, v124\n\t"
        "v_exp_f32 v125, v125\n\t"
        "v_exp_f32 v126, v126\n\t"
        "v_smfmac_f32_16x16x64_bf16 v[140:143], v[144:147], v[112:119], v148\n\t"
        "buffer_load_dwordx4 v[184:187], v149, s[40:43], s44 offen offset:0 sc1\n\t"
        "v_exp_f32 v127, v127\n\t"
        "v_exp_f32 v96, v96\n\t"
        "v_exp_f32 v97, v97\n\t"
        "v_smfmac_f32_32x32x32_bf16 v[48:63], v[196:199], v[120:127], v148\n\t"
        "v_exp_f32 v98, v98\n\t"
        "v_exp_f32 v99, v99\n\t"
        "v_exp_f32 v100, v100\n\t"
        "v_smfmac_f32_16x16x64_bf16 v[140:143], v[144:147], v[120:127], v148\n\t"
        "v_exp_f32 v101, v101\n\t"
        "v_mfma_f32_32x32x16_bf16 v[112:127], v[200:203], v[72:75], 0\n\t"
        "v_exp_f32 v102, v102\n\t"
        "v_exp_f32 v103, v103\n\t"
        "v_exp_f32 v104, v104\n\t"
        "v_mfma_f32_32x32x16_bf16 v[112:127], v[204:207], v[76:79], v[112:127]\n\t"
        "v_exp_f32 v105, v105\n\t"
        "v_exp_f32 v106, v106\n\t"
        "v_exp_f32 v107, v107\n\t"
        "v_smfmac_f32_32x32x32_bf16 v[0:15], v[208:211], v[96:103], v148\n\t"
        "v_exp_f32 v108, v108\n\t"
        "v_exp_f32 v109, v109\n\t"
        "v_exp_f32 v110, v110\n\t"
        "v_smfmac_f32_16x16x64_bf16 v[128:131], v[144:147], v[96:103], v148\n\t"
        "buffer_load_dwordx4 v[188:191], v149, s[40:43], s44 offen offset:1024 sc1\n\t"
        "v_exp_f32 v111, v111\n\t"
        "v_exp_f32 v112, v112\n\t"
        "v_exp_f32 v113, v113\n\t"
        "v_smfmac_f32_32x32x32_bf16 v[0:15], v[212:215], v[104:111], v148\n\t"
        "v_exp_f32 v114, v114\n\t"
        "v_exp_f32 v115, v115\n\t"
        "v_exp_f32 v116, v116\n\t"
        "v_smfmac_f32_16x16x64_bf16 v[128:131], v[144:147], v[104:111], v148\n\t"
        "v_exp_f32 v117, v117\n\t"
        "v_mfma_f32_32x32x16_bf16 v[96:111], v[200:203], v[80:83], 0\n\t"
        "v_exp_f32 v118, v118\n\t"
        "v_exp_f32 v119, v119\n\t"
        "v_exp_f32 v120, v120\n\t"
        "v_mfma_f32_32x32x16_bf16 v[96:111], v[204:207], v[84:87], v[96:111]\n\t"
        "v_exp_f32 v121, v121\n\t"
        "v_exp_f32 v122, v122\n\t"
        "v_exp_f32 v123, v123\n\t"
        "v_smfmac_f32_32x32x32_bf16 v[16:31], v[208:211], v[112:119], v148\n\t"
        "v_exp_f32 v124, v124\n\t"
        "v_exp_f32 v125, v125\n\t"
        "v_exp_f32 v126, v126\n\t"
        "v_smfmac_f32_16x16x64_bf16 v[132:135], v[144:147], v[112:119], v148\n\t"
        "buffer_load_dwordx4 v[192:195], v149, s[40:43], s44 offen offset:2048 sc1\n\t"
        "v_exp_f32 v127, v127\n\t"
        "v_exp_f32 v96, v96\n\t"
        "v_exp_f32 v97, v97\n\t"
        "v_smfmac_f32_32x32x32_bf16 v[16:31], v[212:215], v[120:127], v148\n\t"
        "v_exp_f32 v98, v98\n\t"
        "v_exp_f32 v99, v99\n\t"
        "v_exp_f32 v100, v100\n\t"
        "v_smfmac_f32_16x16x64_bf16 v[132:135], v[144:147], v[120:127], v148\n\t"
        "v_exp_f32 v101, v101\n\t"
        "v_mfma_f32_32x32x16_bf16 v[112:127], v[200:203], v[88:91], 0\n\t"
        "v_exp_f32 v102, v102\n\t"
        "v_exp_f32 v103, v103\n\t"
        "v_exp_f32 v104, v104\n\t"
        "v_mfma_f32_32x32x16_bf16 v[112:127], v[204:207], v[92:95], v[112:127]\n\t"
        "v_exp_f32 v105, v105\n\t"
        "v_exp_f32 v106, v106\n\t"
        "v_exp_f32 v107, v107\n\t"
        "v_smfmac_f32_32x32x32_bf16 v[32:47], v[208:211], v[96:103], v148\n\t"
        "v_exp_f32 v108, v108\n\t"
        "v_exp_f32 v109, v109\n\t"
        "v_exp_f32 v110, v110\n\t"
        "v_smfmac_f32_16x16x64_bf16 v[136:139], v[144:147], v[96:103], v148\n\t"
        "buffer_load_dwordx4 v[196:199], v149, s[40:43], s44 offen offset:3072 sc1\n\t"
        "v_exp_f32 v111, v111\n\t"
        "v_exp_f32 v112, v112\n\t"
        "v_exp_f32 v113, v113\n\t"
        "v_smfmac_f32_32x32x32_bf16 v[32:47], v[212:215], v[104:111], v148\n\t"
        "v_exp_f32 v114, v114\n\t"
        "v_exp_f32 v115, v115\n\t"
        "v_exp_f32 v116, v116\n\t"
        "v_smfmac_f32_16x16x64_bf16 v[136:139], v[144:147], v[104:111], v148\n\t"
        "v_exp_f32 v117, v117\n\t"
        "s_waitcnt vmcnt(8)\n\t"
        "v_mfma_f32_32x32x16_bf16 v[96:111], v[152:155], v[64:67], 0\n\t"
        "v_exp_f32 v118, v118\n\t"
        "v_exp_f32 v119, v119\n\t"
        "v_exp_f32 v120, v120\n\t"
        "v_mfma_f32_32x32x16_bf16 v[96:111], v[156:159], v[68:71], v[96:111]\n\t"
        "v_exp_f32 v121, v121\n\t"
        "v_exp_f32 v122, v122\n\t"
        "v_exp_f32 v123, v123\n\t"
        "v_smfmac_f32_32x32x32_bf16 v[48:63], v[208:211], v[112:119], v148\n\t"
        "v_exp_f32 v124, v124\n\t"
        "v_exp_f32 v125, v125\n\t"
        "v_exp_f32 v126, v126\n\t"
        "v_smfmac_f32_16x16x64_bf16 v[140:143], v[144:147], v[112:119], v148\n\t"
        "buffer_load_dwordx4 v[200:203], v150, s[40:43], s44 offen offset:0 sc1\n\t"
        "v_exp_f32 v127, v127\n\t"
        "v_exp_f32 v96, v96\n\t"
        "v_exp_f32 v97, v97\n\t"
        "v_smfmac_f32_32x32x32_bf16 v[48:63], v[212:215], v[120:127], v148\n\t"
        "v_exp_f32 v98, v98\n\t"
        "v_exp_f32 v99, v99\n\t"
        "v_exp_f32 v100, v100\n\t"
        "v_smfmac_f32_16x16x64_bf16 v[140:143], v[144:147], v[120:127], v148\n\t"
        "v_exp_f32 v101, v101\n\t"
        "v_mfma_f32_32x32x16_bf16 v[112:127], v[152:155], v[72:75], 0\n\t"
        "v_exp_f32 v102, v102\n\t"
        "v_exp_f32 v103, v103\n\t"
        "v_exp_f32 v104, v104\n\t"
        "v_mfma_f32_32x32x16_bf16 v[112:127], v[156:159], v[76:79], v[112:127]\n\t"
        "v_exp_f32 v105, v105\n\t"
        "v_exp_f32 v106, v106\n\t"
        "v_exp_f32 v107, v107\n\t"
        "v_smfmac_f32_32x32x32_bf16 v[0:15], v[160:163], v[96:103], v148\n\t"
        "v_exp_f32 v108, v108\n\t"
        "v_exp_f32 v109, v109\n\t"
        "v_exp_f32 v110, v110\n\t"
        "v_smfmac_f32_16x16x64_bf16 v[128:131], v[144:147], v[96:103], v148\n\t"
        "buffer_load_dwordx4 v[204:207], v150, s[40:43], s44 offen offset:1024 sc1\n\t"
        "v_exp_f32 v111, v111\n\t"
        "v_exp_f32 v112, v112\n\t"
        "v_exp_f32 v113, v113\n\t"
        "v_smfmac_f32_32x32x32_bf16 v[0:15], v[164:167], v[104:111], v148\n\t"
        "v_exp_f32 v114, v114\n\t"
        "v_exp_f32 v115, v115\n\t"
        "v_exp_f32 v116, v116\n\t"
        "v_smfmac_f32_16x16x64_bf16 v[128:131], v[144:147], v[104:111], v148\n\t"
        "v_exp_f32 v117, v117\n\t"
        "v_mfma_f32_32x32x16_bf16 v[96:111], v[152:155], v[80:83], 0\n\t"
        "v_exp_f32 v118, v118\n\t"
        "v_exp_f32 v119, v119\n\t"
        "v_exp_f32 v120, v120\n\t"
        "v_mfma_f32_32x32x16_bf16 v[96:111], v[156:159], v[84:87], v[96:111]\n\t"
        "v_exp_f32 v121, v121\n\t"
        "v_exp_f32 v122, v122\n\t"
        "v_exp_f32 v123, v123\n\t"
        "v_smfmac_f32_32x32x32_bf16 v[16:31], v[160:163], v[112:119], v148\n\t"
        "v_exp_f32 v124, v124\n\t"
        "v_exp_f32 v125, v125\n\t"
        "v_exp_f32 v126, v126\n\t"
        "v_smfmac_f32_16x16x64_bf16 v[132:135], v[144:147], v[112:119], v148\n\t"
        "buffer_load_dwordx4 v[208:211], v150, s[40:43], s44 offen offset:2048 sc1\n\t"
        "v_exp_f32 v127, v127\n\t"
        "v_exp_f32 v96, v96\n\t"
        "v_exp_f32 v97, v97\n\t"
        "v_smfmac_f32_32x32x32_bf16 v[16:31], v[164:167], v[120:127], v148\n\t"
        "v_exp_f32 v98, v98\n\t"
        "v_exp_f32 v99, v99\n\t"
        "v_exp_f32 v100, v100\n\t"
        "v_smfmac_f32_16x16x64_bf16 v[132:135], v[144:147], v[120:127], v148\n\t"
        "v_exp_f32 v101, v101\n\t"
        "v_mfma_f32_32x32x16_bf16 v[112:127], v[152:155], v[88:91], 0\n\t"
        "v_exp_f32 v102, v102\n\t"
        "v_exp_f32 v103, v103\n\t"
        "v_exp_f32 v104, v104\n\t"
        "v_mfma_f32_32x32x16_bf16 v[112:127], v[156:159], v[92:95], v[112:127]\n\t"
        "v_exp_f32 v105, v105\n\t"
        "v_exp_f32 v106, v106\n\t"
        "v_exp_f32 v107, v107\n\t"
        "v_smfmac_f32_32x32x32_bf16 v[32:47], v[160:163], v[96:103], v148\n\t"
        "v_exp_f32 v108, v108\n\t"
        "v_exp_f32 v109, v109\n\t"
        "v_exp_f32 v110, v110\n\t"
        "v_smfmac_f32_16x16x64_bf16 v[136:139], v[144:147], v[96:103], v148\n\t"
        "buffer_load_dwordx4 v[212:215], v150, s[40:43], s44 offen offset:3072 sc1\n\t"
        "v_exp_f32 v111, v111\n\t"
        "v_exp_f32 v112, v112\n\t"
        "v_exp_f32 v113, v113\n\t"
        "v_smfmac_f32_32x32x32_bf16 v[32:47], v[164:167], v[104:111], v148\n\t"
        "s_add_u32 s44, s44, 0x4000\n\t"
        "s_and_b32 s44, s44, 0x3ffff\n\t"
        "v_exp_f32 v114, v114\n\t"
        "v_exp_f32 v115, v115\n\t"
        "v_exp_f32 v116, v116\n\t"
        "v_smfmac_f32_16x16x64_bf16 v[136:139], v[144:147], v[104:111], v148\n\t"
        "v_exp_f32 v117, v117\n\t"
        "s_waitcnt vmcnt(8)\n\t"
        "v_mfma_f32_32x32x16_bf16 v[96:111], v[168:171], v[64:67], 0\n\t"
        "v_exp_f32 v118, v118\n\t"
        "v_exp_f32 v119, v119\n\t"
        "v_exp_f32 v120, v120\n\t"
        "v_mfma_f32_32x32x16_bf16 v[96:111], v[172:175], v[68:71], v[96:111]\n\t"
        "v_exp_f32 v121, v121\n\t"
        "v_exp_f32 v122, v122\n\t"
        "v_exp_f32 v123, v123\n\t"
        "v_smfmac_f32_32x32x32_bf16 v[48:63], v[160:163], v[112:119], v148\n\t"
        "v_exp_f32 v124, v124\n\t"
        "v_exp_f32 v125, v125\n\t"
        "v_exp_f32 v126, v126\n\t"
        "v_smfmac_f32_16x16x64_bf16 v[140:143], v[144:147], v[112:119], v148\n\t"
        "buffer_load_dwordx4 v[152:155], v149, s[40:43], s44 offen offset:0 sc1\n\t"
        "v_exp_f32 v127, v127\n\t"
        "v_exp_f32 v96, v96\n\t"
        "v_exp_f32 v97, v97\n\t"
        "v_smfmac_f32_32x32x32_bf16 v[48:63], v[164:167], v[120:127], v148\n\t"
        "v_exp_f32 v98, v98\n\t"
        "v_exp_f32 v99, v99\n\t"
        "v_exp_f32 v100, v100\n\t"
        "v_smfmac_f32_16x16x64_bf16 v[140:143], v[144:147], v[120:127], v148\n\t"
        "v_exp_f32 v101, v101\n\t"
        "v_mfma_f32_32x32x16_bf16 v[112:127], v[168:171], v[72:75], 0\n\t"
        "v_exp_f32 v102, v102\n\t"
        "v_exp_f32 v103, v103\n\t"
        "v_exp_f32 v104, v104\n\t"
        "v_mfma_f32_32x32x16_bf16 v[112:127], v[172:175], v[76:79], v[112:127]\n\t"
        "v_exp_f32 v105, v105\n\t"
        "v_exp_f32 v106, v106\n\t"
        "v_exp_f32 v107, v107\n\t"
        "v_smfmac_f32_32x32x32_bf16 v[0:15], v[176:179], v[96:103], v148\n\t"
        "v_exp_f32 v108, v108\n\t"
        "v_exp_f32 v109, v109\n\t"
        "v_exp_f32 v110, v110\n\t"
        "v_smfmac_f32_16x16x64_bf16 v[128:131], v[144:147], v[96:103], v148\n\t"
        "buffer_load_dwordx4 v[156:159], v149, s[40:43], s44 offen offset:1024 sc1\n\t"
        "v_exp_f32 v111, v111\n\t"
        "v_exp_f32 v112, v112\n\t"
        "v_exp_f32 v113, v113\n\t"
        "v_smfmac_f32_32x32x32_bf16 v[0:15], v[180:183], v[104:111], v148\n\t"
        "v_exp_f32 v114, v114\n\t"
        "v_exp_f32 v115, v115\n\t"
        "v_exp_f32 v116, v116\n\t"
        "v_smfmac_f32_16x16x64_bf16 v[128:131], v[144:147], v[104:111], v148\n\t"
        "v_exp_f32 v117, v117\n\t"
        "v_mfma_f32_32x32x16_bf16 v[96:111], v[168:171], v[80:83], 0\n\t"
        "v_exp_f32 v118, v118\n\t"
        "v_exp_f32 v119, v119\n\t"
        "v_exp_f32 v120, v120\n\t"
        "v_mfma_f32_32x32x16_bf16 v[96:111], v[172:175], v[84:87], v[96:111]\n\t"
        "v_exp_f32 v121, v121\n\t"
        "v_exp_f32 v122, v122\n\t"
        "v_exp_f32 v123, v123\n\t"
        "v_smfmac_f32_32x32x32_bf16 v[16:31], v[176:179], v[112:119], v148\n\t"
        "v_exp_f32 v124, v124\n\t"
        "v_exp_f32 v125, v125\n\t"
        "v_exp_f32 v126, v126\n\t"
        "v_smfmac_f32_16x16x64_bf16 v[132:135], v[144:147], v[112:119], v148\n\t"
        "buffer_load_dwordx4 v[160:163], v149, s[40:43], s44 offen offset:2048 sc1\n\t"
        "v_exp_f32 v127, v127\n\t"
        "v_exp_f32 v96, v96\n\t"
        "v_exp_f32 v97, v97\n\t"
        "v_smfmac_f32_32x32x32_bf16 v[16:31], v[180:183], v[120:127], v148\n\t"
        "v_exp_f32 v98, v98\n\t"
        "v_exp_f32 v99, v99\n\t"
        "v_exp_f32 v100, v100\n\t"
        "v_smfmac_f32_16x16x64_bf16 v[132:135], v[144:147], v[120:127], v148\n\t"
        "v_exp_f32 v101, v101\n\t"
        "v_mfma_f32_32x32x16_bf16 v[112:127], v[168:171], v[88:91], 0\n\t"
        "v_exp_f32 v102, v102\n\t"
        "v_exp_f32 v103, v103\n\t"
        "v_exp_f32 v104, v104\n\t"
        "v_mfma_f32_32x32x16_bf16 v[112:127], v[172:175], v[92:95], v[112:127]\n\t"
        "v_exp_f32 v105, v105\n\t"
        "v_exp_f32 v106, v106\n\t"
        "v_exp_f32 v107, v107\n\t"
        "v_smfmac_f32_32x32x32_bf16 v[32:47], v[176:179], v[96:103], v148\n\t"
        "v_exp_f32 v108, v108\n\t"
        "v_exp_f32 v109, v109\n\t"
        "v_exp_f32 v110, v110\n\t"
        "v_smfmac_f32_16x16x64_bf16 v[136:139], v[144:147], v[96:103], v148\n\t"
        "buffer_load_dwordx4 v[164:167], v149, s[40:43], s44 offen offset:3072 sc1\n\t"
        "v_exp_f32 v111, v111\n\t"
        "s_sub_u32 s45, s45, 1\n\t"
        "s_cmp_lg_u32 s45, 0\n\t"
        "s_cbranch_scc1 1b\n\t"
        "v_exp_f32 v112, v112\n\t"
        "v_exp_f32 v113, v113\n\t"
        "v_smfmac_f32_32x32x32_bf16 v[32:47], v[180:183], v[104:111], v148\n\t"
        "v_exp_f32 v114, v114\n\t"
        "v_exp_f32 v115, v115\n\t"
        "v_exp_f32 v116, v116\n\t"
        "v_smfmac_f32_16x16x64_bf16 v[136:139], v[144:147], v[104:111], v148\n\t"
        "v_exp_f32 v117, v117\n\t"
        "s_waitcnt vmcnt(8)\n\t"
        "v_mfma_f32_32x32x16_bf16 v[96:111], v[184:187], v[64:67], 0\n\t"
        "v_exp_f32 v118, v118\n\t"
        "v_exp_f32 v119, v119\n\t"
        "v_exp_f32 v120, v120\n\t"
        "v_mfma_f32_32x32x16_bf16 v[96:111], v[188:191], v[68:71], v[96:111]\n\t"
        "v_exp_f32 v121, v121\n\t"
        "v_exp_f32 v122, v122\n\t"
        "v_exp_f32 v123, v123\n\t"
        "v_smfmac_f32_32x32x32_bf16 v[48:63], v[176:179], v[112:119], v148\n\t"
        "v_exp_f32 v124, v124\n\t"
        "v_exp_f32 v125, v125\n\t"
        "v_exp_f32 v126, v126\n\t"
        "v_smfmac_f32_16x16x64_bf16 v[140:143], v[144:147], v[112:119], v148\n\t"
        "v_exp_f32 v127, v127\n\t"
        "v_exp_f32 v96, v96\n\t"
        "v_exp_f32 v97, v97\n\t"
        "v_smfmac_f32_32x32x32_bf16 v[48:63], v[180:183], v[120:127], v148\n\t"
        "v_exp_f32 v98, v98\n\t"
        "v_exp_f32 v99, v99\n\t"
        "v_exp_f32 v100, v100\n\t"
        "v_smfmac_f32_16x16x64_bf16 v[140:143], v[144:147], v[120:127], v148\n\t"
        "v_exp_f32 v101, v101\n\t"
        "v_mfma_f32_32x32x16_bf16 v[112:127], v[184:187], v[72:75], 0\n\t"
        "v_exp_f32 v102, v102\n\t"
        "v_exp_f32 v103, v103\n\t"
        "v_exp_f32 v104, v104\n\t"
        "v_mfma_f32_32x32x16_bf16 v[112:127], v[188:191], v[76:79], v[112:127]\n\t"
        "v_exp_f32 v105, v105\n\t"
        "v_exp_f32 v106, v106\n\t"
        "v_exp_f32 v107, v107\n\t"
        "v_smfmac_f32_32x32x32_bf16 v[0:15], v[192:195], v[96:103], v148\n\t"
        "v_exp_f32 v108, v108\n\t"
        "v_exp_f32 v109, v109\n\t"
        "v_exp_f32 v110, v110\n\t"
        "v_smfmac_f32_16x16x64_bf16 v[128:131], v[144:147], v[96:103], v148\n\t"
        "v_exp_f32 v111, v111\n\t"
        "v_exp_f32 v112, v112\n\t"
        "v_exp_f32 v113, v113\n\t"
        "v_smfmac_f32_32x32x32_bf16 v[0:15], v[196:199], v[104:111], v148\n\t"
        "v_exp_f32 v114, v114\n\t"
        "v_exp_f32 v115, v115\n\t"
        "v_exp_f32 v116, v116\n\t"
        "v_smfmac_f32_16x16x64_bf16 v[128:131], v[144:147], v[104:111], v148\n\t"
        "v_exp_f32 v117, v117\n\t"
        "v_mfma_f32_32x32x16_bf16 v[96:111], v[184:187], v[80:83], 0\n\t"
        "v_exp_f32 v118, v118\n\t"
        "v_exp_f32 v119, v119\n\t"
        "v_exp_f32 v120, v120\n\t"
        "v_mfma_f32_32x32x16_bf16 v[96:111], v[188:191], v[84:87], v[96:111]\n\t"
        "v_exp_f32 v121, v121\n\t"
        "v_exp_f32 v122, v122\n\t"
        "v_exp_f32 v123, v123\n\t"
        "v_smfmac_f32_32x32x32_bf16 v[16:31], v[192:195], v[112:119], v148\n\t"
        "v_exp_f32 v124, v124\n\t"
        "v_exp_f32 v125, v125\n\t"
        "v_exp_f32 v126, v126\n\t"
        "v_smfmac_f32_16x16x64_bf16 v[132:135], v[144:147], v[112:119], v148\n\t"
        "v_exp_f32 v127, v127\n\t"
        "v_exp_f32 v96, v96\n\t"
        "v_exp_f32 v97, v97\n\t"
        "v_smfmac_f32_32x32x32_bf16 v[16:31], v[196:199], v[120:127], v148\n\t"
        "v_exp_f32 v98, v98\n\t"
        "v_exp_f32 v99, v99\n\t"
        "v_exp_f32 v100, v100\n\t"
        "v_smfmac_f32_16x16x64_bf16 v[132:135], v[144:147], v[120:127], v148\n\t"
        "v_exp_f32 v101, v101\n\t"
        "v_mfma_f32_32x32x16_bf16 v[112:127], v[184:187], v[88:91], 0\n\t"
        "v_exp_f32 v102, v102\n\t"
        "v_exp_f32 v103, v103\n\t"
        "v_exp_f32 v104, v104\n\t"
        "v_mfma_f32_32x32x16_bf16 v[112:127], v[188:191], v[92:95], v[112:127]\n\t"
        "v_exp_f32 v105, v105\n\t"
        "v_exp_f32 v106, v106\n\t"
        "v_exp_f32 v107, v107\n\t"
        "v_smfmac_f32_32x32x32_bf16 v[32:47], v[192:195], v[96:103], v148\n\t"
        "v_exp_f32 v108, v108\n\t"
        "v_exp_f32 v109, v109\n\t"
        "v_exp_f32 v110, v110\n\t"
        "v_smfmac_f32_16x16x64_bf16 v[136:139], v[144:147], v[96:103], v148\n\t"
        "v_exp_f32 v111, v111\n\t"
        "v_exp_f32 v112, v112\n\t"
        "v_exp_f32 v113, v113\n\t"
        "v_smfmac_f32_32x32x32_bf16 v[32:47], v[196:199], v[104:111], v148\n\t"
        "v_exp_f32 v114, v114\n\t"
        "v_exp_f32 v115, v115\n\t"
        "v_exp_f32 v116, v116\n\t"
        "v_smfmac_f32_16x16x64_bf16 v[136:139], v[144:147], v[104:111], v148\n\t"
        "v_exp_f32 v117, v117\n\t"
        "s_waitcnt vmcnt(4)\n\t"
        "v_mfma_f32_32x32x16_bf16 v[96:111], v[200:203], v[64:67], 0\n\t"
        "v_exp_f32 v118, v118\n\t"
        "v_exp_f32 v119, v119\n\t"
        "v_exp_f32 v120, v120\n\t"
        "v_mfma_f32_32x32x16_bf16 v[96:111], v[204:207], v[68:71], v[96:111]\n\t"
        "v_exp_f32 v121, v121\n\t"
        "v_exp_f32 v122, v122\n\t"
        "v_exp_f32 v123, v123\n\t"
        "v_smfmac_f32_32x32x32_bf16 v[48:63], v[192:195], v[112:119], v148\n\t"
        "v_exp_f32 v124, v124\n\t"
        "v_exp_f32 v125, v125\n\t"
        "v_exp_f32 v126, v126\n\t"
        "v_smfmac_f32_16x16x64_bf16 v[140:143], v[144:147], v[112:119], v148\n\t"
        "v_exp_f32 v127, v127\n\t"
        "v_exp_f32 v96, v96\n\t"
        "v_exp_f32 v97, v97\n\t"
        "v_smfmac_f32_32x32x32_bf16 v[48:63], v[196:199], v[120:127], v148\n\t"
        "v_exp_f32 v98, v98\n\t"
        "v_exp_f32 v99, v99\n\t"
        "v_exp_f32 v100, v100\n\t"
        "v_smfmac_f32_16x16x64_bf16 v[140:143], v[144:147], v[120:127], v148\n\t"
        "v_exp_f32 v101, v101\n\t"
        "v_mfma_f32_32x32x16_bf16 v[112:127], v[200:203], v[72:75], 0\n\t"
        "v_exp_f32 v102, v102\n\t"
        "v_exp_f32 v103, v103\n\t"
        "v_exp_f32 v104, v104\n\t"
        "v_mfma_f32_32x32x16_bf16 v[112:127], v[204:207], v[76:79], v[112:127]\n\t"
        "v_exp_f32 v105, v105\n\t"
        "v_exp_f32 v106, v106\n\t"
        "v_exp_f32 v107, v107\n\t"
        "v_smfmac_f32_32x32x32_bf16 v[0:15], v[208:211], v[96:103], v148\n\t"
        "v_exp_f32 v108, v108\n\t"
        "v_exp_f32 v109, v109\n\t"
        "v_exp_f32 v110, v110\n\t"
        "v_smfmac_f32_16x16x64_bf16 v[128:131], v[144:147], v[96:103], v148\n\t"
        "v_exp_f32 v111, v111\n\t"
        "v_exp_f32 v112, v112\n\t"
        "v_exp_f32 v113, v113\n\t"
        "v_smfmac_f32_32x32x32_bf16 v[0:15], v[212:215], v[104:111], v148\n\t"
        "v_exp_f32 v114, v114\n\t"
        "v_exp_f32 v115, v115\n\t"
        "v_exp_f32 v116, v116\n\t"
        "v_smfmac_f32_16x16x64_bf16 v[128:131], v[144:147], v[104:111], v148\n\t"
        "v_exp_f32 v117, v117\n\t"
        "v_mfma_f32_32x32x16_bf16 v[96:111], v[200:203], v[80:83], 0\n\t"
        "v_exp_f32 v118, v118\n\t"
        "v_exp_f32 v119, v119\n\t"
        "v_exp_f32 v120, v120\n\t"
        "v_mfma_f32_32x32x16_bf16 v[96:111], v[204:207], v[84:87], v[96:111]\n\t"
        "v_exp_f32 v121, v121\n\t"
        "v_exp_f32 v122, v122\n\t"
        "v_exp_f32 v123, v123\n\t"
        "v_smfmac_f32_32x32x32_bf16 v[16:31], v[208:211], v[112:119], v148\n\t"
        "v_exp_f32 v124, v124\n\t"
        "v_exp_f32 v125, v125\n\t"
        "v_exp_f32 v126, v126\n\t"
        "v_smfmac_f32_16x16x64_bf16 v[132:135], v[144:147], v[112:119], v148\n\t"
        "v_exp_f32 v127, v127\n\t"
        "v_exp_f32 v96, v96\n\t"
        "v_exp_f32 v97, v97\n\t"
        "v_smfmac_f32_32x32x32_bf16 v[16:31], v[212:215], v[120:127], v148\n\t"
        "v_exp_f32 v98, v98\n\t"
        "v_exp_f32 v99, v99\n\t"
        "v_exp_f32 v100, v100\n\t"
        "v_smfmac_f32_16x16x64_bf16 v[132:135], v[144:147], v[120:127], v148\n\t"
        "v_exp_f32 v101, v101\n\t"
        "v_mfma_f32_32x32x16_bf16 v[112:127], v[200:203], v[88:91], 0\n\t"
        "v_exp_f32 v102, v102\n\t"
        "v_exp_f32 v103, v103\n\t"
        "v_exp_f32 v104, v104\n\t"
        "v_mfma_f32_32x32x16_bf16 v[112:127], v[204:207], v[92:95], v[112:127]\n\t"
        "v_exp_f32 v105, v105\n\t"
        "v_exp_f32 v106, v106\n\t"
        "v_exp_f32 v107, v107\n\t"
        "v_smfmac_f32_32x32x32_bf16 v[32:47], v[208:211], v[96:103], v148\n\t"
        "v_exp_f32 v108, v108\n\t"
        "v_exp_f32 v109, v109\n\t"
        "v_exp_f32 v110, v110\n\t"
        "v_smfmac_f32_16x16x64_bf16 v[136:139], v[144:147], v[96:103], v148\n\t"
        "v_exp_f32 v111, v111\n\t"
        "v_exp_f32 v112, v112\n\t"
        "v_exp_f32 v113, v113\n\t"
        "v_smfmac_f32_32x32x32_bf16 v[32:47], v[212:215], v[104:111], v148\n\t"
        "v_exp_f32 v114, v114\n\t"
        "v_exp_f32 v115, v115\n\t"
        "v_exp_f32 v116, v116\n\t"
        "v_smfmac_f32_16x16x64_bf16 v[136:139], v[144:147], v[104:111], v148\n\t"
        "v_exp_f32 v117, v117\n\t"
        "v_exp_f32 v118, v118\n\t"
        "v_exp_f32 v119, v119\n\t"
        "v_exp_f32 v120, v120\n\t"
        "v_exp_f32 v121, v121\n\t"
        "v_exp_f32 v122, v122\n\t"
        "v_exp_f32 v123, v123\n\t"
        "v_smfmac_f32_32x32x32_bf16 v[48:63], v[208:211], v[112:119], v148\n\t"
        "v_exp_f32 v124, v124\n\t"
        "v_exp_f32 v125, v125\n\t"
        "v_exp_f32 v126, v126\n\t"
        "v_smfmac_f32_16x16x64_bf16 v[140:143], v[144:147], v[112:119], v148\n\t"
        "v_exp_f32 v127, v127\n\t"
        "s_nop 1\n\t"
        "v_smfmac_f32_32x32x32_bf16 v[48:63], v[212:215], v[120:127], v148\n\t"
        "v_smfmac_f32_16x16x64_bf16 v[140:143], v[144:147], v[120:127], v148\n\t"
        "s_waitcnt vmcnt(0)\n\t"
        "s_nop 7\n\t"
        : "={v[0:15]}"(o0), "={v[16:31]}"(o1), "={v[32:47]}"(o2), "={v[48:63]}"(o3), "={v[128:143]}"(lv), "+{s44}"(soff), "+{v[152:167]}"(kva), "+{v[168:183]}"(kvb), "+{v[184:191]}"(fv) STAMP_OUTS
        : "{v[64:71]}"(q[0]), "{v[72:79]}"(q[1]), "{v[80:87]}"(q[2]), "{v[88:95]}"(q[3]), "{v[144:147]}"(ones), "{v148}"(0xddddu), "{v149}"(kvoff), "{v150}"(kvoff + 4096),
          "{s[40:43]}"(srd)
        : "v96", "v97", "v98", "v99", "v100", "v101", "v102", "v103", "v104", "v105", "v106", "v107", "v108", "v109", "v110", "v111", "v112", "v113", "v114", "v115", "v116", "v117", "v118", "v119", "v120", "v121", "v122", "v123", "v124", "v125", "v126", "v127", "v151", "v192", "v193", "v194", "v195", "v196", "v197", "v198", "v199", "v200", "v201", "v202", "v203", "v204", "v205", "v206", "v207", "v208", "v209", "v210", "v211", "v212", "v213", "v214", "v215", "s45", "vcc", "scc", "memory");
      STAMP_PUT(6 + 16 * L);
      STAMPV(9 + 16 * L);
      PHASE_VARS;
#pragma unroll
      for (int g = 0; g < 8; ++g) wrc[g] = *reinterpret_cast<const u32x2*>(w_r + tn * 64 + g * 8 + 4 * hi);
      wa = *reinterpret_cast<const u32x4*>(w_r + lane * 8); wb = *reinterpret_cast<const u32x4*>(w_r + 512 + lane * 8);
      if (L == 0) {
#pragma unroll
        for (int i = 0; i < 4; ++i) res[i] = *reinterpret_cast<const u32x4*>(x0 + (size_t)(tile * 32 + r32) * D_ + tn * 64 + i * 16 + hi * 8);
      } else {
#pragma unroll
        for (int i = 0; i < 4; ++i) res[i] = ldg(rs, OFF_X1 + (tile * 64 + 4 * tn + i) * 1024 + lane * 16);
      }
      float l0 = lv[0], l1 = lv[4], l2 = lv[8], l3 = lv[12], m0 = 0.f, m1 = 0.f, m2 = 0.f, m3 = 0.f;
      auto put = [&](int u, const f32x16& o, float l, float m) {
        char* d = smem + L_XCH + (wid * 2 + u) * 4096 + lane * 16;
#pragma unroll
        for (int i = 0; i < 4; ++i) { f32x4 v = {o[4 * i], o[4 * i + 1], o[4 * i + 2], o[4 * i + 3]}; *reinterpret_cast<f32x4*>(d + i * 1024) = v; }
        f32x2 lm = {l, m};
        *reinterpret_cast<f32x2*>(smem + L_XCH + 32768 + (wid * 2 + u) * 256 + r32 * 8) = lm;
      };
      {
        put(0, o0, l0, m0); put(1, o1, l1, m1);
        SBAR();
        const f32x16 t = (o0 + o1) + (o2 + o3);
        float sum = 0.f;
#pragma unroll
        for (int e = 0; e < 16; e += 4) sum += (t[e] + t[e + 1]) + (t[e + 2] + t[e + 3]);
        const float lmax = fmaxf(fmaxf(l0, l1), fmaxf(l2, l3)), lmin = fminf(fminf(l0, l1), fminf(l2, l3));
        if (!(fabsf(sum) < 1e37f) | !((l0 + l1) + (l2 + l3) < 1e27f) | !(lmin > 1e-9f) | !(lmax < 1e27f)) words[0] = 1u;
      }
      BAR_LGKM();
      const bool redo = __builtin_amdgcn_readfirstlane(words[0]) != 0u;
      if (redo) {
        attn_safe(rs, kvoff, q[0], ones, o0, l0, m0); attn_safe(rs, kvoff, q[1], ones, o1, l1, m1);
        attn_safe(rs, kvoff, q[2], ones, o2, l2, m2); attn_safe(rs, kvoff, q[3], ones, o3, l3, m3);
        BARRIER();
        put(0, o0, l0, m0); put(1, o1, l1, m1);
        BAR_LGKM();
      }
      f32x4 pv[2][4];
      f32x2 lm[2];
#pragma unroll
      for (int u = 0; u < 2; ++u) {
        lm[u] = *reinterpret_cast<const f32x2*>(smem + L_XCH + 32768 + ((wid ^ 2) * 2 + u) * 256 + r32 * 8);
#pragma unroll
        for (int i = 0; i < 4; ++i) pv[u][i] = *reinterpret_cast<const f32x4*>(smem + L_XCH + ((wid ^ 2) * 2 + u) * 4096 + lane * 16 + i * 1024);
      }
      SBAR();
      auto fin = [&](int u, const f32x16& o, float l, float m) {
        float g1 = RCP(l + lm[u][0]), g2 = g1;
        if (redo) {
          const float mm = fmaxf(m, lm[u][1]), f1 = EX2(m - mm), f2 = EX2(lm[u][1] - mm), inv = RCP(l * f1 + lm[u][0] * f2);
          g1 = f1 * inv; g2 = f2 * inv;
        }
        const unsigned dst = OFF_OF + L * (4 * MBY) + ((tm * 4 + 2 * kh + u) * 64 + (tn * 2 + hh) * 2) * 1024 + lane * 16;
#pragma unroll
        for (int c = 0; c < 2; ++c) {
          u32x4 w;
#pragma unroll
          for (int e = 0; e < 4; ++e) w[e] = pack2(o[8 * c + 2 * e] * g1 + pv[u][2 * c + (e >> 1)][2 * (e & 1)] * g2, o[8 * c + 2 * e + 1] * g1 + pv[u][2 * c + (e >> 1)][2 * (e & 1) + 1] * g2);
          stg(rs, dst + c * 1024, w);
          SBAR();
        }
      };
      fin(0, o2, l2, m2); fin(1, o3, l3, m3);
    }
    STAMPV(10 + 16 * L);
    {
      f32x16 acc[2];
      STAMP_DECL;
      { PHASE_VARS;
      const unsigned fl = FL_O + (L * 16 + tm) * 8192 + kh * 4096;
      BAR_LGKM();
      DRAIN();
      STAMPV(11 + 16 * L);
      __builtin_amdgcn_raw_buffer_store_b32(TAG, rs, lane == 0 ? fl + (tn * 2 + hh) * 128 : OOB, 0, 16);
      asm volatile("" ::: "memory");
      {
        float t = 0.f;
#pragma unroll
        for (int e = 0; e < 4; ++e) t = dot2(wa[e], 0x3f803f80u, dot2(wb[e], 0x3f803f80u, t));
        wsum = wave_sum(t);
      }
      if (L == 0) {
#pragma unroll
        for (int i = 0; i < 4; ++i) res[i] = fo_swap(res[i]);
      } else {
        const bool routed = (int)words[3] == tile * 32 + r32;
        const float mb1 = reinterpret_cast<const float*>(smem + L_AB)[256 + wid * 64 + r32], rd1 = reinterpret_cast<const float*>(smem + L_AB)[256 + wid * 64 + 32 + r32];
#pragma unroll
        for (int i = 0; i < 4; ++i) {
          const u32x4 pr = *reinterpret_cast<const u32x4*>(smem + L_X2P + (i * 2 + hi) * 16);
#pragma unroll
          for (int e = 0; e < 4; ++e) {
            const unsigned h2 = ln_pair(res[i][e], mb1, rd1);
            unsigned t = pack2(lo16(res[i][e]) + lo16(h2), hi16(res[i][e]) + hi16(h2));
            PIN(t);
            res[i][e] = routed ? pr[e] : t;
          }
        }
      }
#pragma unroll
      for (int g = 0; g < 8; ++g) PIN(wrc[g]);
#pragma unroll
      for (int i = 0; i < 4; ++i) PIN(res[i]);
      PIN(wsum);
      unsigned soff = tn * 4096;
      const unsigned a0 = lbase + wrow * 128 + ((wch ^ wsw) << 4) + 8 * (tp & 1), a1 = lbase + wrow * 128 + (((4 + wch) ^ wsw) << 4) + 8 * (tp & 1);
      asm volatile(
        "ds_read_b64_tr_b16 v[32:33], v193 offset:0\n\t"
        "ds_read_b64_tr_b16 v[34:35], v193 offset:1024\n\t"
        "ds_read_b64_tr_b16 v[36:37], v194 offset:0\n\t"
        "ds_read_b64_tr_b16 v[38:39], v194 offset:1024\n\t"
        "ds_read_b64_tr_b16 v[40:41], v193 offset:2048\n\t"
        "ds_read_b64_tr_b16 v[42:43], v193 offset:3072\n\t"
        "ds_read_b64_tr_b16 v[44:45], v194 offset:2048\n\t"
        "ds_read_b64_tr_b16 v[46:47], v194 offset:3072\n\t"
        "ds_read_b64_tr_b16 v[48:49], v193 offset:4096\n\t"
        "ds_read_b64_tr_b16 v[50:51], v193 offset:5120\n\t"
        "ds_read_b64_tr_b16 v[52:53], v194 offset:4096\n\t"
        "ds_read_b64_tr_b16 v[54:55], v194 offset:5120\n\t"
        "ds_read_b64_tr_b16 v[56:57], v193 offset:6144\n\t"
        "ds_read_b64_tr_b16 v[58:59], v193 offset:7168\n\t"
        "ds_read_b64_tr_b16 v[60:61], v194 offset:6144\n\t"
        "ds_read_b64_tr_b16 v[62:63], v194 offset:7168\n\t"
        "s_mov_b32 s45, 0x8000\n\t"
        "2:\n\t"
        "buffer_load_dword v200, v198, s[40:43], 0 offen sc1\n\t"
        "s_sub_u32 s45, s45, 1\n\t"
        "s_waitcnt vmcnt(0)\n\t"
        "s_cbranch_scc1 3f\n\t"
        "v_cmp_ne_u32 vcc, v199, v200\n\t"
        "s_cbranch_vccz 3f\n\t"
        "s_sleep 1\n\t"
        "s_branch 2b\n\t"
        "3:\n\t"
        "buffer_load_dwordx4 v[64:67], v192, s[40:43], s44 offen offset:0 sc1\n\t"
        "buffer_load_dwordx4 v[68:71], v192, s[40:43], s44 offen offset:1024 sc1\n\t"
        "buffer_load_dwordx4 v[72:75], v192, s[40:43], s44 offen offset:2048 sc1\n\t"
        "buffer_load_dwordx4 v[76:79], v192, s[40:43], s44 offen offset:3072 sc1\n\t"
        "s_add_u32 s44, s44, 0x1000\n\t"
        "s_and_b32 s44, s44, 0xffff\n\t"
        "buffer_load_dwordx4 v[80:83], v192, s[40:43], s44 offen offset:0 sc1\n\t"
        "buffer_load_dwordx4 v[84:87], v192, s[40:43], s44 offen offset:1024 sc1\n\t"
        "buffer_load_dwordx4 v[88:91], v192, s[40:43], s44 offen offset:2048 sc1\n\t"
        "buffer_load_dwordx4 v[92:95], v192, s[40:43], s44 offen offset:3072 sc1\n\t"
        "s_add_u32 s44, s44, 0x1000\n\t"
        "s_and_b32 s44, s44, 0xffff\n\t"
        "buffer_load_dwordx4 v[96:99], v192, s[40:43], s44 offen offset:0 sc1\n\t"
        "buffer_load_dwordx4 v[100:103], v192, s[40:43], s44 offen offset:1024 sc1\n\t"
        "buffer_load_dwordx4 v[104:107], v192, s[40:43], s44 offen offset:2048 sc1\n\t"
        "buffer_load_dwordx4 v[108:111], v192, s[40:43], s44 offen offset:3072 sc1\n\t"
        "s_add_u32 s44, s44, 0x1000\n\t"
        "s_and_b32 s44, s44, 0xffff\n\t"
        "buffer_load_dwordx4 v[112:115], v192, s[40:43], s44 offen offset:0 sc1\n\t"
        "buffer_load_dwordx4 v[116:119], v192, s[40:43], s44 offen offset:1024 sc1\n\t"
        "buffer_load_dwordx4 v[120:123], v192, s[40:43], s44 offen offset:2048 sc1\n\t"
        "buffer_load_dwordx4 v[124:127], v192, s[40:43], s44 offen offset:3072 sc1\n\t"
        "s_add_u32 s44, s44, 0x1000\n\t"
        "s_and_b32 s44, s44, 0xffff\n\t"
        "buffer_load_dwordx4 v[128:131], v192, s[40:43], s44 offen offset:0 sc1\n\t"
        "buffer_load_dwordx4 v[132:135], v192, s[40:43], s44 offen offset:1024 sc1\n\t"
        "buffer_load_dwordx4 v[136:139], v192, s[40:43], s44 offen offset:2048 sc1\n\t"
        "buffer_load_dwordx4 v[140:143], v192, s[40:43], s44 offen offset:3072 sc1\n\t"
        "s_add_u32 s44, s44, 0x1000\n\t"
        "s_and_b32 s44, s44, 0xffff\n\t"
        "buffer_load_dwordx4 v[144:147], v192, s[40:43], s44 offen offset:0 sc1\n\t"
        "buffer_load_dwordx4 v[148:151], v192, s[40:43], s44 offen offset:1024 sc1\n\t"
        "buffer_load_dwordx4 v[152:155], v192, s[40:43], s44 offen offset:2048 sc1\n\t"
        "buffer_load_dwordx4 v[156:159], v192, s[40:43], s44 offen offset:3072 sc1\n\t"
        "s_add_u32 s44, s44, 0x1000\n\t"
        "s_and_b32 s44, s44, 0xffff\n\t"
        "buffer_load_dwordx4 v[160:163], v192, s[40:43], s44 offen offset:0 sc1\n\t"
        "buffer_load_dwordx4 v[164:167], v192, s[40:43], s44 offen offset:1024 sc1\n\t"
        "buffer_load_dwordx4 v[168:171], v192, s[40:43], s44 offen offset:2048 sc1\n\t"
        "buffer_load_dwordx4 v[172:175], v192, s[40:43], s44 offen offset:3072 sc1\n\t"
        "s_add_u32 s44, s44, 0x1000\n\t"
        "s_and_b32 s44, s44, 0xffff\n\t"
        "buffer_load_dwordx4 v[176:179], v192, s[40:43], s44 offen offset:0 sc1\n\t"
        "buffer_load_dwordx4 v[180:183], v192, s[40:43], s44 offen offset:1024 sc1\n\t"
        "buffer_load_dwordx4 v[184:187], v192, s[40:43], s44 offen offset:2048 sc1\n\t"
        "buffer_load_dwordx4 v[188:191], v192, s[40:43], s44 offen offset:3072 sc1\n\t"
        "s_waitcnt vmcnt(31) lgkmcnt(14)\n\t"
        "v_mfma_f32_32x32x16_bf16 v[0:15], v[32:35], v[64:67], 0\n\t"
        "ds_read_b64_tr_b16 v[32:33], v193 offset:8192\n\t"
        "ds_read_b64_tr_b16 v[34:35], v193 offset:9216\n\t"
        "s_waitcnt lgkmcnt(14)\n\t"
        "v_mfma_f32_32x32x16_bf16 v[16:31], v[36:39], v[64:67], 0\n\t"
        "ds_read_b64_tr_b16 v[36:37], v194 offset:8192\n\t"
        "ds_read_b64_tr_b16 v[38:39], v194 offset:9216\n\t"
        "s_add_u32 s44, s44, 0x1000\n\t"
        "s_and_b32 s44, s44, 0xffff\n\t"
        "buffer_load_dwordx4 v[64:67], v192, s[40:43], s44 offen offset:0 sc1\n\t"
        "s_waitcnt vmcnt(31) lgkmcnt(14)\n\t"
        "v_mfma_f32_32x32x16_bf16 v[0:15], v[40:43], v[68:71], v[0:15]\n\t"
        "ds_read_b64_tr_b16 v[40:41], v193 offset:10240\n\t"
        "ds_read_b64_tr_b16 v[42:43], v193 offset:11264\n\t"
        "s_waitcnt lgkmcnt(14)\n\t"
        "v_mfma_f32_32x32x16_bf16 v[16:31], v[44:47], v[68:71], v[16:31]\n\t"
        "ds_read_b64_tr_b16 v[44:45], v194 offset:10240\n\t"
        "ds_read_b64_tr_b16 v[46:47], v194 offset:11264\n\t"
        "buffer_load_dwordx4 v[68:71], v192, s[40:43], s44 offen offset:1024 sc1\n\t"
        "s_waitcnt vmcnt(31) lgkmcnt(14)\n\t"
        "v_mfma_f32_32x32x16_bf16 v[0:15], v[48:51], v[72:75], v[0:15]\n\t"
        "ds_read_b64_tr_b16 v[48:49], v193 offset:12288\n\t"
        "ds_read_b64_tr_b16 v[50:51], v193 offset:13312\n\t"
        "s_waitcnt lgkmcnt(14)\n\t"
        "v_mfma_f32_32x32x16_bf16 v[16:31], v[52:55], v[72:75], v[16:31]\n\t"
        "ds_read_b64_tr_b16 v[52:53], v194 offset:12288\n\t"
        "ds_read_b64_tr_b16 v[54:55], v194 offset:13312\n\t"
        "buffer_load_dwordx4 v[72:75], v192, s[40:43], s44 offen offset:2048 sc1\n\t"
        "s_waitcnt vmcnt(31) lgkmcnt(14)\n\t"
        "v_mfma_f32_32x32x16_bf16 v[0:15], v[56:59], v[76:79], v[0:15]\n\t"
        "ds_read_b64_tr_b16 v[56:57], v193 offset:14336\n\t"
        "ds_read_b64_tr_b16 v[58:59], v193 offset:15360\n\t"
        "s_waitcnt lgkmcnt(14)\n\t"
        "v_mfma_f32_32x32x16_bf16 v[16:31], v[60:63], v[76:79], v[16:31]\n\t"
        "ds_read_b64_tr_b16 v[60:61], v194 offset:14336\n\t"
        "ds_read_b64_tr_b16 v[62:63], v194 offset:15360\n\t"
        "buffer_load_dwordx4 v[76:79], v192, s[40:43], s44 offen offset:3072 sc1\n\t"
        "s_waitcnt vmcnt(31) lgkmcnt(14)\n\t"
        "v_mfma_f32_32x32x16_bf16 v[0:15], v[32:35], v[80:83], v[0:15]\n\t"
        "ds_read_b64_tr_b16 v[32:33], v193 offset:16384\n\t"
        "ds_read_b64_tr_b16 v[34:35], v193 offset:17408\n\t"
        "s_waitcnt lgkmcnt(14)\n\t"
        "v_mfma_f32_32x32x16_bf16 v[16:31], v[36:39], v[80:83], v[16:31]\n\t"
        "ds_read_b64_tr_b16 v[36:37], v194 offset:16384\n\t"
        "ds_read_b64_tr_b16 v[38:39], v194 offset:17408\n\t"
        "s_add_u32 s44, s44, 0x1000\n\t"
        "s_and_b32 s44, s44, 0xffff\n\t"
        "buffer_load_dwordx4 v[80:83], v192, s[40:43], s44 offen offset:0 sc1\n\t"
        "s_waitcnt vmcnt(31) lgkmcnt(14)\n\t"
        "v_mfma_f32_32x32x16_bf16 v[0:15], v[40:43], v[84:87], v[0:15]\n\t"
        "ds_read_b64_tr_b16 v[40:41], v193 offset:18432\n\t"
        "ds_read_b64_tr_b16 v[42:43], v193 offset:19456\n\t"
        "s_waitcnt lgkmcnt(14)\n\t"
        "v_mfma_f32_32x32x16_bf16 v[16:31], v[44:47], v[84:87], v[16:31]\n\t"
        "ds_read_b64_tr_b16 v[44:45], v194 offset:18432\n\t"
        "ds_read_b64_tr_b16 v[46:47], v194 offset:19456\n\t"
        "buffer_load_dwordx4 v[84:87], v192, s[40:43], s44 offen offset:1024 sc1\n\t"
        "s_waitcnt vmcnt(31) lgkmcnt(14)\n\t"
        "v_mfma_f32_32x32x16_bf16 v[0:15], v[48:51], v[88:91], v[0:15]\n\t"
        "ds_read_b64_tr_b16 v[48:49], v193 offset:20480\n\t"
        "ds_read_b64_tr_b16 v[50:51], v193 offset:21504\n\t"
        "s_waitcnt lgkmcnt(14)\n\t"
        "v_mfma_f32_32x32x16_bf16 v[16:31], v[52:55], v[88:91], v[16:31]\n\t"
        "ds_read_b64_tr_b16 v[52:53], v194 offset:20480\n\t"
        "ds_read_b64_tr_b16 v[54:55], v194 offset:21504\n\t"
        "buffer_load_dwordx4 v[88:91], v192, s[40:43], s44 offen offset:2048 sc1\n\t"
        "s_waitcnt vmcnt(31) lgkmcnt(14)\n\t"
        "v_mfma_f32_32x32x16_bf16 v[0:15], v[56:59], v[92:95], v[0:15]\n\t"
        "ds_read_b64_tr_b16 v[56:57], v193 offset:22528\n\t"
        "ds_read_b64_tr_b16 v[58:59], v193 offset:23552\n\t"
        "s_waitcnt lgkmcnt(14)\n\t"
        "v_mfma_f32_32x32x16_bf16 v[16:31], v[60:63], v[92:95], v[16:31]\n\t"
        "ds_read_b64_tr_b16 v[60:61], v194 offset:22528\n\t"
        "ds_read_b64_tr_b16 v[62:63], v194 offset:23552\n\t"
        "buffer_load_dwordx4 v[92:95], v192, s[40:43], s44 offen offset:3072 sc1\n\t"
        "s_waitcnt vmcnt(31) lgkmcnt(14)\n\t"
        "v_mfma_f32_32x32x16_bf16 v[0:15], v[32:35], v[96:99], v[0:15]\n\t"
        "ds_read_b64_tr_b16 v[32:33], v193 offset:24576\n\t"
        "ds_read_b64_tr_b16 v[34:35], v193 offset:25600\n\t"
        "s_waitcnt lgkmcnt(14)\n\t"
        "v_mfma_f32_32x32x16_bf16 v[16:31], v[36:39], v[96:99], v[16:31]\n\t"
        "ds_read_b64_tr_b16 v[36:37], v194 offset:24576\n\t"
        "ds_read_b64_tr_b16 v[38:39], v194 offset:25600\n\t"
        "s_add_u32 s44, s44, 0x1000\n\t"
        "s_and_b32 s44, s44, 0xffff\n\t"
        "buffer_load_dwordx4 v[96:99], v192, s[40:43], s44 offen offset:0 sc1\n\t"
        "s_waitcnt vmcnt(31) lgkmcnt(14)\n\t"
        "v_mfma_f32_32x32x16_bf16 v[0:15], v[40:43], v[100:103], v[0:15]\n\t"
        "ds_read_b64_tr_b16 v[40:41], v193 offset:26624\n\t"
        "ds_read_b64_tr_b16 v[42:43], v193 offset:27648\n\t"
        "s_waitcnt lgkmcnt(14)\n\t"
        "v_mfma_f32_32x32x16_bf16 v[16:31], v[44:47], v[100:103], v[16:31]\n\t"
        "ds_read_b64_tr_b16 v[44:45], v194 offset:26624\n\t"
        "ds_read_b64_tr_b16 v[46:47], v194 offset:27648\n\t"
        "buffer_load_dwordx4 v[100:103], v192, s[40:43], s44 offen offset:1024 sc1\n\t"
        "s_waitcnt vmcnt(31) lgkmcnt(14)\n\t"
        "v_mfma_f32_32x32x16_bf16 v[0:15], v[48:51], v[104:107], v[0:15]\n\t"
        "ds_read_b64_tr_b16 v[48:49], v193 offset:28672\n\t"
        "ds_read_b64_tr_b16 v[50:51], v193 offset:29696\n\t"
        "s_waitcnt lgkmcnt(14)\n\t"
        "v_mfma_f32_32x32x16_bf16 v[16:31], v[52:55], v[104:107], v[16:31]\n\t"
        "ds_read_b64_tr_b16 v[52:53], v194 offset:28672\n\t"
        "ds_read_b64_tr_b16 v[54:55], v194 offset:29696\n\t"
        "buffer_load_dwordx4 v[104:107], v192, s[40:43], s44 offen offset:2048 sc1\n\t"
        "s_waitcnt vmcnt(31) lgkmcnt(14)\n\t"
        "v_mfma_f32_32x32x16_bf16 v[0:15], v[56:59], v[108:111], v[0:15]\n\t"
        "ds_read_b64_tr_b16 v[56:57], v193 offset:30720\n\t"
        "ds_read_b64_tr_b16 v[58:59], v193 offset:31744\n\t"
        "s_waitcnt lgkmcnt(14)\n\t"
        "v_mfma_f32_32x32x16_bf16 v[16:31], v[60:63], v[108:111], v[16:31]\n\t"
        "ds_read_b64_tr_b16 v[60:61], v194 offset:30720\n\t"
        "ds_read_b64_tr_b16 v[62:63], v194 offset:31744\n\t"
        "buffer_load_dwordx4 v[108:111], v192, s[40:43], s44 offen offset:3072 sc1\n\t"
        "s_waitcnt vmcnt(31) lgkmcnt(14)\n\t"
        "v_mfma_f32_32x32x16_bf16 v[0:15], v[32:35], v[112:115], v[0:15]\n\t"
        "ds_read_b64_tr_b16 v[32:33], v193 offset:32768\n\t"
        "ds_read_b64_tr_b16 v[34:35], v193 offset:33792\n\t"
        "s_waitcnt lgkmcnt(14)\n\t"
        "v_mfma_f32_32x32x16_bf16 v[16:31], v[36:39], v[112:115], v[16:31]\n\t"
        "ds_read_b64_tr_b16 v[36:37], v194 offset:32768\n\t"
        "ds_read_b64_tr_b16 v[38:39], v194 offset:33792\n\t"
        "s_add_u32 s44, s44, 0x1000\n\t"
        "s_and_b32 s44, s44, 0xffff\n\t"
        "buffer_load_dwordx4 v[112:115], v192, s[40:43], s44 offen offset:0 sc1\n\t"
        "s_waitcnt vmcnt(31) lgkmcnt(14)\n\t"
        "v_mfma_f32_32x32x16_bf16 v[0:15], v[40:43], v[116:119], v[0:15]\n\t"
        "ds_read_b64_tr_b16 v[40:41], v193 offset:34816\n\t"
        "ds_read_b64_tr_b16 v[42:43], v193 offset:35840\n\t"
        "s_waitcnt lgkmcnt(14)\n\t"
        "v_mfma_f32_32x32x16_bf16 v[16:31], v[44:47], v[116:119], v[16:31]\n\t"
        "ds_read_b64_tr_b16 v[44:45], v194 offset:34816\n\t"
        "ds_read_b64_tr_b16 v[46:47], v194 offset:35840\n\t"
        "buffer_load_dwordx4 v[116:119], v192, s[40:43], s44 offen offset:1024 sc1\n\t"
        "s_waitcnt vmcnt(31) lgkmcnt(14)\n\t"
        "v_mfma_f32_32x32x16_bf16 v[0:15], v[48:51], v[120:123], v[0:15]\n\t"
        "ds_read_b64_tr_b16 v[48:49], v193 offset:36864\n\t"
        "ds_read_b64_tr_b16 v[50:51], v193 offset:37888\n\t"
        "s_waitcnt lgkmcnt(14)\n\t"
        "v_mfma_f32_32x32x16_bf16 v[16:31], v[52:55], v[120:123], v[16:31]\n\t"
        "ds_read_b64_tr_b16 v[52:53], v194 offset:36864\n\t"
        "ds_read_b64_tr_b16 v[54:55], v194 offset:37888\n\t"
        "buffer_load_dwordx4 v[120:123], v192, s[40:43], s44 offen offset:2048 sc1\n\t"
        "s_waitcnt vmcnt(31) lgkmcnt(14)\n\t"
        "v_mfma_f32_32x32x16_bf16 v[0:15], v[56:59], v[124:127], v[0:15]\n\t"
        "ds_read_b64_tr_b16 v[56:57], v193 offset:38912\n\t"
        "ds_read_b64_tr_b16 v[58:59], v193 offset:39936\n\t"
        "s_waitcnt lgkmcnt(14)\n\t"
        "v_mfma_f32_32x32x16_bf16 v[16:31], v[60:63], v[124:127], v[16:31]\n\t"
        "ds_read_b64_tr_b16 v[60:61], v194 offset:38912\n\t"
        "ds_read_b64_tr_b16 v[62:63], v194 offset:39936\n\t"
        "buffer_load_dwordx4 v[124:127], v192, s[40:43], s44 offen offset:3072 sc1\n\t"
        "s_waitcnt vmcnt(31) lgkmcnt(14)\n\t"
        "v_mfma_f32_32x32x16_bf16 v[0:15], v[32:35], v[128:131], v[0:15]\n\t"
        "ds_read_b64_tr_b16 v[32:33], v193 offset:40960\n\t"
        "ds_read_b64_tr_b16 v[34:35], v193 offset:41984\n\t"
        "s_waitcnt lgkmcnt(14)\n\t"
        "v_mfma_f32_32x32x16_bf16 v[16:31], v[36:39], v[128:131], v[16:31]\n\t"
        "ds_read_b64_tr_b16 v[36:37], v194 offset:40960\n\t"
        "ds_read_b64_tr_b16 v[38:39], v194 offset:41984\n\t"
        "s_add_u32 s44, s44, 0x1000\n\t"
        "s_and_b32 s44, s44, 0xffff\n\t"
        "buffer_load_dwordx4 v[128:131], v192, s[40:43], s44 offen offset:0 sc1\n\t"
        "s_waitcnt vmcnt(31) lgkmcnt(14)\n\t"
        "v_mfma_f32_32x32x16_bf16 v[0:15], v[40:43], v[132:135], v[0:15]\n\t"
        "ds_read_b64_tr_b16 v[40:41], v193 offset:43008\n\t"
        "ds_read_b64_tr_b16 v[42:43], v193 offset:44032\n\t"
        "s_waitcnt lgkmcnt(14)\n\t"
        "v_mfma_f32_32x32x16_bf16 v[16:31], v[44:47], v[132:135], v[16:31]\n\t"
        "ds_read_b64_tr_b16 v[44:45], v194 offset:43008\n\t"
        "ds_read_b64_tr_b16 v[46:47], v194 offset:44032\n\t"
        "buffer_load_dwordx4 v[132:135], v192, s[40:43], s44 offen offset:1024 sc1\n\t"
        "s_waitcnt vmcnt(31) lgkmcnt(14)\n\t"
        "v_mfma_f32_32x32x16_bf16 v[0:15], v[48:51], v[136:139], v[0:15]\n\t"
        "ds_read_b64_tr_b16 v[48:49], v193 offset:45056\n\t"
        "ds_read_b64_tr_b16 v[50:51], v193 offset:46080\n\t"
        "s_waitcnt lgkmcnt(14)\n\t"
        "v_mfma_f32_32x32x16_bf16 v[16:31], v[52:55], v[136:139], v[16:31]\n\t"
        "ds_read_b64_tr_b16 v[52:53], v194 offset:45056\n\t"
        "ds_read_b64_tr_b16 v[54:55], v194 offset:46080\n\t"
        "buffer_load_dwordx4 v[136:139], v192, s[40:43], s44 offen offset:2048 sc1\n\t"
        "s_waitcnt vmcnt(31) lgkmcnt(14)\n\t"
        "v_mfma_f32_32x32x16_bf16 v[0:15], v[56:59], v[140:143], v[0:15]\n\t"
        "ds_read_b64_tr_b16 v[56:57], v193 offset:47104\n\t"
        "ds_read_b64_tr_b16 v[58:59], v193 offset:48128\n\t"
        "s_waitcnt lgkmcnt(14)\n\t"
        "v_mfma_f32_32x32x16_bf16 v[16:31], v[60:63], v[140:143], v[16:31]\n\t"
        "ds_read_b64_tr_b16 v[60:61], v194 offset:47104\n\t"
        "ds_read_b64_tr_b16 v[62:63], v194 offset:48128\n\t"
        "buffer_load_dwordx4 v[140:143], v192, s[40:43], s44 offen offset:3072 sc1\n\t"
        "s_waitcnt vmcnt(31) lgkmcnt(14)\n\t"
        "v_mfma_f32_32x32x16_bf16 v[0:15], v[32:35], v[144:147], v[0:15]\n\t"
        "ds_read_b64_tr_b16 v[32:33], v193 offset:49152\n\t"
        "ds_read_b64_tr_b16 v[34:35], v193 offset:50176\n\t"
        "s_waitcnt lgkmcnt(14)\n\t"
        "v_mfma_f32_32x32x16_bf16 v[16:31], v[36:39], v[144:147], v[16:31]\n\t"
        "ds_read_b64_tr_b16 v[36:37], v194 offset:49152\n\t"
        "ds_read_b64_tr_b16 v[38:39], v194 offset:50176\n\t"
        "s_add_u32 s44, s44, 0x1000\n\t"
        "s_and_b32 s44, s44, 0xffff\n\t"
        "buffer_load_dwordx4 v[144:147], v192, s[40:43], s44 offen offset:0 sc1\n\t"
        "s_waitcnt vmcnt(31) lgkmcnt(14)\n\t"
        "v_mfma_f32_32x32x16_bf16 v[0:15], v[40:43], v[148:151], v[0:15]\n\t"
        "ds_read_b64_tr_b16 v[40:41], v193 offset:51200\n\t"
        "ds_read_b64_tr_b16 v[42:43], v193 offset:52224\n\t"
        "s_waitcnt lgkmcnt(14)\n\t"
        "v_mfma_f32_32x32x16_bf16 v[16:31], v[44:47], v[148:151], v[16:31]\n\t"
        "ds_read_b64_tr_b16 v[44:45], v194 offset:51200\n\t"
        "ds_read_b64_tr_b16 v[46:47], v194 offset:52224\n\t"
        "buffer_load_dwordx4 v[148:151], v192, s[40:43], s44 offen offset:1024 sc1\n\t"
        "s_waitcnt vmcnt(31) lgkmcnt(14)\n\t"
        "v_mfma_f32_32x32x16_bf16 v[0:15], v[48:51], v[152:155], v[0:15]\n\t"
        "ds_read_b64_tr_b16 v[48:49], v193 offset:53248\n\t"
        "ds_read_b64_tr_b16 v[50:51], v193 offset:54272\n\t"
        "s_waitcnt lgkmcnt(14)\n\t"
        "v_mfma_f32_32x32x16_bf16 v[16:31], v[52:55], v[152:155], v[16:31]\n\t"
        "ds_read_b64_tr_b16 v[52:53], v194 offset:53248\n\t"
        "ds_read_b64_tr_b16 v[54:55], v194 offset:54272\n\t"
        "buffer_load_dwordx4 v[152:155], v192, s[40:43], s44 offen offset:2048 sc1\n\t"
        "s_waitcnt vmcnt(31) lgkmcnt(14)\n\t"
        "v_mfma_f32_32x32x16_bf16 v[0:15], v[56:59], v[156:159], v[0:15]\n\t"
        "ds_read_b64_tr_b16 v[56:57], v193 offset:55296\n\t"
        "ds_read_b64_tr_b16 v[58:59], v193 offset:56320\n\t"
        "s_waitcnt lgkmcnt(14)\n\t"
        "v_mfma_f32_32x32x16_bf16 v[16:31], v[60:63], v[156:159], v[16:31]\n\t"
        "ds_read_b64_tr_b16 v[60:61], v194 offset:55296\n\t"
        "ds_read_b64_tr_b16 v[62:63], v194 offset:56320\n\t"
        "buffer_load_dwordx4 v[156:159], v192, s[40:43], s44 offen offset:3072 sc1\n\t"
        "s_waitcnt vmcnt(31) lgkmcnt(14)\n\t"
        "v_mfma_f32_32x32x16_bf16 v[0:15], v[32:35], v[160:163], v[0:15]\n\t"
        "ds_read_b64_tr_b16 v[32:33], v193 offset:57344\n\t"
        "ds_read_b64_tr_b16 v[34:35], v193 offset:58368\n\t"
        "s_waitcnt lgkmcnt(14)\n\t"
        "v_mfma_f32_32x32x16_bf16 v[16:31], v[36:39], v[160:163], v[16:31]\n\t"
        "ds_read_b64_tr_b16 v[36:37], v194 offset:57344\n\t"
        "ds_read_b64_tr_b16 v[38:39], v194 offset:58368\n\t"
        "s_add_u32 s44, s44, 0x1000\n\t"
        "s_and_b32 s44, s44, 0xffff\n\t"
        "buffer_load_dwordx4 v[160:163], v192, s[40:43], s44 offen offset:0 sc1\n\t"
        "s_waitcnt vmcnt(31) lgkmcnt(14)\n\t"
        "v_mfma_f32_32x32x16_bf16 v[0:15], v[40:43], v[164:167], v[0:15]\n\t"
        "ds_read_b64_tr_b16 v[40:41], v193 offset:59392\n\t"
        "ds_read_b64_tr_b16 v[42:43], v193 offset:60416\n\t"
        "s_waitcnt lgkmcnt(14)\n\t"
        "v_mfma_f32_32x32x16_bf16 v[16:31], v[44:47], v[164:167], v[16:31]\n\t"
        "ds_read_b64_tr_b16 v[44:45], v194 offset:59392\n\t"
        "ds_read_b64_tr_b16 v[46:47], v194 offset:60416\n\t"
        "buffer_load_dwordx4 v[164:167], v192, s[40:43], s44 offen offset:1024 sc1\n\t"
        "s_waitcnt vmcnt(31) lgkmcnt(14)\n\t"
        "v_mfma_f32_32x32x16_bf16 v[0:15], v[48:51], v[168:171], v[0:15]\n\t"
        "ds_read_b64_tr_b16 v[48:49], v193 offset:61440\n\t"
        "ds_read_b64_tr_b16 v[50:51], v193 offset:62464\n\t"
        "s_waitcnt lgkmcnt(14)\n\t"
        "v_mfma_f32_32x32x16_bf16 v[16:31], v[52:55], v[168:171], v[16:31]\n\t"
        "ds_read_b64_tr_b16 v[52:53], v194 offset:61440\n\t"
        "ds_read_b64_tr_b16 v[54:55], v194 offset:62464\n\t"
        "buffer_load_dwordx4 v[168:171], v192, s[40:43], s44 offen offset:2048 sc1\n\t"
        "s_waitcnt vmcnt(31) lgkmcnt(14)\n\t"
        "v_mfma_f32_32x32x16_bf16 v[0:15], v[56:59], v[172:175], v[0:15]\n\t"
        "ds_read_b64_tr_b16 v[56:57], v193 offset:63488\n\t"
        "ds_read_b64_tr_b16 v[58:59], v193 offset:64512\n\t"
        "s_waitcnt lgkmcnt(14)\n\t"
        "v_mfma_f32_32x32x16_bf16 v[16:31], v[60:63], v[172:175], v[16:31]\n\t"
        "ds_read_b64_tr_b16 v[60:61], v194 offset:63488\n\t"
        "ds_read_b64_tr_b16 v[62:63], v194 offset:64512\n\t"
        "buffer_load_dwordx4 v[172:175], v192, s[40:43], s44 offen offset:3072 sc1\n\t"
        "s_waitcnt vmcnt(31) lgkmcnt(14)\n\t"
        "v_mfma_f32_32x32x16_bf16 v[0:15], v[32:35], v[176:179], v[0:15]\n\t"
        "ds_read_b64_tr_b16 v[32:33], v195 offset:0\n\t"
        "ds_read_b64_tr_b16 v[34:35], v195 offset:1024\n\t"
        "s_waitcnt lgkmcnt(14)\n\t"
        "v_mfma_f32_32x32x16_bf16 v[16:31], v[36:39], v[176:179], v[16:31]\n\t"
        "ds_read_b64_tr_b16 v[36:37], v196 offset:0\n\t"
        "ds_read_b64_tr_b16 v[38:39], v196 offset:1024\n\t"
        "s_add_u32 s44, s44, 0x1000\n\t"
        "s_and_b32 s44, s44, 0xffff\n\t"
        "buffer_load_dwordx4 v[176:179], v192, s[40:43], s44 offen offset:0 sc1\n\t"
        "s_waitcnt vmcnt(31) lgkmcnt(14)\n\t"
        "v_mfma_f32_32x32x16_bf16 v[0:15], v[40:43], v[180:183], v[0:15]\n\t"
        "ds_read_b64_tr_b16 v[40:41], v195 offset:2048\n\t"
        "ds_read_b64_tr_b16 v[42:43], v195 offset:3072\n\t"
        "s_waitcnt lgkmcnt(14)\n\t"
        "v_mfma_f32_32x32x16_bf16 v[16:31], v[44:47], v[180:183], v[16:31]\n\t"
        "ds_read_b64_tr_b16 v[44:45], v196 offset:2048\n\t"
        "ds_read_b64_tr_b16 v[46:47], v196 offset:3072\n\t"
        "buffer_load_dwordx4 v[180:183], v192, s[40:43], s44 offen offset:1024 sc1\n\t"
        "s_waitcnt vmcnt(31) lgkmcnt(14)\n\t"
        "v_mfma_f32_32x32x16_bf16 v[0:15], v[48:51], v[184:187], v[0:15]\n\t"
        "ds_read_b64_tr_b16 v[48:49], v195 offset:4096\n\t"
        "ds_read_b64_tr_b16 v[50:51], v195 offset:5120\n\t"
        "s_waitcnt lgkmcnt(14)\n\t"
        "v_mfma_f32_32x32x16_bf16 v[16:31], v[52:55], v[184:187], v[16:31]\n\t"
        "ds_read_b64_tr_b16 v[52:53], v196 offset:4096\n\t"
        "ds_read_b64_tr_b16 v[54:55], v196 offset:5120\n\t"
        "buffer_load_dwordx4 v[184:187], v192, s[40:43], s44 offen offset:2048 sc1\n\t"
        "s_waitcnt vmcnt(31) lgkmcnt(14)\n\t"
        "v_mfma_f32_32x32x16_bf16 v[0:15], v[56:59], v[188:191], v[0:15]\n\t"
        "ds_read_b64_tr_b16 v[56:57], v195 offset:6144\n\t"
        "ds_read_b64_tr_b16 v[58:59], v195 offset:7168\n\t"
        "s_waitcnt lgkmcnt(14)\n\t"
        "v_mfma_f32_32x32x16_bf16 v[16:31], v[60:63], v[188:191], v[16:31]\n\t"
        "ds_read_b64_tr_b16 v[60:61], v196 offset:6144\n\t"
        "ds_read_b64_tr_b16 v[62:63], v196 offset:7168\n\t"
        "buffer_load_dwordx4 v[188:191], v192, s[40:43], s44 offen offset:3072 sc1\n\t"
        "s_waitcnt vmcnt(31) lgkmcnt(14)\n\t"
        "v_mfma_f32_32x32x16_bf16 v[0:15], v[32:35], v[64:67], v[0:15]\n\t"
        "ds_read_b64_tr_b16 v[32:33], v195 offset:8192\n\t"
        "ds_read_b64_tr_b16 v[34:35], v195 offset:9216\n\t"
        "s_waitcnt lgkmcnt(14)\n\t"
        "v_mfma_f32_32x32x16_bf16 v[16:31], v[36:39], v[64:67], v[16:31]\n\t"
        "ds_read_b64_tr_b16 v[36:37], v196 offset:8192\n\t"
        "ds_read_b64_tr_b16 v[38:39], v196 offset:9216\n\t"
        "s_waitcnt vmcnt(30) lgkmcnt(14)\n\t"
        "v_mfma_f32_32x32x16_bf16 v[0:15], v[40:43], v[68:71], v[0:15]\n\t"
        "ds_read_b64_tr_b16 v[40:41], v195 offset:10240\n\t"
        "ds_read_b64_tr_b16 v[42:43], v195 offset:11264\n\t"
        "s_waitcnt lgkmcnt(14)\n\t"
        "v_mfma_f32_32x32x16_bf16 v[16:31], v[44:47], v[68:71], v[16:31]\n\t"
        "ds_read_b64_tr_b16 v[44:45], v196 offset:10240\n\t"
        "ds_read_b64_tr_b16 v[46:47], v196 offset:11264\n\t"
        "s_waitcnt vmcnt(29) lgkmcnt(14)\n\t"
        "v_mfma_f32_32x32x16_bf16 v[0:15], v[48:51], v[72:75], v[0:15]\n\t"
        "ds_read_b64_tr_b16 v[48:49], v195 offset:12288\n\t"
        "ds_read_b64_tr_b16 v[50:51], v195 offset:13312\n\t"
        "s_waitcnt lgkmcnt(14)\n\t"
        "v_mfma_f32_32x32x16_bf16 v[16:31], v[52:55], v[72:75], v[16:31]\n\t"
        "ds_read_b64_tr_b16 v[52:53], v196 offset:12288\n\t"
        "ds_read_b64_tr_b16 v[54:55], v196 offset:13312\n\t"
        "s_waitcnt vmcnt(28) lgkmcnt(14)\n\t"
        "v_mfma_f32_32x32x16_bf16 v[0:15], v[56:59], v[76:79], v[0:15]\n\t"
        "ds_read_b64_tr_b16 v[56:57], v195 offset:14336\n\t"
        "ds_read_b64_tr_b16 v[58:59], v195 offset:15360\n\t"
        "s_waitcnt lgkmcnt(14)\n\t"
        "v_mfma_f32_32x32x16_bf16 v[16:31], v[60:63], v[76:79], v[16:31]\n\t"
        "ds_read_b64_tr_b16 v[60:61], v196 offset:14336\n\t"
        "ds_read_b64_tr_b16 v[62:63], v196 offset:15360\n\t"
        "s_waitcnt vmcnt(27) lgkmcnt(14)\n\t"
        "v_mfma_f32_32x32x16_bf16 v[0:15], v[32:35], v[80:83], v[0:15]\n\t"
        "ds_read_b64_tr_b16 v[32:33], v195 offset:16384\n\t"
        "ds_read_b64_tr_b16 v[34:35], v195 offset:17408\n\t"
        "s_waitcnt lgkmcnt(14)\n\t"
        "v_mfma_f32_32x32x16_bf16 v[16:31], v[36:39], v[80:83], v[16:31]\n\t"
        "ds_read_b64_tr_b16 v[36:37], v196 offset:16384\n\t"
        "ds_read_b64_tr_b16 v[38:39], v196 offset:17408\n\t"
        "s_waitcnt vmcnt(26) lgkmcnt(14)\n\t"
        "v_mfma_f32_32x32x16_bf16 v[0:15], v[40:43], v[84:87], v[0:15]\n\t"
        "ds_read_b64_tr_b16 v[40:41], v195 offset:18432\n\t"
        "ds_read_b64_tr_b16 v[42:43], v195 offset:19456\n\t"
        "s_waitcnt lgkmcnt(14)\n\t"
        "v_mfma_f32_32x32x16_bf16 v[16:31], v[44:47], v[84:87], v[16:31]\n\t"
        "ds_read_b64_tr_b16 v[44:45], v196 offset:18432\n\t"
        "ds_read_b64_tr_b16 v[46:47], v196 offset:19456\n\t"
        "s_waitcnt vmcnt(25) lgkmcnt(14)\n\t"
        "v_mfma_f32_32x32x16_bf16 v[0:15], v[48:51], v[88:91], v[0:15]\n\t"
        "ds_read_b64_tr_b16 v[48:49], v195 offset:20480\n\t"
        "ds_read_b64_tr_b16 v[50:51], v195 offset:21504\n\t"
        "s_waitcnt lgkmcnt(14)\n\t"
        "v_mfma_f32_32x32x16_bf16 v[16:31], v[52:55], v[88:91], v[16:31]\n\t"
        "ds_read_b64_tr_b16 v[52:53], v196 offset:20480\n\t"
        "ds_read_b64_tr_b16 v[54:55], v196 offset:21504\n\t"
        "s_waitcnt vmcnt(24) lgkmcnt(14)\n\t"
        "v_mfma_f32_32x32x16_bf16 v[0:15], v[56:59], v[92:95], v[0:15]\n\t"
        "ds_read_b64_tr_b16 v[56:57], v195 offset:22528\n\t"
        "ds_read_b64_tr_b16 v[58:59], v195 offset:23552\n\t"
        "s_waitcnt lgkmcnt(14)\n\t"
        "v_mfma_f32_32x32x16_bf16 v[16:31], v[60:63], v[92:95], v[16:31]\n\t"
        "ds_read_b64_tr_b16 v[60:61], v196 offset:22528\n\t"
        "ds_read_b64_tr_b16 v[62:63], v196 offset:23552\n\t"
        "s_waitcnt vmcnt(23) lgkmcnt(14)\n\t"
        "v_mfma_f32_32x32x16_bf16 v[0:15], v[32:35], v[96:99], v[0:15]\n\t"
        "ds_read_b64_tr_b16 v[32:33], v195 offset:24576\n\t"
        "ds_read_b64_tr_b16 v[34:35], v195 offset:25600\n\t"
        "s_waitcnt lgkmcnt(14)\n\t"
        "v_mfma_f32_32x32x16_bf16 v[16:31], v[36:39], v[96:99], v[16:31]\n\t"
        "ds_read_b64_tr_b16 v[36:37], v196 offset:24576\n\t"
        "ds_read_b64_tr_b16 v[38:39], v196 offset:25600\n\t"
        "s_waitcnt vmcnt(22) lgkmcnt(14)\n\t"
        "v_mfma_f32_32x32x16_bf16 v[0:15], v[40:43], v[100:103], v[0:15]\n\t"
        "ds_read_b64_tr_b16 v[40:41], v195 offset:26624\n\t"
        "ds_read_b64_tr_b16 v[42:43], v195 offset:27648\n\t"
        "s_waitcnt lgkmcnt(14)\n\t"
        "v_mfma_f32_32x32x16_bf16 v[16:31], v[44:47], v[100:103], v[16:31]\n\t"
        "ds_read_b64_tr_b16 v[44:45], v196 offset:26624\n\t"
        "ds_read_b64_tr_b16 v[46:47], v196 offset:27648\n\t"
        "s_waitcnt vmcnt(21) lgkmcnt(14)\n\t"
        "v_mfma_f32_32x32x16_bf16 v[0:15], v[48:51], v[104:107], v[0:15]\n\t"
        "ds_read_b64_tr_b16 v[48:49], v195 offset:28672\n\t"
        "ds_read_b64_tr_b16 v[50:51], v195 offset:29696\n\t"
        "s_waitcnt lgkmcnt(14)\n\t"
        "v_mfma_f32_32x32x16_bf16 v[16:31], v[52:55], v[104:107], v[16:31]\n\t"
        "ds_read_b64_tr_b16 v[52:53], v196 offset:28672\n\t"
        "ds_read_b64_tr_b16 v[54:55], v196 offset:29696\n\t"
        "s_waitcnt vmcnt(20) lgkmcnt(14)\n\t"
        "v_mfma_f32_32x32x16_bf16 v[0:15], v[56:59], v[108:111], v[0:15]\n\t"
        "ds_read_b64_tr_b16 v[56:57], v195 offset:30720\n\t"
        "ds_read_b64_tr_b16 v[58:59], v195 offset:31744\n\t"
        "s_waitcnt lgkmcnt(14)\n\t"
        "v_mfma_f32_32x32x16_bf16 v[16:31], v[60:63], v[108:111], v[16:31]\n\t"
        "ds_read_b64_tr_b16 v[60:61], v196 offset:30720\n\t"
        "ds_read_b64_tr_b16 v[62:63], v196 offset:31744\n\t"
        "s_waitcnt vmcnt(19) lgkmcnt(14)\n\t"
        "v_mfma_f32_32x32x16_bf16 v[0:15], v[32:35], v[112:115], v[0:15]\n\t"
        "ds_read_b64_tr_b16 v[32:33], v195 offset:32768\n\t"
        "ds_read_b64_tr_b16 v[34:35], v195 offset:33792\n\t"
        "s_waitcnt lgkmcnt(14)\n\t"
        "v_mfma_f32_32x32x16_bf16 v[16:31], v[36:39], v[112:115], v[16:31]\n\t"
        "ds_read_b64_tr_b16 v[36:37], v196 offset:32768\n\t"
        "ds_read_b64_tr_b16 v[38:39], v196 offset:33792\n\t"
        "s_waitcnt vmcnt(18) lgkmcnt(14)\n\t"
        "v_mfma_f32_32x32x16_bf16 v[0:15], v[40:43], v[116:119], v[0:15]\n\t"
        "ds_read_b64_tr_b16 v[40:41], v195 offset:34816\n\t"
        "ds_read_b64_tr_b16 v[42:43], v195 offset:35840\n\t"
        "s_waitcnt lgkmcnt(14)\n\t"
        "v_mfma_f32_32x32x16_bf16 v[16:31], v[44:47], v[116:119], v[16:31]\n\t"
        "ds_read_b64_tr_b16 v[44:45], v196 offset:34816\n\t"
        "ds_read_b64_tr_b16 v[46:47], v196 offset:35840\n\t"
        "s_waitcnt vmcnt(17) lgkmcnt(14)\n\t"
        "v_mfma_f32_32x32x16_bf16 v[0:15], v[48:51], v[120:123], v[0:15]\n\t"
        "ds_read_b64_tr_b16 v[48:49], v195 offset:36864\n\t"
        "ds_read_b64_tr_b16 v[50:51], v195 offset:37888\n\t"
        "s_waitcnt lgkmcnt(14)\n\t"
        "v_mfma_f32_32x32x16_bf16 v[16:31], v[52:55], v[120:123], v[16:31]\n\t"
        "ds_read_b64_tr_b16 v[52:53], v196 offset:36864\n\t"
        "ds_read_b64_tr_b16 v[54:55], v196 offset:37888\n\t"
        "s_waitcnt vmcnt(16) lgkmcnt(14)\n\t"
        "v_mfma_f32_32x32x16_bf16 v[0:15], v[56:59], v[124:127], v[0:15]\n\t"
        "ds_read_b64_tr_b16 v[56:57], v195 offset:38912\n\t"
        "ds_read_b64_tr_b16 v[58:59], v195 offset:39936\n\t"
        "s_waitcnt lgkmcnt(14)\n\t"
        "v_mfma_f32_32x32x16_bf16 v[16:31], v[60:63], v[124:127], v[16:31]\n\t"
        "ds_read_b64_tr_b16 v[60:61], v196 offset:38912\n\t"
        "ds_read_b64_tr_b16 v[62:63], v196 offset:39936\n\t"
        "s_waitcnt vmcnt(15) lgkmcnt(14)\n\t"
        "v_mfma_f32_32x32x16_bf16 v[0:15], v[32:35], v[128:131], v[0:15]\n\t"
        "ds_read_b64_tr_b16 v[32:33], v195 offset:40960\n\t"
        "ds_read_b64_tr_b16 v[34:35], v195 offset:41984\n\t"
        "s_waitcnt lgkmcnt(14)\n\t"
        "v_mfma_f32_32x32x16_bf16 v[16:31], v[36:39], v[128:131], v[16:31]\n\t"
        "ds_read_b64_tr_b16 v[36:37], v196 offset:40960\n\t"
        "ds_read_b64_tr_b16 v[38:39], v196 offset:41984\n\t"
        "s_waitcnt vmcnt(14) lgkmcnt(14)\n\t"
        "v_mfma_f32_32x32x16_bf16 v[0:15], v[40:43], v[132:135], v[0:15]\n\t"
        "ds_read_b64_tr_b16 v[40:41], v195 offset:43008\n\t"
        "ds_read_b64_tr_b16 v[42:43], v195 offset:44032\n\t"
        "s_waitcnt lgkmcnt(14)\n\t"
        "v_mfma_f32_32x32x16_bf16 v[16:31], v[44:47], v[132:135], v[16:31]\n\t"
        "ds_read_b64_tr_b16 v[44:45], v196 offset:43008\n\t"
        "ds_read_b64_tr_b16 v[46:47], v196 offset:44032\n\t"
        "s_waitcnt vmcnt(13) lgkmcnt(14)\n\t"
        "v_mfma_f32_32x32x16_bf16 v[0:15], v[48:51], v[136:139], v[0:15]\n\t"
        "ds_read_b64_tr_b16 v[48:49], v195 offset:45056\n\t"
        "ds_read_b64_tr_b16 v[50:51], v195 offset:46080\n\t"
        "s_waitcnt lgkmcnt(14)\n\t"
        "v_mfma_f32_32x32x16_bf16 v[16:31], v[52:55], v[136:139], v[16:31]\n\t"
        "ds_read_b64_tr_b16 v[52:53], v196 offset:45056\n\t"
        "ds_read_b64_tr_b16 v[54:55], v196 offset:46080\n\t"
        "s_waitcnt vmcnt(12) lgkmcnt(14)\n\t"
        "v_mfma_f32_32x32x16_bf16 v[0:15], v[56:59], v[140:143], v[0:15]\n\t"
        "ds_read_b64_tr_b16 v[56:57], v195 offset:47104\n\t"
        "ds_read_b64_tr_b16 v[58:59], v195 offset:48128\n\t"
        "s_waitcnt lgkmcnt(14)\n\t"
        "v_mfma_f32_32x32x16_bf16 v[16:31], v[60:63], v[140:143], v[16:31]\n\t"
        "ds_read_b64_tr_b16 v[60:61], v196 offset:47104\n\t"
        "ds_read_b64_tr_b16 v[62:63], v196 offset:48128\n\t"
        "s_waitcnt vmcnt(11) lgkmcnt(14)\n\t"
        "v_mfma_f32_32x32x16_bf16 v[0:15], v[32:35], v[144:147], v[0:15]\n\t"
        "ds_read_b64_tr_b16 v[32:33], v195 offset:49152\n\t"
        "ds_read_b64_tr_b16 v[34:35], v195 offset:50176\n\t"
        "s_waitcnt lgkmcnt(14)\n\t"
        "v_mfma_f32_32x32x16_bf16 v[16:31], v[36:39], v[144:147], v[16:31]\n\t"
        "ds_read_b64_tr_b16 v[36:37], v196 offset:49152\n\t"
        "ds_read_b64_tr_b16 v[38:39], v196 offset:50176\n\t"
        "s_waitcnt vmcnt(10) lgkmcnt(14)\n\t"
        "v_mfma_f32_32x32x16_bf16 v[0:15], v[40:43], v[148:151], v[0:15]\n\t"
        "ds_read_b64_tr_b16 v[40:41], v195 offset:51200\n\t"
        "ds_read_b64_tr_b16 v[42:43], v195 offset:52224\n\t"
        "s_waitcnt lgkmcnt(14)\n\t"
        "v_mfma_f32_32x32x16_bf16 v[16:31], v[44:47], v[148:151], v[16:31]\n\t"
        "ds_read_b64_tr_b16 v[44:45], v196 offset:51200\n\t"
        "ds_read_b64_tr_b16 v[46:47], v196 offset:52224\n\t"
        "s_waitcnt vmcnt(9) lgkmcnt(14)\n\t"
        "v_mfma_f32_32x32x16_bf16 v[0:15], v[48:51], v[152:155], v[0:15]\n\t"
        "ds_read_b64_tr_b16 v[48:49], v195 offset:53248\n\t"
        "ds_read_b64_tr_b16 v[50:51], v195 offset:54272\n\t"
        "s_waitcnt lgkmcnt(14)\n\t"
        "v_mfma_f32_32x32x16_bf16 v[16:31], v[52:55], v[152:155], v[16:31]\n\t"
        "ds_read_b64_tr_b16 v[52:53], v196 offset:53248\n\t"
        "ds_read_b64_tr_b16 v[54:55], v196 offset:54272\n\t"
        "s_waitcnt vmcnt(8) lgkmcnt(14)\n\t"
        "v_mfma_f32_32x32x16_bf16 v[0:15], v[56:59], v[156:159], v[0:15]\n\t"
        "ds_read_b64_tr_b16 v[56:57], v195 offset:55296\n\t"
        "ds_read_b64_tr_b16 v[58:59], v195 offset:56320\n\t"
        "s_waitcnt lgkmcnt(14)\n\t"
        "v_mfma_f32_32x32x16_bf16 v[16:31], v[60:63], v[156:159], v[16:31]\n\t"
        "ds_read_b64_tr_b16 v[60:61], v196 offset:55296\n\t"
        "ds_read_b64_tr_b16 v[62:63], v196 offset:56320\n\t"
        "s_waitcnt vmcnt(7) lgkmcnt(14)\n\t"
        "v_mfma_f32_32x32x16_bf16 v[0:15], v[32:35], v[160:163], v[0:15]\n\t"
        "s_barrier\n\t"
        "ds_read_b64_tr_b16 v[32:33], v195 offset:57344\n\t"
        "ds_read_b64_tr_b16 v[34:35], v195 offset:58368\n\t"
        "s_waitcnt lgkmcnt(14)\n\t"
        "v_mfma_f32_32x32x16_bf16 v[16:31], v[36:39], v[160:163], v[16:31]\n\t"
        "ds_read_b64_tr_b16 v[36:37], v196 offset:57344\n\t"
        "ds_read_b64_tr_b16 v[38:39], v196 offset:58368\n\t"
        "s_waitcnt vmcnt(6) lgkmcnt(14)\n\t"
        "v_mfma_f32_32x32x16_bf16 v[0:15], v[40:43], v[164:167], v[0:15]\n\t"
        "ds_read_b64_tr_b16 v[40:41], v195 offset:59392\n\t"
        "ds_read_b64_tr_b16 v[42:43], v195 offset:60416\n\t"
        "s_waitcnt lgkmcnt(14)\n\t"
        "v_mfma_f32_32x32x16_bf16 v[16:31], v[44:47], v[164:167], v[16:31]\n\t"
        "ds_read_b64_tr_b16 v[44:45], v196 offset:59392\n\t"
        "ds_read_b64_tr_b16 v[46:47], v196 offset:60416\n\t"
        "s_waitcnt vmcnt(5) lgkmcnt(14)\n\t"
        "v_mfma_f32_32x32x16_bf16 v[0:15], v[48:51], v[168:171], v[0:15]\n\t"
        "ds_read_b64_tr_b16 v[48:49], v195 offset:61440\n\t"
        "ds_read_b64_tr_b16 v[50:51], v195 offset:62464\n\t"
        "s_waitcnt lgkmcnt(14)\n\t"
        "v_mfma_f32_32x32x16_bf16 v[16:31], v[52:55], v[168:171], v[16:31]\n\t"
        "ds_read_b64_tr_b16 v[52:53], v196 offset:61440\n\t"
        "ds_read_b64_tr_b16 v[54:55], v196 offset:62464\n\t"
        "s_waitcnt vmcnt(4) lgkmcnt(14)\n\t"
        "v_mfma_f32_32x32x16_bf16 v[0:15], v[56:59], v[172:175], v[0:15]\n\t"
        "ds_read_b64_tr_b16 v[56:57], v195 offset:63488\n\t"
        "ds_read_b64_tr_b16 v[58:59], v195 offset:64512\n\t"
        "s_waitcnt lgkmcnt(14)\n\t"
        "v_mfma_f32_32x32x16_bf16 v[16:31], v[60:63], v[172:175], v[16:31]\n\t"
        "ds_read_b64_tr_b16 v[60:61], v196 offset:63488\n\t"
        "ds_read_b64_tr_b16 v[62:63], v196 offset:64512\n\t"
        "s_waitcnt vmcnt(3) lgkmcnt(14)\n\t"
        "v_mfma_f32_32x32x16_bf16 v[0:15], v[32:35], v[176:179], v[0:15]\n\t"
        "s_waitcnt lgkmcnt(12)\n\t"
        "v_mfma_f32_32x32x16_bf16 v[16:31], v[36:39], v[176:179], v[16:31]\n\t"
        "s_waitcnt vmcnt(2) lgkmcnt(10)\n\t"
        "v_mfma_f32_32x32x16_bf16 v[0:15], v[40:43], v[180:183], v[0:15]\n\t"
        "s_waitcnt lgkmcnt(8)\n\t"
        "v_mfma_f32_32x32x16_bf16 v[16:31], v[44:47], v[180:183], v[16:31]\n\t"
        "s_waitcnt vmcnt(1) lgkmcnt(6)\n\t"
        "v_mfma_f32_32x32x16_bf16 v[0:15], v[48:51], v[184:187], v[0:15]\n\t"
        "s_waitcnt lgkmcnt(4)\n\t"
        "v_mfma_f32_32x32x16_bf16 v[16:31], v[52:55], v[184:187], v[16:31]\n\t"
        "s_waitcnt vmcnt(0) lgkmcnt(2)\n\t"
        "v_mfma_f32_32x32x16_bf16 v[0:15], v[56:59], v[188:191], v[0:15]\n\t"
        "s_waitcnt lgkmcnt(0)\n\t"
        "v_mfma_f32_32x32x16_bf16 v[16:31], v[60:63], v[188:191], v[16:31]\n\t"
        "s_nop 11\n\t"
        : "={v[0:15]}"(acc[0]), "={v[16:31]}"(acc[1]), "+{s44}"(soff) STAMP_OUTS
        : "{v192}"(OFF_OF + L * (4 * MBY) + tile * 65536 + lane * 16), "{v193}"(a0), "{v194}"(a1), "{v195}"(a0 + 65536), "{v196}"(a1 + 65536),
          "{v198}"(fl + r32 * 128), "{v199}"(TAG), "{s[40:43]}"(srd)
        : "v32", "v33", "v34", "v35", "v36", "v37", "v38", "v39", "v40", "v41", "v42", "v43", "v44", "v45", "v46", "v47", "v48", "v49", "v50", "v51", "v52", "v53", "v54", "v55", "v56", "v57", "v58", "v59", "v60", "v61", "v62", "v63", "v64", "v65", "v66", "v67", "v68", "v69", "v70", "v71", "v72", "v73", "v74", "v75", "v76", "v77", "v78", "v79", "v80", "v81", "v82", "v83", "v84", "v85", "v86", "v87", "v88", "v89", "v90", "v91", "v92", "v93", "v94", "v95", "v96", "v97", "v98", "v99", "v100", "v101", "v102", "v103", "v104", "v105", "v106", "v107", "v108", "v109", "v110", "v111", "v112", "v113", "v114", "v115", "v116", "v117", "v118", "v119", "v120", "v121", "v122", "v123", "v124", "v125", "v126", "v127", "v128", "v129", "v130", "v131", "v132", "v133", "v134", "v135", "v136", "v137", "v138", "v139", "v140", "v141", "v142", "v143", "v144", "v145", "v146", "v147", "v148", "v149", "v150", "v151", "v152", "v153", "v154", "v155", "v156", "v157", "v158", "v159", "v160", "v161", "v162", "v163", "v164", "v165", "v166", "v167", "v168", "v169", "v170", "v171", "v172", "v173", "v174", "v175", "v176", "v177", "v178", "v179", "v180", "v181", "v182", "v183", "v184", "v185", "v186", "v187", "v188", "v189", "v190", "v191", "v200", "s45", "vcc", "scc", "memory");
      }
      PHASE_VARS;
      STAMP_PUT(12 + 16 * L);
      STAMPV(15 + 16 * L);
      float s = 0.f, sq = 0.f, d = 0.f;
      const unsigned xdst = (L ? OOB : OFF_X1 + (tile * 64 + 4 * tn) * 1024) + lane * 16;
#pragma unroll
      for (int g = 0; g < 8; ++g) {
#pragma unroll
        for (int e = 0; e < 2; ++e) {
          const unsigned rr = res[g >> 1][2 * (g & 1) + e];
          const unsigned t = pack2(acc[g >> 2][4 * (g & 3) + 2 * e], acc[g >> 2][4 * (g & 3) + 2 * e + 1]);
          x1p[g >> 1][2 * (g & 1) + e] = pack2(lo16(rr) + lo16(t), hi16(rr) + hi16(t));
        }
        if (g & 1) { stg(rs, xdst + (g >> 1) * 1024, x1p[g >> 1]); SBAR(); }
      }
#pragma unroll
      for (int g = 0; g < 8; ++g)
#pragma unroll
        for (int e = 0; e < 2; ++e) {
          const unsigned o = x1p[g >> 1][2 * (g & 1) + e];
          s = dot2(o, 0x3f803f80u, s); sq = dot2(o, o, sq); d = dot2(o, wrc[g][e], d);
        }
      pS = half_swap_sum(s); pQ = half_swap_sum(sq); pD = half_swap_sum(d);
    }
    STAMPV(16 + 16 * L);
  }
  {
    PHASE_VARS;
    float S, Q, Dd, mean, var, mb1, rd1;
    row_stats_sum(rs, OFF_SX + SX_LAYER, tile, tn, lane, pS, pQ, pD, S, Q, Dd);
    STAMPV(33);
    row_stats_fin(smem, 2, tile, lane, S, Q, Dd, wsum, x1p, mean, var, mb1, rd1);
    STAMPV(34);
    u32x4 xo[4];
#pragma unroll
    for (int i = 0; i < 4; ++i) {
#pragma unroll
      for (int e = 0; e < 4; ++e) {
        const unsigned h2 = ln_pair(x1p[i][e], mb1, rd1);
        xo[i][e] = pack2(lo16(x1p[i][e]) + lo16(h2), hi16(x1p[i][e]) + hi16(h2));
      }
      xo[i] = fo_swap(xo[i]);
    }
    lds_wait_ge(lbase + L_W + 4 + 4 * (lane & 1), 2);
    if ((int)words[3] != tile * 32 + r32) {
      u16* dst = out + (size_t)(tile * 32 + r32) * D_ + tn * 64 + hi * 8;
#pragma unroll
      for (int i = 0; i < 4; ++i) st16(dst + i * 16, xo[i]);
    }
    STAMPV(35);
  }
}

extern "C" __global__ __launch_bounds__(512) void prep_kernel(const u16* __restrict__ x0, const u16* __restrict__ wqkv, char* __restrict__ ws) {
  __shared__ __attribute__((aligned(16))) char sm[128 * 144];
  const int tid = threadIdx.x, lane = tid & 63, wid = __builtin_amdgcn_readfirstlane(tid >> 6), b = blockIdx.x;
  if (b >= 256) {
    const int xcd = b & 7, slot = (b - 256) >> 3;
    if (tid < (int)(FL_COUNT / 256)) { u32x4 z = {0u, 0u, 0u, 0u}; st16(ws + OFF_FL + ((b - 256) * (FL_COUNT / 256) + tid) * 128, z); }
    unsigned acc = 0;
    {
      const u16* p = wqkv + (size_t)(slot * 32 + (tid >> 5)) * (3 * D_) + (xcd & 3) * 256 + (tid & 31) * 8;
#pragma unroll
      for (int i = 0; i < 6; ++i) {
        const u32x4 v = *reinterpret_cast<const u32x4*>(p + (size_t)(i & 1) * 16 * (3 * D_) + (i >> 1) * D_);
        acc |= v[0] ^ v[3];
      }
    }
    if (slot < 8) {
      const char *c0, *c1;
      CODE_ADDR(mega_kernel, c0); CODE_ADDR(prep_kernel, c1);
      const long long o = (slot * 512 + tid) * 16;
      if (o + 16 <= c1 - c0) acc |= (*reinterpret_cast<const __attribute__((address_space(1))) u32x4*>((uintptr_t)(c0 + o)))[0];
    }
    asm volatile("" :: "v"(acc));
    return;
  }
  u32x4 v[2];
  asm volatile("global_load_dwordx4 %0, %2, off\n\tglobal_load_dwordx4 %1, %2, off offset:1024"
               : "=&v"(v[0]), "=&v"(v[1]) : "v"(x0 + (size_t)(b * 8 + wid) * D_ + lane * 8) : "memory");
  int pass;
  asm volatile("s_mov_b32 %0, %1" : "=s"(pass) : "n"(0));
#pragma unroll 1
  for (;;) {
    asm volatile("s_cmp_eq_u32 %2, 0\n\ts_cbranch_scc1 1f\n\ts_waitcnt vmcnt(0)\n1:" : "+v"(v[0]), "+v"(v[1]) : "s"(pass) : "memory", "scc");
    float s = 0.f;
#pragma unroll
    for (int i = 0; i < 2; ++i)
#pragma unroll
      for (int e = 0; e < 4; ++e) s = dot2(v[i][e], 0x3f803f80u, s);
    const float mean = wave_sum(s) * (1.0f / 1024.0f);
    float m2 = 0.f;
#pragma unroll
    for (int i = 0; i < 2; ++i)
#pragma unroll
      for (int e = 0; e < 4; ++e) { const float a = lo16(v[i][e]) - mean, c = hi16(v[i][e]) - mean; m2 += a * a + c * c; }
    float mb, den;
    ln_scalars(mean, wave_sum(m2) * (1.0f / 1024.0f), mb, den);
    const float rd = 1.0f / den;
#pragma unroll
    for (int i = 0; i < 2; ++i) {
      char* p = sm + ((32 * i + (lane >> 1)) * 2) * 144 + wid * 16 + (lane & 1) * 8;
      u32x2 lo = {ln_pair(v[i][0], mb, rd), ln_pair(v[i][1], mb, rd)}, hi = {ln_pair(v[i][2], mb, rd), ln_pair(v[i][3], mb, rd)};
      *reinterpret_cast<u32x2*>(p) = lo;
      *reinterpret_cast<u32x2*>(p + 144) = hi;
    }
    if (pass) break;
    asm volatile("s_mov_b32 %0, 1" : "=s"(pass));
  }
  __syncthreads();
  char* dst = ws + OFF_HF + (size_t)(b >> 2) * 65536 + (b & 3) * 128;
#pragma unroll
  for (int i = 0; i < 2; ++i) {
    const int q = i * 512 + tid, pc = q >> 3, tk = q & 7;
    st16(dst + pc * 512 + tk * 16, *reinterpret_cast<const u32x4*>(sm + pc * 144 + tk * 16));
  }
}

extern "C" void kernel_launch(void* const* d_in, const int* in_sizes, int n_in, void* d_out, int out_size,
                              void* d_ws, size_t ws_size, hipStream_t stream) {
  char* ws = (char*)d_ws;
  prep_kernel<<<512, 512, 0, stream>>>((const u16*)d_in[0], (const u16*)d_in[1], ws);
  mega_kernel<<<256, 512, 0, stream>>>((const u16*)d_in[0], (const u16*)d_in[1], (const u16*)d_in[2], (const u16*)d_in[4], (const u16*)d_in[5],
                                       (const u16*)d_in[6], (const u16*)d_in[7], ws, (u16*)d_out);
}
```

```cpp
#include <hip/hip_runtime.h>
#include <hip/hip_bf16.h>
#include <stdint.h>

typedef unsigned short u16;
typedef short s16x4 __attribute__((ext_vector_type(4)));
typedef short s16x8 __attribute__((ext_vector_type(8)));
typedef float f32x16 __attribute__((ext_vector_type(16)));
typedef float f32x8 __attribute__((ext_vector_type(8)));
typedef float f32x4 __attribute__((ext_vector_type(4)));
typedef float f32x2 __attribute__((ext_vector_type(2)));
typedef unsigned int u32x16 __attribute__((ext_vector_type(16)));
typedef unsigned int u32x8 __attribute__((ext_vector_type(8)));
typedef unsigned int u32x4 __attribute__((ext_vector_type(4)));
typedef unsigned int u32x2 __attribute__((ext_vector_type(2)));
typedef __bf16 bf16x2 __attribute__((ext_vector_type(2)));
typedef __bf16 bf16x8 __attribute__((ext_vector_type(8)));
typedef __bf16 bf16x16 __attribute__((ext_vector_type(16)));

#define DI __device__ __forceinline__
#define LDS_PTR(T, p) ((__attribute__((address_space(3))) T*)(p))
#define BARRIER() asm volatile("s_barrier" ::: "memory")
#define BAR_LGKM() asm volatile("s_waitcnt lgkmcnt(0)\n\ts_barrier" ::: "memory")
#define BAR_ALL() asm volatile("s_waitcnt vmcnt(0) lgkmcnt(0)\n\ts_barrier" ::: "memory")
#define DRAIN() asm volatile("s_waitcnt vmcnt(0)" ::: "memory")
#define PACE() ({ asm volatile("s_waitcnt vmcnt(3)" ::: "memory"); })
#define MFMA32(a, b, c) __builtin_amdgcn_mfma_f32_32x32x16_bf16(a, b, c, 0, 0, 0)
#define MFMA16(a, b, c) __builtin_amdgcn_mfma_f32_16x16x32_bf16(a, b, c, 0, 0, 0)
#define PIN(x) asm volatile("" : "+v"(x))
#define EX2(x) __builtin_amdgcn_exp2f(x)
#define RCP(x) __builtin_amdgcn_rcpf(x)
#define SBAR() __builtin_amdgcn_sched_barrier(0)

#define STAMPV(i)
#define STAMP_DECL
#define STAMP_OUTS
#define STAMP_PUT(i)
constexpr int D_ = 1024, F_ = 3584;

constexpr unsigned MBY = 1u << 20;
constexpr unsigned TS = 65536 + 4096, HS = 262144 + 4096;
constexpr unsigned OFF_HF = 0, OFF_X1 = 5 * MBY;
constexpr unsigned OFF_OF = 10 * MBY;
constexpr unsigned OFF_KV = 20 * MBY;
constexpr unsigned OFF_FL = 40 * MBY;
constexpr unsigned FL_KV = OFF_FL;
constexpr unsigned FL_O = OFF_FL + 262144;
constexpr unsigned FL_COUNT = 4096;
constexpr unsigned OFF_CS = 41 * MBY;
constexpr unsigned OFF_SX = 42 * MBY;
constexpr unsigned SX_ST = 0;
constexpr unsigned SX_ROW = 512 * 1024;
constexpr unsigned SX_CAND = SX_ROW + 32 * 1024;
constexpr unsigned SX_YP = SX_CAND + 32 * 1024;
constexpr unsigned SX_LAYER = SX_YP + 896 * 1024;
constexpr unsigned SX_QP = 2 * SX_LAYER;
constexpr unsigned SX_BYTES = SX_QP + 512 * 1024;
constexpr unsigned WS_BYTES = 48 * MBY, OOB = 0x80000000u;
constexpr unsigned SENT = 0xFFFFFFFFu, TAG = 1u;

constexpr int NS = 9, DD = 3, SLOTB = 12288;
constexpr int L_HB = 110592, L_XB = L_HB + 2048;
constexpr int L_XCH = 122880;
constexpr int L_CAND = L_XCH + 32768;
constexpr int L_AB = L_XCH + 34816;
constexpr int L_FULL = 159744, L_FREE = L_FULL + 512;
constexpr int L_CS = L_FREE + 1024, L_PATCH = L_CS + 768;
constexpr int L_GU = L_PATCH + 768, L_YS = L_GU + 256, L_WR = L_YS + 256;
constexpr int L_X2F = L_WR + 128, L_X2P = L_X2F + 128;
constexpr int L_W = L_X2P + 128;
constexpr int LDS_BYTES = 163840;
static_assert(NS * SLOTB <= L_HB && L_W + 64 <= LDS_BYTES && L_AB + 2048 <= L_FULL, "LDS map");

DI int fresh_lane() { int l; asm volatile("v_mbcnt_lo_u32_b32 %0, -1, 0\n\tv_mbcnt_hi_u32_b32 %0, -1, %0" : "=v"(l)); return l; }
#define LANE_VARS const int lane = fresh_lane(), r32 = lane & 31, hi = lane >> 5, tq = (lane & 15) >> 2, tp = lane & 3, tblk = (lane >> 4) & 1, \
  wrow = 4 * hi + tq, wsw = 4 * ((wrow >> 1) & 1), wch = 2 * tblk + (tp >> 1), c24 = r32 < 24 ? r32 : 23; \
  (void)r32; (void)hi; (void)wsw; (void)wch; (void)c24
DI int fresh_s(int v) { asm volatile("" : "+s"(v)); return v; }
#define PHASE_VARS const int tm = fresh_s(tm_), tn = fresh_s(tn_), wid = fresh_s(wid_), tile = tm * 4 + wid, hh = wid & 1, kh = wid >> 1; (void)tile; (void)hh; (void)kh; LANE_VARS
DI float bf2f(u16 v) { return __uint_as_float(((unsigned)v) << 16); }
DI unsigned pack2(float lo, float hi) { f32x2 v = {lo, hi}; return __builtin_bit_cast(unsigned, __builtin_convertvector(v, bf16x2)); }
DI float rbf(float f) { return __uint_as_float(pack2(f, 0.f) << 16); }
DI float lo16(unsigned u) { return __uint_as_float(u << 16); }
DI float hi16(unsigned u) { return __uint_as_float(u & 0xffff0000u); }
DI float opq(float r) { asm("" : "+v"(r)); return r; }
DI float fadd(float a, float b) { return opq(a + b); }
DI float fmul(float a, float b) { return opq(a * b); }
DI float ffma(float a, float b, float c) { return opq(__builtin_fmaf(a, b, c)); }
DI float U2F(unsigned u) { return __uint_as_float(u); }
DI float half_swap_sum(float v) {
  auto r = __builtin_amdgcn_permlane32_swap(__float_as_uint(v), __float_as_uint(v), false, false);
  return fadd(U2F(r[0]), U2F(r[1]));
}
DI float half_swap_max(float v) {
  auto r = __builtin_amdgcn_permlane32_swap(__float_as_uint(v), __float_as_uint(v), false, false);
  return fmaxf(__uint_as_float(r[0]), __uint_as_float(r[1]));
}
template <int CTRL> DI unsigned dpp(unsigned v) { return (unsigned)__builtin_amdgcn_update_dpp(0, (int)v, CTRL, 0xf, 0xf, false); }
DI float row_swap_sum(float v) {
  auto r = __builtin_amdgcn_permlane16_swap(__float_as_uint(v), __float_as_uint(v), false, false);
  return fadd(U2F(r[0]), U2F(r[1]));
}
DI float half_sum(float v) {
  v = fadd(v, U2F(dpp<0xB1>(__float_as_uint(v)))); v = fadd(v, U2F(dpp<0x4E>(__float_as_uint(v))));
  v = fadd(v, U2F(dpp<0x141>(__float_as_uint(v)))); v = fadd(v, U2F(dpp<0x140>(__float_as_uint(v))));
  return row_swap_sum(v);
}
DI float wave_sum(float v) { return half_swap_sum(half_sum(v)); }
DI unsigned half_max_u32(unsigned v) {
  v = max(v, dpp<0xB1>(v)); v = max(v, dpp<0x4E>(v)); v = max(v, dpp<0x141>(v)); v = max(v, dpp<0x140>(v));
  auto r = __builtin_amdgcn_permlane16_swap(v, v, false, false);
  return max(r[0], r[1]);
}
DI unsigned wave_max_u32(unsigned v) {
  v = half_max_u32(v);
  auto r = __builtin_amdgcn_permlane32_swap(v, v, false, false);
  return max(r[0], r[1]);
}
DI u32x4 fo_swap(u32x4 v) {
  auto a = __builtin_amdgcn_permlane32_swap(v[0], v[2], false, false);
  auto b = __builtin_amdgcn_permlane32_swap(v[1], v[3], false, false);
  u32x4 o; o[0] = a[0]; o[1] = b[0]; o[2] = a[1]; o[3] = b[1];
  return o;
}
DI void glds16(const void* gsrc, unsigned lds_dst) {
  unsigned keep;
  asm volatile("s_mov_b32 %0, m0\n\ts_mov_b32 m0, %2\n\ts_nop 0\n\tglobal_load_lds_dwordx4 %1, off\n\ts_mov_b32 m0, %0"
               : "=&s"(keep) : "v"(gsrc), "s"(lds_dst) : "memory");
}
#define GL_ "global_load_lds_dwordx4 "
DI void glds_v4(const void* sb, const unsigned (&v)[4], unsigned m0v) {
  unsigned keep;
  asm volatile("s_mov_b32 %0, m0\n\ts_mov_b32 m0, %6\n\ts_nop 0\n\t" GL_ "%1, %5\n\t" GL_ "%2, %5 offset:1024\n\t" GL_ "%3, %5 offset:2048\n\t"
               GL_ "%4, %5 offset:3072\n\ts_mov_b32 m0, %0" : "=&s"(keep) : "v"(v[0]), "v"(v[1]), "v"(v[2]), "v"(v[3]), "s"(sb), "s"(m0v) : "memory");
}
template <int TH> DI void glds_v4t(const void* sb, const unsigned (&v)[4], unsigned m0v) {
  unsigned keep;
  asm volatile("s_mov_b32 %0, m0\n\ts_mov_b32 m0, %6\n\ts_nop 0\n\t" GL_ "%1, %5\n\ts_waitcnt vmcnt(%7)\n\t" GL_ "%2, %5 offset:1024\n\ts_waitcnt vmcnt(%7)\n\t"
               GL_ "%3, %5 offset:2048\n\ts_waitcnt vmcnt(%7)\n\t" GL_ "%4, %5 offset:3072\n\ts_waitcnt vmcnt(%7)\n\ts_mov_b32 m0, %0"
               : "=&s"(keep) : "v"(v[0]), "v"(v[1]), "v"(v[2]), "v"(v[3]), "s"(sb), "s"(m0v), "n"(TH) : "memory");
}
DI void glds_v3(const void* sb, const unsigned (&v)[3], unsigned m0v) {
  unsigned keep;
  asm volatile("s_mov_b32 %0, m0\n\ts_mov_b32 m0, %5\n\ts_nop 0\n\t" GL_ "%1, %4\n\t" GL_ "%2, %4 offset:1024\n\t" GL_ "%3, %4 offset:2048\n\t"
               "s_mov_b32 m0, %0" : "=&s"(keep) : "v"(v[0]), "v"(v[1]), "v"(v[2]), "s"(sb), "s"(m0v) : "memory");
}
DI float dot2(unsigned a, unsigned b, float c) {
  return __builtin_amdgcn_fdot2_f32_bf16(__builtin_bit_cast(bf16x2, a), __builtin_bit_cast(bf16x2, b), c, false);
}
#define CODE_ADDR(sym, out) do { unsigned long long pc_; unsigned off_; \
  asm volatile("s_getpc_b64 %0\n1:\n\ts_mov_b32 %1, " #sym "-1b" : "=s"(pc_), "=s"(off_)); out = (const char*)(pc_ + (long long)(int)off_); } while (0)
DI void st16(void* p, u32x4 v) { asm volatile("global_store_dwordx4 %0, %1, off sc0 sc1\n\ts_nop 1" :: "v"(p), "v"(v) : "memory"); }
DI void ln_scalars(float mean, float var, float& mb, float& den) { mb = rbf(mean); den = rbf(__builtin_amdgcn_sqrtf(rbf(rbf(var) + 1e-5f))); }
DI unsigned ln_pair(unsigned v, float mb, float rd) {
  const unsigned c = pack2(lo16(v) - mb, hi16(v) - mb);
  return pack2(lo16(c) * rd, hi16(c) * rd);
}
DI unsigned ln_pair_s(unsigned v, float nmb, float rd) {
  const unsigned c = pack2(fadd(lo16(v), nmb), fadd(hi16(v), nmb));
  return pack2(fmul(lo16(c), rd), fmul(hi16(c), rd));
}
template <int N, int M> DI f32x4 sum4(const u32x4 (&v)[M]) {
  f32x4 t;
#pragma unroll
  for (int e = 0; e < 4; ++e) {
    t[e] = U2F(v[0][e]);
#pragma unroll
    for (int i = 1; i < N; ++i) t[e] = fadd(t[e], U2F(v[i][e]));
  }
  return t;
}
#define PERM_LO(x, y) __builtin_amdgcn_perm(y, x, 0x05040100u)
#define PERM_HI(x, y) __builtin_amdgcn_perm(y, x, 0x07060302u)
constexpr unsigned SPIN_CAP = 1u << 15;
DI unsigned nz(unsigned v) { return min(v, SENT - 1u); }
DI unsigned nzf(float v) { return nz(__float_as_uint(v)); }
DI unsigned mx4(unsigned m, const u32x4& v) { return max(max(max(m, v[0]), v[1]), max(v[2], v[3])); }
template <int N> DI bool none_sent(const u32x4 (&v)[N]) {
  unsigned m = 0u;
#pragma unroll
  for (int i = 0; i < N; ++i) m = mx4(m, v[i]);
  return m != SENT;
}
typedef __amdgpu_buffer_rsrc_t rsrc_t;
DI u32x4 ldg(rsrc_t rs, unsigned off) { return __builtin_amdgcn_raw_buffer_load_b128(rs, (int)off, 0, 16); }
DI void stg(rsrc_t rs, unsigned off, u32x4 v) { __builtin_amdgcn_raw_buffer_store_b128(v, rs, (int)off, 0, 16); }
template <int N, int SLEEP = 1, class LD, class OKF> DI void poll_all(u32x4 (&out)[N], LD ld, OKF okf) {
  for (unsigned spins = 0;; ++spins) {
    ld(out);
    SBAR();
    if (__all(okf(out)) || spins > SPIN_CAP) break;
    asm volatile("s_sleep %0" :: "n"(SLEEP) : "memory");
  }
}
DI void lds_set(unsigned addr, unsigned v) { asm volatile("s_waitcnt lgkmcnt(0)\n\tds_write_b32 %0, %1" :: "v"(addr), "v"(v) : "memory"); }
template <int SLEEP = 1> DI void lds_wait_ge(unsigned addr, unsigned need) {
  unsigned spin = 0x100000, v;
  asm volatile("1:\n\tds_read_b32 %0, %2\n\ts_sub_u32 %1, %1, 1\n\ts_waitcnt lgkmcnt(0)\n\ts_cbranch_scc1 2f\n\tv_cmp_gt_u32 vcc, %3, %0\n\ts_cbranch_vccz 2f\n\ts_sleep %4\n\ts_branch 1b\n2:"
               : "=&v"(v), "+s"(spin) : "v"(addr), "s"(need), "n"(SLEEP) : "vcc", "scc", "memory");
}
DI s16x8 tr2(const char* p, int second) {
  s16x4 lo = __builtin_amdgcn_ds_read_tr16_b64_v4i16(LDS_PTR(s16x4, p)), hi = __builtin_amdgcn_ds_read_tr16_b64_v4i16(LDS_PTR(s16x4, p + second));
  return __builtin_shufflevector(lo, hi, 0, 1, 2, 3, 4, 5, 6, 7);
}
DI u32x16 cat4(const u32x4& a, const u32x4& b, const u32x4& c, const u32x4& d) {
  const u32x8 x = __builtin_shufflevector(a, b, 0, 1, 2, 3, 4, 5, 6, 7), y = __builtin_shufflevector(c, d, 0, 1, 2, 3, 4, 5, 6, 7);
  return __builtin_shufflevector(x, y, 0, 1, 2, 3, 4, 5, 6, 7, 8, 9, 10, 11, 12, 13, 14, 15);
}

DI unsigned stats_dst(unsigned sx, int tile, int tn, int lane) { return lane < 32 ? sx + SX_ST + tile * 8192 + tn * 512 + lane * 16 : OOB; }
DI void row_stats_sum(rsrc_t rs, unsigned sx, int tile, int lane, float& S, float& Q, float& Dd) {
  const unsigned exb = sx + SX_ST + tile * 8192 + (lane & 31) * 16;
  u32x4 pv[8];
  poll_all<8>(pv, [&](u32x4 (&v)[8]) {
#pragma unroll
    for (int i = 0; i < 8; ++i) v[i] = ldg(rs, exb + ((lane >> 5) * 8 + i) * 512);
  }, [&](const u32x4 (&v)[8]) { return none_sent(v); });
  S = 0.f; Q = 0.f; Dd = 0.f;
#pragma unroll
  for (int i = 0; i < 8; ++i) { S += __uint_as_float(pv[i][0]); Q += __uint_as_float(pv[i][1]); Dd += __uint_as_float(pv[i][2]); }
}
DI void row_stats_fin(char* smem, int epoch, int tile, int lane, float S, float Q, float Dd, float wsum, const u32x4 (&x1p)[4],
                      float& mean, float& var, float& mb, float& rd) {
  S = half_swap_sum(S); Q = half_swap_sum(Q); Dd = half_swap_sum(Dd);
  mean = S * (1.0f / 1024.0f); var = fmaxf(Q * (1.0f / 1024.0f) - mean * mean, 0.f);
  float den;
  ln_scalars(mean, var, mb, den);
  rd = RCP(den);
  const unsigned rb = pack2((Dd - mb * wsum) * rd, 0.f) & 0xffffu;
  const unsigned mine = (((rb & 0x8000u) ? (~rb & 0xffffu) : (rb | 0x8000u)) << 16) | (unsigned)(2047 - (tile * 32 + (lane & 31)));
  const bool best = half_max_u32(mine) == mine;
  if (best) {
    char* c = smem + L_CAND + (tile & 3) * 144;
#pragma unroll
    for (int i = 0; i < 4; ++i) *reinterpret_cast<u32x4*>(c + (2 * i + (lane >> 5)) * 16) = x1p[i];
    *reinterpret_cast<u32x4*>(c + 128) = u32x4{mine, __float_as_uint(mb), __float_as_uint(rd), 0u};
  }
  lds_set((unsigned)(uintptr_t)smem + L_W + 32 + (tile & 3) * 4, epoch);
}

DI void attn_safe(rsrc_t rs, unsigned kvoff, const u32x8& q, const u32x4& ones, f32x16& o) {
  const s16x8 q0 = __builtin_bit_cast(s16x8, __builtin_shufflevector(q, q, 0, 1, 2, 3)), q1 = __builtin_bit_cast(s16x8, __builtin_shufflevector(q, q, 4, 5, 6, 7));
  f32x4 l = {0.f, 0.f, 0.f, 0.f};
#pragma unroll
  for (int e = 0; e < 16; ++e) o[e] = 0.f;
  float m = -INFINITY;
#pragma unroll 1
  for (int kt = 0; kt < 64; ++kt) {
    const unsigned p = kvoff + kt * 4096;
    const u32x4 k0 = ldg(rs, p), k1 = ldg(rs, p + 1024), v0 = ldg(rs, p + 2048), v1 = ldg(rs, p + 3072);
    f32x16 s = {};
    s = MFMA32(__builtin_bit_cast(s16x8, k0), q0, s);
    s = MFMA32(__builtin_bit_cast(s16x8, k1), q1, s);
    float mt = fmaxf(s[0], s[1]);
#pragma unroll
    for (int e = 2; e < 16; ++e) mt = fmaxf(mt, s[e]);
    const float mn = fmaxf(m, half_swap_max(mt));
    const float alpha = EX2(m - mn);
    m = mn;
#pragma unroll
    for (int e = 0; e < 16; ++e) { s[e] = EX2(s[e] - mn); o[e] *= alpha; }
    l[0] *= alpha;
    const bf16x16 pa = __builtin_bit_cast(bf16x16, __builtin_shufflevector(s, s, 0, 1, 2, 3, 4, 5, 6, 7));
    const bf16x16 pb = __builtin_bit_cast(bf16x16, __builtin_shufflevector(s, s, 8, 9, 10, 11, 12, 13, 14, 15));
    o = __builtin_amdgcn_smfmac_f32_32x32x32_bf16(__builtin_bit_cast(bf16x8, v0), pa, o, 0xdddd, 0, 0);
    l = __builtin_amdgcn_smfmac_f32_16x16x64_bf16(__builtin_bit_cast(bf16x8, ones), pa, l, 0xdddd, 0, 0);
    o = __builtin_amdgcn_smfmac_f32_32x32x32_bf16(__builtin_bit_cast(bf16x8, v1), pb, o, 0xdddd, 0, 0);
    l = __builtin_amdgcn_smfmac_f32_16x16x64_bf16(__builtin_bit_cast(bf16x8, ones), pb, l, 0xdddd, 0, 0);
  }
  o *= 1.0f / l[0];
}

extern "C" __global__ __launch_bounds__(512) void mega_kernel(const u16* __restrict__ x0, const u16* __restrict__ wqkv, const u16* __restrict__ wo,
                                                              const u16* __restrict__ w_r, const u16* __restrict__ wg, const u16* __restrict__ wu,
                                                              const u16* __restrict__ wd, char* ws, u16* __restrict__ out) {
  __shared__ __attribute__((aligned(16))) char smem[LDS_BYTES];
  const int wid = __builtin_amdgcn_readfirstlane(threadIdx.x >> 6);
  STAMPV(0);
  const int bid = blockIdx.x, xcd = bid & 7, loc = bid >> 3;
  const int tm = 8 * (xcd >> 2) + (loc >> 2), tn = 4 * (xcd & 3) + (loc & 3);
  const unsigned lbase = (unsigned)(uintptr_t)smem;
  const rsrc_t rs = __builtin_amdgcn_make_buffer_rsrc(ws, 0, (int)WS_BYTES, 0x00020000);
  volatile __attribute__((address_space(3))) unsigned* words = LDS_PTR(volatile unsigned, smem + L_W);
  if (wid >= 4) {
    auto wo_rows = [&](int g0, int g1, int gs) {
      const int lane = fresh_lane();
      unsigned wov[4];
#pragma unroll
      for (int i = 0; i < 4; ++i) wov[i] = (unsigned)((8 * i + (lane >> 3)) * (D_ * 2) + ((lane & 7) ^ (4 * ((lane >> 4) & 1))) * 16 - i * 1024);
#pragma unroll 1
      for (int g = g0; g < g1 && !0; g += gs) glds_v4t<3>(reinterpret_cast<const char*>(wo) + (size_t)((64 * tn + 32 * g) & 1023) * (D_ * 2) + tn * 128, wov, lbase + g * 4096);
    };
    if (wid < 6) {
      const int lw = wid - 4, lane = fresh_lane();
      __builtin_amdgcn_s_setprio(3);
      const char* wb = reinterpret_cast<const char*>(wqkv + (size_t)(16 * lw) * (3 * D_) + tn * 64);
      unsigned wv[3];
#pragma unroll
      for (int i = 0; i < 3; ++i) {
        const int li = lane + 64 * i, k = li / 24, cp = li % 24, c = cp ^ (4 * ((k >> 1) & 1));
        wv[i] = (unsigned)(k * (3 * D_ * 2) + (c >> 3) * (D_ * 2) + (c & 7) * 16 - i * 1024);
      }
      auto stage = [&](int s, int slot) {
        const char* p = wb + (size_t)((s + 2 * tn) & 31) * (32 * 3 * D_ * 2);
        glds_v3(p, wv, lbase + slot * SLOTB + lw * 6144);
        glds_v3(p + 8 * (3 * D_ * 2), wv, lbase + slot * SLOTB + lw * 6144 + 3072);
      };
      const unsigned fra = lbase + L_FREE + (lane & 3) * 256, fla = lbase + L_FULL + lw * 256 + lane * 4;
      const rsrc_t rw = __builtin_amdgcn_make_buffer_rsrc(const_cast<char*>(wb), 0, 0 ? 0 : 6 * MBY - (16 * lw * 3 * D_ + tn * 64) * 2, 0x00020000);
#pragma unroll 1
      for (int L = 0; L < 2; ++L) {
        const int s0 = L ? NS : 0, lead = L ? 8 : NS - 1;
        int slot = 0;
#pragma unroll 1
        for (int s = s0; s < 32; ++s) {
          if (s >= NS) lds_wait_ge(fra, 32 * L + s - lead);
          stage(s, slot);
          if (s == 0) BARRIER();
          slot = (slot == NS - 1) ? 0 : slot + 1;
          if (s >= s0 + DD) asm volatile("s_waitcnt vmcnt(%0)\n\tds_write_b32 %1, %2" :: "n"(DD * 6), "v"(fla), "v"(32 * L + s - DD + 1) : "memory");
        }
#pragma unroll
        for (int d = DD - 1; d >= 0; --d) asm volatile("s_waitcnt vmcnt(%0)\n\tds_write_b32 %1, %2" :: "n"(d * 6), "v"(fla), "v"(32 * L + 32 - d) : "memory");
        BAR_LGKM();
        lds_wait_ge<8>(fra, 32 * L + 32);
        __builtin_amdgcn_s_sleep(16);
        wo_rows(16 + lw, 30, 2);
        u32x4 pre[NS * 6];
        if (L == 0) {
#pragma unroll
          for (int s = 0; s < NS; ++s) {
#pragma unroll
            for (int i = 0; i < 6; ++i)
              pre[s * 6 + i] = __builtin_amdgcn_raw_buffer_load_b128(rw, wv[i % 3], ((s + 2 * tn) & 31) * (32 * 3 * D_ * 2) + (i / 3) * (8 * 3 * D_ * 2) + (i % 3) * 1024, 0), PACE();
          }
        } else {
#pragma unroll
          for (int i = 0; i < NS * 6; ++i) asm volatile("" : "=v"(pre[i]));
        }
        BAR_ALL();
        wo_rows(30 + lw, 32, 2);
        BAR_ALL();
        if (L == 0) {
          char* d = smem + lw * 6144 + fresh_lane() * 16;
#pragma unroll
          for (int s = 0; s < NS; ++s) {
#pragma unroll
            for (int i = 0; i < 6; ++i) *reinterpret_cast<u32x4*>(d + s * SLOTB + i * 1024) = pre[s * 6 + i];
            lds_set(fla, 32 + s + 1);
          }
        }
      }
      return;
    }
    const int a = wid - 6;
    const bool ffn = loc < 28;
    const int fb = xcd * 28 + (ffn ? loc : 0);
    const int kr = loc, cg = xcd;
    const unsigned wa = lbase + L_W;
    BARRIER();
    s16x8 gf[16], uf[16];
#pragma unroll
    for (int i = 0; i < 16; ++i) asm volatile("" : "=v"(gf[i]), "=v"(uf[i]));
#pragma unroll 1
    for (int L = 0; L < 2; ++L) {
      const unsigned sx = OFF_SX + L * SX_LAYER;
      u32x4 wdf[16];
      if (L == 0) {
        LANE_VARS;
        const u32x4 sv = {SENT, SENT, SENT, SENT};
#pragma unroll
        for (int i = 0; i < 8; ++i) {
          const unsigned o = ((i * 256 + bid) * 128 + a * 64 + lane) * 16;
          stg(rs, o < SX_BYTES ? OFF_SX + o : OOB, sv);
        }
        if (a == 0) {
          const u32x4 w0 = *reinterpret_cast<const u32x4*>(w_r + lane * 8), w1 = *reinterpret_cast<const u32x4*>(w_r + 512 + lane * 8);
          float t = 0.f;
#pragma unroll
          for (int e = 0; e < 4; ++e) t = fadd(fadd(t, fadd(lo16(w0[e]), hi16(w0[e]))), fadd(lo16(w1[e]), hi16(w1[e])));
          words[0] = __float_as_uint(wave_sum(t));
          if ((lane >> 3) == (tn & 7)) *reinterpret_cast<u32x4*>(smem + L_WR + (lane & 7) * 16) = tn < 8 ? w0 : w1;
        }
        const u16* p = wqkv + (size_t)(64 * tm + 32 * a + hi) * (3 * D_) + (c24 >> 3) * D_ + tn * 64 + (c24 & 7) * 8;
        float cs[8] = {0.f, 0.f, 0.f, 0.f, 0.f, 0.f, 0.f, 0.f};
        u32x4 v[16];
#pragma unroll
        for (int i = 0; i < 16; ++i) v[i] = *reinterpret_cast<const u32x4*>(p + (size_t)i * (2 * 3 * D_));
        SBAR();
#pragma unroll
        for (int i = 0; i < 16; ++i)
#pragma unroll
          for (int e = 0; e < 4; ++e) { cs[2 * e] = fadd(cs[2 * e], lo16(v[i][e])); cs[2 * e + 1] = fadd(cs[2 * e + 1], hi16(v[i][e])); }
        u32x4 o;
#pragma unroll
        for (int e = 0; e < 4; ++e) { const float x = half_swap_sum(cs[e]), y = half_swap_sum(cs[4 + e]); o[e] = __float_as_uint(hi ? y : x); }
        stg(rs, r32 < 24 ? OFF_CS + (tn * 32 + tm * 2 + a) * 768 + r32 * 32 + hi * 16 : OOB, o);
      }
      if (L >= 0) {
        BAR_ALL();
        LANE_VARS;
        unsigned gv[4];
#pragma unroll
        for (int i = 0; i < 4; ++i) { gv[i] = (unsigned)((32 * i + (lane >> 1)) * (F_ * 2) + (lane & 1) * 16 - i * 1024); PIN(gv[i]); }
        const rsrc_t rd = __builtin_amdgcn_make_buffer_rsrc(const_cast<u16*>(wd) + (size_t)fb * 16 * D_ + 512 * a, 0, 0 ? 0 : 32768, 0x00020000);
        unsigned l8 = 8 * tblk * (D_ * 2) + (lane & 15) * 8 + hi * 4, gp = (16 * (lane >> 5) + 4 * tblk + tq) * 32 + tp * 8 + a * 32768;
        PIN(l8); PIN(gp);
        lds_wait_ge<8>(lbase + L_FREE + (lane & 3) * 256, 32 * L + 32);
        __builtin_amdgcn_s_sleep(16);
#pragma unroll 1
        for (int i = 0; i < (ffn && L == 0 && !0 ? 8 : 0); ++i) {
          glds_v4t<1>(reinterpret_cast<const char*>(((i & 1) ? wu : wg) + (size_t)(512 * a + 128 * (i >> 1)) * F_ + fb * 16), gv, lbase + a * 32768 + (i & 1) * 16384 + (i >> 1) * 4096);
        }
        unsigned raw[64];
#pragma unroll
        for (int i = 0; i < 64; ++i) {
          raw[i] = __builtin_amdgcn_raw_buffer_load_b32(rd, l8 + (i >> 3) * 128, (i & 7) * 2048, 0);
          if ((i & 3) == 3) asm volatile("s_waitcnt vmcnt(8)" ::: "memory");
        }
        DRAIN();
        if (L == 0) {
#pragma unroll
          for (int t = 0; t < 16; ++t) { gf[t] = tr2(smem + gp + t * 1024, 256); uf[t] = tr2(smem + gp + 16384 + t * 1024, 256); }
#pragma unroll
          for (int t = 0; t < 16; ++t) { PIN(gf[t]); PIN(uf[t]); }
        }
        asm volatile("s_waitcnt lgkmcnt(0)" ::: "memory");
        wo_rows(8 * a, 8 * a + 8, 1);
        STAMPV(7 + 16 * L);
        BAR_ALL();
#pragma unroll
        for (int i = 0; i < 64; ++i) PIN(raw[i]);
#pragma unroll
        for (int g = 0; g < 8; ++g) {
#pragma unroll
          for (int e = 0; e < 4; ++e) { wdf[2 * g][e] = PERM_LO(raw[8 * g + 2 * e], raw[8 * g + 2 * e + 1]); wdf[2 * g + 1][e] = PERM_HI(raw[8 * g + 2 * e], raw[8 * g + 2 * e + 1]); }
          PIN(wdf[2 * g]); PIN(wdf[2 * g + 1]);
        }
        if (L == 0) {
          LANE_VARS;
          f32x4 t[2];
#pragma unroll
          for (int c = 0; c < 2; ++c) {
            u32x4 v[8];
#pragma unroll
            for (int i = 0; i < 8; ++i) v[i] = ldg(rs, OFF_CS + (tn * 32 + 16 * c + 2 * i + hi) * 768 + a * 384 + c24 * 16);
            SBAR();
            t[c] = sum4<8>(v);
            SBAR();
          }
#pragma unroll
          for (int e = 0; e < 4; ++e) t[0][e] = half_swap_sum(fadd(t[0][e], t[1][e]));
          if (lane < 24) *reinterpret_cast<f32x4*>(smem + L_CS + a * 384 + lane * 16) = t[0];
        }
        BAR_LGKM();
      } else {
#pragma unroll
        for (int i = 0; i < 16; ++i) asm volatile("" : "=v"(wdf[i]));
      }
      const rsrc_t rs = __builtin_amdgcn_make_buffer_rsrc(ws, 0, L < 0 ? 0 : (int)WS_BYTES, 0x00020000);
      STAMPV(L < 0 ? 48 + 1 : 1 + 16 * L);
      LANE_VARS;
      unsigned cdst = lane < 8 ? sx + SX_ROW + tm * 2048 + tn * 128 + lane * 16 : sx + SX_CAND + (tn * 16 + tm) * 128;
      cdst = (a == 0 && lane < 9) ? cdst : OOB;
      PIN(cdst);
      lds_wait_ge(wa + 32 + 4 * tp, L + 1);
      unsigned key;
      {
        const unsigned k4 = *reinterpret_cast<const unsigned*>(smem + L_CAND + tp * 144 + 128);
        key = max(k4, dpp<0xB1>(k4)); key = max(key, dpp<0x4E>(key));
        const u32x4 c = *reinterpret_cast<const u32x4*>(smem + L_CAND + (((2047 - (int)(key & 2047u)) >> 5) & 3) * 144 + (lane < 8 ? lane : 8) * 16);
        const u32x4 o = {nz(c[0]), nz(c[1]), nz(c[2]), nz(c[3])};
        stg(rs, cdst, o);
        if (L < 0) key = 0u;
      }
      STAMPV(L < 0 ? 56 : 8 + 16 * L);
      __builtin_amdgcn_s_sleep(10);
      u32x4 v[2];
      poll_all<2>(v, [&](u32x4 (&t)[2]) {
        t[0] = ldg(rs, sx + SX_CAND + (tn * 16 + (lane & 15)) * 128);
        t[1] = ldg(rs, sx + SX_ROW + ((2047 - (int)(key & 2047u)) >> 7) * 2048 + a * 1024 + lane * 16);
        t[0][3] = key;
      }, [&](const u32x4 (&t)[2]) {
        const bool ok = mx4(0u, t[0]) != SENT;
        unsigned k = ok ? t[0][0] : 0u;
        k = max(k, dpp<0xB1>(k)); k = max(k, dpp<0x4E>(k)); k = max(k, dpp<0x141>(k)); k = max(k, dpp<0x140>(k));
        key = max(key, k);
        return ok & (mx4(0u, t[1]) != SENT) & (key == t[0][3]);
      });
      key = __builtin_amdgcn_readfirstlane(key);
      const int idx = 2047 - (int)(key & 2047u);
      const bool owner = (idx >> 7) == tm || L < 0;
      STAMPV(L < 0 ? 48 + 2 : 2 + 16 * L);
      {
        const float mb = __uint_as_float(__builtin_amdgcn_readlane(v[0][1], idx >> 7)), rd = __uint_as_float(__builtin_amdgcn_readlane(v[0][2], idx >> 7));
        u32x4 hb;
#pragma unroll
        for (int e = 0; e < 4; ++e) hb[e] = ln_pair_s(v[1][e], -mb, rd);
        *reinterpret_cast<u32x4*>(smem + L_HB + a * 1024 + lane * 16) = hb;
        *reinterpret_cast<u32x4*>(smem + L_XB + a * 1024 + lane * 16) = v[1];
        words[3] = (unsigned)idx;
        if (L == (1 ? 1 : 5) || (L == 0 && !owner)) lds_set(wa + 4 + 4 * a, L + 1);
      }
      if (ffn) {
        f32x4 ga = {0.f, 0.f, 0.f, 0.f}, ua = {0.f, 0.f, 0.f, 0.f};
        const char* hp = smem + L_HB + a * 1024 + (lane >> 4) * 16;
        s16x8 hf[16];
#pragma unroll
        for (int t = 0; t < 6; ++t) hf[t] = *reinterpret_cast<const s16x8*>(hp + t * 64);
        __builtin_amdgcn_s_setprio(3);
#pragma unroll
        for (int t = 0; t < 16; ++t) {
          SBAR();
          ga = MFMA16(gf[t], hf[t], ga); ua = MFMA16(uf[t], hf[t], ua);
          SBAR();
          if (t + 6 < 16) hf[t + 6] = *reinterpret_cast<const s16x8*>(hp + (t + 6) * 64);
        }
        __builtin_amdgcn_s_setprio(0);
        if ((lane & 15) == 0) {
          *reinterpret_cast<f32x4*>(smem + L_GU + a * 128 + lane) = ga;
          *reinterpret_cast<f32x4*>(smem + L_GU + a * 128 + 64 + lane) = ua;
        }
        lds_set(wa + 16 + 4 * a, L + 1);
        lds_wait_ge(wa + 16 + 4 * (a ^ 1), L + 1);
        const float* gu = reinterpret_cast<const float*>(smem + L_GU) + (lane & 15);
        const float gv = rbf(fadd(gu[0], gu[32])), uv = rbf(fadd(gu[16], gu[48]));
        const float av = fmul(rbf(fmul(gv, RCP(fadd(1.0f, __expf(-gv))))), uv);
        const unsigned ap = pack2(av, U2F(dpp<0xB1>(__float_as_uint(av))));
        u32x4 af;
#pragma unroll
        for (int e = 0; e < 4; ++e) {
          const unsigned t = tblk ? (unsigned)__builtin_amdgcn_readlane(ap, 8 + 2 * e) : (unsigned)__builtin_amdgcn_readlane(ap, 2 * e);
          af[e] = ((lane >> 1) & 1) == hi ? t : 0u;
        }
        f32x4 yd[16];
        const f32x4 zero = {0.f, 0.f, 0.f, 0.f};
        SBAR();
        __builtin_amdgcn_s_setprio(3);
#pragma unroll
        for (int t = 0; t < 16; ++t) yd[t] = MFMA16(__builtin_bit_cast(s16x8, af), __builtin_bit_cast(s16x8, wdf[t]), zero);
        SBAR();
        __builtin_amdgcn_s_setprio(0);
#pragma unroll
        for (int c = 0; c < 2; ++c) {
          u32x4 o;
#pragma unroll
          for (int e = 0; e < 4; ++e) {
            const float x = tblk ? yd[8 * c + 2 + (e & 1)][e & 2] : yd[8 * c + (e & 1)][e & 2], y = tblk ? yd[8 * c + 6 + (e & 1)][e & 2] : yd[8 * c + 4 + (e & 1)][e & 2];
            o[e] = nzf(hi ? y : x);
          }
          stg(rs, sx + SX_YP + fb * 4096 + a * 2048 + c * 1024 + lane * 16, o);
        }
      }
      STAMPV(L < 0 ? 48 + 3 : 3 + 16 * L);
      if (L == 1) lds_set(wa + 4 + 4 * a, 2);
      if (L == 1 && cg != (kr & 7)) break;
      u32x4 wpf[12];
      if (L <= 0) {
        const u16* p = wqkv + (size_t)(32 * kr + 16 * hi + 4 * tblk) * (3 * D_) + (2 * cg + a) * 64 + (lane & 15) * 4;
        u32x2 v[24];
#pragma unroll
        for (int i = 0; i < 24; ++i) v[i] = *reinterpret_cast<const u32x2*>(p + (size_t)(8 * ((i >> 2) & 1) + (i & 3)) * (3 * D_) + (i >> 3) * D_);
        SBAR();
#pragma unroll
        for (int t = 0; t < 12; ++t) {
#pragma unroll
          for (int e = 0; e < 4; ++e) {
            const unsigned x = v[8 * (t >> 2) + 2 * e][(t >> 1) & 1], y = v[8 * (t >> 2) + 2 * e + 1][(t >> 1) & 1];
            wpf[t][e] = (t & 1) ? PERM_HI(x, y) : PERM_LO(x, y);
          }
          PIN(wpf[t]);
        }
      } else {
#pragma unroll
        for (int i = 0; i < 12; ++i) asm volatile("" : "=v"(wpf[i]));
        __builtin_amdgcn_s_sleep(10);
      }
      const unsigned rk = key >> 16;
      float rsig = rbf(RCP(fadd(1.0f, __expf(-lo16((rk & 0x8000u) ? (rk & 0x7fffu) : (~rk & 0xffffu))))));
      PIN(rsig);
      u32x4 pv[14];
      poll_all<14, 4>(pv, [&](u32x4 (&t)[14]) {
#pragma unroll
        for (int i = 0; i < 14; ++i) t[i] = ldg(rs, sx + SX_YP + (112 * a + 8 * i + (lane >> 3)) * 4096 + kr * 128 + (lane & 7) * 16);
      }, [&](const u32x4 (&t)[14]) { return none_sent(t); });
      STAMPV(L < 0 ? 48 + 4 : 4 + 16 * L);
      {
        f32x4 t = sum4<14>(pv);
#pragma unroll
        for (int e = 0; e < 4; ++e) {
          t[e] = fadd(t[e], U2F(dpp<0x128>(__float_as_uint(t[e]))));
          t[e] = half_swap_sum(row_swap_sum(t[e]));
        }
        if (lane < 8) *reinterpret_cast<f32x4*>(smem + L_YS + a * 128 + lane * 16) = t;
      }
      lds_set(wa + 24 + 4 * a, L + 1);
      lds_wait_ge(wa + 24 + 4 * (a ^ 1), L + 1);
      const float ys = fadd(reinterpret_cast<const float*>(smem + L_YS)[r32], reinterpret_cast<const float*>(smem + L_YS)[32 + r32]);
      const int n = kr * 32 + r32;
      const int fo = ((n >> 4) * 2 + ((n >> 2) & 1)) * 8 + ((n >> 3) & 1) * 4 + (n & 3);
      const float xv = bf2f(reinterpret_cast<const u16*>(smem + L_XB)[fo]), hv = bf2f(reinterpret_cast<const u16*>(smem + L_HB)[fo]);
      const unsigned ob = pack2(fadd(xv, rbf(fadd(hv, rbf(fmul(rbf(ys), rsig))))), 0.f) & 0xffffu;
      if (L == 1) {
        if (a == 0 && lane < 32) out[(size_t)idx * D_ + n] = (u16)ob;
        STAMPV(L < 0 ? 48 + 5 : 5 + 16 * L);
        break;
      }
      reinterpret_cast<u16*>(smem + L_X2F + a * 64)[fo & 31] = (u16)ob;
      asm volatile("s_waitcnt lgkmcnt(0)" ::: "memory");
      {
        const s16x8 xa = *reinterpret_cast<const s16x8*>(smem + L_X2F + a * 64 + (lane >> 4) * 16);
        const u32x4 xf = *reinterpret_cast<const u32x4*>(smem + L_X2F + a * 64 + tp * 16);
        f32x4 z[12];
        const f32x4 zero = {0.f, 0.f, 0.f, 0.f};
        SBAR();
        __builtin_amdgcn_s_setprio(3);
#pragma unroll
        for (int t = 0; t < 12; ++t) z[t] = MFMA16(xa, __builtin_bit_cast(s16x8, wpf[t]), zero);
        SBAR();
        __builtin_amdgcn_s_setprio(0);
        const float x2 = lo16(ob), s1 = half_sum(x2), s2 = half_sum(fmul(x2, x2));
        u32x4 o;
#pragma unroll
        for (int e = 0; e < 4; ++e) {
          const unsigned sp = lane < 52 ? (e == 0 ? __float_as_uint(s1) : __float_as_uint(s2)) : xf[e];
          const unsigned x = __float_as_uint(tblk ? z[4 + e][0] : z[e][0]), y = tblk ? sp : __float_as_uint(z[8 + e][0]);
          o[e] = nz(hi ? y : x);
        }
        const unsigned pos = lane < 24 ? lane * 16 : lane < 48 ? lane * 16 + 128 : lane < 52 ? (lane & 1) * 512 + 384 : 400 + tp * 16;
        stg(rs, lane < 56 ? OFF_SX + SX_QP + ((2 * cg + a) * 32 + kr) * 1024 + pos : OOB, o);
      }
      STAMPV(L < 0 ? 48 + 5 : 5 + 16 * L);
      if (owner) {
        __builtin_amdgcn_s_sleep(10);
        const int c25 = r32 < 25 ? r32 : 24;
        u32x4 qv[17];
        poll_all<17, 4>(qv, [&](u32x4 (&t)[17]) {
#pragma unroll
          for (int i = 0; i < 16; ++i) t[i] = ldg(rs, OFF_SX + SX_QP + (tn * 32 + 2 * i + hi) * 1024 + a * 512 + c25 * 16);
          t[16] = ldg(rs, OFF_SX + SX_QP + (tn * 34 + hi) * 1024 + 400 + tp * 16);
        }, [&](const u32x4 (&t)[17]) { return none_sent(t); });
        *reinterpret_cast<u32x4*>(smem + L_X2P + hi * 64 + tp * 16) = qv[16];
        f32x4 t = sum4<16>(qv);
#pragma unroll
        for (int e = 0; e < 4; ++e) t[e] = half_swap_sum(t[e]);
        const float mean = fmul(U2F(__builtin_amdgcn_readlane(__float_as_uint(t[0]), 24)), 1.0f / 1024.0f);
        const float var = fmaxf(ffma(-mean, mean, fmul(U2F(__builtin_amdgcn_readlane(__float_as_uint(t[1]), 24)), 1.0f / 1024.0f)), 0.f);
        float mb, den;
        ln_scalars(mean, var, mb, den);
        const float rd = RCP(den);
        if (lane < 24) {
          const f32x4 cs = *reinterpret_cast<const f32x4*>(smem + L_CS + a * 384 + lane * 16);
#pragma unroll
          for (int e = 0; e < 4; ++e) t[e] = fmul(ffma(-mb, cs[e], t[e]), rd);
          *reinterpret_cast<f32x4*>(smem + L_PATCH + a * 384 + lane * 16) = t;
        }
        lds_set(wa + 4 + 4 * a, L + 1);
      }
      STAMPV(L < 0 ? 48 + 6 : 6 + 16 * L);
    }
    return;
  }
  const int tm_ = tm, tn_ = tn, wid_ = wid;
  *reinterpret_cast<u32x2*>(smem + L_FULL + threadIdx.x * 8) = u32x2{0u, 0u};
  if (threadIdx.x < 16) words[threadIdx.x] = 0u;
  const u32x4 srd = {(unsigned)(uintptr_t)ws, (unsigned)((uintptr_t)ws >> 32) & 0xffffu, WS_BYTES, 0x00020000u};
  u32x4 x1p[4];
  u32x4 pst;
  asm volatile("" : "=v"(x1p[0]), "=v"(x1p[1]), "=v"(x1p[2]), "=v"(x1p[3]), "=v"(pst));
#pragma unroll 1
  for (int L = 0; L < 2; ++L) {
    const unsigned sx = OFF_SX + L * SX_LAYER;
    u32x8 q[4];
    u32x16 kva, kvb;
    {
      f32x16 acc[6];
      u32x16 stA, stB, xr0, xr1, xr2, xr3;
      u32x4 stC;
      unsigned sdst;
      asm volatile("" : "={v[112:127]}"(stB), "={v[128:131]}"(stC));
      { PHASE_VARS;
      stB[8] = lane * 16 + (L ? OFF_X1 : OFF_HF) + tile * TS;
      stB[9] = lbase + wrow * 384 + (((4 * hh + wch) ^ wsw) << 4) + 8 * (tp & 1);
      stB[10] = lbase + wrow * 384 + (((4 * (hh ^ 1) + wch) ^ wsw) << 4) + 8 * (tp & 1);
      stB[13] = lbase + L_FULL + (lane & 1) * 256;
      stB[15] = lbase + L_FREE + wid * 256 + lane * 4;
      stC[0] = 32 * L; sdst = stats_dst(OFF_SX, tile, tn, lane); }
      unsigned soff, slot, need = 32 * L + 1;
      if (L == 0) {
        soff = tn * 4096;
        asm volatile(
          "buffer_load_dwordx4 v[132:135], v120, s[40:43], s44 offen offset:0 sc1\n\t"
          "buffer_load_dwordx4 v[136:139], v120, s[40:43], s44 offen offset:1024 sc1\n\t"
          "buffer_load_dwordx4 v[140:143], v120, s[40:43], s44 offen offset:2048 sc1\n\t"
          "buffer_load_dwordx4 v[144:147], v120, s[40:43], s44 offen offset:3072 sc1\n\t"
          "s_add_u32 s44, s44, 0x1000\n\t"
          "s_and_b32 s44, s44, 0xffff\n\t"
          "buffer_load_dwordx4 v[148:151], v120, s[40:43], s44 offen offset:0 sc1\n\t"
          "buffer_load_dwordx4 v[152:155], v120, s[40:43], s44 offen offset:1024 sc1\n\t"
          "buffer_load_dwordx4 v[156:159], v120, s[40:43], s44 offen offset:2048 sc1\n\t"
          "buffer_load_dwordx4 v[160:163], v120, s[40:43], s44 offen offset:3072 sc1\n\t"
          "s_add_u32 s44, s44, 0x1000\n\t"
          "s_and_b32 s44, s44, 0xffff\n\t"
          "buffer_load_dwordx4 v[164:167], v120, s[40:43], s44 offen offset:0 sc1\n\t"
          "buffer_load_dwordx4 v[168:171], v120, s[40:43], s44 offen offset:1024 sc1\n\t"
          "buffer_load_dwordx4 v[172:175], v120, s[40:43], s44 offen offset:2048 sc1\n\t"
          "buffer_load_dwordx4 v[176:179], v120, s[40:43], s44 offen offset:3072 sc1\n\t"
          "s_add_u32 s44, s44, 0x1000\n\t"
          "s_and_b32 s44, s44, 0xffff\n\t"
          "buffer_load_dwordx4 v[180:183], v120, s[40:43], s44 offen offset:0 sc1\n\t"
          "buffer_load_dwordx4 v[184:187], v120, s[40:43], s44 offen offset:1024 sc1\n\t"
          "buffer_load_dwordx4 v[188:191], v120, s[40:43], s44 offen offset:2048 sc1\n\t"
          "buffer_load_dwordx4 v[192:195], v120, s[40:43], s44 offen offset:3072 sc1\n\t"
          : "={v[132:147]}"(xr0), "={v[148:163]}"(xr1), "={v[164:179]}"(xr2), "={v[180:195]}"(xr3), "+{s44}"(soff) : "{v[112:127]}"(stB), "{s[40:43]}"(srd) : "scc", "memory");
        BAR_LGKM();
      } else {
        xr0 = cat4(x1p[0], x1p[1], x1p[2], x1p[3]);
        asm volatile("" : "={v[148:163]}"(xr1), "={v[164:179]}"(xr2), "={v[180:195]}"(xr3));
      }
      STAMPV(1 + 16 * L);
      asm volatile(
        "v_mov_b32 v123, v121\n\t"
        "v_mov_b32 v124, v122\n\t"
        "s_mov_b64 vcc, -1\n\t"
        "s_cbranch_vccnz 10f\n\t"
        "11:\n\t"
        "s_mov_b32 s46, 12288\n\t"
        "s_add_u32 s47, s47, 1\n\t"
        "ds_read_b64_tr_b16 v[96:97], v123 offset:0\n\t"
        "ds_read_b64_tr_b16 v[98:99], v123 offset:3072\n\t"
        "ds_read_b64_tr_b16 v[100:101], v124 offset:0\n\t"
        "ds_read_b64_tr_b16 v[102:103], v124 offset:3072\n\t"
        "ds_read_b64_tr_b16 v[104:105], v123 offset:128\n\t"
        "ds_read_b64_tr_b16 v[106:107], v123 offset:3200\n\t"
        "ds_read_b64_tr_b16 v[108:109], v124 offset:128\n\t"
        "ds_read_b64_tr_b16 v[110:111], v124 offset:3200\n\t"
        "ds_read_b64_tr_b16 v[112:113], v123 offset:256\n\t"
        "ds_read_b64_tr_b16 v[114:115], v123 offset:3328\n\t"
        "ds_write_b32 v127, v128\n\t"
        "ds_read_b64_tr_b16 v[116:117], v124 offset:256\n\t"
        "ds_read_b64_tr_b16 v[118:119], v124 offset:3328\n\t"
        "s_waitcnt vmcnt(15) lgkmcnt(11)\n\t"
        "v_mfma_f32_32x32x16_bf16 v[0:15], v[96:99], v[132:135], 0\n\t"
        "ds_read_b32 v126, v125\n\t"
        "ds_read_b64_tr_b16 v[96:97], v123 offset:6144\n\t"
        "ds_read_b64_tr_b16 v[98:99], v123 offset:9216\n\t"
        "s_waitcnt lgkmcnt(12)\n\t"
        "v_mfma_f32_32x32x16_bf16 v[16:31], v[100:103], v[132:135], 0\n\t"
        "v_add_u32 v129, s46, v121\n\t"
        "ds_read_b64_tr_b16 v[100:101], v124 offset:6144\n\t"
        "ds_read_b64_tr_b16 v[102:103], v124 offset:9216\n\t"
        "s_waitcnt lgkmcnt(12)\n\t"
        "v_mfma_f32_32x32x16_bf16 v[32:47], v[104:107], v[132:135], 0\n\t"
        "ds_read_b64_tr_b16 v[104:105], v123 offset:6272\n\t"
        "ds_read_b64_tr_b16 v[106:107], v123 offset:9344\n\t"
        "s_waitcnt lgkmcnt(12)\n\t"
        "v_mfma_f32_32x32x16_bf16 v[48:63], v[108:111], v[132:135], 0\n\t"
        "v_add_u32 v130, s46, v122\n\t"
        "ds_read_b64_tr_b16 v[108:109], v124 offset:6272\n\t"
        "ds_read_b64_tr_b16 v[110:111], v124 offset:9344\n\t"
        "s_waitcnt lgkmcnt(12)\n\t"
        "v_mfma_f32_32x32x16_bf16 v[64:79], v[132:135], v[112:115], 0\n\t"
        "ds_read_b64_tr_b16 v[112:113], v123 offset:6400\n\t"
        "ds_read_b64_tr_b16 v[114:115], v123 offset:9472\n\t"
        "s_waitcnt lgkmcnt(10)\n\t"
        "v_mfma_f32_32x32x16_bf16 v[80:95], v[132:135], v[116:119], 0\n\t"
        "v_cmp_gt_u32 vcc, s47, v126\n\t"
        "ds_read_b64_tr_b16 v[116:117], v124 offset:6400\n\t"
        "ds_read_b64_tr_b16 v[118:119], v124 offset:9472\n\t"
        "s_waitcnt vmcnt(14) lgkmcnt(10)\n\t"
        "v_mfma_f32_32x32x16_bf16 v[0:15], v[96:99], v[136:139], v[0:15]\n\t"
        "s_cbranch_vccnz 12f\n\t"
        "13:\n\t"
        "ds_read_b64_tr_b16 v[96:97], v129 offset:0\n\t"
        "ds_read_b64_tr_b16 v[98:99], v129 offset:3072\n\t"
        "s_waitcnt lgkmcnt(10)\n\t"
        "v_mfma_f32_32x32x16_bf16 v[16:31], v[100:103], v[136:139], v[16:31]\n\t"
        "v_add_u32 v128, 1, v128\n\t"
        "ds_read_b64_tr_b16 v[100:101], v130 offset:0\n\t"
        "ds_read_b64_tr_b16 v[102:103], v130 offset:3072\n\t"
        "s_waitcnt lgkmcnt(10)\n\t"
        "v_mfma_f32_32x32x16_bf16 v[32:47], v[104:107], v[136:139], v[32:47]\n\t"
        "ds_read_b64_tr_b16 v[104:105], v129 offset:128\n\t"
        "ds_read_b64_tr_b16 v[106:107], v129 offset:3200\n\t"
        "s_waitcnt lgkmcnt(10)\n\t"
        "v_mfma_f32_32x32x16_bf16 v[48:63], v[108:111], v[136:139], v[48:63]\n\t"
        "s_add_u32 s46, s46, 12288\n\t"
        "s_add_u32 s47, s47, 1\n\t"
        "ds_read_b64_tr_b16 v[108:109], v130 offset:128\n\t"
        "ds_read_b64_tr_b16 v[110:111], v130 offset:3200\n\t"
        "s_waitcnt lgkmcnt(10)\n\t"
        "v_mfma_f32_32x32x16_bf16 v[64:79], v[136:139], v[112:115], v[64:79]\n\t"
        "s_cmp_eq_u32 s46, 110592\n\t"
        "s_cselect_b32 s46, 0, s46\n\t"
        "ds_read_b64_tr_b16 v[112:113], v129 offset:256\n\t"
        "ds_read_b64_tr_b16 v[114:115], v129 offset:3328\n\t"
        "s_waitcnt lgkmcnt(10)\n\t"
        "v_mfma_f32_32x32x16_bf16 v[80:95], v[136:139], v[116:119], v[80:95]\n\t"
        "ds_write_b32 v127, v128\n\t"
        "ds_read_b64_tr_b16 v[116:117], v130 offset:256\n\t"
        "ds_read_b64_tr_b16 v[118:119], v130 offset:3328\n\t"
        "s_cmp_lt_u32 s47, 32\n\t"
        "s_cbranch_scc1 4f\n\t"
        "s_waitcnt vmcnt(0)\n\t"
        "buffer_store_dwordx4 v[196:199], v200, s[40:43], 0 offen sc1\n\t"
        "4:\n\t"
        "s_waitcnt vmcnt(13) lgkmcnt(11)\n\t"
        "v_mfma_f32_32x32x16_bf16 v[0:15], v[96:99], v[140:143], v[0:15]\n\t"
        "ds_read_b32 v126, v125\n\t"
        "ds_read_b64_tr_b16 v[96:97], v129 offset:6144\n\t"
        "ds_read_b64_tr_b16 v[98:99], v129 offset:9216\n\t"
        "s_waitcnt lgkmcnt(12)\n\t"
        "v_mfma_f32_32x32x16_bf16 v[16:31], v[100:103], v[140:143], v[16:31]\n\t"
        "v_add_u32 v123, s46, v121\n\t"
        "ds_read_b64_tr_b16 v[100:101], v130 offset:6144\n\t"
        "ds_read_b64_tr_b16 v[102:103], v130 offset:9216\n\t"
        "s_waitcnt lgkmcnt(12)\n\t"
        "v_mfma_f32_32x32x16_bf16 v[32:47], v[104:107], v[140:143], v[32:47]\n\t"
        "ds_read_b64_tr_b16 v[104:105], v129 offset:6272\n\t"
        "ds_read_b64_tr_b16 v[106:107], v129 offset:9344\n\t"
        "s_waitcnt lgkmcnt(12)\n\t"
        "v_mfma_f32_32x32x16_bf16 v[48:63], v[108:111], v[140:143], v[48:63]\n\t"
        "v_add_u32 v124, s46, v122\n\t"
        "ds_read_b64_tr_b16 v[108:109], v130 offset:6272\n\t"
        "ds_read_b64_tr_b16 v[110:111], v130 offset:9344\n\t"
        "s_waitcnt lgkmcnt(12)\n\t"
        "v_mfma_f32_32x32x16_bf16 v[64:79], v[140:143], v[112:115], v[64:79]\n\t"
        "ds_read_b64_tr_b16 v[112:113], v129 offset:6400\n\t"
        "ds_read_b64_tr_b16 v[114:115], v129 offset:9472\n\t"
        "s_waitcnt lgkmcnt(10)\n\t"
        "v_mfma_f32_32x32x16_bf16 v[80:95], v[140:143], v[116:119], v[80:95]\n\t"
        "v_cmp_gt_u32 vcc, s47, v126\n\t"
        "ds_read_b64_tr_b16 v[116:117], v130 offset:6400\n\t"
        "ds_read_b64_tr_b16 v[118:119], v130 offset:9472\n\t"
        "s_waitcnt vmcnt(12) lgkmcnt(10)\n\t"
        "v_mfma_f32_32x32x16_bf16 v[0:15], v[96:99], v[144:147], v[0:15]\n\t"
        "s_cbranch_vccnz 14f\n\t"
        "15:\n\t"
        "ds_read_b64_tr_b16 v[96:97], v123 offset:0\n\t"
        "ds_read_b64_tr_b16 v[98:99], v123 offset:3072\n\t"
        "s_waitcnt lgkmcnt(10)\n\t"
        "v_mfma_f32_32x32x16_bf16 v[16:31], v[100:103], v[144:147], v[16:31]\n\t"
        "v_add_u32 v128, 1, v128\n\t"
        "ds_read_b64_tr_b16 v[100:101], v124 offset:0\n\t"
        "ds_read_b64_tr_b16 v[102:103], v124 offset:3072\n\t"
        "s_waitcnt lgkmcnt(10)\n\t"
        "v_mfma_f32_32x32x16_bf16 v[32:47], v[104:107], v[144:147], v[32:47]\n\t"
        "ds_read_b64_tr_b16 v[104:105], v123 offset:128\n\t"
        "ds_read_b64_tr_b16 v[106:107], v123 offset:3200\n\t"
        "s_waitcnt lgkmcnt(10)\n\t"
        "v_mfma_f32_32x32x16_bf16 v[48:63], v[108:111], v[144:147], v[48:63]\n\t"
        "s_add_u32 s46, s46, 12288\n\t"
        "s_add_u32 s47, s47, 1\n\t"
        "ds_read_b64_tr_b16 v[108:109], v124 offset:128\n\t"
        "ds_read_b64_tr_b16 v[110:111], v124 offset:3200\n\t"
        "s_waitcnt lgkmcnt(10)\n\t"
        "v_mfma_f32_32x32x16_bf16 v[64:79], v[144:147], v[112:115], v[64:79]\n\t"
        "s_cmp_eq_u32 s46, 110592\n\t"
        "s_cselect_b32 s46, 0, s46\n\t"
        "ds_read_b64_tr_b16 v[112:113], v123 offset:256\n\t"
        "ds_read_b64_tr_b16 v[114:115], v123 offset:3328\n\t"
        "s_waitcnt lgkmcnt(10)\n\t"
        "v_mfma_f32_32x32x16_bf16 v[80:95], v[144:147], v[116:119], v[80:95]\n\t"
        "ds_write_b32 v127, v128\n\t"
        "ds_read_b64_tr_b16 v[116:117], v124 offset:256\n\t"
        "ds_read_b64_tr_b16 v[118:119], v124 offset:3328\n\t"
        "s_waitcnt lgkmcnt(0)\n\t"
        "s_nop 7\n\t"
        "s_branch 9f\n\t"
        "10:\n\t"
        "s_mov_b32 s48, 0x10000\n\t"
        "8:\n\t"
        "ds_read_b32 v126, v125\n\t"
        "s_sub_u32 s48, s48, 1\n\t"
        "s_waitcnt lgkmcnt(0)\n\t"
        "s_cbranch_scc1 11b\n\t"
        "v_cmp_gt_u32 vcc, s47, v126\n\t"
        "s_cbranch_vccnz 8b\n\t"
        "s_branch 11b\n\t"
        "12:\n\t"
        "s_mov_b32 s48, 0x10000\n\t"
        "8:\n\t"
        "ds_read_b32 v126, v125\n\t"
        "s_sub_u32 s48, s48, 1\n\t"
        "s_waitcnt lgkmcnt(0)\n\t"
        "s_cbranch_scc1 13b\n\t"
        "v_cmp_gt_u32 vcc, s47, v126\n\t"
        "s_cbranch_vccnz 8b\n\t"
        "s_branch 13b\n\t"
        "14:\n\t"
        "s_mov_b32 s48, 0x10000\n\t"
        "8:\n\t"
        "ds_read_b32 v126, v125\n\t"
        "s_sub_u32 s48, s48, 1\n\t"
        "s_waitcnt lgkmcnt(0)\n\t"
        "s_cbranch_scc1 15b\n\t"
        "v_cmp_gt_u32 vcc, s47, v126\n\t"
        "s_cbranch_vccnz 8b\n\t"
        "s_branch 15b\n\t"
        "9:\n\t"
        : "={v[0:15]}"(acc[0]), "={v[16:31]}"(acc[1]), "={v[32:47]}"(acc[2]), "={v[48:63]}"(acc[3]), "={v[64:79]}"(acc[4]), "={v[80:95]}"(acc[5]),
          "={v[96:111]}"(stA), "+{v[112:127]}"(stB), "+{v[128:131]}"(stC), "={s46}"(slot), "+{s47}"(need)
        : "{v[132:147]}"(xr0), "{v[196:199]}"(pst), "{v200}"(sdst), "{s[40:43]}"(srd) : "s48", "vcc", "scc", "memory");
      STAMPV(2 + 16 * L);
      float al = 1.f, be = 0.f;
      if (L == 1) {
        PHASE_VARS;
        float S, Q, Dd;
        row_stats_sum(rs, OFF_SX, tile, lane, S, Q, Dd);
        soff = ((tn + 1) & 15) * 4096;
        asm volatile(
          "buffer_load_dwordx4 v[148:151], v120, s[40:43], s44 offen offset:0 sc1\n\t"
          "buffer_load_dwordx4 v[152:155], v120, s[40:43], s44 offen offset:1024 sc1\n\t"
          "buffer_load_dwordx4 v[156:159], v120, s[40:43], s44 offen offset:2048 sc1\n\t"
          "buffer_load_dwordx4 v[160:163], v120, s[40:43], s44 offen offset:3072 sc1\n\t"
          "s_add_u32 s44, s44, 0x1000\n\t"
          "s_and_b32 s44, s44, 0xffff\n\t"
          "buffer_load_dwordx4 v[164:167], v120, s[40:43], s44 offen offset:0 sc1\n\t"
          "buffer_load_dwordx4 v[168:171], v120, s[40:43], s44 offen offset:1024 sc1\n\t"
          "buffer_load_dwordx4 v[172:175], v120, s[40:43], s44 offen offset:2048 sc1\n\t"
          "buffer_load_dwordx4 v[176:179], v120, s[40:43], s44 offen offset:3072 sc1\n\t"
          "s_add_u32 s44, s44, 0x1000\n\t"
          "s_and_b32 s44, s44, 0xffff\n\t"
          "buffer_load_dwordx4 v[180:183], v120, s[40:43], s44 offen offset:0 sc1\n\t"
          "buffer_load_dwordx4 v[184:187], v120, s[40:43], s44 offen offset:1024 sc1\n\t"
          "buffer_load_dwordx4 v[188:191], v120, s[40:43], s44 offen offset:2048 sc1\n\t"
          "buffer_load_dwordx4 v[192:195], v120, s[40:43], s44 offen offset:3072 sc1\n\t"
          : "={v[148:163]}"(xr1), "={v[164:179]}"(xr2), "={v[180:195]}"(xr3), "+{s44}"(soff), "+v"(S), "+v"(Q), "+v"(Dd) : "{v[112:127]}"(stB), "{s[40:43]}"(srd) : "scc", "memory");
        float mean, var, mb1, rd1, mb2, den2;
        row_stats_fin(smem, 1, tile, lane, S, Q, Dd, U2F(words[0]), x1p, mean, var, mb1, rd1);
        ln_scalars(mean + (mean - mb1) * rd1, var * (1.0f + rd1) * (1.0f + rd1), mb2, den2);
        const float rd2 = RCP(den2);
        al = (1.0f + rd1) * rd2; be = -(mb1 * rd1 + mb2) * rd2;
        reinterpret_cast<float*>(smem + L_AB)[wid * 64 + lane] = hi ? be : al;
        reinterpret_cast<float*>(smem + L_AB)[256 + wid * 64 + lane] = hi ? rd1 : mb1;
      }
      const u16* xsrc;
      { PHASE_VARS; xsrc = x0 + (size_t)(tile * 32 + r32) * D_ + tn * 64 + hi * 8; }
      STAMPV(3 + 16 * L);
      soff = ((tn + 1 + 2) & 15) * 4096;
      asm volatile(
        "s_nop 0\n\t"
        "s_add_u32 s44, s44, 0x1000\n\t"
        "s_and_b32 s44, s44, 0xffff\n\t"
        "buffer_load_dwordx4 v[132:135], v120, s[40:43], s44 offen offset:0 sc1\n\t"
        "buffer_load_dwordx4 v[136:139], v120, s[40:43], s44 offen offset:1024 sc1\n\t"
        "buffer_load_dwordx4 v[140:143], v120, s[40:43], s44 offen offset:2048 sc1\n\t"
        "s_cmp_gt_u32 s47, 32\n\t"
        "s_cbranch_scc1 4f\n\t"
        "global_load_dwordx4 %[r0], %[ra], off offset:0\n\t"
        "global_load_dwordx4 %[r1], %[ra], off offset:32\n\t"
        "global_load_dwordx4 %[r2], %[ra], off offset:64\n\t"
        "global_load_dwordx4 %[r3], %[ra], off offset:96\n\t"
        "4:\n\t"
        "s_mov_b32 s45, 3\n\t"
        "1:\n\t"
        "s_waitcnt vmcnt(14) lgkmcnt(11)\n\t"
        "v_mfma_f32_32x32x16_bf16 v[0:15], v[96:99], v[148:151], v[0:15]\n\t"
        "ds_read_b32 v126, v125\n\t"
        "ds_read_b64_tr_b16 v[96:97], v123 offset:6144\n\t"
        "ds_read_b64_tr_b16 v[98:99], v123 offset:9216\n\t"
        "s_waitcnt lgkmcnt(12)\n\t"
        "v_mfma_f32_32x32x16_bf16 v[16:31], v[100:103], v[148:151], v[16:31]\n\t"
        "v_add_u32 v129, s46, v121\n\t"
        "ds_read_b64_tr_b16 v[100:101], v124 offset:6144\n\t"
        "ds_read_b64_tr_b16 v[102:103], v124 offset:9216\n\t"
        "s_waitcnt lgkmcnt(12)\n\t"
        "v_mfma_f32_32x32x16_bf16 v[32:47], v[104:107], v[148:151], v[32:47]\n\t"
        "ds_read_b64_tr_b16 v[104:105], v123 offset:6272\n\t"
        "ds_read_b64_tr_b16 v[106:107], v123 offset:9344\n\t"
        "buffer_load_dwordx4 v[144:147], v120, s[40:43], s44 offen offset:3072 sc1\n\t"
        "s_waitcnt lgkmcnt(12)\n\t"
        "v_mfma_f32_32x32x16_bf16 v[48:63], v[108:111], v[148:151], v[48:63]\n\t"
        "v_add_u32 v130, s46, v122\n\t"
        "ds_read_b64_tr_b16 v[108:109], v124 offset:6272\n\t"
        "ds_read_b64_tr_b16 v[110:111], v124 offset:9344\n\t"
        "s_waitcnt lgkmcnt(12)\n\t"
        "v_mfma_f32_32x32x16_bf16 v[64:79], v[148:151], v[112:115], v[64:79]\n\t"
        "ds_read_b64_tr_b16 v[112:113], v123 offset:6400\n\t"
        "ds_read_b64_tr_b16 v[114:115], v123 offset:9472\n\t"
        "s_waitcnt lgkmcnt(10)\n\t"
        "v_mfma_f32_32x32x16_bf16 v[80:95], v[148:151], v[116:119], v[80:95]\n\t"
        "v_cmp_gt_u32 vcc, s47, v126\n\t"
        "ds_read_b64_tr_b16 v[116:117], v124 offset:6400\n\t"
        "ds_read_b64_tr_b16 v[118:119], v124 offset:9472\n\t"
        "s_waitcnt vmcnt(14) lgkmcnt(10)\n\t"
        "v_mfma_f32_32x32x16_bf16 v[0:15], v[96:99], v[152:155], v[0:15]\n\t"
        "s_cbranch_vccnz 16f\n\t"
        "17:\n\t"
        "ds_read_b64_tr_b16 v[96:97], v129 offset:0\n\t"
        "ds_read_b64_tr_b16 v[98:99], v129 offset:3072\n\t"
        "s_waitcnt lgkmcnt(10)\n\t"
        "v_mfma_f32_32x32x16_bf16 v[16:31], v[100:103], v[152:155], v[16:31]\n\t"
        "v_add_u32 v128, 1, v128\n\t"
        "ds_read_b64_tr_b16 v[100:101], v130 offset:0\n\t"
        "ds_read_b64_tr_b16 v[102:103], v130 offset:3072\n\t"
        "s_waitcnt lgkmcnt(10)\n\t"
        "v_mfma_f32_32x32x16_bf16 v[32:47], v[104:107], v[152:155], v[32:47]\n\t"
        "ds_read_b64_tr_b16 v[104:105], v129 offset:128\n\t"
        "ds_read_b64_tr_b16 v[106:107], v129 offset:3200\n\t"
        "s_add_u32 s44, s44, 0x1000\n\t"
        "s_and_b32 s44, s44, 0xffff\n\t"
        "buffer_load_dwordx4 v[148:151], v120, s[40:43], s44 offen offset:0 sc1\n\t"
        "s_waitcnt lgkmcnt(10)\n\t"
        "v_mfma_f32_32x32x16_bf16 v[48:63], v[108:111], v[152:155], v[48:63]\n\t"
        "s_add_u32 s46, s46, 12288\n\t"
        "s_add_u32 s47, s47, 1\n\t"
        "ds_read_b64_tr_b16 v[108:109], v130 offset:128\n\t"
        "ds_read_b64_tr_b16 v[110:111], v130 offset:3200\n\t"
        "s_waitcnt lgkmcnt(10)\n\t"
        "v_mfma_f32_32x32x16_bf16 v[64:79], v[152:155], v[112:115], v[64:79]\n\t"
        "s_cmp_eq_u32 s46, 110592\n\t"
        "s_cselect_b32 s46, 0, s46\n\t"
        "ds_read_b64_tr_b16 v[112:113], v129 offset:256\n\t"
        "ds_read_b64_tr_b16 v[114:115], v129 offset:3328\n\t"
        "s_waitcnt lgkmcnt(10)\n\t"
        "v_mfma_f32_32x32x16_bf16 v[80:95], v[152:155], v[116:119], v[80:95]\n\t"
        "ds_write_b32 v127, v128\n\t"
        "ds_read_b64_tr_b16 v[116:117], v130 offset:256\n\t"
        "ds_read_b64_tr_b16 v[118:119], v130 offset:3328\n\t"
        "s_waitcnt vmcnt(14) lgkmcnt(11)\n\t"
        "v_mfma_f32_32x32x16_bf16 v[0:15], v[96:99], v[156:159], v[0:15]\n\t"
        "ds_read_b32 v126, v125\n\t"
        "ds_read_b64_tr_b16 v[96:97], v129 offset:6144\n\t"
        "ds_read_b64_tr_b16 v[98:99], v129 offset:9216\n\t"
        "s_waitcnt lgkmcnt(12)\n\t"
        "v_mfma_f32_32x32x16_bf16 v[16:31], v[100:103], v[156:159], v[16:31]\n\t"
        "v_add_u32 v123, s46, v121\n\t"
        "ds_read_b64_tr_b16 v[100:101], v130 offset:6144\n\t"
        "ds_read_b64_tr_b16 v[102:103], v130 offset:9216\n\t"
        "s_waitcnt lgkmcnt(12)\n\t"
        "v_mfma_f32_32x32x16_bf16 v[32:47], v[104:107], v[156:159], v[32:47]\n\t"
        "ds_read_b64_tr_b16 v[104:105], v129 offset:6272\n\t"
        "ds_read_b64_tr_b16 v[106:107], v129 offset:9344\n\t"
        "buffer_load_dwordx4 v[152:155], v120, s[40:43], s44 offen offset:1024 sc1\n\t"
        "s_waitcnt lgkmcnt(12)\n\t"
        "v_mfma_f32_32x32x16_bf16 v[48:63], v[108:111], v[156:159], v[48:63]\n\t"
        "v_add_u32 v124, s46, v122\n\t"
        "ds_read_b64_tr_b16 v[108:109], v130 offset:6272\n\t"
        "ds_read_b64_tr_b16 v[110:111], v130 offset:9344\n\t"
        "s_waitcnt lgkmcnt(12)\n\t"
        "v_mfma_f32_32x32x16_bf16 v[64:79], v[156:159], v[112:115], v[64:79]\n\t"
        "ds_read_b64_tr_b16 v[112:113], v129 offset:6400\n\t"
        "ds_read_b64_tr_b16 v[114:115], v129 offset:9472\n\t"
        "s_waitcnt lgkmcnt(10)\n\t"
        "v_mfma_f32_32x32x16_bf16 v[80:95], v[156:159], v[116:119], v[80:95]\n\t"
        "v_cmp_gt_u32 vcc, s47, v126\n\t"
        "ds_read_b64_tr_b16 v[116:117], v130 offset:6400\n\t"
        "ds_read_b64_tr_b16 v[118:119], v130 offset:9472\n\t"
        "s_waitcnt vmcnt(14) lgkmcnt(10)\n\t"
        "v_mfma_f32_32x32x16_bf16 v[0:15], v[96:99], v[160:163], v[0:15]\n\t"
        "s_cbranch_vccnz 18f\n\t"
        "19:\n\t"
        "ds_read_b64_tr_b16 v[96:97], v123 offset:0\n\t"
        "ds_read_b64_tr_b16 v[98:99], v123 offset:3072\n\t"
        "s_waitcnt lgkmcnt(10)\n\t"
        "v_mfma_f32_32x32x16_bf16 v[16:31], v[100:103], v[160:163], v[16:31]\n\t"
        "v_add_u32 v128, 1, v128\n\t"
        "ds_read_b64_tr_b16 v[100:101], v124 offset:0\n\t"
        "ds_read_b64_tr_b16 v[102:103], v124 offset:3072\n\t"
        "s_waitcnt lgkmcnt(10)\n\t"
        "v_mfma_f32_32x32x16_bf16 v[32:47], v[104:107], v[160:163], v[32:47]\n\t"
        "ds_read_b64_tr_b16 v[104:105], v123 offset:128\n\t"
        "ds_read_b64_tr_b16 v[106:107], v123 offset:3200\n\t"
        "buffer_load_dwordx4 v[156:159], v120, s[40:43], s44 offen offset:2048 sc1\n\t"
        "s_waitcnt lgkmcnt(10)\n\t"
        "v_mfma_f32_32x32x16_bf16 v[48:63], v[108:111], v[160:163], v[48:63]\n\t"
        "s_add_u32 s46, s46, 12288\n\t"
        "s_add_u32 s47, s47, 1\n\t"
        "ds_read_b64_tr_b16 v[108:109], v124 offset:128\n\t"
        "ds_read_b64_tr_b16 v[110:111], v124 offset:3200\n\t"
        "s_waitcnt lgkmcnt(10)\n\t"
        "v_mfma_f32_32x32x16_bf16 v[64:79], v[160:163], v[112:115], v[64:79]\n\t"
        "s_cmp_eq_u32 s46, 110592\n\t"
        "s_cselect_b32 s46, 0, s46\n\t"
        "ds_read_b64_tr_b16 v[112:113], v123 offset:256\n\t"
        "ds_read_b64_tr_b16 v[114:115], v123 offset:3328\n\t"
        "s_waitcnt lgkmcnt(10)\n\t"
        "v_mfma_f32_32x32x16_bf16 v[80:95], v[160:163], v[116:119], v[80:95]\n\t"
        "ds_write_b32 v127, v128\n\t"
        "ds_read_b64_tr_b16 v[116:117], v124 offset:256\n\t"
        "ds_read_b64_tr_b16 v[118:119], v124 offset:3328\n\t"
        "s_waitcnt vmcnt(14) lgkmcnt(11)\n\t"
        "v_mfma_f32_32x32x16_bf16 v[0:15], v[96:99], v[164:167], v[0:15]\n\t"
        "ds_read_b32 v126, v125\n\t"
        "ds_read_b64_tr_b16 v[96:97], v123 offset:6144\n\t"
        "ds_read_b64_tr_b16 v[98:99], v123 offset:9216\n\t"
        "s_waitcnt lgkmcnt(12)\n\t"
        "v_mfma_f32_32x32x16_bf16 v[16:31], v[100:103], v[164:167], v[16:31]\n\t"
        "v_add_u32 v129, s46, v121\n\t"
        "ds_read_b64_tr_b16 v[100:101], v124 offset:6144\n\t"
        "ds_read_b64_tr_b16 v[102:103], v124 offset:9216\n\t"
        "s_waitcnt lgkmcnt(12)\n\t"
        "v_mfma_f32_32x32x16_bf16 v[32:47], v[104:107], v[164:167], v[32:47]\n\t"
        "ds_read_b64_tr_b16 v[104:105], v123 offset:6272\n\t"
        "ds_read_b64_tr_b16 v[106:107], v123 offset:9344\n\t"
        "buffer_load_dwordx4 v[160:163], v120, s[40:43], s44 offen offset:3072 sc1\n\t"
        "s_waitcnt lgkmcnt(12)\n\t"
        "v_mfma_f32_32x32x16_bf16 v[48:63], v[108:111], v[164:167], v[48:63]\n\t"
        "v_add_u32 v130, s46, v122\n\t"
        "ds_read_b64_tr_b16 v[108:109], v124 offset:6272\n\t"
        "ds_read_b64_tr_b16 v[110:111], v124 offset:9344\n\t"
        "s_waitcnt lgkmcnt(12)\n\t"
        "v_mfma_f32_32x32x16_bf16 v[64:79], v[164:167], v[112:115], v[64:79]\n\t"
        "ds_read_b64_tr_b16 v[112:113], v123 offset:6400\n\t"
        "ds_read_b64_tr_b16 v[114:115], v123 offset:9472\n\t"
        "s_waitcnt lgkmcnt(10)\n\t"
        "v_mfma_f32_32x32x16_bf16 v[80:95], v[164:167], v[116:119], v[80:95]\n\t"
        "v_cmp_gt_u32 vcc, s47, v126\n\t"
        "ds_read_b64_tr_b16 v[116:117], v124 offset:6400\n\t"
        "ds_read_b64_tr_b16 v[118:119], v124 offset:9472\n\t"
        "s_waitcnt vmcnt(14) lgkmcnt(10)\n\t"
        "v_mfma_f32_32x32x16_bf16 v[0:15], v[96:99], v[168:171], v[0:15]\n\t"
        "s_cbranch_vccnz 20f\n\t"
        "21:\n\t"
        "ds_read_b64_tr_b16 v[96:97], v129 offset:0\n\t"
        "ds_read_b64_tr_b16 v[98:99], v129 offset:3072\n\t"
        "s_waitcnt lgkmcnt(10)\n\t"
        "v_mfma_f32_32x32x16_bf16 v[16:31], v[100:103], v[168:171], v[16:31]\n\t"
        "v_add_u32 v128, 1, v128\n\t"
        "ds_read_b64_tr_b16 v[100:101], v130 offset:0\n\t"
        "ds_read_b64_tr_b16 v[102:103], v130 offset:3072\n\t"
        "s_waitcnt lgkmcnt(10)\n\t"
        "v_mfma_f32_32x32x16_bf16 v[32:47], v[104:107], v[168:171], v[32:47]\n\t"
        "ds_read_b64_tr_b16 v[104:105], v129 offset:128\n\t"
        "ds_read_b64_tr_b16 v[106:107], v129 offset:3200\n\t"
        "s_add_u32 s44, s44, 0x1000\n\t"
        "s_and_b32 s44, s44, 0xffff\n\t"
        "buffer_load_dwordx4 v[164:167], v120, s[40:43], s44 offen offset:0 sc1\n\t"
        "s_waitcnt lgkmcnt(10)\n\t"
        "v_mfma_f32_32x32x16_bf16 v[48:63], v[108:111], v[168:171], v[48:63]\n\t"
        "s_add_u32 s46, s46, 12288\n\t"
        "s_add_u32 s47, s47, 1\n\t"
        "ds_read_b64_tr_b16 v[108:109], v130 offset:128\n\t"
        "ds_read_b64_tr_b16 v[110:111], v130 offset:3200\n\t"
        "s_waitcnt lgkmcnt(10)\n\t"
        "v_mfma_f32_32x32x16_bf16 v[64:79], v[168:171], v[112:115], v[64:79]\n\t"
        "s_cmp_eq_u32 s46, 110592\n\t"
        "s_cselect_b32 s46, 0, s46\n\t"
        "ds_read_b64_tr_b16 v[112:113], v129 offset:256\n\t"
        "ds_read_b64_tr_b16 v[114:115], v129 offset:3328\n\t"
        "s_waitcnt lgkmcnt(10)\n\t"
        "v_mfma_f32_32x32x16_bf16 v[80:95], v[168:171], v[116:119], v[80:95]\n\t"
        "ds_write_b32 v127, v128\n\t"
        "ds_read_b64_tr_b16 v[116:117], v130 offset:256\n\t"
        "ds_read_b64_tr_b16 v[118:119], v130 offset:3328\n\t"
        "s_waitcnt vmcnt(14) lgkmcnt(11)\n\t"
        "v_mfma_f32_32x32x16_bf16 v[0:15], v[96:99], v[172:175], v[0:15]\n\t"
        "ds_read_b32 v126, v125\n\t"
        "ds_read_b64_tr_b16 v[96:97], v129 offset:6144\n\t"
        "ds_read_b64_tr_b16 v[98:99], v129 offset:9216\n\t"
        "s_waitcnt lgkmcnt(12)\n\t"
        "v_mfma_f32_32x32x16_bf16 v[16:31], v[100:103], v[172:175], v[16:31]\n\t"
        "v_add_u32 v123, s46, v121\n\t"
        "ds_read_b64_tr_b16 v[100:101], v130 offset:6144\n\t"
        "ds_read_b64_tr_b16 v[102:103], v130 offset:9216\n\t"
        "s_waitcnt lgkmcnt(12)\n\t"
        "v_mfma_f32_32x32x16_bf16 v[32:47], v[104:107], v[172:175], v[32:47]\n\t"
        "ds_read_b64_tr_b16 v[104:105], v129 offset:6272\n\t"
        "ds_read_b64_tr_b16 v[106:107], v129 offset:9344\n\t"
        "buffer_load_dwordx4 v[168:171], v120, s[40:43], s44 offen offset:1024 sc1\n\t"
        "s_waitcnt lgkmcnt(12)\n\t"
        "v_mfma_f32_32x32x16_bf16 v[48:63], v[108:111], v[172:175], v[48:63]\n\t"
        "v_add_u32 v124, s46, v122\n\t"
        "ds_read_b64_tr_b16 v[108:109], v130 offset:6272\n\t"
        "ds_read_b64_tr_b16 v[110:111], v130 offset:9344\n\t"
        "s_waitcnt lgkmcnt(12)\n\t"
        "v_mfma_f32_32x32x16_bf16 v[64:79], v[172:175], v[112:115], v[64:79]\n\t"
        "ds_read_b64_tr_b16 v[112:113], v129 offset:6400\n\t"
        "ds_read_b64_tr_b16 v[114:115], v129 offset:9472\n\t"
        "s_waitcnt lgkmcnt(10)\n\t"
        "v_mfma_f32_32x32x16_bf16 v[80:95], v[172:175], v[116:119], v[80:95]\n\t"
        "v_cmp_gt_u32 vcc, s47, v126\n\t"
        "ds_read_b64_tr_b16 v[116:117], v130 offset:6400\n\t"
        "ds_read_b64_tr_b16 v[118:119], v130 offset:9472\n\t"
        "s_waitcnt vmcnt(14) lgkmcnt(10)\n\t"
        "v_mfma_f32_32x32x16_bf16 v[0:15], v[96:99], v[176:179], v[0:15]\n\t"
        "s_cbranch_vccnz 22f\n\t"
        "23:\n\t"
        "ds_read_b64_tr_b16 v[96:97], v123 offset:0\n\t"
        "ds_read_b64_tr_b16 v[98:99], v123 offset:3072\n\t"
        "s_waitcnt lgkmcnt(10)\n\t"
        "v_mfma_f32_32x32x16_bf16 v[16:31], v[100:103], v[176:179], v[16:31]\n\t"
        "v_add_u32 v128, 1, v128\n\t"
        "ds_read_b64_tr_b16 v[100:101], v124 offset:0\n\t"
        "ds_read_b64_tr_b16 v[102:103], v124 offset:3072\n\t"
        "s_waitcnt lgkmcnt(10)\n\t"
        "v_mfma_f32_32x32x16_bf16 v[32:47], v[104:107], v[176:179], v[32:47]\n\t"
        "ds_read_b64_tr_b16 v[104:105], v123 offset:128\n\t"
        "ds_read_b64_tr_b16 v[106:107], v123 offset:3200\n\t"
        "buffer_load_dwordx4 v[172:175], v120, s[40:43], s44 offen offset:2048 sc1\n\t"
        "s_waitcnt lgkmcnt(10)\n\t"
        "v_mfma_f32_32x32x16_bf16 v[48:63], v[108:111], v[176:179], v[48:63]\n\t"
        "s_add_u32 s46, s46, 12288\n\t"
        "s_add_u32 s47, s47, 1\n\t"
        "ds_read_b64_tr_b16 v[108:109], v124 offset:128\n\t"
        "ds_read_b64_tr_b16 v[110:111], v124 offset:3200\n\t"
        "s_waitcnt lgkmcnt(10)\n\t"
        "v_mfma_f32_32x32x16_bf16 v[64:79], v[176:179], v[112:115], v[64:79]\n\t"
        "s_cmp_eq_u32 s46, 110592\n\t"
        "s_cselect_b32 s46, 0, s46\n\t"
        "ds_read_b64_tr_b16 v[112:113], v123 offset:256\n\t"
        "ds_read_b64_tr_b16 v[114:115], v123 offset:3328\n\t"
        "s_waitcnt lgkmcnt(10)\n\t"
        "v_mfma_f32_32x32x16_bf16 v[80:95], v[176:179], v[116:119], v[80:95]\n\t"
        "ds_write_b32 v127, v128\n\t"
        "ds_read_b64_tr_b16 v[116:117], v124 offset:256\n\t"
        "ds_read_b64_tr_b16 v[118:119], v124 offset:3328\n\t"
        "s_waitcnt vmcnt(14) lgkmcnt(11)\n\t"
        "v_mfma_f32_32x32x16_bf16 v[0:15], v[96:99], v[180:183], v[0:15]\n\t"
        "ds_read_b32 v126, v125\n\t"
        "ds_read_b64_tr_b16 v[96:97], v123 offset:6144\n\t"
        "ds_read_b64_tr_b16 v[98:99], v123 offset:9216\n\t"
        "s_waitcnt lgkmcnt(12)\n\t"
        "v_mfma_f32_32x32x16_bf16 v[16:31], v[100:103], v[180:183], v[16:31]\n\t"
        "v_add_u32 v129, s46, v121\n\t"
        "ds_read_b64_tr_b16 v[100:101], v124 offset:6144\n\t"
        "ds_read_b64_tr_b16 v[102:103], v124 offset:9216\n\t"
        "s_waitcnt lgkmcnt(12)\n\t"
        "v_mfma_f32_32x32x16_bf16 v[32:47], v[104:107], v[180:183], v[32:47]\n\t"
        "ds_read_b64_tr_b16 v[104:105], v123 offset:6272\n\t"
        "ds_read_b64_tr_b16 v[106:107], v123 offset:9344\n\t"
        "buffer_load_dwordx4 v[176:179], v120, s[40:43], s44 offen offset:3072 sc1\n\t"
        "s_waitcnt lgkmcnt(12)\n\t"
        "v_mfma_f32_32x32x16_bf16 v[48:63], v[108:111], v[180:183], v[48:63]\n\t"
        "v_add_u32 v130, s46, v122\n\t"
        "ds_read_b64_tr_b16 v[108:109], v124 offset:6272\n\t"
        "ds_read_b64_tr_b16 v[110:111], v124 offset:9344\n\t"
        "s_waitcnt lgkmcnt(12)\n\t"
        "v_mfma_f32_32x32x16_bf16 v[64:79], v[180:183], v[112:115], v[64:79]\n\t"
        "ds_read_b64_tr_b16 v[112:113], v123 offset:6400\n\t"
        "ds_read_b64_tr_b16 v[114:115], v123 offset:9472\n\t"
        "s_waitcnt lgkmcnt(10)\n\t"
        "v_mfma_f32_32x32x16_bf16 v[80:95], v[180:183], v[116:119], v[80:95]\n\t"
        "v_cmp_gt_u32 vcc, s47, v126\n\t"
        "ds_read_b64_tr_b16 v[116:117], v124 offset:6400\n\t"
        "ds_read_b64_tr_b16 v[118:119], v124 offset:9472\n\t"
        "s_waitcnt vmcnt(14) lgkmcnt(10)\n\t"
        "v_mfma_f32_32x32x16_bf16 v[0:15], v[96:99], v[184:187], v[0:15]\n\t"
        "s_cbranch_vccnz 24f\n\t"
        "25:\n\t"
        "ds_read_b64_tr_b16 v[96:97], v129 offset:0\n\t"
        "ds_read_b64_tr_b16 v[98:99], v129 offset:3072\n\t"
        "s_waitcnt lgkmcnt(10)\n\t"
        "v_mfma_f32_32x32x16_bf16 v[16:31], v[100:103], v[184:187], v[16:31]\n\t"
        "v_add_u32 v128, 1, v128\n\t"
        "ds_read_b64_tr_b16 v[100:101], v130 offset:0\n\t"
        "ds_read_b64_tr_b16 v[102:103], v130 offset:3072\n\t"
        "s_waitcnt lgkmcnt(10)\n\t"
        "v_mfma_f32_32x32x16_bf16 v[32:47], v[104:107], v[184:187], v[32:47]\n\t"
        "ds_read_b64_tr_b16 v[104:105], v129 offset:128\n\t"
        "ds_read_b64_tr_b16 v[106:107], v129 offset:3200\n\t"
        "s_add_u32 s44, s44, 0x1000\n\t"
        "s_and_b32 s44, s44, 0xffff\n\t"
        "buffer_load_dwordx4 v[180:183], v120, s[40:43], s44 offen offset:0 sc1\n\t"
        "s_waitcnt lgkmcnt(10)\n\t"
        "v_mfma_f32_32x32x16_bf16 v[48:63], v[108:111], v[184:187], v[48:63]\n\t"
        "s_add_u32 s46, s46, 12288\n\t"
        "s_add_u32 s47, s47, 1\n\t"
        "ds_read_b64_tr_b16 v[108:109], v130 offset:128\n\t"
        "ds_read_b64_tr_b16 v[110:111], v130 offset:3200\n\t"
        "s_waitcnt lgkmcnt(10)\n\t"
        "v_mfma_f32_32x32x16_bf16 v[64:79], v[184:187], v[112:115], v[64:79]\n\t"
        "s_cmp_eq_u32 s46, 110592\n\t"
        "s_cselect_b32 s46, 0, s46\n\t"
        "ds_read_b64_tr_b16 v[112:113], v129 offset:256\n\t"
        "ds_read_b64_tr_b16 v[114:115], v129 offset:3328\n\t"
        "s_waitcnt lgkmcnt(10)\n\t"
        "v_mfma_f32_32x32x16_bf16 v[80:95], v[184:187], v[116:119], v[80:95]\n\t"
        "ds_write_b32 v127, v128\n\t"
        "ds_read_b64_tr_b16 v[116:117], v130 offset:256\n\t"
        "ds_read_b64_tr_b16 v[118:119], v130 offset:3328\n\t"
        "s_waitcnt vmcnt(14) lgkmcnt(11)\n\t"
        "v_mfma_f32_32x32x16_bf16 v[0:15], v[96:99], v[188:191], v[0:15]\n\t"
        "ds_read_b32 v126, v125\n\t"
        "ds_read_b64_tr_b16 v[96:97], v129 offset:6144\n\t"
        "ds_read_b64_tr_b16 v[98:99], v129 offset:9216\n\t"
        "s_waitcnt lgkmcnt(12)\n\t"
        "v_mfma_f32_32x32x16_bf16 v[16:31], v[100:103], v[188:191], v[16:31]\n\t"
        "v_add_u32 v123, s46, v121\n\t"
        "ds_read_b64_tr_b16 v[100:101], v130 offset:6144\n\t"
        "ds_read_b64_tr_b16 v[102:103], v130 offset:9216\n\t"
        "s_waitcnt lgkmcnt(12)\n\t"
        "v_mfma_f32_32x32x16_bf16 v[32:47], v[104:107], v[188:191], v[32:47]\n\t"
        "ds_read_b64_tr_b16 v[104:105], v129 offset:6272\n\t"
        "ds_read_b64_tr_b16 v[106:107], v129 offset:9344\n\t"
        "buffer_load_dwordx4 v[184:187], v120, s[40:43], s44 offen offset:1024 sc1\n\t"
        "s_waitcnt lgkmcnt(12)\n\t"
        "v_mfma_f32_32x32x16_bf16 v[48:63], v[108:111], v[188:191], v[48:63]\n\t"
        "v_add_u32 v124, s46, v122\n\t"
        "ds_read_b64_tr_b16 v[108:109], v130 offset:6272\n\t"
        "ds_read_b64_tr_b16 v[110:111], v130 offset:9344\n\t"
        "s_waitcnt lgkmcnt(12)\n\t"
        "v_mfma_f32_32x32x16_bf16 v[64:79], v[188:191], v[112:115], v[64:79]\n\t"
        "ds_read_b64_tr_b16 v[112:113], v129 offset:6400\n\t"
        "ds_read_b64_tr_b16 v[114:115], v129 offset:9472\n\t"
        "s_waitcnt lgkmcnt(10)\n\t"
        "v_mfma_f32_32x32x16_bf16 v[80:95], v[188:191], v[116:119], v[80:95]\n\t"
        "v_cmp_gt_u32 vcc, s47, v126\n\t"
        "ds_read_b64_tr_b16 v[116:117], v130 offset:6400\n\t"
        "ds_read_b64_tr_b16 v[118:119], v130 offset:9472\n\t"
        "s_waitcnt vmcnt(14) lgkmcnt(10)\n\t"
        "v_mfma_f32_32x32x16_bf16 v[0:15], v[96:99], v[192:195], v[0:15]\n\t"
        "s_cbranch_vccnz 26f\n\t"
        "27:\n\t"
        "ds_read_b64_tr_b16 v[96:97], v123 offset:0\n\t"
        "ds_read_b64_tr_b16 v[98:99], v123 offset:3072\n\t"
        "s_waitcnt lgkmcnt(10)\n\t"
        "v_mfma_f32_32x32x16_bf16 v[16:31], v[100:103], v[192:195], v[16:31]\n\t"
        "v_add_u32 v128, 1, v128\n\t"
        "ds_read_b64_tr_b16 v[100:101], v124 offset:0\n\t"
        "ds_read_b64_tr_b16 v[102:103], v124 offset:3072\n\t"
        "s_waitcnt lgkmcnt(10)\n\t"
        "v_mfma_f32_32x32x16_bf16 v[32:47], v[104:107], v[192:195], v[32:47]\n\t"
        "ds_read_b64_tr_b16 v[104:105], v123 offset:128\n\t"
        "ds_read_b64_tr_b16 v[106:107], v123 offset:3200\n\t"
        "buffer_load_dwordx4 v[188:191], v120, s[40:43], s44 offen offset:2048 sc1\n\t"
        "s_waitcnt lgkmcnt(10)\n\t"
        "v_mfma_f32_32x32x16_bf16 v[48:63], v[108:111], v[192:195], v[48:63]\n\t"
        "s_add_u32 s46, s46, 12288\n\t"
        "s_add_u32 s47, s47, 1\n\t"
        "ds_read_b64_tr_b16 v[108:109], v124 offset:128\n\t"
        "ds_read_b64_tr_b16 v[110:111], v124 offset:3200\n\t"
        "s_waitcnt lgkmcnt(10)\n\t"
        "v_mfma_f32_32x32x16_bf16 v[64:79], v[192:195], v[112:115], v[64:79]\n\t"
        "s_cmp_eq_u32 s46, 110592\n\t"
        "s_cselect_b32 s46, 0, s46\n\t"
        "ds_read_b64_tr_b16 v[112:113], v123 offset:256\n\t"
        "ds_read_b64_tr_b16 v[114:115], v123 offset:3328\n\t"
        "s_waitcnt lgkmcnt(10)\n\t"
        "v_mfma_f32_32x32x16_bf16 v[80:95], v[192:195], v[116:119], v[80:95]\n\t"
        "ds_write_b32 v127, v128\n\t"
        "ds_read_b64_tr_b16 v[116:117], v124 offset:256\n\t"
        "ds_read_b64_tr_b16 v[118:119], v124 offset:3328\n\t"
        "s_waitcnt vmcnt(14) lgkmcnt(11)\n\t"
        "v_mfma_f32_32x32x16_bf16 v[0:15], v[96:99], v[132:135], v[0:15]\n\t"
        "ds_read_b32 v126, v125\n\t"
        "ds_read_b64_tr_b16 v[96:97], v123 offset:6144\n\t"
        "ds_read_b64_tr_b16 v[98:99], v123 offset:9216\n\t"
        "s_waitcnt lgkmcnt(12)\n\t"
        "v_mfma_f32_32x32x16_bf16 v[16:31], v[100:103], v[132:135], v[16:31]\n\t"
        "v_add_u32 v129, s46, v121\n\t"
        "ds_read_b64_tr_b16 v[100:101], v124 offset:6144\n\t"
        "ds_read_b64_tr_b16 v[102:103], v124 offset:9216\n\t"
        "s_waitcnt lgkmcnt(12)\n\t"
        "v_mfma_f32_32x32x16_bf16 v[32:47], v[104:107], v[132:135], v[32:47]\n\t"
        "ds_read_b64_tr_b16 v[104:105], v123 offset:6272\n\t"
        "ds_read_b64_tr_b16 v[106:107], v123 offset:9344\n\t"
        "buffer_load_dwordx4 v[192:195], v120, s[40:43], s44 offen offset:3072 sc1\n\t"
        "s_waitcnt lgkmcnt(12)\n\t"
        "v_mfma_f32_32x32x16_bf16 v[48:63], v[108:111], v[132:135], v[48:63]\n\t"
        "v_add_u32 v130, s46, v122\n\t"
        "ds_read_b64_tr_b16 v[108:109], v124 offset:6272\n\t"
        "ds_read_b64_tr_b16 v[110:111], v124 offset:9344\n\t"
        "s_waitcnt lgkmcnt(12)\n\t"
        "v_mfma_f32_32x32x16_bf16 v[64:79], v[132:135], v[112:115], v[64:79]\n\t"
        "ds_read_b64_tr_b16 v[112:113], v123 offset:6400\n\t"
        "ds_read_b64_tr_b16 v[114:115], v123 offset:9472\n\t"
        "s_waitcnt lgkmcnt(10)\n\t"
        "v_mfma_f32_32x32x16_bf16 v[80:95], v[132:135], v[116:119], v[80:95]\n\t"
        "v_cmp_gt_u32 vcc, s47, v126\n\t"
        "ds_read_b64_tr_b16 v[116:117], v124 offset:6400\n\t"
        "ds_read_b64_tr_b16 v[118:119], v124 offset:9472\n\t"
        "s_waitcnt vmcnt(14) lgkmcnt(10)\n\t"
        "v_mfma_f32_32x32x16_bf16 v[0:15], v[96:99], v[136:139], v[0:15]\n\t"
        "s_cbranch_vccnz 28f\n\t"
        "29:\n\t"
        "ds_read_b64_tr_b16 v[96:97], v129 offset:0\n\t"
        "ds_read_b64_tr_b16 v[98:99], v129 offset:3072\n\t"
        "s_waitcnt lgkmcnt(10)\n\t"
        "v_mfma_f32_32x32x16_bf16 v[16:31], v[100:103], v[136:139], v[16:31]\n\t"
        "v_add_u32 v128, 1, v128\n\t"
        "ds_read_b64_tr_b16 v[100:101], v130 offset:0\n\t"
        "ds_read_b64_tr_b16 v[102:103], v130 offset:3072\n\t"
        "s_waitcnt lgkmcnt(10)\n\t"
        "v_mfma_f32_32x32x16_bf16 v[32:47], v[104:107], v[136:139], v[32:47]\n\t"
        "ds_read_b64_tr_b16 v[104:105], v129 offset:128\n\t"
        "ds_read_b64_tr_b16 v[106:107], v129 offset:3200\n\t"
        "s_add_u32 s44, s44, 0x1000\n\t"
        "s_and_b32 s44, s44, 0xffff\n\t"
        "buffer_load_dwordx4 v[132:135], v120, s[40:43], s44 offen offset:0 sc1\n\t"
        "s_waitcnt lgkmcnt(10)\n\t"
        "v_mfma_f32_32x32x16_bf16 v[48:63], v[108:111], v[136:139], v[48:63]\n\t"
        "s_add_u32 s46, s46, 12288\n\t"
        "s_add_u32 s47, s47, 1\n\t"
        "ds_read_b64_tr_b16 v[108:109], v130 offset:128\n\t"
        "ds_read_b64_tr_b16 v[110:111], v130 offset:3200\n\t"
        "s_waitcnt lgkmcnt(10)\n\t"
        "v_mfma_f32_32x32x16_bf16 v[64:79], v[136:139], v[112:115], v[64:79]\n\t"
        "s_cmp_eq_u32 s46, 110592\n\t"
        "s_cselect_b32 s46, 0, s46\n\t"
        "ds_read_b64_tr_b16 v[112:113], v129 offset:256\n\t"
        "ds_read_b64_tr_b16 v[114:115], v129 offset:3328\n\t"
        "s_waitcnt lgkmcnt(10)\n\t"
        "v_mfma_f32_32x32x16_bf16 v[80:95], v[136:139], v[116:119], v[80:95]\n\t"
        "ds_write_b32 v127, v128\n\t"
        "ds_read_b64_tr_b16 v[116:117], v130 offset:256\n\t"
        "ds_read_b64_tr_b16 v[118:119], v130 offset:3328\n\t"
        "s_waitcnt vmcnt(14) lgkmcnt(11)\n\t"
        "v_mfma_f32_32x32x16_bf16 v[0:15], v[96:99], v[140:143], v[0:15]\n\t"
        "ds_read_b32 v126, v125\n\t"
        "ds_read_b64_tr_b16 v[96:97], v129 offset:6144\n\t"
        "ds_read_b64_tr_b16 v[98:99], v129 offset:9216\n\t"
        "s_waitcnt lgkmcnt(12)\n\t"
        "v_mfma_f32_32x32x16_bf16 v[16:31], v[100:103], v[140:143], v[16:31]\n\t"
        "v_add_u32 v123, s46, v121\n\t"
        "ds_read_b64_tr_b16 v[100:101], v130 offset:6144\n\t"
        "ds_read_b64_tr_b16 v[102:103], v130 offset:9216\n\t"
        "s_waitcnt lgkmcnt(12)\n\t"
        "v_mfma_f32_32x32x16_bf16 v[32:47], v[104:107], v[140:143], v[32:47]\n\t"
        "ds_read_b64_tr_b16 v[104:105], v129 offset:6272\n\t"
        "ds_read_b64_tr_b16 v[106:107], v129 offset:9344\n\t"
        "buffer_load_dwordx4 v[136:139], v120, s[40:43], s44 offen offset:1024 sc1\n\t"
        "s_waitcnt lgkmcnt(12)\n\t"
        "v_mfma_f32_32x32x16_bf16 v[48:63], v[108:111], v[140:143], v[48:63]\n\t"
        "v_add_u32 v124, s46, v122\n\t"
        "ds_read_b64_tr_b16 v[108:109], v130 offset:6272\n\t"
        "ds_read_b64_tr_b16 v[110:111], v130 offset:9344\n\t"
        "s_waitcnt lgkmcnt(12)\n\t"
        "v_mfma_f32_32x32x16_bf16 v[64:79], v[140:143], v[112:115], v[64:79]\n\t"
        "ds_read_b64_tr_b16 v[112:113], v129 offset:6400\n\t"
        "ds_read_b64_tr_b16 v[114:115], v129 offset:9472\n\t"
        "s_waitcnt lgkmcnt(10)\n\t"
        "v_mfma_f32_32x32x16_bf16 v[80:95], v[140:143], v[116:119], v[80:95]\n\t"
        "v_cmp_gt_u32 vcc, s47, v126\n\t"
        "ds_read_b64_tr_b16 v[116:117], v130 offset:6400\n\t"
        "ds_read_b64_tr_b16 v[118:119], v130 offset:9472\n\t"
        "s_waitcnt vmcnt(14) lgkmcnt(10)\n\t"
        "v_mfma_f32_32x32x16_bf16 v[0:15], v[96:99], v[144:147], v[0:15]\n\t"
        "s_cbranch_vccnz 30f\n\t"
        "31:\n\t"
        "ds_read_b64_tr_b16 v[96:97], v123 offset:0\n\t"
        "ds_read_b64_tr_b16 v[98:99], v123 offset:3072\n\t"
        "s_waitcnt lgkmcnt(10)\n\t"
        "v_mfma_f32_32x32x16_bf16 v[16:31], v[100:103], v[144:147], v[16:31]\n\t"
        "v_add_u32 v128, 1, v128\n\t"
        "ds_read_b64_tr_b16 v[100:101], v124 offset:0\n\t"
        "ds_read_b64_tr_b16 v[102:103], v124 offset:3072\n\t"
        "s_waitcnt lgkmcnt(10)\n\t"
        "v_mfma_f32_32x32x16_bf16 v[32:47], v[104:107], v[144:147], v[32:47]\n\t"
        "ds_read_b64_tr_b16 v[104:105], v123 offset:128\n\t"
        "ds_read_b64_tr_b16 v[106:107], v123 offset:3200\n\t"
        "buffer_load_dwordx4 v[140:143], v120, s[40:43], s44 offen offset:2048 sc1\n\t"
        "s_waitcnt lgkmcnt(10)\n\t"
        "v_mfma_f32_32x32x16_bf16 v[48:63], v[108:111], v[144:147], v[48:63]\n\t"
        "s_add_u32 s46, s46, 12288\n\t"
        "s_add_u32 s47, s47, 1\n\t"
        "ds_read_b64_tr_b16 v[108:109], v124 offset:128\n\t"
        "ds_read_b64_tr_b16 v[110:111], v124 offset:3200\n\t"
        "s_waitcnt lgkmcnt(10)\n\t"
        "v_mfma_f32_32x32x16_bf16 v[64:79], v[144:147], v[112:115], v[64:79]\n\t"
        "s_cmp_eq_u32 s46, 110592\n\t"
        "s_cselect_b32 s46, 0, s46\n\t"
        "ds_read_b64_tr_b16 v[112:113], v123 offset:256\n\t"
        "ds_read_b64_tr_b16 v[114:115], v123 offset:3328\n\t"
        "s_waitcnt lgkmcnt(10)\n\t"
        "v_mfma_f32_32x32x16_bf16 v[80:95], v[144:147], v[116:119], v[80:95]\n\t"
        "ds_write_b32 v127, v128\n\t"
        "ds_read_b64_tr_b16 v[116:117], v124 offset:256\n\t"
        "ds_read_b64_tr_b16 v[118:119], v124 offset:3328\n\t"
        "s_sub_u32 s45, s45, 1\n\t"
        "s_cmp_lg_u32 s45, 0\n\t"
        "s_cbranch_scc1 1b\n\t"
        "s_waitcnt vmcnt(14) lgkmcnt(11)\n\t"
        "v_mfma_f32_32x32x16_bf16 v[0:15], v[96:99], v[148:151], v[0:15]\n\t"
        "ds_read_b32 v126, v125\n\t"
        "ds_read_b64_tr_b16 v[96:97], v123 offset:6144\n\t"
        "ds_read_b64_tr_b16 v[98:99], v123 offset:9216\n\t"
        "s_waitcnt lgkmcnt(12)\n\t"
        "v_mfma_f32_32x32x16_bf16 v[16:31], v[100:103], v[148:151], v[16:31]\n\t"
        "v_add_u32 v129, s46, v121\n\t"
        "ds_read_b64_tr_b16 v[100:101], v124 offset:6144\n\t"
        "ds_read_b64_tr_b16 v[102:103], v124 offset:9216\n\t"
        "s_waitcnt lgkmcnt(12)\n\t"
        "v_mfma_f32_32x32x16_bf16 v[32:47], v[104:107], v[148:151], v[32:47]\n\t"
        "ds_read_b64_tr_b16 v[104:105], v123 offset:6272\n\t"
        "ds_read_b64_tr_b16 v[106:107], v123 offset:9344\n\t"
        "s_waitcnt lgkmcnt(12)\n\t"
        "v_mfma_f32_32x32x16_bf16 v[48:63], v[108:111], v[148:151], v[48:63]\n\t"
        "v_add_u32 v130, s46, v122\n\t"
        "ds_read_b64_tr_b16 v[108:109], v124 offset:6272\n\t"
        "ds_read_b64_tr_b16 v[110:111], v124 offset:9344\n\t"
        "s_waitcnt lgkmcnt(12)\n\t"
        "v_mfma_f32_32x32x16_bf16 v[64:79], v[148:151], v[112:115], v[64:79]\n\t"
        "ds_read_b64_tr_b16 v[112:113], v123 offset:6400\n\t"
        "ds_read_b64_tr_b16 v[114:115], v123 offset:9472\n\t"
        "s_waitcnt lgkmcnt(10)\n\t"
        "v_mfma_f32_32x32x16_bf16 v[80:95], v[148:151], v[116:119], v[80:95]\n\t"
        "v_cmp_gt_u32 vcc, s47, v126\n\t"
        "ds_read_b64_tr_b16 v[116:117], v124 offset:6400\n\t"
        "ds_read_b64_tr_b16 v[118:119], v124 offset:9472\n\t"
        "s_waitcnt vmcnt(13) lgkmcnt(10)\n\t"
        "v_mfma_f32_32x32x16_bf16 v[0:15], v[96:99], v[152:155], v[0:15]\n\t"
        "s_cbranch_vccnz 64f\n\t"
        "65:\n\t"
        "ds_read_b64_tr_b16 v[96:97], v129 offset:0\n\t"
        "ds_read_b64_tr_b16 v[98:99], v129 offset:3072\n\t"
        "s_waitcnt lgkmcnt(10)\n\t"
        "v_mfma_f32_32x32x16_bf16 v[16:31], v[100:103], v[152:155], v[16:31]\n\t"
        "v_add_u32 v128, 1, v128\n\t"
        "ds_read_b64_tr_b16 v[100:101], v130 offset:0\n\t"
        "ds_read_b64_tr_b16 v[102:103], v130 offset:3072\n\t"
        "s_waitcnt lgkmcnt(10)\n\t"
        "v_mfma_f32_32x32x16_bf16 v[32:47], v[104:107], v[152:155], v[32:47]\n\t"
        "ds_read_b64_tr_b16 v[104:105], v129 offset:128\n\t"
        "ds_read_b64_tr_b16 v[106:107], v129 offset:3200\n\t"
        "s_waitcnt lgkmcnt(10)\n\t"
        "v_mfma_f32_32x32x16_bf16 v[48:63], v[108:111], v[152:155], v[48:63]\n\t"
        "s_add_u32 s46, s46, 12288\n\t"
        "s_add_u32 s47, s47, 1\n\t"
        "ds_read_b64_tr_b16 v[108:109], v130 offset:128\n\t"
        "ds_read_b64_tr_b16 v[110:111], v130 offset:3200\n\t"
        "s_waitcnt lgkmcnt(10)\n\t"
        "v_mfma_f32_32x32x16_bf16 v[64:79], v[152:155], v[112:115], v[64:79]\n\t"
        "s_cmp_eq_u32 s46, 110592\n\t"
        "s_cselect_b32 s46, 0, s46\n\t"
        "ds_read_b64_tr_b16 v[112:113], v129 offset:256\n\t"
        "ds_read_b64_tr_b16 v[114:115], v129 offset:3328\n\t"
        "s_waitcnt lgkmcnt(10)\n\t"
        "v_mfma_f32_32x32x16_bf16 v[80:95], v[152:155], v[116:119], v[80:95]\n\t"
        "ds_write_b32 v127, v128\n\t"
        "ds_read_b64_tr_b16 v[116:117], v130 offset:256\n\t"
        "ds_read_b64_tr_b16 v[118:119], v130 offset:3328\n\t"
        "s_waitcnt vmcnt(12) lgkmcnt(11)\n\t"
        "v_mfma_f32_32x32x16_bf16 v[0:15], v[96:99], v[156:159], v[0:15]\n\t"
        "ds_read_b32 v126, v125\n\t"
        "ds_read_b64_tr_b16 v[96:97], v129 offset:6144\n\t"
        "ds_read_b64_tr_b16 v[98:99], v129 offset:9216\n\t"
        "s_waitcnt lgkmcnt(12)\n\t"
        "v_mfma_f32_32x32x16_bf16 v[16:31], v[100:103], v[156:159], v[16:31]\n\t"
        "v_add_u32 v123, s46, v121\n\t"
        "ds_read_b64_tr_b16 v[100:101], v130 offset:6144\n\t"
        "ds_read_b64_tr_b16 v[102:103], v130 offset:9216\n\t"
        "s_waitcnt lgkmcnt(12)\n\t"
        "v_mfma_f32_32x32x16_bf16 v[32:47], v[104:107], v[156:159], v[32:47]\n\t"
        "ds_read_b64_tr_b16 v[104:105], v129 offset:6272\n\t"
        "ds_read_b64_tr_b16 v[106:107], v129 offset:9344\n\t"
        "s_waitcnt lgkmcnt(12)\n\t"
        "v_mfma_f32_32x32x16_bf16 v[48:63], v[108:111], v[156:159], v[48:63]\n\t"
        "v_add_u32 v124, s46, v122\n\t"
        "ds_read_b64_tr_b16 v[108:109], v130 offset:6272\n\t"
        "ds_read_b64_tr_b16 v[110:111], v130 offset:9344\n\t"
        "s_waitcnt lgkmcnt(12)\n\t"
        "v_mfma_f32_32x32x16_bf16 v[64:79], v[156:159], v[112:115], v[64:79]\n\t"
        "ds_read_b64_tr_b16 v[112:113], v129 offset:6400\n\t"
        "ds_read_b64_tr_b16 v[114:115], v129 offset:9472\n\t"
        "s_waitcnt lgkmcnt(10)\n\t"
        "v_mfma_f32_32x32x16_bf16 v[80:95], v[156:159], v[116:119], v[80:95]\n\t"
        "v_cmp_gt_u32 vcc, s47, v126\n\t"
        "ds_read_b64_tr_b16 v[116:117], v130 offset:6400\n\t"
        "ds_read_b64_tr_b16 v[118:119], v130 offset:9472\n\t"
        "s_waitcnt vmcnt(11) lgkmcnt(10)\n\t"
        "v_mfma_f32_32x32x16_bf16 v[0:15], v[96:99], v[160:163], v[0:15]\n\t"
        "s_cbranch_vccnz 66f\n\t"
        "67:\n\t"
        "ds_read_b64_tr_b16 v[96:97], v123 offset:0\n\t"
        "ds_read_b64_tr_b16 v[98:99], v123 offset:3072\n\t"
        "s_waitcnt lgkmcnt(10)\n\t"
        "v_mfma_f32_32x32x16_bf16 v[16:31], v[100:103], v[160:163], v[16:31]\n\t"
        "v_add_u32 v128, 1, v128\n\t"
        "ds_read_b64_tr_b16 v[100:101], v124 offset:0\n\t"
        "ds_read_b64_tr_b16 v[102:103], v124 offset:3072\n\t"
        "s_waitcnt lgkmcnt(10)\n\t"
        "v_mfma_f32_32x32x16_bf16 v[32:47], v[104:107], v[160:163], v[32:47]\n\t"
        "ds_read_b64_tr_b16 v[104:105], v123 offset:128\n\t"
        "ds_read_b64_tr_b16 v[106:107], v123 offset:3200\n\t"
        "s_waitcnt lgkmcnt(10)\n\t"
        "v_mfma_f32_32x32x16_bf16 v[48:63], v[108:111], v[160:163], v[48:63]\n\t"
        "s_add_u32 s46, s46, 12288\n\t"
        "s_add_u32 s47, s47, 1\n\t"
        "ds_read_b64_tr_b16 v[108:109], v124 offset:128\n\t"
        "ds_read_b64_tr_b16 v[110:111], v124 offset:3200\n\t"
        "s_waitcnt lgkmcnt(10)\n\t"
        "v_mfma_f32_32x32x16_bf16 v[64:79], v[160:163], v[112:115], v[64:79]\n\t"
        "s_cmp_eq_u32 s46, 110592\n\t"
        "s_cselect_b32 s46, 0, s46\n\t"
        "ds_read_b64_tr_b16 v[112:113], v123 offset:256\n\t"
        "ds_read_b64_tr_b16 v[114:115], v123 offset:3328\n\t"
        "s_waitcnt lgkmcnt(10)\n\t"
        "v_mfma_f32_32x32x16_bf16 v[80:95], v[160:163], v[116:119], v[80:95]\n\t"
        "ds_write_b32 v127, v128\n\t"
        "ds_read_b64_tr_b16 v[116:117], v124 offset:256\n\t"
        "ds_read_b64_tr_b16 v[118:119], v124 offset:3328\n\t"
        "s_waitcnt vmcnt(10) lgkmcnt(11)\n\t"
        "v_mfma_f32_32x32x16_bf16 v[0:15], v[96:99], v[164:167], v[0:15]\n\t"
        "ds_read_b32 v126, v125\n\t"
        "ds_read_b64_tr_b16 v[96:97], v123 offset:6144\n\t"
        "ds_read_b64_tr_b16 v[98:99], v123 offset:9216\n\t"
        "s_waitcnt lgkmcnt(12)\n\t"
        "v_mfma_f32_32x32x16_bf16 v[16:31], v[100:103], v[164:167], v[16:31]\n\t"
        "v_add_u32 v129, s46, v121\n\t"
        "ds_read_b64_tr_b16 v[100:101], v124 offset:6144\n\t"
        "ds_read_b64_tr_b16 v[102:103], v124 offset:9216\n\t"
        "s_waitcnt lgkmcnt(12)\n\t"
        "v_mfma_f32_32x32x16_bf16 v[32:47], v[104:107], v[164:167], v[32:47]\n\t"
        "ds_read_b64_tr_b16 v[104:105], v123 offset:6272\n\t"
        "ds_read_b64_tr_b16 v[106:107], v123 offset:9344\n\t"
        "s_waitcnt lgkmcnt(12)\n\t"
        "v_mfma_f32_32x32x16_bf16 v[48:63], v[108:111], v[164:167], v[48:63]\n\t"
        "v_add_u32 v130, s46, v122\n\t"
        "ds_read_b64_tr_b16 v[108:109], v124 offset:6272\n\t"
        "ds_read_b64_tr_b16 v[110:111], v124 offset:9344\n\t"
        "s_waitcnt lgkmcnt(12)\n\t"
        "v_mfma_f32_32x32x16_bf16 v[64:79], v[164:167], v[112:115], v[64:79]\n\t"
        "ds_read_b64_tr_b16 v[112:113], v123 offset:6400\n\t"
        "ds_read_b64_tr_b16 v[114:115], v123 offset:9472\n\t"
        "s_waitcnt lgkmcnt(10)\n\t"
        "v_mfma_f32_32x32x16_bf16 v[80:95], v[164:167], v[116:119], v[80:95]\n\t"
        "v_cmp_gt_u32 vcc, s47, v126\n\t"
        "ds_read_b64_tr_b16 v[116:117], v124 offset:6400\n\t"
        "ds_read_b64_tr_b16 v[118:119], v124 offset:9472\n\t"
        "s_waitcnt vmcnt(9) lgkmcnt(10)\n\t"
        "v_mfma_f32_32x32x16_bf16 v[0:15], v[96:99], v[168:171], v[0:15]\n\t"
        "s_cbranch_vccnz 68f\n\t"
        "69:\n\t"
        "ds_read_b64_tr_b16 v[96:97], v129 offset:0\n\t"
        "ds_read_b64_tr_b16 v[98:99], v129 offset:3072\n\t"
        "s_waitcnt lgkmcnt(10)\n\t"
        "v_mfma_f32_32x32x16_bf16 v[16:31], v[100:103], v[168:171], v[16:31]\n\t"
        "v_add_u32 v128, 1, v128\n\t"
        "ds_read_b64_tr_b16 v[100:101], v130 offset:0\n\t"
        "ds_read_b64_tr_b16 v[102:103], v130 offset:3072\n\t"
        "s_waitcnt lgkmcnt(10)\n\t"
        "v_mfma_f32_32x32x16_bf16 v[32:47], v[104:107], v[168:171], v[32:47]\n\t"
        "ds_read_b64_tr_b16 v[104:105], v129 offset:128\n\t"
        "ds_read_b64_tr_b16 v[106:107], v129 offset:3200\n\t"
        "s_waitcnt lgkmcnt(10)\n\t"
        "v_mfma_f32_32x32x16_bf16 v[48:63], v[108:111], v[168:171], v[48:63]\n\t"
        "s_add_u32 s46, s46, 12288\n\t"
        "s_add_u32 s47, s47, 1\n\t"
        "ds_read_b64_tr_b16 v[108:109], v130 offset:128\n\t"
        "ds_read_b64_tr_b16 v[110:111], v130 offset:3200\n\t"
        "s_waitcnt lgkmcnt(10)\n\t"
        "v_mfma_f32_32x32x16_bf16 v[64:79], v[168:171], v[112:115], v[64:79]\n\t"
        "s_cmp_eq_u32 s46, 110592\n\t"
        "s_cselect_b32 s46, 0, s46\n\t"
        "ds_read_b64_tr_b16 v[112:113], v129 offset:256\n\t"
        "ds_read_b64_tr_b16 v[114:115], v129 offset:3328\n\t"
        "s_waitcnt lgkmcnt(10)\n\t"
        "v_mfma_f32_32x32x16_bf16 v[80:95], v[168:171], v[116:119], v[80:95]\n\t"
        "ds_write_b32 v127, v128\n\t"
        "ds_read_b64_tr_b16 v[116:117], v130 offset:256\n\t"
        "ds_read_b64_tr_b16 v[118:119], v130 offset:3328\n\t"
        "s_waitcnt vmcnt(8) lgkmcnt(11)\n\t"
        "v_mfma_f32_32x32x16_bf16 v[0:15], v[96:99], v[172:175], v[0:15]\n\t"
        "ds_read_b32 v126, v125\n\t"
        "ds_read_b64_tr_b16 v[96:97], v129 offset:6144\n\t"
        "ds_read_b64_tr_b16 v[98:99], v129 offset:9216\n\t"
        "s_waitcnt lgkmcnt(12)\n\t"
        "v_mfma_f32_32x32x16_bf16 v[16:31], v[100:103], v[172:175], v[16:31]\n\t"
        "v_add_u32 v123, s46, v121\n\t"
        "ds_read_b64_tr_b16 v[100:101], v130 offset:6144\n\t"
        "ds_read_b64_tr_b16 v[102:103], v130 offset:9216\n\t"
        "s_waitcnt lgkmcnt(12)\n\t"
        "v_mfma_f32_32x32x16_bf16 v[32:47], v[104:107], v[172:175], v[32:47]\n\t"
        "ds_read_b64_tr_b16 v[104:105], v129 offset:6272\n\t"
        "ds_read_b64_tr_b16 v[106:107], v129 offset:9344\n\t"
        "s_waitcnt lgkmcnt(12)\n\t"
        "v_mfma_f32_32x32x16_bf16 v[48:63], v[108:111], v[172:175], v[48:63]\n\t"
        "v_add_u32 v124, s46, v122\n\t"
        "ds_read_b64_tr_b16 v[108:109], v130 offset:6272\n\t"
        "ds_read_b64_tr_b16 v[110:111], v130 offset:9344\n\t"
        "s_waitcnt lgkmcnt(12)\n\t"
        "v_mfma_f32_32x32x16_bf16 v[64:79], v[172:175], v[112:115], v[64:79]\n\t"
        "ds_read_b64_tr_b16 v[112:113], v129 offset:6400\n\t"
        "ds_read_b64_tr_b16 v[114:115], v129 offset:9472\n\t"
        "s_waitcnt lgkmcnt(10)\n\t"
        "v_mfma_f32_32x32x16_bf16 v[80:95], v[172:175], v[116:119], v[80:95]\n\t"
        "v_cmp_gt_u32 vcc, s47, v126\n\t"
        "ds_read_b64_tr_b16 v[116:117], v130 offset:6400\n\t"
        "ds_read_b64_tr_b16 v[118:119], v130 offset:9472\n\t"
        "s_waitcnt vmcnt(7) lgkmcnt(10)\n\t"
        "v_mfma_f32_32x32x16_bf16 v[0:15], v[96:99], v[176:179], v[0:15]\n\t"
        "s_cbranch_vccnz 70f\n\t"
        "71:\n\t"
        "ds_read_b64_tr_b16 v[96:97], v123 offset:0\n\t"
        "ds_read_b64_tr_b16 v[98:99], v123 offset:3072\n\t"
        "s_waitcnt lgkmcnt(10)\n\t"
        "v_mfma_f32_32x32x16_bf16 v[16:31], v[100:103], v[176:179], v[16:31]\n\t"
        "v_add_u32 v128, 1, v128\n\t"
        "ds_read_b64_tr_b16 v[100:101], v124 offset:0\n\t"
        "ds_read_b64_tr_b16 v[102:103], v124 offset:3072\n\t"
        "s_waitcnt lgkmcnt(10)\n\t"
        "v_mfma_f32_32x32x16_bf16 v[32:47], v[104:107], v[176:179], v[32:47]\n\t"
        "ds_read_b64_tr_b16 v[104:105], v123 offset:128\n\t"
        "ds_read_b64_tr_b16 v[106:107], v123 offset:3200\n\t"
        "s_waitcnt lgkmcnt(10)\n\t"
        "v_mfma_f32_32x32x16_bf16 v[48:63], v[108:111], v[176:179], v[48:63]\n\t"
        "s_add_u32 s46, s46, 12288\n\t"
        "s_add_u32 s47, s47, 1\n\t"
        "ds_read_b64_tr_b16 v[108:109], v124 offset:128\n\t"
        "ds_read_b64_tr_b16 v[110:111], v124 offset:3200\n\t"
        "s_waitcnt lgkmcnt(10)\n\t"
        "v_mfma_f32_32x32x16_bf16 v[64:79], v[176:179], v[112:115], v[64:79]\n\t"
        "s_cmp_eq_u32 s46, 110592\n\t"
        "s_cselect_b32 s46, 0, s46\n\t"
        "ds_read_b64_tr_b16 v[112:113], v123 offset:256\n\t"
        "ds_read_b64_tr_b16 v[114:115], v123 offset:3328\n\t"
        "s_waitcnt lgkmcnt(10)\n\t"
        "v_mfma_f32_32x32x16_bf16 v[80:95], v[176:179], v[116:119], v[80:95]\n\t"
        "ds_write_b32 v127, v128\n\t"
        "ds_read_b64_tr_b16 v[116:117], v124 offset:256\n\t"
        "ds_read_b64_tr_b16 v[118:119], v124 offset:3328\n\t"
        "s_waitcnt vmcnt(6) lgkmcnt(11)\n\t"
        "v_mfma_f32_32x32x16_bf16 v[0:15], v[96:99], v[180:183], v[0:15]\n\t"
        "ds_read_b32 v126, v125\n\t"
        "ds_read_b64_tr_b16 v[96:97], v123 offset:6144\n\t"
        "ds_read_b64_tr_b16 v[98:99], v123 offset:9216\n\t"
        "s_waitcnt lgkmcnt(12)\n\t"
        "v_mfma_f32_32x32x16_bf16 v[16:31], v[100:103], v[180:183], v[16:31]\n\t"
        "v_add_u32 v129, s46, v121\n\t"
        "ds_read_b64_tr_b16 v[100:101], v124 offset:6144\n\t"
        "ds_read_b64_tr_b16 v[102:103], v124 offset:9216\n\t"
        "s_waitcnt lgkmcnt(12)\n\t"
        "v_mfma_f32_32x32x16_bf16 v[32:47], v[104:107], v[180:183], v[32:47]\n\t"
        "ds_read_b64_tr_b16 v[104:105], v123 offset:6272\n\t"
        "ds_read_b64_tr_b16 v[106:107], v123 offset:9344\n\t"
        "s_waitcnt lgkmcnt(12)\n\t"
        "v_mfma_f32_32x32x16_bf16 v[48:63], v[108:111], v[180:183], v[48:63]\n\t"
        "v_add_u32 v130, s46, v122\n\t"
        "ds_read_b64_tr_b16 v[108:109], v124 offset:6272\n\t"
        "ds_read_b64_tr_b16 v[110:111], v124 offset:9344\n\t"
        "s_waitcnt lgkmcnt(12)\n\t"
        "v_mfma_f32_32x32x16_bf16 v[64:79], v[180:183], v[112:115], v[64:79]\n\t"
        "ds_read_b64_tr_b16 v[112:113], v123 offset:6400\n\t"
        "ds_read_b64_tr_b16 v[114:115], v123 offset:9472\n\t"
        "s_waitcnt lgkmcnt(10)\n\t"
        "v_mfma_f32_32x32x16_bf16 v[80:95], v[180:183], v[116:119], v[80:95]\n\t"
        "v_cmp_gt_u32 vcc, s47, v126\n\t"
        "ds_read_b64_tr_b16 v[116:117], v124 offset:6400\n\t"
        "ds_read_b64_tr_b16 v[118:119], v124 offset:9472\n\t"
        "s_waitcnt vmcnt(5) lgkmcnt(10)\n\t"
        "v_mfma_f32_32x32x16_bf16 v[0:15], v[96:99], v[184:187], v[0:15]\n\t"
        "s_cbranch_vccnz 72f\n\t"
        "73:\n\t"
        "ds_read_b64_tr_b16 v[96:97], v129 offset:0\n\t"
        "ds_read_b64_tr_b16 v[98:99], v129 offset:3072\n\t"
        "s_waitcnt lgkmcnt(10)\n\t"
        "v_mfma_f32_32x32x16_bf16 v[16:31], v[100:103], v[184:187], v[16:31]\n\t"
        "v_add_u32 v128, 1, v128\n\t"
        "ds_read_b64_tr_b16 v[100:101], v130 offset:0\n\t"
        "ds_read_b64_tr_b16 v[102:103], v130 offset:3072\n\t"
        "s_waitcnt lgkmcnt(10)\n\t"
        "v_mfma_f32_32x32x16_bf16 v[32:47], v[104:107], v[184:187], v[32:47]\n\t"
        "ds_read_b64_tr_b16 v[104:105], v129 offset:128\n\t"
        "ds_read_b64_tr_b16 v[106:107], v129 offset:3200\n\t"
        "s_waitcnt lgkmcnt(10)\n\t"
        "v_mfma_f32_32x32x16_bf16 v[48:63], v[108:111], v[184:187], v[48:63]\n\t"
        "s_add_u32 s46, s46, 12288\n\t"
        "s_add_u32 s47, s47, 1\n\t"
        "ds_read_b64_tr_b16 v[108:109], v130 offset:128\n\t"
        "ds_read_b64_tr_b16 v[110:111], v130 offset:3200\n\t"
        "s_waitcnt lgkmcnt(10)\n\t"
        "v_mfma_f32_32x32x16_bf16 v[64:79], v[184:187], v[112:115], v[64:79]\n\t"
        "s_cmp_eq_u32 s46, 110592\n\t"
        "s_cselect_b32 s46, 0, s46\n\t"
        "ds_read_b64_tr_b16 v[112:113], v129 offset:256\n\t"
        "ds_read_b64_tr_b16 v[114:115], v129 offset:3328\n\t"
        "s_waitcnt lgkmcnt(10)\n\t"
        "v_mfma_f32_32x32x16_bf16 v[80:95], v[184:187], v[116:119], v[80:95]\n\t"
        "ds_write_b32 v127, v128\n\t"
        "ds_read_b64_tr_b16 v[116:117], v130 offset:256\n\t"
        "ds_read_b64_tr_b16 v[118:119], v130 offset:3328\n\t"
        "s_waitcnt vmcnt(4) lgkmcnt(11)\n\t"
        "v_mfma_f32_32x32x16_bf16 v[0:15], v[96:99], v[188:191], v[0:15]\n\t"
        "ds_read_b64_tr_b16 v[96:97], v129 offset:6144\n\t"
        "ds_read_b64_tr_b16 v[98:99], v129 offset:9216\n\t"
        "s_waitcnt lgkmcnt(11)\n\t"
        "v_mfma_f32_32x32x16_bf16 v[16:31], v[100:103], v[188:191], v[16:31]\n\t"
        "ds_read_b64_tr_b16 v[100:101], v130 offset:6144\n\t"
        "ds_read_b64_tr_b16 v[102:103], v130 offset:9216\n\t"
        "s_waitcnt lgkmcnt(11)\n\t"
        "v_mfma_f32_32x32x16_bf16 v[32:47], v[104:107], v[188:191], v[32:47]\n\t"
        "ds_read_b64_tr_b16 v[104:105], v129 offset:6272\n\t"
        "ds_read_b64_tr_b16 v[106:107], v129 offset:9344\n\t"
        "s_waitcnt lgkmcnt(11)\n\t"
        "v_mfma_f32_32x32x16_bf16 v[48:63], v[108:111], v[188:191], v[48:63]\n\t"
        "ds_read_b64_tr_b16 v[108:109], v130 offset:6272\n\t"
        "ds_read_b64_tr_b16 v[110:111], v130 offset:9344\n\t"
        "s_waitcnt lgkmcnt(11)\n\t"
        "v_mfma_f32_32x32x16_bf16 v[64:79], v[188:191], v[112:115], v[64:79]\n\t"
        "ds_read_b64_tr_b16 v[112:113], v129 offset:6400\n\t"
        "ds_read_b64_tr_b16 v[114:115], v129 offset:9472\n\t"
        "s_waitcnt lgkmcnt(10)\n\t"
        "v_mfma_f32_32x32x16_bf16 v[80:95], v[188:191], v[116:119], v[80:95]\n\t"
        "ds_read_b64_tr_b16 v[116:117], v130 offset:6400\n\t"
        "ds_read_b64_tr_b16 v[118:119], v130 offset:9472\n\t"
        "s_waitcnt vmcnt(3) lgkmcnt(10)\n\t"
        "v_mfma_f32_32x32x16_bf16 v[0:15], v[96:99], v[192:195], v[0:15]\n\t"
        "s_waitcnt lgkmcnt(8)\n\t"
        "v_mfma_f32_32x32x16_bf16 v[16:31], v[100:103], v[192:195], v[16:31]\n\t"
        "s_waitcnt lgkmcnt(6)\n\t"
        "v_mfma_f32_32x32x16_bf16 v[32:47], v[104:107], v[192:195], v[32:47]\n\t"
        "s_waitcnt lgkmcnt(4)\n\t"
        "v_mfma_f32_32x32x16_bf16 v[48:63], v[108:111], v[192:195], v[48:63]\n\t"
        "s_waitcnt lgkmcnt(2)\n\t"
        "v_mfma_f32_32x32x16_bf16 v[64:79], v[192:195], v[112:115], v[64:79]\n\t"
        "s_waitcnt lgkmcnt(0)\n\t"
        "v_mfma_f32_32x32x16_bf16 v[80:95], v[192:195], v[116:119], v[80:95]\n\t"
        "s_waitcnt vmcnt(0)\n\t"
        "s_nop 10\n\t"
        "s_branch 9f\n\t"
        "16:\n\t"
        "s_mov_b32 s48, 0x10000\n\t"
        "8:\n\t"
        "ds_read_b32 v126, v125\n\t"
        "s_sub_u32 s48, s48, 1\n\t"
        "s_waitcnt lgkmcnt(0)\n\t"
        "s_cbranch_scc1 17b\n\t"
        "v_cmp_gt_u32 vcc, s47, v126\n\t"
        "s_cbranch_vccnz 8b\n\t"
        "s_branch 17b\n\t"
        "18:\n\t"
        "s_mov_b32 s48, 0x10000\n\t"
        "8:\n\t"
        "ds_read_b32 v126, v125\n\t"
        "s_sub_u32 s48, s48, 1\n\t"
        "s_waitcnt lgkmcnt(0)\n\t"
        "s_cbranch_scc1 19b\n\t"
        "v_cmp_gt_u32 vcc, s47, v126\n\t"
        "s_cbranch_vccnz 8b\n\t"
        "s_branch 19b\n\t"
        "20:\n\t"
        "s_mov_b32 s48, 0x10000\n\t"
        "8:\n\t"
        "ds_read_b32 v126, v125\n\t"
        "s_sub_u32 s48, s48, 1\n\t"
        "s_waitcnt lgkmcnt(0)\n\t"
        "s_cbranch_scc1 21b\n\t"
        "v_cmp_gt_u32 vcc, s47, v126\n\t"
        "s_cbranch_vccnz 8b\n\t"
        "s_branch 21b\n\t"
        "22:\n\t"
        "s_mov_b32 s48, 0x10000\n\t"
        "8:\n\t"
        "ds_read_b32 v126, v125\n\t"
        "s_sub_u32 s48, s48, 1\n\t"
        "s_waitcnt lgkmcnt(0)\n\t"
        "s_cbranch_scc1 23b\n\t"
        "v_cmp_gt_u32 vcc, s47, v126\n\t"
        "s_cbranch_vccnz 8b\n\t"
        "s_branch 23b\n\t"
        "24:\n\t"
        "s_mov_b32 s48, 0x10000\n\t"
        "8:\n\t"
        "ds_read_b32 v126, v125\n\t"
        "s_sub_u32 s48, s48, 1\n\t"
        "s_waitcnt lgkmcnt(0)\n\t"
        "s_cbranch_scc1 25b\n\t"
        "v_cmp_gt_u32 vcc, s47, v126\n\t"
        "s_cbranch_vccnz 8b\n\t"
        "s_branch 25b\n\t"
        "26:\n\t"
        "s_mov_b32 s48, 0x10000\n\t"
        "8:\n\t"
        "ds_read_b32 v126, v125\n\t"
        "s_sub_u32 s48, s48, 1\n\t"
        "s_waitcnt lgkmcnt(0)\n\t"
        "s_cbranch_scc1 27b\n\t"
        "v_cmp_gt_u32 vcc, s47, v126\n\t"
        "s_cbranch_vccnz 8b\n\t"
        "s_branch 27b\n\t"
        "28:\n\t"
        "s_mov_b32 s48, 0x10000\n\t"
        "8:\n\t"
        "ds_read_b32 v126, v125\n\t"
        "s_sub_u32 s48, s48, 1\n\t"
        "s_waitcnt lgkmcnt(0)\n\t"
        "s_cbranch_scc1 29b\n\t"
        "v_cmp_gt_u32 vcc, s47, v126\n\t"
        "s_cbranch_vccnz 8b\n\t"
        "s_branch 29b\n\t"
        "30:\n\t"
        "s_mov_b32 s48, 0x10000\n\t"
        "8:\n\t"
        "ds_read_b32 v126, v125\n\t"
        "s_sub_u32 s48, s48, 1\n\t"
        "s_waitcnt lgkmcnt(0)\n\t"
        "s_cbranch_scc1 31b\n\t"
        "v_cmp_gt_u32 vcc, s47, v126\n\t"
        "s_cbranch_vccnz 8b\n\t"
        "s_branch 31b\n\t"
        "64:\n\t"
        "s_mov_b32 s48, 0x10000\n\t"
        "8:\n\t"
        "ds_read_b32 v126, v125\n\t"
        "s_sub_u32 s48, s48, 1\n\t"
        "s_waitcnt lgkmcnt(0)\n\t"
        "s_cbranch_scc1 65b\n\t"
        "v_cmp_gt_u32 vcc, s47, v126\n\t"
        "s_cbranch_vccnz 8b\n\t"
        "s_branch 65b\n\t"
        "66:\n\t"
        "s_mov_b32 s48, 0x10000\n\t"
        "8:\n\t"
        "ds_read_b32 v126, v125\n\t"
        "s_sub_u32 s48, s48, 1\n\t"
        "s_waitcnt lgkmcnt(0)\n\t"
        "s_cbranch_scc1 67b\n\t"
        "v_cmp_gt_u32 vcc, s47, v126\n\t"
        "s_cbranch_vccnz 8b\n\t"
        "s_branch 67b\n\t"
        "68:\n\t"
        "s_mov_b32 s48, 0x10000\n\t"
        "8:\n\t"
        "ds_read_b32 v126, v125\n\t"
        "s_sub_u32 s48, s48, 1\n\t"
        "s_waitcnt lgkmcnt(0)\n\t"
        "s_cbranch_scc1 69b\n\t"
        "v_cmp_gt_u32 vcc, s47, v126\n\t"
        "s_cbranch_vccnz 8b\n\t"
        "s_branch 69b\n\t"
        "70:\n\t"
        "s_mov_b32 s48, 0x10000\n\t"
        "8:\n\t"
        "ds_read_b32 v126, v125\n\t"
        "s_sub_u32 s48, s48, 1\n\t"
        "s_waitcnt lgkmcnt(0)\n\t"
        "s_cbranch_scc1 71b\n\t"
        "v_cmp_gt_u32 vcc, s47, v126\n\t"
        "s_cbranch_vccnz 8b\n\t"
        "s_branch 71b\n\t"
        "72:\n\t"
        "s_mov_b32 s48, 0x10000\n\t"
        "8:\n\t"
        "ds_read_b32 v126, v125\n\t"
        "s_sub_u32 s48, s48, 1\n\t"
        "s_waitcnt lgkmcnt(0)\n\t"
        "s_cbranch_scc1 73b\n\t"
        "v_cmp_gt_u32 vcc, s47, v126\n\t"
        "s_cbranch_vccnz 8b\n\t"
        "s_branch 73b\n\t"
        "9:\n\t"
        : "+{v[0:15]}"(acc[0]), "+{v[16:31]}"(acc[1]), "+{v[32:47]}"(acc[2]), "+{v[48:63]}"(acc[3]), "+{v[64:79]}"(acc[4]), "+{v[80:95]}"(acc[5]),
          "+{v[96:111]}"(stA), "+{v[112:127]}"(stB), "+{v[128:131]}"(stC), "+{v[148:163]}"(xr1), "+{v[164:179]}"(xr2), "+{v[180:195]}"(xr3),
          "+{s44}"(soff), "+{s46}"(slot), "+{s47}"(need), [r0] "+v"(x1p[0]), [r1] "+v"(x1p[1]), [r2] "+v"(x1p[2]), [r3] "+v"(x1p[3])
        : "{s[40:43]}"(srd), [ra] "v"(xsrc) : "v132", "v133", "v134", "v135", "v136", "v137", "v138", "v139", "v140", "v141", "v142", "v143", "v144", "v145", "v146", "v147", "s45", "s48", "vcc", "scc", "memory");
      STAMPV(4 + 16 * L);
      PHASE_VARS;
      if (L == 1) {
        f32x4 av[4], bv[4], cs[4][4];
        float cv[2];
#pragma unroll
        for (int g = 0; g < 4; ++g) {
          av[g] = *reinterpret_cast<const f32x4*>(smem + L_AB + wid * 256 + (8 * g + 4 * hi) * 4);
          bv[g] = *reinterpret_cast<const f32x4*>(smem + L_AB + wid * 256 + 128 + (8 * g + 4 * hi) * 4);
        }
#pragma unroll
        for (int j = 5; j >= 0; --j) {
          const int cb = (j >> 1) * 64 + ((j & 1) ^ hh) * 32;
          if (j >= 4) cv[j - 4] = *reinterpret_cast<const float*>(smem + L_CS + (cb + r32) * 4);
          else {
#pragma unroll
            for (int g = 0; g < 4; ++g) cs[j][g] = *reinterpret_cast<const f32x4*>(smem + L_CS + (cb + 8 * g + 4 * hi) * 4);
          }
        }
        SBAR();
#pragma unroll
        for (int j = 5; j >= 0; --j)
#pragma unroll
          for (int g = 0; g < 4; ++g)
#pragma unroll
            for (int e = 0; e < 4; ++e)
              acc[j][4 * g + e] = j < 4 ? al * acc[j][4 * g + e] + be * cs[j & 3][g][e] : av[g][e] * acc[j][4 * g + e] + bv[g][e] * cv[j & 1];
        SBAR();
        lds_wait_ge(lbase + L_W + 4 + 4 * (lane & 1), 1);
        const int idx = (int)__builtin_amdgcn_readfirstlane(words[3]);
        const bool mine = (idx >> 5) == tile;
        if (mine) {
#pragma unroll
          for (int j = 0; j < 6; ++j) {
            const int cb = (j >> 1) * 64 + ((j & 1) ^ hh) * 32;
#pragma unroll
            for (int g = 0; g < 4; ++g) {
              if (j < 4) {
                const f32x4 pv = *reinterpret_cast<const f32x4*>(smem + L_PATCH + (cb + 8 * g + 4 * hi) * 4);
#pragma unroll
                for (int e = 0; e < 4; ++e) acc[j][4 * g + e] = r32 == (idx & 31) ? pv[e] : acc[j][4 * g + e];
              } else {
                const float pv = *reinterpret_cast<const float*>(smem + L_PATCH + (cb + r32) * 4);
#pragma unroll
                for (int e = 0; e < 4; ++e) acc[j][4 * g + e] = (8 * g + 4 * hi + e) == (idx & 31) ? pv : acc[j][4 * g + e];
              }
            }
          }
        }
      }
      constexpr float QS = 0.17677669529663687f * 1.4426950408889634f;
      u32x4 own[4];
#pragma unroll
      for (int j = 5; j >= 0; --j) {
        u32x4 o[2];
#pragma unroll
        for (int e = 0; e < 8; ++e) {
          unsigned w = pack2(acc[j][2 * e], acc[j][2 * e + 1]);
          if (j < 2) w = pack2(lo16(w) * QS, hi16(w) * QS);
          o[e >> 2][e & 3] = w;
        }
        if (j >= 4) { o[0] = fo_swap(o[0]); o[1] = fo_swap(o[1]); }
        const int hs = (j & 1) ^ hh;
        if (j >= 2) {
          const unsigned dst = OFF_KV + L * (10 * MBY) + (tn * 2 + hs) * HS + tile * 4096 + ((j >> 1) - 1) * 2048 + lane * 16;
          stg(rs, dst, o[0]); stg(rs, dst + 1024, o[1]);
        }
        if (j >= 2 && !(j & 1)) { own[j - 2] = o[0]; own[j - 1] = o[1]; }
        else {
          char* p = smem + L_XCH + (j < 2 ? (hs * 4 + wid) * 2048 : 16384 + wid * 4096 + ((j >> 1) - 1) * 2048) + lane * 16;
          *reinterpret_cast<u32x4*>(p) = o[0]; *reinterpret_cast<u32x4*>(p + 1024) = o[1];
        }
        SBAR();
      }
      BAR_LGKM();
      kva = cat4(own[0], own[1], own[2], own[3]);
      const u32x4* pk = reinterpret_cast<const u32x4*>(smem + L_XCH + 16384 + (wid ^ 1) * 4096 + lane * 16);
#pragma unroll
      for (int u = 0; u < 4; ++u) {
        const u32x4* p = reinterpret_cast<const u32x4*>(smem + L_XCH + (hh * 4 + ((2 * kh + 2 + u) & 3)) * 2048 + lane * 16);
        q[u] = __builtin_shufflevector(p[0], p[64], 0, 1, 2, 3, 4, 5, 6, 7);
      }
      kvb = cat4(pk[0], pk[64], pk[128], pk[192]);
    }
    STAMPV(5 + 16 * L);
    {
      u32x4 ones;
      unsigned kvoff;
      u32x8 fv, cv;
      asm volatile("" : "={v[184:191]}"(fv), "={v[216:223]}"(cv));
      { PHASE_VARS;
        const unsigned fl = FL_KV + (L * 16 + tn) * 8192;
        const unsigned v = (lane & 3) == 0 ? 0x3F803F80u : 0u; const bool up = (lane & 4) != 0; ones[0] = ones[1] = up ? 0u : v; ones[2] = ones[3] = up ? v : 0u;
        kvoff = OFF_KV + L * (10 * MBY) + (tn * 2 + hh) * HS + kh * 8192 + lane * 16;
        fv[0] = lane == 0 ? fl + (kh * 32 + tm * 2 + hh) * 128 : OOB;
        fv[1] = fl + (hi ? (kh ^ 1) * 32 + tm * 2 + (lane & 1) : kh * 32 + r32) * 128;
        fv[2] = TAG; fv[3] = lbase + L_FREE + wid * 256 + lane * 4; fv[4] = 32 * L + 32;
        cv[0] = lbase + L_XCH + wid * 8192 + lane * 16; cv[1] = lbase + L_XCH + (wid ^ 2) * 8192 + lane * 16;
        cv[2] = lbase + L_XCH + 32768 + wid * 512 + lane * 4; cv[3] = lbase + L_XCH + 32768 + (wid ^ 2) * 512 + lane * 4;
        cv[4] = lbase + L_W + 48 + wid * 4; cv[5] = lbase + L_W + 48 + (wid ^ 2) * 4; cv[6] = L + 1; }
      f32x16 o0, o1, o2, o3, lv;
      unsigned soff = tm * 16384;
      STAMP_DECL;
      asm volatile(
        "s_nop 1\n\t"
        "v_mfma_f32_32x32x16_bf16 v[96:111], v[152:155], v[64:67], 0\n\t"
        "v_mfma_f32_32x32x16_bf16 v[96:111], v[156:159], v[68:71], v[96:111]\n\t"
        "s_mov_b32 s46, 0\n\t"
        "v_mov_b32 v0, 0\n\t"
        "v_mov_b32 v1, 0\n\t"
        "v_mov_b32 v2, 0\n\t"
        "v_mov_b32 v3, 0\n\t"
        "v_mov_b32 v4, 0\n\t"
        "v_mov_b32 v5, 0\n\t"
        "v_mov_b32 v6, 0\n\t"
        "v_mov_b32 v7, 0\n\t"
        "v_mov_b32 v8, 0\n\t"
        "v_mov_b32 v9, 0\n\t"
        "v_mov_b32 v10, 0\n\t"
        "v_mov_b32 v11, 0\n\t"
        "v_mov_b32 v12, 0\n\t"
        "v_mov_b32 v13, 0\n\t"
        "v_mov_b32 v14, 0\n\t"
        "v_mov_b32 v15, 0\n\t"
        "v_mov_b32 v16, 0\n\t"
        "v_mov_b32 v17, 0\n\t"
        "v_mov_b32 v18, 0\n\t"
        "v_mov_b32 v19, 0\n\t"
        "v_mov_b32 v20, 0\n\t"
        "v_mov_b32 v21, 0\n\t"
        "v_mov_b32 v22, 0\n\t"
        "v_mov_b32 v23, 0\n\t"
        "v_mov_b32 v24, 0\n\t"
        "v_mov_b32 v25, 0\n\t"
        "v_mov_b32 v26, 0\n\t"
        "v_mov_b32 v27, 0\n\t"
        "v_mov_b32 v28, 0\n\t"
        "v_mov_b32 v29, 0\n\t"
        "v_mov_b32 v30, 0\n\t"
        "v_mov_b32 v31, 0\n\t"
        "v_mov_b32 v32, 0\n\t"
        "v_mov_b32 v33, 0\n\t"
        "v_mov_b32 v34, 0\n\t"
        "v_mov_b32 v35, 0\n\t"
        "v_mov_b32 v36, 0\n\t"
        "v_mov_b32 v37, 0\n\t"
        "v_mov_b32 v38, 0\n\t"
        "v_mov_b32 v39, 0\n\t"
        "v_mov_b32 v40, 0\n\t"
        "v_mov_b32 v41, 0\n\t"
        "v_mov_b32 v42, 0\n\t"
        "v_mov_b32 v43, 0\n\t"
        "v_mov_b32 v44, 0\n\t"
        "v_mov_b32 v45, 0\n\t"
        "v_mov_b32 v46, 0\n\t"
        "v_mov_b32 v47, 0\n\t"
        "v_mov_b32 v48, 0\n\t"
        "v_mov_b32 v49, 0\n\t"
        "v_mov_b32 v50, 0\n\t"
        "v_mov_b32 v51, 0\n\t"
        "v_mov_b32 v52, 0\n\t"
        "v_mov_b32 v53, 0\n\t"
        "v_mov_b32 v54, 0\n\t"
        "v_mov_b32 v55, 0\n\t"
        "v_mov_b32 v56, 0\n\t"
        "v_mov_b32 v57, 0\n\t"
        "v_mov_b32 v58, 0\n\t"
        "v_mov_b32 v59, 0\n\t"
        "v_mov_b32 v60, 0\n\t"
        "v_mov_b32 v61, 0\n\t"
        "v_mov_b32 v62, 0\n\t"
        "v_mov_b32 v63, 0\n\t"
        "v_mov_b32 v128, 0\n\t"
        "v_mov_b32 v129, 0\n\t"
        "v_mov_b32 v130, 0\n\t"
        "v_mov_b32 v131, 0\n\t"
        "v_mov_b32 v132, 0\n\t"
        "v_mov_b32 v133, 0\n\t"
        "v_mov_b32 v134, 0\n\t"
        "v_mov_b32 v135, 0\n\t"
        "v_mov_b32 v136, 0\n\t"
        "v_mov_b32 v137, 0\n\t"
        "v_mov_b32 v138, 0\n\t"
        "v_mov_b32 v139, 0\n\t"
        "v_mov_b32 v140, 0\n\t"
        "v_mov_b32 v141, 0\n\t"
        "v_mov_b32 v142, 0\n\t"
        "v_mov_b32 v143, 0\n\t"
        "s_waitcnt vmcnt(0)\n\t"
        "buffer_store_dword v186, v184, s[40:43], 0 offen sc1\n\t"
        "v_exp_f32 v96, v96\n\t"
        "v_exp_f32 v97, v97\n\t"
        "v_exp_f32 v98, v98\n\t"
        "v_exp_f32 v99, v99\n\t"
        "v_exp_f32 v100, v100\n\t"
        "v_exp_f32 v101, v101\n\t"
        "v_mfma_f32_32x32x16_bf16 v[112:127], v[152:155], v[72:75], 0\n\t"
        "v_exp_f32 v102, v102\n\t"
        "v_exp_f32 v103, v103\n\t"
        "v_exp_f32 v104, v104\n\t"
        "v_mfma_f32_32x32x16_bf16 v[112:127], v[156:159], v[76:79], v[112:127]\n\t"
        "v_exp_f32 v105, v105\n\t"
        "v_exp_f32 v106, v106\n\t"
        "v_exp_f32 v107, v107\n\t"
        "v_smfmac_f32_32x32x32_bf16 v[0:15], v[160:163], v[96:103], v148\n\t"
        "v_exp_f32 v108, v108\n\t"
        "v_exp_f32 v109, v109\n\t"
        "v_exp_f32 v110, v110\n\t"
        "v_smfmac_f32_16x16x64_bf16 v[128:131], v[144:147], v[96:103], v148\n\t"
        "v_exp_f32 v111, v111\n\t"
        "v_exp_f32 v112, v112\n\t"
        "v_exp_f32 v113, v113\n\t"
        "v_smfmac_f32_32x32x32_bf16 v[0:15], v[164:167], v[104:111], v148\n\t"
        "v_exp_f32 v114, v114\n\t"
        "v_exp_f32 v115, v115\n\t"
        "v_exp_f32 v116, v116\n\t"
        "v_smfmac_f32_16x16x64_bf16 v[128:131], v[144:147], v[104:111], v148\n\t"
        "v_exp_f32 v117, v117\n\t"
        "v_mfma_f32_32x32x16_bf16 v[96:111], v[152:155], v[80:83], 0\n\t"
        "v_exp_f32 v118, v118\n\t"
        "v_exp_f32 v119, v119\n\t"
        "v_exp_f32 v120, v120\n\t"
        "v_mfma_f32_32x32x16_bf16 v[96:111], v[156:159], v[84:87], v[96:111]\n\t"
        "v_exp_f32 v121, v121\n\t"
        "v_exp_f32 v122, v122\n\t"
        "v_exp_f32 v123, v123\n\t"
        "v_smfmac_f32_32x32x32_bf16 v[16:31], v[160:163], v[112:119], v148\n\t"
        "v_exp_f32 v124, v124\n\t"
        "v_exp_f32 v125, v125\n\t"
        "v_exp_f32 v126, v126\n\t"
        "v_smfmac_f32_16x16x64_bf16 v[132:135], v[144:147], v[112:119], v148\n\t"
        "v_exp_f32 v127, v127\n\t"
        "v_exp_f32 v96, v96\n\t"
        "v_exp_f32 v97, v97\n\t"
        "v_smfmac_f32_32x32x32_bf16 v[16:31], v[164:167], v[120:127], v148\n\t"
        "v_exp_f32 v98, v98\n\t"
        "v_exp_f32 v99, v99\n\t"
        "v_exp_f32 v100, v100\n\t"
        "v_smfmac_f32_16x16x64_bf16 v[132:135], v[144:147], v[120:127], v148\n\t"
        "v_exp_f32 v101, v101\n\t"
        "v_mfma_f32_32x32x16_bf16 v[112:127], v[152:155], v[88:91], 0\n\t"
        "v_exp_f32 v102, v102\n\t"
        "v_exp_f32 v103, v103\n\t"
        "v_exp_f32 v104, v104\n\t"
        "v_mfma_f32_32x32x16_bf16 v[112:127], v[156:159], v[92:95], v[112:127]\n\t"
        "v_exp_f32 v105, v105\n\t"
        "v_exp_f32 v106, v106\n\t"
        "v_exp_f32 v107, v107\n\t"
        "v_smfmac_f32_32x32x32_bf16 v[32:47], v[160:163], v[96:103], v148\n\t"
        "v_exp_f32 v108, v108\n\t"
        "v_exp_f32 v109, v109\n\t"
        "v_exp_f32 v110, v110\n\t"
        "v_smfmac_f32_16x16x64_bf16 v[136:139], v[144:147], v[96:103], v148\n\t"
        "v_exp_f32 v111, v111\n\t"
        "buffer_load_dword v151, v185, s[40:43], 0 offen sc1\n\t"
        "v_exp_f32 v112, v112\n\t"
        "v_exp_f32 v113, v113\n\t"
        "v_smfmac_f32_32x32x32_bf16 v[32:47], v[164:167], v[104:111], v148\n\t"
        "v_exp_f32 v114, v114\n\t"
        "v_exp_f32 v115, v115\n\t"
        "v_exp_f32 v116, v116\n\t"
        "v_smfmac_f32_16x16x64_bf16 v[136:139], v[144:147], v[104:111], v148\n\t"
        "v_exp_f32 v117, v117\n\t"
        "v_mfma_f32_32x32x16_bf16 v[96:111], v[168:171], v[64:67], 0\n\t"
        "v_exp_f32 v118, v118\n\t"
        "v_exp_f32 v119, v119\n\t"
        "v_exp_f32 v120, v120\n\t"
        "v_mfma_f32_32x32x16_bf16 v[96:111], v[172:175], v[68:71], v[96:111]\n\t"
        "v_exp_f32 v121, v121\n\t"
        "v_exp_f32 v122, v122\n\t"
        "v_exp_f32 v123, v123\n\t"
        "v_smfmac_f32_32x32x32_bf16 v[48:63], v[160:163], v[112:119], v148\n\t"
        "v_exp_f32 v124, v124\n\t"
        "v_exp_f32 v125, v125\n\t"
        "v_exp_f32 v126, v126\n\t"
        "v_smfmac_f32_16x16x64_bf16 v[140:143], v[144:147], v[112:119], v148\n\t"
        "v_exp_f32 v127, v127\n\t"
        "v_exp_f32 v96, v96\n\t"
        "v_exp_f32 v97, v97\n\t"
        "v_smfmac_f32_32x32x32_bf16 v[48:63], v[164:167], v[120:127], v148\n\t"
        "v_exp_f32 v98, v98\n\t"
        "v_exp_f32 v99, v99\n\t"
        "v_exp_f32 v100, v100\n\t"
        "v_smfmac_f32_16x16x64_bf16 v[140:143], v[144:147], v[120:127], v148\n\t"
        "v_exp_f32 v101, v101\n\t"
        "v_mfma_f32_32x32x16_bf16 v[112:127], v[168:171], v[72:75], 0\n\t"
        "v_exp_f32 v102, v102\n\t"
        "v_exp_f32 v103, v103\n\t"
        "v_exp_f32 v104, v104\n\t"
        "v_mfma_f32_32x32x16_bf16 v[112:127], v[172:175], v[76:79], v[112:127]\n\t"
        "v_exp_f32 v105, v105\n\t"
        "v_exp_f32 v106, v106\n\t"
        "v_exp_f32 v107, v107\n\t"
        "v_smfmac_f32_32x32x32_bf16 v[0:15], v[176:179], v[96:103], v148\n\t"
        "v_exp_f32 v108, v108\n\t"
        "v_exp_f32 v109, v109\n\t"
        "v_exp_f32 v110, v110\n\t"
        "v_smfmac_f32_16x16x64_bf16 v[128:131], v[144:147], v[96:103], v148\n\t"
        "v_exp_f32 v111, v111\n\t"
        "s_cmp_lg_u32 s46, 0\n\t"
        "s_cbranch_scc1 31f\n\t"
        "s_waitcnt vmcnt(0)\n\t"
        "v_cmp_ne_u32 vcc, v186, v151\n\t"
        "s_cbranch_vccz 30f\n\t"
        "buffer_load_dword v151, v185, s[40:43], 0 offen sc1\n\t"
        "31:\n\t"
        "v_exp_f32 v112, v112\n\t"
        "v_exp_f32 v113, v113\n\t"
        "v_smfmac_f32_32x32x32_bf16 v[0:15], v[180:183], v[104:111], v148\n\t"
        "v_exp_f32 v114, v114\n\t"
        "v_exp_f32 v115, v115\n\t"
        "v_exp_f32 v116, v116\n\t"
        "v_smfmac_f32_16x16x64_bf16 v[128:131], v[144:147], v[104:111], v148\n\t"
        "v_exp_f32 v117, v117\n\t"
        "v_mfma_f32_32x32x16_bf16 v[96:111], v[168:171], v[80:83], 0\n\t"
        "v_exp_f32 v118, v118\n\t"
        "v_exp_f32 v119, v119\n\t"
        "v_exp_f32 v120, v120\n\t"
        "v_mfma_f32_32x32x16_bf16 v[96:111], v[172:175], v[84:87], v[96:111]\n\t"
        "v_exp_f32 v121, v121\n\t"
        "v_exp_f32 v122, v122\n\t"
        "v_exp_f32 v123, v123\n\t"
        "v_smfmac_f32_32x32x32_bf16 v[16:31], v[176:179], v[112:119], v148\n\t"
        "v_exp_f32 v124, v124\n\t"
        "v_exp_f32 v125, v125\n\t"
        "v_exp_f32 v126, v126\n\t"
        "v_smfmac_f32_16x16x64_bf16 v[132:135], v[144:147], v[112:119], v148\n\t"
        "v_exp_f32 v127, v127\n\t"
        "s_cmp_lg_u32 s46, 0\n\t"
        "s_cbranch_scc1 33f\n\t"
        "s_waitcnt vmcnt(0)\n\t"
        "v_cmp_ne_u32 vcc, v186, v151\n\t"
        "s_cbranch_vccz 32f\n\t"
        "buffer_load_dword v151, v185, s[40:43], 0 offen sc1\n\t"
        "33:\n\t"
        "v_exp_f32 v96, v96\n\t"
        "v_exp_f32 v97, v97\n\t"
        "v_smfmac_f32_32x32x32_bf16 v[16:31], v[180:183], v[120:127], v148\n\t"
        "v_exp_f32 v98, v98\n\t"
        "v_exp_f32 v99, v99\n\t"
        "v_exp_f32 v100, v100\n\t"
        "v_smfmac_f32_16x16x64_bf16 v[132:135], v[144:147], v[120:127], v148\n\t"
        "v_exp_f32 v101, v101\n\t"
        "v_mfma_f32_32x32x16_bf16 v[112:127], v[168:171], v[88:91], 0\n\t"
        "v_exp_f32 v102, v102\n\t"
        "v_exp_f32 v103, v103\n\t"
        "v_exp_f32 v104, v104\n\t"
        "v_mfma_f32_32x32x16_bf16 v[112:127], v[172:175], v[92:95], v[112:127]\n\t"
        "v_exp_f32 v105, v105\n\t"
        "v_exp_f32 v106, v106\n\t"
        "v_exp_f32 v107, v107\n\t"
        "v_smfmac_f32_32x32x32_bf16 v[32:47], v[176:179], v[96:103], v148\n\t"
        "v_exp_f32 v108, v108\n\t"
        "v_exp_f32 v109, v109\n\t"
        "v_exp_f32 v110, v110\n\t"
        "v_smfmac_f32_16x16x64_bf16 v[136:139], v[144:147], v[96:103], v148\n\t"
        "v_exp_f32 v111, v111\n\t"
        "s_cmp_lg_u32 s46, 0\n\t"
        "s_cbranch_scc1 5f\n\t"
        "s_mov_b32 s45, 0x8000\n\t"
        "2:\n\t"
        "s_sub_u32 s45, s45, 1\n\t"
        "s_waitcnt vmcnt(0)\n\t"
        "s_cbranch_scc1 3f\n\t"
        "v_cmp_ne_u32 vcc, v186, v151\n\t"
        "s_cbranch_vccz 3f\n\t"
        "buffer_load_dword v151, v185, s[40:43], 0 offen sc1\n\t"
        "s_branch 2b\n\t"
        "3:\n\t"
        "ds_write_b32 v187, v188\n\t"
        "s_add_u32 s44, s44, 0x4000\n\t"
        "s_and_b32 s44, s44, 0x3ffff\n\t"
        "buffer_load_dwordx4 v[184:187], v149, s[40:43], s44 offen offset:0 sc1\n\t"
        "buffer_load_dwordx4 v[188:191], v149, s[40:43], s44 offen offset:1024 sc1\n\t"
        "buffer_load_dwordx4 v[192:195], v149, s[40:43], s44 offen offset:2048 sc1\n\t"
        "buffer_load_dwordx4 v[196:199], v149, s[40:43], s44 offen offset:3072 sc1\n\t"
        "buffer_load_dwordx4 v[200:203], v150, s[40:43], s44 offen offset:0 sc1\n\t"
        "buffer_load_dwordx4 v[204:207], v150, s[40:43], s44 offen offset:1024 sc1\n\t"
        "buffer_load_dwordx4 v[208:211], v150, s[40:43], s44 offen offset:2048 sc1\n\t"
        "buffer_load_dwordx4 v[212:215], v150, s[40:43], s44 offen offset:3072 sc1\n\t"
        "s_add_u32 s44, s44, 0x4000\n\t"
        "s_and_b32 s44, s44, 0x3ffff\n\t"
        "buffer_load_dwordx4 v[152:155], v149, s[40:43], s44 offen offset:0 sc1\n\t"
        "buffer_load_dwordx4 v[156:159], v149, s[40:43], s44 offen offset:1024 sc1\n\t"
        "buffer_load_dwordx4 v[160:163], v149, s[40:43], s44 offen offset:2048 sc1\n\t"
        "buffer_load_dwordx4 v[164:167], v149, s[40:43], s44 offen offset:3072 sc1\n\t"
        "5:\n\t"
        "s_mov_b32 s45, 7\n\t"
        "1:\n\t"
        "v_exp_f32 v112, v112\n\t"
        "v_exp_f32 v113, v113\n\t"
        "v_smfmac_f32_32x32x32_bf16 v[32:47], v[180:183], v[104:111], v148\n\t"
        "v_exp_f32 v114, v114\n\t"
        "v_exp_f32 v115, v115\n\t"
        "v_exp_f32 v116, v116\n\t"
        "v_smfmac_f32_16x16x64_bf16 v[136:139], v[144:147], v[104:111], v148\n\t"
        "v_exp_f32 v117, v117\n\t"
        "s_waitcnt vmcnt(8)\n\t"
        "v_mfma_f32_32x32x16_bf16 v[96:111], v[184:187], v[64:67], 0\n\t"
        "v_exp_f32 v118, v118\n\t"
        "v_exp_f32 v119, v119\n\t"
        "v_exp_f32 v120, v120\n\t"
        "v_mfma_f32_32x32x16_bf16 v[96:111], v[188:191], v[68:71], v[96:111]\n\t"
        "v_exp_f32 v121, v121\n\t"
        "v_exp_f32 v122, v122\n\t"
        "v_exp_f32 v123, v123\n\t"
        "v_smfmac_f32_32x32x32_bf16 v[48:63], v[176:179], v[112:119], v148\n\t"
        "v_exp_f32 v124, v124\n\t"
        "v_exp_f32 v125, v125\n\t"
        "v_exp_f32 v126, v126\n\t"
        "v_smfmac_f32_16x16x64_bf16 v[140:143], v[144:147], v[112:119], v148\n\t"
        "buffer_load_dwordx4 v[168:171], v150, s[40:43], s44 offen offset:0 sc1\n\t"
        "v_exp_f32 v127, v127\n\t"
        "v_exp_f32 v96, v96\n\t"
        "v_exp_f32 v97, v97\n\t"
        "v_smfmac_f32_32x32x32_bf16 v[48:63], v[180:183], v[120:127], v148\n\t"
        "v_exp_f32 v98, v98\n\t"
        "v_exp_f32 v99, v99\n\t"
        "v_exp_f32 v100, v100\n\t"
        "v_smfmac_f32_16x16x64_bf16 v[140:143], v[144:147], v[120:127], v148\n\t"
        "v_exp_f32 v101, v101\n\t"
        "v_mfma_f32_32x32x16_bf16 v[112:127], v[184:187], v[72:75], 0\n\t"
        "v_exp_f32 v102, v102\n\t"
        "v_exp_f32 v103, v103\n\t"
        "v_exp_f32 v104, v104\n\t"
        "v_mfma_f32_32x32x16_bf16 v[112:127], v[188:191], v[76:79], v[112:127]\n\t"
        "v_exp_f32 v105, v105\n\t"
        "v_exp_f32 v106, v106\n\t"
        "v_exp_f32 v107, v107\n\t"
        "v_smfmac_f32_32x32x32_bf16 v[0:15], v[192:195], v[96:103], v148\n\t"
        "v_exp_f32 v108, v108\n\t"
        "v_exp_f32 v109, v109\n\t"
        "v_exp_f32 v110, v110\n\t"
        "v_smfmac_f32_16x16x64_bf16 v[128:131], v[144:147], v[96:103], v148\n\t"
        "buffer_load_dwordx4 v[172:175], v150, s[40:43], s44 offen offset:1024 sc1\n\t"
        "v_exp_f32 v111, v111\n\t"
        "v_exp_f32 v112, v112\n\t"
        "v_exp_f32 v113, v113\n\t"
        "v_smfmac_f32_32x32x32_bf16 v[0:15], v[196:199], v[104:111], v148\n\t"
        "v_exp_f32 v114, v114\n\t"
        "v_exp_f32 v115, v115\n\t"
        "v_exp_f32 v116, v116\n\t"
        "v_smfmac_f32_16x16x64_bf16 v[128:131], v[144:147], v[104:111], v148\n\t"
        "v_exp_f32 v117, v117\n\t"
        "v_mfma_f32_32x32x16_bf16 v[96:111], v[184:187], v[80:83], 0\n\t"
        "v_exp_f32 v118, v118\n\t"
        "v_exp_f32 v119, v119\n\t"
        "v_exp_f32 v120, v120\n\t"
        "v_mfma_f32_32x32x16_bf16 v[96:111], v[188:191], v[84:87], v[96:111]\n\t"
        "v_exp_f32 v121, v121\n\t"
        "v_exp_f32 v122, v122\n\t"
        "v_exp_f32 v123, v123\n\t"
        "v_smfmac_f32_32x32x32_bf16 v[16:31], v[192:195], v[112:119], v148\n\t"
        "v_exp_f32 v124, v124\n\t"
        "v_exp_f32 v125, v125\n\t"
        "v_exp_f32 v126, v126\n\t"
        "v_smfmac_f32_16x16x64_bf16 v[132:135], v[144:147], v[112:119], v148\n\t"
        "buffer_load_dwordx4 v[176:179], v150, s[40:43], s44 offen offset:2048 sc1\n\t"
        "v_exp_f32 v127, v127\n\t"
        "v_exp_f32 v96, v96\n\t"
        "v_exp_f32 v97, v97\n\t"
        "v_smfmac_f32_32x32x32_bf16 v[16:31], v[196:199], v[120:127], v148\n\t"
        "v_exp_f32 v98, v98\n\t"
        "v_exp_f32 v99, v99\n\t"
        "v_exp_f32 v100, v100\n\t"
        "v_smfmac_f32_16x16x64_bf16 v[132:135], v[144:147], v[120:127], v148\n\t"
        "v_exp_f32 v101, v101\n\t"
        "v_mfma_f32_32x32x16_bf16 v[112:127], v[184:187], v[88:91], 0\n\t"
        "v_exp_f32 v102, v102\n\t"
        "v_exp_f32 v103, v103\n\t"
        "v_exp_f32 v104, v104\n\t"
        "v_mfma_f32_32x32x16_bf16 v[112:127], v[188:191], v[92:95], v[112:127]\n\t"
        "v_exp_f32 v105, v105\n\t"
        "v_exp_f32 v106, v106\n\t"
        "v_exp_f32 v107, v107\n\t"
        "v_smfmac_f32_32x32x32_bf16 v[32:47], v[192:195], v[96:103], v148\n\t"
        "v_exp_f32 v108, v108\n\t"
        "v_exp_f32 v109, v109\n\t"
        "v_exp_f32 v110, v110\n\t"
        "v_smfmac_f32_16x16x64_bf16 v[136:139], v[144:147], v[96:103], v148\n\t"
        "buffer_load_dwordx4 v[180:183], v150, s[40:43], s44 offen offset:3072 sc1\n\t"
        "v_exp_f32 v111, v111\n\t"
        "v_exp_f32 v112, v112\n\t"
        "v_exp_f32 v113, v113\n\t"
        "v_smfmac_f32_32x32x32_bf16 v[32:47], v[196:199], v[104:111], v148\n\t"
        "s_add_u32 s44, s44, 0x4000\n\t"
        "s_and_b32 s44, s44, 0x3ffff\n\t"
        "v_exp_f32 v114, v114\n\t"
        "v_exp_f32 v115, v115\n\t"
        "v_exp_f32 v116, v116\n\t"
        "v_smfmac_f32_16x16x64_bf16 v[136:139], v[144:147], v[104:111], v148\n\t"
        "v_exp_f32 v117, v117\n\t"
        "s_waitcnt vmcnt(8)\n\t"
        "v_mfma_f32_32x32x16_bf16 v[96:111], v[200:203], v[64:67], 0\n\t"
        "v_exp_f32 v118, v118\n\t"
        "v_exp_f32 v119, v119\n\t"
        "v_exp_f32 v120, v120\n\t"
        "v_mfma_f32_32x32x16_bf16 v[96:111], v[204:207], v[68:71], v[96:111]\n\t"
        "v_exp_f32 v121, v121\n\t"
        "v_exp_f32 v122, v122\n\t"
        "v_exp_f32 v123, v123\n\t"
        "v_smfmac_f32_32x32x32_bf16 v[48:63], v[192:195], v[112:119], v148\n\t"
        "v_exp_f32 v124, v124\n\t"
        "v_exp_f32 v125, v125\n\t"
        "v_exp_f32 v126, v126\n\t"
        "v_smfmac_f32_16x16x64_bf16 v[140:143], v[144:147], v[112:119], v148\n\t"
        "buffer_load_dwordx4 v[184:187], v149, s[40:43], s44 offen offset:0 sc1\n\t"
        "v_exp_f32 v127, v127\n\t"
        "v_exp_f32 v96, v96\n\t"
        "v_exp_f32 v97, v97\n\t"
        "v_smfmac_f32_32x32x32_bf16 v[48:63], v[196:199], v[120:127], v148\n\t"
        "v_exp_f32 v98, v98\n\t"
        "v_exp_f32 v99, v99\n\t"
        "v_exp_f32 v100, v100\n\t"
        "v_smfmac_f32_16x16x64_bf16 v[140:143], v[144:147], v[120:127], v148\n\t"
        "v_exp_f32 v101, v101\n\t"
        "v_mfma_f32_32x32x16_bf16 v[112:127], v[200:203], v[72:75], 0\n\t"
        "v_exp_f32 v102, v102\n\t"
        "v_exp_f32 v103, v103\n\t"
        "v_exp_f32 v104, v104\n\t"
        "v_mfma_f32_32x32x16_bf16 v[112:127], v[204:207], v[76:79], v[112:127]\n\t"
        "v_exp_f32 v105, v105\n\t"
        "v_exp_f32 v106, v106\n\t"
        "v_exp_f32 v107, v107\n\t"
        "v_smfmac_f32_32x32x32_bf16 v[0:15], v[208:211], v[96:103], v148\n\t"
        "v_exp_f32 v108, v108\n\t"
        "v_exp_f32 v109, v109\n\t"
        "v_exp_f32 v110, v110\n\t"
        "v_smfmac_f32_16x16x64_bf16 v[128:131], v[144:147], v[96:103], v148\n\t"
        "buffer_load_dwordx4 v[188:191], v149, s[40:43], s44 offen offset:1024 sc1\n\t"
        "v_exp_f32 v111, v111\n\t"
        "v_exp_f32 v112, v112\n\t"
        "v_exp_f32 v113, v113\n\t"
        "v_smfmac_f32_32x32x32_bf16 v[0:15], v[212:215], v[104:111], v148\n\t"
        "v_exp_f32 v114, v114\n\t"
        "v_exp_f32 v115, v115\n\t"
        "v_exp_f32 v116, v116\n\t"
        "v_smfmac_f32_16x16x64_bf16 v[128:131], v[144:147], v[104:111], v148\n\t"
        "v_exp_f32 v117, v117\n\t"
        "v_mfma_f32_32x32x16_bf16 v[96:111], v[200:203], v[80:83], 0\n\t"
        "v_exp_f32 v118, v118\n\t"
        "v_exp_f32 v119, v119\n\t"
        "v_exp_f32 v120, v120\n\t"
        "v_mfma_f32_32x32x16_bf16 v[96:111], v[204:207], v[84:87], v[96:111]\n\t"
        "v_exp_f32 v121, v121\n\t"
        "v_exp_f32 v122, v122\n\t"
        "v_exp_f32 v123, v123\n\t"
        "v_smfmac_f32_32x32x32_bf16 v[16:31], v[208:211], v[112:119], v148\n\t"
        "v_exp_f32 v124, v124\n\t"
        "v_exp_f32 v125, v125\n\t"
        "v_exp_f32 v126, v126\n\t"
        "v_smfmac_f32_16x16x64_bf16 v[132:135], v[144:147], v[112:119], v148\n\t"
        "buffer_load_dwordx4 v[192:195], v149, s[40:43], s44 offen offset:2048 sc1\n\t"
        "v_exp_f32 v127, v127\n\t"
        "v_exp_f32 v96, v96\n\t"
        "v_exp_f32 v97, v97\n\t"
        "v_smfmac_f32_32x32x32_bf16 v[16:31], v[212:215], v[120:127], v148\n\t"
        "v_exp_f32 v98, v98\n\t"
        "v_exp_f32 v99, v99\n\t"
        "v_exp_f32 v100, v100\n\t"
        "v_smfmac_f32_16x16x64_bf16 v[132:135], v[144:147], v[120:127], v148\n\t"
        "v_exp_f32 v101, v101\n\t"
        "v_mfma_f32_32x32x16_bf16 v[112:127], v[200:203], v[88:91], 0\n\t"
        "v_exp_f32 v102, v102\n\t"
        "v_exp_f32 v103, v103\n\t"
        "v_exp_f32 v104, v104\n\t"
        "v_mfma_f32_32x32x16_bf16 v[112:127], v[204:207], v[92:95], v[112:127]\n\t"
        "v_exp_f32 v105, v105\n\t"
        "v_exp_f32 v106, v106\n\t"
        "v_exp_f32 v107, v107\n\t"
        "v_smfmac_f32_32x32x32_bf16 v[32:47], v[208:211], v[96:103], v148\n\t"
        "v_exp_f32 v108, v108\n\t"
        "v_exp_f32 v109, v109\n\t"
        "v_exp_f32 v110, v110\n\t"
        "v_smfmac_f32_16x16x64_bf16 v[136:139], v[144:147], v[96:103], v148\n\t"
        "buffer_load_dwordx4 v[196:199], v149, s[40:43], s44 offen offset:3072 sc1\n\t"
        "v_exp_f32 v111, v111\n\t"
        "v_exp_f32 v112, v112\n\t"
        "v_exp_f32 v113, v113\n\t"
        "v_smfmac_f32_32x32x32_bf16 v[32:47], v[212:215], v[104:111], v148\n\t"
        "v_exp_f32 v114, v114\n\t"
        "v_exp_f32 v115, v115\n\t"
        "v_exp_f32 v116, v116\n\t"
        "v_smfmac_f32_16x16x64_bf16 v[136:139], v[144:147], v[104:111], v148\n\t"
        "v_exp_f32 v117, v117\n\t"
        "s_waitcnt vmcnt(8)\n\t"
        "v_mfma_f32_32x32x16_bf16 v[96:111], v[152:155], v[64:67], 0\n\t"
        "v_exp_f32 v118, v118\n\t"
        "v_exp_f32 v119, v119\n\t"
        "v_exp_f32 v120, v120\n\t"
        "v_mfma_f32_32x32x16_bf16 v[96:111], v[156:159], v[68:71], v[96:111]\n\t"
        "v_exp_f32 v121, v121\n\t"
        "v_exp_f32 v122, v122\n\t"
        "v_exp_f32 v123, v123\n\t"
        "v_smfmac_f32_32x32x32_bf16 v[48:63], v[208:211], v[112:119], v148\n\t"
        "v_exp_f32 v124, v124\n\t"
        "v_exp_f32 v125, v125\n\t"
        "v_exp_f32 v126, v126\n\t"
        "v_smfmac_f32_16x16x64_bf16 v[140:143], v[144:147], v[112:119], v148\n\t"
        "buffer_load_dwordx4 v[200:203], v150, s[40:43], s44 offen offset:0 sc1\n\t"
        "v_exp_f32 v127, v127\n\t"
        "v_exp_f32 v96, v96\n\t"
        "v_exp_f32 v97, v97\n\t"
        "v_smfmac_f32_32x32x32_bf16 v[48:63], v[212:215], v[120:127], v148\n\t"
        "v_exp_f32 v98, v98\n\t"
        "v_exp_f32 v99, v99\n\t"
        "v_exp_f32 v100, v100\n\t"
        "v_smfmac_f32_16x16x64_bf16 v[140:143], v[144:147], v[120:127], v148\n\t"
        "v_exp_f32 v101, v101\n\t"
        "v_mfma_f32_32x32x16_bf16 v[112:127], v[152:155], v[72:75], 0\n\t"
        "v_exp_f32 v102, v102\n\t"
        "v_exp_f32 v103, v103\n\t"
        "v_exp_f32 v104, v104\n\t"
        "v_mfma_f32_32x32x16_bf16 v[112:127], v[156:159], v[76:79], v[112:127]\n\t"
        "v_exp_f32 v105, v105\n\t"
        "v_exp_f32 v106, v106\n\t"
        "v_exp_f32 v107, v107\n\t"
        "v_smfmac_f32_32x32x32_bf16 v[0:15], v[160:163], v[96:103], v148\n\t"
        "v_exp_f32 v108, v108\n\t"
        "v_exp_f32 v109, v109\n\t"
        "v_exp_f32 v110, v110\n\t"
        "v_smfmac_f32_16x16x64_bf16 v[128:131], v[144:147], v[96:103], v148\n\t"
        "buffer_load_dwordx4 v[204:207], v150, s[40:43], s44 offen offset:1024 sc1\n\t"
        "v_exp_f32 v111, v111\n\t"
        "v_exp_f32 v112, v112\n\t"
        "v_exp_f32 v113, v113\n\t"
        "v_smfmac_f32_32x32x32_bf16 v[0:15], v[164:167], v[104:111], v148\n\t"
        "v_exp_f32 v114, v114\n\t"
        "v_exp_f32 v115, v115\n\t"
        "v_exp_f32 v116, v116\n\t"
        "v_smfmac_f32_16x16x64_bf16 v[128:131], v[144:147], v[104:111], v148\n\t"
        "v_exp_f32 v117, v117\n\t"
        "v_mfma_f32_32x32x16_bf16 v[96:111], v[152:155], v[80:83], 0\n\t"
        "v_exp_f32 v118, v118\n\t"
        "v_exp_f32 v119, v119\n\t"
        "v_exp_f32 v120, v120\n\t"
        "v_mfma_f32_32x32x16_bf16 v[96:111], v[156:159], v[84:87], v[96:111]\n\t"
        "v_exp_f32 v121, v121\n\t"
        "v_exp_f32 v122, v122\n\t"
        "v_exp_f32 v123, v123\n\t"
        "v_smfmac_f32_32x32x32_bf16 v[16:31], v[160:163], v[112:119], v148\n\t"
        "v_exp_f32 v124, v124\n\t"
        "v_exp_f32 v125, v125\n\t"
        "v_exp_f32 v126, v126\n\t"
        "v_smfmac_f32_16x16x64_bf16 v[132:135], v[144:147], v[112:119], v148\n\t"
        "buffer_load_dwordx4 v[208:211], v150, s[40:43], s44 offen offset:2048 sc1\n\t"
        "v_exp_f32 v127, v127\n\t"
        "v_exp_f32 v96, v96\n\t"
        "v_exp_f32 v97, v97\n\t"
        "v_smfmac_f32_32x32x32_bf16 v[16:31], v[164:167], v[120:127], v148\n\t"
        "v_exp_f32 v98, v98\n\t"
        "v_exp_f32 v99, v99\n\t"
        "v_exp_f32 v100, v100\n\t"
        "v_smfmac_f32_16x16x64_bf16 v[132:135], v[144:147], v[120:127], v148\n\t"
        "v_exp_f32 v101, v101\n\t"
        "v_mfma_f32_32x32x16_bf16 v[112:127], v[152:155], v[88:91], 0\n\t"
        "v_exp_f32 v102, v102\n\t"
        "v_exp_f32 v103, v103\n\t"
        "v_exp_f32 v104, v104\n\t"
        "v_mfma_f32_32x32x16_bf16 v[112:127], v[156:159], v[92:95], v[112:127]\n\t"
        "v_exp_f32 v105, v105\n\t"
        "v_exp_f32 v106, v106\n\t"
        "v_exp_f32 v107, v107\n\t"
        "v_smfmac_f32_32x32x32_bf16 v[32:47], v[160:163], v[96:103], v148\n\t"
        "v_exp_f32 v108, v108\n\t"
        "v_exp_f32 v109, v109\n\t"
        "v_exp_f32 v110, v110\n\t"
        "v_smfmac_f32_16x16x64_bf16 v[136:139], v[144:147], v[96:103], v148\n\t"
        "buffer_load_dwordx4 v[212:215], v150, s[40:43], s44 offen offset:3072 sc1\n\t"
        "v_exp_f32 v111, v111\n\t"
        "v_exp_f32 v112, v112\n\t"
        "v_exp_f32 v113, v113\n\t"
        "v_smfmac_f32_32x32x32_bf16 v[32:47], v[164:167], v[104:111], v148\n\t"
        "s_add_u32 s44, s44, 0x4000\n\t"
        "s_and_b32 s44, s44, 0x3ffff\n\t"
        "v_exp_f32 v114, v114\n\t"
        "v_exp_f32 v115, v115\n\t"
        "v_exp_f32 v116, v116\n\t"
        "v_smfmac_f32_16x16x64_bf16 v[136:139], v[144:147], v[104:111], v148\n\t"
        "v_exp_f32 v117, v117\n\t"
        "s_waitcnt vmcnt(8)\n\t"
        "v_mfma_f32_32x32x16_bf16 v[96:111], v[168:171], v[64:67], 0\n\t"
        "v_exp_f32 v118, v118\n\t"
        "v_exp_f32 v119, v119\n\t"
        "v_exp_f32 v120, v120\n\t"
        "v_mfma_f32_32x32x16_bf16 v[96:111], v[172:175], v[68:71], v[96:111]\n\t"
        "v_exp_f32 v121, v121\n\t"
        "v_exp_f32 v122, v122\n\t"
        "v_exp_f32 v123, v123\n\t"
        "v_smfmac_f32_32x32x32_bf16 v[48:63], v[160:163], v[112:119], v148\n\t"
        "v_exp_f32 v124, v124\n\t"
        "v_exp_f32 v125, v125\n\t"
        "v_exp_f32 v126, v126\n\t"
        "v_smfmac_f32_16x16x64_bf16 v[140:143], v[144:147], v[112:119], v148\n\t"
        "buffer_load_dwordx4 v[152:155], v149, s[40:43], s44 offen offset:0 sc1\n\t"
        "v_exp_f32 v127, v127\n\t"
        "v_exp_f32 v96, v96\n\t"
        "v_exp_f32 v97, v97\n\t"
        "v_smfmac_f32_32x32x32_bf16 v[48:63], v[164:167], v[120:127], v148\n\t"
        "v_exp_f32 v98, v98\n\t"
        "v_exp_f32 v99, v99\n\t"
        "v_exp_f32 v100, v100\n\t"
        "v_smfmac_f32_16x16x64_bf16 v[140:143], v[144:147], v[120:127], v148\n\t"
        "v_exp_f32 v101, v101\n\t"
        "v_mfma_f32_32x32x16_bf16 v[112:127], v[168:171], v[72:75], 0\n\t"
        "v_exp_f32 v102, v102\n\t"
        "v_exp_f32 v103, v103\n\t"
        "v_exp_f32 v104, v104\n\t"
        "v_mfma_f32_32x32x16_bf16 v[112:127], v[172:175], v[76:79], v[112:127]\n\t"
        "v_exp_f32 v105, v105\n\t"
        "v_exp_f32 v106, v106\n\t"
        "v_exp_f32 v107, v107\n\t"
        "v_smfmac_f32_32x32x32_bf16 v[0:15], v[176:179], v[96:103], v148\n\t"
        "v_exp_f32 v108, v108\n\t"
        "v_exp_f32 v109, v109\n\t"
        "v_exp_f32 v110, v110\n\t"
        "v_smfmac_f32_16x16x64_bf16 v[128:131], v[144:147], v[96:103], v148\n\t"
        "buffer_load_dwordx4 v[156:159], v149, s[40:43], s44 offen offset:1024 sc1\n\t"
        "v_exp_f32 v111, v111\n\t"
        "v_exp_f32 v112, v112\n\t"
        "v_exp_f32 v113, v113\n\t"
        "v_smfmac_f32_32x32x32_bf16 v[0:15], v[180:183], v[104:111], v148\n\t"
        "v_exp_f32 v114, v114\n\t"
        "v_exp_f32 v115, v115\n\t"
        "v_exp_f32 v116, v116\n\t"
        "v_smfmac_f32_16x16x64_bf16 v[128:131], v[144:147], v[104:111], v148\n\t"
        "v_exp_f32 v117, v117\n\t"
        "v_mfma_f32_32x32x16_bf16 v[96:111], v[168:171], v[80:83], 0\n\t"
        "v_exp_f32 v118, v118\n\t"
        "v_exp_f32 v119, v119\n\t"
        "v_exp_f32 v120, v120\n\t"
        "v_mfma_f32_32x32x16_bf16 v[96:111], v[172:175], v[84:87], v[96:111]\n\t"
        "v_exp_f32 v121, v121\n\t"
        "v_exp_f32 v122, v122\n\t"
        "v_exp_f32 v123, v123\n\t"
        "v_smfmac_f32_32x32x32_bf16 v[16:31], v[176:179], v[112:119], v148\n\t"
        "v_exp_f32 v124, v124\n\t"
        "v_exp_f32 v125, v125\n\t"
        "v_exp_f32 v126, v126\n\t"
        "v_smfmac_f32_16x16x64_bf16 v[132:135], v[144:147], v[112:119], v148\n\t"
        "buffer_load_dwordx4 v[160:163], v149, s[40:43], s44 offen offset:2048 sc1\n\t"
        "v_exp_f32 v127, v127\n\t"
        "v_exp_f32 v96, v96\n\t"
        "v_exp_f32 v97, v97\n\t"
        "v_smfmac_f32_32x32x32_bf16 v[16:31], v[180:183], v[120:127], v148\n\t"
        "v_exp_f32 v98, v98\n\t"
        "v_exp_f32 v99, v99\n\t"
        "v_exp_f32 v100, v100\n\t"
        "v_smfmac_f32_16x16x64_bf16 v[132:135], v[144:147], v[120:127], v148\n\t"
        "v_exp_f32 v101, v101\n\t"
        "v_mfma_f32_32x32x16_bf16 v[112:127], v[168:171], v[88:91], 0\n\t"
        "v_exp_f32 v102, v102\n\t"
        "v_exp_f32 v103, v103\n\t"
        "v_exp_f32 v104, v104\n\t"
        "v_mfma_f32_32x32x16_bf16 v[112:127], v[172:175], v[92:95], v[112:127]\n\t"
        "v_exp_f32 v105, v105\n\t"
        "v_exp_f32 v106, v106\n\t"
        "v_exp_f32 v107, v107\n\t"
        "v_smfmac_f32_32x32x32_bf16 v[32:47], v[176:179], v[96:103], v148\n\t"
        "v_exp_f32 v108, v108\n\t"
        "v_exp_f32 v109, v109\n\t"
        "v_exp_f32 v110, v110\n\t"
        "v_smfmac_f32_16x16x64_bf16 v[136:139], v[144:147], v[96:103], v148\n\t"
        "buffer_load_dwordx4 v[164:167], v149, s[40:43], s44 offen offset:3072 sc1\n\t"
        "v_exp_f32 v111, v111\n\t"
        "s_sub_u32 s45, s45, 1\n\t"
        "s_cmp_lg_u32 s45, 0\n\t"
        "s_cbranch_scc1 1b\n\t"
        "v_exp_f32 v112, v112\n\t"
        "v_exp_f32 v113, v113\n\t"
        "v_smfmac_f32_32x32x32_bf16 v[32:47], v[180:183], v[104:111], v148\n\t"
        "v_exp_f32 v114, v114\n\t"
        "v_exp_f32 v115, v115\n\t"
        "v_exp_f32 v116, v116\n\t"
        "v_smfmac_f32_16x16x64_bf16 v[136:139], v[144:147], v[104:111], v148\n\t"
        "v_exp_f32 v117, v117\n\t"
        "s_waitcnt vmcnt(8)\n\t"
        "v_mfma_f32_32x32x16_bf16 v[96:111], v[184:187], v[64:67], 0\n\t"
        "v_exp_f32 v118, v118\n\t"
        "v_exp_f32 v119, v119\n\t"
        "v_exp_f32 v120, v120\n\t"
        "v_mfma_f32_32x32x16_bf16 v[96:111], v[188:191], v[68:71], v[96:111]\n\t"
        "v_exp_f32 v121, v121\n\t"
        "v_exp_f32 v122, v122\n\t"
        "v_exp_f32 v123, v123\n\t"
        "v_smfmac_f32_32x32x32_bf16 v[48:63], v[176:179], v[112:119], v148\n\t"
        "v_exp_f32 v124, v124\n\t"
        "v_exp_f32 v125, v125\n\t"
        "v_exp_f32 v126, v126\n\t"
        "v_smfmac_f32_16x16x64_bf16 v[140:143], v[144:147], v[112:119], v148\n\t"
        "v_exp_f32 v127, v127\n\t"
        "v_exp_f32 v96, v96\n\t"
        "v_exp_f32 v97, v97\n\t"
        "v_smfmac_f32_32x32x32_bf16 v[48:63], v[180:183], v[120:127], v148\n\t"
        "v_exp_f32 v98, v98\n\t"
        "v_exp_f32 v99, v99\n\t"
        "v_exp_f32 v100, v100\n\t"
        "v_smfmac_f32_16x16x64_bf16 v[140:143], v[144:147], v[120:127], v148\n\t"
        "v_exp_f32 v101, v101\n\t"
        "v_mfma_f32_32x32x16_bf16 v[112:127], v[184:187], v[72:75], 0\n\t"
        "v_exp_f32 v102, v102\n\t"
        "v_exp_f32 v103, v103\n\t"
        "v_exp_f32 v104, v104\n\t"
        "v_mfma_f32_32x32x16_bf16 v[112:127], v[188:191], v[76:79], v[112:127]\n\t"
        "v_exp_f32 v105, v105\n\t"
        "v_exp_f32 v106, v106\n\t"
        "v_exp_f32 v107, v107\n\t"
        "v_smfmac_f32_32x32x32_bf16 v[0:15], v[192:195], v[96:103], v148\n\t"
        "v_exp_f32 v108, v108\n\t"
        "v_exp_f32 v109, v109\n\t"
        "v_exp_f32 v110, v110\n\t"
        "v_smfmac_f32_16x16x64_bf16 v[128:131], v[144:147], v[96:103], v148\n\t"
        "v_exp_f32 v111, v111\n\t"
        "v_exp_f32 v112, v112\n\t"
        "v_exp_f32 v113, v113\n\t"
        "v_smfmac_f32_32x32x32_bf16 v[0:15], v[196:199], v[104:111], v148\n\t"
        "v_exp_f32 v114, v114\n\t"
        "v_exp_f32 v115, v115\n\t"
        "v_exp_f32 v116, v116\n\t"
        "v_smfmac_f32_16x16x64_bf16 v[128:131], v[144:147], v[104:111], v148\n\t"
        "v_exp_f32 v117, v117\n\t"
        "s_waitcnt vmcnt(4)\n\t"
        "v_mfma_f32_32x32x16_bf16 v[96:111], v[200:203], v[64:67], 0\n\t"
        "v_exp_f32 v118, v118\n\t"
        "v_exp_f32 v119, v119\n\t"
        "v_exp_f32 v120, v120\n\t"
        "v_mfma_f32_32x32x16_bf16 v[96:111], v[204:207], v[68:71], v[96:111]\n\t"
        "v_exp_f32 v121, v121\n\t"
        "v_exp_f32 v122, v122\n\t"
        "v_exp_f32 v123, v123\n\t"
        "v_smfmac_f32_32x32x32_bf16 v[16:31], v[192:195], v[112:119], v148\n\t"
        "v_exp_f32 v124, v124\n\t"
        "v_exp_f32 v125, v125\n\t"
        "v_exp_f32 v126, v126\n\t"
        "v_smfmac_f32_16x16x64_bf16 v[132:135], v[144:147], v[112:119], v148\n\t"
        "v_exp_f32 v127, v127\n\t"
        "v_exp_f32 v96, v96\n\t"
        "v_exp_f32 v97, v97\n\t"
        "v_smfmac_f32_32x32x32_bf16 v[16:31], v[196:199], v[120:127], v148\n\t"
        "v_exp_f32 v98, v98\n\t"
        "v_exp_f32 v99, v99\n\t"
        "v_exp_f32 v100, v100\n\t"
        "v_smfmac_f32_16x16x64_bf16 v[132:135], v[144:147], v[120:127], v148\n\t"
        "v_exp_f32 v101, v101\n\t"
        "v_mfma_f32_32x32x16_bf16 v[112:127], v[200:203], v[72:75], 0\n\t"
        "v_exp_f32 v102, v102\n\t"
        "v_exp_f32 v103, v103\n\t"
        "v_exp_f32 v104, v104\n\t"
        "v_mfma_f32_32x32x16_bf16 v[112:127], v[204:207], v[76:79], v[112:127]\n\t"
        "v_exp_f32 v105, v105\n\t"
        "v_exp_f32 v106, v106\n\t"
        "v_exp_f32 v107, v107\n\t"
        "v_smfmac_f32_32x32x32_bf16 v[0:15], v[208:211], v[96:103], v148\n\t"
        "v_exp_f32 v108, v108\n\t"
        "v_exp_f32 v109, v109\n\t"
        "v_exp_f32 v110, v110\n\t"
        "v_smfmac_f32_16x16x64_bf16 v[128:131], v[144:147], v[96:103], v148\n\t"
        "v_exp_f32 v111, v111\n\t"
        "v_exp_f32 v112, v112\n\t"
        "v_exp_f32 v113, v113\n\t"
        "v_smfmac_f32_32x32x32_bf16 v[0:15], v[212:215], v[104:111], v148\n\t"
        "v_exp_f32 v114, v114\n\t"
        "v_exp_f32 v115, v115\n\t"
        "v_exp_f32 v116, v116\n\t"
        "v_smfmac_f32_16x16x64_bf16 v[128:131], v[144:147], v[104:111], v148\n\t"
        "v_exp_f32 v117, v117\n\t"
        "v_mfma_f32_32x32x16_bf16 v[96:111], v[184:187], v[80:83], 0\n\t"
        "v_exp_f32 v118, v118\n\t"
        "v_exp_f32 v119, v119\n\t"
        "v_exp_f32 v120, v120\n\t"
        "ds_write_b128 v216, v[0:3] offset:0\n\t"
        "v_mfma_f32_32x32x16_bf16 v[96:111], v[188:191], v[84:87], v[96:111]\n\t"
        "v_exp_f32 v121, v121\n\t"
        "ds_write_b128 v216, v[4:7] offset:1024\n\t"
        "v_exp_f32 v122, v122\n\t"
        "ds_write_b128 v216, v[8:11] offset:2048\n\t"
        "v_exp_f32 v123, v123\n\t"
        "ds_write_b128 v216, v[12:15] offset:3072\n\t"
        "v_smfmac_f32_32x32x32_bf16 v[16:31], v[208:211], v[112:119], v148\n\t"
        "v_exp_f32 v124, v124\n\t"
        "ds_write_b32 v218, v128 offset:0\n\t"
        "v_exp_f32 v125, v125\n\t"
        "v_exp_f32 v126, v126\n\t"
        "v_smfmac_f32_16x16x64_bf16 v[132:135], v[144:147], v[112:119], v148\n\t"
        "v_exp_f32 v127, v127\n\t"
        "v_exp_f32 v96, v96\n\t"
        "v_exp_f32 v97, v97\n\t"
        "v_smfmac_f32_32x32x32_bf16 v[16:31], v[212:215], v[120:127], v148\n\t"
        "v_exp_f32 v98, v98\n\t"
        "v_exp_f32 v99, v99\n\t"
        "v_exp_f32 v100, v100\n\t"
        "v_smfmac_f32_16x16x64_bf16 v[132:135], v[144:147], v[120:127], v148\n\t"
        "v_exp_f32 v101, v101\n\t"
        "v_mfma_f32_32x32x16_bf16 v[112:127], v[184:187], v[88:91], 0\n\t"
        "v_exp_f32 v102, v102\n\t"
        "v_exp_f32 v103, v103\n\t"
        "v_exp_f32 v104, v104\n\t"
        "ds_write_b128 v216, v[16:19] offset:4096\n\t"
        "v_mfma_f32_32x32x16_bf16 v[112:127], v[188:191], v[92:95], v[112:127]\n\t"
        "v_exp_f32 v105, v105\n\t"
        "ds_write_b128 v216, v[20:23] offset:5120\n\t"
        "v_exp_f32 v106, v106\n\t"
        "ds_write_b128 v216, v[24:27] offset:6144\n\t"
        "v_exp_f32 v107, v107\n\t"
        "ds_write_b128 v216, v[28:31] offset:7168\n\t"
        "v_smfmac_f32_32x32x32_bf16 v[32:47], v[192:195], v[96:103], v148\n\t"
        "v_exp_f32 v108, v108\n\t"
        "ds_write_b32 v218, v132 offset:256\n\t"
        "v_exp_f32 v109, v109\n\t"
        "ds_write_b32 v220, v222\n\t"
        "v_exp_f32 v110, v110\n\t"
        "v_smfmac_f32_16x16x64_bf16 v[136:139], v[144:147], v[96:103], v148\n\t"
        "v_exp_f32 v111, v111\n\t"
        "v_exp_f32 v112, v112\n\t"
        "v_exp_f32 v113, v113\n\t"
        "v_smfmac_f32_32x32x32_bf16 v[32:47], v[196:199], v[104:111], v148\n\t"
        "v_exp_f32 v114, v114\n\t"
        "v_exp_f32 v115, v115\n\t"
        "v_exp_f32 v116, v116\n\t"
        "v_smfmac_f32_16x16x64_bf16 v[136:139], v[144:147], v[104:111], v148\n\t"
        "v_exp_f32 v117, v117\n\t"
        "v_mfma_f32_32x32x16_bf16 v[96:111], v[200:203], v[80:83], 0\n\t"
        "v_exp_f32 v118, v118\n\t"
        "v_exp_f32 v119, v119\n\t"
        "v_exp_f32 v120, v120\n\t"
        "v_mfma_f32_32x32x16_bf16 v[96:111], v[204:207], v[84:87], v[96:111]\n\t"
        "v_exp_f32 v121, v121\n\t"
        "v_exp_f32 v122, v122\n\t"
        "v_exp_f32 v123, v123\n\t"
        "v_smfmac_f32_32x32x32_bf16 v[48:63], v[192:195], v[112:119], v148\n\t"
        "v_exp_f32 v124, v124\n\t"
        "v_exp_f32 v125, v125\n\t"
        "v_exp_f32 v126, v126\n\t"
        "v_smfmac_f32_16x16x64_bf16 v[140:143], v[144:147], v[112:119], v148\n\t"
        "v_exp_f32 v127, v127\n\t"
        "v_exp_f32 v96, v96\n\t"
        "v_exp_f32 v97, v97\n\t"
        "v_smfmac_f32_32x32x32_bf16 v[48:63], v[196:199], v[120:127], v148\n\t"
        "v_exp_f32 v98, v98\n\t"
        "v_exp_f32 v99, v99\n\t"
        "v_exp_f32 v100, v100\n\t"
        "v_smfmac_f32_16x16x64_bf16 v[140:143], v[144:147], v[120:127], v148\n\t"
        "v_exp_f32 v101, v101\n\t"
        "v_mfma_f32_32x32x16_bf16 v[112:127], v[200:203], v[88:91], 0\n\t"
        "v_exp_f32 v102, v102\n\t"
        "v_exp_f32 v103, v103\n\t"
        "v_exp_f32 v104, v104\n\t"
        "ds_read_b32 v151, v221\n\t"
        "v_mfma_f32_32x32x16_bf16 v[112:127], v[204:207], v[92:95], v[112:127]\n\t"
        "v_exp_f32 v105, v105\n\t"
        "v_exp_f32 v106, v106\n\t"
        "v_exp_f32 v107, v107\n\t"
        "v_smfmac_f32_32x32x32_bf16 v[32:47], v[208:211], v[96:103], v148\n\t"
        "v_exp_f32 v108, v108\n\t"
        "v_exp_f32 v109, v109\n\t"
        "v_exp_f32 v110, v110\n\t"
        "v_smfmac_f32_16x16x64_bf16 v[136:139], v[144:147], v[96:103], v148\n\t"
        "v_exp_f32 v111, v111\n\t"
        "s_waitcnt lgkmcnt(0)\n\t"
        "v_cmp_ne_u32 vcc, v222, v151\n\t"
        "s_cbranch_vccnz 40f\n\t"
        "41:\n\t"
        "v_exp_f32 v112, v112\n\t"
        "ds_read_b128 v[0:3], v217 offset:0\n\t"
        "v_exp_f32 v113, v113\n\t"
        "ds_read_b128 v[4:7], v217 offset:1024\n\t"
        "v_smfmac_f32_32x32x32_bf16 v[32:47], v[212:215], v[104:111], v148\n\t"
        "v_exp_f32 v114, v114\n\t"
        "ds_read_b128 v[8:11], v217 offset:2048\n\t"
        "v_exp_f32 v115, v115\n\t"
        "ds_read_b128 v[12:15], v217 offset:3072\n\t"
        "v_exp_f32 v116, v116\n\t"
        "ds_read_b32 v128, v219 offset:0\n\t"
        "v_smfmac_f32_16x16x64_bf16 v[136:139], v[144:147], v[104:111], v148\n\t"
        "v_exp_f32 v117, v117\n\t"
        "ds_read_b128 v[16:19], v217 offset:4096\n\t"
        "v_exp_f32 v118, v118\n\t"
        "ds_read_b128 v[20:23], v217 offset:5120\n\t"
        "v_exp_f32 v119, v119\n\t"
        "ds_read_b128 v[24:27], v217 offset:6144\n\t"
        "v_exp_f32 v120, v120\n\t"
        "ds_read_b128 v[28:31], v217 offset:7168\n\t"
        "v_exp_f32 v121, v121\n\t"
        "ds_read_b32 v132, v219 offset:256\n\t"
        "v_exp_f32 v122, v122\n\t"
        "v_exp_f32 v123, v123\n\t"
        "v_smfmac_f32_32x32x32_bf16 v[48:63], v[208:211], v[112:119], v148\n\t"
        "v_exp_f32 v124, v124\n\t"
        "v_exp_f32 v125, v125\n\t"
        "v_exp_f32 v126, v126\n\t"
        "v_smfmac_f32_16x16x64_bf16 v[140:143], v[144:147], v[112:119], v148\n\t"
        "v_exp_f32 v127, v127\n\t"
        "s_nop 1\n\t"
        "v_smfmac_f32_32x32x32_bf16 v[48:63], v[212:215], v[120:127], v148\n\t"
        "v_smfmac_f32_16x16x64_bf16 v[140:143], v[144:147], v[120:127], v148\n\t"
        "s_waitcnt vmcnt(0) lgkmcnt(0)\n\t"
        "s_nop 6\n\t"
        "s_branch 9f\n\t"
        "30:\n\t"
        "ds_write_b32 v187, v188\n\t"
        "s_add_u32 s44, s44, 0x4000\n\t"
        "s_and_b32 s44, s44, 0x3ffff\n\t"
        "buffer_load_dwordx4 v[184:187], v149, s[40:43], s44 offen offset:0 sc1\n\t"
        "buffer_load_dwordx4 v[188:191], v149, s[40:43], s44 offen offset:1024 sc1\n\t"
        "buffer_load_dwordx4 v[192:195], v149, s[40:43], s44 offen offset:2048 sc1\n\t"
        "buffer_load_dwordx4 v[196:199], v149, s[40:43], s44 offen offset:3072 sc1\n\t"
        "buffer_load_dwordx4 v[200:203], v150, s[40:43], s44 offen offset:0 sc1\n\t"
        "buffer_load_dwordx4 v[204:207], v150, s[40:43], s44 offen offset:1024 sc1\n\t"
        "buffer_load_dwordx4 v[208:211], v150, s[40:43], s44 offen offset:2048 sc1\n\t"
        "buffer_load_dwordx4 v[212:215], v150, s[40:43], s44 offen offset:3072 sc1\n\t"
        "s_add_u32 s44, s44, 0x4000\n\t"
        "s_and_b32 s44, s44, 0x3ffff\n\t"
        "buffer_load_dwordx4 v[152:155], v149, s[40:43], s44 offen offset:0 sc1\n\t"
        "buffer_load_dwordx4 v[156:159], v149, s[40:43], s44 offen offset:1024 sc1\n\t"
        "buffer_load_dwordx4 v[160:163], v149, s[40:43], s44 offen offset:2048 sc1\n\t"
        "buffer_load_dwordx4 v[164:167], v149, s[40:43], s44 offen offset:3072 sc1\n\t"
        "s_mov_b32 s46, 1\n\t"
        "s_branch 31b\n\t"
        "32:\n\t"
        "ds_write_b32 v187, v188\n\t"
        "s_add_u32 s44, s44, 0x4000\n\t"
        "s_and_b32 s44, s44, 0x3ffff\n\t"
        "buffer_load_dwordx4 v[184:187], v149, s[40:43], s44 offen offset:0 sc1\n\t"
        "buffer_load_dwordx4 v[188:191], v149, s[40:43], s44 offen offset:1024 sc1\n\t"
        "buffer_load_dwordx4 v[192:195], v149, s[40:43], s44 offen offset:2048 sc1\n\t"
        "buffer_load_dwordx4 v[196:199], v149, s[40:43], s44 offen offset:3072 sc1\n\t"
        "buffer_load_dwordx4 v[200:203], v150, s[40:43], s44 offen offset:0 sc1\n\t"
        "buffer_load_dwordx4 v[204:207], v150, s[40:43], s44 offen offset:1024 sc1\n\t"
        "buffer_load_dwordx4 v[208:211], v150, s[40:43], s44 offen offset:2048 sc1\n\t"
        "buffer_load_dwordx4 v[212:215], v150, s[40:43], s44 offen offset:3072 sc1\n\t"
        "s_add_u32 s44, s44, 0x4000\n\t"
        "s_and_b32 s44, s44, 0x3ffff\n\t"
        "buffer_load_dwordx4 v[152:155], v149, s[40:43], s44 offen offset:0 sc1\n\t"
        "buffer_load_dwordx4 v[156:159], v149, s[40:43], s44 offen offset:1024 sc1\n\t"
        "buffer_load_dwordx4 v[160:163], v149, s[40:43], s44 offen offset:2048 sc1\n\t"
        "buffer_load_dwordx4 v[164:167], v149, s[40:43], s44 offen offset:3072 sc1\n\t"
        "s_mov_b32 s46, 1\n\t"
        "s_branch 33b\n\t"
        "40:\n\t"
        "s_mov_b32 s45, 0x100000\n\t"
        "42:\n\t"
        "ds_read_b32 v151, v221\n\t"
        "s_sub_u32 s45, s45, 1\n\t"
        "s_waitcnt lgkmcnt(0)\n\t"
        "s_cbranch_scc1 41b\n\t"
        "v_cmp_ne_u32 vcc, v222, v151\n\t"
        "s_cbranch_vccnz 42b\n\t"
        "s_branch 41b\n\t"
        "9:\n\t"
        : "={v[0:15]}"(o0), "={v[16:31]}"(o1), "={v[32:47]}"(o2), "={v[48:63]}"(o3), "={v[128:143]}"(lv), "+{s44}"(soff), "+{v[152:167]}"(kva), "+{v[168:183]}"(kvb), "+{v[184:191]}"(fv) STAMP_OUTS
        : "{v[64:71]}"(q[0]), "{v[72:79]}"(q[1]), "{v[80:87]}"(q[2]), "{v[88:95]}"(q[3]), "{v[144:147]}"(ones), "{v148}"(0xddddu), "{v149}"(kvoff), "{v150}"(kvoff + 4096),
          "{v[216:223]}"(cv), "{s[40:43]}"(srd)
        : "v96", "v97", "v98", "v99", "v100", "v101", "v102", "v103", "v104", "v105", "v106", "v107", "v108", "v109", "v110", "v111", "v112", "v113", "v114", "v115", "v116", "v117", "v118", "v119", "v120", "v121", "v122", "v123", "v124", "v125", "v126", "v127", "v151", "v192", "v193", "v194", "v195", "v196", "v197", "v198", "v199", "v200", "v201", "v202", "v203", "v204", "v205", "v206", "v207", "v208", "v209", "v210", "v211", "v212", "v213", "v214", "v215", "s45", "s46", "vcc", "scc", "memory");
      STAMP_PUT(6 + 16 * L);
      STAMPV(9 + 16 * L);
      PHASE_VARS;
      const unsigned dst = OFF_OF + L * (5 * MBY) + (tm * 4 + 2 * kh) * TS + (tn * 2 + hh) * 2048 + lane * 16;
      auto put = [&](int u, const f32x16& o) {
#pragma unroll
        for (int c = 0; c < 2; ++c) {
          u32x4 w;
#pragma unroll
          for (int e = 0; e < 4; ++e) w[e] = pack2(o[8 * c + 2 * e], o[8 * c + 2 * e + 1]);
          stg(rs, dst + u * TS + c * 1024, w);
        }
      };
      const float la = lv[8] + lv[0], lb = lv[12] + lv[4];
      o2 = (o2 + o0) * RCP(la); put(0, o2);
      SBAR();
      o3 = (o3 + o1) * RCP(lb); put(1, o3);
      SBAR();
      {
        const f32x16 t = o2 + o3;
        float sum = 0.f;
#pragma unroll
        for (int e = 0; e < 16; e += 4) sum += (t[e] + t[e + 1]) + (t[e + 2] + t[e + 3]);
        if (__any(!(fabsf(sum) < 1e37f) | !(fmaxf(la, lb) < 1e27f) | !(fminf(la, lb) > 1e-9f) | !(la + lb == la + lb))) {
          DRAIN();
          attn_safe(rs, kvoff - kh * 8192, q[2], ones, o2); put(0, o2);
          attn_safe(rs, kvoff - kh * 8192, q[3], ones, o3); put(1, o3);
        }
      }
    }
    STAMPV(10 + 16 * L);
    {
      f32x16 acc[2];
      u32x4 res[4];
      u32x2 wrc[8];
      STAMP_DECL;
      { PHASE_VARS;
      const unsigned fl = FL_O + (L * 16 + tm) * 8192 + kh * 4096;
      DRAIN();
      STAMPV(11 + 16 * L);
      __builtin_amdgcn_raw_buffer_store_b32(TAG, rs, lane == 0 ? fl + (tn * 2 + hh) * 128 : OOB, 0, 16);
      BARRIER();
#pragma unroll
      for (int g = 0; g < 8; ++g) wrc[g] = *reinterpret_cast<const u32x2*>(smem + L_WR + g * 16 + hi * 8);
      if (L == 0) {
#pragma unroll
        for (int i = 0; i < 4; ++i) res[i] = fo_swap(x1p[i]);
      } else {
        const bool routed = (int)words[3] == tile * 32 + r32;
        const float mb1 = reinterpret_cast<const float*>(smem + L_AB)[256 + wid * 64 + r32], rd1 = reinterpret_cast<const float*>(smem + L_AB)[256 + wid * 64 + 32 + r32];
#pragma unroll
        for (int i = 0; i < 4; ++i) {
          const u32x4 pr = *reinterpret_cast<const u32x4*>(smem + L_X2P + (i * 2 + hi) * 16);
#pragma unroll
          for (int e = 0; e < 4; ++e) {
            const unsigned h2 = ln_pair(x1p[i][e], mb1, rd1);
            unsigned t = pack2(lo16(x1p[i][e]) + lo16(h2), hi16(x1p[i][e]) + hi16(h2));
            PIN(t);
            res[i][e] = routed ? pr[e] : t;
          }
        }
      }
#pragma unroll
      for (int g = 0; g < 8; ++g) PIN(wrc[g]);
#pragma unroll
      for (int i = 0; i < 4; ++i) PIN(res[i]);
      unsigned soff = tn * 4096;
      const unsigned a0 = lbase + wrow * 128 + ((wch ^ wsw) << 4) + 8 * (tp & 1), a1 = lbase + wrow * 128 + (((4 + wch) ^ wsw) << 4) + 8 * (tp & 1);
      asm volatile(
        "ds_read_b64_tr_b16 v[32:33], v193 offset:0\n\t"
        "ds_read_b64_tr_b16 v[34:35], v193 offset:1024\n\t"
        "ds_read_b64_tr_b16 v[36:37], v194 offset:0\n\t"
        "ds_read_b64_tr_b16 v[38:39], v194 offset:1024\n\t"
        "ds_read_b64_tr_b16 v[40:41], v193 offset:2048\n\t"
        "ds_read_b64_tr_b16 v[42:43], v193 offset:3072\n\t"
        "ds_read_b64_tr_b16 v[44:45], v194 offset:2048\n\t"
        "ds_read_b64_tr_b16 v[46:47], v194 offset:3072\n\t"
        "ds_read_b64_tr_b16 v[48:49], v193 offset:4096\n\t"
        "ds_read_b64_tr_b16 v[50:51], v193 offset:5120\n\t"
        "ds_read_b64_tr_b16 v[52:53], v194 offset:4096\n\t"
        "ds_read_b64_tr_b16 v[54:55], v194 offset:5120\n\t"
        "ds_read_b64_tr_b16 v[56:57], v193 offset:6144\n\t"
        "ds_read_b64_tr_b16 v[58:59], v193 offset:7168\n\t"
        "ds_read_b64_tr_b16 v[60:61], v194 offset:6144\n\t"
        "ds_read_b64_tr_b16 v[62:63], v194 offset:7168\n\t"
        "s_mov_b32 s45, 0x8000\n\t"
        "2:\n\t"
        "buffer_load_dword v200, v198, s[40:43], 0 offen sc1\n\t"
        "s_sub_u32 s45, s45, 1\n\t"
        "s_waitcnt vmcnt(0)\n\t"
        "s_cbranch_scc1 3f\n\t"
        "v_cmp_ne_u32 vcc, v199, v200\n\t"
        "s_cbranch_vccz 3f\n\t"
        "s_sleep 1\n\t"
        "s_branch 2b\n\t"
        "3:\n\t"
        "buffer_load_dwordx4 v[64:67], v192, s[40:43], s44 offen offset:0 sc1\n\t"
        "buffer_load_dwordx4 v[68:71], v192, s[40:43], s44 offen offset:1024 sc1\n\t"
        "buffer_load_dwordx4 v[72:75], v192, s[40:43], s44 offen offset:2048 sc1\n\t"
        "buffer_load_dwordx4 v[76:79], v192, s[40:43], s44 offen offset:3072 sc1\n\t"
        "s_add_u32 s44, s44, 0x1000\n\t"
        "s_and_b32 s44, s44, 0xffff\n\t"
        "buffer_load_dwordx4 v[80:83], v192, s[40:43], s44 offen offset:0 sc1\n\t"
        "buffer_load_dwordx4 v[84:87], v192, s[40:43], s44 offen offset:1024 sc1\n\t"
        "buffer_load_dwordx4 v[88:91], v192, s[40:43], s44 offen offset:2048 sc1\n\t"
        "buffer_load_dwordx4 v[92:95], v192, s[40:43], s44 offen offset:3072 sc1\n\t"
        "s_add_u32 s44, s44, 0x1000\n\t"
        "s_and_b32 s44, s44, 0xffff\n\t"
        "buffer_load_dwordx4 v[96:99], v192, s[40:43], s44 offen offset:0 sc1\n\t"
        "buffer_load_dwordx4 v[100:103], v192, s[40:43], s44 offen offset:1024 sc1\n\t"
        "buffer_load_dwordx4 v[104:107], v192, s[40:43], s44 offen offset:2048 sc1\n\t"
        "buffer_load_dwordx4 v[108:111], v192, s[40:43], s44 offen offset:3072 sc1\n\t"
        "s_waitcnt vmcnt(11) lgkmcnt(14)\n\t"
        "v_mfma_f32_32x32x16_bf16 v[0:15], v[32:35], v[64:67], 0\n\t"
        "ds_read_b64_tr_b16 v[32:33], v193 offset:8192\n\t"
        "ds_read_b64_tr_b16 v[34:35], v193 offset:9216\n\t"
        "s_waitcnt lgkmcnt(14)\n\t"
        "v_mfma_f32_32x32x16_bf16 v[16:31], v[36:39], v[64:67], 0\n\t"
        "ds_read_b64_tr_b16 v[36:37], v194 offset:8192\n\t"
        "ds_read_b64_tr_b16 v[38:39], v194 offset:9216\n\t"
        "s_add_u32 s44, s44, 0x1000\n\t"
        "s_and_b32 s44, s44, 0xffff\n\t"
        "buffer_load_dwordx4 v[112:115], v192, s[40:43], s44 offen offset:0 sc1\n\t"
        "s_waitcnt vmcnt(11) lgkmcnt(14)\n\t"
        "v_mfma_f32_32x32x16_bf16 v[0:15], v[40:43], v[68:71], v[0:15]\n\t"
        "ds_read_b64_tr_b16 v[40:41], v193 offset:10240\n\t"
        "ds_read_b64_tr_b16 v[42:43], v193 offset:11264\n\t"
        "s_waitcnt lgkmcnt(14)\n\t"
        "v_mfma_f32_32x32x16_bf16 v[16:31], v[44:47], v[68:71], v[16:31]\n\t"
        "ds_read_b64_tr_b16 v[44:45], v194 offset:10240\n\t"
        "ds_read_b64_tr_b16 v[46:47], v194 offset:11264\n\t"
        "buffer_load_dwordx4 v[116:119], v192, s[40:43], s44 offen offset:1024 sc1\n\t"
        "s_waitcnt vmcnt(11) lgkmcnt(14)\n\t"
        "v_mfma_f32_32x32x16_bf16 v[0:15], v[48:51], v[72:75], v[0:15]\n\t"
        "ds_read_b64_tr_b16 v[48:49], v193 offset:12288\n\t"
        "ds_read_b64_tr_b16 v[50:51], v193 offset:13312\n\t"
        "s_waitcnt lgkmcnt(14)\n\t"
        "v_mfma_f32_32x32x16_bf16 v[16:31], v[52:55], v[72:75], v[16:31]\n\t"
        "ds_read_b64_tr_b16 v[52:53], v194 offset:12288\n\t"
        "ds_read_b64_tr_b16 v[54:55], v194 offset:13312\n\t"
        "buffer_load_dwordx4 v[120:123], v192, s[40:43], s44 offen offset:2048 sc1\n\t"
        "s_waitcnt vmcnt(11) lgkmcnt(14)\n\t"
        "v_mfma_f32_32x32x16_bf16 v[0:15], v[56:59], v[76:79], v[0:15]\n\t"
        "ds_read_b64_tr_b16 v[56:57], v193 offset:14336\n\t"
        "ds_read_b64_tr_b16 v[58:59], v193 offset:15360\n\t"
        "s_waitcnt lgkmcnt(14)\n\t"
        "v_mfma_f32_32x32x16_bf16 v[16:31], v[60:63], v[76:79], v[16:31]\n\t"
        "ds_read_b64_tr_b16 v[60:61], v194 offset:14336\n\t"
        "ds_read_b64_tr_b16 v[62:63], v194 offset:15360\n\t"
        "buffer_load_dwordx4 v[124:127], v192, s[40:43], s44 offen offset:3072 sc1\n\t"
        "s_waitcnt vmcnt(11) lgkmcnt(14)\n\t"
        "v_mfma_f32_32x32x16_bf16 v[0:15], v[32:35], v[80:83], v[0:15]\n\t"
        "ds_read_b64_tr_b16 v[32:33], v193 offset:16384\n\t"
        "ds_read_b64_tr_b16 v[34:35], v193 offset:17408\n\t"
        "s_waitcnt lgkmcnt(14)\n\t"
        "v_mfma_f32_32x32x16_bf16 v[16:31], v[36:39], v[80:83], v[16:31]\n\t"
        "ds_read_b64_tr_b16 v[36:37], v194 offset:16384\n\t"
        "ds_read_b64_tr_b16 v[38:39], v194 offset:17408\n\t"
        "s_add_u32 s44, s44, 0x1000\n\t"
        "s_and_b32 s44, s44, 0xffff\n\t"
        "buffer_load_dwordx4 v[128:131], v192, s[40:43], s44 offen offset:0 sc1\n\t"
        "s_waitcnt vmcnt(11) lgkmcnt(14)\n\t"
        "v_mfma_f32_32x32x16_bf16 v[0:15], v[40:43], v[84:87], v[0:15]\n\t"
        "ds_read_b64_tr_b16 v[40:41], v193 offset:18432\n\t"
        "ds_read_b64_tr_b16 v[42:43], v193 offset:19456\n\t"
        "s_waitcnt lgkmcnt(14)\n\t"
        "v_mfma_f32_32x32x16_bf16 v[16:31], v[44:47], v[84:87], v[16:31]\n\t"
        "ds_read_b64_tr_b16 v[44:45], v194 offset:18432\n\t"
        "ds_read_b64_tr_b16 v[46:47], v194 offset:19456\n\t"
        "buffer_load_dwordx4 v[132:135], v192, s[40:43], s44 offen offset:1024 sc1\n\t"
        "s_waitcnt vmcnt(11) lgkmcnt(14)\n\t"
        "v_mfma_f32_32x32x16_bf16 v[0:15], v[48:51], v[88:91], v[0:15]\n\t"
        "ds_read_b64_tr_b16 v[48:49], v193 offset:20480\n\t"
        "ds_read_b64_tr_b16 v[50:51], v193 offset:21504\n\t"
        "s_waitcnt lgkmcnt(14)\n\t"
        "v_mfma_f32_32x32x16_bf16 v[16:31], v[52:55], v[88:91], v[16:31]\n\t"
        "ds_read_b64_tr_b16 v[52:53], v194 offset:20480\n\t"
        "ds_read_b64_tr_b16 v[54:55], v194 offset:21504\n\t"
        "buffer_load_dwordx4 v[136:139], v192, s[40:43], s44 offen offset:2048 sc1\n\t"
        "s_waitcnt vmcnt(11) lgkmcnt(14)\n\t"
        "v_mfma_f32_32x32x16_bf16 v[0:15], v[56:59], v[92:95], v[0:15]\n\t"
        "ds_read_b64_tr_b16 v[56:57], v193 offset:22528\n\t"
        "ds_read_b64_tr_b16 v[58:59], v193 offset:23552\n\t"
        "s_waitcnt lgkmcnt(14)\n\t"
        "v_mfma_f32_32x32x16_bf16 v[16:31], v[60:63], v[92:95], v[16:31]\n\t"
        "ds_read_b64_tr_b16 v[60:61], v194 offset:22528\n\t"
        "ds_read_b64_tr_b16 v[62:63], v194 offset:23552\n\t"
        "buffer_load_dwordx4 v[140:143], v192, s[40:43], s44 offen offset:3072 sc1\n\t"
        "s_waitcnt vmcnt(11) lgkmcnt(14)\n\t"
        "v_mfma_f32_32x32x16_bf16 v[0:15], v[32:35], v[96:99], v[0:15]\n\t"
        "ds_read_b64_tr_b16 v[32:33], v193 offset:24576\n\t"
        "ds_read_b64_tr_b16 v[34:35], v193 offset:25600\n\t"
        "s_waitcnt lgkmcnt(14)\n\t"
        "v_mfma_f32_32x32x16_bf16 v[16:31], v[36:39], v[96:99], v[16:31]\n\t"
        "ds_read_b64_tr_b16 v[36:37], v194 offset:24576\n\t"
        "ds_read_b64_tr_b16 v[38:39], v194 offset:25600\n\t"
        "s_add_u32 s44, s44, 0x1000\n\t"
        "s_and_b32 s44, s44, 0xffff\n\t"
        "buffer_load_dwordx4 v[144:147], v192, s[40:43], s44 offen offset:0 sc1\n\t"
        "s_waitcnt vmcnt(11) lgkmcnt(14)\n\t"
        "v_mfma_f32_32x32x16_bf16 v[0:15], v[40:43], v[100:103], v[0:15]\n\t"
        "ds_read_b64_tr_b16 v[40:41], v193 offset:26624\n\t"
        "ds_read_b64_tr_b16 v[42:43], v193 offset:27648\n\t"
        "s_waitcnt lgkmcnt(14)\n\t"
        "v_mfma_f32_32x32x16_bf16 v[16:31], v[44:47], v[100:103], v[16:31]\n\t"
        "ds_read_b64_tr_b16 v[44:45], v194 offset:26624\n\t"
        "ds_read_b64_tr_b16 v[46:47], v194 offset:27648\n\t"
        "buffer_load_dwordx4 v[148:151], v192, s[40:43], s44 offen offset:1024 sc1\n\t"
        "s_waitcnt vmcnt(11) lgkmcnt(14)\n\t"
        "v_mfma_f32_32x32x16_bf16 v[0:15], v[48:51], v[104:107], v[0:15]\n\t"
        "ds_read_b64_tr_b16 v[48:49], v193 offset:28672\n\t"
        "ds_read_b64_tr_b16 v[50:51], v193 offset:29696\n\t"
        "s_waitcnt lgkmcnt(14)\n\t"
        "v_mfma_f32_32x32x16_bf16 v[16:31], v[52:55], v[104:107], v[16:31]\n\t"
        "ds_read_b64_tr_b16 v[52:53], v194 offset:28672\n\t"
        "ds_read_b64_tr_b16 v[54:55], v194 offset:29696\n\t"
        "buffer_load_dwordx4 v[152:155], v192, s[40:43], s44 offen offset:2048 sc1\n\t"
        "s_waitcnt vmcnt(11) lgkmcnt(14)\n\t"
        "v_mfma_f32_32x32x16_bf16 v[0:15], v[56:59], v[108:111], v[0:15]\n\t"
        "ds_read_b64_tr_b16 v[56:57], v193 offset:30720\n\t"
        "ds_read_b64_tr_b16 v[58:59], v193 offset:31744\n\t"
        "s_waitcnt lgkmcnt(14)\n\t"
        "v_mfma_f32_32x32x16_bf16 v[16:31], v[60:63], v[108:111], v[16:31]\n\t"
        "ds_read_b64_tr_b16 v[60:61], v194 offset:30720\n\t"
        "ds_read_b64_tr_b16 v[62:63], v194 offset:31744\n\t"
        "buffer_load_dwordx4 v[156:159], v192, s[40:43], s44 offen offset:3072 sc1\n\t"
        "s_waitcnt vmcnt(11) lgkmcnt(14)\n\t"
        "v_mfma_f32_32x32x16_bf16 v[0:15], v[32:35], v[112:115], v[0:15]\n\t"
        "ds_read_b64_tr_b16 v[32:33], v193 offset:32768\n\t"
        "ds_read_b64_tr_b16 v[34:35], v193 offset:33792\n\t"
        "s_waitcnt lgkmcnt(14)\n\t"
        "v_mfma_f32_32x32x16_bf16 v[16:31], v[36:39], v[112:115], v[16:31]\n\t"
        "ds_read_b64_tr_b16 v[36:37], v194 offset:32768\n\t"
        "ds_read_b64_tr_b16 v[38:39], v194 offset:33792\n\t"
        "s_add_u32 s44, s44, 0x1000\n\t"
        "s_and_b32 s44, s44, 0xffff\n\t"
        "buffer_load_dwordx4 v[160:163], v192, s[40:43], s44 offen offset:0 sc1\n\t"
        "s_waitcnt vmcnt(11) lgkmcnt(14)\n\t"
        "v_mfma_f32_32x32x16_bf16 v[0:15], v[40:43], v[116:119], v[0:15]\n\t"
        "ds_read_b64_tr_b16 v[40:41], v193 offset:34816\n\t"
        "ds_read_b64_tr_b16 v[42:43], v193 offset:35840\n\t"
        "s_waitcnt lgkmcnt(14)\n\t"
        "v_mfma_f32_32x32x16_bf16 v[16:31], v[44:47], v[116:119], v[16:31]\n\t"
        "ds_read_b64_tr_b16 v[44:45], v194 offset:34816\n\t"
        "ds_read_b64_tr_b16 v[46:47], v194 offset:35840\n\t"
        "buffer_load_dwordx4 v[164:167], v192, s[40:43], s44 offen offset:1024 sc1\n\t"
        "s_waitcnt vmcnt(11) lgkmcnt(14)\n\t"
        "v_mfma_f32_32x32x16_bf16 v[0:15], v[48:51], v[120:123], v[0:15]\n\t"
        "ds_read_b64_tr_b16 v[48:49], v193 offset:36864\n\t"
        "ds_read_b64_tr_b16 v[50:51], v193 offset:37888\n\t"
        "s_waitcnt lgkmcnt(14)\n\t"
        "v_mfma_f32_32x32x16_bf16 v[16:31], v[52:55], v[120:123], v[16:31]\n\t"
        "ds_read_b64_tr_b16 v[52:53], v194 offset:36864\n\t"
        "ds_read_b64_tr_b16 v[54:55], v194 offset:37888\n\t"
        "buffer_load_dwordx4 v[168:171], v192, s[40:43], s44 offen offset:2048 sc1\n\t"
        "s_waitcnt vmcnt(11) lgkmcnt(14)\n\t"
        "v_mfma_f32_32x32x16_bf16 v[0:15], v[56:59], v[124:127], v[0:15]\n\t"
        "ds_read_b64_tr_b16 v[56:57], v193 offset:38912\n\t"
        "ds_read_b64_tr_b16 v[58:59], v193 offset:39936\n\t"
        "s_waitcnt lgkmcnt(14)\n\t"
        "v_mfma_f32_32x32x16_bf16 v[16:31], v[60:63], v[124:127], v[16:31]\n\t"
        "ds_read_b64_tr_b16 v[60:61], v194 offset:38912\n\t"
        "ds_read_b64_tr_b16 v[62:63], v194 offset:39936\n\t"
        "buffer_load_dwordx4 v[172:175], v192, s[40:43], s44 offen offset:3072 sc1\n\t"
        "s_waitcnt vmcnt(11) lgkmcnt(14)\n\t"
        "v_mfma_f32_32x32x16_bf16 v[0:15], v[32:35], v[128:131], v[0:15]\n\t"
        "ds_read_b64_tr_b16 v[32:33], v193 offset:40960\n\t"
        "ds_read_b64_tr_b16 v[34:35], v193 offset:41984\n\t"
        "s_waitcnt lgkmcnt(14)\n\t"
        "v_mfma_f32_32x32x16_bf16 v[16:31], v[36:39], v[128:131], v[16:31]\n\t"
        "ds_read_b64_tr_b16 v[36:37], v194 offset:40960\n\t"
        "ds_read_b64_tr_b16 v[38:39], v194 offset:41984\n\t"
        "s_add_u32 s44, s44, 0x1000\n\t"
        "s_and_b32 s44, s44, 0xffff\n\t"
        "buffer_load_dwordx4 v[176:179], v192, s[40:43], s44 offen offset:0 sc1\n\t"
        "s_waitcnt vmcnt(11) lgkmcnt(14)\n\t"
        "v_mfma_f32_32x32x16_bf16 v[0:15], v[40:43], v[132:135], v[0:15]\n\t"
        "ds_read_b64_tr_b16 v[40:41], v193 offset:43008\n\t"
        "ds_read_b64_tr_b16 v[42:43], v193 offset:44032\n\t"
        "s_waitcnt lgkmcnt(14)\n\t"
        "v_mfma_f32_32x32x16_bf16 v[16:31], v[44:47], v[132:135], v[16:31]\n\t"
        "ds_read_b64_tr_b16 v[44:45], v194 offset:43008\n\t"
        "ds_read_b64_tr_b16 v[46:47], v194 offset:44032\n\t"
        "buffer_load_dwordx4 v[180:183], v192, s[40:43], s44 offen offset:1024 sc1\n\t"
        "s_waitcnt vmcnt(11) lgkmcnt(14)\n\t"
        "v_mfma_f32_32x32x16_bf16 v[0:15], v[48:51], v[136:139], v[0:15]\n\t"
        "ds_read_b64_tr_b16 v[48:49], v193 offset:45056\n\t"
        "ds_read_b64_tr_b16 v[50:51], v193 offset:46080\n\t"
        "s_waitcnt lgkmcnt(14)\n\t"
        "v_mfma_f32_32x32x16_bf16 v[16:31], v[52:55], v[136:139], v[16:31]\n\t"
        "ds_read_b64_tr_b16 v[52:53], v194 offset:45056\n\t"
        "ds_read_b64_tr_b16 v[54:55], v194 offset:46080\n\t"
        "buffer_load_dwordx4 v[184:187], v192, s[40:43], s44 offen offset:2048 sc1\n\t"
        "s_waitcnt vmcnt(11) lgkmcnt(14)\n\t"
        "v_mfma_f32_32x32x16_bf16 v[0:15], v[56:59], v[140:143], v[0:15]\n\t"
        "ds_read_b64_tr_b16 v[56:57], v193 offset:47104\n\t"
        "ds_read_b64_tr_b16 v[58:59], v193 offset:48128\n\t"
        "s_waitcnt lgkmcnt(14)\n\t"
        "v_mfma_f32_32x32x16_bf16 v[16:31], v[60:63], v[140:143], v[16:31]\n\t"
        "ds_read_b64_tr_b16 v[60:61], v194 offset:47104\n\t"
        "ds_read_b64_tr_b16 v[62:63], v194 offset:48128\n\t"
        "buffer_load_dwordx4 v[188:191], v192, s[40:43], s44 offen offset:3072 sc1\n\t"
        "s_waitcnt vmcnt(11) lgkmcnt(14)\n\t"
        "v_mfma_f32_32x32x16_bf16 v[0:15], v[32:35], v[144:147], v[0:15]\n\t"
        "ds_read_b64_tr_b16 v[32:33], v193 offset:49152\n\t"
        "ds_read_b64_tr_b16 v[34:35], v193 offset:50176\n\t"
        "s_waitcnt lgkmcnt(14)\n\t"
        "v_mfma_f32_32x32x16_bf16 v[16:31], v[36:39], v[144:147], v[16:31]\n\t"
        "ds_read_b64_tr_b16 v[36:37], v194 offset:49152\n\t"
        "ds_read_b64_tr_b16 v[38:39], v194 offset:50176\n\t"
        "s_add_u32 s44, s44, 0x1000\n\t"
        "s_and_b32 s44, s44, 0xffff\n\t"
        "buffer_load_dwordx4 v[64:67], v192, s[40:43], s44 offen offset:0 sc1\n\t"
        "s_waitcnt vmcnt(11) lgkmcnt(14)\n\t"
        "v_mfma_f32_32x32x16_bf16 v[0:15], v[40:43], v[148:151], v[0:15]\n\t"
        "ds_read_b64_tr_b16 v[40:41], v193 offset:51200\n\t"
        "ds_read_b64_tr_b16 v[42:43], v193 offset:52224\n\t"
        "s_waitcnt lgkmcnt(14)\n\t"
        "v_mfma_f32_32x32x16_bf16 v[16:31], v[44:47], v[148:151], v[16:31]\n\t"
        "ds_read_b64_tr_b16 v[44:45], v194 offset:51200\n\t"
        "ds_read_b64_tr_b16 v[46:47], v194 offset:52224\n\t"
        "buffer_load_dwordx4 v[68:71], v192, s[40:43], s44 offen offset:1024 sc1\n\t"
        "s_waitcnt vmcnt(11) lgkmcnt(14)\n\t"
        "v_mfma_f32_32x32x16_bf16 v[0:15], v[48:51], v[152:155], v[0:15]\n\t"
        "ds_read_b64_tr_b16 v[48:49], v193 offset:53248\n\t"
        "ds_read_b64_tr_b16 v[50:51], v193 offset:54272\n\t"
        "s_waitcnt lgkmcnt(14)\n\t"
        "v_mfma_f32_32x32x16_bf16 v[16:31], v[52:55], v[152:155], v[16:31]\n\t"
        "ds_read_b64_tr_b16 v[52:53], v194 offset:53248\n\t"
        "ds_read_b64_tr_b16 v[54:55], v194 offset:54272\n\t"
        "buffer_load_dwordx4 v[72:75], v192, s[40:43], s44 offen offset:2048 sc1\n\t"
        "s_waitcnt vmcnt(11) lgkmcnt(14)\n\t"
        "v_mfma_f32_32x32x16_bf16 v[0:15], v[56:59], v[156:159], v[0:15]\n\t"
        "ds_read_b64_tr_b16 v[56:57], v193 offset:55296\n\t"
        "ds_read_b64_tr_b16 v[58:59], v193 offset:56320\n\t"
        "s_waitcnt lgkmcnt(14)\n\t"
        "v_mfma_f32_32x32x16_bf16 v[16:31], v[60:63], v[156:159], v[16:31]\n\t"
        "ds_read_b64_tr_b16 v[60:61], v194 offset:55296\n\t"
        "ds_read_b64_tr_b16 v[62:63], v194 offset:56320\n\t"
        "buffer_load_dwordx4 v[76:79], v192, s[40:43], s44 offen offset:3072 sc1\n\t"
        "s_waitcnt vmcnt(11) lgkmcnt(14)\n\t"
        "v_mfma_f32_32x32x16_bf16 v[0:15], v[32:35], v[160:163], v[0:15]\n\t"
        "ds_read_b64_tr_b16 v[32:33], v193 offset:57344\n\t"
        "ds_read_b64_tr_b16 v[34:35], v193 offset:58368\n\t"
        "s_waitcnt lgkmcnt(14)\n\t"
        "v_mfma_f32_32x32x16_bf16 v[16:31], v[36:39], v[160:163], v[16:31]\n\t"
        "ds_read_b64_tr_b16 v[36:37], v194 offset:57344\n\t"
        "ds_read_b64_tr_b16 v[38:39], v194 offset:58368\n\t"
        "s_add_u32 s44, s44, 0x1000\n\t"
        "s_and_b32 s44, s44, 0xffff\n\t"
        "buffer_load_dwordx4 v[80:83], v192, s[40:43], s44 offen offset:0 sc1\n\t"
        "s_waitcnt vmcnt(11) lgkmcnt(14)\n\t"
        "v_mfma_f32_32x32x16_bf16 v[0:15], v[40:43], v[164:167], v[0:15]\n\t"
        "ds_read_b64_tr_b16 v[40:41], v193 offset:59392\n\t"
        "ds_read_b64_tr_b16 v[42:43], v193 offset:60416\n\t"
        "s_waitcnt lgkmcnt(14)\n\t"
        "v_mfma_f32_32x32x16_bf16 v[16:31], v[44:47], v[164:167], v[16:31]\n\t"
        "ds_read_b64_tr_b16 v[44:45], v194 offset:59392\n\t"
        "ds_read_b64_tr_b16 v[46:47], v194 offset:60416\n\t"
        "buffer_load_dwordx4 v[84:87], v192, s[40:43], s44 offen offset:1024 sc1\n\t"
        "s_waitcnt vmcnt(11) lgkmcnt(14)\n\t"
        "v_mfma_f32_32x32x16_bf16 v[0:15], v[48:51], v[168:171], v[0:15]\n\t"
        "ds_read_b64_tr_b16 v[48:49], v193 offset:61440\n\t"
        "ds_read_b64_tr_b16 v[50:51], v193 offset:62464\n\t"
        "s_waitcnt lgkmcnt(14)\n\t"
        "v_mfma_f32_32x32x16_bf16 v[16:31], v[52:55], v[168:171], v[16:31]\n\t"
        "ds_read_b64_tr_b16 v[52:53], v194 offset:61440\n\t"
        "ds_read_b64_tr_b16 v[54:55], v194 offset:62464\n\t"
        "buffer_load_dwordx4 v[88:91], v192, s[40:43], s44 offen offset:2048 sc1\n\t"
        "s_waitcnt vmcnt(11) lgkmcnt(14)\n\t"
        "v_mfma_f32_32x32x16_bf16 v[0:15], v[56:59], v[172:175], v[0:15]\n\t"
        "ds_read_b64_tr_b16 v[56:57], v193 offset:63488\n\t"
        "ds_read_b64_tr_b16 v[58:59], v193 offset:64512\n\t"
        "s_waitcnt lgkmcnt(14)\n\t"
        "v_mfma_f32_32x32x16_bf16 v[16:31], v[60:63], v[172:175], v[16:31]\n\t"
        "ds_read_b64_tr_b16 v[60:61], v194 offset:63488\n\t"
        "ds_read_b64_tr_b16 v[62:63], v194 offset:64512\n\t"
        "buffer_load_dwordx4 v[92:95], v192, s[40:43], s44 offen offset:3072 sc1\n\t"
        "s_waitcnt vmcnt(11) lgkmcnt(14)\n\t"
        "v_mfma_f32_32x32x16_bf16 v[0:15], v[32:35], v[176:179], v[0:15]\n\t"
        "ds_read_b64_tr_b16 v[32:33], v195 offset:0\n\t"
        "ds_read_b64_tr_b16 v[34:35], v195 offset:1024\n\t"
        "s_waitcnt lgkmcnt(14)\n\t"
        "v_mfma_f32_32x32x16_bf16 v[16:31], v[36:39], v[176:179], v[16:31]\n\t"
        "ds_read_b64_tr_b16 v[36:37], v196 offset:0\n\t"
        "ds_read_b64_tr_b16 v[38:39], v196 offset:1024\n\t"
        "s_add_u32 s44, s44, 0x1000\n\t"
        "s_and_b32 s44, s44, 0xffff\n\t"
        "buffer_load_dwordx4 v[96:99], v192, s[40:43], s44 offen offset:0 sc1\n\t"
        "s_waitcnt vmcnt(11) lgkmcnt(14)\n\t"
        "v_mfma_f32_32x32x16_bf16 v[0:15], v[40:43], v[180:183], v[0:15]\n\t"
        "ds_read_b64_tr_b16 v[40:41], v195 offset:2048\n\t"
        "ds_read_b64_tr_b16 v[42:43], v195 offset:3072\n\t"
        "s_waitcnt lgkmcnt(14)\n\t"
        "v_mfma_f32_32x32x16_bf16 v[16:31], v[44:47], v[180:183], v[16:31]\n\t"
        "ds_read_b64_tr_b16 v[44:45], v196 offset:2048\n\t"
        "ds_read_b64_tr_b16 v[46:47], v196 offset:3072\n\t"
        "buffer_load_dwordx4 v[100:103], v192, s[40:43], s44 offen offset:1024 sc1\n\t"
        "s_waitcnt vmcnt(11) lgkmcnt(14)\n\t"
        "v_mfma_f32_32x32x16_bf16 v[0:15], v[48:51], v[184:187], v[0:15]\n\t"
        "ds_read_b64_tr_b16 v[48:49], v195 offset:4096\n\t"
        "ds_read_b64_tr_b16 v[50:51], v195 offset:5120\n\t"
        "s_waitcnt lgkmcnt(14)\n\t"
        "v_mfma_f32_32x32x16_bf16 v[16:31], v[52:55], v[184:187], v[16:31]\n\t"
        "ds_read_b64_tr_b16 v[52:53], v196 offset:4096\n\t"
        "ds_read_b64_tr_b16 v[54:55], v196 offset:5120\n\t"
        "buffer_load_dwordx4 v[104:107], v192, s[40:43], s44 offen offset:2048 sc1\n\t"
        "s_waitcnt vmcnt(11) lgkmcnt(14)\n\t"
        "v_mfma_f32_32x32x16_bf16 v[0:15], v[56:59], v[188:191], v[0:15]\n\t"
        "ds_read_b64_tr_b16 v[56:57], v195 offset:6144\n\t"
        "ds_read_b64_tr_b16 v[58:59], v195 offset:7168\n\t"
        "s_waitcnt lgkmcnt(14)\n\t"
        "v_mfma_f32_32x32x16_bf16 v[16:31], v[60:63], v[188:191], v[16:31]\n\t"
        "ds_read_b64_tr_b16 v[60:61], v196 offset:6144\n\t"
        "ds_read_b64_tr_b16 v[62:63], v196 offset:7168\n\t"
        "buffer_load_dwordx4 v[108:111], v192, s[40:43], s44 offen offset:3072 sc1\n\t"
        "s_waitcnt vmcnt(11) lgkmcnt(14)\n\t"
        "v_mfma_f32_32x32x16_bf16 v[0:15], v[32:35], v[64:67], v[0:15]\n\t"
        "ds_read_b64_tr_b16 v[32:33], v195 offset:8192\n\t"
        "ds_read_b64_tr_b16 v[34:35], v195 offset:9216\n\t"
        "s_waitcnt lgkmcnt(14)\n\t"
        "v_mfma_f32_32x32x16_bf16 v[16:31], v[36:39], v[64:67], v[16:31]\n\t"
        "ds_read_b64_tr_b16 v[36:37], v196 offset:8192\n\t"
        "ds_read_b64_tr_b16 v[38:39], v196 offset:9216\n\t"
        "s_add_u32 s44, s44, 0x1000\n\t"
        "s_and_b32 s44, s44, 0xffff\n\t"
        "buffer_load_dwordx4 v[112:115], v192, s[40:43], s44 offen offset:0 sc1\n\t"
        "s_waitcnt vmcnt(11) lgkmcnt(14)\n\t"
        "v_mfma_f32_32x32x16_bf16 v[0:15], v[40:43], v[68:71], v[0:15]\n\t"
        "ds_read_b64_tr_b16 v[40:41], v195 offset:10240\n\t"
        "ds_read_b64_tr_b16 v[42:43], v195 offset:11264\n\t"
        "s_waitcnt lgkmcnt(14)\n\t"
        "v_mfma_f32_32x32x16_bf16 v[16:31], v[44:47], v[68:71], v[16:31]\n\t"
        "ds_read_b64_tr_b16 v[44:45], v196 offset:10240\n\t"
        "ds_read_b64_tr_b16 v[46:47], v196 offset:11264\n\t"
        "buffer_load_dwordx4 v[116:119], v192, s[40:43], s44 offen offset:1024 sc1\n\t"
        "s_waitcnt vmcnt(11) lgkmcnt(14)\n\t"
        "v_mfma_f32_32x32x16_bf16 v[0:15], v[48:51], v[72:75], v[0:15]\n\t"
        "ds_read_b64_tr_b16 v[48:49], v195 offset:12288\n\t"
        "ds_read_b64_tr_b16 v[50:51], v195 offset:13312\n\t"
        "s_waitcnt lgkmcnt(14)\n\t"
        "v_mfma_f32_32x32x16_bf16 v[16:31], v[52:55], v[72:75], v[16:31]\n\t"
        "ds_read_b64_tr_b16 v[52:53], v196 offset:12288\n\t"
        "ds_read_b64_tr_b16 v[54:55], v196 offset:13312\n\t"
        "buffer_load_dwordx4 v[120:123], v192, s[40:43], s44 offen offset:2048 sc1\n\t"
        "s_waitcnt vmcnt(11) lgkmcnt(14)\n\t"
        "v_mfma_f32_32x32x16_bf16 v[0:15], v[56:59], v[76:79], v[0:15]\n\t"
        "ds_read_b64_tr_b16 v[56:57], v195 offset:14336\n\t"
        "ds_read_b64_tr_b16 v[58:59], v195 offset:15360\n\t"
        "s_waitcnt lgkmcnt(14)\n\t"
        "v_mfma_f32_32x32x16_bf16 v[16:31], v[60:63], v[76:79], v[16:31]\n\t"
        "ds_read_b64_tr_b16 v[60:61], v196 offset:14336\n\t"
        "ds_read_b64_tr_b16 v[62:63], v196 offset:15360\n\t"
        "buffer_load_dwordx4 v[124:127], v192, s[40:43], s44 offen offset:3072 sc1\n\t"
        "s_waitcnt vmcnt(11) lgkmcnt(14)\n\t"
        "v_mfma_f32_32x32x16_bf16 v[0:15], v[32:35], v[80:83], v[0:15]\n\t"
        "ds_read_b64_tr_b16 v[32:33], v195 offset:16384\n\t"
        "ds_read_b64_tr_b16 v[34:35], v195 offset:17408\n\t"
        "s_waitcnt lgkmcnt(14)\n\t"
        "v_mfma_f32_32x32x16_bf16 v[16:31], v[36:39], v[80:83], v[16:31]\n\t"
        "ds_read_b64_tr_b16 v[36:37], v196 offset:16384\n\t"
        "ds_read_b64_tr_b16 v[38:39], v196 offset:17408\n\t"
        "s_add_u32 s44, s44, 0x1000\n\t"
        "s_and_b32 s44, s44, 0xffff\n\t"
        "buffer_load_dwordx4 v[128:131], v192, s[40:43], s44 offen offset:0 sc1\n\t"
        "s_waitcnt vmcnt(11) lgkmcnt(14)\n\t"
        "v_mfma_f32_32x32x16_bf16 v[0:15], v[40:43], v[84:87], v[0:15]\n\t"
        "ds_read_b64_tr_b16 v[40:41], v195 offset:18432\n\t"
        "ds_read_b64_tr_b16 v[42:43], v195 offset:19456\n\t"
        "s_waitcnt lgkmcnt(14)\n\t"
        "v_mfma_f32_32x32x16_bf16 v[16:31], v[44:47], v[84:87], v[16:31]\n\t"
        "ds_read_b64_tr_b16 v[44:45], v196 offset:18432\n\t"
        "ds_read_b64_tr_b16 v[46:47], v196 offset:19456\n\t"
        "buffer_load_dwordx4 v[132:135], v192, s[40:43], s44 offen offset:1024 sc1\n\t"
        "s_waitcnt vmcnt(11) lgkmcnt(14)\n\t"
        "v_mfma_f32_32x32x16_bf16 v[0:15], v[48:51], v[88:91], v[0:15]\n\t"
        "ds_read_b64_tr_b16 v[48:49], v195 offset:20480\n\t"
        "ds_read_b64_tr_b16 v[50:51], v195 offset:21504\n\t"
        "s_waitcnt lgkmcnt(14)\n\t"
        "v_mfma_f32_32x32x16_bf16 v[16:31], v[52:55], v[88:91], v[16:31]\n\t"
        "ds_read_b64_tr_b16 v[52:53], v196 offset:20480\n\t"
        "ds_read_b64_tr_b16 v[54:55], v196 offset:21504\n\t"
        "buffer_load_dwordx4 v[136:139], v192, s[40:43], s44 offen offset:2048 sc1\n\t"
        "s_waitcnt vmcnt(11) lgkmcnt(14)\n\t"
        "v_mfma_f32_32x32x16_bf16 v[0:15], v[56:59], v[92:95], v[0:15]\n\t"
        "ds_read_b64_tr_b16 v[56:57], v195 offset:22528\n\t"
        "ds_read_b64_tr_b16 v[58:59], v195 offset:23552\n\t"
        "s_waitcnt lgkmcnt(14)\n\t"
        "v_mfma_f32_32x32x16_bf16 v[16:31], v[60:63], v[92:95], v[16:31]\n\t"
        "ds_read_b64_tr_b16 v[60:61], v196 offset:22528\n\t"
        "ds_read_b64_tr_b16 v[62:63], v196 offset:23552\n\t"
        "buffer_load_dwordx4 v[140:143], v192, s[40:43], s44 offen offset:3072 sc1\n\t"
        "s_waitcnt vmcnt(11) lgkmcnt(14)\n\t"
        "v_mfma_f32_32x32x16_bf16 v[0:15], v[32:35], v[96:99], v[0:15]\n\t"
        "ds_read_b64_tr_b16 v[32:33], v195 offset:24576\n\t"
        "ds_read_b64_tr_b16 v[34:35], v195 offset:25600\n\t"
        "s_waitcnt lgkmcnt(14)\n\t"
        "v_mfma_f32_32x32x16_bf16 v[16:31], v[36:39], v[96:99], v[16:31]\n\t"
        "ds_read_b64_tr_b16 v[36:37], v196 offset:24576\n\t"
        "ds_read_b64_tr_b16 v[38:39], v196 offset:25600\n\t"
        "s_add_u32 s44, s44, 0x1000\n\t"
        "s_and_b32 s44, s44, 0xffff\n\t"
        "buffer_load_dwordx4 v[144:147], v192, s[40:43], s44 offen offset:0 sc1\n\t"
        "s_waitcnt vmcnt(11) lgkmcnt(14)\n\t"
        "v_mfma_f32_32x32x16_bf16 v[0:15], v[40:43], v[100:103], v[0:15]\n\t"
        "ds_read_b64_tr_b16 v[40:41], v195 offset:26624\n\t"
        "ds_read_b64_tr_b16 v[42:43], v195 offset:27648\n\t"
        "s_waitcnt lgkmcnt(14)\n\t"
        "v_mfma_f32_32x32x16_bf16 v[16:31], v[44:47], v[100:103], v[16:31]\n\t"
        "ds_read_b64_tr_b16 v[44:45], v196 offset:26624\n\t"
        "ds_read_b64_tr_b16 v[46:47], v196 offset:27648\n\t"
        "buffer_load_dwordx4 v[148:151], v192, s[40:43], s44 offen offset:1024 sc1\n\t"
        "s_waitcnt vmcnt(11) lgkmcnt(14)\n\t"
        "v_mfma_f32_32x32x16_bf16 v[0:15], v[48:51], v[104:107], v[0:15]\n\t"
        "ds_read_b64_tr_b16 v[48:49], v195 offset:28672\n\t"
        "ds_read_b64_tr_b16 v[50:51], v195 offset:29696\n\t"
        "s_waitcnt lgkmcnt(14)\n\t"
        "v_mfma_f32_32x32x16_bf16 v[16:31], v[52:55], v[104:107], v[16:31]\n\t"
        "ds_read_b64_tr_b16 v[52:53], v196 offset:28672\n\t"
        "ds_read_b64_tr_b16 v[54:55], v196 offset:29696\n\t"
        "buffer_load_dwordx4 v[152:155], v192, s[40:43], s44 offen offset:2048 sc1\n\t"
        "s_waitcnt vmcnt(11) lgkmcnt(14)\n\t"
        "v_mfma_f32_32x32x16_bf16 v[0:15], v[56:59], v[108:111], v[0:15]\n\t"
        "ds_read_b64_tr_b16 v[56:57], v195 offset:30720\n\t"
        "ds_read_b64_tr_b16 v[58:59], v195 offset:31744\n\t"
        "s_waitcnt lgkmcnt(14)\n\t"
        "v_mfma_f32_32x32x16_bf16 v[16:31], v[60:63], v[108:111], v[16:31]\n\t"
        "ds_read_b64_tr_b16 v[60:61], v196 offset:30720\n\t"
        "ds_read_b64_tr_b16 v[62:63], v196 offset:31744\n\t"
        "buffer_load_dwordx4 v[156:159], v192, s[40:43], s44 offen offset:3072 sc1\n\t"
        "s_waitcnt vmcnt(11) lgkmcnt(14)\n\t"
        "v_mfma_f32_32x32x16_bf16 v[0:15], v[32:35], v[112:115], v[0:15]\n\t"
        "ds_read_b64_tr_b16 v[32:33], v195 offset:32768\n\t"
        "ds_read_b64_tr_b16 v[34:35], v195 offset:33792\n\t"
        "s_waitcnt lgkmcnt(14)\n\t"
        "v_mfma_f32_32x32x16_bf16 v[16:31], v[36:39], v[112:115], v[16:31]\n\t"
        "ds_read_b64_tr_b16 v[36:37], v196 offset:32768\n\t"
        "ds_read_b64_tr_b16 v[38:39], v196 offset:33792\n\t"
        "s_add_u32 s44, s44, 0x1000\n\t"
        "s_and_b32 s44, s44, 0xffff\n\t"
        "buffer_load_dwordx4 v[160:163], v192, s[40:43], s44 offen offset:0 sc1\n\t"
        "s_waitcnt vmcnt(11) lgkmcnt(14)\n\t"
        "v_mfma_f32_32x32x16_bf16 v[0:15], v[40:43], v[116:119], v[0:15]\n\t"
        "ds_read_b64_tr_b16 v[40:41], v195 offset:34816\n\t"
        "ds_read_b64_tr_b16 v[42:43], v195 offset:35840\n\t"
        "s_waitcnt lgkmcnt(14)\n\t"
        "v_mfma_f32_32x32x16_bf16 v[16:31], v[44:47], v[116:119], v[16:31]\n\t"
        "ds_read_b64_tr_b16 v[44:45], v196 offset:34816\n\t"
        "ds_read_b64_tr_b16 v[46:47], v196 offset:35840\n\t"
        "buffer_load_dwordx4 v[164:167], v192, s[40:43], s44 offen offset:1024 sc1\n\t"
        "s_waitcnt vmcnt(11) lgkmcnt(14)\n\t"
        "v_mfma_f32_32x32x16_bf16 v[0:15], v[48:51], v[120:123], v[0:15]\n\t"
        "ds_read_b64_tr_b16 v[48:49], v195 offset:36864\n\t"
        "ds_read_b64_tr_b16 v[50:51], v195 offset:37888\n\t"
        "s_waitcnt lgkmcnt(14)\n\t"
        "v_mfma_f32_32x32x16_bf16 v[16:31], v[52:55], v[120:123], v[16:31]\n\t"
        "ds_read_b64_tr_b16 v[52:53], v196 offset:36864\n\t"
        "ds_read_b64_tr_b16 v[54:55], v196 offset:37888\n\t"
        "buffer_load_dwordx4 v[168:171], v192, s[40:43], s44 offen offset:2048 sc1\n\t"
        "s_waitcnt vmcnt(11) lgkmcnt(14)\n\t"
        "v_mfma_f32_32x32x16_bf16 v[0:15], v[56:59], v[124:127], v[0:15]\n\t"
        "ds_read_b64_tr_b16 v[56:57], v195 offset:38912\n\t"
        "ds_read_b64_tr_b16 v[58:59], v195 offset:39936\n\t"
        "s_waitcnt lgkmcnt(14)\n\t"
        "v_mfma_f32_32x32x16_bf16 v[16:31], v[60:63], v[124:127], v[16:31]\n\t"
        "ds_read_b64_tr_b16 v[60:61], v196 offset:38912\n\t"
        "ds_read_b64_tr_b16 v[62:63], v196 offset:39936\n\t"
        "buffer_load_dwordx4 v[172:175], v192, s[40:43], s44 offen offset:3072 sc1\n\t"
        "s_waitcnt vmcnt(11) lgkmcnt(14)\n\t"
        "v_mfma_f32_32x32x16_bf16 v[0:15], v[32:35], v[128:131], v[0:15]\n\t"
        "ds_read_b64_tr_b16 v[32:33], v195 offset:40960\n\t"
        "ds_read_b64_tr_b16 v[34:35], v195 offset:41984\n\t"
        "s_waitcnt lgkmcnt(14)\n\t"
        "v_mfma_f32_32x32x16_bf16 v[16:31], v[36:39], v[128:131], v[16:31]\n\t"
        "ds_read_b64_tr_b16 v[36:37], v196 offset:40960\n\t"
        "ds_read_b64_tr_b16 v[38:39], v196 offset:41984\n\t"
        "s_add_u32 s44, s44, 0x1000\n\t"
        "s_and_b32 s44, s44, 0xffff\n\t"
        "buffer_load_dwordx4 v[176:179], v192, s[40:43], s44 offen offset:0 sc1\n\t"
        "s_waitcnt vmcnt(11) lgkmcnt(14)\n\t"
        "v_mfma_f32_32x32x16_bf16 v[0:15], v[40:43], v[132:135], v[0:15]\n\t"
        "ds_read_b64_tr_b16 v[40:41], v195 offset:43008\n\t"
        "ds_read_b64_tr_b16 v[42:43], v195 offset:44032\n\t"
        "s_waitcnt lgkmcnt(14)\n\t"
        "v_mfma_f32_32x32x16_bf16 v[16:31], v[44:47], v[132:135], v[16:31]\n\t"
        "ds_read_b64_tr_b16 v[44:45], v196 offset:43008\n\t"
        "ds_read_b64_tr_b16 v[46:47], v196 offset:44032\n\t"
        "buffer_load_dwordx4 v[180:183], v192, s[40:43], s44 offen offset:1024 sc1\n\t"
        "s_waitcnt vmcnt(11) lgkmcnt(14)\n\t"
        "v_mfma_f32_32x32x16_bf16 v[0:15], v[48:51], v[136:139], v[0:15]\n\t"
        "ds_read_b64_tr_b16 v[48:49], v195 offset:45056\n\t"
        "ds_read_b64_tr_b16 v[50:51], v195 offset:46080\n\t"
        "s_waitcnt lgkmcnt(14)\n\t"
        "v_mfma_f32_32x32x16_bf16 v[16:31], v[52:55], v[136:139], v[16:31]\n\t"
        "ds_read_b64_tr_b16 v[52:53], v196 offset:45056\n\t"
        "ds_read_b64_tr_b16 v[54:55], v196 offset:46080\n\t"
        "buffer_load_dwordx4 v[184:187], v192, s[40:43], s44 offen offset:2048 sc1\n\t"
        "s_waitcnt vmcnt(11) lgkmcnt(14)\n\t"
        "v_mfma_f32_32x32x16_bf16 v[0:15], v[56:59], v[140:143], v[0:15]\n\t"
        "ds_read_b64_tr_b16 v[56:57], v195 offset:47104\n\t"
        "ds_read_b64_tr_b16 v[58:59], v195 offset:48128\n\t"
        "s_waitcnt lgkmcnt(14)\n\t"
        "v_mfma_f32_32x32x16_bf16 v[16:31], v[60:63], v[140:143], v[16:31]\n\t"
        "ds_read_b64_tr_b16 v[60:61], v196 offset:47104\n\t"
        "ds_read_b64_tr_b16 v[62:63], v196 offset:48128\n\t"
        "buffer_load_dwordx4 v[188:191], v192, s[40:43], s44 offen offset:3072 sc1\n\t"
        "s_waitcnt vmcnt(11) lgkmcnt(14)\n\t"
        "v_mfma_f32_32x32x16_bf16 v[0:15], v[32:35], v[144:147], v[0:15]\n\t"
        "ds_read_b64_tr_b16 v[32:33], v195 offset:49152\n\t"
        "ds_read_b64_tr_b16 v[34:35], v195 offset:50176\n\t"
        "s_waitcnt lgkmcnt(14)\n\t"
        "v_mfma_f32_32x32x16_bf16 v[16:31], v[36:39], v[144:147], v[16:31]\n\t"
        "ds_read_b64_tr_b16 v[36:37], v196 offset:49152\n\t"
        "ds_read_b64_tr_b16 v[38:39], v196 offset:50176\n\t"
        "s_waitcnt vmcnt(10) lgkmcnt(14)\n\t"
        "v_mfma_f32_32x32x16_bf16 v[0:15], v[40:43], v[148:151], v[0:15]\n\t"
        "ds_read_b64_tr_b16 v[40:41], v195 offset:51200\n\t"
        "ds_read_b64_tr_b16 v[42:43], v195 offset:52224\n\t"
        "s_waitcnt lgkmcnt(14)\n\t"
        "v_mfma_f32_32x32x16_bf16 v[16:31], v[44:47], v[148:151], v[16:31]\n\t"
        "ds_read_b64_tr_b16 v[44:45], v196 offset:51200\n\t"
        "ds_read_b64_tr_b16 v[46:47], v196 offset:52224\n\t"
        "s_waitcnt vmcnt(9) lgkmcnt(14)\n\t"
        "v_mfma_f32_32x32x16_bf16 v[0:15], v[48:51], v[152:155], v[0:15]\n\t"
        "ds_read_b64_tr_b16 v[48:49], v195 offset:53248\n\t"
        "ds_read_b64_tr_b16 v[50:51], v195 offset:54272\n\t"
        "s_waitcnt lgkmcnt(14)\n\t"
        "v_mfma_f32_32x32x16_bf16 v[16:31], v[52:55], v[152:155], v[16:31]\n\t"
        "ds_read_b64_tr_b16 v[52:53], v196 offset:53248\n\t"
        "ds_read_b64_tr_b16 v[54:55], v196 offset:54272\n\t"
        "s_waitcnt vmcnt(8) lgkmcnt(14)\n\t"
        "v_mfma_f32_32x32x16_bf16 v[0:15], v[56:59], v[156:159], v[0:15]\n\t"
        "ds_read_b64_tr_b16 v[56:57], v195 offset:55296\n\t"
        "ds_read_b64_tr_b16 v[58:59], v195 offset:56320\n\t"
        "s_waitcnt lgkmcnt(14)\n\t"
        "v_mfma_f32_32x32x16_bf16 v[16:31], v[60:63], v[156:159], v[16:31]\n\t"
        "ds_read_b64_tr_b16 v[60:61], v196 offset:55296\n\t"
        "ds_read_b64_tr_b16 v[62:63], v196 offset:56320\n\t"
        "s_waitcnt vmcnt(7) lgkmcnt(14)\n\t"
        "v_mfma_f32_32x32x16_bf16 v[0:15], v[32:35], v[160:163], v[0:15]\n\t"
        "s_barrier\n\t"
        "ds_read_b64_tr_b16 v[32:33], v195 offset:57344\n\t"
        "ds_read_b64_tr_b16 v[34:35], v195 offset:58368\n\t"
        "s_waitcnt lgkmcnt(14)\n\t"
        "v_mfma_f32_32x32x16_bf16 v[16:31], v[36:39], v[160:163], v[16:31]\n\t"
        "ds_read_b64_tr_b16 v[36:37], v196 offset:57344\n\t"
        "ds_read_b64_tr_b16 v[38:39], v196 offset:58368\n\t"
        "s_waitcnt vmcnt(6) lgkmcnt(14)\n\t"
        "v_mfma_f32_32x32x16_bf16 v[0:15], v[40:43], v[164:167], v[0:15]\n\t"
        "ds_read_b64_tr_b16 v[40:41], v195 offset:59392\n\t"
        "ds_read_b64_tr_b16 v[42:43], v195 offset:60416\n\t"
        "s_waitcnt lgkmcnt(14)\n\t"
        "v_mfma_f32_32x32x16_bf16 v[16:31], v[44:47], v[164:167], v[16:31]\n\t"
        "ds_read_b64_tr_b16 v[44:45], v196 offset:59392\n\t"
        "ds_read_b64_tr_b16 v[46:47], v196 offset:60416\n\t"
        "s_waitcnt vmcnt(5) lgkmcnt(14)\n\t"
        "v_mfma_f32_32x32x16_bf16 v[0:15], v[48:51], v[168:171], v[0:15]\n\t"
        "ds_read_b64_tr_b16 v[48:49], v195 offset:61440\n\t"
        "ds_read_b64_tr_b16 v[50:51], v195 offset:62464\n\t"
        "s_waitcnt lgkmcnt(14)\n\t"
        "v_mfma_f32_32x32x16_bf16 v[16:31], v[52:55], v[168:171], v[16:31]\n\t"
        "ds_read_b64_tr_b16 v[52:53], v196 offset:61440\n\t"
        "ds_read_b64_tr_b16 v[54:55], v196 offset:62464\n\t"
        "s_waitcnt vmcnt(4) lgkmcnt(14)\n\t"
        "v_mfma_f32_32x32x16_bf16 v[0:15], v[56:59], v[172:175], v[0:15]\n\t"
        "ds_read_b64_tr_b16 v[56:57], v195 offset:63488\n\t"
        "ds_read_b64_tr_b16 v[58:59], v195 offset:64512\n\t"
        "s_waitcnt lgkmcnt(14)\n\t"
        "v_mfma_f32_32x32x16_bf16 v[16:31], v[60:63], v[172:175], v[16:31]\n\t"
        "ds_read_b64_tr_b16 v[60:61], v196 offset:63488\n\t"
        "ds_read_b64_tr_b16 v[62:63], v196 offset:64512\n\t"
        "s_waitcnt vmcnt(3) lgkmcnt(14)\n\t"
        "v_mfma_f32_32x32x16_bf16 v[0:15], v[32:35], v[176:179], v[0:15]\n\t"
        "s_waitcnt lgkmcnt(12)\n\t"
        "v_mfma_f32_32x32x16_bf16 v[16:31], v[36:39], v[176:179], v[16:31]\n\t"
        "s_waitcnt vmcnt(2) lgkmcnt(10)\n\t"
        "v_mfma_f32_32x32x16_bf16 v[0:15], v[40:43], v[180:183], v[0:15]\n\t"
        "s_waitcnt lgkmcnt(8)\n\t"
        "v_mfma_f32_32x32x16_bf16 v[16:31], v[44:47], v[180:183], v[16:31]\n\t"
        "s_waitcnt vmcnt(1) lgkmcnt(6)\n\t"
        "v_mfma_f32_32x32x16_bf16 v[0:15], v[48:51], v[184:187], v[0:15]\n\t"
        "s_waitcnt lgkmcnt(4)\n\t"
        "v_mfma_f32_32x32x16_bf16 v[16:31], v[52:55], v[184:187], v[16:31]\n\t"
        "s_waitcnt vmcnt(0) lgkmcnt(2)\n\t"
        "v_mfma_f32_32x32x16_bf16 v[0:15], v[56:59], v[188:191], v[0:15]\n\t"
        "s_waitcnt lgkmcnt(0)\n\t"
        "v_mfma_f32_32x32x16_bf16 v[16:31], v[60:63], v[188:191], v[16:31]\n\t"
        "s_nop 11\n\t"
        : "={v[0:15]}"(acc[0]), "={v[16:31]}"(acc[1]), "+{s44}"(soff) STAMP_OUTS
        : "{v192}"(OFF_OF + L * (5 * MBY) + tile * TS + lane * 16), "{v193}"(a0), "{v194}"(a1), "{v195}"(a0 + 65536), "{v196}"(a1 + 65536),
          "{v198}"(fl + r32 * 128), "{v199}"(TAG), "{s[40:43]}"(srd)
        : "v32", "v33", "v34", "v35", "v36", "v37", "v38", "v39", "v40", "v41", "v42", "v43", "v44", "v45", "v46", "v47", "v48", "v49", "v50", "v51", "v52", "v53", "v54", "v55", "v56", "v57", "v58", "v59", "v60", "v61", "v62", "v63", "v64", "v65", "v66", "v67", "v68", "v69", "v70", "v71", "v72", "v73", "v74", "v75", "v76", "v77", "v78", "v79", "v80", "v81", "v82", "v83", "v84", "v85", "v86", "v87", "v88", "v89", "v90", "v91", "v92", "v93", "v94", "v95", "v96", "v97", "v98", "v99", "v100", "v101", "v102", "v103", "v104", "v105", "v106", "v107", "v108", "v109", "v110", "v111", "v112", "v113", "v114", "v115", "v116", "v117", "v118", "v119", "v120", "v121", "v122", "v123", "v124", "v125", "v126", "v127", "v128", "v129", "v130", "v131", "v132", "v133", "v134", "v135", "v136", "v137", "v138", "v139", "v140", "v141", "v142", "v143", "v144", "v145", "v146", "v147", "v148", "v149", "v150", "v151", "v152", "v153", "v154", "v155", "v156", "v157", "v158", "v159", "v160", "v161", "v162", "v163", "v164", "v165", "v166", "v167", "v168", "v169", "v170", "v171", "v172", "v173", "v174", "v175", "v176", "v177", "v178", "v179", "v180", "v181", "v182", "v183", "v184", "v185", "v186", "v187", "v188", "v189", "v190", "v191", "v200", "s45", "vcc", "scc", "memory");
      }
      PHASE_VARS;
      STAMP_PUT(12 + 16 * L);
      STAMPV(15 + 16 * L);
      float s = 0.f, sq = 0.f, d = 0.f;
      const unsigned xdst = (L ? OOB : OFF_X1 + tile * TS + tn * 4096) + lane * 16;
#pragma unroll
      for (int g = 0; g < 8; ++g) {
#pragma unroll
        for (int e = 0; e < 2; ++e) {
          const unsigned rr = res[g >> 1][2 * (g & 1) + e];
          const unsigned t = pack2(acc[g >> 2][4 * (g & 3) + 2 * e], acc[g >> 2][4 * (g & 3) + 2 * e + 1]);
          x1p[g >> 1][2 * (g & 1) + e] = pack2(lo16(rr) + lo16(t), hi16(rr) + hi16(t));
        }
        if (g & 1) { stg(rs, xdst + (g >> 1) * 1024, x1p[g >> 1]); SBAR(); }
      }
#pragma unroll
      for (int g = 0; g < 8; ++g)
#pragma unroll
        for (int e = 0; e < 2; ++e) {
          const unsigned o = x1p[g >> 1][2 * (g & 1) + e];
          s = dot2(o, 0x3f803f80u, s); sq = dot2(o, o, sq); d = dot2(o, wrc[g][e], d);
        }
      pst = u32x4{nzf(half_swap_sum(s)), nzf(half_swap_sum(sq)), nzf(half_swap_sum(d)), 0u};
    }
    STAMPV(16 + 16 * L);
  }
  {
    PHASE_VARS;
    float S, Q, Dd, mean, var, mb1, rd1;
    stg(rs, stats_dst(OFF_SX + SX_LAYER, tile, tn, lane), pst);
    __builtin_amdgcn_s_sleep(10);
    row_stats_sum(rs, OFF_SX + SX_LAYER, tile, lane, S, Q, Dd);
    STAMPV(33);
    row_stats_fin(smem, 2, tile, lane, S, Q, Dd, U2F(words[0]), x1p, mean, var, mb1, rd1);
    STAMPV(34);
    u32x4 xo[4];
#pragma unroll
    for (int i = 0; i < 4; ++i) {
#pragma unroll
      for (int e = 0; e < 4; ++e) {
        const unsigned h2 = ln_pair(x1p[i][e], mb1, rd1);
        xo[i][e] = pack2(lo16(x1p[i][e]) + lo16(h2), hi16(x1p[i][e]) + hi16(h2));
      }
      *reinterpret_cast<u32x4*>(smem + wid * 4096 + r32 * 128 + ((2 * i + hi) ^ (r32 & 7)) * 16) = fo_swap(xo[i]);
    }
    asm volatile("s_waitcnt lgkmcnt(0)" ::: "memory");
#pragma unroll
    for (int i = 0; i < 4; ++i) { xo[i] = *reinterpret_cast<const u32x4*>(smem + wid * 4096 + i * 1024 + (lane >> 3) * 128 + ((lane ^ (lane >> 3)) & 7) * 16); PIN(xo[i]); }
    u16* dst = out + (size_t)(tile * 32 + (lane >> 3)) * D_ + tn * 64 + (lane & 7) * 8;
    lds_wait_ge(lbase + L_W + 4 + 4 * (lane & 1), 2);
    const int rel = (int)words[3] - tile * 32 - (lane >> 3);
#pragma unroll
    for (int i = 0; i < 4; ++i)
      if (rel != 8 * i) st16(dst + (size_t)i * 8 * D_, xo[i]);
    STAMPV(35);
  }
}

extern "C" __global__ __launch_bounds__(512) void prep_kernel(const u16* __restrict__ x0, const u16* __restrict__ wqkv, char* __restrict__ ws) {
  __shared__ __attribute__((aligned(16))) char sm[128 * 144];
  const int tid = threadIdx.x, lane = tid & 63, wid = __builtin_amdgcn_readfirstlane(tid >> 6), b = blockIdx.x;
  if (b >= 256) {
    const int xcd = b & 7, slot = (b - 256) >> 3;
    if (tid < (int)(FL_COUNT / 256)) { u32x4 z = {0u, 0u, 0u, 0u}; st16(ws + OFF_FL + ((b - 256) * (FL_COUNT / 256) + tid) * 128, z); }
    unsigned acc = 0;
    {
      const u16* p = wqkv + (size_t)(slot * 32 + (tid >> 5)) * (3 * D_) + ((xcd & 3) * 4 + ((tid >> 3) & 3)) * 64 + (tid & 7) * 8;
#pragma unroll
      for (int i = 0; i < 6; ++i) {
        const u32x4 v = *reinterpret_cast<const u32x4*>(p + (size_t)(i & 1) * 16 * (3 * D_) + (i >> 1) * D_);
        acc |= v[0] ^ v[3];
      }
    }
    if (slot < 8) {
      const char *c0, *c1;
      CODE_ADDR(mega_kernel, c0); CODE_ADDR(prep_kernel, c1);
      const long long o = (slot * 512 + tid) * 16;
      if (o + 16 <= c1 - c0) acc |= (*reinterpret_cast<const __attribute__((address_space(1))) u32x4*>((uintptr_t)(c0 + o)))[0];
    }
    asm volatile("" :: "v"(acc));
    return;
  }
  u32x4 v[2];
  asm volatile("global_load_dwordx4 %0, %2, off\n\tglobal_load_dwordx4 %1, %2, off offset:1024"
               : "=&v"(v[0]), "=&v"(v[1]) : "v"(x0 + (size_t)(b * 8 + wid) * D_ + lane * 8) : "memory");
  int pass;
  asm volatile("s_mov_b32 %0, %1" : "=s"(pass) : "n"(0));
#pragma unroll 1
  for (;;) {
    asm volatile("s_cmp_eq_u32 %2, 0\n\ts_cbranch_scc1 1f\n\ts_waitcnt vmcnt(0)\n1:" : "+v"(v[0]), "+v"(v[1]) : "s"(pass) : "memory", "scc");
    float s = 0.f;
#pragma unroll
    for (int i = 0; i < 2; ++i)
#pragma unroll
      for (int e = 0; e < 4; ++e) s = dot2(v[i][e], 0x3f803f80u, s);
    const float mean = wave_sum(s) * (1.0f / 1024.0f);
    float m2 = 0.f;
#pragma unroll
    for (int i = 0; i < 2; ++i)
#pragma unroll
      for (int e = 0; e < 4; ++e) { const float a = lo16(v[i][e]) - mean, c = hi16(v[i][e]) - mean; m2 += a * a + c * c; }
    float mb, den;
    ln_scalars(mean, wave_sum(m2) * (1.0f / 1024.0f), mb, den);
    const float rd = 1.0f / den;
#pragma unroll
    for (int i = 0; i < 2; ++i) {
      char* p = sm + ((32 * i + (lane >> 1)) * 2) * 144 + wid * 16 + (lane & 1) * 8;
      u32x2 lo = {ln_pair(v[i][0], mb, rd), ln_pair(v[i][1], mb, rd)}, hi = {ln_pair(v[i][2], mb, rd), ln_pair(v[i][3], mb, rd)};
      *reinterpret_cast<u32x2*>(p) = lo;
      *reinterpret_cast<u32x2*>(p + 144) = hi;
    }
    if (pass) break;
    asm volatile("s_mov_b32 %0, 1" : "=s"(pass));
  }
  __syncthreads();
  char* dst = ws + OFF_HF + (size_t)(b >> 2) * TS + (b & 3) * 128;
#pragma unroll
  for (int i = 0; i < 2; ++i) {
    const int q = i * 512 + tid, pc = q >> 3, tk = q & 7;
    st16(dst + pc * 512 + tk * 16, *reinterpret_cast<const u32x4*>(sm + pc * 144 + tk * 16));
  }
}

extern "C" void kernel_launch(void* const* d_in, const int* in_sizes, int n_in, void* d_out, int out_size,
                              void* d_ws, size_t ws_size, hipStream_t stream) {
  char* ws = (char*)d_ws;
  prep_kernel<<<512, 512, 0, stream>>>((const u16*)d_in[0], (const u16*)d_in[1], ws);
  mega_kernel<<<256, 512, 0, stream>>>((const u16*)d_in[0], (const u16*)d_in[1], (const u16*)d_in[2], (const u16*)d_in[4], (const u16*)d_in[5],
                                       (const u16*)d_in[6], (const u16*)d_in[7], ws, (u16*)d_out);
}
```

```cpp
#include <hip/hip_runtime.h>
#include <hip/hip_bf16.h>
#include <stdint.h>

typedef unsigned short u16;
typedef short s16x4 __attribute__((ext_vector_type(4)));
typedef short s16x8 __attribute__((ext_vector_type(8)));
typedef float f32x16 __attribute__((ext_vector_type(16)));
typedef float f32x8 __attribute__((ext_vector_type(8)));
typedef float f32x4 __attribute__((ext_vector_type(4)));
typedef float f32x2 __attribute__((ext_vector_type(2)));
typedef unsigned int u32x16 __attribute__((ext_vector_type(16)));
typedef unsigned int u32x8 __attribute__((ext_vector_type(8)));
typedef unsigned int u32x4 __attribute__((ext_vector_type(4)));
typedef unsigned int u32x2 __attribute__((ext_vector_type(2)));
typedef __bf16 bf16x2 __attribute__((ext_vector_type(2)));
typedef __bf16 bf16x8 __attribute__((ext_vector_type(8)));
typedef __bf16 bf16x16 __attribute__((ext_vector_type(16)));

#define DI __device__ __forceinline__
#define LDS_PTR(T, p) ((__attribute__((address_space(3))) T*)(p))
#define BARRIER() asm volatile("s_barrier" ::: "memory")
#define BAR_LGKM() asm volatile("s_waitcnt lgkmcnt(0)\n\ts_barrier" ::: "memory")
#define BAR_ALL() asm volatile("s_waitcnt vmcnt(0) lgkmcnt(0)\n\ts_barrier" ::: "memory")
#define DRAIN() asm volatile("s_waitcnt vmcnt(0)" ::: "memory")
#define PACE() ({ asm volatile("s_sleep 1\n\ts_waitcnt vmcnt(3)" ::: "memory"); })
#define MFMA32(a, b, c) __builtin_amdgcn_mfma_f32_32x32x16_bf16(a, b, c, 0, 0, 0)
#define MFMA16(a, b, c) __builtin_amdgcn_mfma_f32_16x16x32_bf16(a, b, c, 0, 0, 0)
#define PIN(x) asm volatile("" : "+v"(x))
#define EX2(x) __builtin_amdgcn_exp2f(x)
#define RCP(x) __builtin_amdgcn_rcpf(x)
#define SBAR() __builtin_amdgcn_sched_barrier(0)

#define STAMP_INIT
#define STAMPV(i)
#define STAMP_END
#define STAMP_DECL
#define STAMP_OUTS
#define STAMP_PUT(i)
constexpr int D_ = 1024, F_ = 3584;

constexpr unsigned MBY = 1u << 20;
constexpr unsigned TS = 65536 + 4096, HS = 262144 + 4096;
constexpr unsigned OFF_HF = 0, OFF_X1 = 5 * MBY;
constexpr unsigned OFF_OF = 10 * MBY;
constexpr unsigned OFF_KV = 20 * MBY;
constexpr unsigned OFF_FL = 40 * MBY;
constexpr unsigned FL_KV = OFF_FL;
constexpr unsigned FL_O = OFF_FL + 262144;
constexpr unsigned FL_COUNT = 4096;
constexpr unsigned OFF_CS = 41 * MBY;
constexpr unsigned OFF_SX = 42 * MBY;
constexpr unsigned SX_ST = 0;
constexpr unsigned SX_ROW = 512 * 1024;
constexpr unsigned SX_CAND = SX_ROW + 32 * 1024;
constexpr unsigned SX_GP = SX_CAND + 32 * 1024;
constexpr unsigned SX_YP = SX_GP + 224 * 1024;
constexpr unsigned SX_LAYER = SX_YP + 224 * 1024;
constexpr unsigned SX_QP = 2 * SX_LAYER;
constexpr unsigned SX_BYTES = SX_QP + 512 * 1024;
static_assert(SX_BYTES <= 5 * 256 * 128 * 16, "sentinel fill");
constexpr unsigned WS_BYTES = 48 * MBY, OOB = 0x80000000u;
constexpr unsigned SENT = 0xFFFFFFFFu, TAG = 1u;

constexpr int NS = 9, DD = 3, SLOTB = 12288;
constexpr int L_HB = 110592, L_XB = L_HB + 2048;
constexpr int L_XCH = 122880;
constexpr int L_CAND = L_XCH + 32768;
constexpr int L_AB = L_XCH + 34816;
constexpr int L_FULL = 159744, L_FREE = L_FULL + 512;
constexpr int L_CS = L_FREE + 1024, L_PATCH = L_CS + 768;
constexpr int L_GU = L_PATCH + 768, L_YS = L_GU + 256, L_WR = L_YS + 256;
constexpr int L_X2F = L_WR + 128, L_X2P = L_X2F + 128;
constexpr int L_W = L_X2P + 128;
constexpr int LDS_BYTES = 163840;
static_assert(NS * SLOTB <= L_HB && L_W + 64 <= LDS_BYTES && L_AB + 2048 <= L_FULL, "LDS map");

DI int fresh_lane() { int l; asm volatile("v_mbcnt_lo_u32_b32 %0, -1, 0\n\tv_mbcnt_hi_u32_b32 %0, -1, %0" : "=v"(l)); return l; }
#define LANE_VARS const int lane = fresh_lane(), r32 = lane & 31, hi = lane >> 5, tq = (lane & 15) >> 2, tp = lane & 3, tblk = (lane >> 4) & 1, \
  wrow = 4 * hi + tq, wsw = 4 * ((wrow >> 1) & 1), wch = 2 * tblk + (tp >> 1), c24 = r32 < 24 ? r32 : 23; \
  (void)r32; (void)hi; (void)wsw; (void)wch; (void)c24
DI int fresh_s(int v) { asm volatile("" : "+s"(v)); return v; }
#define PHASE_VARS const int tm = fresh_s(tm_), tn = fresh_s(tn_), wid = fresh_s(wid_), tile = tm * 4 + wid, hh = wid & 1, kh = wid >> 1; (void)tile; (void)hh; (void)kh; LANE_VARS
DI float bf2f(u16 v) { return __uint_as_float(((unsigned)v) << 16); }
DI unsigned pack2(float lo, float hi) { f32x2 v = {lo, hi}; return __builtin_bit_cast(unsigned, __builtin_convertvector(v, bf16x2)); }
DI float rbf(float f) { return __uint_as_float(pack2(f, 0.f) << 16); }
DI float lo16(unsigned u) { return __uint_as_float(u << 16); }
DI float hi16(unsigned u) { return __uint_as_float(u & 0xffff0000u); }
DI float opq(float r) { asm("" : "+v"(r)); return r; }
DI float fadd(float a, float b) { return opq(a + b); }
DI float fmul(float a, float b) { return opq(a * b); }
DI float ffma(float a, float b, float c) { return opq(__builtin_fmaf(a, b, c)); }
DI float U2F(unsigned u) { return __uint_as_float(u); }
DI float half_swap_sum(float v) {
  auto r = __builtin_amdgcn_permlane32_swap(__float_as_uint(v), __float_as_uint(v), false, false);
  return fadd(U2F(r[0]), U2F(r[1]));
}
DI float half_swap_max(float v) {
  auto r = __builtin_amdgcn_permlane32_swap(__float_as_uint(v), __float_as_uint(v), false, false);
  return fmaxf(__uint_as_float(r[0]), __uint_as_float(r[1]));
}
template <int CTRL> DI unsigned dpp(unsigned v) { return (unsigned)__builtin_amdgcn_update_dpp(0, (int)v, CTRL, 0xf, 0xf, false); }
DI float row_swap_sum(float v) {
  auto r = __builtin_amdgcn_permlane16_swap(__float_as_uint(v), __float_as_uint(v), false, false);
  return fadd(U2F(r[0]), U2F(r[1]));
}
DI float half_sum(float v) {
  v = fadd(v, U2F(dpp<0xB1>(__float_as_uint(v)))); v = fadd(v, U2F(dpp<0x4E>(__float_as_uint(v))));
  v = fadd(v, U2F(dpp<0x141>(__float_as_uint(v)))); v = fadd(v, U2F(dpp<0x140>(__float_as_uint(v))));
  return row_swap_sum(v);
}
DI float wave_sum(float v) { return half_swap_sum(half_sum(v)); }
DI unsigned half_max_u32(unsigned v) {
  v = max(v, dpp<0xB1>(v)); v = max(v, dpp<0x4E>(v)); v = max(v, dpp<0x141>(v)); v = max(v, dpp<0x140>(v));
  auto r = __builtin_amdgcn_permlane16_swap(v, v, false, false);
  return max(r[0], r[1]);
}
DI unsigned wave_max_u32(unsigned v) {
  v = half_max_u32(v);
  auto r = __builtin_amdgcn_permlane32_swap(v, v, false, false);
  return max(r[0], r[1]);
}
DI u32x4 fo_swap(u32x4 v) {
  auto a = __builtin_amdgcn_permlane32_swap(v[0], v[2], false, false);
  auto b = __builtin_amdgcn_permlane32_swap(v[1], v[3], false, false);
  u32x4 o; o[0] = a[0]; o[1] = b[0]; o[2] = a[1]; o[3] = b[1];
  return o;
}
DI void glds16(const void* gsrc, unsigned lds_dst) {
  unsigned keep;
  asm volatile("s_mov_b32 %0, m0\n\ts_mov_b32 m0, %2\n\ts_nop 0\n\tglobal_load_lds_dwordx4 %1, off\n\ts_mov_b32 m0, %0"
               : "=&s"(keep) : "v"(gsrc), "s"(lds_dst) : "memory");
}
#define GL_ "global_load_lds_dwordx4 "
DI void glds_v4(const void* sb, const unsigned (&v)[4], unsigned m0v) {
  unsigned keep;
  asm volatile("s_mov_b32 %0, m0\n\ts_mov_b32 m0, %6\n\ts_nop 0\n\t" GL_ "%1, %5\n\t" GL_ "%2, %5 offset:1024\n\t" GL_ "%3, %5 offset:2048\n\t"
               GL_ "%4, %5 offset:3072\n\ts_mov_b32 m0, %0" : "=&s"(keep) : "v"(v[0]), "v"(v[1]), "v"(v[2]), "v"(v[3]), "s"(sb), "s"(m0v) : "memory");
}
#define GP_ "\n\ts_sleep %8\n\ts_waitcnt vmcnt(%7)\n\t"
template <int TH, int SL> DI void glds_v4t(const void* sb, const unsigned (&v)[4], unsigned m0v) {
  unsigned keep;
  asm volatile("s_mov_b32 %0, m0\n\ts_mov_b32 m0, %6\n\ts_nop 0\n\t" GL_ "%1, %5" GP_ GL_ "%2, %5 offset:1024" GP_ GL_ "%3, %5 offset:2048" GP_ GL_ "%4, %5 offset:3072" GP_ "s_mov_b32 m0, %0"
               : "=&s"(keep) : "v"(v[0]), "v"(v[1]), "v"(v[2]), "v"(v[3]), "s"(sb), "s"(m0v), "n"(TH), "n"(SL) : "memory");
}
DI void glds_v3(const void* sb, const unsigned (&v)[3], unsigned m0v) {
  unsigned keep;
  asm volatile("s_mov_b32 %0, m0\n\ts_mov_b32 m0, %5\n\ts_nop 0\n\t" GL_ "%1, %4\n\t" GL_ "%2, %4 offset:1024\n\t" GL_ "%3, %4 offset:2048\n\t"
               "s_mov_b32 m0, %0" : "=&s"(keep) : "v"(v[0]), "v"(v[1]), "v"(v[2]), "s"(sb), "s"(m0v) : "memory");
}
DI float dot2(unsigned a, unsigned b, float c) {
  return __builtin_amdgcn_fdot2_f32_bf16(__builtin_bit_cast(bf16x2, a), __builtin_bit_cast(bf16x2, b), c, false);
}
#define CODE_ADDR(sym, out) do { unsigned long long pc_; unsigned off_; \
  asm volatile("s_getpc_b64 %0\n1:\n\ts_mov_b32 %1, " #sym "-1b" : "=s"(pc_), "=s"(off_)); out = (const char*)(pc_ + (long long)(int)off_); } while (0)
DI void st16(void* p, u32x4 v) { asm volatile("global_store_dwordx4 %0, %1, off sc0 sc1\n\ts_nop 1" :: "v"(p), "v"(v) : "memory"); }
DI void ln_scalars(float mean, float var, float& mb, float& den) { mb = rbf(mean); den = rbf(__builtin_amdgcn_sqrtf(rbf(rbf(var) + 1e-5f))); }
DI unsigned ln_pair(unsigned v, float mb, float rd) {
  const unsigned c = pack2(lo16(v) - mb, hi16(v) - mb);
  return pack2(lo16(c) * rd, hi16(c) * rd);
}
DI unsigned ln_pair_s(unsigned v, float nmb, float rd) {
  const unsigned c = pack2(fadd(lo16(v), nmb), fadd(hi16(v), nmb));
  return pack2(fmul(lo16(c), rd), fmul(hi16(c), rd));
}
template <int N, int M> DI f32x4 sum4(const u32x4 (&v)[M]) {
  f32x4 t;
#pragma unroll
  for (int e = 0; e < 4; ++e) {
    t[e] = U2F(v[0][e]);
#pragma unroll
    for (int i = 1; i < N; ++i) t[e] = fadd(t[e], U2F(v[i][e]));
  }
  return t;
}
#define PERM_LO(x, y) __builtin_amdgcn_perm(y, x, 0x05040100u)
#define PERM_HI(x, y) __builtin_amdgcn_perm(y, x, 0x07060302u)
constexpr unsigned SPIN_CAP = 1u << 15;
DI unsigned nz(unsigned v) { return min(v, SENT - 1u); }
DI unsigned nzf(float v) { return nz(__float_as_uint(v)); }
DI unsigned mx4(unsigned m, const u32x4& v) { return max(max(max(m, v[0]), v[1]), max(v[2], v[3])); }
template <int N> DI bool none_sent(const u32x4 (&v)[N]) {
  unsigned m = 0u;
#pragma unroll
  for (int i = 0; i < N; ++i) m = mx4(m, v[i]);
  return m != SENT;
}
typedef __amdgpu_buffer_rsrc_t rsrc_t;
DI u32x4 ldg(rsrc_t rs, unsigned off) { return __builtin_amdgcn_raw_buffer_load_b128(rs, (int)off, 0, 16); }
DI void stg(rsrc_t rs, unsigned off, u32x4 v) { __builtin_amdgcn_raw_buffer_store_b128(v, rs, (int)off, 0, 16); }
template <int N, int SLEEP = 1, class LD, class OKF> DI void poll_all(u32x4 (&out)[N], LD ld, OKF okf) {
  for (unsigned spins = 0;; ++spins) {
    ld(out);
    SBAR();
    if (__all(okf(out)) || spins > SPIN_CAP) break;
    asm volatile("s_sleep %0" :: "n"(SLEEP) : "memory");
  }
}
DI void lds_set(unsigned addr, unsigned v) { asm volatile("s_waitcnt lgkmcnt(0)\n\tds_write_b32 %0, %1" :: "v"(addr), "v"(v) : "memory"); }
template <int SLEEP = 1> DI void lds_wait_ge(unsigned addr, unsigned need) {
  unsigned spin = 0x100000, v;
  asm volatile("1:\n\tds_read_b32 %0, %2\n\ts_sub_u32 %1, %1, 1\n\ts_waitcnt lgkmcnt(0)\n\ts_cbranch_scc1 2f\n\tv_cmp_gt_u32 vcc, %3, %0\n\ts_cbranch_vccz 2f\n\ts_sleep %4\n\ts_branch 1b\n2:"
               : "=&v"(v), "+s"(spin) : "v"(addr), "s"(need), "n"(SLEEP) : "vcc", "scc", "memory");
}
DI s16x8 tr2(const char* p, int second) {
  s16x4 lo = __builtin_amdgcn_ds_read_tr16_b64_v4i16(LDS_PTR(s16x4, p)), hi = __builtin_amdgcn_ds_read_tr16_b64_v4i16(LDS_PTR(s16x4, p + second));
  return __builtin_shufflevector(lo, hi, 0, 1, 2, 3, 4, 5, 6, 7);
}
DI u32x16 cat4(const u32x4& a, const u32x4& b, const u32x4& c, const u32x4& d) {
  const u32x8 x = __builtin_shufflevector(a, b, 0, 1, 2, 3, 4, 5, 6, 7), y = __builtin_shufflevector(c, d, 0, 1, 2, 3, 4, 5, 6, 7);
  return __builtin_shufflevector(x, y, 0, 1, 2, 3, 4, 5, 6, 7, 8, 9, 10, 11, 12, 13, 14, 15);
}

DI unsigned stats_dst(unsigned sx, int tile, int tn, int lane) { return lane < 32 ? sx + SX_ST + tile * 8192 + tn * 512 + lane * 16 : OOB; }
DI void row_stats_sum(rsrc_t rs, unsigned sx, int tile, int lane, float& S, float& Q, float& Dd) {
  const unsigned exb = sx + SX_ST + tile * 8192 + (lane & 31) * 16;
  u32x4 pv[8];
  poll_all<8>(pv, [&](u32x4 (&v)[8]) {
#pragma unroll
    for (int i = 0; i < 8; ++i) v[i] = ldg(rs, exb + ((lane >> 5) * 8 + i) * 512);
  }, [&](const u32x4 (&v)[8]) { return none_sent(v); });
  S = 0.f; Q = 0.f; Dd = 0.f;
#pragma unroll
  for (int i = 0; i < 8; ++i) { S += __uint_as_float(pv[i][0]); Q += __uint_as_float(pv[i][1]); Dd += __uint_as_float(pv[i][2]); }
}
DI void row_stats_fin(char* smem, int epoch, int tile, int lane, float S, float Q, float Dd, float wsum, const u32x4 (&x1p)[4],
                      float& mean, float& var, float& mb, float& rd) {
  S = half_swap_sum(S); Q = half_swap_sum(Q); Dd = half_swap_sum(Dd);
  mean = S * (1.0f / 1024.0f); var = fmaxf(Q * (1.0f / 1024.0f) - mean * mean, 0.f);
  float den;
  ln_scalars(mean, var, mb, den);
  rd = RCP(den);
  const unsigned rb = pack2((Dd - mb * wsum) * rd, 0.f) & 0xffffu;
  const unsigned mine = (((rb & 0x8000u) ? (~rb & 0xffffu) : (rb | 0x8000u)) << 16) | (unsigned)(2047 - (tile * 32 + (lane & 31)));
  const bool best = half_max_u32(mine) == mine;
  if (best) {
    char* c = smem + L_CAND + (tile & 3) * 144;
#pragma unroll
    for (int i = 0; i < 4; ++i) *reinterpret_cast<u32x4*>(c + (2 * i + (lane >> 5)) * 16) = x1p[i];
    *reinterpret_cast<u32x4*>(c + 128) = u32x4{mine, __float_as_uint(mb), __float_as_uint(rd), 0u};
  }
  lds_set((unsigned)(uintptr_t)smem + L_W + 32 + (tile & 3) * 4, epoch);
}

DI void attn_safe(rsrc_t rs, unsigned kvoff, const u32x8& q, const u32x4& ones, f32x16& o) {
  const s16x8 q0 = __builtin_bit_cast(s16x8, __builtin_shufflevector(q, q, 0, 1, 2, 3)), q1 = __builtin_bit_cast(s16x8, __builtin_shufflevector(q, q, 4, 5, 6, 7));
  f32x4 l = {0.f, 0.f, 0.f, 0.f};
#pragma unroll
  for (int e = 0; e < 16; ++e) o[e] = 0.f;
  float m = -INFINITY;
#pragma unroll 1
  for (int kt = 0; kt < 64; ++kt) {
    const unsigned p = kvoff + kt * 4096;
    const u32x4 k0 = ldg(rs, p), k1 = ldg(rs, p + 1024), v0 = ldg(rs, p + 2048), v1 = ldg(rs, p + 3072);
    f32x16 s = {};
    s = MFMA32(__builtin_bit_cast(s16x8, k0), q0, s);
    s = MFMA32(__builtin_bit_cast(s16x8, k1), q1, s);
    float mt = fmaxf(s[0], s[1]);
#pragma unroll
    for (int e = 2; e < 16; ++e) mt = fmaxf(mt, s[e]);
    const float mn = fmaxf(m, half_swap_max(mt));
    const float alpha = EX2(m - mn);
    m = mn;
#pragma unroll
    for (int e = 0; e < 16; ++e) { s[e] = EX2(s[e] - mn); o[e] *= alpha; }
    l[0] *= alpha;
    const bf16x16 pa = __builtin_bit_cast(bf16x16, __builtin_shufflevector(s, s, 0, 1, 2, 3, 4, 5, 6, 7));
    const bf16x16 pb = __builtin_bit_cast(bf16x16, __builtin_shufflevector(s, s, 8, 9, 10, 11, 12, 13, 14, 15));
    o = __builtin_amdgcn_smfmac_f32_32x32x32_bf16(__builtin_bit_cast(bf16x8, v0), pa, o, 0xdddd, 0, 0);
    l = __builtin_amdgcn_smfmac_f32_16x16x64_bf16(__builtin_bit_cast(bf16x8, ones), pa, l, 0xdddd, 0, 0);
    o = __builtin_amdgcn_smfmac_f32_32x32x32_bf16(__builtin_bit_cast(bf16x8, v1), pb, o, 0xdddd, 0, 0);
    l = __builtin_amdgcn_smfmac_f32_16x16x64_bf16(__builtin_bit_cast(bf16x8, ones), pb, l, 0xdddd, 0, 0);
  }
  o *= 1.0f / l[0];
}

extern "C" __global__ __launch_bounds__(512) void mega_kernel(const u16* __restrict__ x0, const u16* __restrict__ wqkv, const u16* __restrict__ wo,
                                                              const u16* __restrict__ w_r, const u16* __restrict__ wg, const u16* __restrict__ wu,
                                                              const u16* __restrict__ wd, char* ws, u16* __restrict__ out) {
  __shared__ __attribute__((aligned(16))) char smem[LDS_BYTES];
  const int wid = __builtin_amdgcn_readfirstlane(threadIdx.x >> 6);
  STAMP_INIT;
  STAMPV(0);
  const int bid = blockIdx.x, xcd = bid & 7, loc = bid >> 3;
  const int tm = 8 * (xcd >> 2) + (loc >> 2), tn = 4 * (xcd & 3) + (loc & 3);
  const unsigned lbase = (unsigned)(uintptr_t)smem;
  const rsrc_t rs = __builtin_amdgcn_make_buffer_rsrc(ws, 0, (int)WS_BYTES, 0x00020000);
  volatile __attribute__((address_space(3))) unsigned* words = LDS_PTR(volatile unsigned, smem + L_W);
  if (wid >= 4) {
    auto wo_rows = [&](int g0, int n, int gs) {
      const int lane = fresh_lane();
      unsigned wov[4];
#pragma unroll
      for (int i = 0; i < 4; ++i) wov[i] = (unsigned)((8 * i + (lane >> 3)) * (D_ * 2) + ((lane & 7) ^ (4 * ((lane >> 4) & 1))) * 16 - i * 1024);
#pragma unroll 1
      for (int i = 0, k = (2 * (tm & 7)) % n, g; i < n && !0; ++i, k = k + 1 == n ? 0 : k + 1) g = g0 + gs * k, glds_v4t<3, 1>(reinterpret_cast<const char*>(wo) + (size_t)((64 * tn + 32 * g) & 1023) * (D_ * 2) + tn * 128, wov, lbase + g * 4096);
    };
    if (wid < 6) {
      const int lw = wid - 4, lane = fresh_lane();
      __builtin_amdgcn_s_setprio(3);
      const char* wb = reinterpret_cast<const char*>(wqkv + (size_t)(16 * lw) * (3 * D_) + tn * 64);
      unsigned wv[3];
#pragma unroll
      for (int i = 0; i < 3; ++i) {
        const int li = lane + 64 * i, k = li / 24, cp = li % 24, c = cp ^ (4 * ((k >> 1) & 1));
        wv[i] = (unsigned)(k * (3 * D_ * 2) + (c >> 3) * (D_ * 2) + (c & 7) * 16 - i * 1024);
      }
      auto stage = [&](int s, int slot) {
        const char* p = wb + (size_t)((s + 2 * tn) & 31) * (32 * 3 * D_ * 2);
        glds_v3(p, wv, lbase + slot * SLOTB + lw * 6144);
        glds_v3(p + 8 * (3 * D_ * 2), wv, lbase + slot * SLOTB + lw * 6144 + 3072);
      };
      const unsigned fra = lbase + L_FREE + (lane & 3) * 256, fla = lbase + L_FULL + lw * 256 + lane * 4;
      const rsrc_t rw = __builtin_amdgcn_make_buffer_rsrc(const_cast<char*>(wb), 0, 0 ? 0 : 6 * MBY - (16 * lw * 3 * D_ + tn * 64) * 2, 0x00020000);
#pragma unroll 1
      for (int L = 0; L < 2; ++L) {
        const int s0 = L ? NS : 0, lead = L ? 8 : NS - 1;
        int slot = 0;
#pragma unroll 1
        for (int s = s0; s < 32; ++s) {
          if (s >= NS) lds_wait_ge(fra, 32 * L + s - lead);
          stage(s, slot);
          if (s == 0) BARRIER();
          slot = (slot == NS - 1) ? 0 : slot + 1;
          if (s >= s0 + DD) asm volatile("s_waitcnt vmcnt(%0)\n\tds_write_b32 %1, %2" :: "n"(DD * 6), "v"(fla), "v"(32 * L + s - DD + 1) : "memory");
        }
#pragma unroll
        for (int d = DD - 1; d >= 0; --d) asm volatile("s_waitcnt vmcnt(%0)\n\tds_write_b32 %1, %2" :: "n"(d * 6), "v"(fla), "v"(32 * L + 32 - d) : "memory");
        BAR_LGKM();
        lds_wait_ge<8>(fra, 32 * L + 32);
        __builtin_amdgcn_s_sleep(16);
        wo_rows(lw, 15, 2);
        u32x4 pre[NS * 6];
        if (L == 0) {
#pragma unroll
          for (int s = 0; s < NS; ++s) {
#pragma unroll
            for (int i = 0; i < 6; ++i)
              pre[s * 6 + i] = __builtin_amdgcn_raw_buffer_load_b128(rw, wv[i % 3], ((s + 2 * tn) & 31) * (32 * 3 * D_ * 2) + (i / 3) * (8 * 3 * D_ * 2) + (i % 3) * 1024, 0), PACE();
          }
        } else {
#pragma unroll
          for (int i = 0; i < NS * 6; ++i) asm volatile("" : "=v"(pre[i]));
        }
        BAR_ALL();
        wo_rows(30 + lw, 1, 2);
        BAR_ALL();
        if (L == 0) {
          char* d = smem + lw * 6144 + fresh_lane() * 16;
#pragma unroll
          for (int s = 0; s < NS; ++s) {
#pragma unroll
            for (int i = 0; i < 6; ++i) *reinterpret_cast<u32x4*>(d + s * SLOTB + i * 1024) = pre[s * 6 + i];
            lds_set(fla, 32 + s + 1);
          }
        }
      }
      return;
    }
    const int a = wid - 6;
    const bool ffn = loc < 28;
    const int grp = xcd * 7 + (ffn ? loc >> 2 : 0), qa = (loc & 3) * 2 + a;
    const int kr = loc, cg = xcd;
    const unsigned wa = lbase + L_W;
    BARRIER();
    auto transpose8 = [](u32x4* v) {
      u32x4 t[8];
#pragma unroll
      for (int j = 0; j < 8; ++j)
#pragma unroll
        for (int e = 0; e < 4; ++e) t[j][e] = (j & 1) ? PERM_HI(v[2 * e][j >> 1], v[2 * e + 1][j >> 1]) : PERM_LO(v[2 * e][j >> 1], v[2 * e + 1][j >> 1]);
#pragma unroll
      for (int j = 0; j < 8; ++j) { v[j] = t[j]; PIN(v[j]); }
    };
    u32x4 gf[32];
    u32x4 wdf[16];
    auto wait_go = [&](int L) {
      lds_wait_ge<8>(lbase + L_FREE + (fresh_lane() & 3) * 256, 32 * L + 32);
      __builtin_amdgcn_s_sleep(16);
    };
    auto load_wd = [&](unsigned dv) {
      const rsrc_t rd = __builtin_amdgcn_make_buffer_rsrc(const_cast<u16*>(wd) + (size_t)grp * 64 * D_ + 128 * qa, 0, 0 ? 0 : 64 * D_ * 2 - 256 * qa, 0x00020000);
#pragma unroll
      for (int i = 0; i < 16; ++i) {
        wdf[i] = __builtin_amdgcn_raw_buffer_load_b128(rd, dv, (32 * (i >> 3) + (i & 7)) * (D_ * 2), 0);
        asm volatile("s_sleep 2\n\ts_waitcnt vmcnt(3)" ::: "memory");
      }
    };
    {
      LANE_VARS;
      const u32x4 sv = {SENT, SENT, SENT, SENT};
#pragma unroll
      for (int i = 0; i < 5; ++i) {
        const unsigned o = ((i * 256 + bid) * 128 + a * 64 + lane) * 16;
        stg(rs, o < SX_BYTES ? OFF_SX + o : OOB, sv);
      }
      if (a == 0) {
        const u32x4 w0 = *reinterpret_cast<const u32x4*>(w_r + lane * 8), w1 = *reinterpret_cast<const u32x4*>(w_r + 512 + lane * 8);
        float t = 0.f;
#pragma unroll
        for (int e = 0; e < 4; ++e) t = fadd(fadd(t, fadd(lo16(w0[e]), hi16(w0[e]))), fadd(lo16(w1[e]), hi16(w1[e])));
        words[0] = __float_as_uint(wave_sum(t));
        if ((lane >> 3) == (tn & 7)) *reinterpret_cast<u32x4*>(smem + L_WR + (lane & 7) * 16) = tn < 8 ? w0 : w1;
      }
      const u16* p = wqkv + (size_t)(64 * tm + 32 * a + hi) * (3 * D_) + (c24 >> 3) * D_ + tn * 64 + (c24 & 7) * 8;
      float cs[8] = {0.f, 0.f, 0.f, 0.f, 0.f, 0.f, 0.f, 0.f};
      u32x4 v[16];
#pragma unroll
      for (int i = 0; i < 16; ++i) v[i] = *reinterpret_cast<const u32x4*>(p + (size_t)i * (2 * 3 * D_));
      SBAR();
#pragma unroll
      for (int i = 0; i < 16; ++i)
#pragma unroll
        for (int e = 0; e < 4; ++e) { cs[2 * e] = fadd(cs[2 * e], lo16(v[i][e])); cs[2 * e + 1] = fadd(cs[2 * e + 1], hi16(v[i][e])); }
      u32x4 o;
#pragma unroll
      for (int e = 0; e < 4; ++e) { const float x = half_swap_sum(cs[e]), y = half_swap_sum(cs[4 + e]); o[e] = __float_as_uint(hi ? y : x); }
      stg(rs, r32 < 24 ? OFF_CS + (tn * 32 + tm * 2 + a) * 768 + r32 * 32 + hi * 16 : OOB, o);
    }
    {
      BAR_ALL();
      LANE_VARS;
      const char* gp = reinterpret_cast<const char*>(((lane & 8) ? wu : wg) + (size_t)(128 * qa + 16 * hi + 4 * tblk) * F_ + 64 * grp + (lane & 7) * 8);
      unsigned dv = (lane >> 4) * (8 * D_ * 2) + (lane & 15) * 16;
      PIN(gp); PIN(dv);
      wait_go(0);
#pragma unroll
      for (int i = 0; i < 32; ++i) {
        gf[i] = *reinterpret_cast<const u32x4*>(gp + (size_t)(0 ? 0 : 32 * (i >> 3) + 8 * ((i >> 2) & 1) + (i & 3)) * (F_ * 2));
        asm volatile("s_sleep 2\n\ts_waitcnt vmcnt(3)" ::: "memory");
      }
      load_wd(dv);
      STAMPV(7);
      BARRIER();
      transpose8(gf); transpose8(gf + 8); transpose8(gf + 16); transpose8(gf + 24);
      transpose8(wdf); transpose8(wdf + 8);
      {
        LANE_VARS;
        f32x4 t[2];
#pragma unroll
        for (int c = 0; c < 2; ++c) {
          u32x4 v[8];
#pragma unroll
          for (int i = 0; i < 8; ++i) v[i] = ldg(rs, OFF_CS + (tn * 32 + 16 * c + 2 * i + hi) * 768 + a * 384 + c24 * 16);
          SBAR();
          t[c] = sum4<8>(v);
          SBAR();
        }
#pragma unroll
        for (int e = 0; e < 4; ++e) t[0][e] = half_swap_sum(fadd(t[0][e], t[1][e]));
        if (lane < 24) *reinterpret_cast<f32x4*>(smem + L_CS + a * 384 + lane * 16) = t[0];
      }
      BAR_LGKM();
    }
#pragma unroll 1
    for (int L = fresh_s(0);; ++L) {
      const unsigned sx = OFF_SX + L * SX_LAYER;
      STAMPV(1 + 16 * L);
      LANE_VARS;
      unsigned cdst = lane < 8 ? sx + SX_ROW + tm * 2048 + tn * 128 + lane * 16 : sx + SX_CAND + (tn * 16 + tm) * 128;
      cdst = (a == 0 && lane < 9) ? cdst : OOB;
      PIN(cdst);
      lds_wait_ge(wa + 32 + 4 * tp, L + 1);
      unsigned key;
      {
        const unsigned k4 = *reinterpret_cast<const unsigned*>(smem + L_CAND + tp * 144 + 128);
        key = max(k4, dpp<0xB1>(k4)); key = max(key, dpp<0x4E>(key));
        const u32x4 c = *reinterpret_cast<const u32x4*>(smem + L_CAND + (((2047 - (int)(key & 2047u)) >> 5) & 3) * 144 + (lane < 8 ? lane : 8) * 16);
        const u32x4 o = {nz(c[0]), nz(c[1]), nz(c[2]), nz(c[3])};
        stg(rs, cdst, o);
      }
      STAMPV(8 + 16 * L);
      __builtin_amdgcn_s_sleep(10);
      u32x4 v[2];
      const unsigned rpc = (tblk ? 4 * kr + tp : 16 * qa + (lane & 15)) * 16;
      poll_all<2>(v, [&](u32x4 (&t)[2]) {
        t[0] = ldg(rs, sx + SX_CAND + (tn * 16 + (lane & 15)) * 128);
        t[1] = ldg(rs, sx + SX_ROW + ((2047 - (int)(key & 2047u)) >> 7) * 2048 + rpc);
        t[0][3] = key;
      }, [&](const u32x4 (&t)[2]) {
        const bool ok = mx4(0u, t[0]) != SENT;
        unsigned k = ok ? t[0][0] : 0u;
        k = max(k, dpp<0xB1>(k)); k = max(k, dpp<0x4E>(k)); k = max(k, dpp<0x141>(k)); k = max(k, dpp<0x140>(k));
        key = max(key, k);
        return ok & (mx4(0u, t[1]) != SENT) & (key == t[0][3]);
      });
      key = __builtin_amdgcn_readfirstlane(key);
      const int idx = 2047 - (int)(key & 2047u);
      const bool owner = (idx >> 7) == tm;
      STAMPV(2 + 16 * L);
      {
        const float mb = __uint_as_float(__builtin_amdgcn_readlane(v[0][1], idx >> 7)), rd = __uint_as_float(__builtin_amdgcn_readlane(v[0][2], idx >> 7));
        u32x4 hb;
#pragma unroll
        for (int e = 0; e < 4; ++e) hb[e] = ln_pair_s(v[1][e], -mb, rd);
        *reinterpret_cast<u32x4*>(smem + L_HB + a * 1024 + lane * 16) = hb;
        *reinterpret_cast<u32x4*>(smem + L_XB + a * 1024 + lane * 16) = v[1];
        words[3] = (unsigned)idx;
        if (L == (1 ? 1 : 5) || (L == 0 && !owner)) lds_set(wa + 4 + 4 * a, L + 1);
      }
      auto pick2 = [&](const f32x4 (&d)[8]) {
        u32x2 o;
#pragma unroll
        for (int e = 0; e < 2; ++e) {
          const float x = tblk ? d[2 + e][0] : d[e][0], y = tblk ? d[6 + e][0] : d[4 + e][0];
          o[e] = nzf(hi ? y : x);
        }
        return o;
      };
      if (ffn) {
        const f32x4 zero = {0.f, 0.f, 0.f, 0.f};
        const unsigned ppos = (lane & 15) * 32 + (lane >> 4) * 8;
        f32x4 d[8];
        {
          s16x8 hf[4];
#pragma unroll
          for (int t = 0; t < 4; ++t) hf[t] = *reinterpret_cast<const s16x8*>(smem + L_HB + a * 1024 + (lane >> 4) * 16 + t * 64);
          SBAR();
          __builtin_amdgcn_s_setprio(3);
#pragma unroll
          for (int t = 0; t < 32; ++t) d[t & 7] = MFMA16(hf[t >> 3], __builtin_bit_cast(s16x8, gf[t]), t < 8 ? zero : d[t & 7]);
          SBAR();
          __builtin_amdgcn_s_setprio(0);
          __builtin_amdgcn_raw_buffer_store_b64(pick2(d), rs, sx + SX_GP + (grp * 8 + qa) * 512 + ppos, 0, 16);
        }
        STAMPV(9 + 16 * L);
        __builtin_amdgcn_s_sleep(10);
        u32x2 gu[8];
        for (unsigned spins = 0;; ++spins) {
#pragma unroll
          for (int i = 0; i < 8; ++i) gu[i] = __builtin_amdgcn_raw_buffer_load_b64(rs, sx + SX_GP + (grp * 8 + i) * 512 + lane * 8, 0, 16);
          SBAR();
          unsigned m = 0u;
#pragma unroll
          for (int i = 0; i < 8; ++i) m = max(max(m, gu[i][0]), gu[i][1]);
          if (__all(m != SENT) || spins > SPIN_CAP) break;
          asm volatile("s_sleep 1" ::: "memory");
        }
        STAMPV(10 + 16 * L);
        float t0 = opq(U2F(gu[0][0])), t1 = opq(U2F(gu[0][1]));
#pragma unroll
        for (int i = 1; i < 8; ++i) { t0 = fadd(t0, U2F(gu[i][0])); t1 = fadd(t1, U2F(gu[i][1])); }
        auto sw = __builtin_amdgcn_permlane32_swap(__float_as_uint(t0), __float_as_uint(t1), false, false);
        const float gv = rbf(U2F(sw[0])), uv = rbf(U2F(sw[1]));
        reinterpret_cast<u16*>(smem + L_GU + a * 128)[2 * r32 + hi] = (u16)pack2(fmul(rbf(fmul(gv, RCP(fadd(1.0f, __expf(-gv))))), uv), 0.f);
        asm volatile("s_waitcnt lgkmcnt(0)" ::: "memory");
        const s16x8 a0 = *reinterpret_cast<const s16x8*>(smem + L_GU + a * 128 + (lane >> 4) * 16), a1 = *reinterpret_cast<const s16x8*>(smem + L_GU + a * 128 + 64 + (lane >> 4) * 16);
        SBAR();
        __builtin_amdgcn_s_setprio(3);
#pragma unroll
        for (int t = 0; t < 16; ++t) d[t & 7] = MFMA16(t < 8 ? a0 : a1, __builtin_bit_cast(s16x8, wdf[t]), t < 8 ? zero : d[t & 7]);
        SBAR();
        __builtin_amdgcn_s_setprio(0);
        __builtin_amdgcn_raw_buffer_store_b64(pick2(d), rs, sx + SX_YP + grp * 4096 + qa * 512 + ppos, 0, 16);
      }
      STAMPV(3 + 16 * L);
      if (L == 1) lds_set(wa + 4 + 4 * a, 2);
      if (L == 1 && (cg != (kr & 7) || a)) { STAMP_END; __builtin_amdgcn_endpgm(); }
      u32x4 wpf[12];
      if (L == 0) {
        const u16* p = wqkv + (size_t)(32 * kr + 16 * hi + 4 * tblk) * (3 * D_) + (2 * cg + a) * 64 + (lane & 15) * 4;
        u32x2 v[24];
#pragma unroll
        for (int i = 0; i < 24; ++i) v[i] = *reinterpret_cast<const u32x2*>(p + (size_t)(8 * ((i >> 2) & 1) + (i & 3)) * (3 * D_) + (i >> 3) * D_);
        SBAR();
#pragma unroll
        for (int t = 0; t < 12; ++t) {
#pragma unroll
          for (int e = 0; e < 4; ++e) {
            const unsigned x = v[8 * (t >> 2) + 2 * e][(t >> 1) & 1], y = v[8 * (t >> 2) + 2 * e + 1][(t >> 1) & 1];
            wpf[t][e] = (t & 1) ? PERM_HI(x, y) : PERM_LO(x, y);
          }
          PIN(wpf[t]);
        }
      } else {
#pragma unroll
        for (int i = 0; i < 12; ++i) asm volatile("" : "=v"(wpf[i]));
        __builtin_amdgcn_s_sleep(10);
      }
      const unsigned rk = key >> 16;
      float rsig = rbf(RCP(fadd(1.0f, __expf(-lo16((rk & 0x8000u) ? (rk & 0x7fffu) : (~rk & 0xffffu))))));
      PIN(rsig);
      const int n = 4 * (lane & 7) + ((lane >> 3) & 3);
      const int fo = ((n >> 4) * 2 + ((n >> 2) & 1)) * 8 + ((n >> 3) & 1) * 4 + (n & 3);
      float xv = bf2f(reinterpret_cast<const u16*>(smem + L_XB + a * 1024 + 256)[fo]), hv = bf2f(reinterpret_cast<const u16*>(smem + L_HB + a * 1024 + 256)[fo]);
      PIN(xv); PIN(hv);
      u32x4 pv[7];
      poll_all<7, 4>(pv, [&](u32x4 (&t)[7]) {
#pragma unroll
        for (int i = 0; i < 7; ++i) t[i] = ldg(rs, sx + SX_YP + (8 * i + (lane >> 3)) * 4096 + kr * 128 + (lane & 7) * 16);
      }, [&](const u32x4 (&t)[7]) { return none_sent(t); });
      STAMPV(4 + 16 * L);
      float ys;
      {
        f32x4 t = sum4<7>(pv);
#pragma unroll
        for (int e = 0; e < 4; ++e) {
          t[e] = fadd(t[e], U2F(dpp<0x128>(__float_as_uint(t[e]))));
          t[e] = half_swap_sum(row_swap_sum(t[e]));
        }
        const float x = (lane & 8) ? t[1] : t[0], y = (lane & 8) ? t[3] : t[2];
        ys = tblk ? y : x;
      }
      const unsigned ob = pack2(fadd(xv, rbf(fadd(hv, rbf(fmul(rbf(ys), rsig))))), 0.f) & 0xffffu;
      if (L == 1) {
        if (lane < 32) out[(size_t)idx * D_ + kr * 32 + n] = (u16)ob;
        STAMPV(5 + 16 * L);
        STAMP_END;
        __builtin_amdgcn_endpgm();
      }
      reinterpret_cast<u16*>(smem + L_X2F + a * 64)[fo] = (u16)ob;
      asm volatile("s_waitcnt lgkmcnt(0)" ::: "memory");
      {
        const s16x8 xa = *reinterpret_cast<const s16x8*>(smem + L_X2F + a * 64 + (lane >> 4) * 16);
        const u32x4 xf = *reinterpret_cast<const u32x4*>(smem + L_X2F + a * 64 + tp * 16);
        f32x4 z[12];
        const f32x4 zero = {0.f, 0.f, 0.f, 0.f};
        SBAR();
        __builtin_amdgcn_s_setprio(3);
#pragma unroll
        for (int t = 0; t < 12; ++t) z[t] = MFMA16(xa, __builtin_bit_cast(s16x8, wpf[t]), zero);
        SBAR();
        __builtin_amdgcn_s_setprio(0);
        const float x2 = lo16(ob), s1 = half_sum(x2), s2 = half_sum(fmul(x2, x2));
        u32x4 o;
#pragma unroll
        for (int e = 0; e < 4; ++e) {
          const unsigned sp = lane < 52 ? (e == 0 ? __float_as_uint(s1) : __float_as_uint(s2)) : xf[e];
          const unsigned x = __float_as_uint(tblk ? z[4 + e][0] : z[e][0]), y = tblk ? sp : __float_as_uint(z[8 + e][0]);
          o[e] = nz(hi ? y : x);
        }
        const unsigned pos = lane < 24 ? lane * 16 : lane < 48 ? lane * 16 + 128 : lane < 52 ? (lane & 1) * 512 + 384 : 400 + tp * 16;
        stg(rs, lane < 56 ? OFF_SX + SX_QP + ((2 * cg + a) * 32 + kr) * 1024 + pos : OOB, o);
      }
      STAMPV(5 + 16 * L);
      if (owner) {
        __builtin_amdgcn_s_sleep(10);
        const int c25 = r32 < 25 ? r32 : 24;
        u32x4 qv[17];
        poll_all<17, 4>(qv, [&](u32x4 (&t)[17]) {
#pragma unroll
          for (int i = 0; i < 16; ++i) t[i] = ldg(rs, OFF_SX + SX_QP + (tn * 32 + 2 * i + hi) * 1024 + a * 512 + c25 * 16);
          t[16] = ldg(rs, OFF_SX + SX_QP + (tn * 34 + hi) * 1024 + 400 + tp * 16);
        }, [&](const u32x4 (&t)[17]) { return none_sent(t); });
        *reinterpret_cast<u32x4*>(smem + L_X2P + hi * 64 + tp * 16) = qv[16];
        f32x4 t = sum4<16>(qv);
#pragma unroll
        for (int e = 0; e < 4; ++e) t[e] = half_swap_sum(t[e]);
        const float mean = fmul(U2F(__builtin_amdgcn_readlane(__float_as_uint(t[0]), 24)), 1.0f / 1024.0f);
        const float var = fmaxf(ffma(-mean, mean, fmul(U2F(__builtin_amdgcn_readlane(__float_as_uint(t[1]), 24)), 1.0f / 1024.0f)), 0.f);
        float mb, den;
        ln_scalars(mean, var, mb, den);
        const float rd = RCP(den);
        if (lane < 24) {
          const f32x4 cs = *reinterpret_cast<const f32x4*>(smem + L_CS + a * 384 + lane * 16);
#pragma unroll
          for (int e = 0; e < 4; ++e) t[e] = fmul(ffma(-mb, cs[e], t[e]), rd);
          *reinterpret_cast<f32x4*>(smem + L_PATCH + a * 384 + lane * 16) = t;
        }
        lds_set(wa + 4 + 4 * a, L + 1);
      }
      STAMPV(6 + 16 * L);
      BAR_ALL();
      unsigned dv = (lane >> 4) * (8 * D_ * 2) + (lane & 15) * 16;
      PIN(dv);
      wait_go(1);
      load_wd(dv);
      STAMPV(23);
      BARRIER();
      transpose8(wdf); transpose8(wdf + 8);
      BARRIER();
    }
  }
  const int tm_ = tm, tn_ = tn, wid_ = wid;
  *reinterpret_cast<u32x2*>(smem + L_FULL + threadIdx.x * 8) = u32x2{0u, 0u};
  if (threadIdx.x < 16) words[threadIdx.x] = 0u;
  const u32x4 srd = {(unsigned)(uintptr_t)ws, (unsigned)((uintptr_t)ws >> 32) & 0xffffu, WS_BYTES, 0x00020000u};
  u32x4 x1p[4];
  u32x4 pst;
  asm volatile("" : "=v"(x1p[0]), "=v"(x1p[1]), "=v"(x1p[2]), "=v"(x1p[3]), "=v"(pst));
#pragma unroll 1
  for (int L = 0; L < 2; ++L) {
    const unsigned sx = OFF_SX + L * SX_LAYER;
    u32x8 q[4];
    u32x16 kva, kvb;
    {
      f32x16 acc[6];
      u32x16 stA, stB, xr0, xr1, xr2, xr3;
      u32x4 stC;
      unsigned sdst;
      asm volatile("" : "={v[112:127]}"(stB), "={v[128:131]}"(stC));
      { PHASE_VARS;
      stB[8] = lane * 16 + (L ? OFF_X1 : OFF_HF) + tile * TS;
      stB[9] = lbase + wrow * 384 + (((4 * hh + wch) ^ wsw) << 4) + 8 * (tp & 1);
      stB[10] = lbase + wrow * 384 + (((4 * (hh ^ 1) + wch) ^ wsw) << 4) + 8 * (tp & 1);
      stB[13] = lbase + L_FULL + (lane & 1) * 256;
      stB[15] = lbase + L_FREE + wid * 256 + lane * 4;
      stC[0] = 32 * L; sdst = stats_dst(OFF_SX, tile, tn, lane); }
      unsigned soff, slot, need = 32 * L + 1;
      if (L == 0) {
        soff = tn * 4096;
        asm volatile(
          "buffer_load_dwordx4 v[132:135], v120, s[40:43], s44 offen offset:0 sc1\n\t"
          "buffer_load_dwordx4 v[136:139], v120, s[40:43], s44 offen offset:1024 sc1\n\t"
          "buffer_load_dwordx4 v[140:143], v120, s[40:43], s44 offen offset:2048 sc1\n\t"
          "buffer_load_dwordx4 v[144:147], v120, s[40:43], s44 offen offset:3072 sc1\n\t"
          "s_add_u32 s44, s44, 0x1000\n\t"
          "s_and_b32 s44, s44, 0xffff\n\t"
          "buffer_load_dwordx4 v[148:151], v120, s[40:43], s44 offen offset:0 sc1\n\t"
          "buffer_load_dwordx4 v[152:155], v120, s[40:43], s44 offen offset:1024 sc1\n\t"
          "buffer_load_dwordx4 v[156:159], v120, s[40:43], s44 offen offset:2048 sc1\n\t"
          "buffer_load_dwordx4 v[160:163], v120, s[40:43], s44 offen offset:3072 sc1\n\t"
          "s_add_u32 s44, s44, 0x1000\n\t"
          "s_and_b32 s44, s44, 0xffff\n\t"
          "buffer_load_dwordx4 v[164:167], v120, s[40:43], s44 offen offset:0 sc1\n\t"
          "buffer_load_dwordx4 v[168:171], v120, s[40:43], s44 offen offset:1024 sc1\n\t"
          "buffer_load_dwordx4 v[172:175], v120, s[40:43], s44 offen offset:2048 sc1\n\t"
          "buffer_load_dwordx4 v[176:179], v120, s[40:43], s44 offen offset:3072 sc1\n\t"
          "s_add_u32 s44, s44, 0x1000\n\t"
          "s_and_b32 s44, s44, 0xffff\n\t"
          "buffer_load_dwordx4 v[180:183], v120, s[40:43], s44 offen offset:0 sc1\n\t"
          "buffer_load_dwordx4 v[184:187], v120, s[40:43], s44 offen offset:1024 sc1\n\t"
          "buffer_load_dwordx4 v[188:191], v120, s[40:43], s44 offen offset:2048 sc1\n\t"
          "buffer_load_dwordx4 v[192:195], v120, s[40:43], s44 offen offset:3072 sc1\n\t"
          : "={v[132:147]}"(xr0), "={v[148:163]}"(xr1), "={v[164:179]}"(xr2), "={v[180:195]}"(xr3), "+{s44}"(soff) : "{v[112:127]}"(stB), "{s[40:43]}"(srd) : "scc", "memory");
        BAR_LGKM();
      } else {
        xr0 = cat4(x1p[0], x1p[1], x1p[2], x1p[3]);
        asm volatile("" : "={v[148:163]}"(xr1), "={v[164:179]}"(xr2), "={v[180:195]}"(xr3));
      }
      STAMPV(1 + 16 * L);
      asm volatile(
        "v_mov_b32 v123, v121\n\t"
        "v_mov_b32 v124, v122\n\t"
        "s_mov_b64 vcc, -1\n\t"
        "s_cbranch_vccnz 10f\n\t"
        "11:\n\t"
        "s_mov_b32 s46, 12288\n\t"
        "s_add_u32 s47, s47, 1\n\t"
        "ds_read_b64_tr_b16 v[96:97], v123 offset:0\n\t"
        "ds_read_b64_tr_b16 v[98:99], v123 offset:3072\n\t"
        "ds_read_b64_tr_b16 v[100:101], v124 offset:0\n\t"
        "ds_read_b64_tr_b16 v[102:103], v124 offset:3072\n\t"
        "ds_read_b64_tr_b16 v[104:105], v123 offset:128\n\t"
        "ds_read_b64_tr_b16 v[106:107], v123 offset:3200\n\t"
        "ds_read_b64_tr_b16 v[108:109], v124 offset:128\n\t"
        "ds_read_b64_tr_b16 v[110:111], v124 offset:3200\n\t"
        "ds_read_b64_tr_b16 v[112:113], v123 offset:256\n\t"
        "ds_read_b64_tr_b16 v[114:115], v123 offset:3328\n\t"
        "ds_write_b32 v127, v128\n\t"
        "ds_read_b64_tr_b16 v[116:117], v124 offset:256\n\t"
        "ds_read_b64_tr_b16 v[118:119], v124 offset:3328\n\t"
        "s_waitcnt vmcnt(15) lgkmcnt(11)\n\t"
        "v_mfma_f32_32x32x16_bf16 v[0:15], v[96:99], v[132:135], 0\n\t"
        "ds_read_b32 v126, v125\n\t"
        "ds_read_b64_tr_b16 v[96:97], v123 offset:6144\n\t"
        "ds_read_b64_tr_b16 v[98:99], v123 offset:9216\n\t"
        "s_waitcnt lgkmcnt(12)\n\t"
        "v_mfma_f32_32x32x16_bf16 v[16:31], v[100:103], v[132:135], 0\n\t"
        "v_add_u32 v129, s46, v121\n\t"
        "ds_read_b64_tr_b16 v[100:101], v124 offset:6144\n\t"
        "ds_read_b64_tr_b16 v[102:103], v124 offset:9216\n\t"
        "s_waitcnt lgkmcnt(12)\n\t"
        "v_mfma_f32_32x32x16_bf16 v[32:47], v[104:107], v[132:135], 0\n\t"
        "ds_read_b64_tr_b16 v[104:105], v123 offset:6272\n\t"
        "ds_read_b64_tr_b16 v[106:107], v123 offset:9344\n\t"
        "s_waitcnt lgkmcnt(12)\n\t"
        "v_mfma_f32_32x32x16_bf16 v[48:63], v[108:111], v[132:135], 0\n\t"
        "v_add_u32 v130, s46, v122\n\t"
        "ds_read_b64_tr_b16 v[108:109], v124 offset:6272\n\t"
        "ds_read_b64_tr_b16 v[110:111], v124 offset:9344\n\t"
        "s_waitcnt lgkmcnt(12)\n\t"
        "v_mfma_f32_32x32x16_bf16 v[64:79], v[132:135], v[112:115], 0\n\t"
        "ds_read_b64_tr_b16 v[112:113], v123 offset:6400\n\t"
        "ds_read_b64_tr_b16 v[114:115], v123 offset:9472\n\t"
        "s_waitcnt lgkmcnt(10)\n\t"
        "v_mfma_f32_32x32x16_bf16 v[80:95], v[132:135], v[116:119], 0\n\t"
        "v_cmp_gt_u32 vcc, s47, v126\n\t"
        "ds_read_b64_tr_b16 v[116:117], v124 offset:6400\n\t"
        "ds_read_b64_tr_b16 v[118:119], v124 offset:9472\n\t"
        "s_waitcnt vmcnt(14) lgkmcnt(10)\n\t"
        "v_mfma_f32_32x32x16_bf16 v[0:15], v[96:99], v[136:139], v[0:15]\n\t"
        "s_cbranch_vccnz 12f\n\t"
        "13:\n\t"
        "ds_read_b64_tr_b16 v[96:97], v129 offset:0\n\t"
        "ds_read_b64_tr_b16 v[98:99], v129 offset:3072\n\t"
        "s_waitcnt lgkmcnt(10)\n\t"
        "v_mfma_f32_32x32x16_bf16 v[16:31], v[100:103], v[136:139], v[16:31]\n\t"
        "v_add_u32 v128, 1, v128\n\t"
        "ds_read_b64_tr_b16 v[100:101], v130 offset:0\n\t"
        "ds_read_b64_tr_b16 v[102:103], v130 offset:3072\n\t"
        "s_waitcnt lgkmcnt(10)\n\t"
        "v_mfma_f32_32x32x16_bf16 v[32:47], v[104:107], v[136:139], v[32:47]\n\t"
        "ds_read_b64_tr_b16 v[104:105], v129 offset:128\n\t"
        "ds_read_b64_tr_b16 v[106:107], v129 offset:3200\n\t"
        "s_waitcnt lgkmcnt(10)\n\t"
        "v_mfma_f32_32x32x16_bf16 v[48:63], v[108:111], v[136:139], v[48:63]\n\t"
        "s_add_u32 s46, s46, 12288\n\t"
        "s_add_u32 s47, s47, 1\n\t"
        "ds_read_b64_tr_b16 v[108:109], v130 offset:128\n\t"
        "ds_read_b64_tr_b16 v[110:111], v130 offset:3200\n\t"
        "s_waitcnt lgkmcnt(10)\n\t"
        "v_mfma_f32_32x32x16_bf16 v[64:79], v[136:139], v[112:115], v[64:79]\n\t"
        "s_cmp_eq_u32 s46, 110592\n\t"
        "s_cselect_b32 s46, 0, s46\n\t"
        "ds_read_b64_tr_b16 v[112:113], v129 offset:256\n\t"
        "ds_read_b64_tr_b16 v[114:115], v129 offset:3328\n\t"
        "s_waitcnt lgkmcnt(10)\n\t"
        "v_mfma_f32_32x32x16_bf16 v[80:95], v[136:139], v[116:119], v[80:95]\n\t"
        "ds_write_b32 v127, v128\n\t"
        "ds_read_b64_tr_b16 v[116:117], v130 offset:256\n\t"
        "ds_read_b64_tr_b16 v[118:119], v130 offset:3328\n\t"
        "s_cmp_lt_u32 s47, 32\n\t"
        "s_cbranch_scc1 4f\n\t"
        "s_waitcnt vmcnt(0)\n\t"
        "buffer_store_dwordx4 v[196:199], v200, s[40:43], 0 offen sc1\n\t"
        "4:\n\t"
        "s_waitcnt vmcnt(13) lgkmcnt(11)\n\t"
        "v_mfma_f32_32x32x16_bf16 v[0:15], v[96:99], v[140:143], v[0:15]\n\t"
        "ds_read_b32 v126, v125\n\t"
        "ds_read_b64_tr_b16 v[96:97], v129 offset:6144\n\t"
        "ds_read_b64_tr_b16 v[98:99], v129 offset:9216\n\t"
        "s_waitcnt lgkmcnt(12)\n\t"
        "v_mfma_f32_32x32x16_bf16 v[16:31], v[100:103], v[140:143], v[16:31]\n\t"
        "v_add_u32 v123, s46, v121\n\t"
        "ds_read_b64_tr_b16 v[100:101], v130 offset:6144\n\t"
        "ds_read_b64_tr_b16 v[102:103], v130 offset:9216\n\t"
        "s_waitcnt lgkmcnt(12)\n\t"
        "v_mfma_f32_32x32x16_bf16 v[32:47], v[104:107], v[140:143], v[32:47]\n\t"
        "ds_read_b64_tr_b16 v[104:105], v129 offset:6272\n\t"
        "ds_read_b64_tr_b16 v[106:107], v129 offset:9344\n\t"
        "s_waitcnt lgkmcnt(12)\n\t"
        "v_mfma_f32_32x32x16_bf16 v[48:63], v[108:111], v[140:143], v[48:63]\n\t"
        "v_add_u32 v124, s46, v122\n\t"
        "ds_read_b64_tr_b16 v[108:109], v130 offset:6272\n\t"
        "ds_read_b64_tr_b16 v[110:111], v130 offset:9344\n\t"
        "s_waitcnt lgkmcnt(12)\n\t"
        "v_mfma_f32_32x32x16_bf16 v[64:79], v[140:143], v[112:115], v[64:79]\n\t"
        "ds_read_b64_tr_b16 v[112:113], v129 offset:6400\n\t"
        "ds_read_b64_tr_b16 v[114:115], v129 offset:9472\n\t"
        "s_waitcnt lgkmcnt(10)\n\t"
        "v_mfma_f32_32x32x16_bf16 v[80:95], v[140:143], v[116:119], v[80:95]\n\t"
        "v_cmp_gt_u32 vcc, s47, v126\n\t"
        "ds_read_b64_tr_b16 v[116:117], v130 offset:6400\n\t"
        "ds_read_b64_tr_b16 v[118:119], v130 offset:9472\n\t"
        "s_waitcnt vmcnt(12) lgkmcnt(10)\n\t"
        "v_mfma_f32_32x32x16_bf16 v[0:15], v[96:99], v[144:147], v[0:15]\n\t"
        "s_cbranch_vccnz 14f\n\t"
        "15:\n\t"
        "ds_read_b64_tr_b16 v[96:97], v123 offset:0\n\t"
        "ds_read_b64_tr_b16 v[98:99], v123 offset:3072\n\t"
        "s_waitcnt lgkmcnt(10)\n\t"
        "v_mfma_f32_32x32x16_bf16 v[16:31], v[100:103], v[144:147], v[16:31]\n\t"
        "v_add_u32 v128, 1, v128\n\t"
        "ds_read_b64_tr_b16 v[100:101], v124 offset:0\n\t"
        "ds_read_b64_tr_b16 v[102:103], v124 offset:3072\n\t"
        "s_waitcnt lgkmcnt(10)\n\t"
        "v_mfma_f32_32x32x16_bf16 v[32:47], v[104:107], v[144:147], v[32:47]\n\t"
        "ds_read_b64_tr_b16 v[104:105], v123 offset:128\n\t"
        "ds_read_b64_tr_b16 v[106:107], v123 offset:3200\n\t"
        "s_waitcnt lgkmcnt(10)\n\t"
        "v_mfma_f32_32x32x16_bf16 v[48:63], v[108:111], v[144:147], v[48:63]\n\t"
        "s_add_u32 s46, s46, 12288\n\t"
        "s_add_u32 s47, s47, 1\n\t"
        "ds_read_b64_tr_b16 v[108:109], v124 offset:128\n\t"
        "ds_read_b64_tr_b16 v[110:111], v124 offset:3200\n\t"
        "s_waitcnt lgkmcnt(10)\n\t"
        "v_mfma_f32_32x32x16_bf16 v[64:79], v[144:147], v[112:115], v[64:79]\n\t"
        "s_cmp_eq_u32 s46, 110592\n\t"
        "s_cselect_b32 s46, 0, s46\n\t"
        "ds_read_b64_tr_b16 v[112:113], v123 offset:256\n\t"
        "ds_read_b64_tr_b16 v[114:115], v123 offset:3328\n\t"
        "s_waitcnt lgkmcnt(10)\n\t"
        "v_mfma_f32_32x32x16_bf16 v[80:95], v[144:147], v[116:119], v[80:95]\n\t"
        "ds_write_b32 v127, v128\n\t"
        "ds_read_b64_tr_b16 v[116:117], v124 offset:256\n\t"
        "ds_read_b64_tr_b16 v[118:119], v124 offset:3328\n\t"
        "s_waitcnt lgkmcnt(0)\n\t"
        "s_nop 7\n\t"
        "s_branch 9f\n\t"
        "10:\n\t"
        "s_mov_b32 s48, 0x10000\n\t"
        "8:\n\t"
        "ds_read_b32 v126, v125\n\t"
        "s_sub_u32 s48, s48, 1\n\t"
        "s_waitcnt lgkmcnt(0)\n\t"
        "s_cbranch_scc1 11b\n\t"
        "v_cmp_gt_u32 vcc, s47, v126\n\t"
        "s_cbranch_vccnz 8b\n\t"
        "s_branch 11b\n\t"
        "12:\n\t"
        "s_mov_b32 s48, 0x10000\n\t"
        "8:\n\t"
        "ds_read_b32 v126, v125\n\t"
        "s_sub_u32 s48, s48, 1\n\t"
        "s_waitcnt lgkmcnt(0)\n\t"
        "s_cbranch_scc1 13b\n\t"
        "v_cmp_gt_u32 vcc, s47, v126\n\t"
        "s_cbranch_vccnz 8b\n\t"
        "s_branch 13b\n\t"
        "14:\n\t"
        "s_mov_b32 s48, 0x10000\n\t"
        "8:\n\t"
        "ds_read_b32 v126, v125\n\t"
        "s_sub_u32 s48, s48, 1\n\t"
        "s_waitcnt lgkmcnt(0)\n\t"
        "s_cbranch_scc1 15b\n\t"
        "v_cmp_gt_u32 vcc, s47, v126\n\t"
        "s_cbranch_vccnz 8b\n\t"
        "s_branch 15b\n\t"
        "9:\n\t"
        : "={v[0:15]}"(acc[0]), "={v[16:31]}"(acc[1]), "={v[32:47]}"(acc[2]), "={v[48:63]}"(acc[3]), "={v[64:79]}"(acc[4]), "={v[80:95]}"(acc[5]),
          "={v[96:111]}"(stA), "+{v[112:127]}"(stB), "+{v[128:131]}"(stC), "={s46}"(slot), "+{s47}"(need)
        : "{v[132:147]}"(xr0), "{v[196:199]}"(pst), "{v200}"(sdst), "{s[40:43]}"(srd) : "s48", "vcc", "scc", "memory");
      STAMPV(2 + 16 * L);
      float al = 1.f, be = 0.f;
      if (L == 1) {
        PHASE_VARS;
        float S, Q, Dd;
        row_stats_sum(rs, OFF_SX, tile, lane, S, Q, Dd);
        soff = ((tn + 1) & 15) * 4096;
        asm volatile(
          "buffer_load_dwordx4 v[148:151], v120, s[40:43], s44 offen offset:0 sc1\n\t"
          "buffer_load_dwordx4 v[152:155], v120, s[40:43], s44 offen offset:1024 sc1\n\t"
          "buffer_load_dwordx4 v[156:159], v120, s[40:43], s44 offen offset:2048 sc1\n\t"
          "buffer_load_dwordx4 v[160:163], v120, s[40:43], s44 offen offset:3072 sc1\n\t"
          "s_add_u32 s44, s44, 0x1000\n\t"
          "s_and_b32 s44, s44, 0xffff\n\t"
          "buffer_load_dwordx4 v[164:167], v120, s[40:43], s44 offen offset:0 sc1\n\t"
          "buffer_load_dwordx4 v[168:171], v120, s[40:43], s44 offen offset:1024 sc1\n\t"
          "buffer_load_dwordx4 v[172:175], v120, s[40:43], s44 offen offset:2048 sc1\n\t"
          "buffer_load_dwordx4 v[176:179], v120, s[40:43], s44 offen offset:3072 sc1\n\t"
          "s_add_u32 s44, s44, 0x1000\n\t"
          "s_and_b32 s44, s44, 0xffff\n\t"
          "buffer_load_dwordx4 v[180:183], v120, s[40:43], s44 offen offset:0 sc1\n\t"
          "buffer_load_dwordx4 v[184:187], v120, s[40:43], s44 offen offset:1024 sc1\n\t"
          "buffer_load_dwordx4 v[188:191], v120, s[40:43], s44 offen offset:2048 sc1\n\t"
          "buffer_load_dwordx4 v[192:195], v120, s[40:43], s44 offen offset:3072 sc1\n\t"
          : "={v[148:163]}"(xr1), "={v[164:179]}"(xr2), "={v[180:195]}"(xr3), "+{s44}"(soff), "+v"(S), "+v"(Q), "+v"(Dd) : "{v[112:127]}"(stB), "{s[40:43]}"(srd) : "scc", "memory");
        float mean, var, mb1, rd1, mb2, den2;
        row_stats_fin(smem, 1, tile, lane, S, Q, Dd, U2F(words[0]), x1p, mean, var, mb1, rd1);
        ln_scalars(mean + (mean - mb1) * rd1, var * (1.0f + rd1) * (1.0f + rd1), mb2, den2);
        const float rd2 = RCP(den2);
        al = (1.0f + rd1) * rd2; be = -(mb1 * rd1 + mb2) * rd2;
        reinterpret_cast<float*>(smem + L_AB)[wid * 64 + lane] = hi ? be : al;
        reinterpret_cast<float*>(smem + L_AB)[256 + wid * 64 + lane] = hi ? rd1 : mb1;
      }
      STAMPV(3 + 16 * L);
      soff = ((tn + 1 + 2) & 15) * 4096;
      asm volatile(
        "s_nop 0\n\t"
        "s_add_u32 s44, s44, 0x1000\n\t"
        "s_and_b32 s44, s44, 0xffff\n\t"
        "buffer_load_dwordx4 v[132:135], v120, s[40:43], s44 offen offset:0 sc1\n\t"
        "buffer_load_dwordx4 v[136:139], v120, s[40:43], s44 offen offset:1024 sc1\n\t"
        "buffer_load_dwordx4 v[140:143], v120, s[40:43], s44 offen offset:2048 sc1\n\t"
        "s_mov_b32 s45, 3\n\t"
        "1:\n\t"
        "s_waitcnt vmcnt(14) lgkmcnt(11)\n\t"
        "v_mfma_f32_32x32x16_bf16 v[0:15], v[96:99], v[148:151], v[0:15]\n\t"
        "ds_read_b32 v126, v125\n\t"
        "ds_read_b64_tr_b16 v[96:97], v123 offset:6144\n\t"
        "ds_read_b64_tr_b16 v[98:99], v123 offset:9216\n\t"
        "s_waitcnt lgkmcnt(12)\n\t"
        "v_mfma_f32_32x32x16_bf16 v[16:31], v[100:103], v[148:151], v[16:31]\n\t"
        "v_add_u32 v129, s46, v121\n\t"
        "ds_read_b64_tr_b16 v[100:101], v124 offset:6144\n\t"
        "ds_read_b64_tr_b16 v[102:103], v124 offset:9216\n\t"
        "s_waitcnt lgkmcnt(12)\n\t"
        "v_mfma_f32_32x32x16_bf16 v[32:47], v[104:107], v[148:151], v[32:47]\n\t"
        "ds_read_b64_tr_b16 v[104:105], v123 offset:6272\n\t"
        "ds_read_b64_tr_b16 v[106:107], v123 offset:9344\n\t"
        "buffer_load_dwordx4 v[144:147], v120, s[40:43], s44 offen offset:3072 sc1\n\t"
        "s_waitcnt lgkmcnt(12)\n\t"
        "v_mfma_f32_32x32x16_bf16 v[48:63], v[108:111], v[148:151], v[48:63]\n\t"
        "v_add_u32 v130, s46, v122\n\t"
        "ds_read_b64_tr_b16 v[108:109], v124 offset:6272\n\t"
        "ds_read_b64_tr_b16 v[110:111], v124 offset:9344\n\t"
        "s_waitcnt lgkmcnt(12)\n\t"
        "v_mfma_f32_32x32x16_bf16 v[64:79], v[148:151], v[112:115], v[64:79]\n\t"
        "ds_read_b64_tr_b16 v[112:113], v123 offset:6400\n\t"
        "ds_read_b64_tr_b16 v[114:115], v123 offset:9472\n\t"
        "s_waitcnt lgkmcnt(10)\n\t"
        "v_mfma_f32_32x32x16_bf16 v[80:95], v[148:151], v[116:119], v[80:95]\n\t"
        "v_cmp_gt_u32 vcc, s47, v126\n\t"
        "ds_read_b64_tr_b16 v[116:117], v124 offset:6400\n\t"
        "ds_read_b64_tr_b16 v[118:119], v124 offset:9472\n\t"
        "s_waitcnt vmcnt(14) lgkmcnt(10)\n\t"
        "v_mfma_f32_32x32x16_bf16 v[0:15], v[96:99], v[152:155], v[0:15]\n\t"
        "s_cbranch_vccnz 16f\n\t"
        "17:\n\t"
        "ds_read_b64_tr_b16 v[96:97], v129 offset:0\n\t"
        "ds_read_b64_tr_b16 v[98:99], v129 offset:3072\n\t"
        "s_waitcnt lgkmcnt(10)\n\t"
        "v_mfma_f32_32x32x16_bf16 v[16:31], v[100:103], v[152:155], v[16:31]\n\t"
        "v_add_u32 v128, 1, v128\n\t"
        "ds_read_b64_tr_b16 v[100:101], v130 offset:0\n\t"
        "ds_read_b64_tr_b16 v[102:103], v130 offset:3072\n\t"
        "s_waitcnt lgkmcnt(10)\n\t"
        "v_mfma_f32_32x32x16_bf16 v[32:47], v[104:107], v[152:155], v[32:47]\n\t"
        "ds_read_b64_tr_b16 v[104:105], v129 offset:128\n\t"
        "ds_read_b64_tr_b16 v[106:107], v129 offset:3200\n\t"
        "s_add_u32 s44, s44, 0x1000\n\t"
        "s_and_b32 s44, s44, 0xffff\n\t"
        "buffer_load_dwordx4 v[148:151], v120, s[40:43], s44 offen offset:0 sc1\n\t"
        "s_waitcnt lgkmcnt(10)\n\t"
        "v_mfma_f32_32x32x16_bf16 v[48:63], v[108:111], v[152:155], v[48:63]\n\t"
        "s_add_u32 s46, s46, 12288\n\t"
        "s_add_u32 s47, s47, 1\n\t"
        "ds_read_b64_tr_b16 v[108:109], v130 offset:128\n\t"
        "ds_read_b64_tr_b16 v[110:111], v130 offset:3200\n\t"
        "s_waitcnt lgkmcnt(10)\n\t"
        "v_mfma_f32_32x32x16_bf16 v[64:79], v[152:155], v[112:115], v[64:79]\n\t"
        "s_cmp_eq_u32 s46, 110592\n\t"
        "s_cselect_b32 s46, 0, s46\n\t"
        "ds_read_b64_tr_b16 v[112:113], v129 offset:256\n\t"
        "ds_read_b64_tr_b16 v[114:115], v129 offset:3328\n\t"
        "s_waitcnt lgkmcnt(10)\n\t"
        "v_mfma_f32_32x32x16_bf16 v[80:95], v[152:155], v[116:119], v[80:95]\n\t"
        "ds_write_b32 v127, v128\n\t"
        "ds_read_b64_tr_b16 v[116:117], v130 offset:256\n\t"
        "ds_read_b64_tr_b16 v[118:119], v130 offset:3328\n\t"
        "s_waitcnt vmcnt(14) lgkmcnt(11)\n\t"
        "v_mfma_f32_32x32x16_bf16 v[0:15], v[96:99], v[156:159], v[0:15]\n\t"
        "ds_read_b32 v126, v125\n\t"
        "ds_read_b64_tr_b16 v[96:97], v129 offset:6144\n\t"
        "ds_read_b64_tr_b16 v[98:99], v129 offset:9216\n\t"
        "s_waitcnt lgkmcnt(12)\n\t"
        "v_mfma_f32_32x32x16_bf16 v[16:31], v[100:103], v[156:159], v[16:31]\n\t"
        "v_add_u32 v123, s46, v121\n\t"
        "ds_read_b64_tr_b16 v[100:101], v130 offset:6144\n\t"
        "ds_read_b64_tr_b16 v[102:103], v130 offset:9216\n\t"
        "s_waitcnt lgkmcnt(12)\n\t"
        "v_mfma_f32_32x32x16_bf16 v[32:47], v[104:107], v[156:159], v[32:47]\n\t"
        "ds_read_b64_tr_b16 v[104:105], v129 offset:6272\n\t"
        "ds_read_b64_tr_b16 v[106:107], v129 offset:9344\n\t"
        "buffer_load_dwordx4 v[152:155], v120, s[40:43], s44 offen offset:1024 sc1\n\t"
        "s_waitcnt lgkmcnt(12)\n\t"
        "v_mfma_f32_32x32x16_bf16 v[48:63], v[108:111], v[156:159], v[48:63]\n\t"
        "v_add_u32 v124, s46, v122\n\t"
        "ds_read_b64_tr_b16 v[108:109], v130 offset:6272\n\t"
        "ds_read_b64_tr_b16 v[110:111], v130 offset:9344\n\t"
        "s_waitcnt lgkmcnt(12)\n\t"
        "v_mfma_f32_32x32x16_bf16 v[64:79], v[156:159], v[112:115], v[64:79]\n\t"
        "ds_read_b64_tr_b16 v[112:113], v129 offset:6400\n\t"
        "ds_read_b64_tr_b16 v[114:115], v129 offset:9472\n\t"
        "s_waitcnt lgkmcnt(10)\n\t"
        "v_mfma_f32_32x32x16_bf16 v[80:95], v[156:159], v[116:119], v[80:95]\n\t"
        "v_cmp_gt_u32 vcc, s47, v126\n\t"
        "ds_read_b64_tr_b16 v[116:117], v130 offset:6400\n\t"
        "ds_read_b64_tr_b16 v[118:119], v130 offset:9472\n\t"
        "s_waitcnt vmcnt(14) lgkmcnt(10)\n\t"
        "v_mfma_f32_32x32x16_bf16 v[0:15], v[96:99], v[160:163], v[0:15]\n\t"
        "s_cbranch_vccnz 18f\n\t"
        "19:\n\t"
        "ds_read_b64_tr_b16 v[96:97], v123 offset:0\n\t"
        "ds_read_b64_tr_b16 v[98:99], v123 offset:3072\n\t"
        "s_waitcnt lgkmcnt(10)\n\t"
        "v_mfma_f32_32x32x16_bf16 v[16:31], v[100:103], v[160:163], v[16:31]\n\t"
        "v_add_u32 v128, 1, v128\n\t"
        "ds_read_b64_tr_b16 v[100:101], v124 offset:0\n\t"
        "ds_read_b64_tr_b16 v[102:103], v124 offset:3072\n\t"
        "s_waitcnt lgkmcnt(10)\n\t"
        "v_mfma_f32_32x32x16_bf16 v[32:47], v[104:107], v[160:163], v[32:47]\n\t"
        "ds_read_b64_tr_b16 v[104:105], v123 offset:128\n\t"
        "ds_read_b64_tr_b16 v[106:107], v123 offset:3200\n\t"
        "buffer_load_dwordx4 v[156:159], v120, s[40:43], s44 offen offset:2048 sc1\n\t"
        "s_waitcnt lgkmcnt(10)\n\t"
        "v_mfma_f32_32x32x16_bf16 v[48:63], v[108:111], v[160:163], v[48:63]\n\t"
        "s_add_u32 s46, s46, 12288\n\t"
        "s_add_u32 s47, s47, 1\n\t"
        "ds_read_b64_tr_b16 v[108:109], v124 offset:128\n\t"
        "ds_read_b64_tr_b16 v[110:111], v124 offset:3200\n\t"
        "s_waitcnt lgkmcnt(10)\n\t"
        "v_mfma_f32_32x32x16_bf16 v[64:79], v[160:163], v[112:115], v[64:79]\n\t"
        "s_cmp_eq_u32 s46, 110592\n\t"
        "s_cselect_b32 s46, 0, s46\n\t"
        "ds_read_b64_tr_b16 v[112:113], v123 offset:256\n\t"
        "ds_read_b64_tr_b16 v[114:115], v123 offset:3328\n\t"
        "s_waitcnt lgkmcnt(10)\n\t"
        "v_mfma_f32_32x32x16_bf16 v[80:95], v[160:163], v[116:119], v[80:95]\n\t"
        "ds_write_b32 v127, v128\n\t"
        "ds_read_b64_tr_b16 v[116:117], v124 offset:256\n\t"
        "ds_read_b64_tr_b16 v[118:119], v124 offset:3328\n\t"
        "s_waitcnt vmcnt(14) lgkmcnt(11)\n\t"
        "v_mfma_f32_32x32x16_bf16 v[0:15], v[96:99], v[164:167], v[0:15]\n\t"
        "ds_read_b32 v126, v125\n\t"
        "ds_read_b64_tr_b16 v[96:97], v123 offset:6144\n\t"
        "ds_read_b64_tr_b16 v[98:99], v123 offset:9216\n\t"
        "s_waitcnt lgkmcnt(12)\n\t"
        "v_mfma_f32_32x32x16_bf16 v[16:31], v[100:103], v[164:167], v[16:31]\n\t"
        "v_add_u32 v129, s46, v121\n\t"
        "ds_read_b64_tr_b16 v[100:101], v124 offset:6144\n\t"
        "ds_read_b64_tr_b16 v[102:103], v124 offset:9216\n\t"
        "s_waitcnt lgkmcnt(12)\n\t"
        "v_mfma_f32_32x32x16_bf16 v[32:47], v[104:107], v[164:167], v[32:47]\n\t"
        "ds_read_b64_tr_b16 v[104:105], v123 offset:6272\n\t"
        "ds_read_b64_tr_b16 v[106:107], v123 offset:9344\n\t"
        "buffer_load_dwordx4 v[160:163], v120, s[40:43], s44 offen offset:3072 sc1\n\t"
        "s_waitcnt lgkmcnt(12)\n\t"
        "v_mfma_f32_32x32x16_bf16 v[48:63], v[108:111], v[164:167], v[48:63]\n\t"
        "v_add_u32 v130, s46, v122\n\t"
        "ds_read_b64_tr_b16 v[108:109], v124 offset:6272\n\t"
        "ds_read_b64_tr_b16 v[110:111], v124 offset:9344\n\t"
        "s_waitcnt lgkmcnt(12)\n\t"
        "v_mfma_f32_32x32x16_bf16 v[64:79], v[164:167], v[112:115], v[64:79]\n\t"
        "ds_read_b64_tr_b16 v[112:113], v123 offset:6400\n\t"
        "ds_read_b64_tr_b16 v[114:115], v123 offset:9472\n\t"
        "s_waitcnt lgkmcnt(10)\n\t"
        "v_mfma_f32_32x32x16_bf16 v[80:95], v[164:167], v[116:119], v[80:95]\n\t"
        "v_cmp_gt_u32 vcc, s47, v126\n\t"
        "ds_read_b64_tr_b16 v[116:117], v124 offset:6400\n\t"
        "ds_read_b64_tr_b16 v[118:119], v124 offset:9472\n\t"
        "s_waitcnt vmcnt(14) lgkmcnt(10)\n\t"
        "v_mfma_f32_32x32x16_bf16 v[0:15], v[96:99], v[168:171], v[0:15]\n\t"
        "s_cbranch_vccnz 20f\n\t"
        "21:\n\t"
        "ds_read_b64_tr_b16 v[96:97], v129 offset:0\n\t"
        "ds_read_b64_tr_b16 v[98:99], v129 offset:3072\n\t"
        "s_waitcnt lgkmcnt(10)\n\t"
        "v_mfma_f32_32x32x16_bf16 v[16:31], v[100:103], v[168:171], v[16:31]\n\t"
        "v_add_u32 v128, 1, v128\n\t"
        "ds_read_b64_tr_b16 v[100:101], v130 offset:0\n\t"
        "ds_read_b64_tr_b16 v[102:103], v130 offset:3072\n\t"
        "s_waitcnt lgkmcnt(10)\n\t"
        "v_mfma_f32_32x32x16_bf16 v[32:47], v[104:107], v[168:171], v[32:47]\n\t"
        "ds_read_b64_tr_b16 v[104:105], v129 offset:128\n\t"
        "ds_read_b64_tr_b16 v[106:107], v129 offset:3200\n\t"
        "s_add_u32 s44, s44, 0x1000\n\t"
        "s_and_b32 s44, s44, 0xffff\n\t"
        "buffer_load_dwordx4 v[164:167], v120, s[40:43], s44 offen offset:0 sc1\n\t"
        "s_waitcnt lgkmcnt(10)\n\t"
        "v_mfma_f32_32x32x16_bf16 v[48:63], v[108:111], v[168:171], v[48:63]\n\t"
        "s_add_u32 s46, s46, 12288\n\t"
        "s_add_u32 s47, s47, 1\n\t"
        "ds_read_b64_tr_b16 v[108:109], v130 offset:128\n\t"
        "ds_read_b64_tr_b16 v[110:111], v130 offset:3200\n\t"
        "s_waitcnt lgkmcnt(10)\n\t"
        "v_mfma_f32_32x32x16_bf16 v[64:79], v[168:171], v[112:115], v[64:79]\n\t"
        "s_cmp_eq_u32 s46, 110592\n\t"
        "s_cselect_b32 s46, 0, s46\n\t"
        "ds_read_b64_tr_b16 v[112:113], v129 offset:256\n\t"
        "ds_read_b64_tr_b16 v[114:115], v129 offset:3328\n\t"
        "s_waitcnt lgkmcnt(10)\n\t"
        "v_mfma_f32_32x32x16_bf16 v[80:95], v[168:171], v[116:119], v[80:95]\n\t"
        "ds_write_b32 v127, v128\n\t"
        "ds_read_b64_tr_b16 v[116:117], v130 offset:256\n\t"
        "ds_read_b64_tr_b16 v[118:119], v130 offset:3328\n\t"
        "s_waitcnt vmcnt(14) lgkmcnt(11)\n\t"
        "v_mfma_f32_32x32x16_bf16 v[0:15], v[96:99], v[172:175], v[0:15]\n\t"
        "ds_read_b32 v126, v125\n\t"
        "ds_read_b64_tr_b16 v[96:97], v129 offset:6144\n\t"
        "ds_read_b64_tr_b16 v[98:99], v129 offset:9216\n\t"
        "s_waitcnt lgkmcnt(12)\n\t"
        "v_mfma_f32_32x32x16_bf16 v[16:31], v[100:103], v[172:175], v[16:31]\n\t"
        "v_add_u32 v123, s46, v121\n\t"
        "ds_read_b64_tr_b16 v[100:101], v130 offset:6144\n\t"
        "ds_read_b64_tr_b16 v[102:103], v130 offset:9216\n\t"
        "s_waitcnt lgkmcnt(12)\n\t"
        "v_mfma_f32_32x32x16_bf16 v[32:47], v[104:107], v[172:175], v[32:47]\n\t"
        "ds_read_b64_tr_b16 v[104:105], v129 offset:6272\n\t"
        "ds_read_b64_tr_b16 v[106:107], v129 offset:9344\n\t"
        "buffer_load_dwordx4 v[168:171], v120, s[40:43], s44 offen offset:1024 sc1\n\t"
        "s_waitcnt lgkmcnt(12)\n\t"
        "v_mfma_f32_32x32x16_bf16 v[48:63], v[108:111], v[172:175], v[48:63]\n\t"
        "v_add_u32 v124, s46, v122\n\t"
        "ds_read_b64_tr_b16 v[108:109], v130 offset:6272\n\t"
        "ds_read_b64_tr_b16 v[110:111], v130 offset:9344\n\t"
        "s_waitcnt lgkmcnt(12)\n\t"
        "v_mfma_f32_32x32x16_bf16 v[64:79], v[172:175], v[112:115], v[64:79]\n\t"
        "ds_read_b64_tr_b16 v[112:113], v129 offset:6400\n\t"
        "ds_read_b64_tr_b16 v[114:115], v129 offset:9472\n\t"
        "s_waitcnt lgkmcnt(10)\n\t"
        "v_mfma_f32_32x32x16_bf16 v[80:95], v[172:175], v[116:119], v[80:95]\n\t"
        "v_cmp_gt_u32 vcc, s47, v126\n\t"
        "ds_read_b64_tr_b16 v[116:117], v130 offset:6400\n\t"
        "ds_read_b64_tr_b16 v[118:119], v130 offset:9472\n\t"
        "s_waitcnt vmcnt(14) lgkmcnt(10)\n\t"
        "v_mfma_f32_32x32x16_bf16 v[0:15], v[96:99], v[176:179], v[0:15]\n\t"
        "s_cbranch_vccnz 22f\n\t"
        "23:\n\t"
        "ds_read_b64_tr_b16 v[96:97], v123 offset:0\n\t"
        "ds_read_b64_tr_b16 v[98:99], v123 offset:3072\n\t"
        "s_waitcnt lgkmcnt(10)\n\t"
        "v_mfma_f32_32x32x16_bf16 v[16:31], v[100:103], v[176:179], v[16:31]\n\t"
        "v_add_u32 v128, 1, v128\n\t"
        "ds_read_b64_tr_b16 v[100:101], v124 offset:0\n\t"
        "ds_read_b64_tr_b16 v[102:103], v124 offset:3072\n\t"
        "s_waitcnt lgkmcnt(10)\n\t"
        "v_mfma_f32_32x32x16_bf16 v[32:47], v[104:107], v[176:179], v[32:47]\n\t"
        "ds_read_b64_tr_b16 v[104:105], v123 offset:128\n\t"
        "ds_read_b64_tr_b16 v[106:107], v123 offset:3200\n\t"
        "buffer_load_dwordx4 v[172:175], v120, s[40:43], s44 offen offset:2048 sc1\n\t"
        "s_waitcnt lgkmcnt(10)\n\t"
        "v_mfma_f32_32x32x16_bf16 v[48:63], v[108:111], v[176:179], v[48:63]\n\t"
        "s_add_u32 s46, s46, 12288\n\t"
        "s_add_u32 s47, s47, 1\n\t"
        "ds_read_b64_tr_b16 v[108:109], v124 offset:128\n\t"
        "ds_read_b64_tr_b16 v[110:111], v124 offset:3200\n\t"
        "s_waitcnt lgkmcnt(10)\n\t"
        "v_mfma_f32_32x32x16_bf16 v[64:79], v[176:179], v[112:115], v[64:79]\n\t"
        "s_cmp_eq_u32 s46, 110592\n\t"
        "s_cselect_b32 s46, 0, s46\n\t"
        "ds_read_b64_tr_b16 v[112:113], v123 offset:256\n\t"
        "ds_read_b64_tr_b16 v[114:115], v123 offset:3328\n\t"
        "s_waitcnt lgkmcnt(10)\n\t"
        "v_mfma_f32_32x32x16_bf16 v[80:95], v[176:179], v[116:119], v[80:95]\n\t"
        "ds_write_b32 v127, v128\n\t"
        "ds_read_b64_tr_b16 v[116:117], v124 offset:256\n\t"
        "ds_read_b64_tr_b16 v[118:119], v124 offset:3328\n\t"
        "s_waitcnt vmcnt(14) lgkmcnt(11)\n\t"
        "v_mfma_f32_32x32x16_bf16 v[0:15], v[96:99], v[180:183], v[0:15]\n\t"
        "ds_read_b32 v126, v125\n\t"
        "ds_read_b64_tr_b16 v[96:97], v123 offset:6144\n\t"
        "ds_read_b64_tr_b16 v[98:99], v123 offset:9216\n\t"
        "s_waitcnt lgkmcnt(12)\n\t"
        "v_mfma_f32_32x32x16_bf16 v[16:31], v[100:103], v[180:183], v[16:31]\n\t"
        "v_add_u32 v129, s46, v121\n\t"
        "ds_read_b64_tr_b16 v[100:101], v124 offset:6144\n\t"
        "ds_read_b64_tr_b16 v[102:103], v124 offset:9216\n\t"
        "s_waitcnt lgkmcnt(12)\n\t"
        "v_mfma_f32_32x32x16_bf16 v[32:47], v[104:107], v[180:183], v[32:47]\n\t"
        "ds_read_b64_tr_b16 v[104:105], v123 offset:6272\n\t"
        "ds_read_b64_tr_b16 v[106:107], v123 offset:9344\n\t"
        "buffer_load_dwordx4 v[176:179], v120, s[40:43], s44 offen offset:3072 sc1\n\t"
        "s_waitcnt lgkmcnt(12)\n\t"
        "v_mfma_f32_32x32x16_bf16 v[48:63], v[108:111], v[180:183], v[48:63]\n\t"
        "v_add_u32 v130, s46, v122\n\t"
        "ds_read_b64_tr_b16 v[108:109], v124 offset:6272\n\t"
        "ds_read_b64_tr_b16 v[110:111], v124 offset:9344\n\t"
        "s_waitcnt lgkmcnt(12)\n\t"
        "v_mfma_f32_32x32x16_bf16 v[64:79], v[180:183], v[112:115], v[64:79]\n\t"
        "ds_read_b64_tr_b16 v[112:113], v123 offset:6400\n\t"
        "ds_read_b64_tr_b16 v[114:115], v123 offset:9472\n\t"
        "s_waitcnt lgkmcnt(10)\n\t"
        "v_mfma_f32_32x32x16_bf16 v[80:95], v[180:183], v[116:119], v[80:95]\n\t"
        "v_cmp_gt_u32 vcc, s47, v126\n\t"
        "ds_read_b64_tr_b16 v[116:117], v124 offset:6400\n\t"
        "ds_read_b64_tr_b16 v[118:119], v124 offset:9472\n\t"
        "s_waitcnt vmcnt(14) lgkmcnt(10)\n\t"
        "v_mfma_f32_32x32x16_bf16 v[0:15], v[96:99], v[184:187], v[0:15]\n\t"
        "s_cbranch_vccnz 24f\n\t"
        "25:\n\t"
        "ds_read_b64_tr_b16 v[96:97], v129 offset:0\n\t"
        "ds_read_b64_tr_b16 v[98:99], v129 offset:3072\n\t"
        "s_waitcnt lgkmcnt(10)\n\t"
        "v_mfma_f32_32x32x16_bf16 v[16:31], v[100:103], v[184:187], v[16:31]\n\t"
        "v_add_u32 v128, 1, v128\n\t"
        "ds_read_b64_tr_b16 v[100:101], v130 offset:0\n\t"
        "ds_read_b64_tr_b16 v[102:103], v130 offset:3072\n\t"
        "s_waitcnt lgkmcnt(10)\n\t"
        "v_mfma_f32_32x32x16_bf16 v[32:47], v[104:107], v[184:187], v[32:47]\n\t"
        "ds_read_b64_tr_b16 v[104:105], v129 offset:128\n\t"
        "ds_read_b64_tr_b16 v[106:107], v129 offset:3200\n\t"
        "s_add_u32 s44, s44, 0x1000\n\t"
        "s_and_b32 s44, s44, 0xffff\n\t"
        "buffer_load_dwordx4 v[180:183], v120, s[40:43], s44 offen offset:0 sc1\n\t"
        "s_waitcnt lgkmcnt(10)\n\t"
        "v_mfma_f32_32x32x16_bf16 v[48:63], v[108:111], v[184:187], v[48:63]\n\t"
        "s_add_u32 s46, s46, 12288\n\t"
        "s_add_u32 s47, s47, 1\n\t"
        "ds_read_b64_tr_b16 v[108:109], v130 offset:128\n\t"
        "ds_read_b64_tr_b16 v[110:111], v130 offset:3200\n\t"
        "s_waitcnt lgkmcnt(10)\n\t"
        "v_mfma_f32_32x32x16_bf16 v[64:79], v[184:187], v[112:115], v[64:79]\n\t"
        "s_cmp_eq_u32 s46, 110592\n\t"
        "s_cselect_b32 s46, 0, s46\n\t"
        "ds_read_b64_tr_b16 v[112:113], v129 offset:256\n\t"
        "ds_read_b64_tr_b16 v[114:115], v129 offset:3328\n\t"
        "s_waitcnt lgkmcnt(10)\n\t"
        "v_mfma_f32_32x32x16_bf16 v[80:95], v[184:187], v[116:119], v[80:95]\n\t"
        "ds_write_b32 v127, v128\n\t"
        "ds_read_b64_tr_b16 v[116:117], v130 offset:256\n\t"
        "ds_read_b64_tr_b16 v[118:119], v130 offset:3328\n\t"
        "s_waitcnt vmcnt(14) lgkmcnt(11)\n\t"
        "v_mfma_f32_32x32x16_bf16 v[0:15], v[96:99], v[188:191], v[0:15]\n\t"
        "ds_read_b32 v126, v125\n\t"
        "ds_read_b64_tr_b16 v[96:97], v129 offset:6144\n\t"
        "ds_read_b64_tr_b16 v[98:99], v129 offset:9216\n\t"
        "s_waitcnt lgkmcnt(12)\n\t"
        "v_mfma_f32_32x32x16_bf16 v[16:31], v[100:103], v[188:191], v[16:31]\n\t"
        "v_add_u32 v123, s46, v121\n\t"
        "ds_read_b64_tr_b16 v[100:101], v130 offset:6144\n\t"
        "ds_read_b64_tr_b16 v[102:103], v130 offset:9216\n\t"
        "s_waitcnt lgkmcnt(12)\n\t"
        "v_mfma_f32_32x32x16_bf16 v[32:47], v[104:107], v[188:191], v[32:47]\n\t"
        "ds_read_b64_tr_b16 v[104:105], v129 offset:6272\n\t"
        "ds_read_b64_tr_b16 v[106:107], v129 offset:9344\n\t"
        "buffer_load_dwordx4 v[184:187], v120, s[40:43], s44 offen offset:1024 sc1\n\t"
        "s_waitcnt lgkmcnt(12)\n\t"
        "v_mfma_f32_32x32x16_bf16 v[48:63], v[108:111], v[188:191], v[48:63]\n\t"
        "v_add_u32 v124, s46, v122\n\t"
        "ds_read_b64_tr_b16 v[108:109], v130 offset:6272\n\t"
        "ds_read_b64_tr_b16 v[110:111], v130 offset:9344\n\t"
        "s_waitcnt lgkmcnt(12)\n\t"
        "v_mfma_f32_32x32x16_bf16 v[64:79], v[188:191], v[112:115], v[64:79]\n\t"
        "ds_read_b64_tr_b16 v[112:113], v129 offset:6400\n\t"
        "ds_read_b64_tr_b16 v[114:115], v129 offset:9472\n\t"
        "s_waitcnt lgkmcnt(10)\n\t"
        "v_mfma_f32_32x32x16_bf16 v[80:95], v[188:191], v[116:119], v[80:95]\n\t"
        "v_cmp_gt_u32 vcc, s47, v126\n\t"
        "ds_read_b64_tr_b16 v[116:117], v130 offset:6400\n\t"
        "ds_read_b64_tr_b16 v[118:119], v130 offset:9472\n\t"
        "s_waitcnt vmcnt(14) lgkmcnt(10)\n\t"
        "v_mfma_f32_32x32x16_bf16 v[0:15], v[96:99], v[192:195], v[0:15]\n\t"
        "s_cbranch_vccnz 26f\n\t"
        "27:\n\t"
        "ds_read_b64_tr_b16 v[96:97], v123 offset:0\n\t"
        "ds_read_b64_tr_b16 v[98:99], v123 offset:3072\n\t"
        "s_waitcnt lgkmcnt(10)\n\t"
        "v_mfma_f32_32x32x16_bf16 v[16:31], v[100:103], v[192:195], v[16:31]\n\t"
        "v_add_u32 v128, 1, v128\n\t"
        "ds_read_b64_tr_b16 v[100:101], v124 offset:0\n\t"
        "ds_read_b64_tr_b16 v[102:103], v124 offset:3072\n\t"
        "s_waitcnt lgkmcnt(10)\n\t"
        "v_mfma_f32_32x32x16_bf16 v[32:47], v[104:107], v[192:195], v[32:47]\n\t"
        "ds_read_b64_tr_b16 v[104:105], v123 offset:128\n\t"
        "ds_read_b64_tr_b16 v[106:107], v123 offset:3200\n\t"
        "buffer_load_dwordx4 v[188:191], v120, s[40:43], s44 offen offset:2048 sc1\n\t"
        "s_waitcnt lgkmcnt(10)\n\t"
        "v_mfma_f32_32x32x16_bf16 v[48:63], v[108:111], v[192:195], v[48:63]\n\t"
        "s_add_u32 s46, s46, 12288\n\t"
        "s_add_u32 s47, s47, 1\n\t"
        "ds_read_b64_tr_b16 v[108:109], v124 offset:128\n\t"
        "ds_read_b64_tr_b16 v[110:111], v124 offset:3200\n\t"
        "s_waitcnt lgkmcnt(10)\n\t"
        "v_mfma_f32_32x32x16_bf16 v[64:79], v[192:195], v[112:115], v[64:79]\n\t"
        "s_cmp_eq_u32 s46, 110592\n\t"
        "s_cselect_b32 s46, 0, s46\n\t"
        "ds_read_b64_tr_b16 v[112:113], v123 offset:256\n\t"
        "ds_read_b64_tr_b16 v[114:115], v123 offset:3328\n\t"
        "s_waitcnt lgkmcnt(10)\n\t"
        "v_mfma_f32_32x32x16_bf16 v[80:95], v[192:195], v[116:119], v[80:95]\n\t"
        "ds_write_b32 v127, v128\n\t"
        "ds_read_b64_tr_b16 v[116:117], v124 offset:256\n\t"
        "ds_read_b64_tr_b16 v[118:119], v124 offset:3328\n\t"
        "s_waitcnt vmcnt(14) lgkmcnt(11)\n\t"
        "v_mfma_f32_32x32x16_bf16 v[0:15], v[96:99], v[132:135], v[0:15]\n\t"
        "ds_read_b32 v126, v125\n\t"
        "ds_read_b64_tr_b16 v[96:97], v123 offset:6144\n\t"
        "ds_read_b64_tr_b16 v[98:99], v123 offset:9216\n\t"
        "s_waitcnt lgkmcnt(12)\n\t"
        "v_mfma_f32_32x32x16_bf16 v[16:31], v[100:103], v[132:135], v[16:31]\n\t"
        "v_add_u32 v129, s46, v121\n\t"
        "ds_read_b64_tr_b16 v[100:101], v124 offset:6144\n\t"
        "ds_read_b64_tr_b16 v[102:103], v124 offset:9216\n\t"
        "s_waitcnt lgkmcnt(12)\n\t"
        "v_mfma_f32_32x32x16_bf16 v[32:47], v[104:107], v[132:135], v[32:47]\n\t"
        "ds_read_b64_tr_b16 v[104:105], v123 offset:6272\n\t"
        "ds_read_b64_tr_b16 v[106:107], v123 offset:9344\n\t"
        "buffer_load_dwordx4 v[192:195], v120, s[40:43], s44 offen offset:3072 sc1\n\t"
        "s_waitcnt lgkmcnt(12)\n\t"
        "v_mfma_f32_32x32x16_bf16 v[48:63], v[108:111], v[132:135], v[48:63]\n\t"
        "v_add_u32 v130, s46, v122\n\t"
        "ds_read_b64_tr_b16 v[108:109], v124 offset:6272\n\t"
        "ds_read_b64_tr_b16 v[110:111], v124 offset:9344\n\t"
        "s_waitcnt lgkmcnt(12)\n\t"
        "v_mfma_f32_32x32x16_bf16 v[64:79], v[132:135], v[112:115], v[64:79]\n\t"
        "ds_read_b64_tr_b16 v[112:113], v123 offset:6400\n\t"
        "ds_read_b64_tr_b16 v[114:115], v123 offset:9472\n\t"
        "s_waitcnt lgkmcnt(10)\n\t"
        "v_mfma_f32_32x32x16_bf16 v[80:95], v[132:135], v[116:119], v[80:95]\n\t"
        "v_cmp_gt_u32 vcc, s47, v126\n\t"
        "ds_read_b64_tr_b16 v[116:117], v124 offset:6400\n\t"
        "ds_read_b64_tr_b16 v[118:119], v124 offset:9472\n\t"
        "s_waitcnt vmcnt(14) lgkmcnt(10)\n\t"
        "v_mfma_f32_32x32x16_bf16 v[0:15], v[96:99], v[136:139], v[0:15]\n\t"
        "s_cbranch_vccnz 28f\n\t"
        "29:\n\t"
        "ds_read_b64_tr_b16 v[96:97], v129 offset:0\n\t"
        "ds_read_b64_tr_b16 v[98:99], v129 offset:3072\n\t"
        "s_waitcnt lgkmcnt(10)\n\t"
        "v_mfma_f32_32x32x16_bf16 v[16:31], v[100:103], v[136:139], v[16:31]\n\t"
        "v_add_u32 v128, 1, v128\n\t"
        "ds_read_b64_tr_b16 v[100:101], v130 offset:0\n\t"
        "ds_read_b64_tr_b16 v[102:103], v130 offset:3072\n\t"
        "s_waitcnt lgkmcnt(10)\n\t"
        "v_mfma_f32_32x32x16_bf16 v[32:47], v[104:107], v[136:139], v[32:47]\n\t"
        "ds_read_b64_tr_b16 v[104:105], v129 offset:128\n\t"
        "ds_read_b64_tr_b16 v[106:107], v129 offset:3200\n\t"
        "s_add_u32 s44, s44, 0x1000\n\t"
        "s_and_b32 s44, s44, 0xffff\n\t"
        "buffer_load_dwordx4 v[132:135], v120, s[40:43], s44 offen offset:0 sc1\n\t"
        "s_waitcnt lgkmcnt(10)\n\t"
        "v_mfma_f32_32x32x16_bf16 v[48:63], v[108:111], v[136:139], v[48:63]\n\t"
        "s_add_u32 s46, s46, 12288\n\t"
        "s_add_u32 s47, s47, 1\n\t"
        "ds_read_b64_tr_b16 v[108:109], v130 offset:128\n\t"
        "ds_read_b64_tr_b16 v[110:111], v130 offset:3200\n\t"
        "s_waitcnt lgkmcnt(10)\n\t"
        "v_mfma_f32_32x32x16_bf16 v[64:79], v[136:139], v[112:115], v[64:79]\n\t"
        "s_cmp_eq_u32 s46, 110592\n\t"
        "s_cselect_b32 s46, 0, s46\n\t"
        "ds_read_b64_tr_b16 v[112:113], v129 offset:256\n\t"
        "ds_read_b64_tr_b16 v[114:115], v129 offset:3328\n\t"
        "s_waitcnt lgkmcnt(10)\n\t"
        "v_mfma_f32_32x32x16_bf16 v[80:95], v[136:139], v[116:119], v[80:95]\n\t"
        "ds_write_b32 v127, v128\n\t"
        "ds_read_b64_tr_b16 v[116:117], v130 offset:256\n\t"
        "ds_read_b64_tr_b16 v[118:119], v130 offset:3328\n\t"
        "s_waitcnt vmcnt(14) lgkmcnt(11)\n\t"
        "v_mfma_f32_32x32x16_bf16 v[0:15], v[96:99], v[140:143], v[0:15]\n\t"
        "ds_read_b32 v126, v125\n\t"
        "ds_read_b64_tr_b16 v[96:97], v129 offset:6144\n\t"
        "ds_read_b64_tr_b16 v[98:99], v129 offset:9216\n\t"
        "s_waitcnt lgkmcnt(12)\n\t"
        "v_mfma_f32_32x32x16_bf16 v[16:31], v[100:103], v[140:143], v[16:31]\n\t"
        "v_add_u32 v123, s46, v121\n\t"
        "ds_read_b64_tr_b16 v[100:101], v130 offset:6144\n\t"
        "ds_read_b64_tr_b16 v[102:103], v130 offset:9216\n\t"
        "s_waitcnt lgkmcnt(12)\n\t"
        "v_mfma_f32_32x32x16_bf16 v[32:47], v[104:107], v[140:143], v[32:47]\n\t"
        "ds_read_b64_tr_b16 v[104:105], v129 offset:6272\n\t"
        "ds_read_b64_tr_b16 v[106:107], v129 offset:9344\n\t"
        "buffer_load_dwordx4 v[136:139], v120, s[40:43], s44 offen offset:1024 sc1\n\t"
        "s_waitcnt lgkmcnt(12)\n\t"
        "v_mfma_f32_32x32x16_bf16 v[48:63], v[108:111], v[140:143], v[48:63]\n\t"
        "v_add_u32 v124, s46, v122\n\t"
        "ds_read_b64_tr_b16 v[108:109], v130 offset:6272\n\t"
        "ds_read_b64_tr_b16 v[110:111], v130 offset:9344\n\t"
        "s_waitcnt lgkmcnt(12)\n\t"
        "v_mfma_f32_32x32x16_bf16 v[64:79], v[140:143], v[112:115], v[64:79]\n\t"
        "ds_read_b64_tr_b16 v[112:113], v129 offset:6400\n\t"
        "ds_read_b64_tr_b16 v[114:115], v129 offset:9472\n\t"
        "s_waitcnt lgkmcnt(10)\n\t"
        "v_mfma_f32_32x32x16_bf16 v[80:95], v[140:143], v[116:119], v[80:95]\n\t"
        "v_cmp_gt_u32 vcc, s47, v126\n\t"
        "ds_read_b64_tr_b16 v[116:117], v130 offset:6400\n\t"
        "ds_read_b64_tr_b16 v[118:119], v130 offset:9472\n\t"
        "s_waitcnt vmcnt(14) lgkmcnt(10)\n\t"
        "v_mfma_f32_32x32x16_bf16 v[0:15], v[96:99], v[144:147], v[0:15]\n\t"
        "s_cbranch_vccnz 30f\n\t"
        "31:\n\t"
        "ds_read_b64_tr_b16 v[96:97], v123 offset:0\n\t"
        "ds_read_b64_tr_b16 v[98:99], v123 offset:3072\n\t"
        "s_waitcnt lgkmcnt(10)\n\t"
        "v_mfma_f32_32x32x16_bf16 v[16:31], v[100:103], v[144:147], v[16:31]\n\t"
        "v_add_u32 v128, 1, v128\n\t"
        "ds_read_b64_tr_b16 v[100:101], v124 offset:0\n\t"
        "ds_read_b64_tr_b16 v[102:103], v124 offset:3072\n\t"
        "s_waitcnt lgkmcnt(10)\n\t"
        "v_mfma_f32_32x32x16_bf16 v[32:47], v[104:107], v[144:147], v[32:47]\n\t"
        "ds_read_b64_tr_b16 v[104:105], v123 offset:128\n\t"
        "ds_read_b64_tr_b16 v[106:107], v123 offset:3200\n\t"
        "buffer_load_dwordx4 v[140:143], v120, s[40:43], s44 offen offset:2048 sc1\n\t"
        "s_waitcnt lgkmcnt(10)\n\t"
        "v_mfma_f32_32x32x16_bf16 v[48:63], v[108:111], v[144:147], v[48:63]\n\t"
        "s_add_u32 s46, s46, 12288\n\t"
        "s_add_u32 s47, s47, 1\n\t"
        "ds_read_b64_tr_b16 v[108:109], v124 offset:128\n\t"
        "ds_read_b64_tr_b16 v[110:111], v124 offset:3200\n\t"
        "s_waitcnt lgkmcnt(10)\n\t"
        "v_mfma_f32_32x32x16_bf16 v[64:79], v[144:147], v[112:115], v[64:79]\n\t"
        "s_cmp_eq_u32 s46, 110592\n\t"
        "s_cselect_b32 s46, 0, s46\n\t"
        "ds_read_b64_tr_b16 v[112:113], v123 offset:256\n\t"
        "ds_read_b64_tr_b16 v[114:115], v123 offset:3328\n\t"
        "s_waitcnt lgkmcnt(10)\n\t"
        "v_mfma_f32_32x32x16_bf16 v[80:95], v[144:147], v[116:119], v[80:95]\n\t"
        "ds_write_b32 v127, v128\n\t"
        "ds_read_b64_tr_b16 v[116:117], v124 offset:256\n\t"
        "ds_read_b64_tr_b16 v[118:119], v124 offset:3328\n\t"
        "s_sub_u32 s45, s45, 1\n\t"
        "s_cmp_lg_u32 s45, 0\n\t"
        "s_cbranch_scc1 1b\n\t"
        "s_waitcnt vmcnt(14) lgkmcnt(11)\n\t"
        "v_mfma_f32_32x32x16_bf16 v[0:15], v[96:99], v[148:151], v[0:15]\n\t"
        "ds_read_b32 v126, v125\n\t"
        "ds_read_b64_tr_b16 v[96:97], v123 offset:6144\n\t"
        "ds_read_b64_tr_b16 v[98:99], v123 offset:9216\n\t"
        "s_waitcnt lgkmcnt(12)\n\t"
        "v_mfma_f32_32x32x16_bf16 v[16:31], v[100:103], v[148:151], v[16:31]\n\t"
        "v_add_u32 v129, s46, v121\n\t"
        "ds_read_b64_tr_b16 v[100:101], v124 offset:6144\n\t"
        "ds_read_b64_tr_b16 v[102:103], v124 offset:9216\n\t"
        "s_waitcnt lgkmcnt(12)\n\t"
        "v_mfma_f32_32x32x16_bf16 v[32:47], v[104:107], v[148:151], v[32:47]\n\t"
        "ds_read_b64_tr_b16 v[104:105], v123 offset:6272\n\t"
        "ds_read_b64_tr_b16 v[106:107], v123 offset:9344\n\t"
        "s_waitcnt lgkmcnt(12)\n\t"
        "v_mfma_f32_32x32x16_bf16 v[48:63], v[108:111], v[148:151], v[48:63]\n\t"
        "v_add_u32 v130, s46, v122\n\t"
        "ds_read_b64_tr_b16 v[108:109], v124 offset:6272\n\t"
        "ds_read_b64_tr_b16 v[110:111], v124 offset:9344\n\t"
        "s_waitcnt lgkmcnt(12)\n\t"
        "v_mfma_f32_32x32x16_bf16 v[64:79], v[148:151], v[112:115], v[64:79]\n\t"
        "ds_read_b64_tr_b16 v[112:113], v123 offset:6400\n\t"
        "ds_read_b64_tr_b16 v[114:115], v123 offset:9472\n\t"
        "s_waitcnt lgkmcnt(10)\n\t"
        "v_mfma_f32_32x32x16_bf16 v[80:95], v[148:151], v[116:119], v[80:95]\n\t"
        "v_cmp_gt_u32 vcc, s47, v126\n\t"
        "ds_read_b64_tr_b16 v[116:117], v124 offset:6400\n\t"
        "ds_read_b64_tr_b16 v[118:119], v124 offset:9472\n\t"
        "s_waitcnt vmcnt(13) lgkmcnt(10)\n\t"
        "v_mfma_f32_32x32x16_bf16 v[0:15], v[96:99], v[152:155], v[0:15]\n\t"
        "s_cbranch_vccnz 64f\n\t"
        "65:\n\t"
        "ds_read_b64_tr_b16 v[96:97], v129 offset:0\n\t"
        "ds_read_b64_tr_b16 v[98:99], v129 offset:3072\n\t"
        "s_waitcnt lgkmcnt(10)\n\t"
        "v_mfma_f32_32x32x16_bf16 v[16:31], v[100:103], v[152:155], v[16:31]\n\t"
        "v_add_u32 v128, 1, v128\n\t"
        "ds_read_b64_tr_b16 v[100:101], v130 offset:0\n\t"
        "ds_read_b64_tr_b16 v[102:103], v130 offset:3072\n\t"
        "s_waitcnt lgkmcnt(10)\n\t"
        "v_mfma_f32_32x32x16_bf16 v[32:47], v[104:107], v[152:155], v[32:47]\n\t"
        "ds_read_b64_tr_b16 v[104:105], v129 offset:128\n\t"
        "ds_read_b64_tr_b16 v[106:107], v129 offset:3200\n\t"
        "s_waitcnt lgkmcnt(10)\n\t"
        "v_mfma_f32_32x32x16_bf16 v[48:63], v[108:111], v[152:155], v[48:63]\n\t"
        "s_add_u32 s46, s46, 12288\n\t"
        "s_add_u32 s47, s47, 1\n\t"
        "ds_read_b64_tr_b16 v[108:109], v130 offset:128\n\t"
        "ds_read_b64_tr_b16 v[110:111], v130 offset:3200\n\t"
        "s_waitcnt lgkmcnt(10)\n\t"
        "v_mfma_f32_32x32x16_bf16 v[64:79], v[152:155], v[112:115], v[64:79]\n\t"
        "s_cmp_eq_u32 s46, 110592\n\t"
        "s_cselect_b32 s46, 0, s46\n\t"
        "ds_read_b64_tr_b16 v[112:113], v129 offset:256\n\t"
        "ds_read_b64_tr_b16 v[114:115], v129 offset:3328\n\t"
        "s_waitcnt lgkmcnt(10)\n\t"
        "v_mfma_f32_32x32x16_bf16 v[80:95], v[152:155], v[116:119], v[80:95]\n\t"
        "ds_write_b32 v127, v128\n\t"
        "ds_read_b64_tr_b16 v[116:117], v130 offset:256\n\t"
        "ds_read_b64_tr_b16 v[118:119], v130 offset:3328\n\t"
        "s_waitcnt vmcnt(12) lgkmcnt(11)\n\t"
        "v_mfma_f32_32x32x16_bf16 v[0:15], v[96:99], v[156:159], v[0:15]\n\t"
        "ds_read_b32 v126, v125\n\t"
        "ds_read_b64_tr_b16 v[96:97], v129 offset:6144\n\t"
        "ds_read_b64_tr_b16 v[98:99], v129 offset:9216\n\t"
        "s_waitcnt lgkmcnt(12)\n\t"
        "v_mfma_f32_32x32x16_bf16 v[16:31], v[100:103], v[156:159], v[16:31]\n\t"
        "v_add_u32 v123, s46, v121\n\t"
        "ds_read_b64_tr_b16 v[100:101], v130 offset:6144\n\t"
        "ds_read_b64_tr_b16 v[102:103], v130 offset:9216\n\t"
        "s_waitcnt lgkmcnt(12)\n\t"
        "v_mfma_f32_32x32x16_bf16 v[32:47], v[104:107], v[156:159], v[32:47]\n\t"
        "ds_read_b64_tr_b16 v[104:105], v129 offset:6272\n\t"
        "ds_read_b64_tr_b16 v[106:107], v129 offset:9344\n\t"
        "s_waitcnt lgkmcnt(12)\n\t"
        "v_mfma_f32_32x32x16_bf16 v[48:63], v[108:111], v[156:159], v[48:63]\n\t"
        "v_add_u32 v124, s46, v122\n\t"
        "ds_read_b64_tr_b16 v[108:109], v130 offset:6272\n\t"
        "ds_read_b64_tr_b16 v[110:111], v130 offset:9344\n\t"
        "s_waitcnt lgkmcnt(12)\n\t"
        "v_mfma_f32_32x32x16_bf16 v[64:79], v[156:159], v[112:115], v[64:79]\n\t"
        "ds_read_b64_tr_b16 v[112:113], v129 offset:6400\n\t"
        "ds_read_b64_tr_b16 v[114:115], v129 offset:9472\n\t"
        "s_waitcnt lgkmcnt(10)\n\t"
        "v_mfma_f32_32x32x16_bf16 v[80:95], v[156:159], v[116:119], v[80:95]\n\t"
        "v_cmp_gt_u32 vcc, s47, v126\n\t"
        "ds_read_b64_tr_b16 v[116:117], v130 offset:6400\n\t"
        "ds_read_b64_tr_b16 v[118:119], v130 offset:9472\n\t"
        "s_waitcnt vmcnt(11) lgkmcnt(10)\n\t"
        "v_mfma_f32_32x32x16_bf16 v[0:15], v[96:99], v[160:163], v[0:15]\n\t"
        "s_cbranch_vccnz 66f\n\t"
        "67:\n\t"
        "ds_read_b64_tr_b16 v[96:97], v123 offset:0\n\t"
        "ds_read_b64_tr_b16 v[98:99], v123 offset:3072\n\t"
        "s_waitcnt lgkmcnt(10)\n\t"
        "v_mfma_f32_32x32x16_bf16 v[16:31], v[100:103], v[160:163], v[16:31]\n\t"
        "v_add_u32 v128, 1, v128\n\t"
        "ds_read_b64_tr_b16 v[100:101], v124 offset:0\n\t"
        "ds_read_b64_tr_b16 v[102:103], v124 offset:3072\n\t"
        "s_waitcnt lgkmcnt(10)\n\t"
        "v_mfma_f32_32x32x16_bf16 v[32:47], v[104:107], v[160:163], v[32:47]\n\t"
        "ds_read_b64_tr_b16 v[104:105], v123 offset:128\n\t"
        "ds_read_b64_tr_b16 v[106:107], v123 offset:3200\n\t"
        "s_waitcnt lgkmcnt(10)\n\t"
        "v_mfma_f32_32x32x16_bf16 v[48:63], v[108:111], v[160:163], v[48:63]\n\t"
        "s_add_u32 s46, s46, 12288\n\t"
        "s_add_u32 s47, s47, 1\n\t"
        "ds_read_b64_tr_b16 v[108:109], v124 offset:128\n\t"
        "ds_read_b64_tr_b16 v[110:111], v124 offset:3200\n\t"
        "s_waitcnt lgkmcnt(10)\n\t"
        "v_mfma_f32_32x32x16_bf16 v[64:79], v[160:163], v[112:115], v[64:79]\n\t"
        "s_cmp_eq_u32 s46, 110592\n\t"
        "s_cselect_b32 s46, 0, s46\n\t"
        "ds_read_b64_tr_b16 v[112:113], v123 offset:256\n\t"
        "ds_read_b64_tr_b16 v[114:115], v123 offset:3328\n\t"
        "s_waitcnt lgkmcnt(10)\n\t"
        "v_mfma_f32_32x32x16_bf16 v[80:95], v[160:163], v[116:119], v[80:95]\n\t"
        "ds_write_b32 v127, v128\n\t"
        "ds_read_b64_tr_b16 v[116:117], v124 offset:256\n\t"
        "ds_read_b64_tr_b16 v[118:119], v124 offset:3328\n\t"
        "s_waitcnt vmcnt(10) lgkmcnt(11)\n\t"
        "v_mfma_f32_32x32x16_bf16 v[0:15], v[96:99], v[164:167], v[0:15]\n\t"
        "ds_read_b32 v126, v125\n\t"
        "ds_read_b64_tr_b16 v[96:97], v123 offset:6144\n\t"
        "ds_read_b64_tr_b16 v[98:99], v123 offset:9216\n\t"
        "s_waitcnt lgkmcnt(12)\n\t"
        "v_mfma_f32_32x32x16_bf16 v[16:31], v[100:103], v[164:167], v[16:31]\n\t"
        "v_add_u32 v129, s46, v121\n\t"
        "ds_read_b64_tr_b16 v[100:101], v124 offset:6144\n\t"
        "ds_read_b64_tr_b16 v[102:103], v124 offset:9216\n\t"
        "s_waitcnt lgkmcnt(12)\n\t"
        "v_mfma_f32_32x32x16_bf16 v[32:47], v[104:107], v[164:167], v[32:47]\n\t"
        "ds_read_b64_tr_b16 v[104:105], v123 offset:6272\n\t"
        "ds_read_b64_tr_b16 v[106:107], v123 offset:9344\n\t"
        "s_waitcnt lgkmcnt(12)\n\t"
        "v_mfma_f32_32x32x16_bf16 v[48:63], v[108:111], v[164:167], v[48:63]\n\t"
        "v_add_u32 v130, s46, v122\n\t"
        "ds_read_b64_tr_b16 v[108:109], v124 offset:6272\n\t"
        "ds_read_b64_tr_b16 v[110:111], v124 offset:9344\n\t"
        "s_waitcnt lgkmcnt(12)\n\t"
        "v_mfma_f32_32x32x16_bf16 v[64:79], v[164:167], v[112:115], v[64:79]\n\t"
        "ds_read_b64_tr_b16 v[112:113], v123 offset:6400\n\t"
        "ds_read_b64_tr_b16 v[114:115], v123 offset:9472\n\t"
        "s_waitcnt lgkmcnt(10)\n\t"
        "v_mfma_f32_32x32x16_bf16 v[80:95], v[164:167], v[116:119], v[80:95]\n\t"
        "v_cmp_gt_u32 vcc, s47, v126\n\t"
        "ds_read_b64_tr_b16 v[116:117], v124 offset:6400\n\t"
        "ds_read_b64_tr_b16 v[118:119], v124 offset:9472\n\t"
        "s_waitcnt vmcnt(9) lgkmcnt(10)\n\t"
        "v_mfma_f32_32x32x16_bf16 v[0:15], v[96:99], v[168:171], v[0:15]\n\t"
        "s_cbranch_vccnz 68f\n\t"
        "69:\n\t"
        "ds_read_b64_tr_b16 v[96:97], v129 offset:0\n\t"
        "ds_read_b64_tr_b16 v[98:99], v129 offset:3072\n\t"
        "s_waitcnt lgkmcnt(10)\n\t"
        "v_mfma_f32_32x32x16_bf16 v[16:31], v[100:103], v[168:171], v[16:31]\n\t"
        "v_add_u32 v128, 1, v128\n\t"
        "ds_read_b64_tr_b16 v[100:101], v130 offset:0\n\t"
        "ds_read_b64_tr_b16 v[102:103], v130 offset:3072\n\t"
        "s_waitcnt lgkmcnt(10)\n\t"
        "v_mfma_f32_32x32x16_bf16 v[32:47], v[104:107], v[168:171], v[32:47]\n\t"
        "ds_read_b64_tr_b16 v[104:105], v129 offset:128\n\t"
        "ds_read_b64_tr_b16 v[106:107], v129 offset:3200\n\t"
        "s_waitcnt lgkmcnt(10)\n\t"
        "v_mfma_f32_32x32x16_bf16 v[48:63], v[108:111], v[168:171], v[48:63]\n\t"
        "s_add_u32 s46, s46, 12288\n\t"
        "s_add_u32 s47, s47, 1\n\t"
        "ds_read_b64_tr_b16 v[108:109], v130 offset:128\n\t"
        "ds_read_b64_tr_b16 v[110:111], v130 offset:3200\n\t"
        "s_waitcnt lgkmcnt(10)\n\t"
        "v_mfma_f32_32x32x16_bf16 v[64:79], v[168:171], v[112:115], v[64:79]\n\t"
        "s_cmp_eq_u32 s46, 110592\n\t"
        "s_cselect_b32 s46, 0, s46\n\t"
        "ds_read_b64_tr_b16 v[112:113], v129 offset:256\n\t"
        "ds_read_b64_tr_b16 v[114:115], v129 offset:3328\n\t"
        "s_waitcnt lgkmcnt(10)\n\t"
        "v_mfma_f32_32x32x16_bf16 v[80:95], v[168:171], v[116:119], v[80:95]\n\t"
        "ds_write_b32 v127, v128\n\t"
        "ds_read_b64_tr_b16 v[116:117], v130 offset:256\n\t"
        "ds_read_b64_tr_b16 v[118:119], v130 offset:3328\n\t"
        "s_waitcnt vmcnt(8) lgkmcnt(11)\n\t"
        "v_mfma_f32_32x32x16_bf16 v[0:15], v[96:99], v[172:175], v[0:15]\n\t"
        "ds_read_b32 v126, v125\n\t"
        "ds_read_b64_tr_b16 v[96:97], v129 offset:6144\n\t"
        "ds_read_b64_tr_b16 v[98:99], v129 offset:9216\n\t"
        "s_waitcnt lgkmcnt(12)\n\t"
        "v_mfma_f32_32x32x16_bf16 v[16:31], v[100:103], v[172:175], v[16:31]\n\t"
        "v_add_u32 v123, s46, v121\n\t"
        "ds_read_b64_tr_b16 v[100:101], v130 offset:6144\n\t"
        "ds_read_b64_tr_b16 v[102:103], v130 offset:9216\n\t"
        "s_waitcnt lgkmcnt(12)\n\t"
        "v_mfma_f32_32x32x16_bf16 v[32:47], v[104:107], v[172:175], v[32:47]\n\t"
        "ds_read_b64_tr_b16 v[104:105], v129 offset:6272\n\t"
        "ds_read_b64_tr_b16 v[106:107], v129 offset:9344\n\t"
        "s_waitcnt lgkmcnt(12)\n\t"
        "v_mfma_f32_32x32x16_bf16 v[48:63], v[108:111], v[172:175], v[48:63]\n\t"
        "v_add_u32 v124, s46, v122\n\t"
        "ds_read_b64_tr_b16 v[108:109], v130 offset:6272\n\t"
        "ds_read_b64_tr_b16 v[110:111], v130 offset:9344\n\t"
        "s_waitcnt lgkmcnt(12)\n\t"
        "v_mfma_f32_32x32x16_bf16 v[64:79], v[172:175], v[112:115], v[64:79]\n\t"
        "ds_read_b64_tr_b16 v[112:113], v129 offset:6400\n\t"
        "ds_read_b64_tr_b16 v[114:115], v129 offset:9472\n\t"
        "s_waitcnt lgkmcnt(10)\n\t"
        "v_mfma_f32_32x32x16_bf16 v[80:95], v[172:175], v[116:119], v[80:95]\n\t"
        "v_cmp_gt_u32 vcc, s47, v126\n\t"
        "ds_read_b64_tr_b16 v[116:117], v130 offset:6400\n\t"
        "ds_read_b64_tr_b16 v[118:119], v130 offset:9472\n\t"
        "s_waitcnt vmcnt(7) lgkmcnt(10)\n\t"
        "v_mfma_f32_32x32x16_bf16 v[0:15], v[96:99], v[176:179], v[0:15]\n\t"
        "s_cbranch_vccnz 70f\n\t"
        "71:\n\t"
        "ds_read_b64_tr_b16 v[96:97], v123 offset:0\n\t"
        "ds_read_b64_tr_b16 v[98:99], v123 offset:3072\n\t"
        "s_waitcnt lgkmcnt(10)\n\t"
        "v_mfma_f32_32x32x16_bf16 v[16:31], v[100:103], v[176:179], v[16:31]\n\t"
        "v_add_u32 v128, 1, v128\n\t"
        "ds_read_b64_tr_b16 v[100:101], v124 offset:0\n\t"
        "ds_read_b64_tr_b16 v[102:103], v124 offset:3072\n\t"
        "s_waitcnt lgkmcnt(10)\n\t"
        "v_mfma_f32_32x32x16_bf16 v[32:47], v[104:107], v[176:179], v[32:47]\n\t"
        "ds_read_b64_tr_b16 v[104:105], v123 offset:128\n\t"
        "ds_read_b64_tr_b16 v[106:107], v123 offset:3200\n\t"
        "s_waitcnt lgkmcnt(10)\n\t"
        "v_mfma_f32_32x32x16_bf16 v[48:63], v[108:111], v[176:179], v[48:63]\n\t"
        "s_add_u32 s46, s46, 12288\n\t"
        "s_add_u32 s47, s47, 1\n\t"
        "ds_read_b64_tr_b16 v[108:109], v124 offset:128\n\t"
        "ds_read_b64_tr_b16 v[110:111], v124 offset:3200\n\t"
        "s_waitcnt lgkmcnt(10)\n\t"
        "v_mfma_f32_32x32x16_bf16 v[64:79], v[176:179], v[112:115], v[64:79]\n\t"
        "s_cmp_eq_u32 s46, 110592\n\t"
        "s_cselect_b32 s46, 0, s46\n\t"
        "ds_read_b64_tr_b16 v[112:113], v123 offset:256\n\t"
        "ds_read_b64_tr_b16 v[114:115], v123 offset:3328\n\t"
        "s_waitcnt lgkmcnt(10)\n\t"
        "v_mfma_f32_32x32x16_bf16 v[80:95], v[176:179], v[116:119], v[80:95]\n\t"
        "ds_write_b32 v127, v128\n\t"
        "ds_read_b64_tr_b16 v[116:117], v124 offset:256\n\t"
        "ds_read_b64_tr_b16 v[118:119], v124 offset:3328\n\t"
        "s_waitcnt vmcnt(6) lgkmcnt(11)\n\t"
        "v_mfma_f32_32x32x16_bf16 v[0:15], v[96:99], v[180:183], v[0:15]\n\t"
        "ds_read_b32 v126, v125\n\t"
        "ds_read_b64_tr_b16 v[96:97], v123 offset:6144\n\t"
        "ds_read_b64_tr_b16 v[98:99], v123 offset:9216\n\t"
        "s_waitcnt lgkmcnt(12)\n\t"
        "v_mfma_f32_32x32x16_bf16 v[16:31], v[100:103], v[180:183], v[16:31]\n\t"
        "v_add_u32 v129, s46, v121\n\t"
        "ds_read_b64_tr_b16 v[100:101], v124 offset:6144\n\t"
        "ds_read_b64_tr_b16 v[102:103], v124 offset:9216\n\t"
        "s_waitcnt lgkmcnt(12)\n\t"
        "v_mfma_f32_32x32x16_bf16 v[32:47], v[104:107], v[180:183], v[32:47]\n\t"
        "ds_read_b64_tr_b16 v[104:105], v123 offset:6272\n\t"
        "ds_read_b64_tr_b16 v[106:107], v123 offset:9344\n\t"
        "s_waitcnt lgkmcnt(12)\n\t"
        "v_mfma_f32_32x32x16_bf16 v[48:63], v[108:111], v[180:183], v[48:63]\n\t"
        "v_add_u32 v130, s46, v122\n\t"
        "ds_read_b64_tr_b16 v[108:109], v124 offset:6272\n\t"
        "ds_read_b64_tr_b16 v[110:111], v124 offset:9344\n\t"
        "s_waitcnt lgkmcnt(12)\n\t"
        "v_mfma_f32_32x32x16_bf16 v[64:79], v[180:183], v[112:115], v[64:79]\n\t"
        "ds_read_b64_tr_b16 v[112:113], v123 offset:6400\n\t"
        "ds_read_b64_tr_b16 v[114:115], v123 offset:9472\n\t"
        "s_waitcnt lgkmcnt(10)\n\t"
        "v_mfma_f32_32x32x16_bf16 v[80:95], v[180:183], v[116:119], v[80:95]\n\t"
        "v_cmp_gt_u32 vcc, s47, v126\n\t"
        "ds_read_b64_tr_b16 v[116:117], v124 offset:6400\n\t"
        "ds_read_b64_tr_b16 v[118:119], v124 offset:9472\n\t"
        "s_waitcnt vmcnt(5) lgkmcnt(10)\n\t"
        "v_mfma_f32_32x32x16_bf16 v[0:15], v[96:99], v[184:187], v[0:15]\n\t"
        "s_cbranch_vccnz 72f\n\t"
        "73:\n\t"
        "ds_read_b64_tr_b16 v[96:97], v129 offset:0\n\t"
        "ds_read_b64_tr_b16 v[98:99], v129 offset:3072\n\t"
        "s_waitcnt lgkmcnt(10)\n\t"
        "v_mfma_f32_32x32x16_bf16 v[16:31], v[100:103], v[184:187], v[16:31]\n\t"
        "v_add_u32 v128, 1, v128\n\t"
        "ds_read_b64_tr_b16 v[100:101], v130 offset:0\n\t"
        "ds_read_b64_tr_b16 v[102:103], v130 offset:3072\n\t"
        "s_waitcnt lgkmcnt(10)\n\t"
        "v_mfma_f32_32x32x16_bf16 v[32:47], v[104:107], v[184:187], v[32:47]\n\t"
        "ds_read_b64_tr_b16 v[104:105], v129 offset:128\n\t"
        "ds_read_b64_tr_b16 v[106:107], v129 offset:3200\n\t"
        "s_waitcnt lgkmcnt(10)\n\t"
        "v_mfma_f32_32x32x16_bf16 v[48:63], v[108:111], v[184:187], v[48:63]\n\t"
        "s_add_u32 s46, s46, 12288\n\t"
        "s_add_u32 s47, s47, 1\n\t"
        "ds_read_b64_tr_b16 v[108:109], v130 offset:128\n\t"
        "ds_read_b64_tr_b16 v[110:111], v130 offset:3200\n\t"
        "s_waitcnt lgkmcnt(10)\n\t"
        "v_mfma_f32_32x32x16_bf16 v[64:79], v[184:187], v[112:115], v[64:79]\n\t"
        "s_cmp_eq_u32 s46, 110592\n\t"
        "s_cselect_b32 s46, 0, s46\n\t"
        "ds_read_b64_tr_b16 v[112:113], v129 offset:256\n\t"
        "ds_read_b64_tr_b16 v[114:115], v129 offset:3328\n\t"
        "s_waitcnt lgkmcnt(10)\n\t"
        "v_mfma_f32_32x32x16_bf16 v[80:95], v[184:187], v[116:119], v[80:95]\n\t"
        "ds_write_b32 v127, v128\n\t"
        "ds_read_b64_tr_b16 v[116:117], v130 offset:256\n\t"
        "ds_read_b64_tr_b16 v[118:119], v130 offset:3328\n\t"
        "s_waitcnt vmcnt(4) lgkmcnt(11)\n\t"
        "v_mfma_f32_32x32x16_bf16 v[0:15], v[96:99], v[188:191], v[0:15]\n\t"
        "ds_read_b64_tr_b16 v[96:97], v129 offset:6144\n\t"
        "ds_read_b64_tr_b16 v[98:99], v129 offset:9216\n\t"
        "s_waitcnt lgkmcnt(11)\n\t"
        "v_mfma_f32_32x32x16_bf16 v[16:31], v[100:103], v[188:191], v[16:31]\n\t"
        "ds_read_b64_tr_b16 v[100:101], v130 offset:6144\n\t"
        "ds_read_b64_tr_b16 v[102:103], v130 offset:9216\n\t"
        "s_waitcnt lgkmcnt(11)\n\t"
        "v_mfma_f32_32x32x16_bf16 v[32:47], v[104:107], v[188:191], v[32:47]\n\t"
        "ds_read_b64_tr_b16 v[104:105], v129 offset:6272\n\t"
        "ds_read_b64_tr_b16 v[106:107], v129 offset:9344\n\t"
        "s_waitcnt lgkmcnt(11)\n\t"
        "v_mfma_f32_32x32x16_bf16 v[48:63], v[108:111], v[188:191], v[48:63]\n\t"
        "ds_read_b64_tr_b16 v[108:109], v130 offset:6272\n\t"
        "ds_read_b64_tr_b16 v[110:111], v130 offset:9344\n\t"
        "s_waitcnt lgkmcnt(11)\n\t"
        "v_mfma_f32_32x32x16_bf16 v[64:79], v[188:191], v[112:115], v[64:79]\n\t"
        "ds_read_b64_tr_b16 v[112:113], v129 offset:6400\n\t"
        "ds_read_b64_tr_b16 v[114:115], v129 offset:9472\n\t"
        "s_waitcnt lgkmcnt(10)\n\t"
        "v_mfma_f32_32x32x16_bf16 v[80:95], v[188:191], v[116:119], v[80:95]\n\t"
        "ds_read_b64_tr_b16 v[116:117], v130 offset:6400\n\t"
        "ds_read_b64_tr_b16 v[118:119], v130 offset:9472\n\t"
        "s_waitcnt vmcnt(3) lgkmcnt(10)\n\t"
        "v_mfma_f32_32x32x16_bf16 v[0:15], v[96:99], v[192:195], v[0:15]\n\t"
        "s_waitcnt lgkmcnt(8)\n\t"
        "v_mfma_f32_32x32x16_bf16 v[16:31], v[100:103], v[192:195], v[16:31]\n\t"
        "s_waitcnt lgkmcnt(6)\n\t"
        "v_mfma_f32_32x32x16_bf16 v[32:47], v[104:107], v[192:195], v[32:47]\n\t"
        "s_waitcnt lgkmcnt(4)\n\t"
        "v_mfma_f32_32x32x16_bf16 v[48:63], v[108:111], v[192:195], v[48:63]\n\t"
        "s_waitcnt lgkmcnt(2)\n\t"
        "v_mfma_f32_32x32x16_bf16 v[64:79], v[192:195], v[112:115], v[64:79]\n\t"
        "s_waitcnt lgkmcnt(0)\n\t"
        "v_mfma_f32_32x32x16_bf16 v[80:95], v[192:195], v[116:119], v[80:95]\n\t"
        "s_waitcnt vmcnt(0)\n\t"
        "s_nop 10\n\t"
        "s_branch 9f\n\t"
        "16:\n\t"
        "s_mov_b32 s48, 0x10000\n\t"
        "8:\n\t"
        "ds_read_b32 v126, v125\n\t"
        "s_sub_u32 s48, s48, 1\n\t"
        "s_waitcnt lgkmcnt(0)\n\t"
        "s_cbranch_scc1 17b\n\t"
        "v_cmp_gt_u32 vcc, s47, v126\n\t"
        "s_cbranch_vccnz 8b\n\t"
        "s_branch 17b\n\t"
        "18:\n\t"
        "s_mov_b32 s48, 0x10000\n\t"
        "8:\n\t"
        "ds_read_b32 v126, v125\n\t"
        "s_sub_u32 s48, s48, 1\n\t"
        "s_waitcnt lgkmcnt(0)\n\t"
        "s_cbranch_scc1 19b\n\t"
        "v_cmp_gt_u32 vcc, s47, v126\n\t"
        "s_cbranch_vccnz 8b\n\t"
        "s_branch 19b\n\t"
        "20:\n\t"
        "s_mov_b32 s48, 0x10000\n\t"
        "8:\n\t"
        "ds_read_b32 v126, v125\n\t"
        "s_sub_u32 s48, s48, 1\n\t"
        "s_waitcnt lgkmcnt(0)\n\t"
        "s_cbranch_scc1 21b\n\t"
        "v_cmp_gt_u32 vcc, s47, v126\n\t"
        "s_cbranch_vccnz 8b\n\t"
        "s_branch 21b\n\t"
        "22:\n\t"
        "s_mov_b32 s48, 0x10000\n\t"
        "8:\n\t"
        "ds_read_b32 v126, v125\n\t"
        "s_sub_u32 s48, s48, 1\n\t"
        "s_waitcnt lgkmcnt(0)\n\t"
        "s_cbranch_scc1 23b\n\t"
        "v_cmp_gt_u32 vcc, s47, v126\n\t"
        "s_cbranch_vccnz 8b\n\t"
        "s_branch 23b\n\t"
        "24:\n\t"
        "s_mov_b32 s48, 0x10000\n\t"
        "8:\n\t"
        "ds_read_b32 v126, v125\n\t"
        "s_sub_u32 s48, s48, 1\n\t"
        "s_waitcnt lgkmcnt(0)\n\t"
        "s_cbranch_scc1 25b\n\t"
        "v_cmp_gt_u32 vcc, s47, v126\n\t"
        "s_cbranch_vccnz 8b\n\t"
        "s_branch 25b\n\t"
        "26:\n\t"
        "s_mov_b32 s48, 0x10000\n\t"
        "8:\n\t"
        "ds_read_b32 v126, v125\n\t"
        "s_sub_u32 s48, s48, 1\n\t"
        "s_waitcnt lgkmcnt(0)\n\t"
        "s_cbranch_scc1 27b\n\t"
        "v_cmp_gt_u32 vcc, s47, v126\n\t"
        "s_cbranch_vccnz 8b\n\t"
        "s_branch 27b\n\t"
        "28:\n\t"
        "s_mov_b32 s48, 0x10000\n\t"
        "8:\n\t"
        "ds_read_b32 v126, v125\n\t"
        "s_sub_u32 s48, s48, 1\n\t"
        "s_waitcnt lgkmcnt(0)\n\t"
        "s_cbranch_scc1 29b\n\t"
        "v_cmp_gt_u32 vcc, s47, v126\n\t"
        "s_cbranch_vccnz 8b\n\t"
        "s_branch 29b\n\t"
        "30:\n\t"
        "s_mov_b32 s48, 0x10000\n\t"
        "8:\n\t"
        "ds_read_b32 v126, v125\n\t"
        "s_sub_u32 s48, s48, 1\n\t"
        "s_waitcnt lgkmcnt(0)\n\t"
        "s_cbranch_scc1 31b\n\t"
        "v_cmp_gt_u32 vcc, s47, v126\n\t"
        "s_cbranch_vccnz 8b\n\t"
        "s_branch 31b\n\t"
        "64:\n\t"
        "s_mov_b32 s48, 0x10000\n\t"
        "8:\n\t"
        "ds_read_b32 v126, v125\n\t"
        "s_sub_u32 s48, s48, 1\n\t"
        "s_waitcnt lgkmcnt(0)\n\t"
        "s_cbranch_scc1 65b\n\t"
        "v_cmp_gt_u32 vcc, s47, v126\n\t"
        "s_cbranch_vccnz 8b\n\t"
        "s_branch 65b\n\t"
        "66:\n\t"
        "s_mov_b32 s48, 0x10000\n\t"
        "8:\n\t"
        "ds_read_b32 v126, v125\n\t"
        "s_sub_u32 s48, s48, 1\n\t"
        "s_waitcnt lgkmcnt(0)\n\t"
        "s_cbranch_scc1 67b\n\t"
        "v_cmp_gt_u32 vcc, s47, v126\n\t"
        "s_cbranch_vccnz 8b\n\t"
        "s_branch 67b\n\t"
        "68:\n\t"
        "s_mov_b32 s48, 0x10000\n\t"
        "8:\n\t"
        "ds_read_b32 v126, v125\n\t"
        "s_sub_u32 s48, s48, 1\n\t"
        "s_waitcnt lgkmcnt(0)\n\t"
        "s_cbranch_scc1 69b\n\t"
        "v_cmp_gt_u32 vcc, s47, v126\n\t"
        "s_cbranch_vccnz 8b\n\t"
        "s_branch 69b\n\t"
        "70:\n\t"
        "s_mov_b32 s48, 0x10000\n\t"
        "8:\n\t"
        "ds_read_b32 v126, v125\n\t"
        "s_sub_u32 s48, s48, 1\n\t"
        "s_waitcnt lgkmcnt(0)\n\t"
        "s_cbranch_scc1 71b\n\t"
        "v_cmp_gt_u32 vcc, s47, v126\n\t"
        "s_cbranch_vccnz 8b\n\t"
        "s_branch 71b\n\t"
        "72:\n\t"
        "s_mov_b32 s48, 0x10000\n\t"
        "8:\n\t"
        "ds_read_b32 v126, v125\n\t"
        "s_sub_u32 s48, s48, 1\n\t"
        "s_waitcnt lgkmcnt(0)\n\t"
        "s_cbranch_scc1 73b\n\t"
        "v_cmp_gt_u32 vcc, s47, v126\n\t"
        "s_cbranch_vccnz 8b\n\t"
        "s_branch 73b\n\t"
        "9:\n\t"
        : "+{v[0:15]}"(acc[0]), "+{v[16:31]}"(acc[1]), "+{v[32:47]}"(acc[2]), "+{v[48:63]}"(acc[3]), "+{v[64:79]}"(acc[4]), "+{v[80:95]}"(acc[5]),
          "+{v[96:111]}"(stA), "+{v[112:127]}"(stB), "+{v[128:131]}"(stC), "+{v[148:163]}"(xr1), "+{v[164:179]}"(xr2), "+{v[180:195]}"(xr3),
          "+{s44}"(soff), "+{s46}"(slot), "+{s47}"(need)
        : "{s[40:43]}"(srd) : "v132", "v133", "v134", "v135", "v136", "v137", "v138", "v139", "v140", "v141", "v142", "v143", "v144", "v145", "v146", "v147", "s45", "s48", "vcc", "scc", "memory");
      STAMPV(4 + 16 * L);
      PHASE_VARS;
      if (L == 1) {
        f32x4 av[4], bv[4], cs[4][4];
        float cv[2];
#pragma unroll
        for (int g = 0; g < 4; ++g) {
          av[g] = *reinterpret_cast<const f32x4*>(smem + L_AB + wid * 256 + (8 * g + 4 * hi) * 4);
          bv[g] = *reinterpret_cast<const f32x4*>(smem + L_AB + wid * 256 + 128 + (8 * g + 4 * hi) * 4);
        }
#pragma unroll
        for (int j = 5; j >= 0; --j) {
          const int cb = (j >> 1) * 64 + ((j & 1) ^ hh) * 32;
          if (j >= 4) cv[j - 4] = *reinterpret_cast<const float*>(smem + L_CS + (cb + r32) * 4);
          else {
#pragma unroll
            for (int g = 0; g < 4; ++g) cs[j][g] = *reinterpret_cast<const f32x4*>(smem + L_CS + (cb + 8 * g + 4 * hi) * 4);
          }
        }
        SBAR();
#pragma unroll
        for (int j = 5; j >= 0; --j)
#pragma unroll
          for (int g = 0; g < 4; ++g)
#pragma unroll
            for (int e = 0; e < 4; ++e)
              acc[j][4 * g + e] = j < 4 ? al * acc[j][4 * g + e] + be * cs[j & 3][g][e] : av[g][e] * acc[j][4 * g + e] + bv[g][e] * cv[j & 1];
        SBAR();
        lds_wait_ge(lbase + L_W + 4 + 4 * (lane & 1), 1);
        const int idx = (int)__builtin_amdgcn_readfirstlane(words[3]);
        const bool mine = (idx >> 5) == tile;
        if (mine) {
#pragma unroll
          for (int j = 0; j < 6; ++j) {
            const int cb = (j >> 1) * 64 + ((j & 1) ^ hh) * 32;
#pragma unroll
            for (int g = 0; g < 4; ++g) {
              if (j < 4) {
                const f32x4 pv = *reinterpret_cast<const f32x4*>(smem + L_PATCH + (cb + 8 * g + 4 * hi) * 4);
#pragma unroll
                for (int e = 0; e < 4; ++e) acc[j][4 * g + e] = r32 == (idx & 31) ? pv[e] : acc[j][4 * g + e];
              } else {
                const float pv = *reinterpret_cast<const float*>(smem + L_PATCH + (cb + r32) * 4);
#pragma unroll
                for (int e = 0; e < 4; ++e) acc[j][4 * g + e] = (8 * g + 4 * hi + e) == (idx & 31) ? pv : acc[j][4 * g + e];
              }
            }
          }
        }
      }
      constexpr float QS = 0.17677669529663687f * 1.4426950408889634f;
      u32x4 own[4];
#pragma unroll
      for (int j = 5; j >= 0; --j) {
        u32x4 o[2];
#pragma unroll
        for (int e = 0; e < 8; ++e) {
          unsigned w = pack2(acc[j][2 * e], acc[j][2 * e + 1]);
          if (j < 2) w = pack2(lo16(w) * QS, hi16(w) * QS);
          o[e >> 2][e & 3] = w;
        }
        if (j >= 4) { o[0] = fo_swap(o[0]); o[1] = fo_swap(o[1]); }
        const int hs = (j & 1) ^ hh;
        if (j >= 2) {
          const unsigned dst = OFF_KV + L * (10 * MBY) + (tn * 2 + hs) * HS + tile * 4096 + ((j >> 1) - 1) * 2048 + lane * 16;
          stg(rs, dst, o[0]); stg(rs, dst + 1024, o[1]);
        }
        if (j >= 2 && !(j & 1)) { own[j - 2] = o[0]; own[j - 1] = o[1]; }
        else {
          char* p = smem + L_XCH + (j < 2 ? (hs * 4 + wid) * 2048 : 16384 + wid * 4096 + ((j >> 1) - 1) * 2048) + lane * 16;
          *reinterpret_cast<u32x4*>(p) = o[0]; *reinterpret_cast<u32x4*>(p + 1024) = o[1];
        }
        SBAR();
      }
      BAR_LGKM();
      kva = cat4(own[0], own[1], own[2], own[3]);
      const u32x4* pk = reinterpret_cast<const u32x4*>(smem + L_XCH + 16384 + (wid ^ 1) * 4096 + lane * 16);
#pragma unroll
      for (int u = 0; u < 4; ++u) {
        const u32x4* p = reinterpret_cast<const u32x4*>(smem + L_XCH + (hh * 4 + ((2 * kh + 2 + u) & 3)) * 2048 + lane * 16);
        q[u] = __builtin_shufflevector(p[0], p[64], 0, 1, 2, 3, 4, 5, 6, 7);
      }
      kvb = cat4(pk[0], pk[64], pk[128], pk[192]);
    }
    STAMPV(5 + 16 * L);
    {
      u32x4 ones;
      unsigned kvoff;
      u32x8 fv, cv;
      asm volatile("" : "={v[184:191]}"(fv), "={v[216:223]}"(cv));
      { PHASE_VARS;
        const unsigned fl = FL_KV + (L * 16 + tn) * 8192;
        const unsigned v = (lane & 3) == 0 ? 0x3F803F80u : 0u; const bool up = (lane & 4) != 0; ones[0] = ones[1] = up ? 0u : v; ones[2] = ones[3] = up ? v : 0u;
        kvoff = OFF_KV + L * (10 * MBY) + (tn * 2 + hh) * HS + kh * 8192 + lane * 16;
        fv[0] = lane == 0 ? fl + (kh * 32 + tm * 2 + hh) * 128 : OOB;
        fv[1] = fl + (hi ? (kh ^ 1) * 32 + tm * 2 + (lane & 1) : kh * 32 + r32) * 128;
        fv[2] = TAG; fv[3] = lbase + L_FREE + wid * 256 + lane * 4; fv[4] = 32 * L + 32;
        cv[0] = lbase + L_XCH + wid * 8192 + lane * 16; cv[1] = lbase + L_XCH + (wid ^ 2) * 8192 + lane * 16;
        cv[2] = lbase + L_XCH + 32768 + wid * 512 + lane * 4; cv[3] = lbase + L_XCH + 32768 + (wid ^ 2) * 512 + lane * 4;
        cv[4] = lbase + L_W + 48 + wid * 4; cv[5] = lbase + L_W + 48 + (wid ^ 2) * 4; cv[6] = L + 1;
        cv[7] = ((tile * 32 + r32) * D_ + tn * 64 + hi * 8) * 2; }
      f32x16 o0, o1, o2, o3, lv;
      unsigned soff = tm * 16384;
      STAMP_DECL;
      asm volatile(
        "s_nop 1\n\t"
        "v_mfma_f32_32x32x16_bf16 v[96:111], v[152:155], v[64:67], 0\n\t"
        "v_mfma_f32_32x32x16_bf16 v[96:111], v[156:159], v[68:71], v[96:111]\n\t"
        "s_mov_b32 s46, 0\n\t"
        "v_mov_b32 v0, 0\n\t"
        "v_mov_b32 v1, 0\n\t"
        "v_mov_b32 v2, 0\n\t"
        "v_mov_b32 v3, 0\n\t"
        "v_mov_b32 v4, 0\n\t"
        "v_mov_b32 v5, 0\n\t"
        "v_mov_b32 v6, 0\n\t"
        "v_mov_b32 v7, 0\n\t"
        "v_mov_b32 v8, 0\n\t"
        "v_mov_b32 v9, 0\n\t"
        "v_mov_b32 v10, 0\n\t"
        "v_mov_b32 v11, 0\n\t"
        "v_mov_b32 v12, 0\n\t"
        "v_mov_b32 v13, 0\n\t"
        "v_mov_b32 v14, 0\n\t"
        "v_mov_b32 v15, 0\n\t"
        "v_mov_b32 v16, 0\n\t"
        "v_mov_b32 v17, 0\n\t"
        "v_mov_b32 v18, 0\n\t"
        "v_mov_b32 v19, 0\n\t"
        "v_mov_b32 v20, 0\n\t"
        "v_mov_b32 v21, 0\n\t"
        "v_mov_b32 v22, 0\n\t"
        "v_mov_b32 v23, 0\n\t"
        "v_mov_b32 v24, 0\n\t"
        "v_mov_b32 v25, 0\n\t"
        "v_mov_b32 v26, 0\n\t"
        "v_mov_b32 v27, 0\n\t"
        "v_mov_b32 v28, 0\n\t"
        "v_mov_b32 v29, 0\n\t"
        "v_mov_b32 v30, 0\n\t"
        "v_mov_b32 v31, 0\n\t"
        "v_mov_b32 v32, 0\n\t"
        "v_mov_b32 v33, 0\n\t"
        "v_mov_b32 v34, 0\n\t"
        "v_mov_b32 v35, 0\n\t"
        "v_mov_b32 v36, 0\n\t"
        "v_mov_b32 v37, 0\n\t"
        "v_mov_b32 v38, 0\n\t"
        "v_mov_b32 v39, 0\n\t"
        "v_mov_b32 v40, 0\n\t"
        "v_mov_b32 v41, 0\n\t"
        "v_mov_b32 v42, 0\n\t"
        "v_mov_b32 v43, 0\n\t"
        "v_mov_b32 v44, 0\n\t"
        "v_mov_b32 v45, 0\n\t"
        "v_mov_b32 v46, 0\n\t"
        "v_mov_b32 v47, 0\n\t"
        "v_mov_b32 v48, 0\n\t"
        "v_mov_b32 v49, 0\n\t"
        "v_mov_b32 v50, 0\n\t"
        "v_mov_b32 v51, 0\n\t"
        "v_mov_b32 v52, 0\n\t"
        "v_mov_b32 v53, 0\n\t"
        "v_mov_b32 v54, 0\n\t"
        "v_mov_b32 v55, 0\n\t"
        "v_mov_b32 v56, 0\n\t"
        "v_mov_b32 v57, 0\n\t"
        "v_mov_b32 v58, 0\n\t"
        "v_mov_b32 v59, 0\n\t"
        "v_mov_b32 v60, 0\n\t"
        "v_mov_b32 v61, 0\n\t"
        "v_mov_b32 v62, 0\n\t"
        "v_mov_b32 v63, 0\n\t"
        "v_mov_b32 v128, 0\n\t"
        "v_mov_b32 v129, 0\n\t"
        "v_mov_b32 v130, 0\n\t"
        "v_mov_b32 v131, 0\n\t"
        "v_mov_b32 v132, 0\n\t"
        "v_mov_b32 v133, 0\n\t"
        "v_mov_b32 v134, 0\n\t"
        "v_mov_b32 v135, 0\n\t"
        "v_mov_b32 v136, 0\n\t"
        "v_mov_b32 v137, 0\n\t"
        "v_mov_b32 v138, 0\n\t"
        "v_mov_b32 v139, 0\n\t"
        "v_mov_b32 v140, 0\n\t"
        "v_mov_b32 v141, 0\n\t"
        "v_mov_b32 v142, 0\n\t"
        "v_mov_b32 v143, 0\n\t"
        "s_waitcnt vmcnt(0)\n\t"
        "buffer_store_dword v186, v184, s[40:43], 0 offen sc1\n\t"
        "v_exp_f32 v96, v96\n\t"
        "v_exp_f32 v97, v97\n\t"
        "v_exp_f32 v98, v98\n\t"
        "v_exp_f32 v99, v99\n\t"
        "v_exp_f32 v100, v100\n\t"
        "v_exp_f32 v101, v101\n\t"
        "v_mfma_f32_32x32x16_bf16 v[112:127], v[152:155], v[72:75], 0\n\t"
        "v_exp_f32 v102, v102\n\t"
        "v_exp_f32 v103, v103\n\t"
        "v_exp_f32 v104, v104\n\t"
        "v_mfma_f32_32x32x16_bf16 v[112:127], v[156:159], v[76:79], v[112:127]\n\t"
        "v_exp_f32 v105, v105\n\t"
        "v_exp_f32 v106, v106\n\t"
        "v_exp_f32 v107, v107\n\t"
        "v_smfmac_f32_32x32x32_bf16 v[0:15], v[160:163], v[96:103], v148\n\t"
        "v_exp_f32 v108, v108\n\t"
        "v_exp_f32 v109, v109\n\t"
        "v_exp_f32 v110, v110\n\t"
        "v_smfmac_f32_16x16x64_bf16 v[128:131], v[144:147], v[96:103], v148\n\t"
        "v_exp_f32 v111, v111\n\t"
        "v_exp_f32 v112, v112\n\t"
        "v_exp_f32 v113, v113\n\t"
        "v_smfmac_f32_32x32x32_bf16 v[0:15], v[164:167], v[104:111], v148\n\t"
        "v_exp_f32 v114, v114\n\t"
        "v_exp_f32 v115, v115\n\t"
        "v_exp_f32 v116, v116\n\t"
        "v_smfmac_f32_16x16x64_bf16 v[128:131], v[144:147], v[104:111], v148\n\t"
        "v_exp_f32 v117, v117\n\t"
        "v_mfma_f32_32x32x16_bf16 v[96:111], v[152:155], v[80:83], 0\n\t"
        "v_exp_f32 v118, v118\n\t"
        "v_exp_f32 v119, v119\n\t"
        "v_exp_f32 v120, v120\n\t"
        "v_mfma_f32_32x32x16_bf16 v[96:111], v[156:159], v[84:87], v[96:111]\n\t"
        "v_exp_f32 v121, v121\n\t"
        "v_exp_f32 v122, v122\n\t"
        "v_exp_f32 v123, v123\n\t"
        "v_smfmac_f32_32x32x32_bf16 v[16:31], v[160:163], v[112:119], v148\n\t"
        "v_exp_f32 v124, v124\n\t"
        "v_exp_f32 v125, v125\n\t"
        "v_exp_f32 v126, v126\n\t"
        "v_smfmac_f32_16x16x64_bf16 v[132:135], v[144:147], v[112:119], v148\n\t"
        "v_exp_f32 v127, v127\n\t"
        "v_exp_f32 v96, v96\n\t"
        "v_exp_f32 v97, v97\n\t"
        "v_smfmac_f32_32x32x32_bf16 v[16:31], v[164:167], v[120:127], v148\n\t"
        "v_exp_f32 v98, v98\n\t"
        "v_exp_f32 v99, v99\n\t"
        "v_exp_f32 v100, v100\n\t"
        "v_smfmac_f32_16x16x64_bf16 v[132:135], v[144:147], v[120:127], v148\n\t"
        "v_exp_f32 v101, v101\n\t"
        "v_mfma_f32_32x32x16_bf16 v[112:127], v[152:155], v[88:91], 0\n\t"
        "v_exp_f32 v102, v102\n\t"
        "v_exp_f32 v103, v103\n\t"
        "v_exp_f32 v104, v104\n\t"
        "v_mfma_f32_32x32x16_bf16 v[112:127], v[156:159], v[92:95], v[112:127]\n\t"
        "v_exp_f32 v105, v105\n\t"
        "v_exp_f32 v106, v106\n\t"
        "v_exp_f32 v107, v107\n\t"
        "v_smfmac_f32_32x32x32_bf16 v[32:47], v[160:163], v[96:103], v148\n\t"
        "v_exp_f32 v108, v108\n\t"
        "v_exp_f32 v109, v109\n\t"
        "v_exp_f32 v110, v110\n\t"
        "v_smfmac_f32_16x16x64_bf16 v[136:139], v[144:147], v[96:103], v148\n\t"
        "v_exp_f32 v111, v111\n\t"
        "buffer_load_dword v151, v185, s[40:43], 0 offen sc1\n\t"
        "v_exp_f32 v112, v112\n\t"
        "v_exp_f32 v113, v113\n\t"
        "v_smfmac_f32_32x32x32_bf16 v[32:47], v[164:167], v[104:111], v148\n\t"
        "v_exp_f32 v114, v114\n\t"
        "v_exp_f32 v115, v115\n\t"
        "v_exp_f32 v116, v116\n\t"
        "v_smfmac_f32_16x16x64_bf16 v[136:139], v[144:147], v[104:111], v148\n\t"
        "v_exp_f32 v117, v117\n\t"
        "v_mfma_f32_32x32x16_bf16 v[96:111], v[168:171], v[64:67], 0\n\t"
        "v_exp_f32 v118, v118\n\t"
        "v_exp_f32 v119, v119\n\t"
        "v_exp_f32 v120, v120\n\t"
        "v_mfma_f32_32x32x16_bf16 v[96:111], v[172:175], v[68:71], v[96:111]\n\t"
        "v_exp_f32 v121, v121\n\t"
        "v_exp_f32 v122, v122\n\t"
        "v_exp_f32 v123, v123\n\t"
        "v_smfmac_f32_32x32x32_bf16 v[48:63], v[160:163], v[112:119], v148\n\t"
        "v_exp_f32 v124, v124\n\t"
        "v_exp_f32 v125, v125\n\t"
        "v_exp_f32 v126, v126\n\t"
        "v_smfmac_f32_16x16x64_bf16 v[140:143], v[144:147], v[112:119], v148\n\t"
        "v_exp_f32 v127, v127\n\t"
        "v_exp_f32 v96, v96\n\t"
        "v_exp_f32 v97, v97\n\t"
        "v_smfmac_f32_32x32x32_bf16 v[48:63], v[164:167], v[120:127], v148\n\t"
        "v_exp_f32 v98, v98\n\t"
        "v_exp_f32 v99, v99\n\t"
        "v_exp_f32 v100, v100\n\t"
        "v_smfmac_f32_16x16x64_bf16 v[140:143], v[144:147], v[120:127], v148\n\t"
        "v_exp_f32 v101, v101\n\t"
        "v_mfma_f32_32x32x16_bf16 v[112:127], v[168:171], v[72:75], 0\n\t"
        "v_exp_f32 v102, v102\n\t"
        "v_exp_f32 v103, v103\n\t"
        "v_exp_f32 v104, v104\n\t"
        "v_mfma_f32_32x32x16_bf16 v[112:127], v[172:175], v[76:79], v[112:127]\n\t"
        "v_exp_f32 v105, v105\n\t"
        "v_exp_f32 v106, v106\n\t"
        "v_exp_f32 v107, v107\n\t"
        "v_smfmac_f32_32x32x32_bf16 v[0:15], v[176:179], v[96:103], v148\n\t"
        "v_exp_f32 v108, v108\n\t"
        "v_exp_f32 v109, v109\n\t"
        "v_exp_f32 v110, v110\n\t"
        "v_smfmac_f32_16x16x64_bf16 v[128:131], v[144:147], v[96:103], v148\n\t"
        "v_exp_f32 v111, v111\n\t"
        "s_cmp_lg_u32 s46, 0\n\t"
        "s_cbranch_scc1 31f\n\t"
        "s_waitcnt vmcnt(0)\n\t"
        "v_cmp_ne_u32 vcc, v186, v151\n\t"
        "s_cbranch_vccz 30f\n\t"
        "buffer_load_dword v151, v185, s[40:43], 0 offen sc1\n\t"
        "31:\n\t"
        "v_exp_f32 v112, v112\n\t"
        "v_exp_f32 v113, v113\n\t"
        "v_smfmac_f32_32x32x32_bf16 v[0:15], v[180:183], v[104:111], v148\n\t"
        "v_exp_f32 v114, v114\n\t"
        "v_exp_f32 v115, v115\n\t"
        "v_exp_f32 v116, v116\n\t"
        "v_smfmac_f32_16x16x64_bf16 v[128:131], v[144:147], v[104:111], v148\n\t"
        "v_exp_f32 v117, v117\n\t"
        "v_mfma_f32_32x32x16_bf16 v[96:111], v[168:171], v[80:83], 0\n\t"
        "v_exp_f32 v118, v118\n\t"
        "v_exp_f32 v119, v119\n\t"
        "v_exp_f32 v120, v120\n\t"
        "v_mfma_f32_32x32x16_bf16 v[96:111], v[172:175], v[84:87], v[96:111]\n\t"
        "v_exp_f32 v121, v121\n\t"
        "v_exp_f32 v122, v122\n\t"
        "v_exp_f32 v123, v123\n\t"
        "v_smfmac_f32_32x32x32_bf16 v[16:31], v[176:179], v[112:119], v148\n\t"
        "v_exp_f32 v124, v124\n\t"
        "v_exp_f32 v125, v125\n\t"
        "v_exp_f32 v126, v126\n\t"
        "v_smfmac_f32_16x16x64_bf16 v[132:135], v[144:147], v[112:119], v148\n\t"
        "v_exp_f32 v127, v127\n\t"
        "s_cmp_lg_u32 s46, 0\n\t"
        "s_cbranch_scc1 33f\n\t"
        "s_waitcnt vmcnt(0)\n\t"
        "v_cmp_ne_u32 vcc, v186, v151\n\t"
        "s_cbranch_vccz 32f\n\t"
        "buffer_load_dword v151, v185, s[40:43], 0 offen sc1\n\t"
        "33:\n\t"
        "v_exp_f32 v96, v96\n\t"
        "v_exp_f32 v97, v97\n\t"
        "v_smfmac_f32_32x32x32_bf16 v[16:31], v[180:183], v[120:127], v148\n\t"
        "v_exp_f32 v98, v98\n\t"
        "v_exp_f32 v99, v99\n\t"
        "v_exp_f32 v100, v100\n\t"
        "v_smfmac_f32_16x16x64_bf16 v[132:135], v[144:147], v[120:127], v148\n\t"
        "v_exp_f32 v101, v101\n\t"
        "v_mfma_f32_32x32x16_bf16 v[112:127], v[168:171], v[88:91], 0\n\t"
        "v_exp_f32 v102, v102\n\t"
        "v_exp_f32 v103, v103\n\t"
        "v_exp_f32 v104, v104\n\t"
        "v_mfma_f32_32x32x16_bf16 v[112:127], v[172:175], v[92:95], v[112:127]\n\t"
        "v_exp_f32 v105, v105\n\t"
        "v_exp_f32 v106, v106\n\t"
        "v_exp_f32 v107, v107\n\t"
        "v_smfmac_f32_32x32x32_bf16 v[32:47], v[176:179], v[96:103], v148\n\t"
        "v_exp_f32 v108, v108\n\t"
        "v_exp_f32 v109, v109\n\t"
        "v_exp_f32 v110, v110\n\t"
        "v_smfmac_f32_16x16x64_bf16 v[136:139], v[144:147], v[96:103], v148\n\t"
        "v_exp_f32 v111, v111\n\t"
        "s_cmp_lg_u32 s46, 0\n\t"
        "s_cbranch_scc1 5f\n\t"
        "s_mov_b32 s45, 0x8000\n\t"
        "2:\n\t"
        "s_sub_u32 s45, s45, 1\n\t"
        "s_waitcnt vmcnt(0)\n\t"
        "s_cbranch_scc1 3f\n\t"
        "v_cmp_ne_u32 vcc, v186, v151\n\t"
        "s_cbranch_vccz 3f\n\t"
        "buffer_load_dword v151, v185, s[40:43], 0 offen sc1\n\t"
        "s_branch 2b\n\t"
        "3:\n\t"
        "ds_write_b32 v187, v188\n\t"
        "s_add_u32 s44, s44, 0x4000\n\t"
        "s_and_b32 s44, s44, 0x3ffff\n\t"
        "buffer_load_dwordx4 v[184:187], v149, s[40:43], s44 offen offset:0 sc1\n\t"
        "buffer_load_dwordx4 v[188:191], v149, s[40:43], s44 offen offset:1024 sc1\n\t"
        "buffer_load_dwordx4 v[192:195], v149, s[40:43], s44 offen offset:2048 sc1\n\t"
        "buffer_load_dwordx4 v[196:199], v149, s[40:43], s44 offen offset:3072 sc1\n\t"
        "buffer_load_dwordx4 v[200:203], v150, s[40:43], s44 offen offset:0 sc1\n\t"
        "buffer_load_dwordx4 v[204:207], v150, s[40:43], s44 offen offset:1024 sc1\n\t"
        "buffer_load_dwordx4 v[208:211], v150, s[40:43], s44 offen offset:2048 sc1\n\t"
        "buffer_load_dwordx4 v[212:215], v150, s[40:43], s44 offen offset:3072 sc1\n\t"
        "s_add_u32 s44, s44, 0x4000\n\t"
        "s_and_b32 s44, s44, 0x3ffff\n\t"
        "buffer_load_dwordx4 v[152:155], v149, s[40:43], s44 offen offset:0 sc1\n\t"
        "buffer_load_dwordx4 v[156:159], v149, s[40:43], s44 offen offset:1024 sc1\n\t"
        "buffer_load_dwordx4 v[160:163], v149, s[40:43], s44 offen offset:2048 sc1\n\t"
        "buffer_load_dwordx4 v[164:167], v149, s[40:43], s44 offen offset:3072 sc1\n\t"
        "5:\n\t"
        "s_mov_b32 s45, 7\n\t"
        "1:\n\t"
        "v_exp_f32 v112, v112\n\t"
        "v_exp_f32 v113, v113\n\t"
        "v_smfmac_f32_32x32x32_bf16 v[32:47], v[180:183], v[104:111], v148\n\t"
        "v_exp_f32 v114, v114\n\t"
        "v_exp_f32 v115, v115\n\t"
        "v_exp_f32 v116, v116\n\t"
        "v_smfmac_f32_16x16x64_bf16 v[136:139], v[144:147], v[104:111], v148\n\t"
        "v_exp_f32 v117, v117\n\t"
        "s_waitcnt vmcnt(8)\n\t"
        "v_mfma_f32_32x32x16_bf16 v[96:111], v[184:187], v[64:67], 0\n\t"
        "v_exp_f32 v118, v118\n\t"
        "v_exp_f32 v119, v119\n\t"
        "v_exp_f32 v120, v120\n\t"
        "v_mfma_f32_32x32x16_bf16 v[96:111], v[188:191], v[68:71], v[96:111]\n\t"
        "v_exp_f32 v121, v121\n\t"
        "v_exp_f32 v122, v122\n\t"
        "v_exp_f32 v123, v123\n\t"
        "v_smfmac_f32_32x32x32_bf16 v[48:63], v[176:179], v[112:119], v148\n\t"
        "v_exp_f32 v124, v124\n\t"
        "v_exp_f32 v125, v125\n\t"
        "v_exp_f32 v126, v126\n\t"
        "v_smfmac_f32_16x16x64_bf16 v[140:143], v[144:147], v[112:119], v148\n\t"
        "buffer_load_dwordx4 v[168:171], v150, s[40:43], s44 offen offset:0 sc1\n\t"
        "v_exp_f32 v127, v127\n\t"
        "v_exp_f32 v96, v96\n\t"
        "v_exp_f32 v97, v97\n\t"
        "v_smfmac_f32_32x32x32_bf16 v[48:63], v[180:183], v[120:127], v148\n\t"
        "v_exp_f32 v98, v98\n\t"
        "v_exp_f32 v99, v99\n\t"
        "v_exp_f32 v100, v100\n\t"
        "v_smfmac_f32_16x16x64_bf16 v[140:143], v[144:147], v[120:127], v148\n\t"
        "v_exp_f32 v101, v101\n\t"
        "v_mfma_f32_32x32x16_bf16 v[112:127], v[184:187], v[72:75], 0\n\t"
        "v_exp_f32 v102, v102\n\t"
        "v_exp_f32 v103, v103\n\t"
        "v_exp_f32 v104, v104\n\t"
        "v_mfma_f32_32x32x16_bf16 v[112:127], v[188:191], v[76:79], v[112:127]\n\t"
        "v_exp_f32 v105, v105\n\t"
        "v_exp_f32 v106, v106\n\t"
        "v_exp_f32 v107, v107\n\t"
        "v_smfmac_f32_32x32x32_bf16 v[0:15], v[192:195], v[96:103], v148\n\t"
        "v_exp_f32 v108, v108\n\t"
        "v_exp_f32 v109, v109\n\t"
        "v_exp_f32 v110, v110\n\t"
        "v_smfmac_f32_16x16x64_bf16 v[128:131], v[144:147], v[96:103], v148\n\t"
        "buffer_load_dwordx4 v[172:175], v150, s[40:43], s44 offen offset:1024 sc1\n\t"
        "v_exp_f32 v111, v111\n\t"
        "v_exp_f32 v112, v112\n\t"
        "v_exp_f32 v113, v113\n\t"
        "v_smfmac_f32_32x32x32_bf16 v[0:15], v[196:199], v[104:111], v148\n\t"
        "v_exp_f32 v114, v114\n\t"
        "v_exp_f32 v115, v115\n\t"
        "v_exp_f32 v116, v116\n\t"
        "v_smfmac_f32_16x16x64_bf16 v[128:131], v[144:147], v[104:111], v148\n\t"
        "v_exp_f32 v117, v117\n\t"
        "v_mfma_f32_32x32x16_bf16 v[96:111], v[184:187], v[80:83], 0\n\t"
        "v_exp_f32 v118, v118\n\t"
        "v_exp_f32 v119, v119\n\t"
        "v_exp_f32 v120, v120\n\t"
        "v_mfma_f32_32x32x16_bf16 v[96:111], v[188:191], v[84:87], v[96:111]\n\t"
        "v_exp_f32 v121, v121\n\t"
        "v_exp_f32 v122, v122\n\t"
        "v_exp_f32 v123, v123\n\t"
        "v_smfmac_f32_32x32x32_bf16 v[16:31], v[192:195], v[112:119], v148\n\t"
        "v_exp_f32 v124, v124\n\t"
        "v_exp_f32 v125, v125\n\t"
        "v_exp_f32 v126, v126\n\t"
        "v_smfmac_f32_16x16x64_bf16 v[132:135], v[144:147], v[112:119], v148\n\t"
        "buffer_load_dwordx4 v[176:179], v150, s[40:43], s44 offen offset:2048 sc1\n\t"
        "v_exp_f32 v127, v127\n\t"
        "v_exp_f32 v96, v96\n\t"
        "v_exp_f32 v97, v97\n\t"
        "v_smfmac_f32_32x32x32_bf16 v[16:31], v[196:199], v[120:127], v148\n\t"
        "v_exp_f32 v98, v98\n\t"
        "v_exp_f32 v99, v99\n\t"
        "v_exp_f32 v100, v100\n\t"
        "v_smfmac_f32_16x16x64_bf16 v[132:135], v[144:147], v[120:127], v148\n\t"
        "v_exp_f32 v101, v101\n\t"
        "v_mfma_f32_32x32x16_bf16 v[112:127], v[184:187], v[88:91], 0\n\t"
        "v_exp_f32 v102, v102\n\t"
        "v_exp_f32 v103, v103\n\t"
        "v_exp_f32 v104, v104\n\t"
        "v_mfma_f32_32x32x16_bf16 v[112:127], v[188:191], v[92:95], v[112:127]\n\t"
        "v_exp_f32 v105, v105\n\t"
        "v_exp_f32 v106, v106\n\t"
        "v_exp_f32 v107, v107\n\t"
        "v_smfmac_f32_32x32x32_bf16 v[32:47], v[192:195], v[96:103], v148\n\t"
        "v_exp_f32 v108, v108\n\t"
        "v_exp_f32 v109, v109\n\t"
        "v_exp_f32 v110, v110\n\t"
        "v_smfmac_f32_16x16x64_bf16 v[136:139], v[144:147], v[96:103], v148\n\t"
        "buffer_load_dwordx4 v[180:183], v150, s[40:43], s44 offen offset:3072 sc1\n\t"
        "v_exp_f32 v111, v111\n\t"
        "v_exp_f32 v112, v112\n\t"
        "v_exp_f32 v113, v113\n\t"
        "v_smfmac_f32_32x32x32_bf16 v[32:47], v[196:199], v[104:111], v148\n\t"
        "s_add_u32 s44, s44, 0x4000\n\t"
        "s_and_b32 s44, s44, 0x3ffff\n\t"
        "v_exp_f32 v114, v114\n\t"
        "v_exp_f32 v115, v115\n\t"
        "v_exp_f32 v116, v116\n\t"
        "v_smfmac_f32_16x16x64_bf16 v[136:139], v[144:147], v[104:111], v148\n\t"
        "v_exp_f32 v117, v117\n\t"
        "s_waitcnt vmcnt(8)\n\t"
        "v_mfma_f32_32x32x16_bf16 v[96:111], v[200:203], v[64:67], 0\n\t"
        "v_exp_f32 v118, v118\n\t"
        "v_exp_f32 v119, v119\n\t"
        "v_exp_f32 v120, v120\n\t"
        "v_mfma_f32_32x32x16_bf16 v[96:111], v[204:207], v[68:71], v[96:111]\n\t"
        "v_exp_f32 v121, v121\n\t"
        "v_exp_f32 v122, v122\n\t"
        "v_exp_f32 v123, v123\n\t"
        "v_smfmac_f32_32x32x32_bf16 v[48:63], v[192:195], v[112:119], v148\n\t"
        "v_exp_f32 v124, v124\n\t"
        "v_exp_f32 v125, v125\n\t"
        "v_exp_f32 v126, v126\n\t"
        "v_smfmac_f32_16x16x64_bf16 v[140:143], v[144:147], v[112:119], v148\n\t"
        "buffer_load_dwordx4 v[184:187], v149, s[40:43], s44 offen offset:0 sc1\n\t"
        "v_exp_f32 v127, v127\n\t"
        "v_exp_f32 v96, v96\n\t"
        "v_exp_f32 v97, v97\n\t"
        "v_smfmac_f32_32x32x32_bf16 v[48:63], v[196:199], v[120:127], v148\n\t"
        "v_exp_f32 v98, v98\n\t"
        "v_exp_f32 v99, v99\n\t"
        "v_exp_f32 v100, v100\n\t"
        "v_smfmac_f32_16x16x64_bf16 v[140:143], v[144:147], v[120:127], v148\n\t"
        "v_exp_f32 v101, v101\n\t"
        "v_mfma_f32_32x32x16_bf16 v[112:127], v[200:203], v[72:75], 0\n\t"
        "v_exp_f32 v102, v102\n\t"
        "v_exp_f32 v103, v103\n\t"
        "v_exp_f32 v104, v104\n\t"
        "v_mfma_f32_32x32x16_bf16 v[112:127], v[204:207], v[76:79], v[112:127]\n\t"
        "v_exp_f32 v105, v105\n\t"
        "v_exp_f32 v106, v106\n\t"
        "v_exp_f32 v107, v107\n\t"
        "v_smfmac_f32_32x32x32_bf16 v[0:15], v[208:211], v[96:103], v148\n\t"
        "v_exp_f32 v108, v108\n\t"
        "v_exp_f32 v109, v109\n\t"
        "v_exp_f32 v110, v110\n\t"
        "v_smfmac_f32_16x16x64_bf16 v[128:131], v[144:147], v[96:103], v148\n\t"
        "buffer_load_dwordx4 v[188:191], v149, s[40:43], s44 offen offset:1024 sc1\n\t"
        "v_exp_f32 v111, v111\n\t"
        "v_exp_f32 v112, v112\n\t"
        "v_exp_f32 v113, v113\n\t"
        "v_smfmac_f32_32x32x32_bf16 v[0:15], v[212:215], v[104:111], v148\n\t"
        "v_exp_f32 v114, v114\n\t"
        "v_exp_f32 v115, v115\n\t"
        "v_exp_f32 v116, v116\n\t"
        "v_smfmac_f32_16x16x64_bf16 v[128:131], v[144:147], v[104:111], v148\n\t"
        "v_exp_f32 v117, v117\n\t"
        "v_mfma_f32_32x32x16_bf16 v[96:111], v[200:203], v[80:83], 0\n\t"
        "v_exp_f32 v118, v118\n\t"
        "v_exp_f32 v119, v119\n\t"
        "v_exp_f32 v120, v120\n\t"
        "v_mfma_f32_32x32x16_bf16 v[96:111], v[204:207], v[84:87], v[96:111]\n\t"
        "v_exp_f32 v121, v121\n\t"
        "v_exp_f32 v122, v122\n\t"
        "v_exp_f32 v123, v123\n\t"
        "v_smfmac_f32_32x32x32_bf16 v[16:31], v[208:211], v[112:119], v148\n\t"
        "v_exp_f32 v124, v124\n\t"
        "v_exp_f32 v125, v125\n\t"
        "v_exp_f32 v126, v126\n\t"
        "v_smfmac_f32_16x16x64_bf16 v[132:135], v[144:147], v[112:119], v148\n\t"
        "buffer_load_dwordx4 v[192:195], v149, s[40:43], s44 offen offset:2048 sc1\n\t"
        "v_exp_f32 v127, v127\n\t"
        "v_exp_f32 v96, v96\n\t"
        "v_exp_f32 v97, v97\n\t"
        "v_smfmac_f32_32x32x32_bf16 v[16:31], v[212:215], v[120:127], v148\n\t"
        "v_exp_f32 v98, v98\n\t"
        "v_exp_f32 v99, v99\n\t"
        "v_exp_f32 v100, v100\n\t"
        "v_smfmac_f32_16x16x64_bf16 v[132:135], v[144:147], v[120:127], v148\n\t"
        "v_exp_f32 v101, v101\n\t"
        "v_mfma_f32_32x32x16_bf16 v[112:127], v[200:203], v[88:91], 0\n\t"
        "v_exp_f32 v102, v102\n\t"
        "v_exp_f32 v103, v103\n\t"
        "v_exp_f32 v104, v104\n\t"
        "v_mfma_f32_32x32x16_bf16 v[112:127], v[204:207], v[92:95], v[112:127]\n\t"
        "v_exp_f32 v105, v105\n\t"
        "v_exp_f32 v106, v106\n\t"
        "v_exp_f32 v107, v107\n\t"
        "v_smfmac_f32_32x32x32_bf16 v[32:47], v[208:211], v[96:103], v148\n\t"
        "v_exp_f32 v108, v108\n\t"
        "v_exp_f32 v109, v109\n\t"
        "v_exp_f32 v110, v110\n\t"
        "v_smfmac_f32_16x16x64_bf16 v[136:139], v[144:147], v[96:103], v148\n\t"
        "buffer_load_dwordx4 v[196:199], v149, s[40:43], s44 offen offset:3072 sc1\n\t"
        "v_exp_f32 v111, v111\n\t"
        "v_exp_f32 v112, v112\n\t"
        "v_exp_f32 v113, v113\n\t"
        "v_smfmac_f32_32x32x32_bf16 v[32:47], v[212:215], v[104:111], v148\n\t"
        "v_exp_f32 v114, v114\n\t"
        "v_exp_f32 v115, v115\n\t"
        "v_exp_f32 v116, v116\n\t"
        "v_smfmac_f32_16x16x64_bf16 v[136:139], v[144:147], v[104:111], v148\n\t"
        "v_exp_f32 v117, v117\n\t"
        "s_waitcnt vmcnt(8)\n\t"
        "v_mfma_f32_32x32x16_bf16 v[96:111], v[152:155], v[64:67], 0\n\t"
        "v_exp_f32 v118, v118\n\t"
        "v_exp_f32 v119, v119\n\t"
        "v_exp_f32 v120, v120\n\t"
        "v_mfma_f32_32x32x16_bf16 v[96:111], v[156:159], v[68:71], v[96:111]\n\t"
        "v_exp_f32 v121, v121\n\t"
        "v_exp_f32 v122, v122\n\t"
        "v_exp_f32 v123, v123\n\t"
        "v_smfmac_f32_32x32x32_bf16 v[48:63], v[208:211], v[112:119], v148\n\t"
        "v_exp_f32 v124, v124\n\t"
        "v_exp_f32 v125, v125\n\t"
        "v_exp_f32 v126, v126\n\t"
        "v_smfmac_f32_16x16x64_bf16 v[140:143], v[144:147], v[112:119], v148\n\t"
        "buffer_load_dwordx4 v[200:203], v150, s[40:43], s44 offen offset:0 sc1\n\t"
        "v_exp_f32 v127, v127\n\t"
        "v_exp_f32 v96, v96\n\t"
        "v_exp_f32 v97, v97\n\t"
        "v_smfmac_f32_32x32x32_bf16 v[48:63], v[212:215], v[120:127], v148\n\t"
        "v_exp_f32 v98, v98\n\t"
        "v_exp_f32 v99, v99\n\t"
        "v_exp_f32 v100, v100\n\t"
        "v_smfmac_f32_16x16x64_bf16 v[140:143], v[144:147], v[120:127], v148\n\t"
        "v_exp_f32 v101, v101\n\t"
        "v_mfma_f32_32x32x16_bf16 v[112:127], v[152:155], v[72:75], 0\n\t"
        "v_exp_f32 v102, v102\n\t"
        "v_exp_f32 v103, v103\n\t"
        "v_exp_f32 v104, v104\n\t"
        "v_mfma_f32_32x32x16_bf16 v[112:127], v[156:159], v[76:79], v[112:127]\n\t"
        "v_exp_f32 v105, v105\n\t"
        "v_exp_f32 v106, v106\n\t"
        "v_exp_f32 v107, v107\n\t"
        "v_smfmac_f32_32x32x32_bf16 v[0:15], v[160:163], v[96:103], v148\n\t"
        "v_exp_f32 v108, v108\n\t"
        "v_exp_f32 v109, v109\n\t"
        "v_exp_f32 v110, v110\n\t"
        "v_smfmac_f32_16x16x64_bf16 v[128:131], v[144:147], v[96:103], v148\n\t"
        "buffer_load_dwordx4 v[204:207], v150, s[40:43], s44 offen offset:1024 sc1\n\t"
        "v_exp_f32 v111, v111\n\t"
        "v_exp_f32 v112, v112\n\t"
        "v_exp_f32 v113, v113\n\t"
        "v_smfmac_f32_32x32x32_bf16 v[0:15], v[164:167], v[104:111], v148\n\t"
        "v_exp_f32 v114, v114\n\t"
        "v_exp_f32 v115, v115\n\t"
        "v_exp_f32 v116, v116\n\t"
        "v_smfmac_f32_16x16x64_bf16 v[128:131], v[144:147], v[104:111], v148\n\t"
        "v_exp_f32 v117, v117\n\t"
        "v_mfma_f32_32x32x16_bf16 v[96:111], v[152:155], v[80:83], 0\n\t"
        "v_exp_f32 v118, v118\n\t"
        "v_exp_f32 v119, v119\n\t"
        "v_exp_f32 v120, v120\n\t"
        "v_mfma_f32_32x32x16_bf16 v[96:111], v[156:159], v[84:87], v[96:111]\n\t"
        "v_exp_f32 v121, v121\n\t"
        "v_exp_f32 v122, v122\n\t"
        "v_exp_f32 v123, v123\n\t"
        "v_smfmac_f32_32x32x32_bf16 v[16:31], v[160:163], v[112:119], v148\n\t"
        "v_exp_f32 v124, v124\n\t"
        "v_exp_f32 v125, v125\n\t"
        "v_exp_f32 v126, v126\n\t"
        "v_smfmac_f32_16x16x64_bf16 v[132:135], v[144:147], v[112:119], v148\n\t"
        "buffer_load_dwordx4 v[208:211], v150, s[40:43], s44 offen offset:2048 sc1\n\t"
        "v_exp_f32 v127, v127\n\t"
        "v_exp_f32 v96, v96\n\t"
        "v_exp_f32 v97, v97\n\t"
        "v_smfmac_f32_32x32x32_bf16 v[16:31], v[164:167], v[120:127], v148\n\t"
        "v_exp_f32 v98, v98\n\t"
        "v_exp_f32 v99, v99\n\t"
        "v_exp_f32 v100, v100\n\t"
        "v_smfmac_f32_16x16x64_bf16 v[132:135], v[144:147], v[120:127], v148\n\t"
        "v_exp_f32 v101, v101\n\t"
        "v_mfma_f32_32x32x16_bf16 v[112:127], v[152:155], v[88:91], 0\n\t"
        "v_exp_f32 v102, v102\n\t"
        "v_exp_f32 v103, v103\n\t"
        "v_exp_f32 v104, v104\n\t"
        "v_mfma_f32_32x32x16_bf16 v[112:127], v[156:159], v[92:95], v[112:127]\n\t"
        "v_exp_f32 v105, v105\n\t"
        "v_exp_f32 v106, v106\n\t"
        "v_exp_f32 v107, v107\n\t"
        "v_smfmac_f32_32x32x32_bf16 v[32:47], v[160:163], v[96:103], v148\n\t"
        "v_exp_f32 v108, v108\n\t"
        "v_exp_f32 v109, v109\n\t"
        "v_exp_f32 v110, v110\n\t"
        "v_smfmac_f32_16x16x64_bf16 v[136:139], v[144:147], v[96:103], v148\n\t"
        "buffer_load_dwordx4 v[212:215], v150, s[40:43], s44 offen offset:3072 sc1\n\t"
        "v_exp_f32 v111, v111\n\t"
        "v_exp_f32 v112, v112\n\t"
        "v_exp_f32 v113, v113\n\t"
        "v_smfmac_f32_32x32x32_bf16 v[32:47], v[164:167], v[104:111], v148\n\t"
        "s_add_u32 s44, s44, 0x4000\n\t"
        "s_and_b32 s44, s44, 0x3ffff\n\t"
        "v_exp_f32 v114, v114\n\t"
        "v_exp_f32 v115, v115\n\t"
        "v_exp_f32 v116, v116\n\t"
        "v_smfmac_f32_16x16x64_bf16 v[136:139], v[144:147], v[104:111], v148\n\t"
        "v_exp_f32 v117, v117\n\t"
        "s_waitcnt vmcnt(8)\n\t"
        "v_mfma_f32_32x32x16_bf16 v[96:111], v[168:171], v[64:67], 0\n\t"
        "v_exp_f32 v118, v118\n\t"
        "v_exp_f32 v119, v119\n\t"
        "v_exp_f32 v120, v120\n\t"
        "v_mfma_f32_32x32x16_bf16 v[96:111], v[172:175], v[68:71], v[96:111]\n\t"
        "v_exp_f32 v121, v121\n\t"
        "v_exp_f32 v122, v122\n\t"
        "v_exp_f32 v123, v123\n\t"
        "v_smfmac_f32_32x32x32_bf16 v[48:63], v[160:163], v[112:119], v148\n\t"
        "v_exp_f32 v124, v124\n\t"
        "v_exp_f32 v125, v125\n\t"
        "v_exp_f32 v126, v126\n\t"
        "v_smfmac_f32_16x16x64_bf16 v[140:143], v[144:147], v[112:119], v148\n\t"
        "buffer_load_dwordx4 v[152:155], v149, s[40:43], s44 offen offset:0 sc1\n\t"
        "v_exp_f32 v127, v127\n\t"
        "v_exp_f32 v96, v96\n\t"
        "v_exp_f32 v97, v97\n\t"
        "v_smfmac_f32_32x32x32_bf16 v[48:63], v[164:167], v[120:127], v148\n\t"
        "v_exp_f32 v98, v98\n\t"
        "v_exp_f32 v99, v99\n\t"
        "v_exp_f32 v100, v100\n\t"
        "v_smfmac_f32_16x16x64_bf16 v[140:143], v[144:147], v[120:127], v148\n\t"
        "v_exp_f32 v101, v101\n\t"
        "v_mfma_f32_32x32x16_bf16 v[112:127], v[168:171], v[72:75], 0\n\t"
        "v_exp_f32 v102, v102\n\t"
        "v_exp_f32 v103, v103\n\t"
        "v_exp_f32 v104, v104\n\t"
        "v_mfma_f32_32x32x16_bf16 v[112:127], v[172:175], v[76:79], v[112:127]\n\t"
        "v_exp_f32 v105, v105\n\t"
        "v_exp_f32 v106, v106\n\t"
        "v_exp_f32 v107, v107\n\t"
        "v_smfmac_f32_32x32x32_bf16 v[0:15], v[176:179], v[96:103], v148\n\t"
        "v_exp_f32 v108, v108\n\t"
        "v_exp_f32 v109, v109\n\t"
        "v_exp_f32 v110, v110\n\t"
        "v_smfmac_f32_16x16x64_bf16 v[128:131], v[144:147], v[96:103], v148\n\t"
        "buffer_load_dwordx4 v[156:159], v149, s[40:43], s44 offen offset:1024 sc1\n\t"
        "v_exp_f32 v111, v111\n\t"
        "v_exp_f32 v112, v112\n\t"
        "v_exp_f32 v113, v113\n\t"
        "v_smfmac_f32_32x32x32_bf16 v[0:15], v[180:183], v[104:111], v148\n\t"
        "v_exp_f32 v114, v114\n\t"
        "v_exp_f32 v115, v115\n\t"
        "v_exp_f32 v116, v116\n\t"
        "v_smfmac_f32_16x16x64_bf16 v[128:131], v[144:147], v[104:111], v148\n\t"
        "v_exp_f32 v117, v117\n\t"
        "v_mfma_f32_32x32x16_bf16 v[96:111], v[168:171], v[80:83], 0\n\t"
        "v_exp_f32 v118, v118\n\t"
        "v_exp_f32 v119, v119\n\t"
        "v_exp_f32 v120, v120\n\t"
        "v_mfma_f32_32x32x16_bf16 v[96:111], v[172:175], v[84:87], v[96:111]\n\t"
        "v_exp_f32 v121, v121\n\t"
        "v_exp_f32 v122, v122\n\t"
        "v_exp_f32 v123, v123\n\t"
        "v_smfmac_f32_32x32x32_bf16 v[16:31], v[176:179], v[112:119], v148\n\t"
        "v_exp_f32 v124, v124\n\t"
        "v_exp_f32 v125, v125\n\t"
        "v_exp_f32 v126, v126\n\t"
        "v_smfmac_f32_16x16x64_bf16 v[132:135], v[144:147], v[112:119], v148\n\t"
        "buffer_load_dwordx4 v[160:163], v149, s[40:43], s44 offen offset:2048 sc1\n\t"
        "v_exp_f32 v127, v127\n\t"
        "v_exp_f32 v96, v96\n\t"
        "v_exp_f32 v97, v97\n\t"
        "v_smfmac_f32_32x32x32_bf16 v[16:31], v[180:183], v[120:127], v148\n\t"
        "v_exp_f32 v98, v98\n\t"
        "v_exp_f32 v99, v99\n\t"
        "v_exp_f32 v100, v100\n\t"
        "v_smfmac_f32_16x16x64_bf16 v[132:135], v[144:147], v[120:127], v148\n\t"
        "v_exp_f32 v101, v101\n\t"
        "v_mfma_f32_32x32x16_bf16 v[112:127], v[168:171], v[88:91], 0\n\t"
        "v_exp_f32 v102, v102\n\t"
        "v_exp_f32 v103, v103\n\t"
        "v_exp_f32 v104, v104\n\t"
        "v_mfma_f32_32x32x16_bf16 v[112:127], v[172:175], v[92:95], v[112:127]\n\t"
        "v_exp_f32 v105, v105\n\t"
        "v_exp_f32 v106, v106\n\t"
        "v_exp_f32 v107, v107\n\t"
        "v_smfmac_f32_32x32x32_bf16 v[32:47], v[176:179], v[96:103], v148\n\t"
        "v_exp_f32 v108, v108\n\t"
        "v_exp_f32 v109, v109\n\t"
        "v_exp_f32 v110, v110\n\t"
        "v_smfmac_f32_16x16x64_bf16 v[136:139], v[144:147], v[96:103], v148\n\t"
        "buffer_load_dwordx4 v[164:167], v149, s[40:43], s44 offen offset:3072 sc1\n\t"
        "v_exp_f32 v111, v111\n\t"
        "s_sub_u32 s45, s45, 1\n\t"
        "s_cmp_lg_u32 s45, 0\n\t"
        "s_cbranch_scc1 1b\n\t"
        "v_exp_f32 v112, v112\n\t"
        "v_exp_f32 v113, v113\n\t"
        "v_smfmac_f32_32x32x32_bf16 v[32:47], v[180:183], v[104:111], v148\n\t"
        "v_exp_f32 v114, v114\n\t"
        "v_exp_f32 v115, v115\n\t"
        "v_exp_f32 v116, v116\n\t"
        "v_smfmac_f32_16x16x64_bf16 v[136:139], v[144:147], v[104:111], v148\n\t"
        "v_exp_f32 v117, v117\n\t"
        "s_waitcnt vmcnt(8)\n\t"
        "v_mfma_f32_32x32x16_bf16 v[96:111], v[184:187], v[64:67], 0\n\t"
        "v_exp_f32 v118, v118\n\t"
        "v_exp_f32 v119, v119\n\t"
        "v_exp_f32 v120, v120\n\t"
        "v_mfma_f32_32x32x16_bf16 v[96:111], v[188:191], v[68:71], v[96:111]\n\t"
        "v_exp_f32 v121, v121\n\t"
        "v_exp_f32 v122, v122\n\t"
        "v_exp_f32 v123, v123\n\t"
        "v_smfmac_f32_32x32x32_bf16 v[48:63], v[176:179], v[112:119], v148\n\t"
        "v_exp_f32 v124, v124\n\t"
        "v_exp_f32 v125, v125\n\t"
        "v_exp_f32 v126, v126\n\t"
        "v_smfmac_f32_16x16x64_bf16 v[140:143], v[144:147], v[112:119], v148\n\t"
        "v_exp_f32 v127, v127\n\t"
        "v_exp_f32 v96, v96\n\t"
        "v_exp_f32 v97, v97\n\t"
        "v_smfmac_f32_32x32x32_bf16 v[48:63], v[180:183], v[120:127], v148\n\t"
        "v_exp_f32 v98, v98\n\t"
        "v_exp_f32 v99, v99\n\t"
        "v_exp_f32 v100, v100\n\t"
        "v_smfmac_f32_16x16x64_bf16 v[140:143], v[144:147], v[120:127], v148\n\t"
        "v_exp_f32 v101, v101\n\t"
        "v_mfma_f32_32x32x16_bf16 v[112:127], v[184:187], v[72:75], 0\n\t"
        "v_exp_f32 v102, v102\n\t"
        "v_exp_f32 v103, v103\n\t"
        "v_exp_f32 v104, v104\n\t"
        "v_mfma_f32_32x32x16_bf16 v[112:127], v[188:191], v[76:79], v[112:127]\n\t"
        "v_exp_f32 v105, v105\n\t"
        "v_exp_f32 v106, v106\n\t"
        "v_exp_f32 v107, v107\n\t"
        "v_smfmac_f32_32x32x32_bf16 v[0:15], v[192:195], v[96:103], v148\n\t"
        "v_exp_f32 v108, v108\n\t"
        "v_exp_f32 v109, v109\n\t"
        "v_exp_f32 v110, v110\n\t"
        "v_smfmac_f32_16x16x64_bf16 v[128:131], v[144:147], v[96:103], v148\n\t"
        "v_exp_f32 v111, v111\n\t"
        "v_exp_f32 v112, v112\n\t"
        "v_exp_f32 v113, v113\n\t"
        "v_smfmac_f32_32x32x32_bf16 v[0:15], v[196:199], v[104:111], v148\n\t"
        "v_exp_f32 v114, v114\n\t"
        "v_exp_f32 v115, v115\n\t"
        "v_exp_f32 v116, v116\n\t"
        "v_smfmac_f32_16x16x64_bf16 v[128:131], v[144:147], v[104:111], v148\n\t"
        "v_exp_f32 v117, v117\n\t"
        "s_waitcnt vmcnt(4)\n\t"
        "v_mfma_f32_32x32x16_bf16 v[96:111], v[200:203], v[64:67], 0\n\t"
        "v_exp_f32 v118, v118\n\t"
        "v_exp_f32 v119, v119\n\t"
        "v_exp_f32 v120, v120\n\t"
        "v_mfma_f32_32x32x16_bf16 v[96:111], v[204:207], v[68:71], v[96:111]\n\t"
        "v_exp_f32 v121, v121\n\t"
        "v_exp_f32 v122, v122\n\t"
        "v_exp_f32 v123, v123\n\t"
        "v_smfmac_f32_32x32x32_bf16 v[16:31], v[192:195], v[112:119], v148\n\t"
        "v_exp_f32 v124, v124\n\t"
        "v_exp_f32 v125, v125\n\t"
        "v_exp_f32 v126, v126\n\t"
        "v_smfmac_f32_16x16x64_bf16 v[132:135], v[144:147], v[112:119], v148\n\t"
        "v_exp_f32 v127, v127\n\t"
        "s_cmp_lg_u32 s47, 0\n\t"
        "s_cbranch_scc1 4f\n\t"
        "global_load_dwordx4 %[r0], v223, %[xb] offset:0\n\t"
        "global_load_dwordx4 %[r1], v223, %[xb] offset:32\n\t"
        "global_load_dwordx4 %[r2], v223, %[xb] offset:64\n\t"
        "global_load_dwordx4 %[r3], v223, %[xb] offset:96\n\t"
        "4:\n\t"
        "v_exp_f32 v96, v96\n\t"
        "v_exp_f32 v97, v97\n\t"
        "v_smfmac_f32_32x32x32_bf16 v[16:31], v[196:199], v[120:127], v148\n\t"
        "v_exp_f32 v98, v98\n\t"
        "v_exp_f32 v99, v99\n\t"
        "v_exp_f32 v100, v100\n\t"
        "v_smfmac_f32_16x16x64_bf16 v[132:135], v[144:147], v[120:127], v148\n\t"
        "v_exp_f32 v101, v101\n\t"
        "v_mfma_f32_32x32x16_bf16 v[112:127], v[200:203], v[72:75], 0\n\t"
        "v_exp_f32 v102, v102\n\t"
        "v_exp_f32 v103, v103\n\t"
        "v_exp_f32 v104, v104\n\t"
        "v_mfma_f32_32x32x16_bf16 v[112:127], v[204:207], v[76:79], v[112:127]\n\t"
        "v_exp_f32 v105, v105\n\t"
        "v_exp_f32 v106, v106\n\t"
        "v_exp_f32 v107, v107\n\t"
        "v_smfmac_f32_32x32x32_bf16 v[0:15], v[208:211], v[96:103], v148\n\t"
        "v_exp_f32 v108, v108\n\t"
        "v_exp_f32 v109, v109\n\t"
        "v_exp_f32 v110, v110\n\t"
        "v_smfmac_f32_16x16x64_bf16 v[128:131], v[144:147], v[96:103], v148\n\t"
        "v_exp_f32 v111, v111\n\t"
        "v_exp_f32 v112, v112\n\t"
        "v_exp_f32 v113, v113\n\t"
        "v_smfmac_f32_32x32x32_bf16 v[0:15], v[212:215], v[104:111], v148\n\t"
        "v_exp_f32 v114, v114\n\t"
        "v_exp_f32 v115, v115\n\t"
        "v_exp_f32 v116, v116\n\t"
        "v_smfmac_f32_16x16x64_bf16 v[128:131], v[144:147], v[104:111], v148\n\t"
        "v_exp_f32 v117, v117\n\t"
        "v_mfma_f32_32x32x16_bf16 v[96:111], v[184:187], v[80:83], 0\n\t"
        "v_exp_f32 v118, v118\n\t"
        "v_exp_f32 v119, v119\n\t"
        "v_exp_f32 v120, v120\n\t"
        "ds_write_b128 v216, v[0:3] offset:0\n\t"
        "v_mfma_f32_32x32x16_bf16 v[96:111], v[188:191], v[84:87], v[96:111]\n\t"
        "v_exp_f32 v121, v121\n\t"
        "ds_write_b128 v216, v[4:7] offset:1024\n\t"
        "v_exp_f32 v122, v122\n\t"
        "ds_write_b128 v216, v[8:11] offset:2048\n\t"
        "v_exp_f32 v123, v123\n\t"
        "ds_write_b128 v216, v[12:15] offset:3072\n\t"
        "v_smfmac_f32_32x32x32_bf16 v[16:31], v[208:211], v[112:119], v148\n\t"
        "v_exp_f32 v124, v124\n\t"
        "ds_write_b32 v218, v128 offset:0\n\t"
        "v_exp_f32 v125, v125\n\t"
        "v_exp_f32 v126, v126\n\t"
        "v_smfmac_f32_16x16x64_bf16 v[132:135], v[144:147], v[112:119], v148\n\t"
        "v_exp_f32 v127, v127\n\t"
        "v_exp_f32 v96, v96\n\t"
        "v_exp_f32 v97, v97\n\t"
        "v_smfmac_f32_32x32x32_bf16 v[16:31], v[212:215], v[120:127], v148\n\t"
        "v_exp_f32 v98, v98\n\t"
        "v_exp_f32 v99, v99\n\t"
        "v_exp_f32 v100, v100\n\t"
        "v_smfmac_f32_16x16x64_bf16 v[132:135], v[144:147], v[120:127], v148\n\t"
        "v_exp_f32 v101, v101\n\t"
        "v_mfma_f32_32x32x16_bf16 v[112:127], v[184:187], v[88:91], 0\n\t"
        "v_exp_f32 v102, v102\n\t"
        "v_exp_f32 v103, v103\n\t"
        "v_exp_f32 v104, v104\n\t"
        "ds_write_b128 v216, v[16:19] offset:4096\n\t"
        "v_mfma_f32_32x32x16_bf16 v[112:127], v[188:191], v[92:95], v[112:127]\n\t"
        "v_exp_f32 v105, v105\n\t"
        "ds_write_b128 v216, v[20:23] offset:5120\n\t"
        "v_exp_f32 v106, v106\n\t"
        "ds_write_b128 v216, v[24:27] offset:6144\n\t"
        "v_exp_f32 v107, v107\n\t"
        "ds_write_b128 v216, v[28:31] offset:7168\n\t"
        "v_smfmac_f32_32x32x32_bf16 v[32:47], v[192:195], v[96:103], v148\n\t"
        "v_exp_f32 v108, v108\n\t"
        "ds_write_b32 v218, v132 offset:256\n\t"
        "v_exp_f32 v109, v109\n\t"
        "ds_write_b32 v220, v222\n\t"
        "v_exp_f32 v110, v110\n\t"
        "v_smfmac_f32_16x16x64_bf16 v[136:139], v[144:147], v[96:103], v148\n\t"
        "v_exp_f32 v111, v111\n\t"
        "v_exp_f32 v112, v112\n\t"
        "v_exp_f32 v113, v113\n\t"
        "v_smfmac_f32_32x32x32_bf16 v[32:47], v[196:199], v[104:111], v148\n\t"
        "v_exp_f32 v114, v114\n\t"
        "v_exp_f32 v115, v115\n\t"
        "v_exp_f32 v116, v116\n\t"
        "v_smfmac_f32_16x16x64_bf16 v[136:139], v[144:147], v[104:111], v148\n\t"
        "v_exp_f32 v117, v117\n\t"
        "v_mfma_f32_32x32x16_bf16 v[96:111], v[200:203], v[80:83], 0\n\t"
        "v_exp_f32 v118, v118\n\t"
        "v_exp_f32 v119, v119\n\t"
        "v_exp_f32 v120, v120\n\t"
        "v_mfma_f32_32x32x16_bf16 v[96:111], v[204:207], v[84:87], v[96:111]\n\t"
        "v_exp_f32 v121, v121\n\t"
        "v_exp_f32 v122, v122\n\t"
        "v_exp_f32 v123, v123\n\t"
        "v_smfmac_f32_32x32x32_bf16 v[48:63], v[192:195], v[112:119], v148\n\t"
        "v_exp_f32 v124, v124\n\t"
        "v_exp_f32 v125, v125\n\t"
        "v_exp_f32 v126, v126\n\t"
        "v_smfmac_f32_16x16x64_bf16 v[140:143], v[144:147], v[112:119], v148\n\t"
        "v_exp_f32 v127, v127\n\t"
        "v_exp_f32 v96, v96\n\t"
        "v_exp_f32 v97, v97\n\t"
        "v_smfmac_f32_32x32x32_bf16 v[48:63], v[196:199], v[120:127], v148\n\t"
        "v_exp_f32 v98, v98\n\t"
        "v_exp_f32 v99, v99\n\t"
        "v_exp_f32 v100, v100\n\t"
        "v_smfmac_f32_16x16x64_bf16 v[140:143], v[144:147], v[120:127], v148\n\t"
        "v_exp_f32 v101, v101\n\t"
        "v_mfma_f32_32x32x16_bf16 v[112:127], v[200:203], v[88:91], 0\n\t"
        "v_exp_f32 v102, v102\n\t"
        "v_exp_f32 v103, v103\n\t"
        "v_exp_f32 v104, v104\n\t"
        "ds_read_b32 v151, v221\n\t"
        "v_mfma_f32_32x32x16_bf16 v[112:127], v[204:207], v[92:95], v[112:127]\n\t"
        "v_exp_f32 v105, v105\n\t"
        "v_exp_f32 v106, v106\n\t"
        "v_exp_f32 v107, v107\n\t"
        "v_smfmac_f32_32x32x32_bf16 v[32:47], v[208:211], v[96:103], v148\n\t"
        "v_exp_f32 v108, v108\n\t"
        "v_exp_f32 v109, v109\n\t"
        "v_exp_f32 v110, v110\n\t"
        "v_smfmac_f32_16x16x64_bf16 v[136:139], v[144:147], v[96:103], v148\n\t"
        "v_exp_f32 v111, v111\n\t"
        "s_waitcnt lgkmcnt(0)\n\t"
        "v_cmp_ne_u32 vcc, v222, v151\n\t"
        "s_cbranch_vccnz 40f\n\t"
        "41:\n\t"
        "v_exp_f32 v112, v112\n\t"
        "ds_read_b128 v[0:3], v217 offset:0\n\t"
        "v_exp_f32 v113, v113\n\t"
        "ds_read_b128 v[4:7], v217 offset:1024\n\t"
        "v_smfmac_f32_32x32x32_bf16 v[32:47], v[212:215], v[104:111], v148\n\t"
        "v_exp_f32 v114, v114\n\t"
        "ds_read_b128 v[8:11], v217 offset:2048\n\t"
        "v_exp_f32 v115, v115\n\t"
        "ds_read_b128 v[12:15], v217 offset:3072\n\t"
        "v_exp_f32 v116, v116\n\t"
        "ds_read_b32 v128, v219 offset:0\n\t"
        "v_smfmac_f32_16x16x64_bf16 v[136:139], v[144:147], v[104:111], v148\n\t"
        "v_exp_f32 v117, v117\n\t"
        "ds_read_b128 v[16:19], v217 offset:4096\n\t"
        "v_exp_f32 v118, v118\n\t"
        "ds_read_b128 v[20:23], v217 offset:5120\n\t"
        "v_exp_f32 v119, v119\n\t"
        "ds_read_b128 v[24:27], v217 offset:6144\n\t"
        "v_exp_f32 v120, v120\n\t"
        "ds_read_b128 v[28:31], v217 offset:7168\n\t"
        "v_exp_f32 v121, v121\n\t"
        "ds_read_b32 v132, v219 offset:256\n\t"
        "v_exp_f32 v122, v122\n\t"
        "v_exp_f32 v123, v123\n\t"
        "v_smfmac_f32_32x32x32_bf16 v[48:63], v[208:211], v[112:119], v148\n\t"
        "v_exp_f32 v124, v124\n\t"
        "v_exp_f32 v125, v125\n\t"
        "v_exp_f32 v126, v126\n\t"
        "v_smfmac_f32_16x16x64_bf16 v[140:143], v[144:147], v[112:119], v148\n\t"
        "v_exp_f32 v127, v127\n\t"
        "s_nop 1\n\t"
        "v_smfmac_f32_32x32x32_bf16 v[48:63], v[212:215], v[120:127], v148\n\t"
        "v_smfmac_f32_16x16x64_bf16 v[140:143], v[144:147], v[120:127], v148\n\t"
        "s_waitcnt vmcnt(0) lgkmcnt(0)\n\t"
        "s_nop 6\n\t"
        "s_branch 9f\n\t"
        "30:\n\t"
        "ds_write_b32 v187, v188\n\t"
        "s_add_u32 s44, s44, 0x4000\n\t"
        "s_and_b32 s44, s44, 0x3ffff\n\t"
        "buffer_load_dwordx4 v[184:187], v149, s[40:43], s44 offen offset:0 sc1\n\t"
        "buffer_load_dwordx4 v[188:191], v149, s[40:43], s44 offen offset:1024 sc1\n\t"
        "buffer_load_dwordx4 v[192:195], v149, s[40:43], s44 offen offset:2048 sc1\n\t"
        "buffer_load_dwordx4 v[196:199], v149, s[40:43], s44 offen offset:3072 sc1\n\t"
        "buffer_load_dwordx4 v[200:203], v150, s[40:43], s44 offen offset:0 sc1\n\t"
        "buffer_load_dwordx4 v[204:207], v150, s[40:43], s44 offen offset:1024 sc1\n\t"
        "buffer_load_dwordx4 v[208:211], v150, s[40:43], s44 offen offset:2048 sc1\n\t"
        "buffer_load_dwordx4 v[212:215], v150, s[40:43], s44 offen offset:3072 sc1\n\t"
        "s_add_u32 s44, s44, 0x4000\n\t"
        "s_and_b32 s44, s44, 0x3ffff\n\t"
        "buffer_load_dwordx4 v[152:155], v149, s[40:43], s44 offen offset:0 sc1\n\t"
        "buffer_load_dwordx4 v[156:159], v149, s[40:43], s44 offen offset:1024 sc1\n\t"
        "buffer_load_dwordx4 v[160:163], v149, s[40:43], s44 offen offset:2048 sc1\n\t"
        "buffer_load_dwordx4 v[164:167], v149, s[40:43], s44 offen offset:3072 sc1\n\t"
        "s_mov_b32 s46, 1\n\t"
        "s_branch 31b\n\t"
        "32:\n\t"
        "ds_write_b32 v187, v188\n\t"
        "s_add_u32 s44, s44, 0x4000\n\t"
        "s_and_b32 s44, s44, 0x3ffff\n\t"
        "buffer_load_dwordx4 v[184:187], v149, s[40:43], s44 offen offset:0 sc1\n\t"
        "buffer_load_dwordx4 v[188:191], v149, s[40:43], s44 offen offset:1024 sc1\n\t"
        "buffer_load_dwordx4 v[192:195], v149, s[40:43], s44 offen offset:2048 sc1\n\t"
        "buffer_load_dwordx4 v[196:199], v149, s[40:43], s44 offen offset:3072 sc1\n\t"
        "buffer_load_dwordx4 v[200:203], v150, s[40:43], s44 offen offset:0 sc1\n\t"
        "buffer_load_dwordx4 v[204:207], v150, s[40:43], s44 offen offset:1024 sc1\n\t"
        "buffer_load_dwordx4 v[208:211], v150, s[40:43], s44 offen offset:2048 sc1\n\t"
        "buffer_load_dwordx4 v[212:215], v150, s[40:43], s44 offen offset:3072 sc1\n\t"
        "s_add_u32 s44, s44, 0x4000\n\t"
        "s_and_b32 s44, s44, 0x3ffff\n\t"
        "buffer_load_dwordx4 v[152:155], v149, s[40:43], s44 offen offset:0 sc1\n\t"
        "buffer_load_dwordx4 v[156:159], v149, s[40:43], s44 offen offset:1024 sc1\n\t"
        "buffer_load_dwordx4 v[160:163], v149, s[40:43], s44 offen offset:2048 sc1\n\t"
        "buffer_load_dwordx4 v[164:167], v149, s[40:43], s44 offen offset:3072 sc1\n\t"
        "s_mov_b32 s46, 1\n\t"
        "s_branch 33b\n\t"
        "40:\n\t"
        "s_mov_b32 s45, 0x100000\n\t"
        "42:\n\t"
        "ds_read_b32 v151, v221\n\t"
        "s_sub_u32 s45, s45, 1\n\t"
        "s_waitcnt lgkmcnt(0)\n\t"
        "s_cbranch_scc1 41b\n\t"
        "v_cmp_ne_u32 vcc, v222, v151\n\t"
        "s_cbranch_vccnz 42b\n\t"
        "s_branch 41b\n\t"
        "9:\n\t"
        : "={v[0:15]}"(o0), "={v[16:31]}"(o1), "={v[32:47]}"(o2), "={v[48:63]}"(o3), "={v[128:143]}"(lv), "+{s44}"(soff), "+{v[152:167]}"(kva), "+{v[168:183]}"(kvb), "+{v[184:191]}"(fv),
          [r0] "+v"(x1p[0]), [r1] "+v"(x1p[1]), [r2] "+v"(x1p[2]), [r3] "+v"(x1p[3]) STAMP_OUTS
        : "{v[64:71]}"(q[0]), "{v[72:79]}"(q[1]), "{v[80:87]}"(q[2]), "{v[88:95]}"(q[3]), "{v[144:147]}"(ones), "{v148}"(0xddddu), "{v149}"(kvoff), "{v150}"(kvoff + 4096),
          "{v[216:223]}"(cv), "{s[40:43]}"(srd), "{s47}"(L), [xb] "s"(x0)
        : "v96", "v97", "v98", "v99", "v100", "v101", "v102", "v103", "v104", "v105", "v106", "v107", "v108", "v109", "v110", "v111", "v112", "v113", "v114", "v115", "v116", "v117", "v118", "v119", "v120", "v121", "v122", "v123", "v124", "v125", "v126", "v127", "v151", "v192", "v193", "v194", "v195", "v196", "v197", "v198", "v199", "v200", "v201", "v202", "v203", "v204", "v205", "v206", "v207", "v208", "v209", "v210", "v211", "v212", "v213", "v214", "v215", "s45", "s46", "vcc", "scc", "memory");
      STAMP_PUT(6 + 16 * L);
      STAMPV(9 + 16 * L);
      PHASE_VARS;
      const unsigned dst = OFF_OF + L * (5 * MBY) + (tm * 4 + 2 * kh) * TS + (tn * 2 + hh) * 2048 + lane * 16;
      auto put = [&](int u, const f32x16& o) {
#pragma unroll
        for (int c = 0; c < 2; ++c) {
          u32x4 w;
#pragma unroll
          for (int e = 0; e < 4; ++e) w[e] = pack2(o[8 * c + 2 * e], o[8 * c + 2 * e + 1]);
          stg(rs, dst + u * TS + c * 1024, w);
        }
      };
      const float la = lv[8] + lv[0], lb = lv[12] + lv[4];
      o2 = (o2 + o0) * RCP(la); put(0, o2);
      SBAR();
      o3 = (o3 + o1) * RCP(lb); put(1, o3);
      SBAR();
      {
        const f32x16 t = o2 + o3;
        float sum = 0.f;
#pragma unroll
        for (int e = 0; e < 16; e += 4) sum += (t[e] + t[e + 1]) + (t[e + 2] + t[e + 3]);
        if (__any(!(fabsf(sum) < 1e37f) | !(fmaxf(la, lb) < 1e27f) | !(fminf(la, lb) > 1e-9f) | !(la + lb == la + lb))) {
          DRAIN();
          attn_safe(rs, kvoff - kh * 8192, q[2], ones, o2); put(0, o2);
          attn_safe(rs, kvoff - kh * 8192, q[3], ones, o3); put(1, o3);
        }
      }
    }
    STAMPV(10 + 16 * L);
    {
      f32x16 acc[2];
      u32x4 res[4];
      u32x2 wrc[8];
      STAMP_DECL;
      { PHASE_VARS;
      const unsigned fl = FL_O + (L * 16 + tm) * 8192 + kh * 4096;
      DRAIN();
      STAMPV(11 + 16 * L);
      __builtin_amdgcn_raw_buffer_store_b32(TAG, rs, lane == 0 ? fl + (tn * 2 + hh) * 128 : OOB, 0, 16);
      BARRIER();
#pragma unroll
      for (int g = 0; g < 8; ++g) wrc[g] = *reinterpret_cast<const u32x2*>(smem + L_WR + g * 16 + hi * 8);
      if (L == 0) {
#pragma unroll
        for (int i = 0; i < 4; ++i) res[i] = fo_swap(x1p[i]);
      } else {
        const bool routed = (int)words[3] == tile * 32 + r32;
        const float mb1 = reinterpret_cast<const float*>(smem + L_AB)[256 + wid * 64 + r32], rd1 = reinterpret_cast<const float*>(smem + L_AB)[256 + wid * 64 + 32 + r32];
#pragma unroll
        for (int i = 0; i < 4; ++i) {
          const u32x4 pr = *reinterpret_cast<const u32x4*>(smem + L_X2P + (i * 2 + hi) * 16);
#pragma unroll
          for (int e = 0; e < 4; ++e) {
            const unsigned h2 = ln_pair(x1p[i][e], mb1, rd1);
            unsigned t = pack2(lo16(x1p[i][e]) + lo16(h2), hi16(x1p[i][e]) + hi16(h2));
            PIN(t);
            res[i][e] = routed ? pr[e] : t;
          }
        }
      }
#pragma unroll
      for (int g = 0; g < 8; ++g) PIN(wrc[g]);
#pragma unroll
      for (int i = 0; i < 4; ++i) PIN(res[i]);
      unsigned soff = tn * 4096;
      const unsigned a0 = lbase + wrow * 128 + ((wch ^ wsw) << 4) + 8 * (tp & 1), a1 = lbase + wrow * 128 + (((4 + wch) ^ wsw) << 4) + 8 * (tp & 1);
      asm volatile(
        "ds_read_b64_tr_b16 v[32:33], v193 offset:0\n\t"
        "ds_read_b64_tr_b16 v[34:35], v193 offset:1024\n\t"
        "ds_read_b64_tr_b16 v[36:37], v194 offset:0\n\t"
        "ds_read_b64_tr_b16 v[38:39], v194 offset:1024\n\t"
        "ds_read_b64_tr_b16 v[40:41], v193 offset:2048\n\t"
        "ds_read_b64_tr_b16 v[42:43], v193 offset:3072\n\t"
        "ds_read_b64_tr_b16 v[44:45], v194 offset:2048\n\t"
        "ds_read_b64_tr_b16 v[46:47], v194 offset:3072\n\t"
        "ds_read_b64_tr_b16 v[48:49], v193 offset:4096\n\t"
        "ds_read_b64_tr_b16 v[50:51], v193 offset:5120\n\t"
        "ds_read_b64_tr_b16 v[52:53], v194 offset:4096\n\t"
        "ds_read_b64_tr_b16 v[54:55], v194 offset:5120\n\t"
        "ds_read_b64_tr_b16 v[56:57], v193 offset:6144\n\t"
        "ds_read_b64_tr_b16 v[58:59], v193 offset:7168\n\t"
        "ds_read_b64_tr_b16 v[60:61], v194 offset:6144\n\t"
        "ds_read_b64_tr_b16 v[62:63], v194 offset:7168\n\t"
        "s_mov_b32 s45, 0x8000\n\t"
        "2:\n\t"
        "buffer_load_dword v200, v198, s[40:43], 0 offen sc1\n\t"
        "s_sub_u32 s45, s45, 1\n\t"
        "s_waitcnt vmcnt(0)\n\t"
        "s_cbranch_scc1 3f\n\t"
        "v_cmp_ne_u32 vcc, v199, v200\n\t"
        "s_cbranch_vccz 3f\n\t"
        "s_sleep 1\n\t"
        "s_branch 2b\n\t"
        "3:\n\t"
        "buffer_load_dwordx4 v[64:67], v192, s[40:43], s44 offen offset:0 sc1\n\t"
        "buffer_load_dwordx4 v[68:71], v192, s[40:43], s44 offen offset:1024 sc1\n\t"
        "buffer_load_dwordx4 v[72:75], v192, s[40:43], s44 offen offset:2048 sc1\n\t"
        "buffer_load_dwordx4 v[76:79], v192, s[40:43], s44 offen offset:3072 sc1\n\t"
        "s_add_u32 s44, s44, 0x1000\n\t"
        "s_and_b32 s44, s44, 0xffff\n\t"
        "buffer_load_dwordx4 v[80:83], v192, s[40:43], s44 offen offset:0 sc1\n\t"
        "buffer_load_dwordx4 v[84:87], v192, s[40:43], s44 offen offset:1024 sc1\n\t"
        "buffer_load_dwordx4 v[88:91], v192, s[40:43], s44 offen offset:2048 sc1\n\t"
        "buffer_load_dwordx4 v[92:95], v192, s[40:43], s44 offen offset:3072 sc1\n\t"
        "s_add_u32 s44, s44, 0x1000\n\t"
        "s_and_b32 s44, s44, 0xffff\n\t"
        "buffer_load_dwordx4 v[96:99], v192, s[40:43], s44 offen offset:0 sc1\n\t"
        "buffer_load_dwordx4 v[100:103], v192, s[40:43], s44 offen offset:1024 sc1\n\t"
        "buffer_load_dwordx4 v[104:107], v192, s[40:43], s44 offen offset:2048 sc1\n\t"
        "buffer_load_dwordx4 v[108:111], v192, s[40:43], s44 offen offset:3072 sc1\n\t"
        "s_waitcnt vmcnt(11) lgkmcnt(14)\n\t"
        "v_mfma_f32_32x32x16_bf16 v[0:15], v[32:35], v[64:67], 0\n\t"
        "ds_read_b64_tr_b16 v[32:33], v193 offset:8192\n\t"
        "ds_read_b64_tr_b16 v[34:35], v193 offset:9216\n\t"
        "s_waitcnt lgkmcnt(14)\n\t"
        "v_mfma_f32_32x32x16_bf16 v[16:31], v[36:39], v[64:67], 0\n\t"
        "ds_read_b64_tr_b16 v[36:37], v194 offset:8192\n\t"
        "ds_read_b64_tr_b16 v[38:39], v194 offset:9216\n\t"
        "s_add_u32 s44, s44, 0x1000\n\t"
        "s_and_b32 s44, s44, 0xffff\n\t"
        "buffer_load_dwordx4 v[112:115], v192, s[40:43], s44 offen offset:0 sc1\n\t"
        "s_waitcnt vmcnt(11) lgkmcnt(14)\n\t"
        "v_mfma_f32_32x32x16_bf16 v[0:15], v[40:43], v[68:71], v[0:15]\n\t"
        "ds_read_b64_tr_b16 v[40:41], v193 offset:10240\n\t"
        "ds_read_b64_tr_b16 v[42:43], v193 offset:11264\n\t"
        "s_waitcnt lgkmcnt(14)\n\t"
        "v_mfma_f32_32x32x16_bf16 v[16:31], v[44:47], v[68:71], v[16:31]\n\t"
        "ds_read_b64_tr_b16 v[44:45], v194 offset:10240\n\t"
        "ds_read_b64_tr_b16 v[46:47], v194 offset:11264\n\t"
        "buffer_load_dwordx4 v[116:119], v192, s[40:43], s44 offen offset:1024 sc1\n\t"
        "s_waitcnt vmcnt(11) lgkmcnt(14)\n\t"
        "v_mfma_f32_32x32x16_bf16 v[0:15], v[48:51], v[72:75], v[0:15]\n\t"
        "ds_read_b64_tr_b16 v[48:49], v193 offset:12288\n\t"
        "ds_read_b64_tr_b16 v[50:51], v193 offset:13312\n\t"
        "s_waitcnt lgkmcnt(14)\n\t"
        "v_mfma_f32_32x32x16_bf16 v[16:31], v[52:55], v[72:75], v[16:31]\n\t"
        "ds_read_b64_tr_b16 v[52:53], v194 offset:12288\n\t"
        "ds_read_b64_tr_b16 v[54:55], v194 offset:13312\n\t"
        "buffer_load_dwordx4 v[120:123], v192, s[40:43], s44 offen offset:2048 sc1\n\t"
        "s_waitcnt vmcnt(11) lgkmcnt(14)\n\t"
        "v_mfma_f32_32x32x16_bf16 v[0:15], v[56:59], v[76:79], v[0:15]\n\t"
        "ds_read_b64_tr_b16 v[56:57], v193 offset:14336\n\t"
        "ds_read_b64_tr_b16 v[58:59], v193 offset:15360\n\t"
        "s_waitcnt lgkmcnt(14)\n\t"
        "v_mfma_f32_32x32x16_bf16 v[16:31], v[60:63], v[76:79], v[16:31]\n\t"
        "ds_read_b64_tr_b16 v[60:61], v194 offset:14336\n\t"
        "ds_read_b64_tr_b16 v[62:63], v194 offset:15360\n\t"
        "buffer_load_dwordx4 v[124:127], v192, s[40:43], s44 offen offset:3072 sc1\n\t"
        "s_waitcnt vmcnt(11) lgkmcnt(14)\n\t"
        "v_mfma_f32_32x32x16_bf16 v[0:15], v[32:35], v[80:83], v[0:15]\n\t"
        "ds_read_b64_tr_b16 v[32:33], v193 offset:16384\n\t"
        "ds_read_b64_tr_b16 v[34:35], v193 offset:17408\n\t"
        "s_waitcnt lgkmcnt(14)\n\t"
        "v_mfma_f32_32x32x16_bf16 v[16:31], v[36:39], v[80:83], v[16:31]\n\t"
        "ds_read_b64_tr_b16 v[36:37], v194 offset:16384\n\t"
        "ds_read_b64_tr_b16 v[38:39], v194 offset:17408\n\t"
        "s_add_u32 s44, s44, 0x1000\n\t"
        "s_and_b32 s44, s44, 0xffff\n\t"
        "buffer_load_dwordx4 v[128:131], v192, s[40:43], s44 offen offset:0 sc1\n\t"
        "s_waitcnt vmcnt(11) lgkmcnt(14)\n\t"
        "v_mfma_f32_32x32x16_bf16 v[0:15], v[40:43], v[84:87], v[0:15]\n\t"
        "ds_read_b64_tr_b16 v[40:41], v193 offset:18432\n\t"
        "ds_read_b64_tr_b16 v[42:43], v193 offset:19456\n\t"
        "s_waitcnt lgkmcnt(14)\n\t"
        "v_mfma_f32_32x32x16_bf16 v[16:31], v[44:47], v[84:87], v[16:31]\n\t"
        "ds_read_b64_tr_b16 v[44:45], v194 offset:18432\n\t"
        "ds_read_b64_tr_b16 v[46:47], v194 offset:19456\n\t"
        "buffer_load_dwordx4 v[132:135], v192, s[40:43], s44 offen offset:1024 sc1\n\t"
        "s_waitcnt vmcnt(11) lgkmcnt(14)\n\t"
        "v_mfma_f32_32x32x16_bf16 v[0:15], v[48:51], v[88:91], v[0:15]\n\t"
        "ds_read_b64_tr_b16 v[48:49], v193 offset:20480\n\t"
        "ds_read_b64_tr_b16 v[50:51], v193 offset:21504\n\t"
        "s_waitcnt lgkmcnt(14)\n\t"
        "v_mfma_f32_32x32x16_bf16 v[16:31], v[52:55], v[88:91], v[16:31]\n\t"
        "ds_read_b64_tr_b16 v[52:53], v194 offset:20480\n\t"
        "ds_read_b64_tr_b16 v[54:55], v194 offset:21504\n\t"
        "buffer_load_dwordx4 v[136:139], v192, s[40:43], s44 offen offset:2048 sc1\n\t"
        "s_waitcnt vmcnt(11) lgkmcnt(14)\n\t"
        "v_mfma_f32_32x32x16_bf16 v[0:15], v[56:59], v[92:95], v[0:15]\n\t"
        "ds_read_b64_tr_b16 v[56:57], v193 offset:22528\n\t"
        "ds_read_b64_tr_b16 v[58:59], v193 offset:23552\n\t"
        "s_waitcnt lgkmcnt(14)\n\t"
        "v_mfma_f32_32x32x16_bf16 v[16:31], v[60:63], v[92:95], v[16:31]\n\t"
        "ds_read_b64_tr_b16 v[60:61], v194 offset:22528\n\t"
        "ds_read_b64_tr_b16 v[62:63], v194 offset:23552\n\t"
        "buffer_load_dwordx4 v[140:143], v192, s[40:43], s44 offen offset:3072 sc1\n\t"
        "s_waitcnt vmcnt(11) lgkmcnt(14)\n\t"
        "v_mfma_f32_32x32x16_bf16 v[0:15], v[32:35], v[96:99], v[0:15]\n\t"
        "ds_read_b64_tr_b16 v[32:33], v193 offset:24576\n\t"
        "ds_read_b64_tr_b16 v[34:35], v193 offset:25600\n\t"
        "s_waitcnt lgkmcnt(14)\n\t"
        "v_mfma_f32_32x32x16_bf16 v[16:31], v[36:39], v[96:99], v[16:31]\n\t"
        "ds_read_b64_tr_b16 v[36:37], v194 offset:24576\n\t"
        "ds_read_b64_tr_b16 v[38:39], v194 offset:25600\n\t"
        "s_add_u32 s44, s44, 0x1000\n\t"
        "s_and_b32 s44, s44, 0xffff\n\t"
        "buffer_load_dwordx4 v[144:147], v192, s[40:43], s44 offen offset:0 sc1\n\t"
        "s_waitcnt vmcnt(11) lgkmcnt(14)\n\t"
        "v_mfma_f32_32x32x16_bf16 v[0:15], v[40:43], v[100:103], v[0:15]\n\t"
        "ds_read_b64_tr_b16 v[40:41], v193 offset:26624\n\t"
        "ds_read_b64_tr_b16 v[42:43], v193 offset:27648\n\t"
        "s_waitcnt lgkmcnt(14)\n\t"
        "v_mfma_f32_32x32x16_bf16 v[16:31], v[44:47], v[100:103], v[16:31]\n\t"
        "ds_read_b64_tr_b16 v[44:45], v194 offset:26624\n\t"
        "ds_read_b64_tr_b16 v[46:47], v194 offset:27648\n\t"
        "buffer_load_dwordx4 v[148:151], v192, s[40:43], s44 offen offset:1024 sc1\n\t"
        "s_waitcnt vmcnt(11) lgkmcnt(14)\n\t"
        "v_mfma_f32_32x32x16_bf16 v[0:15], v[48:51], v[104:107], v[0:15]\n\t"
        "ds_read_b64_tr_b16 v[48:49], v193 offset:28672\n\t"
        "ds_read_b64_tr_b16 v[50:51], v193 offset:29696\n\t"
        "s_waitcnt lgkmcnt(14)\n\t"
        "v_mfma_f32_32x32x16_bf16 v[16:31], v[52:55], v[104:107], v[16:31]\n\t"
        "ds_read_b64_tr_b16 v[52:53], v194 offset:28672\n\t"
        "ds_read_b64_tr_b16 v[54:55], v194 offset:29696\n\t"
        "buffer_load_dwordx4 v[152:155], v192, s[40:43], s44 offen offset:2048 sc1\n\t"
        "s_waitcnt vmcnt(11) lgkmcnt(14)\n\t"
        "v_mfma_f32_32x32x16_bf16 v[0:15], v[56:59], v[108:111], v[0:15]\n\t"
        "ds_read_b64_tr_b16 v[56:57], v193 offset:30720\n\t"
        "ds_read_b64_tr_b16 v[58:59], v193 offset:31744\n\t"
        "s_waitcnt lgkmcnt(14)\n\t"
        "v_mfma_f32_32x32x16_bf16 v[16:31], v[60:63], v[108:111], v[16:31]\n\t"
        "ds_read_b64_tr_b16 v[60:61], v194 offset:30720\n\t"
        "ds_read_b64_tr_b16 v[62:63], v194 offset:31744\n\t"
        "buffer_load_dwordx4 v[156:159], v192, s[40:43], s44 offen offset:3072 sc1\n\t"
        "s_waitcnt vmcnt(11) lgkmcnt(14)\n\t"
        "v_mfma_f32_32x32x16_bf16 v[0:15], v[32:35], v[112:115], v[0:15]\n\t"
        "ds_read_b64_tr_b16 v[32:33], v193 offset:32768\n\t"
        "ds_read_b64_tr_b16 v[34:35], v193 offset:33792\n\t"
        "s_waitcnt lgkmcnt(14)\n\t"
        "v_mfma_f32_32x32x16_bf16 v[16:31], v[36:39], v[112:115], v[16:31]\n\t"
        "ds_read_b64_tr_b16 v[36:37], v194 offset:32768\n\t"
        "ds_read_b64_tr_b16 v[38:39], v194 offset:33792\n\t"
        "s_add_u32 s44, s44, 0x1000\n\t"
        "s_and_b32 s44, s44, 0xffff\n\t"
        "buffer_load_dwordx4 v[160:163], v192, s[40:43], s44 offen offset:0 sc1\n\t"
        "s_waitcnt vmcnt(11) lgkmcnt(14)\n\t"
        "v_mfma_f32_32x32x16_bf16 v[0:15], v[40:43], v[116:119], v[0:15]\n\t"
        "ds_read_b64_tr_b16 v[40:41], v193 offset:34816\n\t"
        "ds_read_b64_tr_b16 v[42:43], v193 offset:35840\n\t"
        "s_waitcnt lgkmcnt(14)\n\t"
        "v_mfma_f32_32x32x16_bf16 v[16:31], v[44:47], v[116:119], v[16:31]\n\t"
        "ds_read_b64_tr_b16 v[44:45], v194 offset:34816\n\t"
        "ds_read_b64_tr_b16 v[46:47], v194 offset:35840\n\t"
        "buffer_load_dwordx4 v[164:167], v192, s[40:43], s44 offen offset:1024 sc1\n\t"
        "s_waitcnt vmcnt(11) lgkmcnt(14)\n\t"
        "v_mfma_f32_32x32x16_bf16 v[0:15], v[48:51], v[120:123], v[0:15]\n\t"
        "ds_read_b64_tr_b16 v[48:49], v193 offset:36864\n\t"
        "ds_read_b64_tr_b16 v[50:51], v193 offset:37888\n\t"
        "s_waitcnt lgkmcnt(14)\n\t"
        "v_mfma_f32_32x32x16_bf16 v[16:31], v[52:55], v[120:123], v[16:31]\n\t"
        "ds_read_b64_tr_b16 v[52:53], v194 offset:36864\n\t"
        "ds_read_b64_tr_b16 v[54:55], v194 offset:37888\n\t"
        "buffer_load_dwordx4 v[168:171], v192, s[40:43], s44 offen offset:2048 sc1\n\t"
        "s_waitcnt vmcnt(11) lgkmcnt(14)\n\t"
        "v_mfma_f32_32x32x16_bf16 v[0:15], v[56:59], v[124:127], v[0:15]\n\t"
        "ds_read_b64_tr_b16 v[56:57], v193 offset:38912\n\t"
        "ds_read_b64_tr_b16 v[58:59], v193 offset:39936\n\t"
        "s_waitcnt lgkmcnt(14)\n\t"
        "v_mfma_f32_32x32x16_bf16 v[16:31], v[60:63], v[124:127], v[16:31]\n\t"
        "ds_read_b64_tr_b16 v[60:61], v194 offset:38912\n\t"
        "ds_read_b64_tr_b16 v[62:63], v194 offset:39936\n\t"
        "buffer_load_dwordx4 v[172:175], v192, s[40:43], s44 offen offset:3072 sc1\n\t"
        "s_waitcnt vmcnt(11) lgkmcnt(14)\n\t"
        "v_mfma_f32_32x32x16_bf16 v[0:15], v[32:35], v[128:131], v[0:15]\n\t"
        "ds_read_b64_tr_b16 v[32:33], v193 offset:40960\n\t"
        "ds_read_b64_tr_b16 v[34:35], v193 offset:41984\n\t"
        "s_waitcnt lgkmcnt(14)\n\t"
        "v_mfma_f32_32x32x16_bf16 v[16:31], v[36:39], v[128:131], v[16:31]\n\t"
        "ds_read_b64_tr_b16 v[36:37], v194 offset:40960\n\t"
        "ds_read_b64_tr_b16 v[38:39], v194 offset:41984\n\t"
        "s_add_u32 s44, s44, 0x1000\n\t"
        "s_and_b32 s44, s44, 0xffff\n\t"
        "buffer_load_dwordx4 v[176:179], v192, s[40:43], s44 offen offset:0 sc1\n\t"
        "s_waitcnt vmcnt(11) lgkmcnt(14)\n\t"
        "v_mfma_f32_32x32x16_bf16 v[0:15], v[40:43], v[132:135], v[0:15]\n\t"
        "ds_read_b64_tr_b16 v[40:41], v193 offset:43008\n\t"
        "ds_read_b64_tr_b16 v[42:43], v193 offset:44032\n\t"
        "s_waitcnt lgkmcnt(14)\n\t"
        "v_mfma_f32_32x32x16_bf16 v[16:31], v[44:47], v[132:135], v[16:31]\n\t"
        "ds_read_b64_tr_b16 v[44:45], v194 offset:43008\n\t"
        "ds_read_b64_tr_b16 v[46:47], v194 offset:44032\n\t"
        "buffer_load_dwordx4 v[180:183], v192, s[40:43], s44 offen offset:1024 sc1\n\t"
        "s_waitcnt vmcnt(11) lgkmcnt(14)\n\t"
        "v_mfma_f32_32x32x16_bf16 v[0:15], v[48:51], v[136:139], v[0:15]\n\t"
        "ds_read_b64_tr_b16 v[48:49], v193 offset:45056\n\t"
        "ds_read_b64_tr_b16 v[50:51], v193 offset:46080\n\t"
        "s_waitcnt lgkmcnt(14)\n\t"
        "v_mfma_f32_32x32x16_bf16 v[16:31], v[52:55], v[136:139], v[16:31]\n\t"
        "ds_read_b64_tr_b16 v[52:53], v194 offset:45056\n\t"
        "ds_read_b64_tr_b16 v[54:55], v194 offset:46080\n\t"
        "buffer_load_dwordx4 v[184:187], v192, s[40:43], s44 offen offset:2048 sc1\n\t"
        "s_waitcnt vmcnt(11) lgkmcnt(14)\n\t"
        "v_mfma_f32_32x32x16_bf16 v[0:15], v[56:59], v[140:143], v[0:15]\n\t"
        "ds_read_b64_tr_b16 v[56:57], v193 offset:47104\n\t"
        "ds_read_b64_tr_b16 v[58:59], v193 offset:48128\n\t"
        "s_waitcnt lgkmcnt(14)\n\t"
        "v_mfma_f32_32x32x16_bf16 v[16:31], v[60:63], v[140:143], v[16:31]\n\t"
        "ds_read_b64_tr_b16 v[60:61], v194 offset:47104\n\t"
        "ds_read_b64_tr_b16 v[62:63], v194 offset:48128\n\t"
        "buffer_load_dwordx4 v[188:191], v192, s[40:43], s44 offen offset:3072 sc1\n\t"
        "s_waitcnt vmcnt(11) lgkmcnt(14)\n\t"
        "v_mfma_f32_32x32x16_bf16 v[0:15], v[32:35], v[144:147], v[0:15]\n\t"
        "ds_read_b64_tr_b16 v[32:33], v193 offset:49152\n\t"
        "ds_read_b64_tr_b16 v[34:35], v193 offset:50176\n\t"
        "s_waitcnt lgkmcnt(14)\n\t"
        "v_mfma_f32_32x32x16_bf16 v[16:31], v[36:39], v[144:147], v[16:31]\n\t"
        "ds_read_b64_tr_b16 v[36:37], v194 offset:49152\n\t"
        "ds_read_b64_tr_b16 v[38:39], v194 offset:50176\n\t"
        "s_add_u32 s44, s44, 0x1000\n\t"
        "s_and_b32 s44, s44, 0xffff\n\t"
        "buffer_load_dwordx4 v[64:67], v192, s[40:43], s44 offen offset:0 sc1\n\t"
        "s_waitcnt vmcnt(11) lgkmcnt(14)\n\t"
        "v_mfma_f32_32x32x16_bf16 v[0:15], v[40:43], v[148:151], v[0:15]\n\t"
        "ds_read_b64_tr_b16 v[40:41], v193 offset:51200\n\t"
        "ds_read_b64_tr_b16 v[42:43], v193 offset:52224\n\t"
        "s_waitcnt lgkmcnt(14)\n\t"
        "v_mfma_f32_32x32x16_bf16 v[16:31], v[44:47], v[148:151], v[16:31]\n\t"
        "ds_read_b64_tr_b16 v[44:45], v194 offset:51200\n\t"
        "ds_read_b64_tr_b16 v[46:47], v194 offset:52224\n\t"
        "buffer_load_dwordx4 v[68:71], v192, s[40:43], s44 offen offset:1024 sc1\n\t"
        "s_waitcnt vmcnt(11) lgkmcnt(14)\n\t"
        "v_mfma_f32_32x32x16_bf16 v[0:15], v[48:51], v[152:155], v[0:15]\n\t"
        "ds_read_b64_tr_b16 v[48:49], v193 offset:53248\n\t"
        "ds_read_b64_tr_b16 v[50:51], v193 offset:54272\n\t"
        "s_waitcnt lgkmcnt(14)\n\t"
        "v_mfma_f32_32x32x16_bf16 v[16:31], v[52:55], v[152:155], v[16:31]\n\t"
        "ds_read_b64_tr_b16 v[52:53], v194 offset:53248\n\t"
        "ds_read_b64_tr_b16 v[54:55], v194 offset:54272\n\t"
        "buffer_load_dwordx4 v[72:75], v192, s[40:43], s44 offen offset:2048 sc1\n\t"
        "s_waitcnt vmcnt(11) lgkmcnt(14)\n\t"
        "v_mfma_f32_32x32x16_bf16 v[0:15], v[56:59], v[156:159], v[0:15]\n\t"
        "ds_read_b64_tr_b16 v[56:57], v193 offset:55296\n\t"
        "ds_read_b64_tr_b16 v[58:59], v193 offset:56320\n\t"
        "s_waitcnt lgkmcnt(14)\n\t"
        "v_mfma_f32_32x32x16_bf16 v[16:31], v[60:63], v[156:159], v[16:31]\n\t"
        "ds_read_b64_tr_b16 v[60:61], v194 offset:55296\n\t"
        "ds_read_b64_tr_b16 v[62:63], v194 offset:56320\n\t"
        "buffer_load_dwordx4 v[76:79], v192, s[40:43], s44 offen offset:3072 sc1\n\t"
        "s_waitcnt vmcnt(11) lgkmcnt(14)\n\t"
        "v_mfma_f32_32x32x16_bf16 v[0:15], v[32:35], v[160:163], v[0:15]\n\t"
        "ds_read_b64_tr_b16 v[32:33], v193 offset:57344\n\t"
        "ds_read_b64_tr_b16 v[34:35], v193 offset:58368\n\t"
        "s_waitcnt lgkmcnt(14)\n\t"
        "v_mfma_f32_32x32x16_bf16 v[16:31], v[36:39], v[160:163], v[16:31]\n\t"
        "ds_read_b64_tr_b16 v[36:37], v194 offset:57344\n\t"
        "ds_read_b64_tr_b16 v[38:39], v194 offset:58368\n\t"
        "s_add_u32 s44, s44, 0x1000\n\t"
        "s_and_b32 s44, s44, 0xffff\n\t"
        "buffer_load_dwordx4 v[80:83], v192, s[40:43], s44 offen offset:0 sc1\n\t"
        "s_waitcnt vmcnt(11) lgkmcnt(14)\n\t"
        "v_mfma_f32_32x32x16_bf16 v[0:15], v[40:43], v[164:167], v[0:15]\n\t"
        "ds_read_b64_tr_b16 v[40:41], v193 offset:59392\n\t"
        "ds_read_b64_tr_b16 v[42:43], v193 offset:60416\n\t"
        "s_waitcnt lgkmcnt(14)\n\t"
        "v_mfma_f32_32x32x16_bf16 v[16:31], v[44:47], v[164:167], v[16:31]\n\t"
        "ds_read_b64_tr_b16 v[44:45], v194 offset:59392\n\t"
        "ds_read_b64_tr_b16 v[46:47], v194 offset:60416\n\t"
        "buffer_load_dwordx4 v[84:87], v192, s[40:43], s44 offen offset:1024 sc1\n\t"
        "s_waitcnt vmcnt(11) lgkmcnt(14)\n\t"
        "v_mfma_f32_32x32x16_bf16 v[0:15], v[48:51], v[168:171], v[0:15]\n\t"
        "ds_read_b64_tr_b16 v[48:49], v193 offset:61440\n\t"
        "ds_read_b64_tr_b16 v[50:51], v193 offset:62464\n\t"
        "s_waitcnt lgkmcnt(14)\n\t"
        "v_mfma_f32_32x32x16_bf16 v[16:31], v[52:55], v[168:171], v[16:31]\n\t"
        "ds_read_b64_tr_b16 v[52:53], v194 offset:61440\n\t"
        "ds_read_b64_tr_b16 v[54:55], v194 offset:62464\n\t"
        "buffer_load_dwordx4 v[88:91], v192, s[40:43], s44 offen offset:2048 sc1\n\t"
        "s_waitcnt vmcnt(11) lgkmcnt(14)\n\t"
        "v_mfma_f32_32x32x16_bf16 v[0:15], v[56:59], v[172:175], v[0:15]\n\t"
        "ds_read_b64_tr_b16 v[56:57], v193 offset:63488\n\t"
        "ds_read_b64_tr_b16 v[58:59], v193 offset:64512\n\t"
        "s_waitcnt lgkmcnt(14)\n\t"
        "v_mfma_f32_32x32x16_bf16 v[16:31], v[60:63], v[172:175], v[16:31]\n\t"
        "ds_read_b64_tr_b16 v[60:61], v194 offset:63488\n\t"
        "ds_read_b64_tr_b16 v[62:63], v194 offset:64512\n\t"
        "buffer_load_dwordx4 v[92:95], v192, s[40:43], s44 offen offset:3072 sc1\n\t"
        "s_waitcnt vmcnt(11) lgkmcnt(14)\n\t"
        "v_mfma_f32_32x32x16_bf16 v[0:15], v[32:35], v[176:179], v[0:15]\n\t"
        "ds_read_b64_tr_b16 v[32:33], v195 offset:0\n\t"
        "ds_read_b64_tr_b16 v[34:35], v195 offset:1024\n\t"
        "s_waitcnt lgkmcnt(14)\n\t"
        "v_mfma_f32_32x32x16_bf16 v[16:31], v[36:39], v[176:179], v[16:31]\n\t"
        "ds_read_b64_tr_b16 v[36:37], v196 offset:0\n\t"
        "ds_read_b64_tr_b16 v[38:39], v196 offset:1024\n\t"
        "s_add_u32 s44, s44, 0x1000\n\t"
        "s_and_b32 s44, s44, 0xffff\n\t"
        "buffer_load_dwordx4 v[96:99], v192, s[40:43], s44 offen offset:0 sc1\n\t"
        "s_waitcnt vmcnt(11) lgkmcnt(14)\n\t"
        "v_mfma_f32_32x32x16_bf16 v[0:15], v[40:43], v[180:183], v[0:15]\n\t"
        "ds_read_b64_tr_b16 v[40:41], v195 offset:2048\n\t"
        "ds_read_b64_tr_b16 v[42:43], v195 offset:3072\n\t"
        "s_waitcnt lgkmcnt(14)\n\t"
        "v_mfma_f32_32x32x16_bf16 v[16:31], v[44:47], v[180:183], v[16:31]\n\t"
        "ds_read_b64_tr_b16 v[44:45], v196 offset:2048\n\t"
        "ds_read_b64_tr_b16 v[46:47], v196 offset:3072\n\t"
        "buffer_load_dwordx4 v[100:103], v192, s[40:43], s44 offen offset:1024 sc1\n\t"
        "s_waitcnt vmcnt(11) lgkmcnt(14)\n\t"
        "v_mfma_f32_32x32x16_bf16 v[0:15], v[48:51], v[184:187], v[0:15]\n\t"
        "ds_read_b64_tr_b16 v[48:49], v195 offset:4096\n\t"
        "ds_read_b64_tr_b16 v[50:51], v195 offset:5120\n\t"
        "s_waitcnt lgkmcnt(14)\n\t"
        "v_mfma_f32_32x32x16_bf16 v[16:31], v[52:55], v[184:187], v[16:31]\n\t"
        "ds_read_b64_tr_b16 v[52:53], v196 offset:4096\n\t"
        "ds_read_b64_tr_b16 v[54:55], v196 offset:5120\n\t"
        "buffer_load_dwordx4 v[104:107], v192, s[40:43], s44 offen offset:2048 sc1\n\t"
        "s_waitcnt vmcnt(11) lgkmcnt(14)\n\t"
        "v_mfma_f32_32x32x16_bf16 v[0:15], v[56:59], v[188:191], v[0:15]\n\t"
        "ds_read_b64_tr_b16 v[56:57], v195 offset:6144\n\t"
        "ds_read_b64_tr_b16 v[58:59], v195 offset:7168\n\t"
        "s_waitcnt lgkmcnt(14)\n\t"
        "v_mfma_f32_32x32x16_bf16 v[16:31], v[60:63], v[188:191], v[16:31]\n\t"
        "ds_read_b64_tr_b16 v[60:61], v196 offset:6144\n\t"
        "ds_read_b64_tr_b16 v[62:63], v196 offset:7168\n\t"
        "buffer_load_dwordx4 v[108:111], v192, s[40:43], s44 offen offset:3072 sc1\n\t"
        "s_waitcnt vmcnt(11) lgkmcnt(14)\n\t"
        "v_mfma_f32_32x32x16_bf16 v[0:15], v[32:35], v[64:67], v[0:15]\n\t"
        "ds_read_b64_tr_b16 v[32:33], v195 offset:8192\n\t"
        "ds_read_b64_tr_b16 v[34:35], v195 offset:9216\n\t"
        "s_waitcnt lgkmcnt(14)\n\t"
        "v_mfma_f32_32x32x16_bf16 v[16:31], v[36:39], v[64:67], v[16:31]\n\t"
        "ds_read_b64_tr_b16 v[36:37], v196 offset:8192\n\t"
        "ds_read_b64_tr_b16 v[38:39], v196 offset:9216\n\t"
        "s_add_u32 s44, s44, 0x1000\n\t"
        "s_and_b32 s44, s44, 0xffff\n\t"
        "buffer_load_dwordx4 v[112:115], v192, s[40:43], s44 offen offset:0 sc1\n\t"
        "s_waitcnt vmcnt(11) lgkmcnt(14)\n\t"
        "v_mfma_f32_32x32x16_bf16 v[0:15], v[40:43], v[68:71], v[0:15]\n\t"
        "ds_read_b64_tr_b16 v[40:41], v195 offset:10240\n\t"
        "ds_read_b64_tr_b16 v[42:43], v195 offset:11264\n\t"
        "s_waitcnt lgkmcnt(14)\n\t"
        "v_mfma_f32_32x32x16_bf16 v[16:31], v[44:47], v[68:71], v[16:31]\n\t"
        "ds_read_b64_tr_b16 v[44:45], v196 offset:10240\n\t"
        "ds_read_b64_tr_b16 v[46:47], v196 offset:11264\n\t"
        "buffer_load_dwordx4 v[116:119], v192, s[40:43], s44 offen offset:1024 sc1\n\t"
        "s_waitcnt vmcnt(11) lgkmcnt(14)\n\t"
        "v_mfma_f32_32x32x16_bf16 v[0:15], v[48:51], v[72:75], v[0:15]\n\t"
        "ds_read_b64_tr_b16 v[48:49], v195 offset:12288\n\t"
        "ds_read_b64_tr_b16 v[50:51], v195 offset:13312\n\t"
        "s_waitcnt lgkmcnt(14)\n\t"
        "v_mfma_f32_32x32x16_bf16 v[16:31], v[52:55], v[72:75], v[16:31]\n\t"
        "ds_read_b64_tr_b16 v[52:53], v196 offset:12288\n\t"
        "ds_read_b64_tr_b16 v[54:55], v196 offset:13312\n\t"
        "buffer_load_dwordx4 v[120:123], v192, s[40:43], s44 offen offset:2048 sc1\n\t"
        "s_waitcnt vmcnt(11) lgkmcnt(14)\n\t"
        "v_mfma_f32_32x32x16_bf16 v[0:15], v[56:59], v[76:79], v[0:15]\n\t"
        "ds_read_b64_tr_b16 v[56:57], v195 offset:14336\n\t"
        "ds_read_b64_tr_b16 v[58:59], v195 offset:15360\n\t"
        "s_waitcnt lgkmcnt(14)\n\t"
        "v_mfma_f32_32x32x16_bf16 v[16:31], v[60:63], v[76:79], v[16:31]\n\t"
        "ds_read_b64_tr_b16 v[60:61], v196 offset:14336\n\t"
        "ds_read_b64_tr_b16 v[62:63], v196 offset:15360\n\t"
        "buffer_load_dwordx4 v[124:127], v192, s[40:43], s44 offen offset:3072 sc1\n\t"
        "s_waitcnt vmcnt(11) lgkmcnt(14)\n\t"
        "v_mfma_f32_32x32x16_bf16 v[0:15], v[32:35], v[80:83], v[0:15]\n\t"
        "ds_read_b64_tr_b16 v[32:33], v195 offset:16384\n\t"
        "ds_read_b64_tr_b16 v[34:35], v195 offset:17408\n\t"
        "s_waitcnt lgkmcnt(14)\n\t"
        "v_mfma_f32_32x32x16_bf16 v[16:31], v[36:39], v[80:83], v[16:31]\n\t"
        "ds_read_b64_tr_b16 v[36:37], v196 offset:16384\n\t"
        "ds_read_b64_tr_b16 v[38:39], v196 offset:17408\n\t"
        "s_add_u32 s44, s44, 0x1000\n\t"
        "s_and_b32 s44, s44, 0xffff\n\t"
        "buffer_load_dwordx4 v[128:131], v192, s[40:43], s44 offen offset:0 sc1\n\t"
        "s_waitcnt vmcnt(11) lgkmcnt(14)\n\t"
        "v_mfma_f32_32x32x16_bf16 v[0:15], v[40:43], v[84:87], v[0:15]\n\t"
        "ds_read_b64_tr_b16 v[40:41], v195 offset:18432\n\t"
        "ds_read_b64_tr_b16 v[42:43], v195 offset:19456\n\t"
        "s_waitcnt lgkmcnt(14)\n\t"
        "v_mfma_f32_32x32x16_bf16 v[16:31], v[44:47], v[84:87], v[16:31]\n\t"
        "ds_read_b64_tr_b16 v[44:45], v196 offset:18432\n\t"
        "ds_read_b64_tr_b16 v[46:47], v196 offset:19456\n\t"
        "buffer_load_dwordx4 v[132:135], v192, s[40:43], s44 offen offset:1024 sc1\n\t"
        "s_waitcnt vmcnt(11) lgkmcnt(14)\n\t"
        "v_mfma_f32_32x32x16_bf16 v[0:15], v[48:51], v[88:91], v[0:15]\n\t"
        "ds_read_b64_tr_b16 v[48:49], v195 offset:20480\n\t"
        "ds_read_b64_tr_b16 v[50:51], v195 offset:21504\n\t"
        "s_waitcnt lgkmcnt(14)\n\t"
        "v_mfma_f32_32x32x16_bf16 v[16:31], v[52:55], v[88:91], v[16:31]\n\t"
        "ds_read_b64_tr_b16 v[52:53], v196 offset:20480\n\t"
        "ds_read_b64_tr_b16 v[54:55], v196 offset:21504\n\t"
        "buffer_load_dwordx4 v[136:139], v192, s[40:43], s44 offen offset:2048 sc1\n\t"
        "s_waitcnt vmcnt(11) lgkmcnt(14)\n\t"
        "v_mfma_f32_32x32x16_bf16 v[0:15], v[56:59], v[92:95], v[0:15]\n\t"
        "ds_read_b64_tr_b16 v[56:57], v195 offset:22528\n\t"
        "ds_read_b64_tr_b16 v[58:59], v195 offset:23552\n\t"
        "s_waitcnt lgkmcnt(14)\n\t"
        "v_mfma_f32_32x32x16_bf16 v[16:31], v[60:63], v[92:95], v[16:31]\n\t"
        "ds_read_b64_tr_b16 v[60:61], v196 offset:22528\n\t"
        "ds_read_b64_tr_b16 v[62:63], v196 offset:23552\n\t"
        "buffer_load_dwordx4 v[140:143], v192, s[40:43], s44 offen offset:3072 sc1\n\t"
        "s_waitcnt vmcnt(11) lgkmcnt(14)\n\t"
        "v_mfma_f32_32x32x16_bf16 v[0:15], v[32:35], v[96:99], v[0:15]\n\t"
        "ds_read_b64_tr_b16 v[32:33], v195 offset:24576\n\t"
        "ds_read_b64_tr_b16 v[34:35], v195 offset:25600\n\t"
        "s_waitcnt lgkmcnt(14)\n\t"
        "v_mfma_f32_32x32x16_bf16 v[16:31], v[36:39], v[96:99], v[16:31]\n\t"
        "ds_read_b64_tr_b16 v[36:37], v196 offset:24576\n\t"
        "ds_read_b64_tr_b16 v[38:39], v196 offset:25600\n\t"
        "s_add_u32 s44, s44, 0x1000\n\t"
        "s_and_b32 s44, s44, 0xffff\n\t"
        "buffer_load_dwordx4 v[144:147], v192, s[40:43], s44 offen offset:0 sc1\n\t"
        "s_waitcnt vmcnt(11) lgkmcnt(14)\n\t"
        "v_mfma_f32_32x32x16_bf16 v[0:15], v[40:43], v[100:103], v[0:15]\n\t"
        "ds_read_b64_tr_b16 v[40:41], v195 offset:26624\n\t"
        "ds_read_b64_tr_b16 v[42:43], v195 offset:27648\n\t"
        "s_waitcnt lgkmcnt(14)\n\t"
        "v_mfma_f32_32x32x16_bf16 v[16:31], v[44:47], v[100:103], v[16:31]\n\t"
        "ds_read_b64_tr_b16 v[44:45], v196 offset:26624\n\t"
        "ds_read_b64_tr_b16 v[46:47], v196 offset:27648\n\t"
        "buffer_load_dwordx4 v[148:151], v192, s[40:43], s44 offen offset:1024 sc1\n\t"
        "s_waitcnt vmcnt(11) lgkmcnt(14)\n\t"
        "v_mfma_f32_32x32x16_bf16 v[0:15], v[48:51], v[104:107], v[0:15]\n\t"
        "ds_read_b64_tr_b16 v[48:49], v195 offset:28672\n\t"
        "ds_read_b64_tr_b16 v[50:51], v195 offset:29696\n\t"
        "s_waitcnt lgkmcnt(14)\n\t"
        "v_mfma_f32_32x32x16_bf16 v[16:31], v[52:55], v[104:107], v[16:31]\n\t"
        "ds_read_b64_tr_b16 v[52:53], v196 offset:28672\n\t"
        "ds_read_b64_tr_b16 v[54:55], v196 offset:29696\n\t"
        "buffer_load_dwordx4 v[152:155], v192, s[40:43], s44 offen offset:2048 sc1\n\t"
        "s_waitcnt vmcnt(11) lgkmcnt(14)\n\t"
        "v_mfma_f32_32x32x16_bf16 v[0:15], v[56:59], v[108:111], v[0:15]\n\t"
        "ds_read_b64_tr_b16 v[56:57], v195 offset:30720\n\t"
        "ds_read_b64_tr_b16 v[58:59], v195 offset:31744\n\t"
        "s_waitcnt lgkmcnt(14)\n\t"
        "v_mfma_f32_32x32x16_bf16 v[16:31], v[60:63], v[108:111], v[16:31]\n\t"
        "ds_read_b64_tr_b16 v[60:61], v196 offset:30720\n\t"
        "ds_read_b64_tr_b16 v[62:63], v196 offset:31744\n\t"
        "buffer_load_dwordx4 v[156:159], v192, s[40:43], s44 offen offset:3072 sc1\n\t"
        "s_waitcnt vmcnt(11) lgkmcnt(14)\n\t"
        "v_mfma_f32_32x32x16_bf16 v[0:15], v[32:35], v[112:115], v[0:15]\n\t"
        "ds_read_b64_tr_b16 v[32:33], v195 offset:32768\n\t"
        "ds_read_b64_tr_b16 v[34:35], v195 offset:33792\n\t"
        "s_waitcnt lgkmcnt(14)\n\t"
        "v_mfma_f32_32x32x16_bf16 v[16:31], v[36:39], v[112:115], v[16:31]\n\t"
        "ds_read_b64_tr_b16 v[36:37], v196 offset:32768\n\t"
        "ds_read_b64_tr_b16 v[38:39], v196 offset:33792\n\t"
        "s_add_u32 s44, s44, 0x1000\n\t"
        "s_and_b32 s44, s44, 0xffff\n\t"
        "buffer_load_dwordx4 v[160:163], v192, s[40:43], s44 offen offset:0 sc1\n\t"
        "s_waitcnt vmcnt(11) lgkmcnt(14)\n\t"
        "v_mfma_f32_32x32x16_bf16 v[0:15], v[40:43], v[116:119], v[0:15]\n\t"
        "ds_read_b64_tr_b16 v[40:41], v195 offset:34816\n\t"
        "ds_read_b64_tr_b16 v[42:43], v195 offset:35840\n\t"
        "s_waitcnt lgkmcnt(14)\n\t"
        "v_mfma_f32_32x32x16_bf16 v[16:31], v[44:47], v[116:119], v[16:31]\n\t"
        "ds_read_b64_tr_b16 v[44:45], v196 offset:34816\n\t"
        "ds_read_b64_tr_b16 v[46:47], v196 offset:35840\n\t"
        "buffer_load_dwordx4 v[164:167], v192, s[40:43], s44 offen offset:1024 sc1\n\t"
        "s_waitcnt vmcnt(11) lgkmcnt(14)\n\t"
        "v_mfma_f32_32x32x16_bf16 v[0:15], v[48:51], v[120:123], v[0:15]\n\t"
        "ds_read_b64_tr_b16 v[48:49], v195 offset:36864\n\t"
        "ds_read_b64_tr_b16 v[50:51], v195 offset:37888\n\t"
        "s_waitcnt lgkmcnt(14)\n\t"
        "v_mfma_f32_32x32x16_bf16 v[16:31], v[52:55], v[120:123], v[16:31]\n\t"
        "ds_read_b64_tr_b16 v[52:53], v196 offset:36864\n\t"
        "ds_read_b64_tr_b16 v[54:55], v196 offset:37888\n\t"
        "buffer_load_dwordx4 v[168:171], v192, s[40:43], s44 offen offset:2048 sc1\n\t"
        "s_waitcnt vmcnt(11) lgkmcnt(14)\n\t"
        "v_mfma_f32_32x32x16_bf16 v[0:15], v[56:59], v[124:127], v[0:15]\n\t"
        "ds_read_b64_tr_b16 v[56:57], v195 offset:38912\n\t"
        "ds_read_b64_tr_b16 v[58:59], v195 offset:39936\n\t"
        "s_waitcnt lgkmcnt(14)\n\t"
        "v_mfma_f32_32x32x16_bf16 v[16:31], v[60:63], v[124:127], v[16:31]\n\t"
        "ds_read_b64_tr_b16 v[60:61], v196 offset:38912\n\t"
        "ds_read_b64_tr_b16 v[62:63], v196 offset:39936\n\t"
        "buffer_load_dwordx4 v[172:175], v192, s[40:43], s44 offen offset:3072 sc1\n\t"
        "s_waitcnt vmcnt(11) lgkmcnt(14)\n\t"
        "v_mfma_f32_32x32x16_bf16 v[0:15], v[32:35], v[128:131], v[0:15]\n\t"
        "ds_read_b64_tr_b16 v[32:33], v195 offset:40960\n\t"
        "ds_read_b64_tr_b16 v[34:35], v195 offset:41984\n\t"
        "s_waitcnt lgkmcnt(14)\n\t"
        "v_mfma_f32_32x32x16_bf16 v[16:31], v[36:39], v[128:131], v[16:31]\n\t"
        "ds_read_b64_tr_b16 v[36:37], v196 offset:40960\n\t"
        "ds_read_b64_tr_b16 v[38:39], v196 offset:41984\n\t"
        "s_add_u32 s44, s44, 0x1000\n\t"
        "s_and_b32 s44, s44, 0xffff\n\t"
        "buffer_load_dwordx4 v[176:179], v192, s[40:43], s44 offen offset:0 sc1\n\t"
        "s_waitcnt vmcnt(11) lgkmcnt(14)\n\t"
        "v_mfma_f32_32x32x16_bf16 v[0:15], v[40:43], v[132:135], v[0:15]\n\t"
        "ds_read_b64_tr_b16 v[40:41], v195 offset:43008\n\t"
        "ds_read_b64_tr_b16 v[42:43], v195 offset:44032\n\t"
        "s_waitcnt lgkmcnt(14)\n\t"
        "v_mfma_f32_32x32x16_bf16 v[16:31], v[44:47], v[132:135], v[16:31]\n\t"
        "ds_read_b64_tr_b16 v[44:45], v196 offset:43008\n\t"
        "ds_read_b64_tr_b16 v[46:47], v196 offset:44032\n\t"
        "buffer_load_dwordx4 v[180:183], v192, s[40:43], s44 offen offset:1024 sc1\n\t"
        "s_waitcnt vmcnt(11) lgkmcnt(14)\n\t"
        "v_mfma_f32_32x32x16_bf16 v[0:15], v[48:51], v[136:139], v[0:15]\n\t"
        "ds_read_b64_tr_b16 v[48:49], v195 offset:45056\n\t"
        "ds_read_b64_tr_b16 v[50:51], v195 offset:46080\n\t"
        "s_waitcnt lgkmcnt(14)\n\t"
        "v_mfma_f32_32x32x16_bf16 v[16:31], v[52:55], v[136:139], v[16:31]\n\t"
        "ds_read_b64_tr_b16 v[52:53], v196 offset:45056\n\t"
        "ds_read_b64_tr_b16 v[54:55], v196 offset:46080\n\t"
        "buffer_load_dwordx4 v[184:187], v192, s[40:43], s44 offen offset:2048 sc1\n\t"
        "s_waitcnt vmcnt(11) lgkmcnt(14)\n\t"
        "v_mfma_f32_32x32x16_bf16 v[0:15], v[56:59], v[140:143], v[0:15]\n\t"
        "ds_read_b64_tr_b16 v[56:57], v195 offset:47104\n\t"
        "ds_read_b64_tr_b16 v[58:59], v195 offset:48128\n\t"
        "s_waitcnt lgkmcnt(14)\n\t"
        "v_mfma_f32_32x32x16_bf16 v[16:31], v[60:63], v[140:143], v[16:31]\n\t"
        "ds_read_b64_tr_b16 v[60:61], v196 offset:47104\n\t"
        "ds_read_b64_tr_b16 v[62:63], v196 offset:48128\n\t"
        "buffer_load_dwordx4 v[188:191], v192, s[40:43], s44 offen offset:3072 sc1\n\t"
        "s_waitcnt vmcnt(11) lgkmcnt(14)\n\t"
        "v_mfma_f32_32x32x16_bf16 v[0:15], v[32:35], v[144:147], v[0:15]\n\t"
        "ds_read_b64_tr_b16 v[32:33], v195 offset:49152\n\t"
        "ds_read_b64_tr_b16 v[34:35], v195 offset:50176\n\t"
        "s_waitcnt lgkmcnt(14)\n\t"
        "v_mfma_f32_32x32x16_bf16 v[16:31], v[36:39], v[144:147], v[16:31]\n\t"
        "ds_read_b64_tr_b16 v[36:37], v196 offset:49152\n\t"
        "ds_read_b64_tr_b16 v[38:39], v196 offset:50176\n\t"
        "s_waitcnt vmcnt(10) lgkmcnt(14)\n\t"
        "v_mfma_f32_32x32x16_bf16 v[0:15], v[40:43], v[148:151], v[0:15]\n\t"
        "ds_read_b64_tr_b16 v[40:41], v195 offset:51200\n\t"
        "ds_read_b64_tr_b16 v[42:43], v195 offset:52224\n\t"
        "s_waitcnt lgkmcnt(14)\n\t"
        "v_mfma_f32_32x32x16_bf16 v[16:31], v[44:47], v[148:151], v[16:31]\n\t"
        "ds_read_b64_tr_b16 v[44:45], v196 offset:51200\n\t"
        "ds_read_b64_tr_b16 v[46:47], v196 offset:52224\n\t"
        "s_waitcnt vmcnt(9) lgkmcnt(14)\n\t"
        "v_mfma_f32_32x32x16_bf16 v[0:15], v[48:51], v[152:155], v[0:15]\n\t"
        "ds_read_b64_tr_b16 v[48:49], v195 offset:53248\n\t"
        "ds_read_b64_tr_b16 v[50:51], v195 offset:54272\n\t"
        "s_waitcnt lgkmcnt(14)\n\t"
        "v_mfma_f32_32x32x16_bf16 v[16:31], v[52:55], v[152:155], v[16:31]\n\t"
        "ds_read_b64_tr_b16 v[52:53], v196 offset:53248\n\t"
        "ds_read_b64_tr_b16 v[54:55], v196 offset:54272\n\t"
        "s_waitcnt vmcnt(8) lgkmcnt(14)\n\t"
        "v_mfma_f32_32x32x16_bf16 v[0:15], v[56:59], v[156:159], v[0:15]\n\t"
        "ds_read_b64_tr_b16 v[56:57], v195 offset:55296\n\t"
        "ds_read_b64_tr_b16 v[58:59], v195 offset:56320\n\t"
        "s_waitcnt lgkmcnt(14)\n\t"
        "v_mfma_f32_32x32x16_bf16 v[16:31], v[60:63], v[156:159], v[16:31]\n\t"
        "ds_read_b64_tr_b16 v[60:61], v196 offset:55296\n\t"
        "ds_read_b64_tr_b16 v[62:63], v196 offset:56320\n\t"
        "s_waitcnt vmcnt(7) lgkmcnt(14)\n\t"
        "v_mfma_f32_32x32x16_bf16 v[0:15], v[32:35], v[160:163], v[0:15]\n\t"
        "s_barrier\n\t"
        "ds_read_b64_tr_b16 v[32:33], v195 offset:57344\n\t"
        "ds_read_b64_tr_b16 v[34:35], v195 offset:58368\n\t"
        "s_waitcnt lgkmcnt(14)\n\t"
        "v_mfma_f32_32x32x16_bf16 v[16:31], v[36:39], v[160:163], v[16:31]\n\t"
        "ds_read_b64_tr_b16 v[36:37], v196 offset:57344\n\t"
        "ds_read_b64_tr_b16 v[38:39], v196 offset:58368\n\t"
        "s_waitcnt vmcnt(6) lgkmcnt(14)\n\t"
        "v_mfma_f32_32x32x16_bf16 v[0:15], v[40:43], v[164:167], v[0:15]\n\t"
        "ds_read_b64_tr_b16 v[40:41], v195 offset:59392\n\t"
        "ds_read_b64_tr_b16 v[42:43], v195 offset:60416\n\t"
        "s_waitcnt lgkmcnt(14)\n\t"
        "v_mfma_f32_32x32x16_bf16 v[16:31], v[44:47], v[164:167], v[16:31]\n\t"
        "ds_read_b64_tr_b16 v[44:45], v196 offset:59392\n\t"
        "ds_read_b64_tr_b16 v[46:47], v196 offset:60416\n\t"
        "s_waitcnt vmcnt(5) lgkmcnt(14)\n\t"
        "v_mfma_f32_32x32x16_bf16 v[0:15], v[48:51], v[168:171], v[0:15]\n\t"
        "ds_read_b64_tr_b16 v[48:49], v195 offset:61440\n\t"
        "ds_read_b64_tr_b16 v[50:51], v195 offset:62464\n\t"
        "s_waitcnt lgkmcnt(14)\n\t"
        "v_mfma_f32_32x32x16_bf16 v[16:31], v[52:55], v[168:171], v[16:31]\n\t"
        "ds_read_b64_tr_b16 v[52:53], v196 offset:61440\n\t"
        "ds_read_b64_tr_b16 v[54:55], v196 offset:62464\n\t"
        "s_waitcnt vmcnt(4) lgkmcnt(14)\n\t"
        "v_mfma_f32_32x32x16_bf16 v[0:15], v[56:59], v[172:175], v[0:15]\n\t"
        "ds_read_b64_tr_b16 v[56:57], v195 offset:63488\n\t"
        "ds_read_b64_tr_b16 v[58:59], v195 offset:64512\n\t"
        "s_waitcnt lgkmcnt(14)\n\t"
        "v_mfma_f32_32x32x16_bf16 v[16:31], v[60:63], v[172:175], v[16:31]\n\t"
        "ds_read_b64_tr_b16 v[60:61], v196 offset:63488\n\t"
        "ds_read_b64_tr_b16 v[62:63], v196 offset:64512\n\t"
        "s_waitcnt vmcnt(3) lgkmcnt(14)\n\t"
        "v_mfma_f32_32x32x16_bf16 v[0:15], v[32:35], v[176:179], v[0:15]\n\t"
        "s_waitcnt lgkmcnt(12)\n\t"
        "v_mfma_f32_32x32x16_bf16 v[16:31], v[36:39], v[176:179], v[16:31]\n\t"
        "s_waitcnt vmcnt(2) lgkmcnt(10)\n\t"
        "v_mfma_f32_32x32x16_bf16 v[0:15], v[40:43], v[180:183], v[0:15]\n\t"
        "s_waitcnt lgkmcnt(8)\n\t"
        "v_mfma_f32_32x32x16_bf16 v[16:31], v[44:47], v[180:183], v[16:31]\n\t"
        "s_waitcnt vmcnt(1) lgkmcnt(6)\n\t"
        "v_mfma_f32_32x32x16_bf16 v[0:15], v[48:51], v[184:187], v[0:15]\n\t"
        "s_waitcnt lgkmcnt(4)\n\t"
        "v_mfma_f32_32x32x16_bf16 v[16:31], v[52:55], v[184:187], v[16:31]\n\t"
        "s_waitcnt vmcnt(0) lgkmcnt(2)\n\t"
        "v_mfma_f32_32x32x16_bf16 v[0:15], v[56:59], v[188:191], v[0:15]\n\t"
        "s_waitcnt lgkmcnt(0)\n\t"
        "v_mfma_f32_32x32x16_bf16 v[16:31], v[60:63], v[188:191], v[16:31]\n\t"
        "s_nop 11\n\t"
        : "={v[0:15]}"(acc[0]), "={v[16:31]}"(acc[1]), "+{s44}"(soff) STAMP_OUTS
        : "{v192}"(OFF_OF + L * (5 * MBY) + tile * TS + lane * 16), "{v193}"(a0), "{v194}"(a1), "{v195}"(a0 + 65536), "{v196}"(a1 + 65536),
          "{v198}"(fl + r32 * 128), "{v199}"(TAG), "{s[40:43]}"(srd)
        : "v32", "v33", "v34", "v35", "v36", "v37", "v38", "v39", "v40", "v41", "v42", "v43", "v44", "v45", "v46", "v47", "v48", "v49", "v50", "v51", "v52", "v53", "v54", "v55", "v56", "v57", "v58", "v59", "v60", "v61", "v62", "v63", "v64", "v65", "v66", "v67", "v68", "v69", "v70", "v71", "v72", "v73", "v74", "v75", "v76", "v77", "v78", "v79", "v80", "v81", "v82", "v83", "v84", "v85", "v86", "v87", "v88", "v89", "v90", "v91", "v92", "v93", "v94", "v95", "v96", "v97", "v98", "v99", "v100", "v101", "v102", "v103", "v104", "v105", "v106", "v107", "v108", "v109", "v110", "v111", "v112", "v113", "v114", "v115", "v116", "v117", "v118", "v119", "v120", "v121", "v122", "v123", "v124", "v125", "v126", "v127", "v128", "v129", "v130", "v131", "v132", "v133", "v134", "v135", "v136", "v137", "v138", "v139", "v140", "v141", "v142", "v143", "v144", "v145", "v146", "v147", "v148", "v149", "v150", "v151", "v152", "v153", "v154", "v155", "v156", "v157", "v158", "v159", "v160", "v161", "v162", "v163", "v164", "v165", "v166", "v167", "v168", "v169", "v170", "v171", "v172", "v173", "v174", "v175", "v176", "v177", "v178", "v179", "v180", "v181", "v182", "v183", "v184", "v185", "v186", "v187", "v188", "v189", "v190", "v191", "v200", "s45", "vcc", "scc", "memory");
      }
      PHASE_VARS;
      STAMP_PUT(12 + 16 * L);
      STAMPV(15 + 16 * L);
      float s = 0.f, sq = 0.f, d = 0.f;
      const unsigned xdst = (L ? OOB : OFF_X1 + tile * TS + tn * 4096) + lane * 16;
#pragma unroll
      for (int g = 0; g < 8; ++g) {
#pragma unroll
        for (int e = 0; e < 2; ++e) {
          const unsigned rr = res[g >> 1][2 * (g & 1) + e];
          const unsigned t = pack2(acc[g >> 2][4 * (g & 3) + 2 * e], acc[g >> 2][4 * (g & 3) + 2 * e + 1]);
          x1p[g >> 1][2 * (g & 1) + e] = pack2(lo16(rr) + lo16(t), hi16(rr) + hi16(t));
        }
        if (g & 1) { stg(rs, xdst + (g >> 1) * 1024, x1p[g >> 1]); SBAR(); }
      }
#pragma unroll
      for (int g = 0; g < 8; ++g)
#pragma unroll
        for (int e = 0; e < 2; ++e) {
          const unsigned o = x1p[g >> 1][2 * (g & 1) + e];
          s = dot2(o, 0x3f803f80u, s); sq = dot2(o, o, sq); d = dot2(o, wrc[g][e], d);
        }
      pst = u32x4{nzf(half_swap_sum(s)), nzf(half_swap_sum(sq)), nzf(half_swap_sum(d)), 0u};
    }
    STAMPV(16 + 16 * L);
  }
  {
    PHASE_VARS;
    float S, Q, Dd, mean, var, mb1, rd1;
    stg(rs, stats_dst(OFF_SX + SX_LAYER, tile, tn, lane), pst);
    __builtin_amdgcn_s_sleep(10);
    row_stats_sum(rs, OFF_SX + SX_LAYER, tile, lane, S, Q, Dd);
    STAMPV(33);
    row_stats_fin(smem, 2, tile, lane, S, Q, Dd, U2F(words[0]), x1p, mean, var, mb1, rd1);
    STAMPV(34);
    u32x4 xo[4];
#pragma unroll
    for (int i = 0; i < 4; ++i) {
#pragma unroll
      for (int e = 0; e < 4; ++e) {
        const unsigned h2 = ln_pair(x1p[i][e], mb1, rd1);
        xo[i][e] = pack2(lo16(x1p[i][e]) + lo16(h2), hi16(x1p[i][e]) + hi16(h2));
      }
      *reinterpret_cast<u32x4*>(smem + wid * 4096 + r32 * 128 + ((2 * i + hi) ^ (r32 & 7)) * 16) = fo_swap(xo[i]);
    }
    asm volatile("s_waitcnt lgkmcnt(0)" ::: "memory");
#pragma unroll
    for (int i = 0; i < 4; ++i) { xo[i] = *reinterpret_cast<const u32x4*>(smem + wid * 4096 + i * 1024 + (lane >> 3) * 128 + ((lane ^ (lane >> 3)) & 7) * 16); PIN(xo[i]); }
    u16* dst = out + (size_t)(tile * 32 + (lane >> 3)) * D_ + tn * 64 + (lane & 7) * 8;
    lds_wait_ge(lbase + L_W + 4 + 4 * (lane & 1), 2);
    const int rel = (int)words[3] - tile * 32 - (lane >> 3);
#pragma unroll
    for (int i = 0; i < 4; ++i)
      if (rel != 8 * i) st16(dst + (size_t)i * 8 * D_, xo[i]);
    STAMPV(35);
    STAMP_END;
  }
}

extern "C" __global__ __launch_bounds__(512) void prep_kernel(const u16* __restrict__ x0, const u16* __restrict__ wqkv, char* __restrict__ ws) {
  __shared__ __attribute__((aligned(16))) char sm[128 * 144];
  const int tid = threadIdx.x, lane = tid & 63, wid = __builtin_amdgcn_readfirstlane(tid >> 6), b = blockIdx.x;
  if (b >= 256) {
    const int xcd = b & 7, slot = (b - 256) >> 3;
    if (tid < (int)(FL_COUNT / 256)) { u32x4 z = {0u, 0u, 0u, 0u}; st16(ws + OFF_FL + ((b - 256) * (FL_COUNT / 256) + tid) * 128, z); }
    unsigned acc = 0;
    {
      const u16* p = wqkv + (size_t)(slot * 32 + (tid >> 5)) * (3 * D_) + ((xcd & 3) * 4 + ((tid >> 3) & 3)) * 64 + (tid & 7) * 8;
#pragma unroll
      for (int i = 0; i < 6; ++i) {
        const u32x4 v = *reinterpret_cast<const u32x4*>(p + (size_t)(i & 1) * 16 * (3 * D_) + (i >> 1) * D_);
        acc |= v[0] ^ v[3];
      }
    }
    if (slot < 8) {
      const char *c0, *c1;
      CODE_ADDR(mega_kernel, c0); CODE_ADDR(prep_kernel, c1);
      const long long o = (slot * 512 + tid) * 16;
      if (o + 16 <= c1 - c0) acc |= (*reinterpret_cast<const __attribute__((address_space(1))) u32x4*>((uintptr_t)(c0 + o)))[0];
    }
    asm volatile("" :: "v"(acc));
    return;
  }
  u32x4 v[2];
  asm volatile("global_load_dwordx4 %0, %2, off\n\tglobal_load_dwordx4 %1, %2, off offset:1024"
               : "=&v"(v[0]), "=&v"(v[1]) : "v"(x0 + (size_t)(b * 8 + wid) * D_ + lane * 8) : "memory");
  int pass;
  asm volatile("s_mov_b32 %0, %1" : "=s"(pass) : "n"(0));
#pragma unroll 1
  for (;;) {
    asm volatile("s_cmp_eq_u32 %2, 0\n\ts_cbranch_scc1 1f\n\ts_waitcnt vmcnt(0)\n1:" : "+v"(v[0]), "+v"(v[1]) : "s"(pass) : "memory", "scc");
    float s = 0.f;
#pragma unroll
    for (int i = 0; i < 2; ++i)
#pragma unroll
      for (int e = 0; e < 4; ++e) s = dot2(v[i][e], 0x3f803f80u, s);
    const float mean = wave_sum(s) * (1.0f / 1024.0f);
    float m2 = 0.f;
#pragma unroll
    for (int i = 0; i < 2; ++i)
#pragma unroll
      for (int e = 0; e < 4; ++e) { const float a = lo16(v[i][e]) - mean, c = hi16(v[i][e]) - mean; m2 += a * a + c * c; }
    float mb, den;
    ln_scalars(mean, wave_sum(m2) * (1.0f / 1024.0f), mb, den);
    const float rd = 1.0f / den;
#pragma unroll
    for (int i = 0; i < 2; ++i) {
      char* p = sm + ((32 * i + (lane >> 1)) * 2) * 144 + wid * 16 + (lane & 1) * 8;
      u32x2 lo = {ln_pair(v[i][0], mb, rd), ln_pair(v[i][1], mb, rd)}, hi = {ln_pair(v[i][2], mb, rd), ln_pair(v[i][3], mb, rd)};
      *reinterpret_cast<u32x2*>(p) = lo;
      *reinterpret_cast<u32x2*>(p + 144) = hi;
    }
    if (pass) break;
    asm volatile("s_mov_b32 %0, 1" : "=s"(pass));
  }
  __syncthreads();
  char* dst = ws + OFF_HF + (size_t)(b >> 2) * TS + (b & 3) * 128;
#pragma unroll
  for (int i = 0; i < 2; ++i) {
    const int q = i * 512 + tid, pc = q >> 3, tk = q & 7;
    st16(dst + pc * 512 + tk * 16, *reinterpret_cast<const u32x4*>(sm + pc * 144 + tk * 16));
  }
}

extern "C" void kernel_launch(void* const* d_in, const int* in_sizes, int n_in, void* d_out, int out_size,
                              void* d_ws, size_t ws_size, hipStream_t stream) {
  char* ws = (char*)d_ws;
  prep_kernel<<<512, 512, 0, stream>>>((const u16*)d_in[0], (const u16*)d_in[1], ws);
  mega_kernel<<<256, 512, 0, stream>>>((const u16*)d_in[0], (const u16*)d_in[1], (const u16*)d_in[2], (const u16*)d_in[4], (const u16*)d_in[5],
                                       (const u16*)d_in[6], (const u16*)d_in[7], ws, (u16*)d_out);
}
```

```cpp
#include <hip/hip_runtime.h>
#include <hip/hip_bf16.h>
#include <stdint.h>

typedef unsigned short u16;
typedef short s16x4 __attribute__((ext_vector_type(4)));
typedef short s16x8 __attribute__((ext_vector_type(8)));
typedef float f32x16 __attribute__((ext_vector_type(16)));
typedef float f32x8 __attribute__((ext_vector_type(8)));
typedef float f32x4 __attribute__((ext_vector_type(4)));
typedef float f32x2 __attribute__((ext_vector_type(2)));
typedef unsigned int u32x16 __attribute__((ext_vector_type(16)));
typedef unsigned int u32x8 __attribute__((ext_vector_type(8)));
typedef unsigned int u32x4 __attribute__((ext_vector_type(4)));
typedef unsigned int u32x2 __attribute__((ext_vector_type(2)));
typedef __bf16 bf16x2 __attribute__((ext_vector_type(2)));
typedef __bf16 bf16x8 __attribute__((ext_vector_type(8)));
typedef __bf16 bf16x16 __attribute__((ext_vector_type(16)));

#define DI __device__ __forceinline__
#define LDS_PTR(T, p) ((__attribute__((address_space(3))) T*)(p))
#define BARRIER() asm volatile("s_barrier" ::: "memory")
#define BAR_LGKM() asm volatile("s_waitcnt lgkmcnt(0)\n\ts_barrier" ::: "memory")
#define BAR_ALL() asm volatile("s_waitcnt vmcnt(0) lgkmcnt(0)\n\ts_barrier" ::: "memory")
#define DRAIN() asm volatile("s_waitcnt vmcnt(0)" ::: "memory")
#define PACE() ({ asm volatile("s_sleep 1\n\ts_waitcnt vmcnt(3)" ::: "memory"); })
#define MFMA32(a, b, c) __builtin_amdgcn_mfma_f32_32x32x16_bf16(a, b, c, 0, 0, 0)
#define MFMA16(a, b, c) __builtin_amdgcn_mfma_f32_16x16x32_bf16(a, b, c, 0, 0, 0)
#define PIN(x) asm volatile("" : "+v"(x))
#define EX2(x) __builtin_amdgcn_exp2f(x)
#define RCP(x) __builtin_amdgcn_rcpf(x)
#define SBAR() __builtin_amdgcn_sched_barrier(0)

#define STAMP_INIT
#define STAMPV(i)
#define STAMP_END
#define STAMP_DECL
#define STAMP_OUTS
#define STAMP_PUT(i)
constexpr int D_ = 1024, F_ = 3584;

constexpr unsigned MBY = 1u << 20;
constexpr unsigned TS = 65536 + 4096, HS = 262144 + 4096;
constexpr unsigned OFF_HF = 0, OFF_X1 = 5 * MBY;
constexpr unsigned OFF_OF = 10 * MBY;
constexpr unsigned OFF_KV = 20 * MBY;
constexpr unsigned OFF_FL = 40 * MBY;
constexpr unsigned FL_KV = OFF_FL;
constexpr unsigned FL_O = OFF_FL + 65536;
constexpr unsigned FL_COUNT = 1024;
constexpr unsigned OFF_CS = 41 * MBY;
constexpr unsigned OFF_SX = 42 * MBY;
constexpr unsigned SX_ST = 0;
constexpr unsigned SX_ROW = 512 * 1024;
constexpr unsigned SX_CAND = SX_ROW + 32 * 1024;
constexpr unsigned SX_GP = SX_CAND + 32 * 1024;
constexpr unsigned SX_YP = SX_GP + 224 * 1024;
constexpr unsigned SX_LAYER = SX_YP + 224 * 1024;
constexpr unsigned SX_QP = 2 * SX_LAYER;
constexpr unsigned SX_BYTES = SX_QP + 512 * 1024;
static_assert(SX_BYTES <= 5 * 256 * 128 * 16, "sentinel fill");
constexpr unsigned WS_BYTES = 48 * MBY, OOB = 0x80000000u;
constexpr unsigned SENT = 0xFFFFFFFFu, TAG = 1u;

constexpr int NS = 9, DD = 3, SLOTB = 12288;
constexpr int L_HB = 110592, L_XB = L_HB + 2048;
constexpr int L_XCH = 122880;
constexpr int L_CAND = L_XCH + 32768;
constexpr int L_AB = L_XCH + 34816;
constexpr int L_FULL = 159744, L_FREE = L_FULL + 512;
constexpr int L_CS = L_FREE + 1024, L_PATCH = L_CS + 768;
constexpr int L_GU = L_PATCH + 768, L_YS = L_GU + 256, L_WR = L_YS + 256;
constexpr int L_X2F = L_WR + 128, L_X2P = L_X2F + 128;
constexpr int L_W = L_X2P + 128;
constexpr int LDS_BYTES = 163840;
static_assert(NS * SLOTB <= L_HB && L_W + 64 <= LDS_BYTES && L_AB + 2048 <= L_FULL, "LDS map");

DI int fresh_lane() { int l; asm volatile("v_mbcnt_lo_u32_b32 %0, -1, 0\n\tv_mbcnt_hi_u32_b32 %0, -1, %0" : "=v"(l)); return l; }
#define LANE_VARS const int lane = fresh_lane(), r32 = lane & 31, hi = lane >> 5, tq = (lane & 15) >> 2, tp = lane & 3, tblk = (lane >> 4) & 1, \
  wrow = 4 * hi + tq, wsw = 4 * ((wrow >> 1) & 1), wch = 2 * tblk + (tp >> 1), c24 = r32 < 24 ? r32 : 23; \
  (void)r32; (void)hi; (void)wsw; (void)wch; (void)c24
DI int fresh_s(int v) { asm volatile("" : "+s"(v)); return v; }
#define PHASE_VARS const int tm = fresh_s(tm_), tn = fresh_s(tn_), wid = fresh_s(wid_), tile = tm * 4 + wid, hh = wid & 1, kh = wid >> 1; (void)tile; (void)hh; (void)kh; LANE_VARS
DI float bf2f(u16 v) { return __uint_as_float(((unsigned)v) << 16); }
DI unsigned pack2(float lo, float hi) { f32x2 v = {lo, hi}; return __builtin_bit_cast(unsigned, __builtin_convertvector(v, bf16x2)); }
DI float rbf(float f) { return __uint_as_float(pack2(f, 0.f) << 16); }
DI float lo16(unsigned u) { return __uint_as_float(u << 16); }
DI float hi16(unsigned u) { return __uint_as_float(u & 0xffff0000u); }
DI float opq(float r) { asm("" : "+v"(r)); return r; }
DI float fadd(float a, float b) { return opq(a + b); }
DI float fmul(float a, float b) { return opq(a * b); }
DI float ffma(float a, float b, float c) { return opq(__builtin_fmaf(a, b, c)); }
DI float U2F(unsigned u) { return __uint_as_float(u); }
DI float half_swap_sum(float v) {
  auto r = __builtin_amdgcn_permlane32_swap(__float_as_uint(v), __float_as_uint(v), false, false);
  return fadd(U2F(r[0]), U2F(r[1]));
}
DI float half_swap_max(float v) {
  auto r = __builtin_amdgcn_permlane32_swap(__float_as_uint(v), __float_as_uint(v), false, false);
  return fmaxf(__uint_as_float(r[0]), __uint_as_float(r[1]));
}
template <int CTRL> DI unsigned dpp(unsigned v) { return (unsigned)__builtin_amdgcn_update_dpp(0, (int)v, CTRL, 0xf, 0xf, false); }
DI float row_swap_sum(float v) {
  auto r = __builtin_amdgcn_permlane16_swap(__float_as_uint(v), __float_as_uint(v), false, false);
  return fadd(U2F(r[0]), U2F(r[1]));
}
DI float half_sum(float v) {
  v = fadd(v, U2F(dpp<0xB1>(__float_as_uint(v)))); v = fadd(v, U2F(dpp<0x4E>(__float_as_uint(v))));
  v = fadd(v, U2F(dpp<0x141>(__float_as_uint(v)))); v = fadd(v, U2F(dpp<0x140>(__float_as_uint(v))));
  return row_swap_sum(v);
}
DI float wave_sum(float v) { return half_swap_sum(half_sum(v)); }
DI unsigned half_max_u32(unsigned v) {
  v = max(v, dpp<0xB1>(v)); v = max(v, dpp<0x4E>(v)); v = max(v, dpp<0x141>(v)); v = max(v, dpp<0x140>(v));
  auto r = __builtin_amdgcn_permlane16_swap(v, v, false, false);
  return max(r[0], r[1]);
}
DI unsigned wave_max_u32(unsigned v) {
  v = half_max_u32(v);
  auto r = __builtin_amdgcn_permlane32_swap(v, v, false, false);
  return max(r[0], r[1]);
}
DI u32x4 fo_swap(u32x4 v) {
  auto a = __builtin_amdgcn_permlane32_swap(v[0], v[2], false, false);
  auto b = __builtin_amdgcn_permlane32_swap(v[1], v[3], false, false);
  u32x4 o; o[0] = a[0]; o[1] = b[0]; o[2] = a[1]; o[3] = b[1];
  return o;
}
DI void glds16(const void* gsrc, unsigned lds_dst) {
  unsigned keep;
  asm volatile("s_mov_b32 %0, m0\n\ts_mov_b32 m0, %2\n\ts_nop 0\n\tglobal_load_lds_dwordx4 %1, off\n\ts_mov_b32 m0, %0"
               : "=&s"(keep) : "v"(gsrc), "s"(lds_dst) : "memory");
}
#define GL_ "global_load_lds_dwordx4 "
DI void glds_v4(const void* sb, const unsigned (&v)[4], unsigned m0v) {
  unsigned keep;
  asm volatile("s_mov_b32 %0, m0\n\ts_mov_b32 m0, %6\n\ts_nop 0\n\t" GL_ "%1, %5\n\t" GL_ "%2, %5 offset:1024\n\t" GL_ "%3, %5 offset:2048\n\t"
               GL_ "%4, %5 offset:3072\n\ts_mov_b32 m0, %0" : "=&s"(keep) : "v"(v[0]), "v"(v[1]), "v"(v[2]), "v"(v[3]), "s"(sb), "s"(m0v) : "memory");
}
#define GP_ "\n\ts_sleep %8\n\ts_waitcnt vmcnt(%7)\n\t"
template <int TH, int SL> DI void glds_v4t(const void* sb, const unsigned (&v)[4], unsigned m0v) {
  unsigned keep;
  asm volatile("s_mov_b32 %0, m0\n\ts_mov_b32 m0, %6\n\ts_nop 0\n\t" GL_ "%1, %5" GP_ GL_ "%2, %5 offset:1024" GP_ GL_ "%3, %5 offset:2048" GP_ GL_ "%4, %5 offset:3072" GP_ "s_mov_b32 m0, %0"
               : "=&s"(keep) : "v"(v[0]), "v"(v[1]), "v"(v[2]), "v"(v[3]), "s"(sb), "s"(m0v), "n"(TH), "n"(SL) : "memory");
}
DI void glds_v3(const void* sb, const unsigned (&v)[3], unsigned m0v) {
  unsigned keep;
  asm volatile("s_mov_b32 %0, m0\n\ts_mov_b32 m0, %5\n\ts_nop 0\n\t" GL_ "%1, %4\n\t" GL_ "%2, %4 offset:1024\n\t" GL_ "%3, %4 offset:2048\n\t"
               "s_mov_b32 m0, %0" : "=&s"(keep) : "v"(v[0]), "v"(v[1]), "v"(v[2]), "s"(sb), "s"(m0v) : "memory");
}
DI float dot2(unsigned a, unsigned b, float c) {
  return __builtin_amdgcn_fdot2_f32_bf16(__builtin_bit_cast(bf16x2, a), __builtin_bit_cast(bf16x2, b), c, false);
}
#define CODE_ADDR(sym, out) do { unsigned long long pc_; unsigned off_; \
  asm volatile("s_getpc_b64 %0\n1:\n\ts_mov_b32 %1, " #sym "-1b" : "=s"(pc_), "=s"(off_)); out = (const char*)(pc_ + (long long)(int)off_); } while (0)
DI void st16(void* p, u32x4 v) { asm volatile("global_store_dwordx4 %0, %1, off sc0 sc1\n\ts_nop 1" :: "v"(p), "v"(v) : "memory"); }
DI void ln_scalars(float mean, float var, float& mb, float& den) { mb = rbf(mean); den = rbf(__builtin_amdgcn_sqrtf(rbf(rbf(var) + 1e-5f))); }
DI unsigned ln_pair(unsigned v, float mb, float rd) {
  const unsigned c = pack2(lo16(v) - mb, hi16(v) - mb);
  return pack2(lo16(c) * rd, hi16(c) * rd);
}
DI unsigned ln_pair_s(unsigned v, float nmb, float rd) {
  const unsigned c = pack2(fadd(lo16(v), nmb), fadd(hi16(v), nmb));
  return pack2(fmul(lo16(c), rd), fmul(hi16(c), rd));
}
template <int N, int M> DI f32x4 sum4(const u32x4 (&v)[M]) {
  f32x4 t;
#pragma unroll
  for (int e = 0; e < 4; ++e) {
    t[e] = U2F(v[0][e]);
#pragma unroll
    for (int i = 1; i < N; ++i) t[e] = fadd(t[e], U2F(v[i][e]));
  }
  return t;
}
#define PERM_LO(x, y) __builtin_amdgcn_perm(y, x, 0x05040100u)
#define PERM_HI(x, y) __builtin_amdgcn_perm(y, x, 0x07060302u)
constexpr unsigned SPIN_CAP = 1u << 15;
DI unsigned nz(unsigned v) { return min(v, SENT - 1u); }
DI unsigned nzf(float v) { return nz(__float_as_uint(v)); }
DI unsigned mx4(unsigned m, const u32x4& v) { return max(max(max(m, v[0]), v[1]), max(v[2], v[3])); }
template <int N> DI bool none_sent(const u32x4 (&v)[N]) {
  unsigned m = 0u;
#pragma unroll
  for (int i = 0; i < N; ++i) m = mx4(m, v[i]);
  return m != SENT;
}
typedef __amdgpu_buffer_rsrc_t rsrc_t;
DI u32x4 ldg(rsrc_t rs, unsigned off) { return __builtin_amdgcn_raw_buffer_load_b128(rs, (int)off, 0, 16); }
DI void stg(rsrc_t rs, unsigned off, u32x4 v) { __builtin_amdgcn_raw_buffer_store_b128(v, rs, (int)off, 0, 16); }
template <int N, int SLEEP = 1, class LD, class OKF> DI void poll_all(u32x4 (&out)[N], LD ld, OKF okf) {
  for (unsigned spins = 0;; ++spins) {
    ld(out);
    SBAR();
    if (__all(okf(out)) || spins > SPIN_CAP) break;
    asm volatile("s_sleep %0" :: "n"(SLEEP) : "memory");
  }
}
DI void lds_set(unsigned addr, unsigned v) { asm volatile("s_waitcnt lgkmcnt(0)\n\tds_write_b32 %0, %1" :: "v"(addr), "v"(v) : "memory"); }
template <int SLEEP = 1> DI void lds_wait_ge(unsigned addr, unsigned need) {
  unsigned spin = 0x100000, v;
  asm volatile("1:\n\tds_read_b32 %0, %2\n\ts_sub_u32 %1, %1, 1\n\ts_waitcnt lgkmcnt(0)\n\ts_cbranch_scc1 2f\n\tv_cmp_gt_u32 vcc, %3, %0\n\ts_cbranch_vccz 2f\n\ts_sleep %4\n\ts_branch 1b\n2:"
               : "=&v"(v), "+s"(spin) : "v"(addr), "s"(need), "n"(SLEEP) : "vcc", "scc", "memory");
}
DI s16x8 tr2(const char* p, int second) {
  s16x4 lo = __builtin_amdgcn_ds_read_tr16_b64_v4i16(LDS_PTR(s16x4, p)), hi = __builtin_amdgcn_ds_read_tr16_b64_v4i16(LDS_PTR(s16x4, p + second));
  return __builtin_shufflevector(lo, hi, 0, 1, 2, 3, 4, 5, 6, 7);
}
DI u32x16 cat4(const u32x4& a, const u32x4& b, const u32x4& c, const u32x4& d) {
  const u32x8 x = __builtin_shufflevector(a, b, 0, 1, 2, 3, 4, 5, 6, 7), y = __builtin_shufflevector(c, d, 0, 1, 2, 3, 4, 5, 6, 7);
  return __builtin_shufflevector(x, y, 0, 1, 2, 3, 4, 5, 6, 7, 8, 9, 10, 11, 12, 13, 14, 15);
}

DI unsigned stats_dst(unsigned sx, int tile, int tn, int lane) { return lane < 32 ? sx + SX_ST + tile * 8192 + tn * 512 + lane * 16 : OOB; }
DI void row_stats_sum(rsrc_t rs, unsigned sx, int tile, int lane, float& S, float& Q, float& Dd) {
  const unsigned exb = sx + SX_ST + tile * 8192 + (lane & 31) * 16;
  u32x4 pv[8];
  poll_all<8>(pv, [&](u32x4 (&v)[8]) {
#pragma unroll
    for (int i = 0; i < 8; ++i) v[i] = ldg(rs, exb + ((lane >> 5) * 8 + i) * 512);
  }, [&](const u32x4 (&v)[8]) { return none_sent(v); });
  S = 0.f; Q = 0.f; Dd = 0.f;
#pragma unroll
  for (int i = 0; i < 8; ++i) { S += __uint_as_float(pv[i][0]); Q += __uint_as_float(pv[i][1]); Dd += __uint_as_float(pv[i][2]); }
}
DI void row_stats_fin(char* smem, int epoch, int tile, int lane, float S, float Q, float Dd, float wsum, const u32x4 (&x1p)[4],
                      float& mean, float& var, float& mb, float& rd) {
  S = half_swap_sum(S); Q = half_swap_sum(Q); Dd = half_swap_sum(Dd);
  mean = S * (1.0f / 1024.0f); var = fmaxf(Q * (1.0f / 1024.0f) - mean * mean, 0.f);
  float den;
  ln_scalars(mean, var, mb, den);
  rd = RCP(den);
  const unsigned rb = pack2((Dd - mb * wsum) * rd, 0.f) & 0xffffu;
  const unsigned mine = (((rb & 0x8000u) ? (~rb & 0xffffu) : (rb | 0x8000u)) << 16) | (unsigned)(2047 - (tile * 32 + (lane & 31)));
  const bool best = half_max_u32(mine) == mine;
  if (best) {
    char* c = smem + L_CAND + (tile & 3) * 144;
#pragma unroll
    for (int i = 0; i < 4; ++i) *reinterpret_cast<u32x4*>(c + (2 * i + (lane >> 5)) * 16) = x1p[i];
    *reinterpret_cast<u32x4*>(c + 128) = u32x4{mine, __float_as_uint(mb), __float_as_uint(rd), 0u};
  }
  lds_set((unsigned)(uintptr_t)smem + L_W + 32 + (tile & 3) * 4, epoch);
}

DI void attn_safe(rsrc_t rs, unsigned kvoff, const u32x8& q, const u32x4& ones, f32x16& o) {
  const s16x8 q0 = __builtin_bit_cast(s16x8, __builtin_shufflevector(q, q, 0, 1, 2, 3)), q1 = __builtin_bit_cast(s16x8, __builtin_shufflevector(q, q, 4, 5, 6, 7));
  f32x4 l = {0.f, 0.f, 0.f, 0.f};
#pragma unroll
  for (int e = 0; e < 16; ++e) o[e] = 0.f;
  float m = -INFINITY;
#pragma unroll 1
  for (int kt = 0; kt < 64; ++kt) {
    const unsigned p = kvoff + kt * 4096;
    const u32x4 k0 = ldg(rs, p), k1 = ldg(rs, p + 1024), v0 = ldg(rs, p + 2048), v1 = ldg(rs, p + 3072);
    f32x16 s = {};
    s = MFMA32(__builtin_bit_cast(s16x8, k0), q0, s);
    s = MFMA32(__builtin_bit_cast(s16x8, k1), q1, s);
    float mt = fmaxf(s[0], s[1]);
#pragma unroll
    for (int e = 2; e < 16; ++e) mt = fmaxf(mt, s[e]);
    const float mn = fmaxf(m, half_swap_max(mt));
    const float alpha = EX2(m - mn);
    m = mn;
#pragma unroll
    for (int e = 0; e < 16; ++e) { s[e] = EX2(s[e] - mn); o[e] *= alpha; }
    l[0] *= alpha;
    const bf16x16 pa = __builtin_bit_cast(bf16x16, __builtin_shufflevector(s, s, 0, 1, 2, 3, 4, 5, 6, 7));
    const bf16x16 pb = __builtin_bit_cast(bf16x16, __builtin_shufflevector(s, s, 8, 9, 10, 11, 12, 13, 14, 15));
    o = __builtin_amdgcn_smfmac_f32_32x32x32_bf16(__builtin_bit_cast(bf16x8, v0), pa, o, 0xdddd, 0, 0);
    l = __builtin_amdgcn_smfmac_f32_16x16x64_bf16(__builtin_bit_cast(bf16x8, ones), pa, l, 0xdddd, 0, 0);
    o = __builtin_amdgcn_smfmac_f32_32x32x32_bf16(__builtin_bit_cast(bf16x8, v1), pb, o, 0xdddd, 0, 0);
    l = __builtin_amdgcn_smfmac_f32_16x16x64_bf16(__builtin_bit_cast(bf16x8, ones), pb, l, 0xdddd, 0, 0);
  }
  o *= 1.0f / l[0];
}

extern "C" __global__ __launch_bounds__(512) void mega_kernel(const u16* __restrict__ x0, const u16* __restrict__ wqkv, const u16* __restrict__ wo,
                                                              const u16* __restrict__ w_r, const u16* __restrict__ wg, const u16* __restrict__ wu,
                                                              const u16* __restrict__ wd, char* ws, u16* __restrict__ out) {
  __shared__ __attribute__((aligned(16))) char smem[LDS_BYTES];
  const int wid = __builtin_amdgcn_readfirstlane(threadIdx.x >> 6);
  STAMP_INIT;
  STAMPV(0);
  const int bid = blockIdx.x, xcd = bid & 7, loc = bid >> 3;
  const int tm = 8 * (xcd >> 2) + (loc >> 2), tn = 4 * (xcd & 3) + (loc & 3);
  const unsigned lbase = (unsigned)(uintptr_t)smem;
  const rsrc_t rs = __builtin_amdgcn_make_buffer_rsrc(ws, 0, (int)WS_BYTES, 0x00020000);
  volatile __attribute__((address_space(3))) unsigned* words = LDS_PTR(volatile unsigned, smem + L_W);
  if (wid >= 4) {
    auto wo_rows = [&](int g0, int n, int gs) {
      const int lane = fresh_lane();
      unsigned wov[4];
#pragma unroll
      for (int i = 0; i < 4; ++i) wov[i] = (unsigned)((8 * i + (lane >> 3)) * (D_ * 2) + ((lane & 7) ^ (4 * ((lane >> 4) & 1))) * 16 - i * 1024);
#pragma unroll 1
      for (int i = 0, k = (2 * (tm & 7)) % n, g; i < n && !0; ++i, k = k + 1 == n ? 0 : k + 1) g = g0 + gs * k, glds_v4t<3, 1>(reinterpret_cast<const char*>(wo) + (size_t)((64 * tn + 32 * g) & 1023) * (D_ * 2) + tn * 128, wov, lbase + g * 4096);
    };
    if (wid < 6) {
      const int lw = wid - 4, lane = fresh_lane();
      __builtin_amdgcn_s_setprio(3);
      const char* wb = reinterpret_cast<const char*>(wqkv + (size_t)(16 * lw) * (3 * D_) + tn * 64);
      unsigned wv[3];
#pragma unroll
      for (int i = 0; i < 3; ++i) {
        const int li = lane + 64 * i, k = li / 24, cp = li % 24, c = cp ^ (4 * ((k >> 1) & 1));
        wv[i] = (unsigned)(k * (3 * D_ * 2) + (c >> 3) * (D_ * 2) + (c & 7) * 16 - i * 1024);
      }
      auto stage = [&](int s, int slot) {
        const char* p = wb + (size_t)((s + 2 * tn) & 31) * (32 * 3 * D_ * 2);
        glds_v3(p, wv, lbase + slot * SLOTB + lw * 6144);
        glds_v3(p + 8 * (3 * D_ * 2), wv, lbase + slot * SLOTB + lw * 6144 + 3072);
      };
      const unsigned fra = lbase + L_FREE + (lane & 3) * 256, fla = lbase + L_FULL + lw * 256 + lane * 4;
      auto exchange = [&](unsigned line0, int me) {
        __builtin_amdgcn_raw_buffer_store_b32(TAG, rs, lane == 0 ? line0 + me * 128 : OOB, 0, 16);
        __builtin_amdgcn_s_sleep(8);
        for (unsigned spins = 0;; ++spins) {
          const unsigned v = __builtin_amdgcn_raw_buffer_load_b32(rs, line0 + (lane & 15) * 128, 0, 16);
          if (__all(v == TAG) || spins > SPIN_CAP) break;
          asm volatile("s_sleep 1" ::: "memory");
        }
      };
      const rsrc_t rw = __builtin_amdgcn_make_buffer_rsrc(const_cast<char*>(wb), 0, 0 ? 0 : 6 * MBY - (16 * lw * 3 * D_ + tn * 64) * 2, 0x00020000);
#pragma unroll 1
      for (int L = 0; L < 2; ++L) {
        const int s0 = L ? NS : 0, lead = L ? 8 : NS - 1;
        int slot = 0;
#pragma unroll 1
        for (int s = s0; s < 32; ++s) {
          if (s >= NS) lds_wait_ge(fra, 32 * L + s - lead);
          stage(s, slot);
          if (s == 0) BARRIER();
          slot = (slot == NS - 1) ? 0 : slot + 1;
          if (s >= s0 + DD) asm volatile("s_waitcnt vmcnt(%0)\n\tds_write_b32 %1, %2" :: "n"(DD * 6), "v"(fla), "v"(32 * L + s - DD + 1) : "memory");
        }
#pragma unroll
        for (int d = DD - 1; d >= 0; --d) asm volatile("s_waitcnt vmcnt(%0)\n\tds_write_b32 %1, %2" :: "n"(d * 6), "v"(fla), "v"(32 * L + 32 - d) : "memory");
        BAR_LGKM();
        if (lw == 0) {
          exchange(FL_KV + (L * 16 + tn) * 2048, tm);
          lds_wait_ge(fra, 32 * L + 32);
          lds_set(lbase + L_W + 16, L + 1);
        } else lds_wait_ge<8>(lbase + L_W + 16, L + 1);
        __builtin_amdgcn_s_sleep(24);
        wo_rows(lw, 15, 2);
        u32x4 pre[NS * 6];
        if (L == 0) {
#pragma unroll
          for (int s = 0; s < NS; ++s) {
#pragma unroll
            for (int i = 0; i < 6; ++i)
              pre[s * 6 + i] = __builtin_amdgcn_raw_buffer_load_b128(rw, wv[i % 3], ((s + 2 * tn) & 31) * (32 * 3 * D_ * 2) + (i / 3) * (8 * 3 * D_ * 2) + (i % 3) * 1024, 0), PACE();
          }
        } else {
#pragma unroll
          for (int i = 0; i < NS * 6; ++i) asm volatile("" : "=v"(pre[i]));
        }
        BAR_ALL();
        if (lw == 0) { exchange(FL_O + (L * 16 + tm) * 2048, tn); lds_set(lbase + L_W + 20, L + 1); }
        else wo_rows(30, 2, 1);
        BAR_ALL();
        if (L == 0) {
          char* d = smem + lw * 6144 + fresh_lane() * 16;
#pragma unroll
          for (int s = 0; s < NS; ++s) {
#pragma unroll
            for (int i = 0; i < 6; ++i) *reinterpret_cast<u32x4*>(d + s * SLOTB + i * 1024) = pre[s * 6 + i];
            lds_set(fla, 32 + s + 1);
          }
        }
      }
      return;
    }
    const int a = wid - 6;
    const bool ffn = loc < 28;
    const int grp = xcd * 7 + (ffn ? loc >> 2 : 0), qa = (loc & 3) * 2 + a;
    const int kr = loc, cg = xcd;
    const unsigned wa = lbase + L_W;
    BARRIER();
    auto transpose8 = [](u32x4* v) {
      u32x4 t[8];
#pragma unroll
      for (int j = 0; j < 8; ++j)
#pragma unroll
        for (int e = 0; e < 4; ++e) t[j][e] = (j & 1) ? PERM_HI(v[2 * e][j >> 1], v[2 * e + 1][j >> 1]) : PERM_LO(v[2 * e][j >> 1], v[2 * e + 1][j >> 1]);
#pragma unroll
      for (int j = 0; j < 8; ++j) { v[j] = t[j]; PIN(v[j]); }
    };
    u32x4 gf[32];
    u32x4 wdf[16];
    auto wait_go = [&](int L) {
      lds_wait_ge<8>(lbase + L_W + 16, L + 1);
      __builtin_amdgcn_s_sleep(24);
    };
    auto load_wd = [&](unsigned dv) {
      const rsrc_t rd = __builtin_amdgcn_make_buffer_rsrc(const_cast<u16*>(wd) + (size_t)grp * 64 * D_ + 128 * qa, 0, 0 ? 0 : 64 * D_ * 2 - 256 * qa, 0x00020000);
#pragma unroll
      for (int i = 0; i < 16; ++i) {
        wdf[i] = __builtin_amdgcn_raw_buffer_load_b128(rd, dv, (32 * (i >> 3) + (i & 7)) * (D_ * 2), 0);
        asm volatile("s_sleep 2\n\ts_waitcnt vmcnt(3)" ::: "memory");
      }
    };
    {
      LANE_VARS;
      const u32x4 sv = {SENT, SENT, SENT, SENT};
#pragma unroll
      for (int i = 0; i < 5; ++i) {
        const unsigned o = ((i * 256 + bid) * 128 + a * 64 + lane) * 16;
        stg(rs, o < SX_BYTES ? OFF_SX + o : OOB, sv);
      }
      if (a == 0) {
        const u32x4 w0 = *reinterpret_cast<const u32x4*>(w_r + lane * 8), w1 = *reinterpret_cast<const u32x4*>(w_r + 512 + lane * 8);
        float t = 0.f;
#pragma unroll
        for (int e = 0; e < 4; ++e) t = fadd(fadd(t, fadd(lo16(w0[e]), hi16(w0[e]))), fadd(lo16(w1[e]), hi16(w1[e])));
        words[0] = __float_as_uint(wave_sum(t));
        if ((lane >> 3) == (tn & 7)) *reinterpret_cast<u32x4*>(smem + L_WR + (lane & 7) * 16) = tn < 8 ? w0 : w1;
      }
      const u16* p = wqkv + (size_t)(64 * tm + 32 * a + hi) * (3 * D_) + (c24 >> 3) * D_ + tn * 64 + (c24 & 7) * 8;
      float cs[8] = {0.f, 0.f, 0.f, 0.f, 0.f, 0.f, 0.f, 0.f};
      u32x4 v[16];
#pragma unroll
      for (int i = 0; i < 16; ++i) v[i] = *reinterpret_cast<const u32x4*>(p + (size_t)i * (2 * 3 * D_));
      SBAR();
#pragma unroll
      for (int i = 0; i < 16; ++i)
#pragma unroll
        for (int e = 0; e < 4; ++e) { cs[2 * e] = fadd(cs[2 * e], lo16(v[i][e])); cs[2 * e + 1] = fadd(cs[2 * e + 1], hi16(v[i][e])); }
      u32x4 o;
#pragma unroll
      for (int e = 0; e < 4; ++e) { const float x = half_swap_sum(cs[e]), y = half_swap_sum(cs[4 + e]); o[e] = __float_as_uint(hi ? y : x); }
      stg(rs, r32 < 24 ? OFF_CS + (tn * 32 + tm * 2 + a) * 768 + r32 * 32 + hi * 16 : OOB, o);
    }
    {
      BAR_ALL();
      LANE_VARS;
      const char* gp = reinterpret_cast<const char*>(((lane & 8) ? wu : wg) + (size_t)(128 * qa + 16 * hi + 4 * tblk) * F_ + 64 * grp + (lane & 7) * 8);
      unsigned dv = (lane >> 4) * (8 * D_ * 2) + (lane & 15) * 16;
      PIN(gp); PIN(dv);
      wait_go(0);
#pragma unroll
      for (int i = 0; i < 32; ++i) {
        gf[i] = *reinterpret_cast<const u32x4*>(gp + (size_t)(0 ? 0 : 32 * (i >> 3) + 8 * ((i >> 2) & 1) + (i & 3)) * (F_ * 2));
        asm volatile("s_sleep 2\n\ts_waitcnt vmcnt(3)" ::: "memory");
      }
      load_wd(dv);
      STAMPV(7);
      BARRIER();
      transpose8(gf); transpose8(gf + 8); transpose8(gf + 16); transpose8(gf + 24);
      transpose8(wdf); transpose8(wdf + 8);
      {
        LANE_VARS;
        f32x4 t[2];
#pragma unroll
        for (int c = 0; c < 2; ++c) {
          u32x4 v[8];
#pragma unroll
          for (int i = 0; i < 8; ++i) v[i] = ldg(rs, OFF_CS + (tn * 32 + 16 * c + 2 * i + hi) * 768 + a * 384 + c24 * 16);
          SBAR();
          t[c] = sum4<8>(v);
          SBAR();
        }
#pragma unroll
        for (int e = 0; e < 4; ++e) t[0][e] = half_swap_sum(fadd(t[0][e], t[1][e]));
        if (lane < 24) *reinterpret_cast<f32x4*>(smem + L_CS + a * 384 + lane * 16) = t[0];
      }
      BAR_LGKM();
    }
#pragma unroll 1
    for (int L = fresh_s(0);; ++L) {
      const unsigned sx = OFF_SX + L * SX_LAYER;
      STAMPV(1 + 16 * L);
      LANE_VARS;
      unsigned cdst = lane < 8 ? sx + SX_ROW + tm * 2048 + tn * 128 + lane * 16 : sx + SX_CAND + (tn * 16 + tm) * 128;
      cdst = (a == 0 && lane < 9) ? cdst : OOB;
      PIN(cdst);
      lds_wait_ge(wa + 32 + 4 * tp, L + 1);
      unsigned key;
      {
        const unsigned k4 = *reinterpret_cast<const unsigned*>(smem + L_CAND + tp * 144 + 128);
        key = max(k4, dpp<0xB1>(k4)); key = max(key, dpp<0x4E>(key));
        const u32x4 c = *reinterpret_cast<const u32x4*>(smem + L_CAND + (((2047 - (int)(key & 2047u)) >> 5) & 3) * 144 + (lane < 8 ? lane : 8) * 16);
        const u32x4 o = {nz(c[0]), nz(c[1]), nz(c[2]), nz(c[3])};
        stg(rs, cdst, o);
      }
      STAMPV(8 + 16 * L);
      __builtin_amdgcn_s_sleep(10);
      u32x4 v[2];
      const unsigned rpc = (tblk ? 4 * kr + tp : 16 * qa + (lane & 15)) * 16;
      poll_all<2>(v, [&](u32x4 (&t)[2]) {
        t[0] = ldg(rs, sx + SX_CAND + (tn * 16 + (lane & 15)) * 128);
        t[1] = ldg(rs, sx + SX_ROW + ((2047 - (int)(key & 2047u)) >> 7) * 2048 + rpc);
        t[0][3] = key;
      }, [&](const u32x4 (&t)[2]) {
        const bool ok = mx4(0u, t[0]) != SENT;
        unsigned k = ok ? t[0][0] : 0u;
        k = max(k, dpp<0xB1>(k)); k = max(k, dpp<0x4E>(k)); k = max(k, dpp<0x141>(k)); k = max(k, dpp<0x140>(k));
        key = max(key, k);
        return ok & (mx4(0u, t[1]) != SENT) & (key == t[0][3]);
      });
      key = __builtin_amdgcn_readfirstlane(key);
      const int idx = 2047 - (int)(key & 2047u);
      const bool owner = (idx >> 7) == tm;
      STAMPV(2 + 16 * L);
      {
        const float mb = __uint_as_float(__builtin_amdgcn_readlane(v[0][1], idx >> 7)), rd = __uint_as_float(__builtin_amdgcn_readlane(v[0][2], idx >> 7));
        u32x4 hb;
#pragma unroll
        for (int e = 0; e < 4; ++e) hb[e] = ln_pair_s(v[1][e], -mb, rd);
        *reinterpret_cast<u32x4*>(smem + L_HB + a * 1024 + lane * 16) = hb;
        *reinterpret_cast<u32x4*>(smem + L_XB + a * 1024 + lane * 16) = v[1];
        words[3] = (unsigned)idx;
        if (L == (1 ? 1 : 5) || (L == 0 && !owner)) lds_set(wa + 4 + 4 * a, L + 1);
      }
      auto pick2 = [&](const f32x4 (&d)[8]) {
        u32x2 o;
#pragma unroll
        for (int e = 0; e < 2; ++e) {
          const float x = tblk ? d[2 + e][0] : d[e][0], y = tblk ? d[6 + e][0] : d[4 + e][0];
          o[e] = nzf(hi ? y : x);
        }
        return o;
      };
      if (ffn) {
        const f32x4 zero = {0.f, 0.f, 0.f, 0.f};
        const unsigned ppos = (lane & 15) * 32 + (lane >> 4) * 8;
        f32x4 d[8];
        {
          s16x8 hf[4];
#pragma unroll
          for (int t = 0; t < 4; ++t) hf[t] = *reinterpret_cast<const s16x8*>(smem + L_HB + a * 1024 + (lane >> 4) * 16 + t * 64);
          SBAR();
          __builtin_amdgcn_s_setprio(3);
#pragma unroll
          for (int t = 0; t < 32; ++t) d[t & 7] = MFMA16(hf[t >> 3], __builtin_bit_cast(s16x8, gf[t]), t < 8 ? zero : d[t & 7]);
          SBAR();
          __builtin_amdgcn_s_setprio(0);
          __builtin_amdgcn_raw_buffer_store_b64(pick2(d), rs, sx + SX_GP + (grp * 8 + qa) * 512 + ppos, 0, 16);
        }
        STAMPV(9 + 16 * L);
        __builtin_amdgcn_s_sleep(10);
        u32x2 gu[8];
        for (unsigned spins = 0;; ++spins) {
#pragma unroll
          for (int i = 0; i < 8; ++i) gu[i] = __builtin_amdgcn_raw_buffer_load_b64(rs, sx + SX_GP + (grp * 8 + i) * 512 + lane * 8, 0, 16);
          SBAR();
          unsigned m = 0u;
#pragma unroll
          for (int i = 0; i < 8; ++i) m = max(max(m, gu[i][0]), gu[i][1]);
          if (__all(m != SENT) || spins > SPIN_CAP) break;
          asm volatile("s_sleep 1" ::: "memory");
        }
        STAMPV(10 + 16 * L);
        float t0 = opq(U2F(gu[0][0])), t1 = opq(U2F(gu[0][1]));
#pragma unroll
        for (int i = 1; i < 8; ++i) { t0 = fadd(t0, U2F(gu[i][0])); t1 = fadd(t1, U2F(gu[i][1])); }
        auto sw = __builtin_amdgcn_permlane32_swap(__float_as_uint(t0), __float_as_uint(t1), false, false);
        const float gv = rbf(U2F(sw[0])), uv = rbf(U2F(sw[1]));
        reinterpret_cast<u16*>(smem + L_GU + a * 128)[2 * r32 + hi] = (u16)pack2(fmul(rbf(fmul(gv, RCP(fadd(1.0f, __expf(-gv))))), uv), 0.f);
        asm volatile("s_waitcnt lgkmcnt(0)" ::: "memory");
        const s16x8 a0 = *reinterpret_cast<const s16x8*>(smem + L_GU + a * 128 + (lane >> 4) * 16), a1 = *reinterpret_cast<const s16x8*>(smem + L_GU + a * 128 + 64 + (lane >> 4) * 16);
        SBAR();
        __builtin_amdgcn_s_setprio(3);
#pragma unroll
        for (int t = 0; t < 16; ++t) d[t & 7] = MFMA16(t < 8 ? a0 : a1, __builtin_bit_cast(s16x8, wdf[t]), t < 8 ? zero : d[t & 7]);
        SBAR();
        __builtin_amdgcn_s_setprio(0);
        __builtin_amdgcn_raw_buffer_store_b64(pick2(d), rs, sx + SX_YP + grp * 4096 + qa * 512 + ppos, 0, 16);
      }
      STAMPV(3 + 16 * L);
      if (L == 1) lds_set(wa + 4 + 4 * a, 2);
      if (L == 1 && (cg != (kr & 7) || a)) { STAMP_END; __builtin_amdgcn_endpgm(); }
      u32x4 wpf[12];
      if (L == 0) {
        const u16* p = wqkv + (size_t)(32 * kr + 16 * hi + 4 * tblk) * (3 * D_) + (2 * cg + a) * 64 + (lane & 15) * 4;
        u32x2 v[24];
#pragma unroll
        for (int i = 0; i < 24; ++i) v[i] = *reinterpret_cast<const u32x2*>(p + (size_t)(8 * ((i >> 2) & 1) + (i & 3)) * (3 * D_) + (i >> 3) * D_);
        SBAR();
#pragma unroll
        for (int t = 0; t < 12; ++t) {
#pragma unroll
          for (int e = 0; e < 4; ++e) {
            const unsigned x = v[8 * (t >> 2) + 2 * e][(t >> 1) & 1], y = v[8 * (t >> 2) + 2 * e + 1][(t >> 1) & 1];
            wpf[t][e] = (t & 1) ? PERM_HI(x, y) : PERM_LO(x, y);
          }
          PIN(wpf[t]);
        }
      } else {
#pragma unroll
        for (int i = 0; i < 12; ++i) asm volatile("" : "=v"(wpf[i]));
        __builtin_amdgcn_s_sleep(10);
      }
      const unsigned rk = key >> 16;
      float rsig = rbf(RCP(fadd(1.0f, __expf(-lo16((rk & 0x8000u) ? (rk & 0x7fffu) : (~rk & 0xffffu))))));
      PIN(rsig);
      const int n = 4 * (lane & 7) + ((lane >> 3) & 3);
      const int fo = ((n >> 4) * 2 + ((n >> 2) & 1)) * 8 + ((n >> 3) & 1) * 4 + (n & 3);
      float xv = bf2f(reinterpret_cast<const u16*>(smem + L_XB + a * 1024 + 256)[fo]), hv = bf2f(reinterpret_cast<const u16*>(smem + L_HB + a * 1024 + 256)[fo]);
      PIN(xv); PIN(hv);
      u32x4 pv[7];
      poll_all<7, 4>(pv, [&](u32x4 (&t)[7]) {
#pragma unroll
        for (int i = 0; i < 7; ++i) t[i] = ldg(rs, sx + SX_YP + (8 * i + (lane >> 3)) * 4096 + kr * 128 + (lane & 7) * 16);
      }, [&](const u32x4 (&t)[7]) { return none_sent(t); });
      STAMPV(4 + 16 * L);
      float ys;
      {
        f32x4 t = sum4<7>(pv);
#pragma unroll
        for (int e = 0; e < 4; ++e) {
          t[e] = fadd(t[e], U2F(dpp<0x128>(__float_as_uint(t[e]))));
          t[e] = half_swap_sum(row_swap_sum(t[e]));
        }
        const float x = (lane & 8) ? t[1] : t[0], y = (lane & 8) ? t[3] : t[2];
        ys = tblk ? y : x;
      }
      const unsigned ob = pack2(fadd(xv, rbf(fadd(hv, rbf(fmul(rbf(ys), rsig))))), 0.f) & 0xffffu;
      if (L == 1) {
        if (lane < 32) out[(size_t)idx * D_ + kr * 32 + n] = (u16)ob;
        STAMPV(5 + 16 * L);
        STAMP_END;
        __builtin_amdgcn_endpgm();
      }
      reinterpret_cast<u16*>(smem + L_X2F + a * 64)[fo] = (u16)ob;
      asm volatile("s_waitcnt lgkmcnt(0)" ::: "memory");
      {
        const s16x8 xa = *reinterpret_cast<const s16x8*>(smem + L_X2F + a * 64 + (lane >> 4) * 16);
        const u32x4 xf = *reinterpret_cast<const u32x4*>(smem + L_X2F + a * 64 + tp * 16);
        f32x4 z[12];
        const f32x4 zero = {0.f, 0.f, 0.f, 0.f};
        SBAR();
        __builtin_amdgcn_s_setprio(3);
#pragma unroll
        for (int t = 0; t < 12; ++t) z[t] = MFMA16(xa, __builtin_bit_cast(s16x8, wpf[t]), zero);
        SBAR();
        __builtin_amdgcn_s_setprio(0);
        const float x2 = lo16(ob), s1 = half_sum(x2), s2 = half_sum(fmul(x2, x2));
        u32x4 o;
#pragma unroll
        for (int e = 0; e < 4; ++e) {
          const unsigned sp = lane < 52 ? (e == 0 ? __float_as_uint(s1) : __float_as_uint(s2)) : xf[e];
          const unsigned x = __float_as_uint(tblk ? z[4 + e][0] : z[e][0]), y = tblk ? sp : __float_as_uint(z[8 + e][0]);
          o[e] = nz(hi ? y : x);
        }
        const unsigned pos = lane < 24 ? lane * 16 : lane < 48 ? lane * 16 + 128 : lane < 52 ? (lane & 1) * 512 + 384 : 400 + tp * 16;
        stg(rs, lane < 56 ? OFF_SX + SX_QP + ((2 * cg + a) * 32 + kr) * 1024 + pos : OOB, o);
      }
      STAMPV(5 + 16 * L);
      if (owner) {
        __builtin_amdgcn_s_sleep(10);
        const int c25 = r32 < 25 ? r32 : 24;
        u32x4 qv[17];
        poll_all<17, 4>(qv, [&](u32x4 (&t)[17]) {
#pragma unroll
          for (int i = 0; i < 16; ++i) t[i] = ldg(rs, OFF_SX + SX_QP + (tn * 32 + 2 * i + hi) * 1024 + a * 512 + c25 * 16);
          t[16] = ldg(rs, OFF_SX + SX_QP + (tn * 34 + hi) * 1024 + 400 + tp * 16);
        }, [&](const u32x4 (&t)[17]) { return none_sent(t); });
        *reinterpret_cast<u32x4*>(smem + L_X2P + hi * 64 + tp * 16) = qv[16];
        f32x4 t = sum4<16>(qv);
#pragma unroll
        for (int e = 0; e < 4; ++e) t[e] = half_swap_sum(t[e]);
        const float mean = fmul(U2F(__builtin_amdgcn_readlane(__float_as_uint(t[0]), 24)), 1.0f / 1024.0f);
        const float var = fmaxf(ffma(-mean, mean, fmul(U2F(__builtin_amdgcn_readlane(__float_as_uint(t[1]), 24)), 1.0f / 1024.0f)), 0.f);
        float mb, den;
        ln_scalars(mean, var, mb, den);
        const float rd = RCP(den);
        if (lane < 24) {
          const f32x4 cs = *reinterpret_cast<const f32x4*>(smem + L_CS + a * 384 + lane * 16);
#pragma unroll
          for (int e = 0; e < 4; ++e) t[e] = fmul(ffma(-mb, cs[e], t[e]), rd);
          *reinterpret_cast<f32x4*>(smem + L_PATCH + a * 384 + lane * 16) = t;
        }
        lds_set(wa + 4 + 4 * a, L + 1);
      }
      STAMPV(6 + 16 * L);
      BAR_ALL();
      unsigned dv = (lane >> 4) * (8 * D_ * 2) + (lane & 15) * 16;
      PIN(dv);
      wait_go(1);
      load_wd(dv);
      STAMPV(23);
      BARRIER();
      transpose8(wdf); transpose8(wdf + 8);
      BARRIER();
    }
  }
  const int tm_ = tm, tn_ = tn, wid_ = wid;
  *reinterpret_cast<u32x2*>(smem + L_FULL + threadIdx.x * 8) = u32x2{0u, 0u};
  if (threadIdx.x < 16) words[threadIdx.x] = 0u;
  const u32x4 srd = {(unsigned)(uintptr_t)ws, (unsigned)((uintptr_t)ws >> 32) & 0xffffu, WS_BYTES, 0x00020000u};
  u32x4 x1p[4];
  u32x4 pst;
  asm volatile("" : "=v"(x1p[0]), "=v"(x1p[1]), "=v"(x1p[2]), "=v"(x1p[3]), "=v"(pst));
#pragma unroll 1
  for (int L = 0; L < 2; ++L) {
    const unsigned sx = OFF_SX + L * SX_LAYER;
    u32x8 q[4];
    u32x16 kva, kvb;
    f32x16 oz[5];
    {
      f32x16 acc[6];
      u32x16 stA, stB, xr0, xr1, xr2, xr3;
      u32x4 stC;
      unsigned sdst;
      asm volatile("" : "={v[112:127]}"(stB), "={v[128:131]}"(stC));
      { PHASE_VARS;
      stB[8] = lane * 16 + (L ? OFF_X1 : OFF_HF) + tile * TS;
      stB[9] = lbase + wrow * 384 + (((4 * hh + wch) ^ wsw) << 4) + 8 * (tp & 1);
      stB[10] = lbase + wrow * 384 + (((4 * (hh ^ 1) + wch) ^ wsw) << 4) + 8 * (tp & 1);
      stB[13] = lbase + L_FULL + (lane & 1) * 256;
      stB[15] = lbase + L_FREE + wid * 256 + lane * 4;
      stC[0] = 32 * L; sdst = stats_dst(OFF_SX, tile, tn, lane); }
      unsigned soff, slot, need = 32 * L + 1;
      if (L == 0) {
        soff = tn * 4096;
        asm volatile(
          "buffer_load_dwordx4 v[132:135], v120, s[40:43], s44 offen offset:0 sc1\n\t"
          "buffer_load_dwordx4 v[136:139], v120, s[40:43], s44 offen offset:1024 sc1\n\t"
          "buffer_load_dwordx4 v[140:143], v120, s[40:43], s44 offen offset:2048 sc1\n\t"
          "buffer_load_dwordx4 v[144:147], v120, s[40:43], s44 offen offset:3072 sc1\n\t"
          "s_add_u32 s44, s44, 0x1000\n\t"
          "s_and_b32 s44, s44, 0xffff\n\t"
          "buffer_load_dwordx4 v[148:151], v120, s[40:43], s44 offen offset:0 sc1\n\t"
          "buffer_load_dwordx4 v[152:155], v120, s[40:43], s44 offen offset:1024 sc1\n\t"
          "buffer_load_dwordx4 v[156:159], v120, s[40:43], s44 offen offset:2048 sc1\n\t"
          "buffer_load_dwordx4 v[160:163], v120, s[40:43], s44 offen offset:3072 sc1\n\t"
          "s_add_u32 s44, s44, 0x1000\n\t"
          "s_and_b32 s44, s44, 0xffff\n\t"
          "buffer_load_dwordx4 v[164:167], v120, s[40:43], s44 offen offset:0 sc1\n\t"
          "buffer_load_dwordx4 v[168:171], v120, s[40:43], s44 offen offset:1024 sc1\n\t"
          "buffer_load_dwordx4 v[172:175], v120, s[40:43], s44 offen offset:2048 sc1\n\t"
          "buffer_load_dwordx4 v[176:179], v120, s[40:43], s44 offen offset:3072 sc1\n\t"
          "s_add_u32 s44, s44, 0x1000\n\t"
          "s_and_b32 s44, s44, 0xffff\n\t"
          "buffer_load_dwordx4 v[180:183], v120, s[40:43], s44 offen offset:0 sc1\n\t"
          "buffer_load_dwordx4 v[184:187], v120, s[40:43], s44 offen offset:1024 sc1\n\t"
          "buffer_load_dwordx4 v[188:191], v120, s[40:43], s44 offen offset:2048 sc1\n\t"
          "buffer_load_dwordx4 v[192:195], v120, s[40:43], s44 offen offset:3072 sc1\n\t"
          : "={v[132:147]}"(xr0), "={v[148:163]}"(xr1), "={v[164:179]}"(xr2), "={v[180:195]}"(xr3), "+{s44}"(soff) : "{v[112:127]}"(stB), "{s[40:43]}"(srd) : "scc", "memory");
        BAR_LGKM();
      } else {
        xr0 = cat4(x1p[0], x1p[1], x1p[2], x1p[3]);
        asm volatile("" : "={v[148:163]}"(xr1), "={v[164:179]}"(xr2), "={v[180:195]}"(xr3));
      }
      STAMPV(1 + 16 * L);
      asm volatile(
        "v_mov_b32 v123, v121\n\t"
        "v_mov_b32 v124, v122\n\t"
        "s_mov_b64 vcc, -1\n\t"
        "s_cbranch_vccnz 10f\n\t"
        "11:\n\t"
        "s_mov_b32 s46, 12288\n\t"
        "s_add_u32 s47, s47, 1\n\t"
        "ds_read_b64_tr_b16 v[96:97], v123 offset:0\n\t"
        "ds_read_b64_tr_b16 v[98:99], v123 offset:3072\n\t"
        "ds_read_b64_tr_b16 v[100:101], v124 offset:0\n\t"
        "ds_read_b64_tr_b16 v[102:103], v124 offset:3072\n\t"
        "ds_read_b64_tr_b16 v[104:105], v123 offset:128\n\t"
        "ds_read_b64_tr_b16 v[106:107], v123 offset:3200\n\t"
        "ds_read_b64_tr_b16 v[108:109], v124 offset:128\n\t"
        "ds_read_b64_tr_b16 v[110:111], v124 offset:3200\n\t"
        "ds_read_b64_tr_b16 v[112:113], v123 offset:256\n\t"
        "ds_read_b64_tr_b16 v[114:115], v123 offset:3328\n\t"
        "ds_write_b32 v127, v128\n\t"
        "ds_read_b64_tr_b16 v[116:117], v124 offset:256\n\t"
        "ds_read_b64_tr_b16 v[118:119], v124 offset:3328\n\t"
        "s_waitcnt vmcnt(15) lgkmcnt(11)\n\t"
        "v_mfma_f32_32x32x16_bf16 v[0:15], v[96:99], v[132:135], 0\n\t"
        "ds_read_b32 v126, v125\n\t"
        "ds_read_b64_tr_b16 v[96:97], v123 offset:6144\n\t"
        "ds_read_b64_tr_b16 v[98:99], v123 offset:9216\n\t"
        "s_waitcnt lgkmcnt(12)\n\t"
        "v_mfma_f32_32x32x16_bf16 v[16:31], v[100:103], v[132:135], 0\n\t"
        "v_add_u32 v129, s46, v121\n\t"
        "ds_read_b64_tr_b16 v[100:101], v124 offset:6144\n\t"
        "ds_read_b64_tr_b16 v[102:103], v124 offset:9216\n\t"
        "s_waitcnt lgkmcnt(12)\n\t"
        "v_mfma_f32_32x32x16_bf16 v[32:47], v[104:107], v[132:135], 0\n\t"
        "ds_read_b64_tr_b16 v[104:105], v123 offset:6272\n\t"
        "ds_read_b64_tr_b16 v[106:107], v123 offset:9344\n\t"
        "s_waitcnt lgkmcnt(12)\n\t"
        "v_mfma_f32_32x32x16_bf16 v[48:63], v[108:111], v[132:135], 0\n\t"
        "v_add_u32 v130, s46, v122\n\t"
        "ds_read_b64_tr_b16 v[108:109], v124 offset:6272\n\t"
        "ds_read_b64_tr_b16 v[110:111], v124 offset:9344\n\t"
        "s_waitcnt lgkmcnt(12)\n\t"
        "v_mfma_f32_32x32x16_bf16 v[64:79], v[132:135], v[112:115], 0\n\t"
        "ds_read_b64_tr_b16 v[112:113], v123 offset:6400\n\t"
        "ds_read_b64_tr_b16 v[114:115], v123 offset:9472\n\t"
        "s_waitcnt lgkmcnt(10)\n\t"
        "v_mfma_f32_32x32x16_bf16 v[80:95], v[132:135], v[116:119], 0\n\t"
        "v_cmp_gt_u32 vcc, s47, v126\n\t"
        "ds_read_b64_tr_b16 v[116:117], v124 offset:6400\n\t"
        "ds_read_b64_tr_b16 v[118:119], v124 offset:9472\n\t"
        "s_waitcnt vmcnt(14) lgkmcnt(10)\n\t"
        "v_mfma_f32_32x32x16_bf16 v[0:15], v[96:99], v[136:139], v[0:15]\n\t"
        "s_cbranch_vccnz 12f\n\t"
        "13:\n\t"
        "ds_read_b64_tr_b16 v[96:97], v129 offset:0\n\t"
        "ds_read_b64_tr_b16 v[98:99], v129 offset:3072\n\t"
        "s_waitcnt lgkmcnt(10)\n\t"
        "v_mfma_f32_32x32x16_bf16 v[16:31], v[100:103], v[136:139], v[16:31]\n\t"
        "v_add_u32 v128, 1, v128\n\t"
        "ds_read_b64_tr_b16 v[100:101], v130 offset:0\n\t"
        "ds_read_b64_tr_b16 v[102:103], v130 offset:3072\n\t"
        "s_waitcnt lgkmcnt(10)\n\t"
        "v_mfma_f32_32x32x16_bf16 v[32:47], v[104:107], v[136:139], v[32:47]\n\t"
        "ds_read_b64_tr_b16 v[104:105], v129 offset:128\n\t"
        "ds_read_b64_tr_b16 v[106:107], v129 offset:3200\n\t"
        "s_waitcnt lgkmcnt(10)\n\t"
        "v_mfma_f32_32x32x16_bf16 v[48:63], v[108:111], v[136:139], v[48:63]\n\t"
        "s_add_u32 s46, s46, 12288\n\t"
        "s_add_u32 s47, s47, 1\n\t"
        "ds_read_b64_tr_b16 v[108:109], v130 offset:128\n\t"
        "ds_read_b64_tr_b16 v[110:111], v130 offset:3200\n\t"
        "s_waitcnt lgkmcnt(10)\n\t"
        "v_mfma_f32_32x32x16_bf16 v[64:79], v[136:139], v[112:115], v[64:79]\n\t"
        "s_cmp_eq_u32 s46, 110592\n\t"
        "s_cselect_b32 s46, 0, s46\n\t"
        "ds_read_b64_tr_b16 v[112:113], v129 offset:256\n\t"
        "ds_read_b64_tr_b16 v[114:115], v129 offset:3328\n\t"
        "s_waitcnt lgkmcnt(10)\n\t"
        "v_mfma_f32_32x32x16_bf16 v[80:95], v[136:139], v[116:119], v[80:95]\n\t"
        "ds_write_b32 v127, v128\n\t"
        "ds_read_b64_tr_b16 v[116:117], v130 offset:256\n\t"
        "ds_read_b64_tr_b16 v[118:119], v130 offset:3328\n\t"
        "s_cmp_lt_u32 s47, 32\n\t"
        "s_cbranch_scc1 4f\n\t"
        "s_waitcnt vmcnt(0)\n\t"
        "buffer_store_dwordx4 v[196:199], v200, s[40:43], 0 offen sc1\n\t"
        "4:\n\t"
        "s_waitcnt vmcnt(13) lgkmcnt(11)\n\t"
        "v_mfma_f32_32x32x16_bf16 v[0:15], v[96:99], v[140:143], v[0:15]\n\t"
        "ds_read_b32 v126, v125\n\t"
        "ds_read_b64_tr_b16 v[96:97], v129 offset:6144\n\t"
        "ds_read_b64_tr_b16 v[98:99], v129 offset:9216\n\t"
        "s_waitcnt lgkmcnt(12)\n\t"
        "v_mfma_f32_32x32x16_bf16 v[16:31], v[100:103], v[140:143], v[16:31]\n\t"
        "v_add_u32 v123, s46, v121\n\t"
        "ds_read_b64_tr_b16 v[100:101], v130 offset:6144\n\t"
        "ds_read_b64_tr_b16 v[102:103], v130 offset:9216\n\t"
        "s_waitcnt lgkmcnt(12)\n\t"
        "v_mfma_f32_32x32x16_bf16 v[32:47], v[104:107], v[140:143], v[32:47]\n\t"
        "ds_read_b64_tr_b16 v[104:105], v129 offset:6272\n\t"
        "ds_read_b64_tr_b16 v[106:107], v129 offset:9344\n\t"
        "s_waitcnt lgkmcnt(12)\n\t"
        "v_mfma_f32_32x32x16_bf16 v[48:63], v[108:111], v[140:143], v[48:63]\n\t"
        "v_add_u32 v124, s46, v122\n\t"
        "ds_read_b64_tr_b16 v[108:109], v130 offset:6272\n\t"
        "ds_read_b64_tr_b16 v[110:111], v130 offset:9344\n\t"
        "s_waitcnt lgkmcnt(12)\n\t"
        "v_mfma_f32_32x32x16_bf16 v[64:79], v[140:143], v[112:115], v[64:79]\n\t"
        "ds_read_b64_tr_b16 v[112:113], v129 offset:6400\n\t"
        "ds_read_b64_tr_b16 v[114:115], v129 offset:9472\n\t"
        "s_waitcnt lgkmcnt(10)\n\t"
        "v_mfma_f32_32x32x16_bf16 v[80:95], v[140:143], v[116:119], v[80:95]\n\t"
        "v_cmp_gt_u32 vcc, s47, v126\n\t"
        "ds_read_b64_tr_b16 v[116:117], v130 offset:6400\n\t"
        "ds_read_b64_tr_b16 v[118:119], v130 offset:9472\n\t"
        "s_waitcnt vmcnt(12) lgkmcnt(10)\n\t"
        "v_mfma_f32_32x32x16_bf16 v[0:15], v[96:99], v[144:147], v[0:15]\n\t"
        "s_cbranch_vccnz 14f\n\t"
        "15:\n\t"
        "ds_read_b64_tr_b16 v[96:97], v123 offset:0\n\t"
        "ds_read_b64_tr_b16 v[98:99], v123 offset:3072\n\t"
        "s_waitcnt lgkmcnt(10)\n\t"
        "v_mfma_f32_32x32x16_bf16 v[16:31], v[100:103], v[144:147], v[16:31]\n\t"
        "v_add_u32 v128, 1, v128\n\t"
        "ds_read_b64_tr_b16 v[100:101], v124 offset:0\n\t"
        "ds_read_b64_tr_b16 v[102:103], v124 offset:3072\n\t"
        "s_waitcnt lgkmcnt(10)\n\t"
        "v_mfma_f32_32x32x16_bf16 v[32:47], v[104:107], v[144:147], v[32:47]\n\t"
        "ds_read_b64_tr_b16 v[104:105], v123 offset:128\n\t"
        "ds_read_b64_tr_b16 v[106:107], v123 offset:3200\n\t"
        "s_waitcnt lgkmcnt(10)\n\t"
        "v_mfma_f32_32x32x16_bf16 v[48:63], v[108:111], v[144:147], v[48:63]\n\t"
        "s_add_u32 s46, s46, 12288\n\t"
        "s_add_u32 s47, s47, 1\n\t"
        "ds_read_b64_tr_b16 v[108:109], v124 offset:128\n\t"
        "ds_read_b64_tr_b16 v[110:111], v124 offset:3200\n\t"
        "s_waitcnt lgkmcnt(10)\n\t"
        "v_mfma_f32_32x32x16_bf16 v[64:79], v[144:147], v[112:115], v[64:79]\n\t"
        "s_cmp_eq_u32 s46, 110592\n\t"
        "s_cselect_b32 s46, 0, s46\n\t"
        "ds_read_b64_tr_b16 v[112:113], v123 offset:256\n\t"
        "ds_read_b64_tr_b16 v[114:115], v123 offset:3328\n\t"
        "s_waitcnt lgkmcnt(10)\n\t"
        "v_mfma_f32_32x32x16_bf16 v[80:95], v[144:147], v[116:119], v[80:95]\n\t"
        "ds_write_b32 v127, v128\n\t"
        "ds_read_b64_tr_b16 v[116:117], v124 offset:256\n\t"
        "ds_read_b64_tr_b16 v[118:119], v124 offset:3328\n\t"
        "s_waitcnt lgkmcnt(0)\n\t"
        "s_nop 7\n\t"
        "s_branch 9f\n\t"
        "10:\n\t"
        "s_mov_b32 s48, 0x10000\n\t"
        "8:\n\t"
        "ds_read_b32 v126, v125\n\t"
        "s_sub_u32 s48, s48, 1\n\t"
        "s_waitcnt lgkmcnt(0)\n\t"
        "s_cbranch_scc1 11b\n\t"
        "v_cmp_gt_u32 vcc, s47, v126\n\t"
        "s_cbranch_vccnz 8b\n\t"
        "s_branch 11b\n\t"
        "12:\n\t"
        "s_mov_b32 s48, 0x10000\n\t"
        "8:\n\t"
        "ds_read_b32 v126, v125\n\t"
        "s_sub_u32 s48, s48, 1\n\t"
        "s_waitcnt lgkmcnt(0)\n\t"
        "s_cbranch_scc1 13b\n\t"
        "v_cmp_gt_u32 vcc, s47, v126\n\t"
        "s_cbranch_vccnz 8b\n\t"
        "s_branch 13b\n\t"
        "14:\n\t"
        "s_mov_b32 s48, 0x10000\n\t"
        "8:\n\t"
        "ds_read_b32 v126, v125\n\t"
        "s_sub_u32 s48, s48, 1\n\t"
        "s_waitcnt lgkmcnt(0)\n\t"
        "s_cbranch_scc1 15b\n\t"
        "v_cmp_gt_u32 vcc, s47, v126\n\t"
        "s_cbranch_vccnz 8b\n\t"
        "s_branch 15b\n\t"
        "9:\n\t"
        : "={v[0:15]}"(acc[0]), "={v[16:31]}"(acc[1]), "={v[32:47]}"(acc[2]), "={v[48:63]}"(acc[3]), "={v[64:79]}"(acc[4]), "={v[80:95]}"(acc[5]),
          "={v[96:111]}"(stA), "+{v[112:127]}"(stB), "+{v[128:131]}"(stC), "={s46}"(slot), "+{s47}"(need)
        : "{v[132:147]}"(xr0), "{v[196:199]}"(pst), "{v200}"(sdst), "{s[40:43]}"(srd) : "s48", "vcc", "scc", "memory");
      STAMPV(2 + 16 * L);
      float al = 1.f, be = 0.f;
      if (L == 1) {
        PHASE_VARS;
        float S, Q, Dd;
        row_stats_sum(rs, OFF_SX, tile, lane, S, Q, Dd);
        soff = ((tn + 1) & 15) * 4096;
        asm volatile(
          "buffer_load_dwordx4 v[148:151], v120, s[40:43], s44 offen offset:0 sc1\n\t"
          "buffer_load_dwordx4 v[152:155], v120, s[40:43], s44 offen offset:1024 sc1\n\t"
          "buffer_load_dwordx4 v[156:159], v120, s[40:43], s44 offen offset:2048 sc1\n\t"
          "buffer_load_dwordx4 v[160:163], v120, s[40:43], s44 offen offset:3072 sc1\n\t"
          "s_add_u32 s44, s44, 0x1000\n\t"
          "s_and_b32 s44, s44, 0xffff\n\t"
          "buffer_load_dwordx4 v[164:167], v120, s[40:43], s44 offen offset:0 sc1\n\t"
          "buffer_load_dwordx4 v[168:171], v120, s[40:43], s44 offen offset:1024 sc1\n\t"
          "buffer_load_dwordx4 v[172:175], v120, s[40:43], s44 offen offset:2048 sc1\n\t"
          "buffer_load_dwordx4 v[176:179], v120, s[40:43], s44 offen offset:3072 sc1\n\t"
          "s_add_u32 s44, s44, 0x1000\n\t"
          "s_and_b32 s44, s44, 0xffff\n\t"
          "buffer_load_dwordx4 v[180:183], v120, s[40:43], s44 offen offset:0 sc1\n\t"
          "buffer_load_dwordx4 v[184:187], v120, s[40:43], s44 offen offset:1024 sc1\n\t"
          "buffer_load_dwordx4 v[188:191], v120, s[40:43], s44 offen offset:2048 sc1\n\t"
          "buffer_load_dwordx4 v[192:195], v120, s[40:43], s44 offen offset:3072 sc1\n\t"
          : "={v[148:163]}"(xr1), "={v[164:179]}"(xr2), "={v[180:195]}"(xr3), "+{s44}"(soff), "+v"(S), "+v"(Q), "+v"(Dd) : "{v[112:127]}"(stB), "{s[40:43]}"(srd) : "scc", "memory");
        float mean, var, mb1, rd1, mb2, den2;
        row_stats_fin(smem, 1, tile, lane, S, Q, Dd, U2F(words[0]), x1p, mean, var, mb1, rd1);
        ln_scalars(mean + (mean - mb1) * rd1, var * (1.0f + rd1) * (1.0f + rd1), mb2, den2);
        const float rd2 = RCP(den2);
        al = (1.0f + rd1) * rd2; be = -(mb1 * rd1 + mb2) * rd2;
        reinterpret_cast<float*>(smem + L_AB)[wid * 64 + lane] = hi ? be : al;
        reinterpret_cast<float*>(smem + L_AB)[256 + wid * 64 + lane] = hi ? rd1 : mb1;
      }
      STAMPV(3 + 16 * L);
      soff = ((tn + 1 + 2) & 15) * 4096;
      asm volatile(
        "s_nop 0\n\t"
        "s_add_u32 s44, s44, 0x1000\n\t"
        "s_and_b32 s44, s44, 0xffff\n\t"
        "buffer_load_dwordx4 v[132:135], v120, s[40:43], s44 offen offset:0 sc1\n\t"
        "buffer_load_dwordx4 v[136:139], v120, s[40:43], s44 offen offset:1024 sc1\n\t"
        "buffer_load_dwordx4 v[140:143], v120, s[40:43], s44 offen offset:2048 sc1\n\t"
        "s_mov_b32 s45, 3\n\t"
        "1:\n\t"
        "s_waitcnt vmcnt(14) lgkmcnt(11)\n\t"
        "v_mfma_f32_32x32x16_bf16 v[0:15], v[96:99], v[148:151], v[0:15]\n\t"
        "ds_read_b32 v126, v125\n\t"
        "ds_read_b64_tr_b16 v[96:97], v123 offset:6144\n\t"
        "ds_read_b64_tr_b16 v[98:99], v123 offset:9216\n\t"
        "s_waitcnt lgkmcnt(12)\n\t"
        "v_mfma_f32_32x32x16_bf16 v[16:31], v[100:103], v[148:151], v[16:31]\n\t"
        "v_add_u32 v129, s46, v121\n\t"
        "ds_read_b64_tr_b16 v[100:101], v124 offset:6144\n\t"
        "ds_read_b64_tr_b16 v[102:103], v124 offset:9216\n\t"
        "s_waitcnt lgkmcnt(12)\n\t"
        "v_mfma_f32_32x32x16_bf16 v[32:47], v[104:107], v[148:151], v[32:47]\n\t"
        "ds_read_b64_tr_b16 v[104:105], v123 offset:6272\n\t"
        "ds_read_b64_tr_b16 v[106:107], v123 offset:9344\n\t"
        "buffer_load_dwordx4 v[144:147], v120, s[40:43], s44 offen offset:3072 sc1\n\t"
        "s_waitcnt lgkmcnt(12)\n\t"
        "v_mfma_f32_32x32x16_bf16 v[48:63], v[108:111], v[148:151], v[48:63]\n\t"
        "v_add_u32 v130, s46, v122\n\t"
        "ds_read_b64_tr_b16 v[108:109], v124 offset:6272\n\t"
        "ds_read_b64_tr_b16 v[110:111], v124 offset:9344\n\t"
        "s_waitcnt lgkmcnt(12)\n\t"
        "v_mfma_f32_32x32x16_bf16 v[64:79], v[148:151], v[112:115], v[64:79]\n\t"
        "ds_read_b64_tr_b16 v[112:113], v123 offset:6400\n\t"
        "ds_read_b64_tr_b16 v[114:115], v123 offset:9472\n\t"
        "s_waitcnt lgkmcnt(10)\n\t"
        "v_mfma_f32_32x32x16_bf16 v[80:95], v[148:151], v[116:119], v[80:95]\n\t"
        "v_cmp_gt_u32 vcc, s47, v126\n\t"
        "ds_read_b64_tr_b16 v[116:117], v124 offset:6400\n\t"
        "ds_read_b64_tr_b16 v[118:119], v124 offset:9472\n\t"
        "s_waitcnt vmcnt(14) lgkmcnt(10)\n\t"
        "v_mfma_f32_32x32x16_bf16 v[0:15], v[96:99], v[152:155], v[0:15]\n\t"
        "s_cbranch_vccnz 16f\n\t"
        "17:\n\t"
        "ds_read_b64_tr_b16 v[96:97], v129 offset:0\n\t"
        "ds_read_b64_tr_b16 v[98:99], v129 offset:3072\n\t"
        "s_waitcnt lgkmcnt(10)\n\t"
        "v_mfma_f32_32x32x16_bf16 v[16:31], v[100:103], v[152:155], v[16:31]\n\t"
        "v_add_u32 v128, 1, v128\n\t"
        "ds_read_b64_tr_b16 v[100:101], v130 offset:0\n\t"
        "ds_read_b64_tr_b16 v[102:103], v130 offset:3072\n\t"
        "s_waitcnt lgkmcnt(10)\n\t"
        "v_mfma_f32_32x32x16_bf16 v[32:47], v[104:107], v[152:155], v[32:47]\n\t"
        "ds_read_b64_tr_b16 v[104:105], v129 offset:128\n\t"
        "ds_read_b64_tr_b16 v[106:107], v129 offset:3200\n\t"
        "s_add_u32 s44, s44, 0x1000\n\t"
        "s_and_b32 s44, s44, 0xffff\n\t"
        "buffer_load_dwordx4 v[148:151], v120, s[40:43], s44 offen offset:0 sc1\n\t"
        "s_waitcnt lgkmcnt(10)\n\t"
        "v_mfma_f32_32x32x16_bf16 v[48:63], v[108:111], v[152:155], v[48:63]\n\t"
        "s_add_u32 s46, s46, 12288\n\t"
        "s_add_u32 s47, s47, 1\n\t"
        "ds_read_b64_tr_b16 v[108:109], v130 offset:128\n\t"
        "ds_read_b64_tr_b16 v[110:111], v130 offset:3200\n\t"
        "s_waitcnt lgkmcnt(10)\n\t"
        "v_mfma_f32_32x32x16_bf16 v[64:79], v[152:155], v[112:115], v[64:79]\n\t"
        "s_cmp_eq_u32 s46, 110592\n\t"
        "s_cselect_b32 s46, 0, s46\n\t"
        "ds_read_b64_tr_b16 v[112:113], v129 offset:256\n\t"
        "ds_read_b64_tr_b16 v[114:115], v129 offset:3328\n\t"
        "s_waitcnt lgkmcnt(10)\n\t"
        "v_mfma_f32_32x32x16_bf16 v[80:95], v[152:155], v[116:119], v[80:95]\n\t"
        "ds_write_b32 v127, v128\n\t"
        "ds_read_b64_tr_b16 v[116:117], v130 offset:256\n\t"
        "ds_read_b64_tr_b16 v[118:119], v130 offset:3328\n\t"
        "s_waitcnt vmcnt(14) lgkmcnt(11)\n\t"
        "v_mfma_f32_32x32x16_bf16 v[0:15], v[96:99], v[156:159], v[0:15]\n\t"
        "ds_read_b32 v126, v125\n\t"
        "ds_read_b64_tr_b16 v[96:97], v129 offset:6144\n\t"
        "ds_read_b64_tr_b16 v[98:99], v129 offset:9216\n\t"
        "s_waitcnt lgkmcnt(12)\n\t"
        "v_mfma_f32_32x32x16_bf16 v[16:31], v[100:103], v[156:159], v[16:31]\n\t"
        "v_add_u32 v123, s46, v121\n\t"
        "ds_read_b64_tr_b16 v[100:101], v130 offset:6144\n\t"
        "ds_read_b64_tr_b16 v[102:103], v130 offset:9216\n\t"
        "s_waitcnt lgkmcnt(12)\n\t"
        "v_mfma_f32_32x32x16_bf16 v[32:47], v[104:107], v[156:159], v[32:47]\n\t"
        "ds_read_b64_tr_b16 v[104:105], v129 offset:6272\n\t"
        "ds_read_b64_tr_b16 v[106:107], v129 offset:9344\n\t"
        "buffer_load_dwordx4 v[152:155], v120, s[40:43], s44 offen offset:1024 sc1\n\t"
        "s_waitcnt lgkmcnt(12)\n\t"
        "v_mfma_f32_32x32x16_bf16 v[48:63], v[108:111], v[156:159], v[48:63]\n\t"
        "v_add_u32 v124, s46, v122\n\t"
        "ds_read_b64_tr_b16 v[108:109], v130 offset:6272\n\t"
        "ds_read_b64_tr_b16 v[110:111], v130 offset:9344\n\t"
        "s_waitcnt lgkmcnt(12)\n\t"
        "v_mfma_f32_32x32x16_bf16 v[64:79], v[156:159], v[112:115], v[64:79]\n\t"
        "ds_read_b64_tr_b16 v[112:113], v129 offset:6400\n\t"
        "ds_read_b64_tr_b16 v[114:115], v129 offset:9472\n\t"
        "s_waitcnt lgkmcnt(10)\n\t"
        "v_mfma_f32_32x32x16_bf16 v[80:95], v[156:159], v[116:119], v[80:95]\n\t"
        "v_cmp_gt_u32 vcc, s47, v126\n\t"
        "ds_read_b64_tr_b16 v[116:117], v130 offset:6400\n\t"
        "ds_read_b64_tr_b16 v[118:119], v130 offset:9472\n\t"
        "s_waitcnt vmcnt(14) lgkmcnt(10)\n\t"
        "v_mfma_f32_32x32x16_bf16 v[0:15], v[96:99], v[160:163], v[0:15]\n\t"
        "s_cbranch_vccnz 18f\n\t"
        "19:\n\t"
        "ds_read_b64_tr_b16 v[96:97], v123 offset:0\n\t"
        "ds_read_b64_tr_b16 v[98:99], v123 offset:3072\n\t"
        "s_waitcnt lgkmcnt(10)\n\t"
        "v_mfma_f32_32x32x16_bf16 v[16:31], v[100:103], v[160:163], v[16:31]\n\t"
        "v_add_u32 v128, 1, v128\n\t"
        "ds_read_b64_tr_b16 v[100:101], v124 offset:0\n\t"
        "ds_read_b64_tr_b16 v[102:103], v124 offset:3072\n\t"
        "s_waitcnt lgkmcnt(10)\n\t"
        "v_mfma_f32_32x32x16_bf16 v[32:47], v[104:107], v[160:163], v[32:47]\n\t"
        "ds_read_b64_tr_b16 v[104:105], v123 offset:128\n\t"
        "ds_read_b64_tr_b16 v[106:107], v123 offset:3200\n\t"
        "buffer_load_dwordx4 v[156:159], v120, s[40:43], s44 offen offset:2048 sc1\n\t"
        "s_waitcnt lgkmcnt(10)\n\t"
        "v_mfma_f32_32x32x16_bf16 v[48:63], v[108:111], v[160:163], v[48:63]\n\t"
        "s_add_u32 s46, s46, 12288\n\t"
        "s_add_u32 s47, s47, 1\n\t"
        "ds_read_b64_tr_b16 v[108:109], v124 offset:128\n\t"
        "ds_read_b64_tr_b16 v[110:111], v124 offset:3200\n\t"
        "s_waitcnt lgkmcnt(10)\n\t"
        "v_mfma_f32_32x32x16_bf16 v[64:79], v[160:163], v[112:115], v[64:79]\n\t"
        "s_cmp_eq_u32 s46, 110592\n\t"
        "s_cselect_b32 s46, 0, s46\n\t"
        "ds_read_b64_tr_b16 v[112:113], v123 offset:256\n\t"
        "ds_read_b64_tr_b16 v[114:115], v123 offset:3328\n\t"
        "s_waitcnt lgkmcnt(10)\n\t"
        "v_mfma_f32_32x32x16_bf16 v[80:95], v[160:163], v[116:119], v[80:95]\n\t"
        "ds_write_b32 v127, v128\n\t"
        "ds_read_b64_tr_b16 v[116:117], v124 offset:256\n\t"
        "ds_read_b64_tr_b16 v[118:119], v124 offset:3328\n\t"
        "s_waitcnt vmcnt(14) lgkmcnt(11)\n\t"
        "v_mfma_f32_32x32x16_bf16 v[0:15], v[96:99], v[164:167], v[0:15]\n\t"
        "ds_read_b32 v126, v125\n\t"
        "ds_read_b64_tr_b16 v[96:97], v123 offset:6144\n\t"
        "ds_read_b64_tr_b16 v[98:99], v123 offset:9216\n\t"
        "s_waitcnt lgkmcnt(12)\n\t"
        "v_mfma_f32_32x32x16_bf16 v[16:31], v[100:103], v[164:167], v[16:31]\n\t"
        "v_add_u32 v129, s46, v121\n\t"
        "ds_read_b64_tr_b16 v[100:101], v124 offset:6144\n\t"
        "ds_read_b64_tr_b16 v[102:103], v124 offset:9216\n\t"
        "s_waitcnt lgkmcnt(12)\n\t"
        "v_mfma_f32_32x32x16_bf16 v[32:47], v[104:107], v[164:167], v[32:47]\n\t"
        "ds_read_b64_tr_b16 v[104:105], v123 offset:6272\n\t"
        "ds_read_b64_tr_b16 v[106:107], v123 offset:9344\n\t"
        "buffer_load_dwordx4 v[160:163], v120, s[40:43], s44 offen offset:3072 sc1\n\t"
        "s_waitcnt lgkmcnt(12)\n\t"
        "v_mfma_f32_32x32x16_bf16 v[48:63], v[108:111], v[164:167], v[48:63]\n\t"
        "v_add_u32 v130, s46, v122\n\t"
        "ds_read_b64_tr_b16 v[108:109], v124 offset:6272\n\t"
        "ds_read_b64_tr_b16 v[110:111], v124 offset:9344\n\t"
        "s_waitcnt lgkmcnt(12)\n\t"
        "v_mfma_f32_32x32x16_bf16 v[64:79], v[164:167], v[112:115], v[64:79]\n\t"
        "ds_read_b64_tr_b16 v[112:113], v123 offset:6400\n\t"
        "ds_read_b64_tr_b16 v[114:115], v123 offset:9472\n\t"
        "s_waitcnt lgkmcnt(10)\n\t"
        "v_mfma_f32_32x32x16_bf16 v[80:95], v[164:167], v[116:119], v[80:95]\n\t"
        "v_cmp_gt_u32 vcc, s47, v126\n\t"
        "ds_read_b64_tr_b16 v[116:117], v124 offset:6400\n\t"
        "ds_read_b64_tr_b16 v[118:119], v124 offset:9472\n\t"
        "s_waitcnt vmcnt(14) lgkmcnt(10)\n\t"
        "v_mfma_f32_32x32x16_bf16 v[0:15], v[96:99], v[168:171], v[0:15]\n\t"
        "s_cbranch_vccnz 20f\n\t"
        "21:\n\t"
        "ds_read_b64_tr_b16 v[96:97], v129 offset:0\n\t"
        "ds_read_b64_tr_b16 v[98:99], v129 offset:3072\n\t"
        "s_waitcnt lgkmcnt(10)\n\t"
        "v_mfma_f32_32x32x16_bf16 v[16:31], v[100:103], v[168:171], v[16:31]\n\t"
        "v_add_u32 v128, 1, v128\n\t"
        "ds_read_b64_tr_b16 v[100:101], v130 offset:0\n\t"
        "ds_read_b64_tr_b16 v[102:103], v130 offset:3072\n\t"
        "s_waitcnt lgkmcnt(10)\n\t"
        "v_mfma_f32_32x32x16_bf16 v[32:47], v[104:107], v[168:171], v[32:47]\n\t"
        "ds_read_b64_tr_b16 v[104:105], v129 offset:128\n\t"
        "ds_read_b64_tr_b16 v[106:107], v129 offset:3200\n\t"
        "s_add_u32 s44, s44, 0x1000\n\t"
        "s_and_b32 s44, s44, 0xffff\n\t"
        "buffer_load_dwordx4 v[164:167], v120, s[40:43], s44 offen offset:0 sc1\n\t"
        "s_waitcnt lgkmcnt(10)\n\t"
        "v_mfma_f32_32x32x16_bf16 v[48:63], v[108:111], v[168:171], v[48:63]\n\t"
        "s_add_u32 s46, s46, 12288\n\t"
        "s_add_u32 s47, s47, 1\n\t"
        "ds_read_b64_tr_b16 v[108:109], v130 offset:128\n\t"
        "ds_read_b64_tr_b16 v[110:111], v130 offset:3200\n\t"
        "s_waitcnt lgkmcnt(10)\n\t"
        "v_mfma_f32_32x32x16_bf16 v[64:79], v[168:171], v[112:115], v[64:79]\n\t"
        "s_cmp_eq_u32 s46, 110592\n\t"
        "s_cselect_b32 s46, 0, s46\n\t"
        "ds_read_b64_tr_b16 v[112:113], v129 offset:256\n\t"
        "ds_read_b64_tr_b16 v[114:115], v129 offset:3328\n\t"
        "s_waitcnt lgkmcnt(10)\n\t"
        "v_mfma_f32_32x32x16_bf16 v[80:95], v[168:171], v[116:119], v[80:95]\n\t"
        "ds_write_b32 v127, v128\n\t"
        "ds_read_b64_tr_b16 v[116:117], v130 offset:256\n\t"
        "ds_read_b64_tr_b16 v[118:119], v130 offset:3328\n\t"
        "s_waitcnt vmcnt(14) lgkmcnt(11)\n\t"
        "v_mfma_f32_32x32x16_bf16 v[0:15], v[96:99], v[172:175], v[0:15]\n\t"
        "ds_read_b32 v126, v125\n\t"
        "ds_read_b64_tr_b16 v[96:97], v129 offset:6144\n\t"
        "ds_read_b64_tr_b16 v[98:99], v129 offset:9216\n\t"
        "s_waitcnt lgkmcnt(12)\n\t"
        "v_mfma_f32_32x32x16_bf16 v[16:31], v[100:103], v[172:175], v[16:31]\n\t"
        "v_add_u32 v123, s46, v121\n\t"
        "ds_read_b64_tr_b16 v[100:101], v130 offset:6144\n\t"
        "ds_read_b64_tr_b16 v[102:103], v130 offset:9216\n\t"
        "s_waitcnt lgkmcnt(12)\n\t"
        "v_mfma_f32_32x32x16_bf16 v[32:47], v[104:107], v[172:175], v[32:47]\n\t"
        "ds_read_b64_tr_b16 v[104:105], v129 offset:6272\n\t"
        "ds_read_b64_tr_b16 v[106:107], v129 offset:9344\n\t"
        "buffer_load_dwordx4 v[168:171], v120, s[40:43], s44 offen offset:1024 sc1\n\t"
        "s_waitcnt lgkmcnt(12)\n\t"
        "v_mfma_f32_32x32x16_bf16 v[48:63], v[108:111], v[172:175], v[48:63]\n\t"
        "v_add_u32 v124, s46, v122\n\t"
        "ds_read_b64_tr_b16 v[108:109], v130 offset:6272\n\t"
        "ds_read_b64_tr_b16 v[110:111], v130 offset:9344\n\t"
        "s_waitcnt lgkmcnt(12)\n\t"
        "v_mfma_f32_32x32x16_bf16 v[64:79], v[172:175], v[112:115], v[64:79]\n\t"
        "ds_read_b64_tr_b16 v[112:113], v129 offset:6400\n\t"
        "ds_read_b64_tr_b16 v[114:115], v129 offset:9472\n\t"
        "s_waitcnt lgkmcnt(10)\n\t"
        "v_mfma_f32_32x32x16_bf16 v[80:95], v[172:175], v[116:119], v[80:95]\n\t"
        "v_cmp_gt_u32 vcc, s47, v126\n\t"
        "ds_read_b64_tr_b16 v[116:117], v130 offset:6400\n\t"
        "ds_read_b64_tr_b16 v[118:119], v130 offset:9472\n\t"
        "s_waitcnt vmcnt(14) lgkmcnt(10)\n\t"
        "v_mfma_f32_32x32x16_bf16 v[0:15], v[96:99], v[176:179], v[0:15]\n\t"
        "s_cbranch_vccnz 22f\n\t"
        "23:\n\t"
        "ds_read_b64_tr_b16 v[96:97], v123 offset:0\n\t"
        "ds_read_b64_tr_b16 v[98:99], v123 offset:3072\n\t"
        "s_waitcnt lgkmcnt(10)\n\t"
        "v_mfma_f32_32x32x16_bf16 v[16:31], v[100:103], v[176:179], v[16:31]\n\t"
        "v_add_u32 v128, 1, v128\n\t"
        "ds_read_b64_tr_b16 v[100:101], v124 offset:0\n\t"
        "ds_read_b64_tr_b16 v[102:103], v124 offset:3072\n\t"
        "s_waitcnt lgkmcnt(10)\n\t"
        "v_mfma_f32_32x32x16_bf16 v[32:47], v[104:107], v[176:179], v[32:47]\n\t"
        "ds_read_b64_tr_b16 v[104:105], v123 offset:128\n\t"
        "ds_read_b64_tr_b16 v[106:107], v123 offset:3200\n\t"
        "buffer_load_dwordx4 v[172:175], v120, s[40:43], s44 offen offset:2048 sc1\n\t"
        "s_waitcnt lgkmcnt(10)\n\t"
        "v_mfma_f32_32x32x16_bf16 v[48:63], v[108:111], v[176:179], v[48:63]\n\t"
        "s_add_u32 s46, s46, 12288\n\t"
        "s_add_u32 s47, s47, 1\n\t"
        "ds_read_b64_tr_b16 v[108:109], v124 offset:128\n\t"
        "ds_read_b64_tr_b16 v[110:111], v124 offset:3200\n\t"
        "s_waitcnt lgkmcnt(10)\n\t"
        "v_mfma_f32_32x32x16_bf16 v[64:79], v[176:179], v[112:115], v[64:79]\n\t"
        "s_cmp_eq_u32 s46, 110592\n\t"
        "s_cselect_b32 s46, 0, s46\n\t"
        "ds_read_b64_tr_b16 v[112:113], v123 offset:256\n\t"
        "ds_read_b64_tr_b16 v[114:115], v123 offset:3328\n\t"
        "s_waitcnt lgkmcnt(10)\n\t"
        "v_mfma_f32_32x32x16_bf16 v[80:95], v[176:179], v[116:119], v[80:95]\n\t"
        "ds_write_b32 v127, v128\n\t"
        "ds_read_b64_tr_b16 v[116:117], v124 offset:256\n\t"
        "ds_read_b64_tr_b16 v[118:119], v124 offset:3328\n\t"
        "s_waitcnt vmcnt(14) lgkmcnt(11)\n\t"
        "v_mfma_f32_32x32x16_bf16 v[0:15], v[96:99], v[180:183], v[0:15]\n\t"
        "ds_read_b32 v126, v125\n\t"
        "ds_read_b64_tr_b16 v[96:97], v123 offset:6144\n\t"
        "ds_read_b64_tr_b16 v[98:99], v123 offset:9216\n\t"
        "s_waitcnt lgkmcnt(12)\n\t"
        "v_mfma_f32_32x32x16_bf16 v[16:31], v[100:103], v[180:183], v[16:31]\n\t"
        "v_add_u32 v129, s46, v121\n\t"
        "ds_read_b64_tr_b16 v[100:101], v124 offset:6144\n\t"
        "ds_read_b64_tr_b16 v[102:103], v124 offset:9216\n\t"
        "s_waitcnt lgkmcnt(12)\n\t"
        "v_mfma_f32_32x32x16_bf16 v[32:47], v[104:107], v[180:183], v[32:47]\n\t"
        "ds_read_b64_tr_b16 v[104:105], v123 offset:6272\n\t"
        "ds_read_b64_tr_b16 v[106:107], v123 offset:9344\n\t"
        "buffer_load_dwordx4 v[176:179], v120, s[40:43], s44 offen offset:3072 sc1\n\t"
        "s_waitcnt lgkmcnt(12)\n\t"
        "v_mfma_f32_32x32x16_bf16 v[48:63], v[108:111], v[180:183], v[48:63]\n\t"
        "v_add_u32 v130, s46, v122\n\t"
        "ds_read_b64_tr_b16 v[108:109], v124 offset:6272\n\t"
        "ds_read_b64_tr_b16 v[110:111], v124 offset:9344\n\t"
        "s_waitcnt lgkmcnt(12)\n\t"
        "v_mfma_f32_32x32x16_bf16 v[64:79], v[180:183], v[112:115], v[64:79]\n\t"
        "ds_read_b64_tr_b16 v[112:113], v123 offset:6400\n\t"
        "ds_read_b64_tr_b16 v[114:115], v123 offset:9472\n\t"
        "s_waitcnt lgkmcnt(10)\n\t"
        "v_mfma_f32_32x32x16_bf16 v[80:95], v[180:183], v[116:119], v[80:95]\n\t"
        "v_cmp_gt_u32 vcc, s47, v126\n\t"
        "ds_read_b64_tr_b16 v[116:117], v124 offset:6400\n\t"
        "ds_read_b64_tr_b16 v[118:119], v124 offset:9472\n\t"
        "s_waitcnt vmcnt(14) lgkmcnt(10)\n\t"
        "v_mfma_f32_32x32x16_bf16 v[0:15], v[96:99], v[184:187], v[0:15]\n\t"
        "s_cbranch_vccnz 24f\n\t"
        "25:\n\t"
        "ds_read_b64_tr_b16 v[96:97], v129 offset:0\n\t"
        "ds_read_b64_tr_b16 v[98:99], v129 offset:3072\n\t"
        "s_waitcnt lgkmcnt(10)\n\t"
        "v_mfma_f32_32x32x16_bf16 v[16:31], v[100:103], v[184:187], v[16:31]\n\t"
        "v_add_u32 v128, 1, v128\n\t"
        "ds_read_b64_tr_b16 v[100:101], v130 offset:0\n\t"
        "ds_read_b64_tr_b16 v[102:103], v130 offset:3072\n\t"
        "s_waitcnt lgkmcnt(10)\n\t"
        "v_mfma_f32_32x32x16_bf16 v[32:47], v[104:107], v[184:187], v[32:47]\n\t"
        "ds_read_b64_tr_b16 v[104:105], v129 offset:128\n\t"
        "ds_read_b64_tr_b16 v[106:107], v129 offset:3200\n\t"
        "s_add_u32 s44, s44, 0x1000\n\t"
        "s_and_b32 s44, s44, 0xffff\n\t"
        "buffer_load_dwordx4 v[180:183], v120, s[40:43], s44 offen offset:0 sc1\n\t"
        "s_waitcnt lgkmcnt(10)\n\t"
        "v_mfma_f32_32x32x16_bf16 v[48:63], v[108:111], v[184:187], v[48:63]\n\t"
        "s_add_u32 s46, s46, 12288\n\t"
        "s_add_u32 s47, s47, 1\n\t"
        "ds_read_b64_tr_b16 v[108:109], v130 offset:128\n\t"
        "ds_read_b64_tr_b16 v[110:111], v130 offset:3200\n\t"
        "s_waitcnt lgkmcnt(10)\n\t"
        "v_mfma_f32_32x32x16_bf16 v[64:79], v[184:187], v[112:115], v[64:79]\n\t"
        "s_cmp_eq_u32 s46, 110592\n\t"
        "s_cselect_b32 s46, 0, s46\n\t"
        "ds_read_b64_tr_b16 v[112:113], v129 offset:256\n\t"
        "ds_read_b64_tr_b16 v[114:115], v129 offset:3328\n\t"
        "s_waitcnt lgkmcnt(10)\n\t"
        "v_mfma_f32_32x32x16_bf16 v[80:95], v[184:187], v[116:119], v[80:95]\n\t"
        "ds_write_b32 v127, v128\n\t"
        "ds_read_b64_tr_b16 v[116:117], v130 offset:256\n\t"
        "ds_read_b64_tr_b16 v[118:119], v130 offset:3328\n\t"
        "s_waitcnt vmcnt(14) lgkmcnt(11)\n\t"
        "v_mfma_f32_32x32x16_bf16 v[0:15], v[96:99], v[188:191], v[0:15]\n\t"
        "ds_read_b32 v126, v125\n\t"
        "ds_read_b64_tr_b16 v[96:97], v129 offset:6144\n\t"
        "ds_read_b64_tr_b16 v[98:99], v129 offset:9216\n\t"
        "s_waitcnt lgkmcnt(12)\n\t"
        "v_mfma_f32_32x32x16_bf16 v[16:31], v[100:103], v[188:191], v[16:31]\n\t"
        "v_add_u32 v123, s46, v121\n\t"
        "ds_read_b64_tr_b16 v[100:101], v130 offset:6144\n\t"
        "ds_read_b64_tr_b16 v[102:103], v130 offset:9216\n\t"
        "s_waitcnt lgkmcnt(12)\n\t"
        "v_mfma_f32_32x32x16_bf16 v[32:47], v[104:107], v[188:191], v[32:47]\n\t"
        "ds_read_b64_tr_b16 v[104:105], v129 offset:6272\n\t"
        "ds_read_b64_tr_b16 v[106:107], v129 offset:9344\n\t"
        "buffer_load_dwordx4 v[184:187], v120, s[40:43], s44 offen offset:1024 sc1\n\t"
        "s_waitcnt lgkmcnt(12)\n\t"
        "v_mfma_f32_32x32x16_bf16 v[48:63], v[108:111], v[188:191], v[48:63]\n\t"
        "v_add_u32 v124, s46, v122\n\t"
        "ds_read_b64_tr_b16 v[108:109], v130 offset:6272\n\t"
        "ds_read_b64_tr_b16 v[110:111], v130 offset:9344\n\t"
        "s_waitcnt lgkmcnt(12)\n\t"
        "v_mfma_f32_32x32x16_bf16 v[64:79], v[188:191], v[112:115], v[64:79]\n\t"
        "ds_read_b64_tr_b16 v[112:113], v129 offset:6400\n\t"
        "ds_read_b64_tr_b16 v[114:115], v129 offset:9472\n\t"
        "s_waitcnt lgkmcnt(10)\n\t"
        "v_mfma_f32_32x32x16_bf16 v[80:95], v[188:191], v[116:119], v[80:95]\n\t"
        "v_cmp_gt_u32 vcc, s47, v126\n\t"
        "ds_read_b64_tr_b16 v[116:117], v130 offset:6400\n\t"
        "ds_read_b64_tr_b16 v[118:119], v130 offset:9472\n\t"
        "s_waitcnt vmcnt(14) lgkmcnt(10)\n\t"
        "v_mfma_f32_32x32x16_bf16 v[0:15], v[96:99], v[192:195], v[0:15]\n\t"
        "s_cbranch_vccnz 26f\n\t"
        "27:\n\t"
        "ds_read_b64_tr_b16 v[96:97], v123 offset:0\n\t"
        "ds_read_b64_tr_b16 v[98:99], v123 offset:3072\n\t"
        "s_waitcnt lgkmcnt(10)\n\t"
        "v_mfma_f32_32x32x16_bf16 v[16:31], v[100:103], v[192:195], v[16:31]\n\t"
        "v_add_u32 v128, 1, v128\n\t"
        "ds_read_b64_tr_b16 v[100:101], v124 offset:0\n\t"
        "ds_read_b64_tr_b16 v[102:103], v124 offset:3072\n\t"
        "s_waitcnt lgkmcnt(10)\n\t"
        "v_mfma_f32_32x32x16_bf16 v[32:47], v[104:107], v[192:195], v[32:47]\n\t"
        "ds_read_b64_tr_b16 v[104:105], v123 offset:128\n\t"
        "ds_read_b64_tr_b16 v[106:107], v123 offset:3200\n\t"
        "buffer_load_dwordx4 v[188:191], v120, s[40:43], s44 offen offset:2048 sc1\n\t"
        "s_waitcnt lgkmcnt(10)\n\t"
        "v_mfma_f32_32x32x16_bf16 v[48:63], v[108:111], v[192:195], v[48:63]\n\t"
        "s_add_u32 s46, s46, 12288\n\t"
        "s_add_u32 s47, s47, 1\n\t"
        "ds_read_b64_tr_b16 v[108:109], v124 offset:128\n\t"
        "ds_read_b64_tr_b16 v[110:111], v124 offset:3200\n\t"
        "s_waitcnt lgkmcnt(10)\n\t"
        "v_mfma_f32_32x32x16_bf16 v[64:79], v[192:195], v[112:115], v[64:79]\n\t"
        "s_cmp_eq_u32 s46, 110592\n\t"
        "s_cselect_b32 s46, 0, s46\n\t"
        "ds_read_b64_tr_b16 v[112:113], v123 offset:256\n\t"
        "ds_read_b64_tr_b16 v[114:115], v123 offset:3328\n\t"
        "s_waitcnt lgkmcnt(10)\n\t"
        "v_mfma_f32_32x32x16_bf16 v[80:95], v[192:195], v[116:119], v[80:95]\n\t"
        "ds_write_b32 v127, v128\n\t"
        "ds_read_b64_tr_b16 v[116:117], v124 offset:256\n\t"
        "ds_read_b64_tr_b16 v[118:119], v124 offset:3328\n\t"
        "s_waitcnt vmcnt(14) lgkmcnt(11)\n\t"
        "v_mfma_f32_32x32x16_bf16 v[0:15], v[96:99], v[132:135], v[0:15]\n\t"
        "ds_read_b32 v126, v125\n\t"
        "ds_read_b64_tr_b16 v[96:97], v123 offset:6144\n\t"
        "ds_read_b64_tr_b16 v[98:99], v123 offset:9216\n\t"
        "s_waitcnt lgkmcnt(12)\n\t"
        "v_mfma_f32_32x32x16_bf16 v[16:31], v[100:103], v[132:135], v[16:31]\n\t"
        "v_add_u32 v129, s46, v121\n\t"
        "ds_read_b64_tr_b16 v[100:101], v124 offset:6144\n\t"
        "ds_read_b64_tr_b16 v[102:103], v124 offset:9216\n\t"
        "s_waitcnt lgkmcnt(12)\n\t"
        "v_mfma_f32_32x32x16_bf16 v[32:47], v[104:107], v[132:135], v[32:47]\n\t"
        "ds_read_b64_tr_b16 v[104:105], v123 offset:6272\n\t"
        "ds_read_b64_tr_b16 v[106:107], v123 offset:9344\n\t"
        "buffer_load_dwordx4 v[192:195], v120, s[40:43], s44 offen offset:3072 sc1\n\t"
        "s_waitcnt lgkmcnt(12)\n\t"
        "v_mfma_f32_32x32x16_bf16 v[48:63], v[108:111], v[132:135], v[48:63]\n\t"
        "v_add_u32 v130, s46, v122\n\t"
        "ds_read_b64_tr_b16 v[108:109], v124 offset:6272\n\t"
        "ds_read_b64_tr_b16 v[110:111], v124 offset:9344\n\t"
        "s_waitcnt lgkmcnt(12)\n\t"
        "v_mfma_f32_32x32x16_bf16 v[64:79], v[132:135], v[112:115], v[64:79]\n\t"
        "ds_read_b64_tr_b16 v[112:113], v123 offset:6400\n\t"
        "ds_read_b64_tr_b16 v[114:115], v123 offset:9472\n\t"
        "s_waitcnt lgkmcnt(10)\n\t"
        "v_mfma_f32_32x32x16_bf16 v[80:95], v[132:135], v[116:119], v[80:95]\n\t"
        "v_cmp_gt_u32 vcc, s47, v126\n\t"
        "ds_read_b64_tr_b16 v[116:117], v124 offset:6400\n\t"
        "ds_read_b64_tr_b16 v[118:119], v124 offset:9472\n\t"
        "s_waitcnt vmcnt(14) lgkmcnt(10)\n\t"
        "v_mfma_f32_32x32x16_bf16 v[0:15], v[96:99], v[136:139], v[0:15]\n\t"
        "s_cbranch_vccnz 28f\n\t"
        "29:\n\t"
        "ds_read_b64_tr_b16 v[96:97], v129 offset:0\n\t"
        "ds_read_b64_tr_b16 v[98:99], v129 offset:3072\n\t"
        "s_waitcnt lgkmcnt(10)\n\t"
        "v_mfma_f32_32x32x16_bf16 v[16:31], v[100:103], v[136:139], v[16:31]\n\t"
        "v_add_u32 v128, 1, v128\n\t"
        "ds_read_b64_tr_b16 v[100:101], v130 offset:0\n\t"
        "ds_read_b64_tr_b16 v[102:103], v130 offset:3072\n\t"
        "s_waitcnt lgkmcnt(10)\n\t"
        "v_mfma_f32_32x32x16_bf16 v[32:47], v[104:107], v[136:139], v[32:47]\n\t"
        "ds_read_b64_tr_b16 v[104:105], v129 offset:128\n\t"
        "ds_read_b64_tr_b16 v[106:107], v129 offset:3200\n\t"
        "s_add_u32 s44, s44, 0x1000\n\t"
        "s_and_b32 s44, s44, 0xffff\n\t"
        "buffer_load_dwordx4 v[132:135], v120, s[40:43], s44 offen offset:0 sc1\n\t"
        "s_waitcnt lgkmcnt(10)\n\t"
        "v_mfma_f32_32x32x16_bf16 v[48:63], v[108:111], v[136:139], v[48:63]\n\t"
        "s_add_u32 s46, s46, 12288\n\t"
        "s_add_u32 s47, s47, 1\n\t"
        "ds_read_b64_tr_b16 v[108:109], v130 offset:128\n\t"
        "ds_read_b64_tr_b16 v[110:111], v130 offset:3200\n\t"
        "s_waitcnt lgkmcnt(10)\n\t"
        "v_mfma_f32_32x32x16_bf16 v[64:79], v[136:139], v[112:115], v[64:79]\n\t"
        "s_cmp_eq_u32 s46, 110592\n\t"
        "s_cselect_b32 s46, 0, s46\n\t"
        "ds_read_b64_tr_b16 v[112:113], v129 offset:256\n\t"
        "ds_read_b64_tr_b16 v[114:115], v129 offset:3328\n\t"
        "s_waitcnt lgkmcnt(10)\n\t"
        "v_mfma_f32_32x32x16_bf16 v[80:95], v[136:139], v[116:119], v[80:95]\n\t"
        "ds_write_b32 v127, v128\n\t"
        "ds_read_b64_tr_b16 v[116:117], v130 offset:256\n\t"
        "ds_read_b64_tr_b16 v[118:119], v130 offset:3328\n\t"
        "s_waitcnt vmcnt(14) lgkmcnt(11)\n\t"
        "v_mfma_f32_32x32x16_bf16 v[0:15], v[96:99], v[140:143], v[0:15]\n\t"
        "ds_read_b32 v126, v125\n\t"
        "ds_read_b64_tr_b16 v[96:97], v129 offset:6144\n\t"
        "ds_read_b64_tr_b16 v[98:99], v129 offset:9216\n\t"
        "s_waitcnt lgkmcnt(12)\n\t"
        "v_mfma_f32_32x32x16_bf16 v[16:31], v[100:103], v[140:143], v[16:31]\n\t"
        "v_add_u32 v123, s46, v121\n\t"
        "ds_read_b64_tr_b16 v[100:101], v130 offset:6144\n\t"
        "ds_read_b64_tr_b16 v[102:103], v130 offset:9216\n\t"
        "s_waitcnt lgkmcnt(12)\n\t"
        "v_mfma_f32_32x32x16_bf16 v[32:47], v[104:107], v[140:143], v[32:47]\n\t"
        "ds_read_b64_tr_b16 v[104:105], v129 offset:6272\n\t"
        "ds_read_b64_tr_b16 v[106:107], v129 offset:9344\n\t"
        "buffer_load_dwordx4 v[136:139], v120, s[40:43], s44 offen offset:1024 sc1\n\t"
        "s_waitcnt lgkmcnt(12)\n\t"
        "v_mfma_f32_32x32x16_bf16 v[48:63], v[108:111], v[140:143], v[48:63]\n\t"
        "v_add_u32 v124, s46, v122\n\t"
        "ds_read_b64_tr_b16 v[108:109], v130 offset:6272\n\t"
        "ds_read_b64_tr_b16 v[110:111], v130 offset:9344\n\t"
        "s_waitcnt lgkmcnt(12)\n\t"
        "v_mfma_f32_32x32x16_bf16 v[64:79], v[140:143], v[112:115], v[64:79]\n\t"
        "ds_read_b64_tr_b16 v[112:113], v129 offset:6400\n\t"
        "ds_read_b64_tr_b16 v[114:115], v129 offset:9472\n\t"
        "s_waitcnt lgkmcnt(10)\n\t"
        "v_mfma_f32_32x32x16_bf16 v[80:95], v[140:143], v[116:119], v[80:95]\n\t"
        "v_cmp_gt_u32 vcc, s47, v126\n\t"
        "ds_read_b64_tr_b16 v[116:117], v130 offset:6400\n\t"
        "ds_read_b64_tr_b16 v[118:119], v130 offset:9472\n\t"
        "s_waitcnt vmcnt(14) lgkmcnt(10)\n\t"
        "v_mfma_f32_32x32x16_bf16 v[0:15], v[96:99], v[144:147], v[0:15]\n\t"
        "s_cbranch_vccnz 30f\n\t"
        "31:\n\t"
        "ds_read_b64_tr_b16 v[96:97], v123 offset:0\n\t"
        "ds_read_b64_tr_b16 v[98:99], v123 offset:3072\n\t"
        "s_waitcnt lgkmcnt(10)\n\t"
        "v_mfma_f32_32x32x16_bf16 v[16:31], v[100:103], v[144:147], v[16:31]\n\t"
        "v_add_u32 v128, 1, v128\n\t"
        "ds_read_b64_tr_b16 v[100:101], v124 offset:0\n\t"
        "ds_read_b64_tr_b16 v[102:103], v124 offset:3072\n\t"
        "s_waitcnt lgkmcnt(10)\n\t"
        "v_mfma_f32_32x32x16_bf16 v[32:47], v[104:107], v[144:147], v[32:47]\n\t"
        "ds_read_b64_tr_b16 v[104:105], v123 offset:128\n\t"
        "ds_read_b64_tr_b16 v[106:107], v123 offset:3200\n\t"
        "buffer_load_dwordx4 v[140:143], v120, s[40:43], s44 offen offset:2048 sc1\n\t"
        "s_waitcnt lgkmcnt(10)\n\t"
        "v_mfma_f32_32x32x16_bf16 v[48:63], v[108:111], v[144:147], v[48:63]\n\t"
        "s_add_u32 s46, s46, 12288\n\t"
        "s_add_u32 s47, s47, 1\n\t"
        "ds_read_b64_tr_b16 v[108:109], v124 offset:128\n\t"
        "ds_read_b64_tr_b16 v[110:111], v124 offset:3200\n\t"
        "s_waitcnt lgkmcnt(10)\n\t"
        "v_mfma_f32_32x32x16_bf16 v[64:79], v[144:147], v[112:115], v[64:79]\n\t"
        "s_cmp_eq_u32 s46, 110592\n\t"
        "s_cselect_b32 s46, 0, s46\n\t"
        "ds_read_b64_tr_b16 v[112:113], v123 offset:256\n\t"
        "ds_read_b64_tr_b16 v[114:115], v123 offset:3328\n\t"
        "s_waitcnt lgkmcnt(10)\n\t"
        "v_mfma_f32_32x32x16_bf16 v[80:95], v[144:147], v[116:119], v[80:95]\n\t"
        "ds_write_b32 v127, v128\n\t"
        "ds_read_b64_tr_b16 v[116:117], v124 offset:256\n\t"
        "ds_read_b64_tr_b16 v[118:119], v124 offset:3328\n\t"
        "s_sub_u32 s45, s45, 1\n\t"
        "s_cmp_lg_u32 s45, 0\n\t"
        "s_cbranch_scc1 1b\n\t"
        "s_waitcnt vmcnt(14) lgkmcnt(11)\n\t"
        "v_mfma_f32_32x32x16_bf16 v[0:15], v[96:99], v[148:151], v[0:15]\n\t"
        "ds_read_b32 v126, v125\n\t"
        "ds_read_b64_tr_b16 v[96:97], v123 offset:6144\n\t"
        "ds_read_b64_tr_b16 v[98:99], v123 offset:9216\n\t"
        "s_waitcnt lgkmcnt(12)\n\t"
        "v_mfma_f32_32x32x16_bf16 v[16:31], v[100:103], v[148:151], v[16:31]\n\t"
        "v_add_u32 v129, s46, v121\n\t"
        "ds_read_b64_tr_b16 v[100:101], v124 offset:6144\n\t"
        "ds_read_b64_tr_b16 v[102:103], v124 offset:9216\n\t"
        "s_waitcnt lgkmcnt(12)\n\t"
        "v_mfma_f32_32x32x16_bf16 v[32:47], v[104:107], v[148:151], v[32:47]\n\t"
        "ds_read_b64_tr_b16 v[104:105], v123 offset:6272\n\t"
        "ds_read_b64_tr_b16 v[106:107], v123 offset:9344\n\t"
        "s_waitcnt lgkmcnt(12)\n\t"
        "v_mfma_f32_32x32x16_bf16 v[48:63], v[108:111], v[148:151], v[48:63]\n\t"
        "v_add_u32 v130, s46, v122\n\t"
        "ds_read_b64_tr_b16 v[108:109], v124 offset:6272\n\t"
        "ds_read_b64_tr_b16 v[110:111], v124 offset:9344\n\t"
        "s_waitcnt lgkmcnt(12)\n\t"
        "v_mfma_f32_32x32x16_bf16 v[64:79], v[148:151], v[112:115], v[64:79]\n\t"
        "ds_read_b64_tr_b16 v[112:113], v123 offset:6400\n\t"
        "ds_read_b64_tr_b16 v[114:115], v123 offset:9472\n\t"
        "s_waitcnt lgkmcnt(10)\n\t"
        "v_mfma_f32_32x32x16_bf16 v[80:95], v[148:151], v[116:119], v[80:95]\n\t"
        "v_cmp_gt_u32 vcc, s47, v126\n\t"
        "ds_read_b64_tr_b16 v[116:117], v124 offset:6400\n\t"
        "ds_read_b64_tr_b16 v[118:119], v124 offset:9472\n\t"
        "s_waitcnt vmcnt(13) lgkmcnt(10)\n\t"
        "v_mfma_f32_32x32x16_bf16 v[0:15], v[96:99], v[152:155], v[0:15]\n\t"
        "s_cbranch_vccnz 64f\n\t"
        "65:\n\t"
        "ds_read_b64_tr_b16 v[96:97], v129 offset:0\n\t"
        "ds_read_b64_tr_b16 v[98:99], v129 offset:3072\n\t"
        "s_waitcnt lgkmcnt(10)\n\t"
        "v_mfma_f32_32x32x16_bf16 v[16:31], v[100:103], v[152:155], v[16:31]\n\t"
        "v_add_u32 v128, 1, v128\n\t"
        "ds_read_b64_tr_b16 v[100:101], v130 offset:0\n\t"
        "ds_read_b64_tr_b16 v[102:103], v130 offset:3072\n\t"
        "s_waitcnt lgkmcnt(10)\n\t"
        "v_mfma_f32_32x32x16_bf16 v[32:47], v[104:107], v[152:155], v[32:47]\n\t"
        "ds_read_b64_tr_b16 v[104:105], v129 offset:128\n\t"
        "ds_read_b64_tr_b16 v[106:107], v129 offset:3200\n\t"
        "s_waitcnt lgkmcnt(10)\n\t"
        "v_mfma_f32_32x32x16_bf16 v[48:63], v[108:111], v[152:155], v[48:63]\n\t"
        "s_add_u32 s46, s46, 12288\n\t"
        "s_add_u32 s47, s47, 1\n\t"
        "ds_read_b64_tr_b16 v[108:109], v130 offset:128\n\t"
        "ds_read_b64_tr_b16 v[110:111], v130 offset:3200\n\t"
        "s_waitcnt lgkmcnt(10)\n\t"
        "v_mfma_f32_32x32x16_bf16 v[64:79], v[152:155], v[112:115], v[64:79]\n\t"
        "s_cmp_eq_u32 s46, 110592\n\t"
        "s_cselect_b32 s46, 0, s46\n\t"
        "ds_read_b64_tr_b16 v[112:113], v129 offset:256\n\t"
        "ds_read_b64_tr_b16 v[114:115], v129 offset:3328\n\t"
        "s_waitcnt lgkmcnt(10)\n\t"
        "v_mfma_f32_32x32x16_bf16 v[80:95], v[152:155], v[116:119], v[80:95]\n\t"
        "ds_write_b32 v127, v128\n\t"
        "ds_read_b64_tr_b16 v[116:117], v130 offset:256\n\t"
        "ds_read_b64_tr_b16 v[118:119], v130 offset:3328\n\t"
        "s_waitcnt vmcnt(12) lgkmcnt(11)\n\t"
        "v_mfma_f32_32x32x16_bf16 v[0:15], v[96:99], v[156:159], v[0:15]\n\t"
        "ds_read_b32 v126, v125\n\t"
        "ds_read_b64_tr_b16 v[96:97], v129 offset:6144\n\t"
        "ds_read_b64_tr_b16 v[98:99], v129 offset:9216\n\t"
        "s_waitcnt lgkmcnt(12)\n\t"
        "v_mfma_f32_32x32x16_bf16 v[16:31], v[100:103], v[156:159], v[16:31]\n\t"
        "v_add_u32 v123, s46, v121\n\t"
        "ds_read_b64_tr_b16 v[100:101], v130 offset:6144\n\t"
        "ds_read_b64_tr_b16 v[102:103], v130 offset:9216\n\t"
        "s_waitcnt lgkmcnt(12)\n\t"
        "v_mfma_f32_32x32x16_bf16 v[32:47], v[104:107], v[156:159], v[32:47]\n\t"
        "ds_read_b64_tr_b16 v[104:105], v129 offset:6272\n\t"
        "ds_read_b64_tr_b16 v[106:107], v129 offset:9344\n\t"
        "s_waitcnt lgkmcnt(12)\n\t"
        "v_mfma_f32_32x32x16_bf16 v[48:63], v[108:111], v[156:159], v[48:63]\n\t"
        "v_add_u32 v124, s46, v122\n\t"
        "ds_read_b64_tr_b16 v[108:109], v130 offset:6272\n\t"
        "ds_read_b64_tr_b16 v[110:111], v130 offset:9344\n\t"
        "s_waitcnt lgkmcnt(12)\n\t"
        "v_mfma_f32_32x32x16_bf16 v[64:79], v[156:159], v[112:115], v[64:79]\n\t"
        "ds_read_b64_tr_b16 v[112:113], v129 offset:6400\n\t"
        "ds_read_b64_tr_b16 v[114:115], v129 offset:9472\n\t"
        "s_waitcnt lgkmcnt(10)\n\t"
        "v_mfma_f32_32x32x16_bf16 v[80:95], v[156:159], v[116:119], v[80:95]\n\t"
        "v_cmp_gt_u32 vcc, s47, v126\n\t"
        "ds_read_b64_tr_b16 v[116:117], v130 offset:6400\n\t"
        "ds_read_b64_tr_b16 v[118:119], v130 offset:9472\n\t"
        "s_waitcnt vmcnt(11) lgkmcnt(10)\n\t"
        "v_mfma_f32_32x32x16_bf16 v[0:15], v[96:99], v[160:163], v[0:15]\n\t"
        "s_cbranch_vccnz 66f\n\t"
        "67:\n\t"
        "ds_read_b64_tr_b16 v[96:97], v123 offset:0\n\t"
        "ds_read_b64_tr_b16 v[98:99], v123 offset:3072\n\t"
        "s_waitcnt lgkmcnt(10)\n\t"
        "v_mfma_f32_32x32x16_bf16 v[16:31], v[100:103], v[160:163], v[16:31]\n\t"
        "v_add_u32 v128, 1, v128\n\t"
        "ds_read_b64_tr_b16 v[100:101], v124 offset:0\n\t"
        "ds_read_b64_tr_b16 v[102:103], v124 offset:3072\n\t"
        "s_waitcnt lgkmcnt(10)\n\t"
        "v_mfma_f32_32x32x16_bf16 v[32:47], v[104:107], v[160:163], v[32:47]\n\t"
        "ds_read_b64_tr_b16 v[104:105], v123 offset:128\n\t"
        "ds_read_b64_tr_b16 v[106:107], v123 offset:3200\n\t"
        "s_waitcnt lgkmcnt(10)\n\t"
        "v_mfma_f32_32x32x16_bf16 v[48:63], v[108:111], v[160:163], v[48:63]\n\t"
        "s_add_u32 s46, s46, 12288\n\t"
        "s_add_u32 s47, s47, 1\n\t"
        "ds_read_b64_tr_b16 v[108:109], v124 offset:128\n\t"
        "ds_read_b64_tr_b16 v[110:111], v124 offset:3200\n\t"
        "s_waitcnt lgkmcnt(10)\n\t"
        "v_mfma_f32_32x32x16_bf16 v[64:79], v[160:163], v[112:115], v[64:79]\n\t"
        "s_cmp_eq_u32 s46, 110592\n\t"
        "s_cselect_b32 s46, 0, s46\n\t"
        "ds_read_b64_tr_b16 v[112:113], v123 offset:256\n\t"
        "ds_read_b64_tr_b16 v[114:115], v123 offset:3328\n\t"
        "s_waitcnt lgkmcnt(10)\n\t"
        "v_mfma_f32_32x32x16_bf16 v[80:95], v[160:163], v[116:119], v[80:95]\n\t"
        "ds_write_b32 v127, v128\n\t"
        "ds_read_b64_tr_b16 v[116:117], v124 offset:256\n\t"
        "ds_read_b64_tr_b16 v[118:119], v124 offset:3328\n\t"
        "s_waitcnt vmcnt(10) lgkmcnt(11)\n\t"
        "v_mfma_f32_32x32x16_bf16 v[0:15], v[96:99], v[164:167], v[0:15]\n\t"
        "ds_read_b32 v126, v125\n\t"
        "ds_read_b64_tr_b16 v[96:97], v123 offset:6144\n\t"
        "ds_read_b64_tr_b16 v[98:99], v123 offset:9216\n\t"
        "s_waitcnt lgkmcnt(12)\n\t"
        "v_mfma_f32_32x32x16_bf16 v[16:31], v[100:103], v[164:167], v[16:31]\n\t"
        "v_add_u32 v129, s46, v121\n\t"
        "ds_read_b64_tr_b16 v[100:101], v124 offset:6144\n\t"
        "ds_read_b64_tr_b16 v[102:103], v124 offset:9216\n\t"
        "s_waitcnt lgkmcnt(12)\n\t"
        "v_mfma_f32_32x32x16_bf16 v[32:47], v[104:107], v[164:167], v[32:47]\n\t"
        "ds_read_b64_tr_b16 v[104:105], v123 offset:6272\n\t"
        "ds_read_b64_tr_b16 v[106:107], v123 offset:9344\n\t"
        "s_waitcnt lgkmcnt(12)\n\t"
        "v_mfma_f32_32x32x16_bf16 v[48:63], v[108:111], v[164:167], v[48:63]\n\t"
        "v_add_u32 v130, s46, v122\n\t"
        "ds_read_b64_tr_b16 v[108:109], v124 offset:6272\n\t"
        "ds_read_b64_tr_b16 v[110:111], v124 offset:9344\n\t"
        "s_waitcnt lgkmcnt(12)\n\t"
        "v_mfma_f32_32x32x16_bf16 v[64:79], v[164:167], v[112:115], v[64:79]\n\t"
        "ds_read_b64_tr_b16 v[112:113], v123 offset:6400\n\t"
        "ds_read_b64_tr_b16 v[114:115], v123 offset:9472\n\t"
        "s_waitcnt lgkmcnt(10)\n\t"
        "v_mfma_f32_32x32x16_bf16 v[80:95], v[164:167], v[116:119], v[80:95]\n\t"
        "v_cmp_gt_u32 vcc, s47, v126\n\t"
        "ds_read_b64_tr_b16 v[116:117], v124 offset:6400\n\t"
        "ds_read_b64_tr_b16 v[118:119], v124 offset:9472\n\t"
        "s_waitcnt vmcnt(9) lgkmcnt(10)\n\t"
        "v_mfma_f32_32x32x16_bf16 v[0:15], v[96:99], v[168:171], v[0:15]\n\t"
        "s_cbranch_vccnz 68f\n\t"
        "69:\n\t"
        "ds_read_b64_tr_b16 v[96:97], v129 offset:0\n\t"
        "ds_read_b64_tr_b16 v[98:99], v129 offset:3072\n\t"
        "s_waitcnt lgkmcnt(10)\n\t"
        "v_mfma_f32_32x32x16_bf16 v[16:31], v[100:103], v[168:171], v[16:31]\n\t"
        "v_add_u32 v128, 1, v128\n\t"
        "ds_read_b64_tr_b16 v[100:101], v130 offset:0\n\t"
        "ds_read_b64_tr_b16 v[102:103], v130 offset:3072\n\t"
        "s_waitcnt lgkmcnt(10)\n\t"
        "v_mfma_f32_32x32x16_bf16 v[32:47], v[104:107], v[168:171], v[32:47]\n\t"
        "ds_read_b64_tr_b16 v[104:105], v129 offset:128\n\t"
        "ds_read_b64_tr_b16 v[106:107], v129 offset:3200\n\t"
        "s_waitcnt lgkmcnt(10)\n\t"
        "v_mfma_f32_32x32x16_bf16 v[48:63], v[108:111], v[168:171], v[48:63]\n\t"
        "s_add_u32 s46, s46, 12288\n\t"
        "s_add_u32 s47, s47, 1\n\t"
        "ds_read_b64_tr_b16 v[108:109], v130 offset:128\n\t"
        "ds_read_b64_tr_b16 v[110:111], v130 offset:3200\n\t"
        "s_waitcnt lgkmcnt(10)\n\t"
        "v_mfma_f32_32x32x16_bf16 v[64:79], v[168:171], v[112:115], v[64:79]\n\t"
        "s_cmp_eq_u32 s46, 110592\n\t"
        "s_cselect_b32 s46, 0, s46\n\t"
        "ds_read_b64_tr_b16 v[112:113], v129 offset:256\n\t"
        "ds_read_b64_tr_b16 v[114:115], v129 offset:3328\n\t"
        "s_waitcnt lgkmcnt(10)\n\t"
        "v_mfma_f32_32x32x16_bf16 v[80:95], v[168:171], v[116:119], v[80:95]\n\t"
        "ds_write_b32 v127, v128\n\t"
        "ds_read_b64_tr_b16 v[116:117], v130 offset:256\n\t"
        "ds_read_b64_tr_b16 v[118:119], v130 offset:3328\n\t"
        "s_waitcnt vmcnt(8) lgkmcnt(11)\n\t"
        "v_mfma_f32_32x32x16_bf16 v[0:15], v[96:99], v[172:175], v[0:15]\n\t"
        "ds_read_b32 v126, v125\n\t"
        "ds_read_b64_tr_b16 v[96:97], v129 offset:6144\n\t"
        "ds_read_b64_tr_b16 v[98:99], v129 offset:9216\n\t"
        "s_waitcnt lgkmcnt(12)\n\t"
        "v_mfma_f32_32x32x16_bf16 v[16:31], v[100:103], v[172:175], v[16:31]\n\t"
        "v_add_u32 v123, s46, v121\n\t"
        "ds_read_b64_tr_b16 v[100:101], v130 offset:6144\n\t"
        "ds_read_b64_tr_b16 v[102:103], v130 offset:9216\n\t"
        "s_waitcnt lgkmcnt(12)\n\t"
        "v_mfma_f32_32x32x16_bf16 v[32:47], v[104:107], v[172:175], v[32:47]\n\t"
        "ds_read_b64_tr_b16 v[104:105], v129 offset:6272\n\t"
        "ds_read_b64_tr_b16 v[106:107], v129 offset:9344\n\t"
        "s_waitcnt lgkmcnt(12)\n\t"
        "v_mfma_f32_32x32x16_bf16 v[48:63], v[108:111], v[172:175], v[48:63]\n\t"
        "v_add_u32 v124, s46, v122\n\t"
        "ds_read_b64_tr_b16 v[108:109], v130 offset:6272\n\t"
        "ds_read_b64_tr_b16 v[110:111], v130 offset:9344\n\t"
        "s_waitcnt lgkmcnt(12)\n\t"
        "v_mfma_f32_32x32x16_bf16 v[64:79], v[172:175], v[112:115], v[64:79]\n\t"
        "ds_read_b64_tr_b16 v[112:113], v129 offset:6400\n\t"
        "ds_read_b64_tr_b16 v[114:115], v129 offset:9472\n\t"
        "s_waitcnt lgkmcnt(10)\n\t"
        "v_mfma_f32_32x32x16_bf16 v[80:95], v[172:175], v[116:119], v[80:95]\n\t"
        "v_cmp_gt_u32 vcc, s47, v126\n\t"
        "ds_read_b64_tr_b16 v[116:117], v130 offset:6400\n\t"
        "ds_read_b64_tr_b16 v[118:119], v130 offset:9472\n\t"
        "s_waitcnt vmcnt(7) lgkmcnt(10)\n\t"
        "v_mfma_f32_32x32x16_bf16 v[0:15], v[96:99], v[176:179], v[0:15]\n\t"
        "s_cbranch_vccnz 70f\n\t"
        "71:\n\t"
        "ds_read_b64_tr_b16 v[96:97], v123 offset:0\n\t"
        "ds_read_b64_tr_b16 v[98:99], v123 offset:3072\n\t"
        "s_waitcnt lgkmcnt(10)\n\t"
        "v_mfma_f32_32x32x16_bf16 v[16:31], v[100:103], v[176:179], v[16:31]\n\t"
        "v_add_u32 v128, 1, v128\n\t"
        "ds_read_b64_tr_b16 v[100:101], v124 offset:0\n\t"
        "ds_read_b64_tr_b16 v[102:103], v124 offset:3072\n\t"
        "s_waitcnt lgkmcnt(10)\n\t"
        "v_mfma_f32_32x32x16_bf16 v[32:47], v[104:107], v[176:179], v[32:47]\n\t"
        "ds_read_b64_tr_b16 v[104:105], v123 offset:128\n\t"
        "ds_read_b64_tr_b16 v[106:107], v123 offset:3200\n\t"
        "s_waitcnt lgkmcnt(10)\n\t"
        "v_mfma_f32_32x32x16_bf16 v[48:63], v[108:111], v[176:179], v[48:63]\n\t"
        "s_add_u32 s46, s46, 12288\n\t"
        "s_add_u32 s47, s47, 1\n\t"
        "ds_read_b64_tr_b16 v[108:109], v124 offset:128\n\t"
        "ds_read_b64_tr_b16 v[110:111], v124 offset:3200\n\t"
        "s_waitcnt lgkmcnt(10)\n\t"
        "v_mfma_f32_32x32x16_bf16 v[64:79], v[176:179], v[112:115], v[64:79]\n\t"
        "s_cmp_eq_u32 s46, 110592\n\t"
        "s_cselect_b32 s46, 0, s46\n\t"
        "ds_read_b64_tr_b16 v[112:113], v123 offset:256\n\t"
        "ds_read_b64_tr_b16 v[114:115], v123 offset:3328\n\t"
        "s_waitcnt lgkmcnt(10)\n\t"
        "v_mfma_f32_32x32x16_bf16 v[80:95], v[176:179], v[116:119], v[80:95]\n\t"
        "ds_write_b32 v127, v128\n\t"
        "ds_read_b64_tr_b16 v[116:117], v124 offset:256\n\t"
        "ds_read_b64_tr_b16 v[118:119], v124 offset:3328\n\t"
        "s_waitcnt vmcnt(6) lgkmcnt(11)\n\t"
        "v_mfma_f32_32x32x16_bf16 v[0:15], v[96:99], v[180:183], v[0:15]\n\t"
        "ds_read_b32 v126, v125\n\t"
        "ds_read_b64_tr_b16 v[96:97], v123 offset:6144\n\t"
        "ds_read_b64_tr_b16 v[98:99], v123 offset:9216\n\t"
        "s_waitcnt lgkmcnt(12)\n\t"
        "v_mfma_f32_32x32x16_bf16 v[16:31], v[100:103], v[180:183], v[16:31]\n\t"
        "v_add_u32 v129, s46, v121\n\t"
        "ds_read_b64_tr_b16 v[100:101], v124 offset:6144\n\t"
        "ds_read_b64_tr_b16 v[102:103], v124 offset:9216\n\t"
        "s_waitcnt lgkmcnt(12)\n\t"
        "v_mfma_f32_32x32x16_bf16 v[32:47], v[104:107], v[180:183], v[32:47]\n\t"
        "ds_read_b64_tr_b16 v[104:105], v123 offset:6272\n\t"
        "ds_read_b64_tr_b16 v[106:107], v123 offset:9344\n\t"
        "s_waitcnt lgkmcnt(12)\n\t"
        "v_mfma_f32_32x32x16_bf16 v[48:63], v[108:111], v[180:183], v[48:63]\n\t"
        "v_add_u32 v130, s46, v122\n\t"
        "ds_read_b64_tr_b16 v[108:109], v124 offset:6272\n\t"
        "ds_read_b64_tr_b16 v[110:111], v124 offset:9344\n\t"
        "s_waitcnt lgkmcnt(12)\n\t"
        "v_mfma_f32_32x32x16_bf16 v[64:79], v[180:183], v[112:115], v[64:79]\n\t"
        "ds_read_b64_tr_b16 v[112:113], v123 offset:6400\n\t"
        "ds_read_b64_tr_b16 v[114:115], v123 offset:9472\n\t"
        "s_waitcnt lgkmcnt(10)\n\t"
        "v_mfma_f32_32x32x16_bf16 v[80:95], v[180:183], v[116:119], v[80:95]\n\t"
        "v_cmp_gt_u32 vcc, s47, v126\n\t"
        "ds_read_b64_tr_b16 v[116:117], v124 offset:6400\n\t"
        "ds_read_b64_tr_b16 v[118:119], v124 offset:9472\n\t"
        "s_waitcnt vmcnt(5) lgkmcnt(10)\n\t"
        "v_mfma_f32_32x32x16_bf16 v[0:15], v[96:99], v[184:187], v[0:15]\n\t"
        "s_cbranch_vccnz 72f\n\t"
        "73:\n\t"
        "ds_read_b64_tr_b16 v[96:97], v129 offset:0\n\t"
        "ds_read_b64_tr_b16 v[98:99], v129 offset:3072\n\t"
        "s_waitcnt lgkmcnt(10)\n\t"
        "v_mfma_f32_32x32x16_bf16 v[16:31], v[100:103], v[184:187], v[16:31]\n\t"
        "v_add_u32 v128, 1, v128\n\t"
        "ds_read_b64_tr_b16 v[100:101], v130 offset:0\n\t"
        "ds_read_b64_tr_b16 v[102:103], v130 offset:3072\n\t"
        "s_waitcnt lgkmcnt(10)\n\t"
        "v_mfma_f32_32x32x16_bf16 v[32:47], v[104:107], v[184:187], v[32:47]\n\t"
        "ds_read_b64_tr_b16 v[104:105], v129 offset:128\n\t"
        "ds_read_b64_tr_b16 v[106:107], v129 offset:3200\n\t"
        "s_waitcnt lgkmcnt(10)\n\t"
        "v_mfma_f32_32x32x16_bf16 v[48:63], v[108:111], v[184:187], v[48:63]\n\t"
        "s_add_u32 s46, s46, 12288\n\t"
        "s_add_u32 s47, s47, 1\n\t"
        "ds_read_b64_tr_b16 v[108:109], v130 offset:128\n\t"
        "ds_read_b64_tr_b16 v[110:111], v130 offset:3200\n\t"
        "s_waitcnt lgkmcnt(10)\n\t"
        "v_mfma_f32_32x32x16_bf16 v[64:79], v[184:187], v[112:115], v[64:79]\n\t"
        "s_cmp_eq_u32 s46, 110592\n\t"
        "s_cselect_b32 s46, 0, s46\n\t"
        "ds_read_b64_tr_b16 v[112:113], v129 offset:256\n\t"
        "ds_read_b64_tr_b16 v[114:115], v129 offset:3328\n\t"
        "s_waitcnt lgkmcnt(10)\n\t"
        "v_mfma_f32_32x32x16_bf16 v[80:95], v[184:187], v[116:119], v[80:95]\n\t"
        "ds_write_b32 v127, v128\n\t"
        "ds_read_b64_tr_b16 v[116:117], v130 offset:256\n\t"
        "ds_read_b64_tr_b16 v[118:119], v130 offset:3328\n\t"
        "s_waitcnt vmcnt(4) lgkmcnt(11)\n\t"
        "v_mfma_f32_32x32x16_bf16 v[0:15], v[96:99], v[188:191], v[0:15]\n\t"
        "ds_read_b64_tr_b16 v[96:97], v129 offset:6144\n\t"
        "ds_read_b64_tr_b16 v[98:99], v129 offset:9216\n\t"
        "s_waitcnt lgkmcnt(11)\n\t"
        "v_mfma_f32_32x32x16_bf16 v[16:31], v[100:103], v[188:191], v[16:31]\n\t"
        "ds_read_b64_tr_b16 v[100:101], v130 offset:6144\n\t"
        "ds_read_b64_tr_b16 v[102:103], v130 offset:9216\n\t"
        "s_waitcnt lgkmcnt(11)\n\t"
        "v_mfma_f32_32x32x16_bf16 v[32:47], v[104:107], v[188:191], v[32:47]\n\t"
        "ds_read_b64_tr_b16 v[104:105], v129 offset:6272\n\t"
        "ds_read_b64_tr_b16 v[106:107], v129 offset:9344\n\t"
        "s_waitcnt lgkmcnt(11)\n\t"
        "v_mfma_f32_32x32x16_bf16 v[48:63], v[108:111], v[188:191], v[48:63]\n\t"
        "ds_read_b64_tr_b16 v[108:109], v130 offset:6272\n\t"
        "ds_read_b64_tr_b16 v[110:111], v130 offset:9344\n\t"
        "s_waitcnt lgkmcnt(11)\n\t"
        "v_mfma_f32_32x32x16_bf16 v[64:79], v[188:191], v[112:115], v[64:79]\n\t"
        "ds_read_b64_tr_b16 v[112:113], v129 offset:6400\n\t"
        "ds_read_b64_tr_b16 v[114:115], v129 offset:9472\n\t"
        "s_waitcnt lgkmcnt(10)\n\t"
        "v_mfma_f32_32x32x16_bf16 v[80:95], v[188:191], v[116:119], v[80:95]\n\t"
        "ds_read_b64_tr_b16 v[116:117], v130 offset:6400\n\t"
        "ds_read_b64_tr_b16 v[118:119], v130 offset:9472\n\t"
        "s_waitcnt vmcnt(3) lgkmcnt(10)\n\t"
        "v_mfma_f32_32x32x16_bf16 v[0:15], v[96:99], v[192:195], v[0:15]\n\t"
        "s_waitcnt lgkmcnt(8)\n\t"
        "v_mfma_f32_32x32x16_bf16 v[16:31], v[100:103], v[192:195], v[16:31]\n\t"
        "s_waitcnt lgkmcnt(6)\n\t"
        "v_mfma_f32_32x32x16_bf16 v[32:47], v[104:107], v[192:195], v[32:47]\n\t"
        "s_waitcnt lgkmcnt(4)\n\t"
        "v_mfma_f32_32x32x16_bf16 v[48:63], v[108:111], v[192:195], v[48:63]\n\t"
        "s_waitcnt lgkmcnt(2)\n\t"
        "v_mfma_f32_32x32x16_bf16 v[64:79], v[192:195], v[112:115], v[64:79]\n\t"
        "s_waitcnt lgkmcnt(0)\n\t"
        "v_mfma_f32_32x32x16_bf16 v[80:95], v[192:195], v[116:119], v[80:95]\n\t"
        "s_waitcnt vmcnt(0)\n\t"
        "s_nop 10\n\t"
        "s_branch 9f\n\t"
        "16:\n\t"
        "s_mov_b32 s48, 0x10000\n\t"
        "8:\n\t"
        "ds_read_b32 v126, v125\n\t"
        "s_sub_u32 s48, s48, 1\n\t"
        "s_waitcnt lgkmcnt(0)\n\t"
        "s_cbranch_scc1 17b\n\t"
        "v_cmp_gt_u32 vcc, s47, v126\n\t"
        "s_cbranch_vccnz 8b\n\t"
        "s_branch 17b\n\t"
        "18:\n\t"
        "s_mov_b32 s48, 0x10000\n\t"
        "8:\n\t"
        "ds_read_b32 v126, v125\n\t"
        "s_sub_u32 s48, s48, 1\n\t"
        "s_waitcnt lgkmcnt(0)\n\t"
        "s_cbranch_scc1 19b\n\t"
        "v_cmp_gt_u32 vcc, s47, v126\n\t"
        "s_cbranch_vccnz 8b\n\t"
        "s_branch 19b\n\t"
        "20:\n\t"
        "s_mov_b32 s48, 0x10000\n\t"
        "8:\n\t"
        "ds_read_b32 v126, v125\n\t"
        "s_sub_u32 s48, s48, 1\n\t"
        "s_waitcnt lgkmcnt(0)\n\t"
        "s_cbranch_scc1 21b\n\t"
        "v_cmp_gt_u32 vcc, s47, v126\n\t"
        "s_cbranch_vccnz 8b\n\t"
        "s_branch 21b\n\t"
        "22:\n\t"
        "s_mov_b32 s48, 0x10000\n\t"
        "8:\n\t"
        "ds_read_b32 v126, v125\n\t"
        "s_sub_u32 s48, s48, 1\n\t"
        "s_waitcnt lgkmcnt(0)\n\t"
        "s_cbranch_scc1 23b\n\t"
        "v_cmp_gt_u32 vcc, s47, v126\n\t"
        "s_cbranch_vccnz 8b\n\t"
        "s_branch 23b\n\t"
        "24:\n\t"
        "s_mov_b32 s48, 0x10000\n\t"
        "8:\n\t"
        "ds_read_b32 v126, v125\n\t"
        "s_sub_u32 s48, s48, 1\n\t"
        "s_waitcnt lgkmcnt(0)\n\t"
        "s_cbranch_scc1 25b\n\t"
        "v_cmp_gt_u32 vcc, s47, v126\n\t"
        "s_cbranch_vccnz 8b\n\t"
        "s_branch 25b\n\t"
        "26:\n\t"
        "s_mov_b32 s48, 0x10000\n\t"
        "8:\n\t"
        "ds_read_b32 v126, v125\n\t"
        "s_sub_u32 s48, s48, 1\n\t"
        "s_waitcnt lgkmcnt(0)\n\t"
        "s_cbranch_scc1 27b\n\t"
        "v_cmp_gt_u32 vcc, s47, v126\n\t"
        "s_cbranch_vccnz 8b\n\t"
        "s_branch 27b\n\t"
        "28:\n\t"
        "s_mov_b32 s48, 0x10000\n\t"
        "8:\n\t"
        "ds_read_b32 v126, v125\n\t"
        "s_sub_u32 s48, s48, 1\n\t"
        "s_waitcnt lgkmcnt(0)\n\t"
        "s_cbranch_scc1 29b\n\t"
        "v_cmp_gt_u32 vcc, s47, v126\n\t"
        "s_cbranch_vccnz 8b\n\t"
        "s_branch 29b\n\t"
        "30:\n\t"
        "s_mov_b32 s48, 0x10000\n\t"
        "8:\n\t"
        "ds_read_b32 v126, v125\n\t"
        "s_sub_u32 s48, s48, 1\n\t"
        "s_waitcnt lgkmcnt(0)\n\t"
        "s_cbranch_scc1 31b\n\t"
        "v_cmp_gt_u32 vcc, s47, v126\n\t"
        "s_cbranch_vccnz 8b\n\t"
        "s_branch 31b\n\t"
        "64:\n\t"
        "s_mov_b32 s48, 0x10000\n\t"
        "8:\n\t"
        "ds_read_b32 v126, v125\n\t"
        "s_sub_u32 s48, s48, 1\n\t"
        "s_waitcnt lgkmcnt(0)\n\t"
        "s_cbranch_scc1 65b\n\t"
        "v_cmp_gt_u32 vcc, s47, v126\n\t"
        "s_cbranch_vccnz 8b\n\t"
        "s_branch 65b\n\t"
        "66:\n\t"
        "s_mov_b32 s48, 0x10000\n\t"
        "8:\n\t"
        "ds_read_b32 v126, v125\n\t"
        "s_sub_u32 s48, s48, 1\n\t"
        "s_waitcnt lgkmcnt(0)\n\t"
        "s_cbranch_scc1 67b\n\t"
        "v_cmp_gt_u32 vcc, s47, v126\n\t"
        "s_cbranch_vccnz 8b\n\t"
        "s_branch 67b\n\t"
        "68:\n\t"
        "s_mov_b32 s48, 0x10000\n\t"
        "8:\n\t"
        "ds_read_b32 v126, v125\n\t"
        "s_sub_u32 s48, s48, 1\n\t"
        "s_waitcnt lgkmcnt(0)\n\t"
        "s_cbranch_scc1 69b\n\t"
        "v_cmp_gt_u32 vcc, s47, v126\n\t"
        "s_cbranch_vccnz 8b\n\t"
        "s_branch 69b\n\t"
        "70:\n\t"
        "s_mov_b32 s48, 0x10000\n\t"
        "8:\n\t"
        "ds_read_b32 v126, v125\n\t"
        "s_sub_u32 s48, s48, 1\n\t"
        "s_waitcnt lgkmcnt(0)\n\t"
        "s_cbranch_scc1 71b\n\t"
        "v_cmp_gt_u32 vcc, s47, v126\n\t"
        "s_cbranch_vccnz 8b\n\t"
        "s_branch 71b\n\t"
        "72:\n\t"
        "s_mov_b32 s48, 0x10000\n\t"
        "8:\n\t"
        "ds_read_b32 v126, v125\n\t"
        "s_sub_u32 s48, s48, 1\n\t"
        "s_waitcnt lgkmcnt(0)\n\t"
        "s_cbranch_scc1 73b\n\t"
        "v_cmp_gt_u32 vcc, s47, v126\n\t"
        "s_cbranch_vccnz 8b\n\t"
        "s_branch 73b\n\t"
        "9:\n\t"
        : "+{v[0:15]}"(acc[0]), "+{v[16:31]}"(acc[1]), "+{v[32:47]}"(acc[2]), "+{v[48:63]}"(acc[3]), "+{v[64:79]}"(acc[4]), "+{v[80:95]}"(acc[5]),
          "+{v[96:111]}"(stA), "+{v[112:127]}"(stB), "+{v[128:131]}"(stC), "+{v[148:163]}"(xr1), "+{v[164:179]}"(xr2), "+{v[180:195]}"(xr3),
          "+{s44}"(soff), "+{s46}"(slot), "+{s47}"(need)
        : "{s[40:43]}"(srd) : "v132", "v133", "v134", "v135", "v136", "v137", "v138", "v139", "v140", "v141", "v142", "v143", "v144", "v145", "v146", "v147", "s45", "s48", "vcc", "scc", "memory");
      STAMPV(4 + 16 * L);
      PHASE_VARS;
      if (L == 1) {
        f32x4 av[4], bv[4], cs[4][4];
        float cv[2];
#pragma unroll
        for (int g = 0; g < 4; ++g) {
          av[g] = *reinterpret_cast<const f32x4*>(smem + L_AB + wid * 256 + (8 * g + 4 * hi) * 4);
          bv[g] = *reinterpret_cast<const f32x4*>(smem + L_AB + wid * 256 + 128 + (8 * g + 4 * hi) * 4);
        }
#pragma unroll
        for (int j = 5; j >= 0; --j) {
          const int cb = (j >> 1) * 64 + ((j & 1) ^ hh) * 32;
          if (j >= 4) cv[j - 4] = *reinterpret_cast<const float*>(smem + L_CS + (cb + r32) * 4);
          else {
#pragma unroll
            for (int g = 0; g < 4; ++g) cs[j][g] = *reinterpret_cast<const f32x4*>(smem + L_CS + (cb + 8 * g + 4 * hi) * 4);
          }
        }
        SBAR();
#pragma unroll
        for (int j = 5; j >= 0; --j)
#pragma unroll
          for (int g = 0; g < 4; ++g)
#pragma unroll
            for (int e = 0; e < 4; ++e)
              acc[j][4 * g + e] = j < 4 ? al * acc[j][4 * g + e] + be * cs[j & 3][g][e] : av[g][e] * acc[j][4 * g + e] + bv[g][e] * cv[j & 1];
        SBAR();
        lds_wait_ge(lbase + L_W + 4 + 4 * (lane & 1), 1);
        const int idx = (int)__builtin_amdgcn_readfirstlane(words[3]);
        const bool mine = (idx >> 5) == tile;
        if (mine) {
#pragma unroll
          for (int j = 0; j < 6; ++j) {
            const int cb = (j >> 1) * 64 + ((j & 1) ^ hh) * 32;
#pragma unroll
            for (int g = 0; g < 4; ++g) {
              if (j < 4) {
                const f32x4 pv = *reinterpret_cast<const f32x4*>(smem + L_PATCH + (cb + 8 * g + 4 * hi) * 4);
#pragma unroll
                for (int e = 0; e < 4; ++e) acc[j][4 * g + e] = r32 == (idx & 31) ? pv[e] : acc[j][4 * g + e];
              } else {
                const float pv = *reinterpret_cast<const float*>(smem + L_PATCH + (cb + r32) * 4);
#pragma unroll
                for (int e = 0; e < 4; ++e) acc[j][4 * g + e] = (8 * g + 4 * hi + e) == (idx & 31) ? pv : acc[j][4 * g + e];
              }
            }
          }
        }
      }
      constexpr float QS = 0.17677669529663687f * 1.4426950408889634f;
      u32x4 own[4];
#pragma unroll
      for (int j = 5; j >= 0; --j) {
        u32x4 o[2];
#pragma unroll
        for (int e = 0; e < 8; ++e) {
          unsigned w = pack2(acc[j][2 * e], acc[j][2 * e + 1]);
          if (j < 2) w = pack2(lo16(w) * QS, hi16(w) * QS);
          o[e >> 2][e & 3] = w;
        }
        if (j >= 4) { o[0] = fo_swap(o[0]); o[1] = fo_swap(o[1]); }
        const int hs = (j & 1) ^ hh;
        if (j >= 2) {
          const unsigned dst = OFF_KV + L * (10 * MBY) + (tn * 2 + hs) * HS + tile * 4096 + ((j >> 1) - 1) * 2048 + lane * 16;
          stg(rs, dst, o[0]); stg(rs, dst + 1024, o[1]);
        }
        if (j >= 2 && !(j & 1)) { own[j - 2] = o[0]; own[j - 1] = o[1]; }
        else {
          char* p = smem + L_XCH + (j < 2 ? (hs * 4 + wid) * 2048 : 16384 + wid * 4096 + ((j >> 1) - 1) * 2048) + lane * 16;
          *reinterpret_cast<u32x4*>(p) = o[0]; *reinterpret_cast<u32x4*>(p + 1024) = o[1];
        }
        SBAR();
      }
      asm volatile(
        "v_mov_b64 v[0:1], 0\n\t"
        "v_mov_b64 v[2:3], 0\n\t"
        "v_mov_b64 v[4:5], 0\n\t"
        "v_mov_b64 v[6:7], 0\n\t"
        "v_mov_b64 v[8:9], 0\n\t"
        "v_mov_b64 v[10:11], 0\n\t"
        "v_mov_b64 v[12:13], 0\n\t"
        "v_mov_b64 v[14:15], 0\n\t"
        "v_mov_b64 v[16:17], 0\n\t"
        "v_mov_b64 v[18:19], 0\n\t"
        "v_mov_b64 v[20:21], 0\n\t"
        "v_mov_b64 v[22:23], 0\n\t"
        "v_mov_b64 v[24:25], 0\n\t"
        "v_mov_b64 v[26:27], 0\n\t"
        "v_mov_b64 v[28:29], 0\n\t"
        "v_mov_b64 v[30:31], 0\n\t"
        "v_mov_b64 v[32:33], 0\n\t"
        "v_mov_b64 v[34:35], 0\n\t"
        "v_mov_b64 v[36:37], 0\n\t"
        "v_mov_b64 v[38:39], 0\n\t"
        "v_mov_b64 v[40:41], 0\n\t"
        "v_mov_b64 v[42:43], 0\n\t"
        "v_mov_b64 v[44:45], 0\n\t"
        "v_mov_b64 v[46:47], 0\n\t"
        "v_mov_b64 v[48:49], 0\n\t"
        "v_mov_b64 v[50:51], 0\n\t"
        "v_mov_b64 v[52:53], 0\n\t"
        "v_mov_b64 v[54:55], 0\n\t"
        "v_mov_b64 v[56:57], 0\n\t"
        "v_mov_b64 v[58:59], 0\n\t"
        "v_mov_b64 v[60:61], 0\n\t"
        "v_mov_b64 v[62:63], 0\n\t"
        "v_mov_b64 v[128:129], 0\n\t"
        "v_mov_b64 v[130:131], 0\n\t"
        "v_mov_b64 v[132:133], 0\n\t"
        "v_mov_b64 v[134:135], 0\n\t"
        "v_mov_b64 v[136:137], 0\n\t"
        "v_mov_b64 v[138:139], 0\n\t"
        "v_mov_b64 v[140:141], 0\n\t"
        "v_mov_b64 v[142:143], 0\n\t"
        : "={v[0:15]}"(oz[0]), "={v[16:31]}"(oz[1]), "={v[32:47]}"(oz[2]), "={v[48:63]}"(oz[3]), "={v[128:143]}"(oz[4]));
      BAR_ALL();
      kva = cat4(own[0], own[1], own[2], own[3]);
      const u32x4* pk = reinterpret_cast<const u32x4*>(smem + L_XCH + 16384 + (wid ^ 1) * 4096 + lane * 16);
#pragma unroll
      for (int u = 0; u < 4; ++u) {
        const u32x4* p = reinterpret_cast<const u32x4*>(smem + L_XCH + (hh * 4 + ((2 * kh + 2 + u) & 3)) * 2048 + lane * 16);
        q[u] = __builtin_shufflevector(p[0], p[64], 0, 1, 2, 3, 4, 5, 6, 7);
      }
      kvb = cat4(pk[0], pk[64], pk[128], pk[192]);
    }
    STAMPV(5 + 16 * L);
    {
      u32x4 ones;
      unsigned kvoff;
      u32x8 fv, cv;
      asm volatile("" : "={v[184:191]}"(fv), "={v[216:223]}"(cv));
      { PHASE_VARS;
        const unsigned v = (lane & 3) == 0 ? 0x3F803F80u : 0u; const bool up = (lane & 4) != 0; ones[0] = ones[1] = up ? 0u : v; ones[2] = ones[3] = up ? v : 0u;
        kvoff = OFF_KV + L * (10 * MBY) + (tn * 2 + hh) * HS + kh * 8192 + lane * 16;
        fv[1] = lbase + L_FREE + wid * 256 + lane * 4; fv[2] = 32 * L + 32;
        cv[0] = lbase + L_XCH + wid * 8192 + lane * 16; cv[1] = lbase + L_XCH + (wid ^ 2) * 8192 + lane * 16;
        cv[2] = lbase + L_XCH + 32768 + wid * 512 + lane * 4; cv[3] = lbase + L_XCH + 32768 + (wid ^ 2) * 512 + lane * 4;
        cv[4] = lbase + L_W + 48 + wid * 4; cv[5] = lbase + L_W + 48 + (wid ^ 2) * 4; cv[6] = L + 1;
        cv[7] = ((tile * 32 + r32) * D_ + tn * 64 + hi * 8) * 2; }
      f32x16 o0 = oz[0], o1 = oz[1], o2 = oz[2], o3 = oz[3], lv = oz[4];
      unsigned soff = tm * 16384;
      STAMP_DECL;
      asm volatile(
        "s_nop 1\n\t"
        "v_mfma_f32_32x32x16_bf16 v[96:111], v[152:155], v[64:67], 0\n\t"
        "v_mfma_f32_32x32x16_bf16 v[96:111], v[156:159], v[68:71], v[96:111]\n\t"
        "s_mov_b32 s46, 0\n\t"
        "ds_write_b32 v185, v186\n\t"
        "s_nop 9\n\t"
        "v_exp_f32 v96, v96\n\t"
        "v_exp_f32 v97, v97\n\t"
        "v_exp_f32 v98, v98\n\t"
        "v_exp_f32 v99, v99\n\t"
        "v_exp_f32 v100, v100\n\t"
        "v_exp_f32 v101, v101\n\t"
        "v_mfma_f32_32x32x16_bf16 v[112:127], v[152:155], v[72:75], 0\n\t"
        "v_exp_f32 v102, v102\n\t"
        "v_exp_f32 v103, v103\n\t"
        "v_exp_f32 v104, v104\n\t"
        "v_mfma_f32_32x32x16_bf16 v[112:127], v[156:159], v[76:79], v[112:127]\n\t"
        "v_exp_f32 v105, v105\n\t"
        "v_exp_f32 v106, v106\n\t"
        "v_exp_f32 v107, v107\n\t"
        "v_smfmac_f32_32x32x32_bf16 v[0:15], v[160:163], v[96:103], v148\n\t"
        "v_exp_f32 v108, v108\n\t"
        "v_exp_f32 v109, v109\n\t"
        "v_exp_f32 v110, v110\n\t"
        "v_smfmac_f32_16x16x64_bf16 v[128:131], v[144:147], v[96:103], v148\n\t"
        "v_exp_f32 v111, v111\n\t"
        "v_exp_f32 v112, v112\n\t"
        "v_exp_f32 v113, v113\n\t"
        "v_smfmac_f32_32x32x32_bf16 v[0:15], v[164:167], v[104:111], v148\n\t"
        "v_exp_f32 v114, v114\n\t"
        "v_exp_f32 v115, v115\n\t"
        "v_exp_f32 v116, v116\n\t"
        "v_smfmac_f32_16x16x64_bf16 v[128:131], v[144:147], v[104:111], v148\n\t"
        "v_exp_f32 v117, v117\n\t"
        "v_mfma_f32_32x32x16_bf16 v[96:111], v[152:155], v[80:83], 0\n\t"
        "v_exp_f32 v118, v118\n\t"
        "v_exp_f32 v119, v119\n\t"
        "v_exp_f32 v120, v120\n\t"
        "v_mfma_f32_32x32x16_bf16 v[96:111], v[156:159], v[84:87], v[96:111]\n\t"
        "v_exp_f32 v121, v121\n\t"
        "v_exp_f32 v122, v122\n\t"
        "v_exp_f32 v123, v123\n\t"
        "v_smfmac_f32_32x32x32_bf16 v[16:31], v[160:163], v[112:119], v148\n\t"
        "v_exp_f32 v124, v124\n\t"
        "v_exp_f32 v125, v125\n\t"
        "v_exp_f32 v126, v126\n\t"
        "v_smfmac_f32_16x16x64_bf16 v[132:135], v[144:147], v[112:119], v148\n\t"
        "v_exp_f32 v127, v127\n\t"
        "v_exp_f32 v96, v96\n\t"
        "v_exp_f32 v97, v97\n\t"
        "v_smfmac_f32_32x32x32_bf16 v[16:31], v[164:167], v[120:127], v148\n\t"
        "v_exp_f32 v98, v98\n\t"
        "v_exp_f32 v99, v99\n\t"
        "v_exp_f32 v100, v100\n\t"
        "v_smfmac_f32_16x16x64_bf16 v[132:135], v[144:147], v[120:127], v148\n\t"
        "v_exp_f32 v101, v101\n\t"
        "v_mfma_f32_32x32x16_bf16 v[112:127], v[152:155], v[88:91], 0\n\t"
        "v_exp_f32 v102, v102\n\t"
        "v_exp_f32 v103, v103\n\t"
        "v_exp_f32 v104, v104\n\t"
        "v_mfma_f32_32x32x16_bf16 v[112:127], v[156:159], v[92:95], v[112:127]\n\t"
        "v_exp_f32 v105, v105\n\t"
        "v_exp_f32 v106, v106\n\t"
        "v_exp_f32 v107, v107\n\t"
        "v_smfmac_f32_32x32x32_bf16 v[32:47], v[160:163], v[96:103], v148\n\t"
        "v_exp_f32 v108, v108\n\t"
        "v_exp_f32 v109, v109\n\t"
        "v_exp_f32 v110, v110\n\t"
        "v_smfmac_f32_16x16x64_bf16 v[136:139], v[144:147], v[96:103], v148\n\t"
        "v_exp_f32 v111, v111\n\t"
        "v_exp_f32 v112, v112\n\t"
        "v_exp_f32 v113, v113\n\t"
        "v_smfmac_f32_32x32x32_bf16 v[32:47], v[164:167], v[104:111], v148\n\t"
        "v_exp_f32 v114, v114\n\t"
        "v_exp_f32 v115, v115\n\t"
        "v_exp_f32 v116, v116\n\t"
        "v_smfmac_f32_16x16x64_bf16 v[136:139], v[144:147], v[104:111], v148\n\t"
        "v_exp_f32 v117, v117\n\t"
        "v_mfma_f32_32x32x16_bf16 v[96:111], v[168:171], v[64:67], 0\n\t"
        "v_exp_f32 v118, v118\n\t"
        "v_exp_f32 v119, v119\n\t"
        "v_exp_f32 v120, v120\n\t"
        "v_mfma_f32_32x32x16_bf16 v[96:111], v[172:175], v[68:71], v[96:111]\n\t"
        "v_exp_f32 v121, v121\n\t"
        "v_exp_f32 v122, v122\n\t"
        "v_exp_f32 v123, v123\n\t"
        "v_smfmac_f32_32x32x32_bf16 v[48:63], v[160:163], v[112:119], v148\n\t"
        "v_exp_f32 v124, v124\n\t"
        "v_exp_f32 v125, v125\n\t"
        "v_exp_f32 v126, v126\n\t"
        "v_smfmac_f32_16x16x64_bf16 v[140:143], v[144:147], v[112:119], v148\n\t"
        "v_exp_f32 v127, v127\n\t"
        "v_exp_f32 v96, v96\n\t"
        "v_exp_f32 v97, v97\n\t"
        "v_smfmac_f32_32x32x32_bf16 v[48:63], v[164:167], v[120:127], v148\n\t"
        "v_exp_f32 v98, v98\n\t"
        "v_exp_f32 v99, v99\n\t"
        "v_exp_f32 v100, v100\n\t"
        "v_smfmac_f32_16x16x64_bf16 v[140:143], v[144:147], v[120:127], v148\n\t"
        "v_exp_f32 v101, v101\n\t"
        "v_mfma_f32_32x32x16_bf16 v[112:127], v[168:171], v[72:75], 0\n\t"
        "v_exp_f32 v102, v102\n\t"
        "v_exp_f32 v103, v103\n\t"
        "v_exp_f32 v104, v104\n\t"
        "ds_read_b32 v151, v224\n\t"
        "v_mfma_f32_32x32x16_bf16 v[112:127], v[172:175], v[76:79], v[112:127]\n\t"
        "v_exp_f32 v105, v105\n\t"
        "v_exp_f32 v106, v106\n\t"
        "v_exp_f32 v107, v107\n\t"
        "v_smfmac_f32_32x32x32_bf16 v[0:15], v[176:179], v[96:103], v148\n\t"
        "v_exp_f32 v108, v108\n\t"
        "v_exp_f32 v109, v109\n\t"
        "v_exp_f32 v110, v110\n\t"
        "v_smfmac_f32_16x16x64_bf16 v[128:131], v[144:147], v[96:103], v148\n\t"
        "v_exp_f32 v111, v111\n\t"
        "s_cmp_lg_u32 s46, 0\n\t"
        "s_cbranch_scc1 31f\n\t"
        "s_waitcnt lgkmcnt(0)\n\t"
        "v_cmp_ne_u32 vcc, v222, v151\n\t"
        "s_cbranch_vccz 30f\n\t"
        "31:\n\t"
        "v_exp_f32 v112, v112\n\t"
        "v_exp_f32 v113, v113\n\t"
        "v_smfmac_f32_32x32x32_bf16 v[0:15], v[180:183], v[104:111], v148\n\t"
        "v_exp_f32 v114, v114\n\t"
        "v_exp_f32 v115, v115\n\t"
        "v_exp_f32 v116, v116\n\t"
        "v_smfmac_f32_16x16x64_bf16 v[128:131], v[144:147], v[104:111], v148\n\t"
        "v_exp_f32 v117, v117\n\t"
        "v_mfma_f32_32x32x16_bf16 v[96:111], v[168:171], v[80:83], 0\n\t"
        "v_exp_f32 v118, v118\n\t"
        "v_exp_f32 v119, v119\n\t"
        "v_exp_f32 v120, v120\n\t"
        "ds_read_b32 v151, v224\n\t"
        "v_mfma_f32_32x32x16_bf16 v[96:111], v[172:175], v[84:87], v[96:111]\n\t"
        "v_exp_f32 v121, v121\n\t"
        "v_exp_f32 v122, v122\n\t"
        "v_exp_f32 v123, v123\n\t"
        "v_smfmac_f32_32x32x32_bf16 v[16:31], v[176:179], v[112:119], v148\n\t"
        "v_exp_f32 v124, v124\n\t"
        "v_exp_f32 v125, v125\n\t"
        "v_exp_f32 v126, v126\n\t"
        "v_smfmac_f32_16x16x64_bf16 v[132:135], v[144:147], v[112:119], v148\n\t"
        "v_exp_f32 v127, v127\n\t"
        "s_cmp_lg_u32 s46, 0\n\t"
        "s_cbranch_scc1 33f\n\t"
        "s_waitcnt lgkmcnt(0)\n\t"
        "v_cmp_ne_u32 vcc, v222, v151\n\t"
        "s_cbranch_vccz 32f\n\t"
        "33:\n\t"
        "v_exp_f32 v96, v96\n\t"
        "v_exp_f32 v97, v97\n\t"
        "v_smfmac_f32_32x32x32_bf16 v[16:31], v[180:183], v[120:127], v148\n\t"
        "v_exp_f32 v98, v98\n\t"
        "v_exp_f32 v99, v99\n\t"
        "v_exp_f32 v100, v100\n\t"
        "v_smfmac_f32_16x16x64_bf16 v[132:135], v[144:147], v[120:127], v148\n\t"
        "v_exp_f32 v101, v101\n\t"
        "v_mfma_f32_32x32x16_bf16 v[112:127], v[168:171], v[88:91], 0\n\t"
        "v_exp_f32 v102, v102\n\t"
        "v_exp_f32 v103, v103\n\t"
        "v_exp_f32 v104, v104\n\t"
        "v_mfma_f32_32x32x16_bf16 v[112:127], v[172:175], v[92:95], v[112:127]\n\t"
        "v_exp_f32 v105, v105\n\t"
        "v_exp_f32 v106, v106\n\t"
        "v_exp_f32 v107, v107\n\t"
        "v_smfmac_f32_32x32x32_bf16 v[32:47], v[176:179], v[96:103], v148\n\t"
        "v_exp_f32 v108, v108\n\t"
        "v_exp_f32 v109, v109\n\t"
        "v_exp_f32 v110, v110\n\t"
        "v_smfmac_f32_16x16x64_bf16 v[136:139], v[144:147], v[96:103], v148\n\t"
        "v_exp_f32 v111, v111\n\t"
        "s_cmp_lg_u32 s46, 0\n\t"
        "s_cbranch_scc1 5f\n\t"
        "s_mov_b32 s45, 0x200000\n\t"
        "2:\n\t"
        "ds_read_b32 v151, v224\n\t"
        "s_sub_u32 s45, s45, 1\n\t"
        "s_waitcnt lgkmcnt(0)\n\t"
        "s_cbranch_scc1 3f\n\t"
        "v_cmp_ne_u32 vcc, v222, v151\n\t"
        "s_cbranch_vccnz 2b\n\t"
        "3:\n\t"
        "s_add_u32 s44, s44, 0x4000\n\t"
        "s_and_b32 s44, s44, 0x3ffff\n\t"
        "buffer_load_dwordx4 v[184:187], v149, s[40:43], s44 offen offset:0 sc1\n\t"
        "buffer_load_dwordx4 v[188:191], v149, s[40:43], s44 offen offset:1024 sc1\n\t"
        "buffer_load_dwordx4 v[192:195], v149, s[40:43], s44 offen offset:2048 sc1\n\t"
        "buffer_load_dwordx4 v[196:199], v149, s[40:43], s44 offen offset:3072 sc1\n\t"
        "buffer_load_dwordx4 v[200:203], v150, s[40:43], s44 offen offset:0 sc1\n\t"
        "buffer_load_dwordx4 v[204:207], v150, s[40:43], s44 offen offset:1024 sc1\n\t"
        "buffer_load_dwordx4 v[208:211], v150, s[40:43], s44 offen offset:2048 sc1\n\t"
        "buffer_load_dwordx4 v[212:215], v150, s[40:43], s44 offen offset:3072 sc1\n\t"
        "s_add_u32 s44, s44, 0x4000\n\t"
        "s_and_b32 s44, s44, 0x3ffff\n\t"
        "buffer_load_dwordx4 v[152:155], v149, s[40:43], s44 offen offset:0 sc1\n\t"
        "buffer_load_dwordx4 v[156:159], v149, s[40:43], s44 offen offset:1024 sc1\n\t"
        "buffer_load_dwordx4 v[160:163], v149, s[40:43], s44 offen offset:2048 sc1\n\t"
        "buffer_load_dwordx4 v[164:167], v149, s[40:43], s44 offen offset:3072 sc1\n\t"
        "5:\n\t"
        "s_mov_b32 s45, 7\n\t"
        "1:\n\t"
        "v_exp_f32 v112, v112\n\t"
        "v_exp_f32 v113, v113\n\t"
        "v_smfmac_f32_32x32x32_bf16 v[32:47], v[180:183], v[104:111], v148\n\t"
        "v_exp_f32 v114, v114\n\t"
        "v_exp_f32 v115, v115\n\t"
        "v_exp_f32 v116, v116\n\t"
        "v_smfmac_f32_16x16x64_bf16 v[136:139], v[144:147], v[104:111], v148\n\t"
        "v_exp_f32 v117, v117\n\t"
        "s_waitcnt vmcnt(8)\n\t"
        "v_mfma_f32_32x32x16_bf16 v[96:111], v[184:187], v[64:67], 0\n\t"
        "v_exp_f32 v118, v118\n\t"
        "v_exp_f32 v119, v119\n\t"
        "v_exp_f32 v120, v120\n\t"
        "v_mfma_f32_32x32x16_bf16 v[96:111], v[188:191], v[68:71], v[96:111]\n\t"
        "v_exp_f32 v121, v121\n\t"
        "v_exp_f32 v122, v122\n\t"
        "v_exp_f32 v123, v123\n\t"
        "v_smfmac_f32_32x32x32_bf16 v[48:63], v[176:179], v[112:119], v148\n\t"
        "v_exp_f32 v124, v124\n\t"
        "v_exp_f32 v125, v125\n\t"
        "v_exp_f32 v126, v126\n\t"
        "v_smfmac_f32_16x16x64_bf16 v[140:143], v[144:147], v[112:119], v148\n\t"
        "buffer_load_dwordx4 v[168:171], v150, s[40:43], s44 offen offset:0 sc1\n\t"
        "v_exp_f32 v127, v127\n\t"
        "v_exp_f32 v96, v96\n\t"
        "v_exp_f32 v97, v97\n\t"
        "v_smfmac_f32_32x32x32_bf16 v[48:63], v[180:183], v[120:127], v148\n\t"
        "v_exp_f32 v98, v98\n\t"
        "v_exp_f32 v99, v99\n\t"
        "v_exp_f32 v100, v100\n\t"
        "v_smfmac_f32_16x16x64_bf16 v[140:143], v[144:147], v[120:127], v148\n\t"
        "v_exp_f32 v101, v101\n\t"
        "v_mfma_f32_32x32x16_bf16 v[112:127], v[184:187], v[72:75], 0\n\t"
        "v_exp_f32 v102, v102\n\t"
        "v_exp_f32 v103, v103\n\t"
        "v_exp_f32 v104, v104\n\t"
        "v_mfma_f32_32x32x16_bf16 v[112:127], v[188:191], v[76:79], v[112:127]\n\t"
        "v_exp_f32 v105, v105\n\t"
        "v_exp_f32 v106, v106\n\t"
        "v_exp_f32 v107, v107\n\t"
        "v_smfmac_f32_32x32x32_bf16 v[0:15], v[192:195], v[96:103], v148\n\t"
        "v_exp_f32 v108, v108\n\t"
        "v_exp_f32 v109, v109\n\t"
        "v_exp_f32 v110, v110\n\t"
        "v_smfmac_f32_16x16x64_bf16 v[128:131], v[144:147], v[96:103], v148\n\t"
        "buffer_load_dwordx4 v[172:175], v150, s[40:43], s44 offen offset:1024 sc1\n\t"
        "v_exp_f32 v111, v111\n\t"
        "v_exp_f32 v112, v112\n\t"
        "v_exp_f32 v113, v113\n\t"
        "v_smfmac_f32_32x32x32_bf16 v[0:15], v[196:199], v[104:111], v148\n\t"
        "v_exp_f32 v114, v114\n\t"
        "v_exp_f32 v115, v115\n\t"
        "v_exp_f32 v116, v116\n\t"
        "v_smfmac_f32_16x16x64_bf16 v[128:131], v[144:147], v[104:111], v148\n\t"
        "v_exp_f32 v117, v117\n\t"
        "v_mfma_f32_32x32x16_bf16 v[96:111], v[184:187], v[80:83], 0\n\t"
        "v_exp_f32 v118, v118\n\t"
        "v_exp_f32 v119, v119\n\t"
        "v_exp_f32 v120, v120\n\t"
        "v_mfma_f32_32x32x16_bf16 v[96:111], v[188:191], v[84:87], v[96:111]\n\t"
        "v_exp_f32 v121, v121\n\t"
        "v_exp_f32 v122, v122\n\t"
        "v_exp_f32 v123, v123\n\t"
        "v_smfmac_f32_32x32x32_bf16 v[16:31], v[192:195], v[112:119], v148\n\t"
        "v_exp_f32 v124, v124\n\t"
        "v_exp_f32 v125, v125\n\t"
        "v_exp_f32 v126, v126\n\t"
        "v_smfmac_f32_16x16x64_bf16 v[132:135], v[144:147], v[112:119], v148\n\t"
        "buffer_load_dwordx4 v[176:179], v150, s[40:43], s44 offen offset:2048 sc1\n\t"
        "v_exp_f32 v127, v127\n\t"
        "v_exp_f32 v96, v96\n\t"
        "v_exp_f32 v97, v97\n\t"
        "v_smfmac_f32_32x32x32_bf16 v[16:31], v[196:199], v[120:127], v148\n\t"
        "v_exp_f32 v98, v98\n\t"
        "v_exp_f32 v99, v99\n\t"
        "v_exp_f32 v100, v100\n\t"
        "v_smfmac_f32_16x16x64_bf16 v[132:135], v[144:147], v[120:127], v148\n\t"
        "v_exp_f32 v101, v101\n\t"
        "v_mfma_f32_32x32x16_bf16 v[112:127], v[184:187], v[88:91], 0\n\t"
        "v_exp_f32 v102, v102\n\t"
        "v_exp_f32 v103, v103\n\t"
        "v_exp_f32 v104, v104\n\t"
        "v_mfma_f32_32x32x16_bf16 v[112:127], v[188:191], v[92:95], v[112:127]\n\t"
        "v_exp_f32 v105, v105\n\t"
        "v_exp_f32 v106, v106\n\t"
        "v_exp_f32 v107, v107\n\t"
        "v_smfmac_f32_32x32x32_bf16 v[32:47], v[192:195], v[96:103], v148\n\t"
        "v_exp_f32 v108, v108\n\t"
        "v_exp_f32 v109, v109\n\t"
        "v_exp_f32 v110, v110\n\t"
        "v_smfmac_f32_16x16x64_bf16 v[136:139], v[144:147], v[96:103], v148\n\t"
        "buffer_load_dwordx4 v[180:183], v150, s[40:43], s44 offen offset:3072 sc1\n\t"
        "v_exp_f32 v111, v111\n\t"
        "v_exp_f32 v112, v112\n\t"
        "v_exp_f32 v113, v113\n\t"
        "v_smfmac_f32_32x32x32_bf16 v[32:47], v[196:199], v[104:111], v148\n\t"
        "s_add_u32 s44, s44, 0x4000\n\t"
        "s_and_b32 s44, s44, 0x3ffff\n\t"
        "v_exp_f32 v114, v114\n\t"
        "v_exp_f32 v115, v115\n\t"
        "v_exp_f32 v116, v116\n\t"
        "v_smfmac_f32_16x16x64_bf16 v[136:139], v[144:147], v[104:111], v148\n\t"
        "v_exp_f32 v117, v117\n\t"
        "s_waitcnt vmcnt(8)\n\t"
        "v_mfma_f32_32x32x16_bf16 v[96:111], v[200:203], v[64:67], 0\n\t"
        "v_exp_f32 v118, v118\n\t"
        "v_exp_f32 v119, v119\n\t"
        "v_exp_f32 v120, v120\n\t"
        "v_mfma_f32_32x32x16_bf16 v[96:111], v[204:207], v[68:71], v[96:111]\n\t"
        "v_exp_f32 v121, v121\n\t"
        "v_exp_f32 v122, v122\n\t"
        "v_exp_f32 v123, v123\n\t"
        "v_smfmac_f32_32x32x32_bf16 v[48:63], v[192:195], v[112:119], v148\n\t"
        "v_exp_f32 v124, v124\n\t"
        "v_exp_f32 v125, v125\n\t"
        "v_exp_f32 v126, v126\n\t"
        "v_smfmac_f32_16x16x64_bf16 v[140:143], v[144:147], v[112:119], v148\n\t"
        "buffer_load_dwordx4 v[184:187], v149, s[40:43], s44 offen offset:0 sc1\n\t"
        "v_exp_f32 v127, v127\n\t"
        "v_exp_f32 v96, v96\n\t"
        "v_exp_f32 v97, v97\n\t"
        "v_smfmac_f32_32x32x32_bf16 v[48:63], v[196:199], v[120:127], v148\n\t"
        "v_exp_f32 v98, v98\n\t"
        "v_exp_f32 v99, v99\n\t"
        "v_exp_f32 v100, v100\n\t"
        "v_smfmac_f32_16x16x64_bf16 v[140:143], v[144:147], v[120:127], v148\n\t"
        "v_exp_f32 v101, v101\n\t"
        "v_mfma_f32_32x32x16_bf16 v[112:127], v[200:203], v[72:75], 0\n\t"
        "v_exp_f32 v102, v102\n\t"
        "v_exp_f32 v103, v103\n\t"
        "v_exp_f32 v104, v104\n\t"
        "v_mfma_f32_32x32x16_bf16 v[112:127], v[204:207], v[76:79], v[112:127]\n\t"
        "v_exp_f32 v105, v105\n\t"
        "v_exp_f32 v106, v106\n\t"
        "v_exp_f32 v107, v107\n\t"
        "v_smfmac_f32_32x32x32_bf16 v[0:15], v[208:211], v[96:103], v148\n\t"
        "v_exp_f32 v108, v108\n\t"
        "v_exp_f32 v109, v109\n\t"
        "v_exp_f32 v110, v110\n\t"
        "v_smfmac_f32_16x16x64_bf16 v[128:131], v[144:147], v[96:103], v148\n\t"
        "buffer_load_dwordx4 v[188:191], v149, s[40:43], s44 offen offset:1024 sc1\n\t"
        "v_exp_f32 v111, v111\n\t"
        "v_exp_f32 v112, v112\n\t"
        "v_exp_f32 v113, v113\n\t"
        "v_smfmac_f32_32x32x32_bf16 v[0:15], v[212:215], v[104:111], v148\n\t"
        "v_exp_f32 v114, v114\n\t"
        "v_exp_f32 v115, v115\n\t"
        "v_exp_f32 v116, v116\n\t"
        "v_smfmac_f32_16x16x64_bf16 v[128:131], v[144:147], v[104:111], v148\n\t"
        "v_exp_f32 v117, v117\n\t"
        "v_mfma_f32_32x32x16_bf16 v[96:111], v[200:203], v[80:83], 0\n\t"
        "v_exp_f32 v118, v118\n\t"
        "v_exp_f32 v119, v119\n\t"
        "v_exp_f32 v120, v120\n\t"
        "v_mfma_f32_32x32x16_bf16 v[96:111], v[204:207], v[84:87], v[96:111]\n\t"
        "v_exp_f32 v121, v121\n\t"
        "v_exp_f32 v122, v122\n\t"
        "v_exp_f32 v123, v123\n\t"
        "v_smfmac_f32_32x32x32_bf16 v[16:31], v[208:211], v[112:119], v148\n\t"
        "v_exp_f32 v124, v124\n\t"
        "v_exp_f32 v125, v125\n\t"
        "v_exp_f32 v126, v126\n\t"
        "v_smfmac_f32_16x16x64_bf16 v[132:135], v[144:147], v[112:119], v148\n\t"
        "buffer_load_dwordx4 v[192:195], v149, s[40:43], s44 offen offset:2048 sc1\n\t"
        "v_exp_f32 v127, v127\n\t"
        "v_exp_f32 v96, v96\n\t"
        "v_exp_f32 v97, v97\n\t"
        "v_smfmac_f32_32x32x32_bf16 v[16:31], v[212:215], v[120:127], v148\n\t"
        "v_exp_f32 v98, v98\n\t"
        "v_exp_f32 v99, v99\n\t"
        "v_exp_f32 v100, v100\n\t"
        "v_smfmac_f32_16x16x64_bf16 v[132:135], v[144:147], v[120:127], v148\n\t"
        "v_exp_f32 v101, v101\n\t"
        "v_mfma_f32_32x32x16_bf16 v[112:127], v[200:203], v[88:91], 0\n\t"
        "v_exp_f32 v102, v102\n\t"
        "v_exp_f32 v103, v103\n\t"
        "v_exp_f32 v104, v104\n\t"
        "v_mfma_f32_32x32x16_bf16 v[112:127], v[204:207], v[92:95], v[112:127]\n\t"
        "v_exp_f32 v105, v105\n\t"
        "v_exp_f32 v106, v106\n\t"
        "v_exp_f32 v107, v107\n\t"
        "v_smfmac_f32_32x32x32_bf16 v[32:47], v[208:211], v[96:103], v148\n\t"
        "v_exp_f32 v108, v108\n\t"
        "v_exp_f32 v109, v109\n\t"
        "v_exp_f32 v110, v110\n\t"
        "v_smfmac_f32_16x16x64_bf16 v[136:139], v[144:147], v[96:103], v148\n\t"
        "buffer_load_dwordx4 v[196:199], v149, s[40:43], s44 offen offset:3072 sc1\n\t"
        "v_exp_f32 v111, v111\n\t"
        "v_exp_f32 v112, v112\n\t"
        "v_exp_f32 v113, v113\n\t"
        "v_smfmac_f32_32x32x32_bf16 v[32:47], v[212:215], v[104:111], v148\n\t"
        "v_exp_f32 v114, v114\n\t"
        "v_exp_f32 v115, v115\n\t"
        "v_exp_f32 v116, v116\n\t"
        "v_smfmac_f32_16x16x64_bf16 v[136:139], v[144:147], v[104:111], v148\n\t"
        "v_exp_f32 v117, v117\n\t"
        "s_waitcnt vmcnt(8)\n\t"
        "v_mfma_f32_32x32x16_bf16 v[96:111], v[152:155], v[64:67], 0\n\t"
        "v_exp_f32 v118, v118\n\t"
        "v_exp_f32 v119, v119\n\t"
        "v_exp_f32 v120, v120\n\t"
        "v_mfma_f32_32x32x16_bf16 v[96:111], v[156:159], v[68:71], v[96:111]\n\t"
        "v_exp_f32 v121, v121\n\t"
        "v_exp_f32 v122, v122\n\t"
        "v_exp_f32 v123, v123\n\t"
        "v_smfmac_f32_32x32x32_bf16 v[48:63], v[208:211], v[112:119], v148\n\t"
        "v_exp_f32 v124, v124\n\t"
        "v_exp_f32 v125, v125\n\t"
        "v_exp_f32 v126, v126\n\t"
        "v_smfmac_f32_16x16x64_bf16 v[140:143], v[144:147], v[112:119], v148\n\t"
        "buffer_load_dwordx4 v[200:203], v150, s[40:43], s44 offen offset:0 sc1\n\t"
        "v_exp_f32 v127, v127\n\t"
        "v_exp_f32 v96, v96\n\t"
        "v_exp_f32 v97, v97\n\t"
        "v_smfmac_f32_32x32x32_bf16 v[48:63], v[212:215], v[120:127], v148\n\t"
        "v_exp_f32 v98, v98\n\t"
        "v_exp_f32 v99, v99\n\t"
        "v_exp_f32 v100, v100\n\t"
        "v_smfmac_f32_16x16x64_bf16 v[140:143], v[144:147], v[120:127], v148\n\t"
        "v_exp_f32 v101, v101\n\t"
        "v_mfma_f32_32x32x16_bf16 v[112:127], v[152:155], v[72:75], 0\n\t"
        "v_exp_f32 v102, v102\n\t"
        "v_exp_f32 v103, v103\n\t"
        "v_exp_f32 v104, v104\n\t"
        "v_mfma_f32_32x32x16_bf16 v[112:127], v[156:159], v[76:79], v[112:127]\n\t"
        "v_exp_f32 v105, v105\n\t"
        "v_exp_f32 v106, v106\n\t"
        "v_exp_f32 v107, v107\n\t"
        "v_smfmac_f32_32x32x32_bf16 v[0:15], v[160:163], v[96:103], v148\n\t"
        "v_exp_f32 v108, v108\n\t"
        "v_exp_f32 v109, v109\n\t"
        "v_exp_f32 v110, v110\n\t"
        "v_smfmac_f32_16x16x64_bf16 v[128:131], v[144:147], v[96:103], v148\n\t"
        "buffer_load_dwordx4 v[204:207], v150, s[40:43], s44 offen offset:1024 sc1\n\t"
        "v_exp_f32 v111, v111\n\t"
        "v_exp_f32 v112, v112\n\t"
        "v_exp_f32 v113, v113\n\t"
        "v_smfmac_f32_32x32x32_bf16 v[0:15], v[164:167], v[104:111], v148\n\t"
        "v_exp_f32 v114, v114\n\t"
        "v_exp_f32 v115, v115\n\t"
        "v_exp_f32 v116, v116\n\t"
        "v_smfmac_f32_16x16x64_bf16 v[128:131], v[144:147], v[104:111], v148\n\t"
        "v_exp_f32 v117, v117\n\t"
        "v_mfma_f32_32x32x16_bf16 v[96:111], v[152:155], v[80:83], 0\n\t"
        "v_exp_f32 v118, v118\n\t"
        "v_exp_f32 v119, v119\n\t"
        "v_exp_f32 v120, v120\n\t"
        "v_mfma_f32_32x32x16_bf16 v[96:111], v[156:159], v[84:87], v[96:111]\n\t"
        "v_exp_f32 v121, v121\n\t"
        "v_exp_f32 v122, v122\n\t"
        "v_exp_f32 v123, v123\n\t"
        "v_smfmac_f32_32x32x32_bf16 v[16:31], v[160:163], v[112:119], v148\n\t"
        "v_exp_f32 v124, v124\n\t"
        "v_exp_f32 v125, v125\n\t"
        "v_exp_f32 v126, v126\n\t"
        "v_smfmac_f32_16x16x64_bf16 v[132:135], v[144:147], v[112:119], v148\n\t"
        "buffer_load_dwordx4 v[208:211], v150, s[40:43], s44 offen offset:2048 sc1\n\t"
        "v_exp_f32 v127, v127\n\t"
        "v_exp_f32 v96, v96\n\t"
        "v_exp_f32 v97, v97\n\t"
        "v_smfmac_f32_32x32x32_bf16 v[16:31], v[164:167], v[120:127], v148\n\t"
        "v_exp_f32 v98, v98\n\t"
        "v_exp_f32 v99, v99\n\t"
        "v_exp_f32 v100, v100\n\t"
        "v_smfmac_f32_16x16x64_bf16 v[132:135], v[144:147], v[120:127], v148\n\t"
        "v_exp_f32 v101, v101\n\t"
        "v_mfma_f32_32x32x16_bf16 v[112:127], v[152:155], v[88:91], 0\n\t"
        "v_exp_f32 v102, v102\n\t"
        "v_exp_f32 v103, v103\n\t"
        "v_exp_f32 v104, v104\n\t"
        "v_mfma_f32_32x32x16_bf16 v[112:127], v[156:159], v[92:95], v[112:127]\n\t"
        "v_exp_f32 v105, v105\n\t"
        "v_exp_f32 v106, v106\n\t"
        "v_exp_f32 v107, v107\n\t"
        "v_smfmac_f32_32x32x32_bf16 v[32:47], v[160:163], v[96:103], v148\n\t"
        "v_exp_f32 v108, v108\n\t"
        "v_exp_f32 v109, v109\n\t"
        "v_exp_f32 v110, v110\n\t"
        "v_smfmac_f32_16x16x64_bf16 v[136:139], v[144:147], v[96:103], v148\n\t"
        "buffer_load_dwordx4 v[212:215], v150, s[40:43], s44 offen offset:3072 sc1\n\t"
        "v_exp_f32 v111, v111\n\t"
        "v_exp_f32 v112, v112\n\t"
        "v_exp_f32 v113, v113\n\t"
        "v_smfmac_f32_32x32x32_bf16 v[32:47], v[164:167], v[104:111], v148\n\t"
        "s_add_u32 s44, s44, 0x4000\n\t"
        "s_and_b32 s44, s44, 0x3ffff\n\t"
        "v_exp_f32 v114, v114\n\t"
        "v_exp_f32 v115, v115\n\t"
        "v_exp_f32 v116, v116\n\t"
        "v_smfmac_f32_16x16x64_bf16 v[136:139], v[144:147], v[104:111], v148\n\t"
        "v_exp_f32 v117, v117\n\t"
        "s_waitcnt vmcnt(8)\n\t"
        "v_mfma_f32_32x32x16_bf16 v[96:111], v[168:171], v[64:67], 0\n\t"
        "v_exp_f32 v118, v118\n\t"
        "v_exp_f32 v119, v119\n\t"
        "v_exp_f32 v120, v120\n\t"
        "v_mfma_f32_32x32x16_bf16 v[96:111], v[172:175], v[68:71], v[96:111]\n\t"
        "v_exp_f32 v121, v121\n\t"
        "v_exp_f32 v122, v122\n\t"
        "v_exp_f32 v123, v123\n\t"
        "v_smfmac_f32_32x32x32_bf16 v[48:63], v[160:163], v[112:119], v148\n\t"
        "v_exp_f32 v124, v124\n\t"
        "v_exp_f32 v125, v125\n\t"
        "v_exp_f32 v126, v126\n\t"
        "v_smfmac_f32_16x16x64_bf16 v[140:143], v[144:147], v[112:119], v148\n\t"
        "buffer_load_dwordx4 v[152:155], v149, s[40:43], s44 offen offset:0 sc1\n\t"
        "v_exp_f32 v127, v127\n\t"
        "v_exp_f32 v96, v96\n\t"
        "v_exp_f32 v97, v97\n\t"
        "v_smfmac_f32_32x32x32_bf16 v[48:63], v[164:167], v[120:127], v148\n\t"
        "v_exp_f32 v98, v98\n\t"
        "v_exp_f32 v99, v99\n\t"
        "v_exp_f32 v100, v100\n\t"
        "v_smfmac_f32_16x16x64_bf16 v[140:143], v[144:147], v[120:127], v148\n\t"
        "v_exp_f32 v101, v101\n\t"
        "v_mfma_f32_32x32x16_bf16 v[112:127], v[168:171], v[72:75], 0\n\t"
        "v_exp_f32 v102, v102\n\t"
        "v_exp_f32 v103, v103\n\t"
        "v_exp_f32 v104, v104\n\t"
        "v_mfma_f32_32x32x16_bf16 v[112:127], v[172:175], v[76:79], v[112:127]\n\t"
        "v_exp_f32 v105, v105\n\t"
        "v_exp_f32 v106, v106\n\t"
        "v_exp_f32 v107, v107\n\t"
        "v_smfmac_f32_32x32x32_bf16 v[0:15], v[176:179], v[96:103], v148\n\t"
        "v_exp_f32 v108, v108\n\t"
        "v_exp_f32 v109, v109\n\t"
        "v_exp_f32 v110, v110\n\t"
        "v_smfmac_f32_16x16x64_bf16 v[128:131], v[144:147], v[96:103], v148\n\t"
        "buffer_load_dwordx4 v[156:159], v149, s[40:43], s44 offen offset:1024 sc1\n\t"
        "v_exp_f32 v111, v111\n\t"
        "v_exp_f32 v112, v112\n\t"
        "v_exp_f32 v113, v113\n\t"
        "v_smfmac_f32_32x32x32_bf16 v[0:15], v[180:183], v[104:111], v148\n\t"
        "v_exp_f32 v114, v114\n\t"
        "v_exp_f32 v115, v115\n\t"
        "v_exp_f32 v116, v116\n\t"
        "v_smfmac_f32_16x16x64_bf16 v[128:131], v[144:147], v[104:111], v148\n\t"
        "v_exp_f32 v117, v117\n\t"
        "v_mfma_f32_32x32x16_bf16 v[96:111], v[168:171], v[80:83], 0\n\t"
        "v_exp_f32 v118, v118\n\t"
        "v_exp_f32 v119, v119\n\t"
        "v_exp_f32 v120, v120\n\t"
        "v_mfma_f32_32x32x16_bf16 v[96:111], v[172:175], v[84:87], v[96:111]\n\t"
        "v_exp_f32 v121, v121\n\t"
        "v_exp_f32 v122, v122\n\t"
        "v_exp_f32 v123, v123\n\t"
        "v_smfmac_f32_32x32x32_bf16 v[16:31], v[176:179], v[112:119], v148\n\t"
        "v_exp_f32 v124, v124\n\t"
        "v_exp_f32 v125, v125\n\t"
        "v_exp_f32 v126, v126\n\t"
        "v_smfmac_f32_16x16x64_bf16 v[132:135], v[144:147], v[112:119], v148\n\t"
        "buffer_load_dwordx4 v[160:163], v149, s[40:43], s44 offen offset:2048 sc1\n\t"
        "v_exp_f32 v127, v127\n\t"
        "v_exp_f32 v96, v96\n\t"
        "v_exp_f32 v97, v97\n\t"
        "v_smfmac_f32_32x32x32_bf16 v[16:31], v[180:183], v[120:127], v148\n\t"
        "v_exp_f32 v98, v98\n\t"
        "v_exp_f32 v99, v99\n\t"
        "v_exp_f32 v100, v100\n\t"
        "v_smfmac_f32_16x16x64_bf16 v[132:135], v[144:147], v[120:127], v148\n\t"
        "v_exp_f32 v101, v101\n\t"
        "v_mfma_f32_32x32x16_bf16 v[112:127], v[168:171], v[88:91], 0\n\t"
        "v_exp_f32 v102, v102\n\t"
        "v_exp_f32 v103, v103\n\t"
        "v_exp_f32 v104, v104\n\t"
        "v_mfma_f32_32x32x16_bf16 v[112:127], v[172:175], v[92:95], v[112:127]\n\t"
        "v_exp_f32 v105, v105\n\t"
        "v_exp_f32 v106, v106\n\t"
        "v_exp_f32 v107, v107\n\t"
        "v_smfmac_f32_32x32x32_bf16 v[32:47], v[176:179], v[96:103], v148\n\t"
        "v_exp_f32 v108, v108\n\t"
        "v_exp_f32 v109, v109\n\t"
        "v_exp_f32 v110, v110\n\t"
        "v_smfmac_f32_16x16x64_bf16 v[136:139], v[144:147], v[96:103], v148\n\t"
        "buffer_load_dwordx4 v[164:167], v149, s[40:43], s44 offen offset:3072 sc1\n\t"
        "v_exp_f32 v111, v111\n\t"
        "s_sub_u32 s45, s45, 1\n\t"
        "s_cmp_lg_u32 s45, 0\n\t"
        "s_cbranch_scc1 1b\n\t"
        "v_exp_f32 v112, v112\n\t"
        "v_exp_f32 v113, v113\n\t"
        "v_smfmac_f32_32x32x32_bf16 v[32:47], v[180:183], v[104:111], v148\n\t"
        "v_exp_f32 v114, v114\n\t"
        "v_exp_f32 v115, v115\n\t"
        "v_exp_f32 v116, v116\n\t"
        "v_smfmac_f32_16x16x64_bf16 v[136:139], v[144:147], v[104:111], v148\n\t"
        "v_exp_f32 v117, v117\n\t"
        "s_waitcnt vmcnt(8)\n\t"
        "v_mfma_f32_32x32x16_bf16 v[96:111], v[184:187], v[64:67], 0\n\t"
        "v_exp_f32 v118, v118\n\t"
        "v_exp_f32 v119, v119\n\t"
        "v_exp_f32 v120, v120\n\t"
        "v_mfma_f32_32x32x16_bf16 v[96:111], v[188:191], v[68:71], v[96:111]\n\t"
        "v_exp_f32 v121, v121\n\t"
        "v_exp_f32 v122, v122\n\t"
        "v_exp_f32 v123, v123\n\t"
        "v_smfmac_f32_32x32x32_bf16 v[48:63], v[176:179], v[112:119], v148\n\t"
        "v_exp_f32 v124, v124\n\t"
        "v_exp_f32 v125, v125\n\t"
        "v_exp_f32 v126, v126\n\t"
        "v_smfmac_f32_16x16x64_bf16 v[140:143], v[144:147], v[112:119], v148\n\t"
        "v_exp_f32 v127, v127\n\t"
        "v_exp_f32 v96, v96\n\t"
        "v_exp_f32 v97, v97\n\t"
        "v_smfmac_f32_32x32x32_bf16 v[48:63], v[180:183], v[120:127], v148\n\t"
        "v_exp_f32 v98, v98\n\t"
        "v_exp_f32 v99, v99\n\t"
        "v_exp_f32 v100, v100\n\t"
        "v_smfmac_f32_16x16x64_bf16 v[140:143], v[144:147], v[120:127], v148\n\t"
        "v_exp_f32 v101, v101\n\t"
        "v_mfma_f32_32x32x16_bf16 v[112:127], v[184:187], v[72:75], 0\n\t"
        "v_exp_f32 v102, v102\n\t"
        "v_exp_f32 v103, v103\n\t"
        "v_exp_f32 v104, v104\n\t"
        "v_mfma_f32_32x32x16_bf16 v[112:127], v[188:191], v[76:79], v[112:127]\n\t"
        "v_exp_f32 v105, v105\n\t"
        "v_exp_f32 v106, v106\n\t"
        "v_exp_f32 v107, v107\n\t"
        "v_smfmac_f32_32x32x32_bf16 v[0:15], v[192:195], v[96:103], v148\n\t"
        "v_exp_f32 v108, v108\n\t"
        "v_exp_f32 v109, v109\n\t"
        "v_exp_f32 v110, v110\n\t"
        "v_smfmac_f32_16x16x64_bf16 v[128:131], v[144:147], v[96:103], v148\n\t"
        "v_exp_f32 v111, v111\n\t"
        "v_exp_f32 v112, v112\n\t"
        "v_exp_f32 v113, v113\n\t"
        "v_smfmac_f32_32x32x32_bf16 v[0:15], v[196:199], v[104:111], v148\n\t"
        "v_exp_f32 v114, v114\n\t"
        "v_exp_f32 v115, v115\n\t"
        "v_exp_f32 v116, v116\n\t"
        "v_smfmac_f32_16x16x64_bf16 v[128:131], v[144:147], v[104:111], v148\n\t"
        "v_exp_f32 v117, v117\n\t"
        "s_waitcnt vmcnt(4)\n\t"
        "v_mfma_f32_32x32x16_bf16 v[96:111], v[200:203], v[64:67], 0\n\t"
        "v_exp_f32 v118, v118\n\t"
        "v_exp_f32 v119, v119\n\t"
        "v_exp_f32 v120, v120\n\t"
        "v_mfma_f32_32x32x16_bf16 v[96:111], v[204:207], v[68:71], v[96:111]\n\t"
        "v_exp_f32 v121, v121\n\t"
        "v_exp_f32 v122, v122\n\t"
        "v_exp_f32 v123, v123\n\t"
        "v_smfmac_f32_32x32x32_bf16 v[16:31], v[192:195], v[112:119], v148\n\t"
        "v_exp_f32 v124, v124\n\t"
        "v_exp_f32 v125, v125\n\t"
        "v_exp_f32 v126, v126\n\t"
        "v_smfmac_f32_16x16x64_bf16 v[132:135], v[144:147], v[112:119], v148\n\t"
        "v_exp_f32 v127, v127\n\t"
        "s_cmp_lg_u32 s47, 0\n\t"
        "s_cbranch_scc1 4f\n\t"
        "global_load_dwordx4 %[r0], v223, %[xb] offset:0\n\t"
        "global_load_dwordx4 %[r1], v223, %[xb] offset:32\n\t"
        "global_load_dwordx4 %[r2], v223, %[xb] offset:64\n\t"
        "global_load_dwordx4 %[r3], v223, %[xb] offset:96\n\t"
        "4:\n\t"
        "v_exp_f32 v96, v96\n\t"
        "v_exp_f32 v97, v97\n\t"
        "v_smfmac_f32_32x32x32_bf16 v[16:31], v[196:199], v[120:127], v148\n\t"
        "v_exp_f32 v98, v98\n\t"
        "v_exp_f32 v99, v99\n\t"
        "v_exp_f32 v100, v100\n\t"
        "v_smfmac_f32_16x16x64_bf16 v[132:135], v[144:147], v[120:127], v148\n\t"
        "v_exp_f32 v101, v101\n\t"
        "v_mfma_f32_32x32x16_bf16 v[112:127], v[200:203], v[72:75], 0\n\t"
        "v_exp_f32 v102, v102\n\t"
        "v_exp_f32 v103, v103\n\t"
        "v_exp_f32 v104, v104\n\t"
        "v_mfma_f32_32x32x16_bf16 v[112:127], v[204:207], v[76:79], v[112:127]\n\t"
        "v_exp_f32 v105, v105\n\t"
        "v_exp_f32 v106, v106\n\t"
        "v_exp_f32 v107, v107\n\t"
        "v_smfmac_f32_32x32x32_bf16 v[0:15], v[208:211], v[96:103], v148\n\t"
        "v_exp_f32 v108, v108\n\t"
        "v_exp_f32 v109, v109\n\t"
        "v_exp_f32 v110, v110\n\t"
        "v_smfmac_f32_16x16x64_bf16 v[128:131], v[144:147], v[96:103], v148\n\t"
        "v_exp_f32 v111, v111\n\t"
        "v_exp_f32 v112, v112\n\t"
        "v_exp_f32 v113, v113\n\t"
        "v_smfmac_f32_32x32x32_bf16 v[0:15], v[212:215], v[104:111], v148\n\t"
        "v_exp_f32 v114, v114\n\t"
        "v_exp_f32 v115, v115\n\t"
        "v_exp_f32 v116, v116\n\t"
        "v_smfmac_f32_16x16x64_bf16 v[128:131], v[144:147], v[104:111], v148\n\t"
        "v_exp_f32 v117, v117\n\t"
        "v_mfma_f32_32x32x16_bf16 v[96:111], v[184:187], v[80:83], 0\n\t"
        "v_exp_f32 v118, v118\n\t"
        "v_exp_f32 v119, v119\n\t"
        "v_exp_f32 v120, v120\n\t"
        "ds_write_b128 v216, v[0:3] offset:0\n\t"
        "v_mfma_f32_32x32x16_bf16 v[96:111], v[188:191], v[84:87], v[96:111]\n\t"
        "v_exp_f32 v121, v121\n\t"
        "ds_write_b128 v216, v[4:7] offset:1024\n\t"
        "v_exp_f32 v122, v122\n\t"
        "ds_write_b128 v216, v[8:11] offset:2048\n\t"
        "v_exp_f32 v123, v123\n\t"
        "ds_write_b128 v216, v[12:15] offset:3072\n\t"
        "v_smfmac_f32_32x32x32_bf16 v[16:31], v[208:211], v[112:119], v148\n\t"
        "v_exp_f32 v124, v124\n\t"
        "ds_write_b32 v218, v128 offset:0\n\t"
        "v_exp_f32 v125, v125\n\t"
        "v_exp_f32 v126, v126\n\t"
        "v_smfmac_f32_16x16x64_bf16 v[132:135], v[144:147], v[112:119], v148\n\t"
        "v_exp_f32 v127, v127\n\t"
        "v_exp_f32 v96, v96\n\t"
        "v_exp_f32 v97, v97\n\t"
        "v_smfmac_f32_32x32x32_bf16 v[16:31], v[212:215], v[120:127], v148\n\t"
        "v_exp_f32 v98, v98\n\t"
        "v_exp_f32 v99, v99\n\t"
        "v_exp_f32 v100, v100\n\t"
        "v_smfmac_f32_16x16x64_bf16 v[132:135], v[144:147], v[120:127], v148\n\t"
        "v_exp_f32 v101, v101\n\t"
        "v_mfma_f32_32x32x16_bf16 v[112:127], v[184:187], v[88:91], 0\n\t"
        "v_exp_f32 v102, v102\n\t"
        "v_exp_f32 v103, v103\n\t"
        "v_exp_f32 v104, v104\n\t"
        "ds_write_b128 v216, v[16:19] offset:4096\n\t"
        "v_mfma_f32_32x32x16_bf16 v[112:127], v[188:191], v[92:95], v[112:127]\n\t"
        "v_exp_f32 v105, v105\n\t"
        "ds_write_b128 v216, v[20:23] offset:5120\n\t"
        "v_exp_f32 v106, v106\n\t"
        "ds_write_b128 v216, v[24:27] offset:6144\n\t"
        "v_exp_f32 v107, v107\n\t"
        "ds_write_b128 v216, v[28:31] offset:7168\n\t"
        "v_smfmac_f32_32x32x32_bf16 v[32:47], v[192:195], v[96:103], v148\n\t"
        "v_exp_f32 v108, v108\n\t"
        "ds_write_b32 v218, v132 offset:256\n\t"
        "v_exp_f32 v109, v109\n\t"
        "ds_write_b32 v220, v222\n\t"
        "v_exp_f32 v110, v110\n\t"
        "v_smfmac_f32_16x16x64_bf16 v[136:139], v[144:147], v[96:103], v148\n\t"
        "v_exp_f32 v111, v111\n\t"
        "v_exp_f32 v112, v112\n\t"
        "v_exp_f32 v113, v113\n\t"
        "v_smfmac_f32_32x32x32_bf16 v[32:47], v[196:199], v[104:111], v148\n\t"
        "v_exp_f32 v114, v114\n\t"
        "v_exp_f32 v115, v115\n\t"
        "v_exp_f32 v116, v116\n\t"
        "v_smfmac_f32_16x16x64_bf16 v[136:139], v[144:147], v[104:111], v148\n\t"
        "v_exp_f32 v117, v117\n\t"
        "v_mfma_f32_32x32x16_bf16 v[96:111], v[200:203], v[80:83], 0\n\t"
        "v_exp_f32 v118, v118\n\t"
        "v_exp_f32 v119, v119\n\t"
        "v_exp_f32 v120, v120\n\t"
        "v_mfma_f32_32x32x16_bf16 v[96:111], v[204:207], v[84:87], v[96:111]\n\t"
        "v_exp_f32 v121, v121\n\t"
        "v_exp_f32 v122, v122\n\t"
        "v_exp_f32 v123, v123\n\t"
        "v_smfmac_f32_32x32x32_bf16 v[48:63], v[192:195], v[112:119], v148\n\t"
        "v_exp_f32 v124, v124\n\t"
        "v_exp_f32 v125, v125\n\t"
        "v_exp_f32 v126, v126\n\t"
        "v_smfmac_f32_16x16x64_bf16 v[140:143], v[144:147], v[112:119], v148\n\t"
        "v_exp_f32 v127, v127\n\t"
        "v_exp_f32 v96, v96\n\t"
        "v_exp_f32 v97, v97\n\t"
        "v_smfmac_f32_32x32x32_bf16 v[48:63], v[196:199], v[120:127], v148\n\t"
        "v_exp_f32 v98, v98\n\t"
        "v_exp_f32 v99, v99\n\t"
        "v_exp_f32 v100, v100\n\t"
        "v_smfmac_f32_16x16x64_bf16 v[140:143], v[144:147], v[120:127], v148\n\t"
        "v_exp_f32 v101, v101\n\t"
        "v_mfma_f32_32x32x16_bf16 v[112:127], v[200:203], v[88:91], 0\n\t"
        "v_exp_f32 v102, v102\n\t"
        "v_exp_f32 v103, v103\n\t"
        "v_exp_f32 v104, v104\n\t"
        "ds_read_b32 v151, v221\n\t"
        "v_mfma_f32_32x32x16_bf16 v[112:127], v[204:207], v[92:95], v[112:127]\n\t"
        "v_exp_f32 v105, v105\n\t"
        "v_exp_f32 v106, v106\n\t"
        "v_exp_f32 v107, v107\n\t"
        "v_smfmac_f32_32x32x32_bf16 v[32:47], v[208:211], v[96:103], v148\n\t"
        "v_exp_f32 v108, v108\n\t"
        "v_exp_f32 v109, v109\n\t"
        "v_exp_f32 v110, v110\n\t"
        "v_smfmac_f32_16x16x64_bf16 v[136:139], v[144:147], v[96:103], v148\n\t"
        "v_exp_f32 v111, v111\n\t"
        "s_waitcnt lgkmcnt(0)\n\t"
        "v_cmp_ne_u32 vcc, v222, v151\n\t"
        "s_cbranch_vccnz 40f\n\t"
        "41:\n\t"
        "v_exp_f32 v112, v112\n\t"
        "ds_read_b128 v[0:3], v217 offset:0\n\t"
        "v_exp_f32 v113, v113\n\t"
        "ds_read_b128 v[4:7], v217 offset:1024\n\t"
        "v_smfmac_f32_32x32x32_bf16 v[32:47], v[212:215], v[104:111], v148\n\t"
        "v_exp_f32 v114, v114\n\t"
        "ds_read_b128 v[8:11], v217 offset:2048\n\t"
        "v_exp_f32 v115, v115\n\t"
        "ds_read_b128 v[12:15], v217 offset:3072\n\t"
        "v_exp_f32 v116, v116\n\t"
        "ds_read_b32 v128, v219 offset:0\n\t"
        "v_smfmac_f32_16x16x64_bf16 v[136:139], v[144:147], v[104:111], v148\n\t"
        "v_exp_f32 v117, v117\n\t"
        "ds_read_b128 v[16:19], v217 offset:4096\n\t"
        "v_exp_f32 v118, v118\n\t"
        "ds_read_b128 v[20:23], v217 offset:5120\n\t"
        "v_exp_f32 v119, v119\n\t"
        "ds_read_b128 v[24:27], v217 offset:6144\n\t"
        "v_exp_f32 v120, v120\n\t"
        "ds_read_b128 v[28:31], v217 offset:7168\n\t"
        "v_exp_f32 v121, v121\n\t"
        "ds_read_b32 v132, v219 offset:256\n\t"
        "v_exp_f32 v122, v122\n\t"
        "v_exp_f32 v123, v123\n\t"
        "v_smfmac_f32_32x32x32_bf16 v[48:63], v[208:211], v[112:119], v148\n\t"
        "v_exp_f32 v124, v124\n\t"
        "v_exp_f32 v125, v125\n\t"
        "v_exp_f32 v126, v126\n\t"
        "v_smfmac_f32_16x16x64_bf16 v[140:143], v[144:147], v[112:119], v148\n\t"
        "v_exp_f32 v127, v127\n\t"
        "s_nop 1\n\t"
        "v_smfmac_f32_32x32x32_bf16 v[48:63], v[212:215], v[120:127], v148\n\t"
        "v_smfmac_f32_16x16x64_bf16 v[140:143], v[144:147], v[120:127], v148\n\t"
        "s_waitcnt vmcnt(0) lgkmcnt(0)\n\t"
        "s_nop 6\n\t"
        "s_branch 9f\n\t"
        "30:\n\t"
        "s_add_u32 s44, s44, 0x4000\n\t"
        "s_and_b32 s44, s44, 0x3ffff\n\t"
        "buffer_load_dwordx4 v[184:187], v149, s[40:43], s44 offen offset:0 sc1\n\t"
        "buffer_load_dwordx4 v[188:191], v149, s[40:43], s44 offen offset:1024 sc1\n\t"
        "buffer_load_dwordx4 v[192:195], v149, s[40:43], s44 offen offset:2048 sc1\n\t"
        "buffer_load_dwordx4 v[196:199], v149, s[40:43], s44 offen offset:3072 sc1\n\t"
        "buffer_load_dwordx4 v[200:203], v150, s[40:43], s44 offen offset:0 sc1\n\t"
        "buffer_load_dwordx4 v[204:207], v150, s[40:43], s44 offen offset:1024 sc1\n\t"
        "buffer_load_dwordx4 v[208:211], v150, s[40:43], s44 offen offset:2048 sc1\n\t"
        "buffer_load_dwordx4 v[212:215], v150, s[40:43], s44 offen offset:3072 sc1\n\t"
        "s_add_u32 s44, s44, 0x4000\n\t"
        "s_and_b32 s44, s44, 0x3ffff\n\t"
        "buffer_load_dwordx4 v[152:155], v149, s[40:43], s44 offen offset:0 sc1\n\t"
        "buffer_load_dwordx4 v[156:159], v149, s[40:43], s44 offen offset:1024 sc1\n\t"
        "buffer_load_dwordx4 v[160:163], v149, s[40:43], s44 offen offset:2048 sc1\n\t"
        "buffer_load_dwordx4 v[164:167], v149, s[40:43], s44 offen offset:3072 sc1\n\t"
        "s_mov_b32 s46, 1\n\t"
        "s_branch 31b\n\t"
        "32:\n\t"
        "s_add_u32 s44, s44, 0x4000\n\t"
        "s_and_b32 s44, s44, 0x3ffff\n\t"
        "buffer_load_dwordx4 v[184:187], v149, s[40:43], s44 offen offset:0 sc1\n\t"
        "buffer_load_dwordx4 v[188:191], v149, s[40:43], s44 offen offset:1024 sc1\n\t"
        "buffer_load_dwordx4 v[192:195], v149, s[40:43], s44 offen offset:2048 sc1\n\t"
        "buffer_load_dwordx4 v[196:199], v149, s[40:43], s44 offen offset:3072 sc1\n\t"
        "buffer_load_dwordx4 v[200:203], v150, s[40:43], s44 offen offset:0 sc1\n\t"
        "buffer_load_dwordx4 v[204:207], v150, s[40:43], s44 offen offset:1024 sc1\n\t"
        "buffer_load_dwordx4 v[208:211], v150, s[40:43], s44 offen offset:2048 sc1\n\t"
        "buffer_load_dwordx4 v[212:215], v150, s[40:43], s44 offen offset:3072 sc1\n\t"
        "s_add_u32 s44, s44, 0x4000\n\t"
        "s_and_b32 s44, s44, 0x3ffff\n\t"
        "buffer_load_dwordx4 v[152:155], v149, s[40:43], s44 offen offset:0 sc1\n\t"
        "buffer_load_dwordx4 v[156:159], v149, s[40:43], s44 offen offset:1024 sc1\n\t"
        "buffer_load_dwordx4 v[160:163], v149, s[40:43], s44 offen offset:2048 sc1\n\t"
        "buffer_load_dwordx4 v[164:167], v149, s[40:43], s44 offen offset:3072 sc1\n\t"
        "s_mov_b32 s46, 1\n\t"
        "s_branch 33b\n\t"
        "40:\n\t"
        "s_mov_b32 s45, 0x100000\n\t"
        "42:\n\t"
        "ds_read_b32 v151, v221\n\t"
        "s_sub_u32 s45, s45, 1\n\t"
        "s_waitcnt lgkmcnt(0)\n\t"
        "s_cbranch_scc1 41b\n\t"
        "v_cmp_ne_u32 vcc, v222, v151\n\t"
        "s_cbranch_vccnz 42b\n\t"
        "s_branch 41b\n\t"
        "9:\n\t"
        : "+{v[0:15]}"(o0), "+{v[16:31]}"(o1), "+{v[32:47]}"(o2), "+{v[48:63]}"(o3), "+{v[128:143]}"(lv), "+{s44}"(soff), "+{v[152:167]}"(kva), "+{v[168:183]}"(kvb), "+{v[184:191]}"(fv),
          [r0] "+v"(x1p[0]), [r1] "+v"(x1p[1]), [r2] "+v"(x1p[2]), [r3] "+v"(x1p[3]) STAMP_OUTS
        : "{v[64:71]}"(q[0]), "{v[72:79]}"(q[1]), "{v[80:87]}"(q[2]), "{v[88:95]}"(q[3]), "{v[144:147]}"(ones), "{v148}"(0xddddu), "{v149}"(kvoff), "{v150}"(kvoff + 4096),
          "{v[216:223]}"(cv), "{v224}"(lbase + L_W + 16), "{s[40:43]}"(srd), "{s47}"(L), [xb] "s"(x0)
        : "v96", "v97", "v98", "v99", "v100", "v101", "v102", "v103", "v104", "v105", "v106", "v107", "v108", "v109", "v110", "v111", "v112", "v113", "v114", "v115", "v116", "v117", "v118", "v119", "v120", "v121", "v122", "v123", "v124", "v125", "v126", "v127", "v151", "v192", "v193", "v194", "v195", "v196", "v197", "v198", "v199", "v200", "v201", "v202", "v203", "v204", "v205", "v206", "v207", "v208", "v209", "v210", "v211", "v212", "v213", "v214", "v215", "s45", "s46", "vcc", "scc", "memory");
      STAMP_PUT(6 + 16 * L);
      STAMPV(9 + 16 * L);
      PHASE_VARS;
      const unsigned dst = OFF_OF + L * (5 * MBY) + (tm * 4 + 2 * kh) * TS + (tn * 2 + hh) * 2048 + lane * 16;
      auto put = [&](int u, const f32x16& o) {
#pragma unroll
        for (int c = 0; c < 2; ++c) {
          u32x4 w;
#pragma unroll
          for (int e = 0; e < 4; ++e) w[e] = pack2(o[8 * c + 2 * e], o[8 * c + 2 * e + 1]);
          stg(rs, dst + u * TS + c * 1024, w);
        }
      };
      const float la = lv[8] + lv[0], lb = lv[12] + lv[4];
      o2 = (o2 + o0) * RCP(la); put(0, o2);
      SBAR();
      o3 = (o3 + o1) * RCP(lb); put(1, o3);
      SBAR();
      {
        const f32x16 t = o2 + o3;
        float sum = 0.f;
#pragma unroll
        for (int e = 0; e < 16; e += 4) sum += (t[e] + t[e + 1]) + (t[e + 2] + t[e + 3]);
        if (__any(!(fabsf(sum) < 1e37f) | !(fmaxf(la, lb) < 1e27f) | !(fminf(la, lb) > 1e-9f) | !(la + lb == la + lb))) {
          DRAIN();
          attn_safe(rs, kvoff - kh * 8192, q[2], ones, o2); put(0, o2);
          attn_safe(rs, kvoff - kh * 8192, q[3], ones, o3); put(1, o3);
        }
      }
    }
    STAMPV(10 + 16 * L);
    {
      f32x16 acc[2];
      u32x4 res[4];
      u32x2 wrc[8];
      STAMP_DECL;
      { PHASE_VARS;
      BAR_ALL();
      STAMPV(11 + 16 * L);
#pragma unroll
      for (int g = 0; g < 8; ++g) wrc[g] = *reinterpret_cast<const u32x2*>(smem + L_WR + g * 16 + hi * 8);
      if (L == 0) {
#pragma unroll
        for (int i = 0; i < 4; ++i) res[i] = fo_swap(x1p[i]);
      } else {
        const bool routed = (int)words[3] == tile * 32 + r32;
        const float mb1 = reinterpret_cast<const float*>(smem + L_AB)[256 + wid * 64 + r32], rd1 = reinterpret_cast<const float*>(smem + L_AB)[256 + wid * 64 + 32 + r32];
#pragma unroll
        for (int i = 0; i < 4; ++i) {
          const u32x4 pr = *reinterpret_cast<const u32x4*>(smem + L_X2P + (i * 2 + hi) * 16);
#pragma unroll
          for (int e = 0; e < 4; ++e) {
            const unsigned h2 = ln_pair(x1p[i][e], mb1, rd1);
            unsigned t = pack2(lo16(x1p[i][e]) + lo16(h2), hi16(x1p[i][e]) + hi16(h2));
            PIN(t);
            res[i][e] = routed ? pr[e] : t;
          }
        }
      }
#pragma unroll
      for (int g = 0; g < 8; ++g) PIN(wrc[g]);
#pragma unroll
      for (int i = 0; i < 4; ++i) PIN(res[i]);
      unsigned soff = tn * 4096;
      const unsigned a0 = lbase + wrow * 128 + ((wch ^ wsw) << 4) + 8 * (tp & 1), a1 = lbase + wrow * 128 + (((4 + wch) ^ wsw) << 4) + 8 * (tp & 1);
      asm volatile(
        "ds_read_b64_tr_b16 v[32:33], v193 offset:0\n\t"
        "ds_read_b64_tr_b16 v[34:35], v193 offset:1024\n\t"
        "ds_read_b64_tr_b16 v[36:37], v194 offset:0\n\t"
        "ds_read_b64_tr_b16 v[38:39], v194 offset:1024\n\t"
        "ds_read_b64_tr_b16 v[40:41], v193 offset:2048\n\t"
        "ds_read_b64_tr_b16 v[42:43], v193 offset:3072\n\t"
        "ds_read_b64_tr_b16 v[44:45], v194 offset:2048\n\t"
        "ds_read_b64_tr_b16 v[46:47], v194 offset:3072\n\t"
        "ds_read_b64_tr_b16 v[48:49], v193 offset:4096\n\t"
        "ds_read_b64_tr_b16 v[50:51], v193 offset:5120\n\t"
        "ds_read_b64_tr_b16 v[52:53], v194 offset:4096\n\t"
        "ds_read_b64_tr_b16 v[54:55], v194 offset:5120\n\t"
        "ds_read_b64_tr_b16 v[56:57], v193 offset:6144\n\t"
        "ds_read_b64_tr_b16 v[58:59], v193 offset:7168\n\t"
        "ds_read_b64_tr_b16 v[60:61], v194 offset:6144\n\t"
        "ds_read_b64_tr_b16 v[62:63], v194 offset:7168\n\t"
        "s_mov_b32 s45, 0x200000\n\t"
        "2:\n\t"
        "ds_read_b32 v200, v198\n\t"
        "s_sub_u32 s45, s45, 1\n\t"
        "s_waitcnt lgkmcnt(0)\n\t"
        "s_cbranch_scc1 3f\n\t"
        "v_cmp_ne_u32 vcc, v199, v200\n\t"
        "s_cbranch_vccnz 2b\n\t"
        "3:\n\t"
        "buffer_load_dwordx4 v[64:67], v192, s[40:43], s44 offen offset:0 sc1\n\t"
        "buffer_load_dwordx4 v[68:71], v192, s[40:43], s44 offen offset:1024 sc1\n\t"
        "buffer_load_dwordx4 v[72:75], v192, s[40:43], s44 offen offset:2048 sc1\n\t"
        "buffer_load_dwordx4 v[76:79], v192, s[40:43], s44 offen offset:3072 sc1\n\t"
        "s_add_u32 s44, s44, 0x1000\n\t"
        "s_and_b32 s44, s44, 0xffff\n\t"
        "buffer_load_dwordx4 v[80:83], v192, s[40:43], s44 offen offset:0 sc1\n\t"
        "buffer_load_dwordx4 v[84:87], v192, s[40:43], s44 offen offset:1024 sc1\n\t"
        "buffer_load_dwordx4 v[88:91], v192, s[40:43], s44 offen offset:2048 sc1\n\t"
        "buffer_load_dwordx4 v[92:95], v192, s[40:43], s44 offen offset:3072 sc1\n\t"
        "s_add_u32 s44, s44, 0x1000\n\t"
        "s_and_b32 s44, s44, 0xffff\n\t"
        "buffer_load_dwordx4 v[96:99], v192, s[40:43], s44 offen offset:0 sc1\n\t"
        "buffer_load_dwordx4 v[100:103], v192, s[40:43], s44 offen offset:1024 sc1\n\t"
        "buffer_load_dwordx4 v[104:107], v192, s[40:43], s44 offen offset:2048 sc1\n\t"
        "buffer_load_dwordx4 v[108:111], v192, s[40:43], s44 offen offset:3072 sc1\n\t"
        "s_waitcnt vmcnt(11) lgkmcnt(14)\n\t"
        "v_mfma_f32_32x32x16_bf16 v[0:15], v[32:35], v[64:67], 0\n\t"
        "ds_read_b64_tr_b16 v[32:33], v193 offset:8192\n\t"
        "ds_read_b64_tr_b16 v[34:35], v193 offset:9216\n\t"
        "s_waitcnt lgkmcnt(14)\n\t"
        "v_mfma_f32_32x32x16_bf16 v[16:31], v[36:39], v[64:67], 0\n\t"
        "ds_read_b64_tr_b16 v[36:37], v194 offset:8192\n\t"
        "ds_read_b64_tr_b16 v[38:39], v194 offset:9216\n\t"
        "s_add_u32 s44, s44, 0x1000\n\t"
        "s_and_b32 s44, s44, 0xffff\n\t"
        "buffer_load_dwordx4 v[112:115], v192, s[40:43], s44 offen offset:0 sc1\n\t"
        "s_waitcnt vmcnt(11) lgkmcnt(14)\n\t"
        "v_mfma_f32_32x32x16_bf16 v[0:15], v[40:43], v[68:71], v[0:15]\n\t"
        "ds_read_b64_tr_b16 v[40:41], v193 offset:10240\n\t"
        "ds_read_b64_tr_b16 v[42:43], v193 offset:11264\n\t"
        "s_waitcnt lgkmcnt(14)\n\t"
        "v_mfma_f32_32x32x16_bf16 v[16:31], v[44:47], v[68:71], v[16:31]\n\t"
        "ds_read_b64_tr_b16 v[44:45], v194 offset:10240\n\t"
        "ds_read_b64_tr_b16 v[46:47], v194 offset:11264\n\t"
        "buffer_load_dwordx4 v[116:119], v192, s[40:43], s44 offen offset:1024 sc1\n\t"
        "s_waitcnt vmcnt(11) lgkmcnt(14)\n\t"
        "v_mfma_f32_32x32x16_bf16 v[0:15], v[48:51], v[72:75], v[0:15]\n\t"
        "ds_read_b64_tr_b16 v[48:49], v193 offset:12288\n\t"
        "ds_read_b64_tr_b16 v[50:51], v193 offset:13312\n\t"
        "s_waitcnt lgkmcnt(14)\n\t"
        "v_mfma_f32_32x32x16_bf16 v[16:31], v[52:55], v[72:75], v[16:31]\n\t"
        "ds_read_b64_tr_b16 v[52:53], v194 offset:12288\n\t"
        "ds_read_b64_tr_b16 v[54:55], v194 offset:13312\n\t"
        "buffer_load_dwordx4 v[120:123], v192, s[40:43], s44 offen offset:2048 sc1\n\t"
        "s_waitcnt vmcnt(11) lgkmcnt(14)\n\t"
        "v_mfma_f32_32x32x16_bf16 v[0:15], v[56:59], v[76:79], v[0:15]\n\t"
        "ds_read_b64_tr_b16 v[56:57], v193 offset:14336\n\t"
        "ds_read_b64_tr_b16 v[58:59], v193 offset:15360\n\t"
        "s_waitcnt lgkmcnt(14)\n\t"
        "v_mfma_f32_32x32x16_bf16 v[16:31], v[60:63], v[76:79], v[16:31]\n\t"
        "ds_read_b64_tr_b16 v[60:61], v194 offset:14336\n\t"
        "ds_read_b64_tr_b16 v[62:63], v194 offset:15360\n\t"
        "buffer_load_dwordx4 v[124:127], v192, s[40:43], s44 offen offset:3072 sc1\n\t"
        "s_waitcnt vmcnt(11) lgkmcnt(14)\n\t"
        "v_mfma_f32_32x32x16_bf16 v[0:15], v[32:35], v[80:83], v[0:15]\n\t"
        "ds_read_b64_tr_b16 v[32:33], v193 offset:16384\n\t"
        "ds_read_b64_tr_b16 v[34:35], v193 offset:17408\n\t"
        "s_waitcnt lgkmcnt(14)\n\t"
        "v_mfma_f32_32x32x16_bf16 v[16:31], v[36:39], v[80:83], v[16:31]\n\t"
        "ds_read_b64_tr_b16 v[36:37], v194 offset:16384\n\t"
        "ds_read_b64_tr_b16 v[38:39], v194 offset:17408\n\t"
        "s_add_u32 s44, s44, 0x1000\n\t"
        "s_and_b32 s44, s44, 0xffff\n\t"
        "buffer_load_dwordx4 v[128:131], v192, s[40:43], s44 offen offset:0 sc1\n\t"
        "s_waitcnt vmcnt(11) lgkmcnt(14)\n\t"
        "v_mfma_f32_32x32x16_bf16 v[0:15], v[40:43], v[84:87], v[0:15]\n\t"
        "ds_read_b64_tr_b16 v[40:41], v193 offset:18432\n\t"
        "ds_read_b64_tr_b16 v[42:43], v193 offset:19456\n\t"
        "s_waitcnt lgkmcnt(14)\n\t"
        "v_mfma_f32_32x32x16_bf16 v[16:31], v[44:47], v[84:87], v[16:31]\n\t"
        "ds_read_b64_tr_b16 v[44:45], v194 offset:18432\n\t"
        "ds_read_b64_tr_b16 v[46:47], v194 offset:19456\n\t"
        "buffer_load_dwordx4 v[132:135], v192, s[40:43], s44 offen offset:1024 sc1\n\t"
        "s_waitcnt vmcnt(11) lgkmcnt(14)\n\t"
        "v_mfma_f32_32x32x16_bf16 v[0:15], v[48:51], v[88:91], v[0:15]\n\t"
        "ds_read_b64_tr_b16 v[48:49], v193 offset:20480\n\t"
        "ds_read_b64_tr_b16 v[50:51], v193 offset:21504\n\t"
        "s_waitcnt lgkmcnt(14)\n\t"
        "v_mfma_f32_32x32x16_bf16 v[16:31], v[52:55], v[88:91], v[16:31]\n\t"
        "ds_read_b64_tr_b16 v[52:53], v194 offset:20480\n\t"
        "ds_read_b64_tr_b16 v[54:55], v194 offset:21504\n\t"
        "buffer_load_dwordx4 v[136:139], v192, s[40:43], s44 offen offset:2048 sc1\n\t"
        "s_waitcnt vmcnt(11) lgkmcnt(14)\n\t"
        "v_mfma_f32_32x32x16_bf16 v[0:15], v[56:59], v[92:95], v[0:15]\n\t"
        "ds_read_b64_tr_b16 v[56:57], v193 offset:22528\n\t"
        "ds_read_b64_tr_b16 v[58:59], v193 offset:23552\n\t"
        "s_waitcnt lgkmcnt(14)\n\t"
        "v_mfma_f32_32x32x16_bf16 v[16:31], v[60:63], v[92:95], v[16:31]\n\t"
        "ds_read_b64_tr_b16 v[60:61], v194 offset:22528\n\t"
        "ds_read_b64_tr_b16 v[62:63], v194 offset:23552\n\t"
        "buffer_load_dwordx4 v[140:143], v192, s[40:43], s44 offen offset:3072 sc1\n\t"
        "s_waitcnt vmcnt(11) lgkmcnt(14)\n\t"
        "v_mfma_f32_32x32x16_bf16 v[0:15], v[32:35], v[96:99], v[0:15]\n\t"
        "ds_read_b64_tr_b16 v[32:33], v193 offset:24576\n\t"
        "ds_read_b64_tr_b16 v[34:35], v193 offset:25600\n\t"
        "s_waitcnt lgkmcnt(14)\n\t"
        "v_mfma_f32_32x32x16_bf16 v[16:31], v[36:39], v[96:99], v[16:31]\n\t"
        "ds_read_b64_tr_b16 v[36:37], v194 offset:24576\n\t"
        "ds_read_b64_tr_b16 v[38:39], v194 offset:25600\n\t"
        "s_add_u32 s44, s44, 0x1000\n\t"
        "s_and_b32 s44, s44, 0xffff\n\t"
        "buffer_load_dwordx4 v[144:147], v192, s[40:43], s44 offen offset:0 sc1\n\t"
        "s_waitcnt vmcnt(11) lgkmcnt(14)\n\t"
        "v_mfma_f32_32x32x16_bf16 v[0:15], v[40:43], v[100:103], v[0:15]\n\t"
        "ds_read_b64_tr_b16 v[40:41], v193 offset:26624\n\t"
        "ds_read_b64_tr_b16 v[42:43], v193 offset:27648\n\t"
        "s_waitcnt lgkmcnt(14)\n\t"
        "v_mfma_f32_32x32x16_bf16 v[16:31], v[44:47], v[100:103], v[16:31]\n\t"
        "ds_read_b64_tr_b16 v[44:45], v194 offset:26624\n\t"
        "ds_read_b64_tr_b16 v[46:47], v194 offset:27648\n\t"
        "buffer_load_dwordx4 v[148:151], v192, s[40:43], s44 offen offset:1024 sc1\n\t"
        "s_waitcnt vmcnt(11) lgkmcnt(14)\n\t"
        "v_mfma_f32_32x32x16_bf16 v[0:15], v[48:51], v[104:107], v[0:15]\n\t"
        "ds_read_b64_tr_b16 v[48:49], v193 offset:28672\n\t"
        "ds_read_b64_tr_b16 v[50:51], v193 offset:29696\n\t"
        "s_waitcnt lgkmcnt(14)\n\t"
        "v_mfma_f32_32x32x16_bf16 v[16:31], v[52:55], v[104:107], v[16:31]\n\t"
        "ds_read_b64_tr_b16 v[52:53], v194 offset:28672\n\t"
        "ds_read_b64_tr_b16 v[54:55], v194 offset:29696\n\t"
        "buffer_load_dwordx4 v[152:155], v192, s[40:43], s44 offen offset:2048 sc1\n\t"
        "s_waitcnt vmcnt(11) lgkmcnt(14)\n\t"
        "v_mfma_f32_32x32x16_bf16 v[0:15], v[56:59], v[108:111], v[0:15]\n\t"
        "ds_read_b64_tr_b16 v[56:57], v193 offset:30720\n\t"
        "ds_read_b64_tr_b16 v[58:59], v193 offset:31744\n\t"
        "s_waitcnt lgkmcnt(14)\n\t"
        "v_mfma_f32_32x32x16_bf16 v[16:31], v[60:63], v[108:111], v[16:31]\n\t"
        "ds_read_b64_tr_b16 v[60:61], v194 offset:30720\n\t"
        "ds_read_b64_tr_b16 v[62:63], v194 offset:31744\n\t"
        "buffer_load_dwordx4 v[156:159], v192, s[40:43], s44 offen offset:3072 sc1\n\t"
        "s_waitcnt vmcnt(11) lgkmcnt(14)\n\t"
        "v_mfma_f32_32x32x16_bf16 v[0:15], v[32:35], v[112:115], v[0:15]\n\t"
        "ds_read_b64_tr_b16 v[32:33], v193 offset:32768\n\t"
        "ds_read_b64_tr_b16 v[34:35], v193 offset:33792\n\t"
        "s_waitcnt lgkmcnt(14)\n\t"
        "v_mfma_f32_32x32x16_bf16 v[16:31], v[36:39], v[112:115], v[16:31]\n\t"
        "ds_read_b64_tr_b16 v[36:37], v194 offset:32768\n\t"
        "ds_read_b64_tr_b16 v[38:39], v194 offset:33792\n\t"
        "s_add_u32 s44, s44, 0x1000\n\t"
        "s_and_b32 s44, s44, 0xffff\n\t"
        "buffer_load_dwordx4 v[160:163], v192, s[40:43], s44 offen offset:0 sc1\n\t"
        "s_waitcnt vmcnt(11) lgkmcnt(14)\n\t"
        "v_mfma_f32_32x32x16_bf16 v[0:15], v[40:43], v[116:119], v[0:15]\n\t"
        "ds_read_b64_tr_b16 v[40:41], v193 offset:34816\n\t"
        "ds_read_b64_tr_b16 v[42:43], v193 offset:35840\n\t"
        "s_waitcnt lgkmcnt(14)\n\t"
        "v_mfma_f32_32x32x16_bf16 v[16:31], v[44:47], v[116:119], v[16:31]\n\t"
        "ds_read_b64_tr_b16 v[44:45], v194 offset:34816\n\t"
        "ds_read_b64_tr_b16 v[46:47], v194 offset:35840\n\t"
        "buffer_load_dwordx4 v[164:167], v192, s[40:43], s44 offen offset:1024 sc1\n\t"
        "s_waitcnt vmcnt(11) lgkmcnt(14)\n\t"
        "v_mfma_f32_32x32x16_bf16 v[0:15], v[48:51], v[120:123], v[0:15]\n\t"
        "ds_read_b64_tr_b16 v[48:49], v193 offset:36864\n\t"
        "ds_read_b64_tr_b16 v[50:51], v193 offset:37888\n\t"
        "s_waitcnt lgkmcnt(14)\n\t"
        "v_mfma_f32_32x32x16_bf16 v[16:31], v[52:55], v[120:123], v[16:31]\n\t"
        "ds_read_b64_tr_b16 v[52:53], v194 offset:36864\n\t"
        "ds_read_b64_tr_b16 v[54:55], v194 offset:37888\n\t"
        "buffer_load_dwordx4 v[168:171], v192, s[40:43], s44 offen offset:2048 sc1\n\t"
        "s_waitcnt vmcnt(11) lgkmcnt(14)\n\t"
        "v_mfma_f32_32x32x16_bf16 v[0:15], v[56:59], v[124:127], v[0:15]\n\t"
        "ds_read_b64_tr_b16 v[56:57], v193 offset:38912\n\t"
        "ds_read_b64_tr_b16 v[58:59], v193 offset:39936\n\t"
        "s_waitcnt lgkmcnt(14)\n\t"
        "v_mfma_f32_32x32x16_bf16 v[16:31], v[60:63], v[124:127], v[16:31]\n\t"
        "ds_read_b64_tr_b16 v[60:61], v194 offset:38912\n\t"
        "ds_read_b64_tr_b16 v[62:63], v194 offset:39936\n\t"
        "buffer_load_dwordx4 v[172:175], v192, s[40:43], s44 offen offset:3072 sc1\n\t"
        "s_waitcnt vmcnt(11) lgkmcnt(14)\n\t"
        "v_mfma_f32_32x32x16_bf16 v[0:15], v[32:35], v[128:131], v[0:15]\n\t"
        "ds_read_b64_tr_b16 v[32:33], v193 offset:40960\n\t"
        "ds_read_b64_tr_b16 v[34:35], v193 offset:41984\n\t"
        "s_waitcnt lgkmcnt(14)\n\t"
        "v_mfma_f32_32x32x16_bf16 v[16:31], v[36:39], v[128:131], v[16:31]\n\t"
        "ds_read_b64_tr_b16 v[36:37], v194 offset:40960\n\t"
        "ds_read_b64_tr_b16 v[38:39], v194 offset:41984\n\t"
        "s_add_u32 s44, s44, 0x1000\n\t"
        "s_and_b32 s44, s44, 0xffff\n\t"
        "buffer_load_dwordx4 v[176:179], v192, s[40:43], s44 offen offset:0 sc1\n\t"
        "s_waitcnt vmcnt(11) lgkmcnt(14)\n\t"
        "v_mfma_f32_32x32x16_bf16 v[0:15], v[40:43], v[132:135], v[0:15]\n\t"
        "ds_read_b64_tr_b16 v[40:41], v193 offset:43008\n\t"
        "ds_read_b64_tr_b16 v[42:43], v193 offset:44032\n\t"
        "s_waitcnt lgkmcnt(14)\n\t"
        "v_mfma_f32_32x32x16_bf16 v[16:31], v[44:47], v[132:135], v[16:31]\n\t"
        "ds_read_b64_tr_b16 v[44:45], v194 offset:43008\n\t"
        "ds_read_b64_tr_b16 v[46:47], v194 offset:44032\n\t"
        "buffer_load_dwordx4 v[180:183], v192, s[40:43], s44 offen offset:1024 sc1\n\t"
        "s_waitcnt vmcnt(11) lgkmcnt(14)\n\t"
        "v_mfma_f32_32x32x16_bf16 v[0:15], v[48:51], v[136:139], v[0:15]\n\t"
        "ds_read_b64_tr_b16 v[48:49], v193 offset:45056\n\t"
        "ds_read_b64_tr_b16 v[50:51], v193 offset:46080\n\t"
        "s_waitcnt lgkmcnt(14)\n\t"
        "v_mfma_f32_32x32x16_bf16 v[16:31], v[52:55], v[136:139], v[16:31]\n\t"
        "ds_read_b64_tr_b16 v[52:53], v194 offset:45056\n\t"
        "ds_read_b64_tr_b16 v[54:55], v194 offset:46080\n\t"
        "buffer_load_dwordx4 v[184:187], v192, s[40:43], s44 offen offset:2048 sc1\n\t"
        "s_waitcnt vmcnt(11) lgkmcnt(14)\n\t"
        "v_mfma_f32_32x32x16_bf16 v[0:15], v[56:59], v[140:143], v[0:15]\n\t"
        "ds_read_b64_tr_b16 v[56:57], v193 offset:47104\n\t"
        "ds_read_b64_tr_b16 v[58:59], v193 offset:48128\n\t"
        "s_waitcnt lgkmcnt(14)\n\t"
        "v_mfma_f32_32x32x16_bf16 v[16:31], v[60:63], v[140:143], v[16:31]\n\t"
        "ds_read_b64_tr_b16 v[60:61], v194 offset:47104\n\t"
        "ds_read_b64_tr_b16 v[62:63], v194 offset:48128\n\t"
        "buffer_load_dwordx4 v[188:191], v192, s[40:43], s44 offen offset:3072 sc1\n\t"
        "s_waitcnt vmcnt(11) lgkmcnt(14)\n\t"
        "v_mfma_f32_32x32x16_bf16 v[0:15], v[32:35], v[144:147], v[0:15]\n\t"
        "ds_read_b64_tr_b16 v[32:33], v193 offset:49152\n\t"
        "ds_read_b64_tr_b16 v[34:35], v193 offset:50176\n\t"
        "s_waitcnt lgkmcnt(14)\n\t"
        "v_mfma_f32_32x32x16_bf16 v[16:31], v[36:39], v[144:147], v[16:31]\n\t"
        "ds_read_b64_tr_b16 v[36:37], v194 offset:49152\n\t"
        "ds_read_b64_tr_b16 v[38:39], v194 offset:50176\n\t"
        "s_add_u32 s44, s44, 0x1000\n\t"
        "s_and_b32 s44, s44, 0xffff\n\t"
        "buffer_load_dwordx4 v[64:67], v192, s[40:43], s44 offen offset:0 sc1\n\t"
        "s_waitcnt vmcnt(11) lgkmcnt(14)\n\t"
        "v_mfma_f32_32x32x16_bf16 v[0:15], v[40:43], v[148:151], v[0:15]\n\t"
        "ds_read_b64_tr_b16 v[40:41], v193 offset:51200\n\t"
        "ds_read_b64_tr_b16 v[42:43], v193 offset:52224\n\t"
        "s_waitcnt lgkmcnt(14)\n\t"
        "v_mfma_f32_32x32x16_bf16 v[16:31], v[44:47], v[148:151], v[16:31]\n\t"
        "ds_read_b64_tr_b16 v[44:45], v194 offset:51200\n\t"
        "ds_read_b64_tr_b16 v[46:47], v194 offset:52224\n\t"
        "buffer_load_dwordx4 v[68:71], v192, s[40:43], s44 offen offset:1024 sc1\n\t"
        "s_waitcnt vmcnt(11) lgkmcnt(14)\n\t"
        "v_mfma_f32_32x32x16_bf16 v[0:15], v[48:51], v[152:155], v[0:15]\n\t"
        "ds_read_b64_tr_b16 v[48:49], v193 offset:53248\n\t"
        "ds_read_b64_tr_b16 v[50:51], v193 offset:54272\n\t"
        "s_waitcnt lgkmcnt(14)\n\t"
        "v_mfma_f32_32x32x16_bf16 v[16:31], v[52:55], v[152:155], v[16:31]\n\t"
        "ds_read_b64_tr_b16 v[52:53], v194 offset:53248\n\t"
        "ds_read_b64_tr_b16 v[54:55], v194 offset:54272\n\t"
        "buffer_load_dwordx4 v[72:75], v192, s[40:43], s44 offen offset:2048 sc1\n\t"
        "s_waitcnt vmcnt(11) lgkmcnt(14)\n\t"
        "v_mfma_f32_32x32x16_bf16 v[0:15], v[56:59], v[156:159], v[0:15]\n\t"
        "ds_read_b64_tr_b16 v[56:57], v193 offset:55296\n\t"
        "ds_read_b64_tr_b16 v[58:59], v193 offset:56320\n\t"
        "s_waitcnt lgkmcnt(14)\n\t"
        "v_mfma_f32_32x32x16_bf16 v[16:31], v[60:63], v[156:159], v[16:31]\n\t"
        "ds_read_b64_tr_b16 v[60:61], v194 offset:55296\n\t"
        "ds_read_b64_tr_b16 v[62:63], v194 offset:56320\n\t"
        "buffer_load_dwordx4 v[76:79], v192, s[40:43], s44 offen offset:3072 sc1\n\t"
        "s_waitcnt vmcnt(11) lgkmcnt(14)\n\t"
        "v_mfma_f32_32x32x16_bf16 v[0:15], v[32:35], v[160:163], v[0:15]\n\t"
        "ds_read_b64_tr_b16 v[32:33], v193 offset:57344\n\t"
        "ds_read_b64_tr_b16 v[34:35], v193 offset:58368\n\t"
        "s_waitcnt lgkmcnt(14)\n\t"
        "v_mfma_f32_32x32x16_bf16 v[16:31], v[36:39], v[160:163], v[16:31]\n\t"
        "ds_read_b64_tr_b16 v[36:37], v194 offset:57344\n\t"
        "ds_read_b64_tr_b16 v[38:39], v194 offset:58368\n\t"
        "s_add_u32 s44, s44, 0x1000\n\t"
        "s_and_b32 s44, s44, 0xffff\n\t"
        "buffer_load_dwordx4 v[80:83], v192, s[40:43], s44 offen offset:0 sc1\n\t"
        "s_waitcnt vmcnt(11) lgkmcnt(14)\n\t"
        "v_mfma_f32_32x32x16_bf16 v[0:15], v[40:43], v[164:167], v[0:15]\n\t"
        "ds_read_b64_tr_b16 v[40:41], v193 offset:59392\n\t"
        "ds_read_b64_tr_b16 v[42:43], v193 offset:60416\n\t"
        "s_waitcnt lgkmcnt(14)\n\t"
        "v_mfma_f32_32x32x16_bf16 v[16:31], v[44:47], v[164:167], v[16:31]\n\t"
        "ds_read_b64_tr_b16 v[44:45], v194 offset:59392\n\t"
        "ds_read_b64_tr_b16 v[46:47], v194 offset:60416\n\t"
        "buffer_load_dwordx4 v[84:87], v192, s[40:43], s44 offen offset:1024 sc1\n\t"
        "s_waitcnt vmcnt(11) lgkmcnt(14)\n\t"
        "v_mfma_f32_32x32x16_bf16 v[0:15], v[48:51], v[168:171], v[0:15]\n\t"
        "ds_read_b64_tr_b16 v[48:49], v193 offset:61440\n\t"
        "ds_read_b64_tr_b16 v[50:51], v193 offset:62464\n\t"
        "s_waitcnt lgkmcnt(14)\n\t"
        "v_mfma_f32_32x32x16_bf16 v[16:31], v[52:55], v[168:171], v[16:31]\n\t"
        "ds_read_b64_tr_b16 v[52:53], v194 offset:61440\n\t"
        "ds_read_b64_tr_b16 v[54:55], v194 offset:62464\n\t"
        "buffer_load_dwordx4 v[88:91], v192, s[40:43], s44 offen offset:2048 sc1\n\t"
        "s_waitcnt vmcnt(11) lgkmcnt(14)\n\t"
        "v_mfma_f32_32x32x16_bf16 v[0:15], v[56:59], v[172:175], v[0:15]\n\t"
        "ds_read_b64_tr_b16 v[56:57], v193 offset:63488\n\t"
        "ds_read_b64_tr_b16 v[58:59], v193 offset:64512\n\t"
        "s_waitcnt lgkmcnt(14)\n\t"
        "v_mfma_f32_32x32x16_bf16 v[16:31], v[60:63], v[172:175], v[16:31]\n\t"
        "ds_read_b64_tr_b16 v[60:61], v194 offset:63488\n\t"
        "ds_read_b64_tr_b16 v[62:63], v194 offset:64512\n\t"
        "buffer_load_dwordx4 v[92:95], v192, s[40:43], s44 offen offset:3072 sc1\n\t"
        "s_waitcnt vmcnt(11) lgkmcnt(14)\n\t"
        "v_mfma_f32_32x32x16_bf16 v[0:15], v[32:35], v[176:179], v[0:15]\n\t"
        "ds_read_b64_tr_b16 v[32:33], v195 offset:0\n\t"
        "ds_read_b64_tr_b16 v[34:35], v195 offset:1024\n\t"
        "s_waitcnt lgkmcnt(14)\n\t"
        "v_mfma_f32_32x32x16_bf16 v[16:31], v[36:39], v[176:179], v[16:31]\n\t"
        "ds_read_b64_tr_b16 v[36:37], v196 offset:0\n\t"
        "ds_read_b64_tr_b16 v[38:39], v196 offset:1024\n\t"
        "s_add_u32 s44, s44, 0x1000\n\t"
        "s_and_b32 s44, s44, 0xffff\n\t"
        "buffer_load_dwordx4 v[96:99], v192, s[40:43], s44 offen offset:0 sc1\n\t"
        "s_waitcnt vmcnt(11) lgkmcnt(14)\n\t"
        "v_mfma_f32_32x32x16_bf16 v[0:15], v[40:43], v[180:183], v[0:15]\n\t"
        "ds_read_b64_tr_b16 v[40:41], v195 offset:2048\n\t"
        "ds_read_b64_tr_b16 v[42:43], v195 offset:3072\n\t"
        "s_waitcnt lgkmcnt(14)\n\t"
        "v_mfma_f32_32x32x16_bf16 v[16:31], v[44:47], v[180:183], v[16:31]\n\t"
        "ds_read_b64_tr_b16 v[44:45], v196 offset:2048\n\t"
        "ds_read_b64_tr_b16 v[46:47], v196 offset:3072\n\t"
        "buffer_load_dwordx4 v[100:103], v192, s[40:43], s44 offen offset:1024 sc1\n\t"
        "s_waitcnt vmcnt(11) lgkmcnt(14)\n\t"
        "v_mfma_f32_32x32x16_bf16 v[0:15], v[48:51], v[184:187], v[0:15]\n\t"
        "ds_read_b64_tr_b16 v[48:49], v195 offset:4096\n\t"
        "ds_read_b64_tr_b16 v[50:51], v195 offset:5120\n\t"
        "s_waitcnt lgkmcnt(14)\n\t"
        "v_mfma_f32_32x32x16_bf16 v[16:31], v[52:55], v[184:187], v[16:31]\n\t"
        "ds_read_b64_tr_b16 v[52:53], v196 offset:4096\n\t"
        "ds_read_b64_tr_b16 v[54:55], v196 offset:5120\n\t"
        "buffer_load_dwordx4 v[104:107], v192, s[40:43], s44 offen offset:2048 sc1\n\t"
        "s_waitcnt vmcnt(11) lgkmcnt(14)\n\t"
        "v_mfma_f32_32x32x16_bf16 v[0:15], v[56:59], v[188:191], v[0:15]\n\t"
        "ds_read_b64_tr_b16 v[56:57], v195 offset:6144\n\t"
        "ds_read_b64_tr_b16 v[58:59], v195 offset:7168\n\t"
        "s_waitcnt lgkmcnt(14)\n\t"
        "v_mfma_f32_32x32x16_bf16 v[16:31], v[60:63], v[188:191], v[16:31]\n\t"
        "ds_read_b64_tr_b16 v[60:61], v196 offset:6144\n\t"
        "ds_read_b64_tr_b16 v[62:63], v196 offset:7168\n\t"
        "buffer_load_dwordx4 v[108:111], v192, s[40:43], s44 offen offset:3072 sc1\n\t"
        "s_waitcnt vmcnt(11) lgkmcnt(14)\n\t"
        "v_mfma_f32_32x32x16_bf16 v[0:15], v[32:35], v[64:67], v[0:15]\n\t"
        "ds_read_b64_tr_b16 v[32:33], v195 offset:8192\n\t"
        "ds_read_b64_tr_b16 v[34:35], v195 offset:9216\n\t"
        "s_waitcnt lgkmcnt(14)\n\t"
        "v_mfma_f32_32x32x16_bf16 v[16:31], v[36:39], v[64:67], v[16:31]\n\t"
        "ds_read_b64_tr_b16 v[36:37], v196 offset:8192\n\t"
        "ds_read_b64_tr_b16 v[38:39], v196 offset:9216\n\t"
        "s_add_u32 s44, s44, 0x1000\n\t"
        "s_and_b32 s44, s44, 0xffff\n\t"
        "buffer_load_dwordx4 v[112:115], v192, s[40:43], s44 offen offset:0 sc1\n\t"
        "s_waitcnt vmcnt(11) lgkmcnt(14)\n\t"
        "v_mfma_f32_32x32x16_bf16 v[0:15], v[40:43], v[68:71], v[0:15]\n\t"
        "ds_read_b64_tr_b16 v[40:41], v195 offset:10240\n\t"
        "ds_read_b64_tr_b16 v[42:43], v195 offset:11264\n\t"
        "s_waitcnt lgkmcnt(14)\n\t"
        "v_mfma_f32_32x32x16_bf16 v[16:31], v[44:47], v[68:71], v[16:31]\n\t"
        "ds_read_b64_tr_b16 v[44:45], v196 offset:10240\n\t"
        "ds_read_b64_tr_b16 v[46:47], v196 offset:11264\n\t"
        "buffer_load_dwordx4 v[116:119], v192, s[40:43], s44 offen offset:1024 sc1\n\t"
        "s_waitcnt vmcnt(11) lgkmcnt(14)\n\t"
        "v_mfma_f32_32x32x16_bf16 v[0:15], v[48:51], v[72:75], v[0:15]\n\t"
        "ds_read_b64_tr_b16 v[48:49], v195 offset:12288\n\t"
        "ds_read_b64_tr_b16 v[50:51], v195 offset:13312\n\t"
        "s_waitcnt lgkmcnt(14)\n\t"
        "v_mfma_f32_32x32x16_bf16 v[16:31], v[52:55], v[72:75], v[16:31]\n\t"
        "ds_read_b64_tr_b16 v[52:53], v196 offset:12288\n\t"
        "ds_read_b64_tr_b16 v[54:55], v196 offset:13312\n\t"
        "buffer_load_dwordx4 v[120:123], v192, s[40:43], s44 offen offset:2048 sc1\n\t"
        "s_waitcnt vmcnt(11) lgkmcnt(14)\n\t"
        "v_mfma_f32_32x32x16_bf16 v[0:15], v[56:59], v[76:79], v[0:15]\n\t"
        "ds_read_b64_tr_b16 v[56:57], v195 offset:14336\n\t"
        "ds_read_b64_tr_b16 v[58:59], v195 offset:15360\n\t"
        "s_waitcnt lgkmcnt(14)\n\t"
        "v_mfma_f32_32x32x16_bf16 v[16:31], v[60:63], v[76:79], v[16:31]\n\t"
        "ds_read_b64_tr_b16 v[60:61], v196 offset:14336\n\t"
        "ds_read_b64_tr_b16 v[62:63], v196 offset:15360\n\t"
        "buffer_load_dwordx4 v[124:127], v192, s[40:43], s44 offen offset:3072 sc1\n\t"
        "s_waitcnt vmcnt(11) lgkmcnt(14)\n\t"
        "v_mfma_f32_32x32x16_bf16 v[0:15], v[32:35], v[80:83], v[0:15]\n\t"
        "ds_read_b64_tr_b16 v[32:33], v195 offset:16384\n\t"
        "ds_read_b64_tr_b16 v[34:35], v195 offset:17408\n\t"
        "s_waitcnt lgkmcnt(14)\n\t"
        "v_mfma_f32_32x32x16_bf16 v[16:31], v[36:39], v[80:83], v[16:31]\n\t"
        "ds_read_b64_tr_b16 v[36:37], v196 offset:16384\n\t"
        "ds_read_b64_tr_b16 v[38:39], v196 offset:17408\n\t"
        "s_add_u32 s44, s44, 0x1000\n\t"
        "s_and_b32 s44, s44, 0xffff\n\t"
        "buffer_load_dwordx4 v[128:131], v192, s[40:43], s44 offen offset:0 sc1\n\t"
        "s_waitcnt vmcnt(11) lgkmcnt(14)\n\t"
        "v_mfma_f32_32x32x16_bf16 v[0:15], v[40:43], v[84:87], v[0:15]\n\t"
        "ds_read_b64_tr_b16 v[40:41], v195 offset:18432\n\t"
        "ds_read_b64_tr_b16 v[42:43], v195 offset:19456\n\t"
        "s_waitcnt lgkmcnt(14)\n\t"
        "v_mfma_f32_32x32x16_bf16 v[16:31], v[44:47], v[84:87], v[16:31]\n\t"
        "ds_read_b64_tr_b16 v[44:45], v196 offset:18432\n\t"
        "ds_read_b64_tr_b16 v[46:47], v196 offset:19456\n\t"
        "buffer_load_dwordx4 v[132:135], v192, s[40:43], s44 offen offset:1024 sc1\n\t"
        "s_waitcnt vmcnt(11) lgkmcnt(14)\n\t"
        "v_mfma_f32_32x32x16_bf16 v[0:15], v[48:51], v[88:91], v[0:15]\n\t"
        "ds_read_b64_tr_b16 v[48:49], v195 offset:20480\n\t"
        "ds_read_b64_tr_b16 v[50:51], v195 offset:21504\n\t"
        "s_waitcnt lgkmcnt(14)\n\t"
        "v_mfma_f32_32x32x16_bf16 v[16:31], v[52:55], v[88:91], v[16:31]\n\t"
        "ds_read_b64_tr_b16 v[52:53], v196 offset:20480\n\t"
        "ds_read_b64_tr_b16 v[54:55], v196 offset:21504\n\t"
        "buffer_load_dwordx4 v[136:139], v192, s[40:43], s44 offen offset:2048 sc1\n\t"
        "s_waitcnt vmcnt(11) lgkmcnt(14)\n\t"
        "v_mfma_f32_32x32x16_bf16 v[0:15], v[56:59], v[92:95], v[0:15]\n\t"
        "ds_read_b64_tr_b16 v[56:57], v195 offset:22528\n\t"
        "ds_read_b64_tr_b16 v[58:59], v195 offset:23552\n\t"
        "s_waitcnt lgkmcnt(14)\n\t"
        "v_mfma_f32_32x32x16_bf16 v[16:31], v[60:63], v[92:95], v[16:31]\n\t"
        "ds_read_b64_tr_b16 v[60:61], v196 offset:22528\n\t"
        "ds_read_b64_tr_b16 v[62:63], v196 offset:23552\n\t"
        "buffer_load_dwordx4 v[140:143], v192, s[40:43], s44 offen offset:3072 sc1\n\t"
        "s_waitcnt vmcnt(11) lgkmcnt(14)\n\t"
        "v_mfma_f32_32x32x16_bf16 v[0:15], v[32:35], v[96:99], v[0:15]\n\t"
        "ds_read_b64_tr_b16 v[32:33], v195 offset:24576\n\t"
        "ds_read_b64_tr_b16 v[34:35], v195 offset:25600\n\t"
        "s_waitcnt lgkmcnt(14)\n\t"
        "v_mfma_f32_32x32x16_bf16 v[16:31], v[36:39], v[96:99], v[16:31]\n\t"
        "ds_read_b64_tr_b16 v[36:37], v196 offset:24576\n\t"
        "ds_read_b64_tr_b16 v[38:39], v196 offset:25600\n\t"
        "s_add_u32 s44, s44, 0x1000\n\t"
        "s_and_b32 s44, s44, 0xffff\n\t"
        "buffer_load_dwordx4 v[144:147], v192, s[40:43], s44 offen offset:0 sc1\n\t"
        "s_waitcnt vmcnt(11) lgkmcnt(14)\n\t"
        "v_mfma_f32_32x32x16_bf16 v[0:15], v[40:43], v[100:103], v[0:15]\n\t"
        "ds_read_b64_tr_b16 v[40:41], v195 offset:26624\n\t"
        "ds_read_b64_tr_b16 v[42:43], v195 offset:27648\n\t"
        "s_waitcnt lgkmcnt(14)\n\t"
        "v_mfma_f32_32x32x16_bf16 v[16:31], v[44:47], v[100:103], v[16:31]\n\t"
        "ds_read_b64_tr_b16 v[44:45], v196 offset:26624\n\t"
        "ds_read_b64_tr_b16 v[46:47], v196 offset:27648\n\t"
        "buffer_load_dwordx4 v[148:151], v192, s[40:43], s44 offen offset:1024 sc1\n\t"
        "s_waitcnt vmcnt(11) lgkmcnt(14)\n\t"
        "v_mfma_f32_32x32x16_bf16 v[0:15], v[48:51], v[104:107], v[0:15]\n\t"
        "ds_read_b64_tr_b16 v[48:49], v195 offset:28672\n\t"
        "ds_read_b64_tr_b16 v[50:51], v195 offset:29696\n\t"
        "s_waitcnt lgkmcnt(14)\n\t"
        "v_mfma_f32_32x32x16_bf16 v[16:31], v[52:55], v[104:107], v[16:31]\n\t"
        "ds_read_b64_tr_b16 v[52:53], v196 offset:28672\n\t"
        "ds_read_b64_tr_b16 v[54:55], v196 offset:29696\n\t"
        "buffer_load_dwordx4 v[152:155], v192, s[40:43], s44 offen offset:2048 sc1\n\t"
        "s_waitcnt vmcnt(11) lgkmcnt(14)\n\t"
        "v_mfma_f32_32x32x16_bf16 v[0:15], v[56:59], v[108:111], v[0:15]\n\t"
        "ds_read_b64_tr_b16 v[56:57], v195 offset:30720\n\t"
        "ds_read_b64_tr_b16 v[58:59], v195 offset:31744\n\t"
        "s_waitcnt lgkmcnt(14)\n\t"
        "v_mfma_f32_32x32x16_bf16 v[16:31], v[60:63], v[108:111], v[16:31]\n\t"
        "ds_read_b64_tr_b16 v[60:61], v196 offset:30720\n\t"
        "ds_read_b64_tr_b16 v[62:63], v196 offset:31744\n\t"
        "buffer_load_dwordx4 v[156:159], v192, s[40:43], s44 offen offset:3072 sc1\n\t"
        "s_waitcnt vmcnt(11) lgkmcnt(14)\n\t"
        "v_mfma_f32_32x32x16_bf16 v[0:15], v[32:35], v[112:115], v[0:15]\n\t"
        "ds_read_b64_tr_b16 v[32:33], v195 offset:32768\n\t"
        "ds_read_b64_tr_b16 v[34:35], v195 offset:33792\n\t"
        "s_waitcnt lgkmcnt(14)\n\t"
        "v_mfma_f32_32x32x16_bf16 v[16:31], v[36:39], v[112:115], v[16:31]\n\t"
        "ds_read_b64_tr_b16 v[36:37], v196 offset:32768\n\t"
        "ds_read_b64_tr_b16 v[38:39], v196 offset:33792\n\t"
        "s_add_u32 s44, s44, 0x1000\n\t"
        "s_and_b32 s44, s44, 0xffff\n\t"
        "buffer_load_dwordx4 v[160:163], v192, s[40:43], s44 offen offset:0 sc1\n\t"
        "s_waitcnt vmcnt(11) lgkmcnt(14)\n\t"
        "v_mfma_f32_32x32x16_bf16 v[0:15], v[40:43], v[116:119], v[0:15]\n\t"
        "ds_read_b64_tr_b16 v[40:41], v195 offset:34816\n\t"
        "ds_read_b64_tr_b16 v[42:43], v195 offset:35840\n\t"
        "s_waitcnt lgkmcnt(14)\n\t"
        "v_mfma_f32_32x32x16_bf16 v[16:31], v[44:47], v[116:119], v[16:31]\n\t"
        "ds_read_b64_tr_b16 v[44:45], v196 offset:34816\n\t"
        "ds_read_b64_tr_b16 v[46:47], v196 offset:35840\n\t"
        "buffer_load_dwordx4 v[164:167], v192, s[40:43], s44 offen offset:1024 sc1\n\t"
        "s_waitcnt vmcnt(11) lgkmcnt(14)\n\t"
        "v_mfma_f32_32x32x16_bf16 v[0:15], v[48:51], v[120:123], v[0:15]\n\t"
        "ds_read_b64_tr_b16 v[48:49], v195 offset:36864\n\t"
        "ds_read_b64_tr_b16 v[50:51], v195 offset:37888\n\t"
        "s_waitcnt lgkmcnt(14)\n\t"
        "v_mfma_f32_32x32x16_bf16 v[16:31], v[52:55], v[120:123], v[16:31]\n\t"
        "ds_read_b64_tr_b16 v[52:53], v196 offset:36864\n\t"
        "ds_read_b64_tr_b16 v[54:55], v196 offset:37888\n\t"
        "buffer_load_dwordx4 v[168:171], v192, s[40:43], s44 offen offset:2048 sc1\n\t"
        "s_waitcnt vmcnt(11) lgkmcnt(14)\n\t"
        "v_mfma_f32_32x32x16_bf16 v[0:15], v[56:59], v[124:127], v[0:15]\n\t"
        "ds_read_b64_tr_b16 v[56:57], v195 offset:38912\n\t"
        "ds_read_b64_tr_b16 v[58:59], v195 offset:39936\n\t"
        "s_waitcnt lgkmcnt(14)\n\t"
        "v_mfma_f32_32x32x16_bf16 v[16:31], v[60:63], v[124:127], v[16:31]\n\t"
        "ds_read_b64_tr_b16 v[60:61], v196 offset:38912\n\t"
        "ds_read_b64_tr_b16 v[62:63], v196 offset:39936\n\t"
        "buffer_load_dwordx4 v[172:175], v192, s[40:43], s44 offen offset:3072 sc1\n\t"
        "s_waitcnt vmcnt(11) lgkmcnt(14)\n\t"
        "v_mfma_f32_32x32x16_bf16 v[0:15], v[32:35], v[128:131], v[0:15]\n\t"
        "ds_read_b64_tr_b16 v[32:33], v195 offset:40960\n\t"
        "ds_read_b64_tr_b16 v[34:35], v195 offset:41984\n\t"
        "s_waitcnt lgkmcnt(14)\n\t"
        "v_mfma_f32_32x32x16_bf16 v[16:31], v[36:39], v[128:131], v[16:31]\n\t"
        "ds_read_b64_tr_b16 v[36:37], v196 offset:40960\n\t"
        "ds_read_b64_tr_b16 v[38:39], v196 offset:41984\n\t"
        "s_add_u32 s44, s44, 0x1000\n\t"
        "s_and_b32 s44, s44, 0xffff\n\t"
        "buffer_load_dwordx4 v[176:179], v192, s[40:43], s44 offen offset:0 sc1\n\t"
        "s_waitcnt vmcnt(11) lgkmcnt(14)\n\t"
        "v_mfma_f32_32x32x16_bf16 v[0:15], v[40:43], v[132:135], v[0:15]\n\t"
        "ds_read_b64_tr_b16 v[40:41], v195 offset:43008\n\t"
        "ds_read_b64_tr_b16 v[42:43], v195 offset:44032\n\t"
        "s_waitcnt lgkmcnt(14)\n\t"
        "v_mfma_f32_32x32x16_bf16 v[16:31], v[44:47], v[132:135], v[16:31]\n\t"
        "ds_read_b64_tr_b16 v[44:45], v196 offset:43008\n\t"
        "ds_read_b64_tr_b16 v[46:47], v196 offset:44032\n\t"
        "buffer_load_dwordx4 v[180:183], v192, s[40:43], s44 offen offset:1024 sc1\n\t"
        "s_waitcnt vmcnt(11) lgkmcnt(14)\n\t"
        "v_mfma_f32_32x32x16_bf16 v[0:15], v[48:51], v[136:139], v[0:15]\n\t"
        "ds_read_b64_tr_b16 v[48:49], v195 offset:45056\n\t"
        "ds_read_b64_tr_b16 v[50:51], v195 offset:46080\n\t"
        "s_waitcnt lgkmcnt(14)\n\t"
        "v_mfma_f32_32x32x16_bf16 v[16:31], v[52:55], v[136:139], v[16:31]\n\t"
        "ds_read_b64_tr_b16 v[52:53], v196 offset:45056\n\t"
        "ds_read_b64_tr_b16 v[54:55], v196 offset:46080\n\t"
        "buffer_load_dwordx4 v[184:187], v192, s[40:43], s44 offen offset:2048 sc1\n\t"
        "s_waitcnt vmcnt(11) lgkmcnt(14)\n\t"
        "v_mfma_f32_32x32x16_bf16 v[0:15], v[56:59], v[140:143], v[0:15]\n\t"
        "ds_read_b64_tr_b16 v[56:57], v195 offset:47104\n\t"
        "ds_read_b64_tr_b16 v[58:59], v195 offset:48128\n\t"
        "s_waitcnt lgkmcnt(14)\n\t"
        "v_mfma_f32_32x32x16_bf16 v[16:31], v[60:63], v[140:143], v[16:31]\n\t"
        "ds_read_b64_tr_b16 v[60:61], v196 offset:47104\n\t"
        "ds_read_b64_tr_b16 v[62:63], v196 offset:48128\n\t"
        "buffer_load_dwordx4 v[188:191], v192, s[40:43], s44 offen offset:3072 sc1\n\t"
        "s_waitcnt vmcnt(11) lgkmcnt(14)\n\t"
        "v_mfma_f32_32x32x16_bf16 v[0:15], v[32:35], v[144:147], v[0:15]\n\t"
        "ds_read_b64_tr_b16 v[32:33], v195 offset:49152\n\t"
        "ds_read_b64_tr_b16 v[34:35], v195 offset:50176\n\t"
        "s_waitcnt lgkmcnt(14)\n\t"
        "v_mfma_f32_32x32x16_bf16 v[16:31], v[36:39], v[144:147], v[16:31]\n\t"
        "ds_read_b64_tr_b16 v[36:37], v196 offset:49152\n\t"
        "ds_read_b64_tr_b16 v[38:39], v196 offset:50176\n\t"
        "s_waitcnt vmcnt(10) lgkmcnt(14)\n\t"
        "v_mfma_f32_32x32x16_bf16 v[0:15], v[40:43], v[148:151], v[0:15]\n\t"
        "ds_read_b64_tr_b16 v[40:41], v195 offset:51200\n\t"
        "ds_read_b64_tr_b16 v[42:43], v195 offset:52224\n\t"
        "s_waitcnt lgkmcnt(14)\n\t"
        "v_mfma_f32_32x32x16_bf16 v[16:31], v[44:47], v[148:151], v[16:31]\n\t"
        "ds_read_b64_tr_b16 v[44:45], v196 offset:51200\n\t"
        "ds_read_b64_tr_b16 v[46:47], v196 offset:52224\n\t"
        "s_waitcnt vmcnt(9) lgkmcnt(14)\n\t"
        "v_mfma_f32_32x32x16_bf16 v[0:15], v[48:51], v[152:155], v[0:15]\n\t"
        "ds_read_b64_tr_b16 v[48:49], v195 offset:53248\n\t"
        "ds_read_b64_tr_b16 v[50:51], v195 offset:54272\n\t"
        "s_waitcnt lgkmcnt(14)\n\t"
        "v_mfma_f32_32x32x16_bf16 v[16:31], v[52:55], v[152:155], v[16:31]\n\t"
        "ds_read_b64_tr_b16 v[52:53], v196 offset:53248\n\t"
        "ds_read_b64_tr_b16 v[54:55], v196 offset:54272\n\t"
        "s_waitcnt vmcnt(8) lgkmcnt(14)\n\t"
        "v_mfma_f32_32x32x16_bf16 v[0:15], v[56:59], v[156:159], v[0:15]\n\t"
        "ds_read_b64_tr_b16 v[56:57], v195 offset:55296\n\t"
        "ds_read_b64_tr_b16 v[58:59], v195 offset:56320\n\t"
        "s_waitcnt lgkmcnt(14)\n\t"
        "v_mfma_f32_32x32x16_bf16 v[16:31], v[60:63], v[156:159], v[16:31]\n\t"
        "ds_read_b64_tr_b16 v[60:61], v196 offset:55296\n\t"
        "ds_read_b64_tr_b16 v[62:63], v196 offset:56320\n\t"
        "s_waitcnt vmcnt(7) lgkmcnt(14)\n\t"
        "v_mfma_f32_32x32x16_bf16 v[0:15], v[32:35], v[160:163], v[0:15]\n\t"
        "s_barrier\n\t"
        "ds_read_b64_tr_b16 v[32:33], v195 offset:57344\n\t"
        "ds_read_b64_tr_b16 v[34:35], v195 offset:58368\n\t"
        "s_waitcnt lgkmcnt(14)\n\t"
        "v_mfma_f32_32x32x16_bf16 v[16:31], v[36:39], v[160:163], v[16:31]\n\t"
        "ds_read_b64_tr_b16 v[36:37], v196 offset:57344\n\t"
        "ds_read_b64_tr_b16 v[38:39], v196 offset:58368\n\t"
        "s_waitcnt vmcnt(6) lgkmcnt(14)\n\t"
        "v_mfma_f32_32x32x16_bf16 v[0:15], v[40:43], v[164:167], v[0:15]\n\t"
        "ds_read_b64_tr_b16 v[40:41], v195 offset:59392\n\t"
        "ds_read_b64_tr_b16 v[42:43], v195 offset:60416\n\t"
        "s_waitcnt lgkmcnt(14)\n\t"
        "v_mfma_f32_32x32x16_bf16 v[16:31], v[44:47], v[164:167], v[16:31]\n\t"
        "ds_read_b64_tr_b16 v[44:45], v196 offset:59392\n\t"
        "ds_read_b64_tr_b16 v[46:47], v196 offset:60416\n\t"
        "s_waitcnt vmcnt(5) lgkmcnt(14)\n\t"
        "v_mfma_f32_32x32x16_bf16 v[0:15], v[48:51], v[168:171], v[0:15]\n\t"
        "ds_read_b64_tr_b16 v[48:49], v195 offset:61440\n\t"
        "ds_read_b64_tr_b16 v[50:51], v195 offset:62464\n\t"
        "s_waitcnt lgkmcnt(14)\n\t"
        "v_mfma_f32_32x32x16_bf16 v[16:31], v[52:55], v[168:171], v[16:31]\n\t"
        "ds_read_b64_tr_b16 v[52:53], v196 offset:61440\n\t"
        "ds_read_b64_tr_b16 v[54:55], v196 offset:62464\n\t"
        "s_waitcnt vmcnt(4) lgkmcnt(14)\n\t"
        "v_mfma_f32_32x32x16_bf16 v[0:15], v[56:59], v[172:175], v[0:15]\n\t"
        "ds_read_b64_tr_b16 v[56:57], v195 offset:63488\n\t"
        "ds_read_b64_tr_b16 v[58:59], v195 offset:64512\n\t"
        "s_waitcnt lgkmcnt(14)\n\t"
        "v_mfma_f32_32x32x16_bf16 v[16:31], v[60:63], v[172:175], v[16:31]\n\t"
        "ds_read_b64_tr_b16 v[60:61], v196 offset:63488\n\t"
        "ds_read_b64_tr_b16 v[62:63], v196 offset:64512\n\t"
        "s_waitcnt vmcnt(3) lgkmcnt(14)\n\t"
        "v_mfma_f32_32x32x16_bf16 v[0:15], v[32:35], v[176:179], v[0:15]\n\t"
        "s_waitcnt lgkmcnt(12)\n\t"
        "v_mfma_f32_32x32x16_bf16 v[16:31], v[36:39], v[176:179], v[16:31]\n\t"
        "s_waitcnt vmcnt(2) lgkmcnt(10)\n\t"
        "v_mfma_f32_32x32x16_bf16 v[0:15], v[40:43], v[180:183], v[0:15]\n\t"
        "s_waitcnt lgkmcnt(8)\n\t"
        "v_mfma_f32_32x32x16_bf16 v[16:31], v[44:47], v[180:183], v[16:31]\n\t"
        "s_waitcnt vmcnt(1) lgkmcnt(6)\n\t"
        "v_mfma_f32_32x32x16_bf16 v[0:15], v[48:51], v[184:187], v[0:15]\n\t"
        "s_waitcnt lgkmcnt(4)\n\t"
        "v_mfma_f32_32x32x16_bf16 v[16:31], v[52:55], v[184:187], v[16:31]\n\t"
        "s_waitcnt vmcnt(0) lgkmcnt(2)\n\t"
        "v_mfma_f32_32x32x16_bf16 v[0:15], v[56:59], v[188:191], v[0:15]\n\t"
        "s_waitcnt lgkmcnt(0)\n\t"
        "v_mfma_f32_32x32x16_bf16 v[16:31], v[60:63], v[188:191], v[16:31]\n\t"
        "s_nop 11\n\t"
        : "={v[0:15]}"(acc[0]), "={v[16:31]}"(acc[1]), "+{s44}"(soff) STAMP_OUTS
        : "{v192}"(OFF_OF + L * (5 * MBY) + tile * TS + lane * 16), "{v193}"(a0), "{v194}"(a1), "{v195}"(a0 + 65536), "{v196}"(a1 + 65536),
          "{v198}"(lbase + L_W + 20), "{v199}"(L + 1), "{s[40:43]}"(srd)
        : "v32", "v33", "v34", "v35", "v36", "v37", "v38", "v39", "v40", "v41", "v42", "v43", "v44", "v45", "v46", "v47", "v48", "v49", "v50", "v51", "v52", "v53", "v54", "v55", "v56", "v57", "v58", "v59", "v60", "v61", "v62", "v63", "v64", "v65", "v66", "v67", "v68", "v69", "v70", "v71", "v72", "v73", "v74", "v75", "v76", "v77", "v78", "v79", "v80", "v81", "v82", "v83", "v84", "v85", "v86", "v87", "v88", "v89", "v90", "v91", "v92", "v93", "v94", "v95", "v96", "v97", "v98", "v99", "v100", "v101", "v102", "v103", "v104", "v105", "v106", "v107", "v108", "v109", "v110", "v111", "v112", "v113", "v114", "v115", "v116", "v117", "v118", "v119", "v120", "v121", "v122", "v123", "v124", "v125", "v126", "v127", "v128", "v129", "v130", "v131", "v132", "v133", "v134", "v135", "v136", "v137", "v138", "v139", "v140", "v141", "v142", "v143", "v144", "v145", "v146", "v147", "v148", "v149", "v150", "v151", "v152", "v153", "v154", "v155", "v156", "v157", "v158", "v159", "v160", "v161", "v162", "v163", "v164", "v165", "v166", "v167", "v168", "v169", "v170", "v171", "v172", "v173", "v174", "v175", "v176", "v177", "v178", "v179", "v180", "v181", "v182", "v183", "v184", "v185", "v186", "v187", "v188", "v189", "v190", "v191", "v200", "s45", "vcc", "scc", "memory");
      }
      PHASE_VARS;
      STAMP_PUT(12 + 16 * L);
      STAMPV(15 + 16 * L);
      float s = 0.f, sq = 0.f, d = 0.f;
      const unsigned xdst = (L ? OOB : OFF_X1 + tile * TS + tn * 4096) + lane * 16;
#pragma unroll
      for (int g = 0; g < 8; ++g) {
#pragma unroll
        for (int e = 0; e < 2; ++e) {
          const unsigned rr = res[g >> 1][2 * (g & 1) + e];
          const unsigned t = pack2(acc[g >> 2][4 * (g & 3) + 2 * e], acc[g >> 2][4 * (g & 3) + 2 * e + 1]);
          x1p[g >> 1][2 * (g & 1) + e] = pack2(lo16(rr) + lo16(t), hi16(rr) + hi16(t));
        }
        if (g & 1) { stg(rs, xdst + (g >> 1) * 1024, x1p[g >> 1]); SBAR(); }
      }
#pragma unroll
      for (int g = 0; g < 8; ++g)
#pragma unroll
        for (int e = 0; e < 2; ++e) {
          const unsigned o = x1p[g >> 1][2 * (g & 1) + e];
          s = dot2(o, 0x3f803f80u, s); sq = dot2(o, o, sq); d = dot2(o, wrc[g][e], d);
        }
      pst = u32x4{nzf(half_swap_sum(s)), nzf(half_swap_sum(sq)), nzf(half_swap_sum(d)), 0u};
    }
    STAMPV(16 + 16 * L);
  }
  {
    PHASE_VARS;
    float S, Q, Dd, mean, var, mb1, rd1;
    stg(rs, stats_dst(OFF_SX + SX_LAYER, tile, tn, lane), pst);
    __builtin_amdgcn_s_sleep(10);
    row_stats_sum(rs, OFF_SX + SX_LAYER, tile, lane, S, Q, Dd);
    STAMPV(33);
    row_stats_fin(smem, 2, tile, lane, S, Q, Dd, U2F(words[0]), x1p, mean, var, mb1, rd1);
    STAMPV(34);
    u32x4 xo[4];
#pragma unroll
    for (int i = 0; i < 4; ++i) {
#pragma unroll
      for (int e = 0; e < 4; ++e) {
        const unsigned h2 = ln_pair(x1p[i][e], mb1, rd1);
        xo[i][e] = pack2(lo16(x1p[i][e]) + lo16(h2), hi16(x1p[i][e]) + hi16(h2));
      }
      *reinterpret_cast<u32x4*>(smem + wid * 4096 + r32 * 128 + ((2 * i + hi) ^ (r32 & 7)) * 16) = fo_swap(xo[i]);
    }
    asm volatile("s_waitcnt lgkmcnt(0)" ::: "memory");
#pragma unroll
    for (int i = 0; i < 4; ++i) { xo[i] = *reinterpret_cast<const u32x4*>(smem + wid * 4096 + i * 1024 + (lane >> 3) * 128 + ((lane ^ (lane >> 3)) & 7) * 16); PIN(xo[i]); }
    u16* dst = out + (size_t)(tile * 32 + (lane >> 3)) * D_ + tn * 64 + (lane & 7) * 8;
    lds_wait_ge(lbase + L_W + 4 + 4 * (lane & 1), 2);
    const int rel = (int)words[3] - tile * 32 - (lane >> 3);
#pragma unroll
    for (int i = 0; i < 4; ++i)
      if (rel != 8 * i) st16(dst + (size_t)i * 8 * D_, xo[i]);
    STAMPV(35);
    STAMP_END;
  }
}

extern "C" __global__ __launch_bounds__(512) void prep_kernel(const u16* __restrict__ x0, const u16* __restrict__ wqkv, char* __restrict__ ws) {
  __shared__ __attribute__((aligned(16))) char sm[128 * 144];
  const int tid = threadIdx.x, lane = tid & 63, wid = __builtin_amdgcn_readfirstlane(tid >> 6), b = blockIdx.x;
  if (b >= 256) {
    const int xcd = b & 7, slot = (b - 256) >> 3;
    if (tid < (int)(FL_COUNT / 256)) { u32x4 z = {0u, 0u, 0u, 0u}; st16(ws + OFF_FL + ((b - 256) * (FL_COUNT / 256) + tid) * 128, z); }
    unsigned acc = 0;
    {
      const u16* p = wqkv + (size_t)(slot * 32 + (tid >> 5)) * (3 * D_) + ((xcd & 3) * 4 + ((tid >> 3) & 3)) * 64 + (tid & 7) * 8;
#pragma unroll
      for (int i = 0; i < 6; ++i) {
        const u32x4 v = *reinterpret_cast<const u32x4*>(p + (size_t)(i & 1) * 16 * (3 * D_) + (i >> 1) * D_);
        acc |= v[0] ^ v[3];
      }
    }
    if (slot < 8) {
      const char *c0, *c1;
      CODE_ADDR(mega_kernel, c0); CODE_ADDR(prep_kernel, c1);
      const long long o = (slot * 512 + tid) * 16;
      if (o + 16 <= c1 - c0) acc |= (*reinterpret_cast<const __attribute__((address_space(1))) u32x4*>((uintptr_t)(c0 + o)))[0];
    }
    asm volatile("" :: "v"(acc));
    return;
  }
  u32x4 v[2];
  asm volatile("global_load_dwordx4 %0, %2, off\n\tglobal_load_dwordx4 %1, %2, off offset:1024"
               : "=&v"(v[0]), "=&v"(v[1]) : "v"(x0 + (size_t)(b * 8 + wid) * D_ + lane * 8) : "memory");
  int pass;
  asm volatile("s_mov_b32 %0, %1" : "=s"(pass) : "n"(0));
#pragma unroll 1
  for (;;) {
    asm volatile("s_cmp_eq_u32 %2, 0\n\ts_cbranch_scc1 1f\n\ts_waitcnt vmcnt(0)\n1:" : "+v"(v[0]), "+v"(v[1]) : "s"(pass) : "memory", "scc");
    float s = 0.f;
#pragma unroll
    for (int i = 0; i < 2; ++i)
#pragma unroll
      for (int e = 0; e < 4; ++e) s = dot2(v[i][e], 0x3f803f80u, s);
    const float mean = wave_sum(s) * (1.0f / 1024.0f);
    float m2 = 0.f;
#pragma unroll
    for (int i = 0; i < 2; ++i)
#pragma unroll
      for (int e = 0; e < 4; ++e) { const float a = lo16(v[i][e]) - mean, c = hi16(v[i][e]) - mean; m2 += a * a + c * c; }
    float mb, den;
    ln_scalars(mean, wave_sum(m2) * (1.0f / 1024.0f), mb, den);
    const float rd = 1.0f / den;
#pragma unroll
    for (int i = 0; i < 2; ++i) {
      char* p = sm + ((32 * i + (lane >> 1)) * 2) * 144 + wid * 16 + (lane & 1) * 8;
      u32x2 lo = {ln_pair(v[i][0], mb, rd), ln_pair(v[i][1], mb, rd)}, hi = {ln_pair(v[i][2], mb, rd), ln_pair(v[i][3], mb, rd)};
      *reinterpret_cast<u32x2*>(p) = lo;
      *reinterpret_cast<u32x2*>(p + 144) = hi;
    }
    if (pass) break;
    asm volatile("s_mov_b32 %0, 1" : "=s"(pass));
  }
  __syncthreads();
  char* dst = ws + OFF_HF + (size_t)(b >> 2) * TS + (b & 3) * 128;
#pragma unroll
  for (int i = 0; i < 2; ++i) {
    const int q = i * 512 + tid, pc = q >> 3, tk = q & 7;
    st16(dst + pc * 512 + tk * 16, *reinterpret_cast<const u32x4*>(sm + pc * 144 + tk * 16));
  }
}

extern "C" void kernel_launch(void* const* d_in, const int* in_sizes, int n_in, void* d_out, int out_size,
                              void* d_ws, size_t ws_size, hipStream_t stream) {
  char* ws = (char*)d_ws;
  prep_kernel<<<512, 512, 0, stream>>>((const u16*)d_in[0], (const u16*)d_in[1], ws);
  mega_kernel<<<256, 512, 0, stream>>>((const u16*)d_in[0], (const u16*)d_in[1], (const u16*)d_in[2], (const u16*)d_in[4], (const u16*)d_in[5],
                                       (const u16*)d_in[6], (const u16*)d_in[7], ws, (u16*)d_out);
}
```

```cpp
#include <hip/hip_runtime.h>
#include <hip/hip_bf16.h>
#include <stdint.h>

typedef unsigned short u16;
typedef short s16x4 __attribute__((ext_vector_type(4)));
typedef short s16x8 __attribute__((ext_vector_type(8)));
typedef float f32x16 __attribute__((ext_vector_type(16)));
typedef float f32x8 __attribute__((ext_vector_type(8)));
typedef float f32x4 __attribute__((ext_vector_type(4)));
typedef float f32x2 __attribute__((ext_vector_type(2)));
typedef unsigned int u32x16 __attribute__((ext_vector_type(16)));
typedef unsigned int u32x8 __attribute__((ext_vector_type(8)));
typedef unsigned int u32x4 __attribute__((ext_vector_type(4)));
typedef unsigned int u32x2 __attribute__((ext_vector_type(2)));
typedef __bf16 bf16x2 __attribute__((ext_vector_type(2)));
typedef __bf16 bf16x8 __attribute__((ext_vector_type(8)));
typedef __bf16 bf16x16 __attribute__((ext_vector_type(16)));

#define DI __device__ __forceinline__
#define LDS_PTR(T, p) ((__attribute__((address_space(3))) T*)(p))
#define BARRIER() asm volatile("s_barrier" ::: "memory")
#define BAR_LGKM() asm volatile("s_waitcnt lgkmcnt(0)\n\ts_barrier" ::: "memory")
#define BAR_ALL() asm volatile("s_waitcnt vmcnt(0) lgkmcnt(0)\n\ts_barrier" ::: "memory")
#define DRAIN() asm volatile("s_waitcnt vmcnt(0)" ::: "memory")
#define PACE() ({ asm volatile("s_sleep 1\n\ts_waitcnt vmcnt(3)" ::: "memory"); })
#define MFMA32(a, b, c) __builtin_amdgcn_mfma_f32_32x32x16_bf16(a, b, c, 0, 0, 0)
#define MFMA16(a, b, c) __builtin_amdgcn_mfma_f32_16x16x32_bf16(a, b, c, 0, 0, 0)
#define PIN(x) asm volatile("" : "+v"(x))
#define EX2(x) __builtin_amdgcn_exp2f(x)
#define RCP(x) __builtin_amdgcn_rcpf(x)
#define SBAR() __builtin_amdgcn_sched_barrier(0)

#define STAMP_INIT
#define STAMPV(i)
#define STAMP_END
#define STAMP_DECL
#define STAMP_OUTS
#define STAMP_PUT(i)
constexpr int D_ = 1024, F_ = 3584;

constexpr unsigned MBY = 1u << 20;
constexpr unsigned TS = 65536 + 4096, HS = 262144 + 4096;
constexpr unsigned OFF_HF = 0, OFF_X1 = 5 * MBY;
constexpr unsigned OFF_OF = 10 * MBY;
constexpr unsigned OFF_KV = 20 * MBY;
constexpr unsigned OFF_FL = 40 * MBY;
constexpr unsigned FL_KV = OFF_FL;
constexpr unsigned FL_O = OFF_FL + 65536;
constexpr unsigned FL_X = OFF_FL + 131072;
constexpr unsigned FL_COUNT = 1280;
constexpr unsigned OFF_CS = 41 * MBY;
constexpr unsigned OFF_SX = 42 * MBY;
constexpr unsigned SX_ST = 0;
constexpr unsigned SX_ROW = 512 * 1024;
constexpr unsigned SX_CAND = SX_ROW + 32 * 1024;
constexpr unsigned SX_GP = SX_CAND + 32 * 1024;
constexpr unsigned SX_YP = SX_GP + 224 * 1024;
constexpr unsigned SX_LAYER = SX_YP + 224 * 1024;
constexpr unsigned SX_QP = 2 * SX_LAYER;
constexpr unsigned SX_BYTES = SX_QP + 512 * 1024;
static_assert(SX_BYTES <= 5 * 256 * 128 * 16, "sentinel fill");
constexpr unsigned WS_BYTES = 48 * MBY, OOB = 0x80000000u;
constexpr unsigned SENT = 0xFFFFFFFFu, TAG = 1u;

constexpr int NS = 9, DD = 3, SLOTB = 12288;
constexpr int L_HB = 110592, L_XB = L_HB + 2048;
constexpr int L_XCH = 122880;
constexpr int L_SL = L_XCH + 8192;
constexpr int L_CAND = L_XCH + 32768;
constexpr int L_AB = L_XCH + 34816;
constexpr int L_FULL = 159744, L_FREE = L_FULL + 512;
constexpr int L_CS = L_FREE + 1024, L_PATCH = L_CS + 768;
constexpr int L_GU = L_PATCH + 768, L_YS = L_GU + 256, L_WR = L_YS + 256;
constexpr int L_X2F = L_WR + 128, L_X2P = L_X2F + 128;
constexpr int L_W = L_X2P + 128;
constexpr int LDS_BYTES = 163840;
static_assert(NS * SLOTB <= L_HB && L_W + 64 <= LDS_BYTES && L_AB + 2048 <= L_FULL, "LDS map");

DI int fresh_lane() { int l; asm volatile("v_mbcnt_lo_u32_b32 %0, -1, 0\n\tv_mbcnt_hi_u32_b32 %0, -1, %0" : "=v"(l)); return l; }
#define LANE_VARS const int lane = fresh_lane(), r32 = lane & 31, hi = lane >> 5, tq = (lane & 15) >> 2, tp = lane & 3, tblk = (lane >> 4) & 1, \
  wrow = 4 * hi + tq, wsw = 4 * ((wrow >> 1) & 1), wch = 2 * tblk + (tp >> 1), c24 = r32 < 24 ? r32 : 23; \
  (void)r32; (void)hi; (void)wsw; (void)wch; (void)c24
DI int fresh_s(int v) { asm volatile("" : "+s"(v)); return v; }
#define PHASE_VARS const int tm = fresh_s(tm_), tn = fresh_s(tn_), wid = fresh_s(wid_), tile = tm * 4 + wid, hh = wid & 1, kh = wid >> 1; (void)tile; (void)hh; (void)kh; LANE_VARS
DI float bf2f(u16 v) { return __uint_as_float(((unsigned)v) << 16); }
DI unsigned pack2(float lo, float hi) { f32x2 v = {lo, hi}; return __builtin_bit_cast(unsigned, __builtin_convertvector(v, bf16x2)); }
DI float rbf(float f) { return __uint_as_float(pack2(f, 0.f) << 16); }
DI float lo16(unsigned u) { return __uint_as_float(u << 16); }
DI float hi16(unsigned u) { return __uint_as_float(u & 0xffff0000u); }
DI float opq(float r) { asm("" : "+v"(r)); return r; }
DI float fadd(float a, float b) { return opq(a + b); }
DI float fmul(float a, float b) { return opq(a * b); }
DI float ffma(float a, float b, float c) { return opq(__builtin_fmaf(a, b, c)); }
DI float U2F(unsigned u) { return __uint_as_float(u); }
DI float half_swap_sum(float v) {
  auto r = __builtin_amdgcn_permlane32_swap(__float_as_uint(v), __float_as_uint(v), false, false);
  return fadd(U2F(r[0]), U2F(r[1]));
}
DI float half_swap_max(float v) {
  auto r = __builtin_amdgcn_permlane32_swap(__float_as_uint(v), __float_as_uint(v), false, false);
  return fmaxf(__uint_as_float(r[0]), __uint_as_float(r[1]));
}
template <int CTRL> DI unsigned dpp(unsigned v) { return (unsigned)__builtin_amdgcn_update_dpp(0, (int)v, CTRL, 0xf, 0xf, false); }
DI float row_swap_sum(float v) {
  auto r = __builtin_amdgcn_permlane16_swap(__float_as_uint(v), __float_as_uint(v), false, false);
  return fadd(U2F(r[0]), U2F(r[1]));
}
DI float half_sum(float v) {
  v = fadd(v, U2F(dpp<0xB1>(__float_as_uint(v)))); v = fadd(v, U2F(dpp<0x4E>(__float_as_uint(v))));
  v = fadd(v, U2F(dpp<0x141>(__float_as_uint(v)))); v = fadd(v, U2F(dpp<0x140>(__float_as_uint(v))));
  return row_swap_sum(v);
}
DI float wave_sum(float v) { return half_swap_sum(half_sum(v)); }
DI unsigned half_max_u32(unsigned v) {
  v = max(v, dpp<0xB1>(v)); v = max(v, dpp<0x4E>(v)); v = max(v, dpp<0x141>(v)); v = max(v, dpp<0x140>(v));
  auto r = __builtin_amdgcn_permlane16_swap(v, v, false, false);
  return max(r[0], r[1]);
}
DI unsigned wave_max_u32(unsigned v) {
  v = half_max_u32(v);
  auto r = __builtin_amdgcn_permlane32_swap(v, v, false, false);
  return max(r[0], r[1]);
}
DI u32x4 fo_swap(u32x4 v) {
  auto a = __builtin_amdgcn_permlane32_swap(v[0], v[2], false, false);
  auto b = __builtin_amdgcn_permlane32_swap(v[1], v[3], false, false);
  u32x4 o; o[0] = a[0]; o[1] = b[0]; o[2] = a[1]; o[3] = b[1];
  return o;
}
DI void glds16(const void* gsrc, unsigned lds_dst) {
  unsigned keep;
  asm volatile("s_mov_b32 %0, m0\n\ts_mov_b32 m0, %2\n\ts_nop 0\n\tglobal_load_lds_dwordx4 %1, off\n\ts_mov_b32 m0, %0"
               : "=&s"(keep) : "v"(gsrc), "s"(lds_dst) : "memory");
}
#define GL_ "global_load_lds_dwordx4 "
DI void glds_v4(const void* sb, const unsigned (&v)[4], unsigned m0v) {
  unsigned keep;
  asm volatile("s_mov_b32 %0, m0\n\ts_mov_b32 m0, %6\n\ts_nop 0\n\t" GL_ "%1, %5\n\t" GL_ "%2, %5 offset:1024\n\t" GL_ "%3, %5 offset:2048\n\t"
               GL_ "%4, %5 offset:3072\n\ts_mov_b32 m0, %0" : "=&s"(keep) : "v"(v[0]), "v"(v[1]), "v"(v[2]), "v"(v[3]), "s"(sb), "s"(m0v) : "memory");
}
#define GP_ "\n\ts_sleep %8\n\ts_waitcnt vmcnt(%7)\n\t"
template <int TH, int SL> DI void glds_v4t(const void* sb, const unsigned (&v)[4], unsigned m0v) {
  unsigned keep;
  asm volatile("s_mov_b32 %0, m0\n\ts_mov_b32 m0, %6\n\ts_nop 0\n\t" GL_ "%1, %5" GP_ GL_ "%2, %5 offset:1024" GP_ GL_ "%3, %5 offset:2048" GP_ GL_ "%4, %5 offset:3072" GP_ "s_mov_b32 m0, %0"
               : "=&s"(keep) : "v"(v[0]), "v"(v[1]), "v"(v[2]), "v"(v[3]), "s"(sb), "s"(m0v), "n"(TH), "n"(SL) : "memory");
}
DI void glds_v3(const void* sb, const unsigned (&v)[3], unsigned m0v) {
  unsigned keep;
  asm volatile("s_mov_b32 %0, m0\n\ts_mov_b32 m0, %5\n\ts_nop 0\n\t" GL_ "%1, %4\n\t" GL_ "%2, %4 offset:1024\n\t" GL_ "%3, %4 offset:2048\n\t"
               "s_mov_b32 m0, %0" : "=&s"(keep) : "v"(v[0]), "v"(v[1]), "v"(v[2]), "s"(sb), "s"(m0v) : "memory");
}
DI float dot2(unsigned a, unsigned b, float c) {
  return __builtin_amdgcn_fdot2_f32_bf16(__builtin_bit_cast(bf16x2, a), __builtin_bit_cast(bf16x2, b), c, false);
}
#define CODE_ADDR(sym, out) do { unsigned long long pc_; unsigned off_; \
  asm volatile("s_getpc_b64 %0\n1:\n\ts_mov_b32 %1, " #sym "-1b" : "=s"(pc_), "=s"(off_)); out = (const char*)(pc_ + (long long)(int)off_); } while (0)
DI void st16(void* p, u32x4 v) { asm volatile("global_store_dwordx4 %0, %1, off sc0 sc1\n\ts_nop 1" :: "v"(p), "v"(v) : "memory"); }
DI void ln_scalars(float mean, float var, float& mb, float& den) { mb = rbf(mean); den = rbf(__builtin_amdgcn_sqrtf(rbf(rbf(var) + 1e-5f))); }
DI unsigned ln_pair(unsigned v, float mb, float rd) {
  const unsigned c = pack2(lo16(v) - mb, hi16(v) - mb);
  return pack2(lo16(c) * rd, hi16(c) * rd);
}
DI unsigned ln_pair_s(unsigned v, float nmb, float rd) {
  const unsigned c = pack2(fadd(lo16(v), nmb), fadd(hi16(v), nmb));
  return pack2(fmul(lo16(c), rd), fmul(hi16(c), rd));
}
template <int N, int M> DI f32x4 sum4(const u32x4 (&v)[M]) {
  f32x4 t;
#pragma unroll
  for (int e = 0; e < 4; ++e) {
    t[e] = U2F(v[0][e]);
#pragma unroll
    for (int i = 1; i < N; ++i) t[e] = fadd(t[e], U2F(v[i][e]));
  }
  return t;
}
#define PERM_LO(x, y) __builtin_amdgcn_perm(y, x, 0x05040100u)
#define PERM_HI(x, y) __builtin_amdgcn_perm(y, x, 0x07060302u)
constexpr unsigned SPIN_CAP = 1u << 15;
DI unsigned nz(unsigned v) { return min(v, SENT - 1u); }
DI unsigned nzf(float v) { return nz(__float_as_uint(v)); }
DI unsigned mx4(unsigned m, const u32x4& v) { return max(max(max(m, v[0]), v[1]), max(v[2], v[3])); }
template <int N> DI bool none_sent(const u32x4 (&v)[N]) {
  unsigned m = 0u;
#pragma unroll
  for (int i = 0; i < N; ++i) m = mx4(m, v[i]);
  return m != SENT;
}
typedef __amdgpu_buffer_rsrc_t rsrc_t;
DI u32x4 ldg(rsrc_t rs, unsigned off) { return __builtin_amdgcn_raw_buffer_load_b128(rs, (int)off, 0, 16); }
DI void stg(rsrc_t rs, unsigned off, u32x4 v) { __builtin_amdgcn_raw_buffer_store_b128(v, rs, (int)off, 0, 16); }
DI void poll_pause(bool slow) { if (slow) __builtin_amdgcn_s_sleep(4); asm volatile("s_sleep 1" ::: "memory"); }
template <int N, class LD, class OKF> DI void poll_all(u32x4 (&out)[N], bool slow, LD ld, OKF okf) {
  for (unsigned spins = 0;; ++spins) {
    ld(out);
    SBAR();
    if (__all(okf(out)) || spins > SPIN_CAP) break;
    poll_pause(slow);
  }
}
DI void lds_set(unsigned addr, unsigned v) { asm volatile("s_waitcnt lgkmcnt(0)\n\tds_write_b32 %0, %1" :: "v"(addr), "v"(v) : "memory"); }
template <int SLEEP = 1> DI void lds_wait_ge(unsigned addr, unsigned need) {
  unsigned spin = 0x100000, v;
  asm volatile("1:\n\tds_read_b32 %0, %2\n\ts_sub_u32 %1, %1, 1\n\ts_waitcnt lgkmcnt(0)\n\ts_cbranch_scc1 2f\n\tv_cmp_gt_u32 vcc, %3, %0\n\ts_cbranch_vccz 2f\n\ts_sleep %4\n\ts_branch 1b\n2:"
               : "=&v"(v), "+s"(spin) : "v"(addr), "s"(need), "n"(SLEEP) : "vcc", "scc", "memory");
}
DI s16x8 tr2(const char* p, int second) {
  s16x4 lo = __builtin_amdgcn_ds_read_tr16_b64_v4i16(LDS_PTR(s16x4, p)), hi = __builtin_amdgcn_ds_read_tr16_b64_v4i16(LDS_PTR(s16x4, p + second));
  return __builtin_shufflevector(lo, hi, 0, 1, 2, 3, 4, 5, 6, 7);
}
DI u32x16 cat4(const u32x4& a, const u32x4& b, const u32x4& c, const u32x4& d) {
  const u32x8 x = __builtin_shufflevector(a, b, 0, 1, 2, 3, 4, 5, 6, 7), y = __builtin_shufflevector(c, d, 0, 1, 2, 3, 4, 5, 6, 7);
  return __builtin_shufflevector(x, y, 0, 1, 2, 3, 4, 5, 6, 7, 8, 9, 10, 11, 12, 13, 14, 15);
}

DI unsigned stats_dst(unsigned sx, int tile, int tn, int lane) { return lane < 32 ? sx + SX_ST + tile * 8192 + tn * 512 + lane * 16 : OOB; }
DI void row_stats_sum(rsrc_t rs, unsigned sx, int tile, int lane, float& S, float& Q, float& Dd) {
  const unsigned exb = sx + SX_ST + tile * 8192 + (lane & 31) * 16;
  u32x4 pv[8];
  poll_all<8>(pv, false, [&](u32x4 (&v)[8]) {
#pragma unroll
    for (int i = 0; i < 8; ++i) v[i] = ldg(rs, exb + ((lane >> 5) * 8 + i) * 512);
  }, [&](const u32x4 (&v)[8]) { return none_sent(v); });
  S = 0.f; Q = 0.f; Dd = 0.f;
#pragma unroll
  for (int i = 0; i < 8; ++i) { S += __uint_as_float(pv[i][0]); Q += __uint_as_float(pv[i][1]); Dd += __uint_as_float(pv[i][2]); }
}
DI void row_stats_fin(char* smem, int epoch, int tile, int lane, float S, float Q, float Dd, float wsum, const u32x4 (&x1p)[4],
                      float& mean, float& var, float& mb, float& rd) {
  S = half_swap_sum(S); Q = half_swap_sum(Q); Dd = half_swap_sum(Dd);
  mean = S * (1.0f / 1024.0f); var = fmaxf(Q * (1.0f / 1024.0f) - mean * mean, 0.f);
  float den;
  ln_scalars(mean, var, mb, den);
  rd = RCP(den);
  const unsigned rb = pack2((Dd - mb * wsum) * rd, 0.f) & 0xffffu;
  const unsigned mine = (((rb & 0x8000u) ? (~rb & 0xffffu) : (rb | 0x8000u)) << 16) | (unsigned)(2047 - (tile * 32 + (lane & 31)));
  const bool best = half_max_u32(mine) == mine;
  if (best) {
    char* c = smem + L_CAND + (tile & 3) * 144;
#pragma unroll
    for (int i = 0; i < 4; ++i) *reinterpret_cast<u32x4*>(c + (2 * i + (lane >> 5)) * 16) = x1p[i];
    *reinterpret_cast<u32x4*>(c + 128) = u32x4{mine, __float_as_uint(mb), __float_as_uint(rd), 0u};
  }
  lds_set((unsigned)(uintptr_t)smem + L_W + 32 + (tile & 3) * 4, epoch);
}

DI void attn_safe(rsrc_t rs, unsigned kvoff, const u32x8& q, const u32x4& ones, f32x16& o) {
  const s16x8 q0 = __builtin_bit_cast(s16x8, __builtin_shufflevector(q, q, 0, 1, 2, 3)), q1 = __builtin_bit_cast(s16x8, __builtin_shufflevector(q, q, 4, 5, 6, 7));
  f32x4 l = {0.f, 0.f, 0.f, 0.f};
#pragma unroll
  for (int e = 0; e < 16; ++e) o[e] = 0.f;
  float m = -INFINITY;
#pragma unroll 1
  for (int kt = 0; kt < 64; ++kt) {
    const unsigned p = kvoff + kt * 4096;
    const u32x4 k0 = ldg(rs, p), k1 = ldg(rs, p + 1024), v0 = ldg(rs, p + 2048), v1 = ldg(rs, p + 3072);
    f32x16 s = {};
    s = MFMA32(__builtin_bit_cast(s16x8, k0), q0, s);
    s = MFMA32(__builtin_bit_cast(s16x8, k1), q1, s);
    float mt = fmaxf(s[0], s[1]);
#pragma unroll
    for (int e = 2; e < 16; ++e) mt = fmaxf(mt, s[e]);
    const float mn = fmaxf(m, half_swap_max(mt));
    const float alpha = EX2(m - mn);
    m = mn;
#pragma unroll
    for (int e = 0; e < 16; ++e) { s[e] = EX2(s[e] - mn); o[e] *= alpha; }
    l[0] *= alpha;
    const bf16x16 pa = __builtin_bit_cast(bf16x16, __builtin_shufflevector(s, s, 0, 1, 2, 3, 4, 5, 6, 7));
    const bf16x16 pb = __builtin_bit_cast(bf16x16, __builtin_shufflevector(s, s, 8, 9, 10, 11, 12, 13, 14, 15));
    o = __builtin_amdgcn_smfmac_f32_32x32x32_bf16(__builtin_bit_cast(bf16x8, v0), pa, o, 0xdddd, 0, 0);
    l = __builtin_amdgcn_smfmac_f32_16x16x64_bf16(__builtin_bit_cast(bf16x8, ones), pa, l, 0xdddd, 0, 0);
    o = __builtin_amdgcn_smfmac_f32_32x32x32_bf16(__builtin_bit_cast(bf16x8, v1), pb, o, 0xdddd, 0, 0);
    l = __builtin_amdgcn_smfmac_f32_16x16x64_bf16(__builtin_bit_cast(bf16x8, ones), pb, l, 0xdddd, 0, 0);
  }
  o *= 1.0f / l[0];
}

extern "C" __global__ __launch_bounds__(512) void mega_kernel(const u16* __restrict__ x0, const u16* __restrict__ wqkv, const u16* __restrict__ wo,
                                                              const u16* __restrict__ w_r, const u16* __restrict__ wg, const u16* __restrict__ wu,
                                                              const u16* __restrict__ wd, char* ws, u16* __restrict__ out) {
  __shared__ __attribute__((aligned(16))) char smem[LDS_BYTES];
  const int wid = __builtin_amdgcn_readfirstlane(threadIdx.x >> 6);
  STAMP_INIT;
  STAMPV(0);
  const int bid = blockIdx.x, xcd = bid & 7, loc = bid >> 3;
  const int tm = 8 * (xcd >> 2) + (loc >> 2), tn = 4 * (xcd & 3) + (loc & 3);
  const unsigned lbase = (unsigned)(uintptr_t)smem;
  const rsrc_t rs = __builtin_amdgcn_make_buffer_rsrc(ws, 0, (int)WS_BYTES, 0x00020000);
  volatile __attribute__((address_space(3))) unsigned* words = LDS_PTR(volatile unsigned, smem + L_W);
  if (wid >= 4) {
    auto wo_rows = [&](int g0, int n, int gs) {
      const int lane = fresh_lane();
      unsigned wov[4];
#pragma unroll
      for (int i = 0; i < 4; ++i) wov[i] = (unsigned)((8 * i + (lane >> 3)) * (D_ * 2) + ((lane & 7) ^ (4 * ((lane >> 4) & 1))) * 16 - i * 1024);
#pragma unroll 1
      for (int i = 0, k = (2 * (tm & 7)) % n, g; i < n && !0; ++i, k = k + 1 == n ? 0 : k + 1) g = g0 + gs * k, glds_v4t<3, 1>(reinterpret_cast<const char*>(wo) + (size_t)((64 * tn + 32 * g) & 1023) * (D_ * 2) + tn * 128, wov, lbase + g * 4096);
    };
    if (wid < 6) {
      const int lw = wid - 4, lane = fresh_lane();
      __builtin_amdgcn_s_setprio(3);
      const char* wb = reinterpret_cast<const char*>(wqkv + (size_t)(16 * lw) * (3 * D_) + tn * 64);
      unsigned wv[3];
#pragma unroll
      for (int i = 0; i < 3; ++i) {
        const int li = lane + 64 * i, k = li / 24, cp = li % 24, c = cp ^ (4 * ((k >> 1) & 1));
        wv[i] = (unsigned)(k * (3 * D_ * 2) + (c >> 3) * (D_ * 2) + (c & 7) * 16 - i * 1024);
      }
      auto stage = [&](int s, int slot) {
        const char* p = wb + (size_t)((s + 2 * tn) & 31) * (32 * 3 * D_ * 2);
        glds_v3(p, wv, lbase + slot * SLOTB + lw * 6144);
        glds_v3(p + 8 * (3 * D_ * 2), wv, lbase + slot * SLOTB + lw * 6144 + 3072);
      };
      const unsigned fra = lbase + L_FREE + (lane & 3) * 256, fla = lbase + L_FULL + lw * 256 + lane * 4;
      auto exchange = [&](unsigned line0, int me) {
        __builtin_amdgcn_raw_buffer_store_b32(TAG, rs, lane == 0 ? line0 + me * 128 : OOB, 0, 16);
        __builtin_amdgcn_s_sleep(8);
        for (unsigned spins = 0;; ++spins) {
          const unsigned v = __builtin_amdgcn_raw_buffer_load_b32(rs, line0 + (lane & 15) * 128, 0, 16);
          if (__all(v == TAG) || spins > SPIN_CAP) break;
          asm volatile("s_sleep 1" ::: "memory");
        }
      };
      const rsrc_t rw = __builtin_amdgcn_make_buffer_rsrc(const_cast<char*>(wb), 0, 0 ? 0 : 6 * MBY - (16 * lw * 3 * D_ + tn * 64) * 2, 0x00020000);
#pragma unroll 1
      for (int L = 0; L < 2; ++L) {
        const int s0 = L ? NS : 0, lead = L ? 8 : NS - 1;
        int slot = 0;
#pragma unroll 1
        for (int s = s0; s < 32; ++s) {
          if (s >= NS) lds_wait_ge(fra, 32 * L + s - lead);
          stage(s, slot);
          if (s == 0) BARRIER();
          slot = (slot == NS - 1) ? 0 : slot + 1;
          if (s >= s0 + DD) asm volatile("s_waitcnt vmcnt(%0)\n\tds_write_b32 %1, %2" :: "n"(DD * 6), "v"(fla), "v"(32 * L + s - DD + 1) : "memory");
        }
#pragma unroll
        for (int d = DD - 1; d >= 0; --d) asm volatile("s_waitcnt vmcnt(%0)\n\tds_write_b32 %1, %2" :: "n"(d * 6), "v"(fla), "v"(32 * L + 32 - d) : "memory");
        BAR_LGKM();
        if (lw == 0) {
          exchange(FL_KV + (L * 16 + tn) * 2048, tm);
          lds_wait_ge(fra, 32 * L + 32);
          lds_set(lbase + L_W + 16, L + 1);
        } else lds_wait_ge<8>(lbase + L_W + 16, L + 1);
        __builtin_amdgcn_s_sleep(24);
        wo_rows(lw, 15, 2);
        u32x4 pre[NS * 6];
        if (L == 0) {
#pragma unroll
          for (int s = 0; s < NS; ++s) {
#pragma unroll
            for (int i = 0; i < 6; ++i)
              pre[s * 6 + i] = __builtin_amdgcn_raw_buffer_load_b128(rw, wv[i % 3], ((s + 2 * tn) & 31) * (32 * 3 * D_ * 2) + (i / 3) * (8 * 3 * D_ * 2) + (i % 3) * 1024, 0), PACE();
          }
        } else {
#pragma unroll
          for (int i = 0; i < NS * 6; ++i) asm volatile("" : "=v"(pre[i]));
        }
        BAR_ALL();
        if (lw == 0) { exchange(FL_O + (L * 16 + tm) * 2048, tn); lds_set(lbase + L_W + 20, L + 1); }
        else wo_rows(30, 2, 1);
        BAR_ALL();
        if (L == 0) {
          char* d = smem + lw * 6144 + fresh_lane() * 16;
#pragma unroll
          for (int s = 0; s < NS; ++s) {
#pragma unroll
            for (int i = 0; i < 6; ++i) *reinterpret_cast<u32x4*>(d + s * SLOTB + i * 1024) = pre[s * 6 + i];
            lds_set(fla, 32 + s + 1);
          }
          if (lw == 0) { lds_wait_ge(fra, 33); exchange(FL_X + tm * 2048, tn); lds_set(lbase + L_W + 24, 1); }
          else lds_wait_ge<2>(lbase + L_W + 24, 1);
        }
      }
      return;
    }
    const int a = wid - 6;
    const bool ffn = loc < 28;
    const int grp = xcd * 7 + (ffn ? loc >> 2 : 0), qa = (loc & 3) * 2 + a;
    const int kr = loc, cg = xcd;
    const unsigned wa = lbase + L_W;
    BARRIER();
    auto transpose8 = [](u32x4* v) {
      u32x4 t[8];
#pragma unroll
      for (int j = 0; j < 8; ++j)
#pragma unroll
        for (int e = 0; e < 4; ++e) t[j][e] = (j & 1) ? PERM_HI(v[2 * e][j >> 1], v[2 * e + 1][j >> 1]) : PERM_LO(v[2 * e][j >> 1], v[2 * e + 1][j >> 1]);
#pragma unroll
      for (int j = 0; j < 8; ++j) { v[j] = t[j]; PIN(v[j]); }
    };
    u32x4 gf[32];
    u32x4 wdf[16];
    auto wait_go = [&](int L) {
      lds_wait_ge<8>(lbase + L_W + 16, L + 1);
      __builtin_amdgcn_s_sleep(24);
    };
    auto load_wd = [&](unsigned dv) {
      const rsrc_t rd = __builtin_amdgcn_make_buffer_rsrc(const_cast<u16*>(wd) + (size_t)grp * 64 * D_ + 128 * qa, 0, 0 ? 0 : 64 * D_ * 2 - 256 * qa, 0x00020000);
#pragma unroll
      for (int i = 0; i < 16; ++i) {
        wdf[i] = __builtin_amdgcn_raw_buffer_load_b128(rd, dv, (32 * (i >> 3) + (i & 7)) * (D_ * 2), 0);
        asm volatile("s_sleep 2\n\ts_waitcnt vmcnt(3)" ::: "memory");
      }
    };
    {
      LANE_VARS;
      const u32x4 sv = {SENT, SENT, SENT, SENT};
#pragma unroll
      for (int i = 0; i < 5; ++i) {
        const unsigned o = ((i * 256 + bid) * 128 + a * 64 + lane) * 16;
        stg(rs, o < SX_BYTES ? OFF_SX + o : OOB, sv);
      }
      if (a == 0) {
        const u32x4 w0 = *reinterpret_cast<const u32x4*>(w_r + lane * 8), w1 = *reinterpret_cast<const u32x4*>(w_r + 512 + lane * 8);
        float t = 0.f;
#pragma unroll
        for (int e = 0; e < 4; ++e) t = fadd(fadd(t, fadd(lo16(w0[e]), hi16(w0[e]))), fadd(lo16(w1[e]), hi16(w1[e])));
        words[0] = __float_as_uint(wave_sum(t));
        if ((lane >> 3) == (tn & 7)) *reinterpret_cast<u32x4*>(smem + L_WR + (lane & 7) * 16) = tn < 8 ? w0 : w1;
      }
      const u16* p = wqkv + (size_t)(64 * tm + 32 * a + hi) * (3 * D_) + (c24 >> 3) * D_ + tn * 64 + (c24 & 7) * 8;
      float cs[8] = {0.f, 0.f, 0.f, 0.f, 0.f, 0.f, 0.f, 0.f};
      u32x4 v[16];
#pragma unroll
      for (int i = 0; i < 16; ++i) v[i] = *reinterpret_cast<const u32x4*>(p + (size_t)i * (2 * 3 * D_));
      SBAR();
#pragma unroll
      for (int i = 0; i < 16; ++i)
#pragma unroll
        for (int e = 0; e < 4; ++e) { cs[2 * e] = fadd(cs[2 * e], lo16(v[i][e])); cs[2 * e + 1] = fadd(cs[2 * e + 1], hi16(v[i][e])); }
      u32x4 o;
#pragma unroll
      for (int e = 0; e < 4; ++e) { const float x = half_swap_sum(cs[e]), y = half_swap_sum(cs[4 + e]); o[e] = __float_as_uint(hi ? y : x); }
      stg(rs, r32 < 24 ? OFF_CS + (tn * 32 + tm * 2 + a) * 768 + r32 * 32 + hi * 16 : OOB, o);
    }
    {
      BAR_ALL();
      LANE_VARS;
      const char* gp = reinterpret_cast<const char*>(((lane & 8) ? wu : wg) + (size_t)(128 * qa + 16 * hi + 4 * tblk) * F_ + 64 * grp + (lane & 7) * 8);
      unsigned dv = (lane >> 4) * (8 * D_ * 2) + (lane & 15) * 16;
      PIN(gp); PIN(dv);
      unsigned sv[3];
#pragma unroll
      for (int i = 0; i < 3; ++i) { const int li = lane + 64 * i, k = li / 24, c = li % 24; sv[i] = (unsigned)(k * (3 * D_ * 2) + (c >> 3) * (D_ * 2) + (c & 7) * 16 - i * 1024); PIN(sv[i]); }
      wait_go(0);
#pragma unroll
      for (int i = 0; i < 32; ++i) {
        gf[i] = *reinterpret_cast<const u32x4*>(gp + (size_t)(0 ? 0 : 32 * (i >> 3) + 8 * ((i >> 2) & 1) + (i & 3)) * (F_ * 2));
        asm volatile("s_sleep 2\n\ts_waitcnt vmcnt(3)" ::: "memory");
      }
      load_wd(dv);
      STAMPV(7);
      BARRIER();
      __builtin_amdgcn_s_sleep(2);
#pragma unroll
      for (int g = 0; g < 4; ++g) glds_v3(wqkv + (size_t)(32 * kr + 8 * g) * (3 * D_) + (2 * cg + a) * 64, sv, lbase + L_SL + a * 12288 + g * 3072);
      transpose8(gf); transpose8(gf + 8); transpose8(gf + 16); transpose8(gf + 24);
      transpose8(wdf); transpose8(wdf + 8);
      {
        LANE_VARS;
        f32x4 t[2];
#pragma unroll
        for (int c = 0; c < 2; ++c) {
          u32x4 v[8];
#pragma unroll
          for (int i = 0; i < 8; ++i) v[i] = ldg(rs, OFF_CS + (tn * 32 + 16 * c + 2 * i + hi) * 768 + a * 384 + c24 * 16);
          SBAR();
          t[c] = sum4<8>(v);
          SBAR();
        }
#pragma unroll
        for (int e = 0; e < 4; ++e) t[0][e] = half_swap_sum(fadd(t[0][e], t[1][e]));
        if (lane < 24) *reinterpret_cast<f32x4*>(smem + L_CS + a * 384 + lane * 16) = t[0];
      }
      BAR_LGKM();
    }
#pragma unroll 1
    for (int L = fresh_s(0);; ++L) {
      const unsigned sx = OFF_SX + L * SX_LAYER;
      STAMPV(1 + 16 * L);
      LANE_VARS;
      if (0 && L == 0) { words[3] = 5000u; lds_set(wa + 4 + 4 * a, 1); } else {
      unsigned cdst = lane < 8 ? sx + SX_ROW + tm * 2048 + tn * 128 + lane * 16 : sx + SX_CAND + (tn * 16 + tm) * 128;
      cdst = (a == 0 && lane < 9) ? cdst : OOB;
      PIN(cdst);
      lds_wait_ge(wa + 32 + 4 * tp, L + 1);
      unsigned key;
      {
        const unsigned k4 = *reinterpret_cast<const unsigned*>(smem + L_CAND + tp * 144 + 128);
        key = max(k4, dpp<0xB1>(k4)); key = max(key, dpp<0x4E>(key));
        const u32x4 c = *reinterpret_cast<const u32x4*>(smem + L_CAND + (((2047 - (int)(key & 2047u)) >> 5) & 3) * 144 + (lane < 8 ? lane : 8) * 16);
        const u32x4 o = {nz(c[0]), nz(c[1]), nz(c[2]), nz(c[3])};
        stg(rs, cdst, o);
      }
      STAMPV(8 + 16 * L);
      __builtin_amdgcn_s_sleep(10);
      if (L == 0) __builtin_amdgcn_s_sleep(0);
      u32x4 v[2];
      const unsigned rpc = (tblk ? 4 * kr + tp : 16 * qa + (lane & 15)) * 16;
      poll_all<2>(v, L == 0, [&](u32x4 (&t)[2]) {
        t[0] = ldg(rs, sx + SX_CAND + (tn * 16 + (lane & 15)) * 128);
        t[1] = ldg(rs, sx + SX_ROW + ((2047 - (int)(key & 2047u)) >> 7) * 2048 + rpc);
        t[0][3] = key;
      }, [&](const u32x4 (&t)[2]) {
        const bool ok = mx4(0u, t[0]) != SENT;
        unsigned k = ok ? t[0][0] : 0u;
        k = max(k, dpp<0xB1>(k)); k = max(k, dpp<0x4E>(k)); k = max(k, dpp<0x141>(k)); k = max(k, dpp<0x140>(k));
        key = max(key, k);
        return ok & (mx4(0u, t[1]) != SENT) & (key == t[0][3]);
      });
      key = __builtin_amdgcn_readfirstlane(key);
      const int idx = 2047 - (int)(key & 2047u);
      const bool owner = (idx >> 7) == tm;
      STAMPV(2 + 16 * L);
      {
        const float mb = __uint_as_float(__builtin_amdgcn_readlane(v[0][1], idx >> 7)), rd = __uint_as_float(__builtin_amdgcn_readlane(v[0][2], idx >> 7));
        u32x4 hb;
#pragma unroll
        for (int e = 0; e < 4; ++e) hb[e] = ln_pair_s(v[1][e], -mb, rd);
        *reinterpret_cast<u32x4*>(smem + L_HB + a * 1024 + lane * 16) = hb;
        *reinterpret_cast<u32x4*>(smem + L_XB + a * 1024 + lane * 16) = v[1];
        words[3] = (unsigned)idx;
        if (L == 1) lds_set(wa + 4 + 4 * a, 2);
      }
      auto pick2 = [&](const f32x4 (&d)[8]) {
        u32x2 o;
#pragma unroll
        for (int e = 0; e < 2; ++e) {
          const float x = tblk ? d[2 + e][0] : d[e][0], y = tblk ? d[6 + e][0] : d[4 + e][0];
          o[e] = nzf(hi ? y : x);
        }
        return o;
      };
      if (ffn) {
        const f32x4 zero = {0.f, 0.f, 0.f, 0.f};
        const unsigned ppos = (lane & 15) * 32 + (lane >> 4) * 8;
        f32x4 d[8];
        {
          s16x8 hf[4];
#pragma unroll
          for (int t = 0; t < 4; ++t) hf[t] = *reinterpret_cast<const s16x8*>(smem + L_HB + a * 1024 + (lane >> 4) * 16 + t * 64);
          SBAR();
          __builtin_amdgcn_s_setprio(3);
#pragma unroll
          for (int t = 0; t < 32; ++t) d[t & 7] = MFMA16(hf[t >> 3], __builtin_bit_cast(s16x8, gf[t]), t < 8 ? zero : d[t & 7]);
          SBAR();
          __builtin_amdgcn_s_setprio(0);
          __builtin_amdgcn_raw_buffer_store_b64(pick2(d), rs, sx + SX_GP + (grp * 8 + qa) * 512 + ppos, 0, 16);
        }
        STAMPV(9 + 16 * L);
        __builtin_amdgcn_s_sleep(10);
        if (L == 0) __builtin_amdgcn_s_sleep(0);
        u32x2 gu[8];
        for (unsigned spins = 0;; ++spins) {
#pragma unroll
          for (int i = 0; i < 8; ++i) gu[i] = __builtin_amdgcn_raw_buffer_load_b64(rs, sx + SX_GP + (grp * 8 + i) * 512 + lane * 8, 0, 16);
          SBAR();
          unsigned m = 0u;
#pragma unroll
          for (int i = 0; i < 8; ++i) m = max(max(m, gu[i][0]), gu[i][1]);
          if (__all(m != SENT) || spins > SPIN_CAP) break;
          poll_pause(L == 0);
        }
        STAMPV(10 + 16 * L);
        float t0 = opq(U2F(gu[0][0])), t1 = opq(U2F(gu[0][1]));
#pragma unroll
        for (int i = 1; i < 8; ++i) { t0 = fadd(t0, U2F(gu[i][0])); t1 = fadd(t1, U2F(gu[i][1])); }
        auto sw = __builtin_amdgcn_permlane32_swap(__float_as_uint(t0), __float_as_uint(t1), false, false);
        const float gv = rbf(U2F(sw[0])), uv = rbf(U2F(sw[1]));
        reinterpret_cast<u16*>(smem + L_GU + a * 128)[2 * r32 + hi] = (u16)pack2(fmul(rbf(fmul(gv, RCP(fadd(1.0f, __expf(-gv))))), uv), 0.f);
        asm volatile("s_waitcnt lgkmcnt(0)" ::: "memory");
        const s16x8 a0 = *reinterpret_cast<const s16x8*>(smem + L_GU + a * 128 + (lane >> 4) * 16), a1 = *reinterpret_cast<const s16x8*>(smem + L_GU + a * 128 + 64 + (lane >> 4) * 16);
        SBAR();
        __builtin_amdgcn_s_setprio(3);
#pragma unroll
        for (int t = 0; t < 16; ++t) d[t & 7] = MFMA16(t < 8 ? a0 : a1, __builtin_bit_cast(s16x8, wdf[t]), t < 8 ? zero : d[t & 7]);
        SBAR();
        __builtin_amdgcn_s_setprio(0);
        __builtin_amdgcn_raw_buffer_store_b64(pick2(d), rs, sx + SX_YP + grp * 4096 + qa * 512 + ppos, 0, 16);
      }
      STAMPV(3 + 16 * L);
      if (L == 1 && (cg != (kr & 7) || a)) { STAMP_END; __builtin_amdgcn_endpgm(); }
      s16x8 wpf[12];
#pragma unroll
      for (int t = 0; t < 12; ++t) wpf[t] = tr2(smem + L_SL + a * 12288 + (16 * hi + 4 * tblk + tq) * 384 + t * 32 + tp * 8, 8 * 384);
#pragma unroll
      for (int t = 0; t < 12; ++t) PIN(wpf[t]);
      if (L == 0 && !owner) lds_set(wa + 4 + 4 * a, 1);
      __builtin_amdgcn_s_sleep(10 - 4);
      if (L == 0) __builtin_amdgcn_s_sleep(0);
      if (L == 0 && !ffn) __builtin_amdgcn_s_sleep(64);
      const unsigned rk = key >> 16;
      float rsig = rbf(RCP(fadd(1.0f, __expf(-lo16((rk & 0x8000u) ? (rk & 0x7fffu) : (~rk & 0xffffu))))));
      PIN(rsig);
      const int n = 4 * (lane & 7) + ((lane >> 3) & 3);
      const int fo = ((n >> 4) * 2 + ((n >> 2) & 1)) * 8 + ((n >> 3) & 1) * 4 + (n & 3);
      float xv = bf2f(reinterpret_cast<const u16*>(smem + L_XB + a * 1024 + 256)[fo]), hv = bf2f(reinterpret_cast<const u16*>(smem + L_HB + a * 1024 + 256)[fo]);
      PIN(xv); PIN(hv);
      u32x4 pv[7];
      poll_all<7>(pv, L == 0, [&](u32x4 (&t)[7]) {
#pragma unroll
        for (int i = 0; i < 7; ++i) t[i] = ldg(rs, sx + SX_YP + (8 * i + (lane >> 3)) * 4096 + kr * 128 + (lane & 7) * 16);
      }, [&](const u32x4 (&t)[7]) { return none_sent(t); });
      STAMPV(4 + 16 * L);
      float ys;
      {
        f32x4 t = sum4<7>(pv);
#pragma unroll
        for (int e = 0; e < 4; ++e) {
          t[e] = fadd(t[e], U2F(dpp<0x128>(__float_as_uint(t[e]))));
          t[e] = half_swap_sum(row_swap_sum(t[e]));
        }
        const float x = (lane & 8) ? t[1] : t[0], y = (lane & 8) ? t[3] : t[2];
        ys = tblk ? y : x;
      }
      const unsigned ob = pack2(fadd(xv, rbf(fadd(hv, rbf(fmul(rbf(ys), rsig))))), 0.f) & 0xffffu;
      if (L == 1) {
        if (lane < 32) out[(size_t)idx * D_ + kr * 32 + n] = (u16)ob;
        STAMPV(5 + 16 * L);
        STAMP_END;
        __builtin_amdgcn_endpgm();
      }
      reinterpret_cast<u16*>(smem + L_X2F + a * 64)[fo] = (u16)ob;
      asm volatile("s_waitcnt lgkmcnt(0)" ::: "memory");
      {
        const s16x8 xa = *reinterpret_cast<const s16x8*>(smem + L_X2F + a * 64 + (lane >> 4) * 16);
        const u32x4 xf = *reinterpret_cast<const u32x4*>(smem + L_X2F + a * 64 + tp * 16);
        f32x4 z[12];
        const f32x4 zero = {0.f, 0.f, 0.f, 0.f};
        SBAR();
        __builtin_amdgcn_s_setprio(3);
#pragma unroll
        for (int t = 0; t < 12; ++t) z[t] = MFMA16(xa, wpf[t], zero);
        SBAR();
        __builtin_amdgcn_s_setprio(0);
        const float x2 = lo16(ob), s1 = half_sum(x2), s2 = half_sum(fmul(x2, x2));
        u32x4 o;
#pragma unroll
        for (int e = 0; e < 4; ++e) {
          const unsigned sp = lane < 52 ? (e == 0 ? __float_as_uint(s1) : __float_as_uint(s2)) : xf[e];
          const unsigned x = __float_as_uint(tblk ? z[4 + e][0] : z[e][0]), y = tblk ? sp : __float_as_uint(z[8 + e][0]);
          o[e] = nz(hi ? y : x);
        }
        const unsigned pos = lane < 24 ? lane * 16 : lane < 48 ? lane * 16 + 128 : lane < 52 ? (lane & 1) * 512 + 384 : 400 + tp * 16;
        stg(rs, lane < 56 ? OFF_SX + SX_QP + ((2 * cg + a) * 32 + kr) * 1024 + pos : OOB, o);
      }
      STAMPV(5 + 16 * L);
      if (owner) {
        __builtin_amdgcn_s_sleep(10);
        const int c25 = r32 < 25 ? r32 : 24, pl = 24 * a + c24, col = 64 * (pl >> 4) + (pl & 15);
        float cs[4];
#pragma unroll
        for (int e = 0; e < 4; ++e) { cs[e] = reinterpret_cast<const float*>(smem + L_CS)[col + 16 * e]; PIN(cs[e]); }
        u32x4 qv[17];
        poll_all<17>(qv, true, [&](u32x4 (&t)[17]) {
#pragma unroll
          for (int i = 0; i < 16; ++i) t[i] = ldg(rs, OFF_SX + SX_QP + (tn * 32 + 2 * i + hi) * 1024 + a * 512 + c25 * 16);
          t[16] = ldg(rs, OFF_SX + SX_QP + (tn * 34 + hi) * 1024 + 400 + tp * 16);
        }, [&](const u32x4 (&t)[17]) { return none_sent(t); });
        *reinterpret_cast<u32x4*>(smem + L_X2P + hi * 64 + tp * 16) = qv[16];
        f32x4 t = sum4<16>(qv);
#pragma unroll
        for (int e = 0; e < 4; ++e) t[e] = half_swap_sum(t[e]);
        const float mean = fmul(U2F(__builtin_amdgcn_readlane(__float_as_uint(t[0]), 24)), 1.0f / 1024.0f);
        const float var = fmaxf(ffma(-mean, mean, fmul(U2F(__builtin_amdgcn_readlane(__float_as_uint(t[1]), 24)), 1.0f / 1024.0f)), 0.f);
        float mb, den;
        ln_scalars(mean, var, mb, den);
        const float rd = RCP(den);
        if (lane < 24) {
#pragma unroll
          for (int e = 0; e < 4; ++e) reinterpret_cast<float*>(smem + L_PATCH)[col + 16 * e] = fmul(ffma(-mb, cs[e], t[e]), rd);
        }
        lds_set(wa + 4 + 4 * a, L + 1);
      }
      STAMPV(6 + 16 * L);
      }
      BAR_ALL();
      unsigned dv = (lane >> 4) * (8 * D_ * 2) + (lane & 15) * 16;
      PIN(dv);
      wait_go(1);
      load_wd(dv);
      STAMPV(23);
      BARRIER();
      transpose8(wdf); transpose8(wdf + 8);
      BARRIER();
    }
  }
  const int tm_ = tm, tn_ = tn, wid_ = wid;
  *reinterpret_cast<u32x2*>(smem + L_FULL + threadIdx.x * 8) = u32x2{0u, 0u};
  if (threadIdx.x < 16) words[threadIdx.x] = 0u;
  const u32x4 srd = {(unsigned)(uintptr_t)ws, (unsigned)((uintptr_t)ws >> 32) & 0xffffu, WS_BYTES, 0x00020000u};
  u32x4 x1p[4];
  asm volatile("" : "=v"(x1p[0]), "=v"(x1p[1]), "=v"(x1p[2]), "=v"(x1p[3]));
#pragma unroll 1
  for (int L = 0; L < 2; ++L) {
    const unsigned sx = OFF_SX + L * SX_LAYER;
    u32x8 q[4];
    u32x16 kva, kvb;
    f32x16 oz[5];
    {
      f32x16 acc[6];
      u32x16 stA, stB, xr0, xr1, xr2, xr3;
      u32x4 stC;
      asm volatile("" : "={v[112:127]}"(stB), "={v[128:131]}"(stC));
      { PHASE_VARS;
      stB[8] = lane * 16 + (L ? OFF_X1 : OFF_HF) + tile * TS;
      stB[9] = lbase + wrow * 384 + (((4 * hh + wch) ^ wsw) << 4) + 8 * (tp & 1);
      stB[10] = lbase + wrow * 384 + (((4 * (hh ^ 1) + wch) ^ wsw) << 4) + 8 * (tp & 1);
      stB[13] = lbase + L_FULL + (lane & 1) * 256;
      stB[15] = lbase + L_FREE + wid * 256 + lane * 4;
      stC[0] = 32 * L; }
      unsigned soff, slot, need = 32 * L + 1;
      if (L == 0) {
        soff = tn * 4096;
        asm volatile(
          "buffer_load_dwordx4 v[132:135], v120, s[40:43], s44 offen offset:0 sc1\n\t"
          "buffer_load_dwordx4 v[136:139], v120, s[40:43], s44 offen offset:1024 sc1\n\t"
          "buffer_load_dwordx4 v[140:143], v120, s[40:43], s44 offen offset:2048 sc1\n\t"
          "buffer_load_dwordx4 v[144:147], v120, s[40:43], s44 offen offset:3072 sc1\n\t"
          "s_add_u32 s44, s44, 0x1000\n\t"
          "s_and_b32 s44, s44, 0xffff\n\t"
          "buffer_load_dwordx4 v[148:151], v120, s[40:43], s44 offen offset:0 sc1\n\t"
          "buffer_load_dwordx4 v[152:155], v120, s[40:43], s44 offen offset:1024 sc1\n\t"
          "buffer_load_dwordx4 v[156:159], v120, s[40:43], s44 offen offset:2048 sc1\n\t"
          "buffer_load_dwordx4 v[160:163], v120, s[40:43], s44 offen offset:3072 sc1\n\t"
          "s_add_u32 s44, s44, 0x1000\n\t"
          "s_and_b32 s44, s44, 0xffff\n\t"
          "buffer_load_dwordx4 v[164:167], v120, s[40:43], s44 offen offset:0 sc1\n\t"
          "buffer_load_dwordx4 v[168:171], v120, s[40:43], s44 offen offset:1024 sc1\n\t"
          "buffer_load_dwordx4 v[172:175], v120, s[40:43], s44 offen offset:2048 sc1\n\t"
          "buffer_load_dwordx4 v[176:179], v120, s[40:43], s44 offen offset:3072 sc1\n\t"
          "s_add_u32 s44, s44, 0x1000\n\t"
          "s_and_b32 s44, s44, 0xffff\n\t"
          "buffer_load_dwordx4 v[180:183], v120, s[40:43], s44 offen offset:0 sc1\n\t"
          "buffer_load_dwordx4 v[184:187], v120, s[40:43], s44 offen offset:1024 sc1\n\t"
          "buffer_load_dwordx4 v[188:191], v120, s[40:43], s44 offen offset:2048 sc1\n\t"
          "buffer_load_dwordx4 v[192:195], v120, s[40:43], s44 offen offset:3072 sc1\n\t"
          : "={v[132:147]}"(xr0), "={v[148:163]}"(xr1), "={v[164:179]}"(xr2), "={v[180:195]}"(xr3), "+{s44}"(soff) : "{v[112:127]}"(stB), "{s[40:43]}"(srd) : "scc", "memory");
        BAR_LGKM();
      } else {
        xr0 = cat4(x1p[0], x1p[1], x1p[2], x1p[3]);
        asm volatile("" : "={v[148:163]}"(xr1), "={v[164:179]}"(xr2), "={v[180:195]}"(xr3));
      }
      STAMPV(1 + 16 * L);
      asm volatile(
        "v_mov_b32 v123, v121\n\t"
        "v_mov_b32 v124, v122\n\t"
        "s_mov_b64 vcc, -1\n\t"
        "s_cbranch_vccnz 10f\n\t"
        "11:\n\t"
        "s_mov_b32 s46, 12288\n\t"
        "s_add_u32 s47, s47, 1\n\t"
        "ds_read_b64_tr_b16 v[96:97], v123 offset:0\n\t"
        "ds_read_b64_tr_b16 v[98:99], v123 offset:3072\n\t"
        "ds_read_b64_tr_b16 v[100:101], v124 offset:0\n\t"
        "ds_read_b64_tr_b16 v[102:103], v124 offset:3072\n\t"
        "ds_read_b64_tr_b16 v[104:105], v123 offset:128\n\t"
        "ds_read_b64_tr_b16 v[106:107], v123 offset:3200\n\t"
        "ds_read_b64_tr_b16 v[108:109], v124 offset:128\n\t"
        "ds_read_b64_tr_b16 v[110:111], v124 offset:3200\n\t"
        "ds_read_b64_tr_b16 v[112:113], v123 offset:256\n\t"
        "ds_read_b64_tr_b16 v[114:115], v123 offset:3328\n\t"
        "ds_write_b32 v127, v128\n\t"
        "ds_read_b64_tr_b16 v[116:117], v124 offset:256\n\t"
        "ds_read_b64_tr_b16 v[118:119], v124 offset:3328\n\t"
        "s_waitcnt vmcnt(15) lgkmcnt(11)\n\t"
        "v_mfma_f32_32x32x16_bf16 v[0:15], v[96:99], v[132:135], 0\n\t"
        "ds_read_b32 v126, v125\n\t"
        "ds_read_b64_tr_b16 v[96:97], v123 offset:6144\n\t"
        "ds_read_b64_tr_b16 v[98:99], v123 offset:9216\n\t"
        "s_waitcnt lgkmcnt(12)\n\t"
        "v_mfma_f32_32x32x16_bf16 v[16:31], v[100:103], v[132:135], 0\n\t"
        "v_add_u32 v129, s46, v121\n\t"
        "ds_read_b64_tr_b16 v[100:101], v124 offset:6144\n\t"
        "ds_read_b64_tr_b16 v[102:103], v124 offset:9216\n\t"
        "s_waitcnt lgkmcnt(12)\n\t"
        "v_mfma_f32_32x32x16_bf16 v[32:47], v[104:107], v[132:135], 0\n\t"
        "ds_read_b64_tr_b16 v[104:105], v123 offset:6272\n\t"
        "ds_read_b64_tr_b16 v[106:107], v123 offset:9344\n\t"
        "s_waitcnt lgkmcnt(12)\n\t"
        "v_mfma_f32_32x32x16_bf16 v[48:63], v[108:111], v[132:135], 0\n\t"
        "v_add_u32 v130, s46, v122\n\t"
        "ds_read_b64_tr_b16 v[108:109], v124 offset:6272\n\t"
        "ds_read_b64_tr_b16 v[110:111], v124 offset:9344\n\t"
        "s_waitcnt lgkmcnt(12)\n\t"
        "v_mfma_f32_32x32x16_bf16 v[64:79], v[132:135], v[112:115], 0\n\t"
        "ds_read_b64_tr_b16 v[112:113], v123 offset:6400\n\t"
        "ds_read_b64_tr_b16 v[114:115], v123 offset:9472\n\t"
        "s_waitcnt lgkmcnt(10)\n\t"
        "v_mfma_f32_32x32x16_bf16 v[80:95], v[132:135], v[116:119], 0\n\t"
        "v_cmp_gt_u32 vcc, s47, v126\n\t"
        "ds_read_b64_tr_b16 v[116:117], v124 offset:6400\n\t"
        "ds_read_b64_tr_b16 v[118:119], v124 offset:9472\n\t"
        "s_waitcnt vmcnt(14) lgkmcnt(10)\n\t"
        "v_mfma_f32_32x32x16_bf16 v[0:15], v[96:99], v[136:139], v[0:15]\n\t"
        "s_cbranch_vccnz 12f\n\t"
        "13:\n\t"
        "ds_read_b64_tr_b16 v[96:97], v129 offset:0\n\t"
        "ds_read_b64_tr_b16 v[98:99], v129 offset:3072\n\t"
        "s_waitcnt lgkmcnt(10)\n\t"
        "v_mfma_f32_32x32x16_bf16 v[16:31], v[100:103], v[136:139], v[16:31]\n\t"
        "v_add_u32 v128, 1, v128\n\t"
        "ds_read_b64_tr_b16 v[100:101], v130 offset:0\n\t"
        "ds_read_b64_tr_b16 v[102:103], v130 offset:3072\n\t"
        "s_waitcnt lgkmcnt(10)\n\t"
        "v_mfma_f32_32x32x16_bf16 v[32:47], v[104:107], v[136:139], v[32:47]\n\t"
        "ds_read_b64_tr_b16 v[104:105], v129 offset:128\n\t"
        "ds_read_b64_tr_b16 v[106:107], v129 offset:3200\n\t"
        "s_waitcnt lgkmcnt(10)\n\t"
        "v_mfma_f32_32x32x16_bf16 v[48:63], v[108:111], v[136:139], v[48:63]\n\t"
        "s_add_u32 s46, s46, 12288\n\t"
        "s_add_u32 s47, s47, 1\n\t"
        "ds_read_b64_tr_b16 v[108:109], v130 offset:128\n\t"
        "ds_read_b64_tr_b16 v[110:111], v130 offset:3200\n\t"
        "s_waitcnt lgkmcnt(10)\n\t"
        "v_mfma_f32_32x32x16_bf16 v[64:79], v[136:139], v[112:115], v[64:79]\n\t"
        "s_cmp_eq_u32 s46, 110592\n\t"
        "s_cselect_b32 s46, 0, s46\n\t"
        "ds_read_b64_tr_b16 v[112:113], v129 offset:256\n\t"
        "ds_read_b64_tr_b16 v[114:115], v129 offset:3328\n\t"
        "s_waitcnt lgkmcnt(10)\n\t"
        "v_mfma_f32_32x32x16_bf16 v[80:95], v[136:139], v[116:119], v[80:95]\n\t"
        "s_cmp_lt_u32 s47, 32\n\t"
        "s_cbranch_scc1 4f\n\t"
        "s_waitcnt vmcnt(0)\n\t"
        "4:\n\t"
        "ds_write_b32 v127, v128\n\t"
        "ds_read_b64_tr_b16 v[116:117], v130 offset:256\n\t"
        "ds_read_b64_tr_b16 v[118:119], v130 offset:3328\n\t"
        "s_waitcnt vmcnt(13) lgkmcnt(11)\n\t"
        "v_mfma_f32_32x32x16_bf16 v[0:15], v[96:99], v[140:143], v[0:15]\n\t"
        "ds_read_b32 v126, v125\n\t"
        "ds_read_b64_tr_b16 v[96:97], v129 offset:6144\n\t"
        "ds_read_b64_tr_b16 v[98:99], v129 offset:9216\n\t"
        "s_waitcnt lgkmcnt(12)\n\t"
        "v_mfma_f32_32x32x16_bf16 v[16:31], v[100:103], v[140:143], v[16:31]\n\t"
        "v_add_u32 v123, s46, v121\n\t"
        "ds_read_b64_tr_b16 v[100:101], v130 offset:6144\n\t"
        "ds_read_b64_tr_b16 v[102:103], v130 offset:9216\n\t"
        "s_waitcnt lgkmcnt(12)\n\t"
        "v_mfma_f32_32x32x16_bf16 v[32:47], v[104:107], v[140:143], v[32:47]\n\t"
        "ds_read_b64_tr_b16 v[104:105], v129 offset:6272\n\t"
        "ds_read_b64_tr_b16 v[106:107], v129 offset:9344\n\t"
        "s_waitcnt lgkmcnt(12)\n\t"
        "v_mfma_f32_32x32x16_bf16 v[48:63], v[108:111], v[140:143], v[48:63]\n\t"
        "v_add_u32 v124, s46, v122\n\t"
        "ds_read_b64_tr_b16 v[108:109], v130 offset:6272\n\t"
        "ds_read_b64_tr_b16 v[110:111], v130 offset:9344\n\t"
        "s_waitcnt lgkmcnt(12)\n\t"
        "v_mfma_f32_32x32x16_bf16 v[64:79], v[140:143], v[112:115], v[64:79]\n\t"
        "ds_read_b64_tr_b16 v[112:113], v129 offset:6400\n\t"
        "ds_read_b64_tr_b16 v[114:115], v129 offset:9472\n\t"
        "s_waitcnt lgkmcnt(10)\n\t"
        "v_mfma_f32_32x32x16_bf16 v[80:95], v[140:143], v[116:119], v[80:95]\n\t"
        "v_cmp_gt_u32 vcc, s47, v126\n\t"
        "ds_read_b64_tr_b16 v[116:117], v130 offset:6400\n\t"
        "ds_read_b64_tr_b16 v[118:119], v130 offset:9472\n\t"
        "s_waitcnt vmcnt(12) lgkmcnt(10)\n\t"
        "v_mfma_f32_32x32x16_bf16 v[0:15], v[96:99], v[144:147], v[0:15]\n\t"
        "s_cbranch_vccnz 14f\n\t"
        "15:\n\t"
        "ds_read_b64_tr_b16 v[96:97], v123 offset:0\n\t"
        "ds_read_b64_tr_b16 v[98:99], v123 offset:3072\n\t"
        "s_waitcnt lgkmcnt(10)\n\t"
        "v_mfma_f32_32x32x16_bf16 v[16:31], v[100:103], v[144:147], v[16:31]\n\t"
        "v_add_u32 v128, 1, v128\n\t"
        "ds_read_b64_tr_b16 v[100:101], v124 offset:0\n\t"
        "ds_read_b64_tr_b16 v[102:103], v124 offset:3072\n\t"
        "s_waitcnt lgkmcnt(10)\n\t"
        "v_mfma_f32_32x32x16_bf16 v[32:47], v[104:107], v[144:147], v[32:47]\n\t"
        "ds_read_b64_tr_b16 v[104:105], v123 offset:128\n\t"
        "ds_read_b64_tr_b16 v[106:107], v123 offset:3200\n\t"
        "s_waitcnt lgkmcnt(10)\n\t"
        "v_mfma_f32_32x32x16_bf16 v[48:63], v[108:111], v[144:147], v[48:63]\n\t"
        "s_add_u32 s46, s46, 12288\n\t"
        "s_add_u32 s47, s47, 1\n\t"
        "ds_read_b64_tr_b16 v[108:109], v124 offset:128\n\t"
        "ds_read_b64_tr_b16 v[110:111], v124 offset:3200\n\t"
        "s_waitcnt lgkmcnt(10)\n\t"
        "v_mfma_f32_32x32x16_bf16 v[64:79], v[144:147], v[112:115], v[64:79]\n\t"
        "s_cmp_eq_u32 s46, 110592\n\t"
        "s_cselect_b32 s46, 0, s46\n\t"
        "ds_read_b64_tr_b16 v[112:113], v123 offset:256\n\t"
        "ds_read_b64_tr_b16 v[114:115], v123 offset:3328\n\t"
        "s_waitcnt lgkmcnt(10)\n\t"
        "v_mfma_f32_32x32x16_bf16 v[80:95], v[144:147], v[116:119], v[80:95]\n\t"
        "ds_write_b32 v127, v128\n\t"
        "ds_read_b64_tr_b16 v[116:117], v124 offset:256\n\t"
        "ds_read_b64_tr_b16 v[118:119], v124 offset:3328\n\t"
        "s_waitcnt lgkmcnt(0)\n\t"
        "s_nop 7\n\t"
        "s_branch 9f\n\t"
        "10:\n\t"
        "s_mov_b32 s48, 0x10000\n\t"
        "8:\n\t"
        "ds_read_b32 v126, v125\n\t"
        "s_sub_u32 s48, s48, 1\n\t"
        "s_waitcnt lgkmcnt(0)\n\t"
        "s_cbranch_scc1 11b\n\t"
        "v_cmp_gt_u32 vcc, s47, v126\n\t"
        "s_cbranch_vccnz 8b\n\t"
        "s_branch 11b\n\t"
        "12:\n\t"
        "s_mov_b32 s48, 0x10000\n\t"
        "8:\n\t"
        "ds_read_b32 v126, v125\n\t"
        "s_sub_u32 s48, s48, 1\n\t"
        "s_waitcnt lgkmcnt(0)\n\t"
        "s_cbranch_scc1 13b\n\t"
        "v_cmp_gt_u32 vcc, s47, v126\n\t"
        "s_cbranch_vccnz 8b\n\t"
        "s_branch 13b\n\t"
        "14:\n\t"
        "s_mov_b32 s48, 0x10000\n\t"
        "8:\n\t"
        "ds_read_b32 v126, v125\n\t"
        "s_sub_u32 s48, s48, 1\n\t"
        "s_waitcnt lgkmcnt(0)\n\t"
        "s_cbranch_scc1 15b\n\t"
        "v_cmp_gt_u32 vcc, s47, v126\n\t"
        "s_cbranch_vccnz 8b\n\t"
        "s_branch 15b\n\t"
        "9:\n\t"
        : "={v[0:15]}"(acc[0]), "={v[16:31]}"(acc[1]), "={v[32:47]}"(acc[2]), "={v[48:63]}"(acc[3]), "={v[64:79]}"(acc[4]), "={v[80:95]}"(acc[5]),
          "={v[96:111]}"(stA), "+{v[112:127]}"(stB), "+{v[128:131]}"(stC), "={s46}"(slot), "+{s47}"(need)
        : "{v[132:147]}"(xr0) : "s48", "vcc", "scc", "memory");
      STAMPV(2 + 16 * L);
      float al = 1.f, be = 0.f;
      if (L == 1) {
        PHASE_VARS;
        float S, Q, Dd;
        row_stats_sum(rs, OFF_SX, tile, lane, S, Q, Dd);
        float mean, var, mb1, rd1, mb2, den2;
        row_stats_fin(smem, 1, tile, lane, S, Q, Dd, U2F(words[0]), x1p, mean, var, mb1, rd1);
        ln_scalars(mean + (mean - mb1) * rd1, var * (1.0f + rd1) * (1.0f + rd1), mb2, den2);
        const float rd2 = RCP(den2);
        al = (1.0f + rd1) * rd2; be = -(mb1 * rd1 + mb2) * rd2;
        reinterpret_cast<float*>(smem + L_AB)[wid * 64 + lane] = hi ? be : al;
        reinterpret_cast<float*>(smem + L_AB)[256 + wid * 64 + lane] = hi ? rd1 : mb1;
        STAMPV(36);
        lds_wait_ge<0>(lbase + L_W + 24, 1);
        soff = ((tn + 1) & 15) * 4096;
        asm volatile(
          "buffer_load_dwordx4 v[148:151], v120, s[40:43], s44 offen offset:0 sc1\n\t"
          "buffer_load_dwordx4 v[152:155], v120, s[40:43], s44 offen offset:1024 sc1\n\t"
          "buffer_load_dwordx4 v[156:159], v120, s[40:43], s44 offen offset:2048 sc1\n\t"
          "buffer_load_dwordx4 v[160:163], v120, s[40:43], s44 offen offset:3072 sc1\n\t"
          "s_add_u32 s44, s44, 0x1000\n\t"
          "s_and_b32 s44, s44, 0xffff\n\t"
          "buffer_load_dwordx4 v[164:167], v120, s[40:43], s44 offen offset:0 sc1\n\t"
          "buffer_load_dwordx4 v[168:171], v120, s[40:43], s44 offen offset:1024 sc1\n\t"
          "buffer_load_dwordx4 v[172:175], v120, s[40:43], s44 offen offset:2048 sc1\n\t"
          "buffer_load_dwordx4 v[176:179], v120, s[40:43], s44 offen offset:3072 sc1\n\t"
          "s_add_u32 s44, s44, 0x1000\n\t"
          "s_and_b32 s44, s44, 0xffff\n\t"
          "buffer_load_dwordx4 v[180:183], v120, s[40:43], s44 offen offset:0 sc1\n\t"
          "buffer_load_dwordx4 v[184:187], v120, s[40:43], s44 offen offset:1024 sc1\n\t"
          "buffer_load_dwordx4 v[188:191], v120, s[40:43], s44 offen offset:2048 sc1\n\t"
          "buffer_load_dwordx4 v[192:195], v120, s[40:43], s44 offen offset:3072 sc1\n\t"
          : "={v[148:163]}"(xr1), "={v[164:179]}"(xr2), "={v[180:195]}"(xr3), "+{s44}"(soff) : "{v[112:127]}"(stB), "{s[40:43]}"(srd) : "scc", "memory");
      }
      STAMPV(3 + 16 * L);
      soff = ((tn + 1 + 2) & 15) * 4096;
      asm volatile(
        "s_nop 0\n\t"
        "s_add_u32 s44, s44, 0x1000\n\t"
        "s_and_b32 s44, s44, 0xffff\n\t"
        "buffer_load_dwordx4 v[132:135], v120, s[40:43], s44 offen offset:0 sc1\n\t"
        "buffer_load_dwordx4 v[136:139], v120, s[40:43], s44 offen offset:1024 sc1\n\t"
        "buffer_load_dwordx4 v[140:143], v120, s[40:43], s44 offen offset:2048 sc1\n\t"
        "s_mov_b32 s45, 3\n\t"
        "1:\n\t"
        "s_waitcnt vmcnt(14) lgkmcnt(11)\n\t"
        "v_mfma_f32_32x32x16_bf16 v[0:15], v[96:99], v[148:151], v[0:15]\n\t"
        "ds_read_b32 v126, v125\n\t"
        "ds_read_b64_tr_b16 v[96:97], v123 offset:6144\n\t"
        "ds_read_b64_tr_b16 v[98:99], v123 offset:9216\n\t"
        "s_waitcnt lgkmcnt(12)\n\t"
        "v_mfma_f32_32x32x16_bf16 v[16:31], v[100:103], v[148:151], v[16:31]\n\t"
        "v_add_u32 v129, s46, v121\n\t"
        "ds_read_b64_tr_b16 v[100:101], v124 offset:6144\n\t"
        "ds_read_b64_tr_b16 v[102:103], v124 offset:9216\n\t"
        "s_waitcnt lgkmcnt(12)\n\t"
        "v_mfma_f32_32x32x16_bf16 v[32:47], v[104:107], v[148:151], v[32:47]\n\t"
        "ds_read_b64_tr_b16 v[104:105], v123 offset:6272\n\t"
        "ds_read_b64_tr_b16 v[106:107], v123 offset:9344\n\t"
        "buffer_load_dwordx4 v[144:147], v120, s[40:43], s44 offen offset:3072 sc1\n\t"
        "s_waitcnt lgkmcnt(12)\n\t"
        "v_mfma_f32_32x32x16_bf16 v[48:63], v[108:111], v[148:151], v[48:63]\n\t"
        "v_add_u32 v130, s46, v122\n\t"
        "ds_read_b64_tr_b16 v[108:109], v124 offset:6272\n\t"
        "ds_read_b64_tr_b16 v[110:111], v124 offset:9344\n\t"
        "s_waitcnt lgkmcnt(12)\n\t"
        "v_mfma_f32_32x32x16_bf16 v[64:79], v[148:151], v[112:115], v[64:79]\n\t"
        "ds_read_b64_tr_b16 v[112:113], v123 offset:6400\n\t"
        "ds_read_b64_tr_b16 v[114:115], v123 offset:9472\n\t"
        "s_waitcnt lgkmcnt(10)\n\t"
        "v_mfma_f32_32x32x16_bf16 v[80:95], v[148:151], v[116:119], v[80:95]\n\t"
        "v_cmp_gt_u32 vcc, s47, v126\n\t"
        "ds_read_b64_tr_b16 v[116:117], v124 offset:6400\n\t"
        "ds_read_b64_tr_b16 v[118:119], v124 offset:9472\n\t"
        "s_waitcnt vmcnt(14) lgkmcnt(10)\n\t"
        "v_mfma_f32_32x32x16_bf16 v[0:15], v[96:99], v[152:155], v[0:15]\n\t"
        "s_cbranch_vccnz 16f\n\t"
        "17:\n\t"
        "ds_read_b64_tr_b16 v[96:97], v129 offset:0\n\t"
        "ds_read_b64_tr_b16 v[98:99], v129 offset:3072\n\t"
        "s_waitcnt lgkmcnt(10)\n\t"
        "v_mfma_f32_32x32x16_bf16 v[16:31], v[100:103], v[152:155], v[16:31]\n\t"
        "v_add_u32 v128, 1, v128\n\t"
        "ds_read_b64_tr_b16 v[100:101], v130 offset:0\n\t"
        "ds_read_b64_tr_b16 v[102:103], v130 offset:3072\n\t"
        "s_waitcnt lgkmcnt(10)\n\t"
        "v_mfma_f32_32x32x16_bf16 v[32:47], v[104:107], v[152:155], v[32:47]\n\t"
        "ds_read_b64_tr_b16 v[104:105], v129 offset:128\n\t"
        "ds_read_b64_tr_b16 v[106:107], v129 offset:3200\n\t"
        "s_add_u32 s44, s44, 0x1000\n\t"
        "s_and_b32 s44, s44, 0xffff\n\t"
        "buffer_load_dwordx4 v[148:151], v120, s[40:43], s44 offen offset:0 sc1\n\t"
        "s_waitcnt lgkmcnt(10)\n\t"
        "v_mfma_f32_32x32x16_bf16 v[48:63], v[108:111], v[152:155], v[48:63]\n\t"
        "s_add_u32 s46, s46, 12288\n\t"
        "s_add_u32 s47, s47, 1\n\t"
        "ds_read_b64_tr_b16 v[108:109], v130 offset:128\n\t"
        "ds_read_b64_tr_b16 v[110:111], v130 offset:3200\n\t"
        "s_waitcnt lgkmcnt(10)\n\t"
        "v_mfma_f32_32x32x16_bf16 v[64:79], v[152:155], v[112:115], v[64:79]\n\t"
        "s_cmp_eq_u32 s46, 110592\n\t"
        "s_cselect_b32 s46, 0, s46\n\t"
        "ds_read_b64_tr_b16 v[112:113], v129 offset:256\n\t"
        "ds_read_b64_tr_b16 v[114:115], v129 offset:3328\n\t"
        "s_waitcnt lgkmcnt(10)\n\t"
        "v_mfma_f32_32x32x16_bf16 v[80:95], v[152:155], v[116:119], v[80:95]\n\t"
        "ds_write_b32 v127, v128\n\t"
        "ds_read_b64_tr_b16 v[116:117], v130 offset:256\n\t"
        "ds_read_b64_tr_b16 v[118:119], v130 offset:3328\n\t"
        "s_waitcnt vmcnt(14) lgkmcnt(11)\n\t"
        "v_mfma_f32_32x32x16_bf16 v[0:15], v[96:99], v[156:159], v[0:15]\n\t"
        "ds_read_b32 v126, v125\n\t"
        "ds_read_b64_tr_b16 v[96:97], v129 offset:6144\n\t"
        "ds_read_b64_tr_b16 v[98:99], v129 offset:9216\n\t"
        "s_waitcnt lgkmcnt(12)\n\t"
        "v_mfma_f32_32x32x16_bf16 v[16:31], v[100:103], v[156:159], v[16:31]\n\t"
        "v_add_u32 v123, s46, v121\n\t"
        "ds_read_b64_tr_b16 v[100:101], v130 offset:6144\n\t"
        "ds_read_b64_tr_b16 v[102:103], v130 offset:9216\n\t"
        "s_waitcnt lgkmcnt(12)\n\t"
        "v_mfma_f32_32x32x16_bf16 v[32:47], v[104:107], v[156:159], v[32:47]\n\t"
        "ds_read_b64_tr_b16 v[104:105], v129 offset:6272\n\t"
        "ds_read_b64_tr_b16 v[106:107], v129 offset:9344\n\t"
        "buffer_load_dwordx4 v[152:155], v120, s[40:43], s44 offen offset:1024 sc1\n\t"
        "s_waitcnt lgkmcnt(12)\n\t"
        "v_mfma_f32_32x32x16_bf16 v[48:63], v[108:111], v[156:159], v[48:63]\n\t"
        "v_add_u32 v124, s46, v122\n\t"
        "ds_read_b64_tr_b16 v[108:109], v130 offset:6272\n\t"
        "ds_read_b64_tr_b16 v[110:111], v130 offset:9344\n\t"
        "s_waitcnt lgkmcnt(12)\n\t"
        "v_mfma_f32_32x32x16_bf16 v[64:79], v[156:159], v[112:115], v[64:79]\n\t"
        "ds_read_b64_tr_b16 v[112:113], v129 offset:6400\n\t"
        "ds_read_b64_tr_b16 v[114:115], v129 offset:9472\n\t"
        "s_waitcnt lgkmcnt(10)\n\t"
        "v_mfma_f32_32x32x16_bf16 v[80:95], v[156:159], v[116:119], v[80:95]\n\t"
        "v_cmp_gt_u32 vcc, s47, v126\n\t"
        "ds_read_b64_tr_b16 v[116:117], v130 offset:6400\n\t"
        "ds_read_b64_tr_b16 v[118:119], v130 offset:9472\n\t"
        "s_waitcnt vmcnt(14) lgkmcnt(10)\n\t"
        "v_mfma_f32_32x32x16_bf16 v[0:15], v[96:99], v[160:163], v[0:15]\n\t"
        "s_cbranch_vccnz 18f\n\t"
        "19:\n\t"
        "ds_read_b64_tr_b16 v[96:97], v123 offset:0\n\t"
        "ds_read_b64_tr_b16 v[98:99], v123 offset:3072\n\t"
        "s_waitcnt lgkmcnt(10)\n\t"
        "v_mfma_f32_32x32x16_bf16 v[16:31], v[100:103], v[160:163], v[16:31]\n\t"
        "v_add_u32 v128, 1, v128\n\t"
        "ds_read_b64_tr_b16 v[100:101], v124 offset:0\n\t"
        "ds_read_b64_tr_b16 v[102:103], v124 offset:3072\n\t"
        "s_waitcnt lgkmcnt(10)\n\t"
        "v_mfma_f32_32x32x16_bf16 v[32:47], v[104:107], v[160:163], v[32:47]\n\t"
        "ds_read_b64_tr_b16 v[104:105], v123 offset:128\n\t"
        "ds_read_b64_tr_b16 v[106:107], v123 offset:3200\n\t"
        "buffer_load_dwordx4 v[156:159], v120, s[40:43], s44 offen offset:2048 sc1\n\t"
        "s_waitcnt lgkmcnt(10)\n\t"
        "v_mfma_f32_32x32x16_bf16 v[48:63], v[108:111], v[160:163], v[48:63]\n\t"
        "s_add_u32 s46, s46, 12288\n\t"
        "s_add_u32 s47, s47, 1\n\t"
        "ds_read_b64_tr_b16 v[108:109], v124 offset:128\n\t"
        "ds_read_b64_tr_b16 v[110:111], v124 offset:3200\n\t"
        "s_waitcnt lgkmcnt(10)\n\t"
        "v_mfma_f32_32x32x16_bf16 v[64:79], v[160:163], v[112:115], v[64:79]\n\t"
        "s_cmp_eq_u32 s46, 110592\n\t"
        "s_cselect_b32 s46, 0, s46\n\t"
        "ds_read_b64_tr_b16 v[112:113], v123 offset:256\n\t"
        "ds_read_b64_tr_b16 v[114:115], v123 offset:3328\n\t"
        "s_waitcnt lgkmcnt(10)\n\t"
        "v_mfma_f32_32x32x16_bf16 v[80:95], v[160:163], v[116:119], v[80:95]\n\t"
        "ds_write_b32 v127, v128\n\t"
        "ds_read_b64_tr_b16 v[116:117], v124 offset:256\n\t"
        "ds_read_b64_tr_b16 v[118:119], v124 offset:3328\n\t"
        "s_waitcnt vmcnt(14) lgkmcnt(11)\n\t"
        "v_mfma_f32_32x32x16_bf16 v[0:15], v[96:99], v[164:167], v[0:15]\n\t"
        "ds_read_b32 v126, v125\n\t"
        "ds_read_b64_tr_b16 v[96:97], v123 offset:6144\n\t"
        "ds_read_b64_tr_b16 v[98:99], v123 offset:9216\n\t"
        "s_waitcnt lgkmcnt(12)\n\t"
        "v_mfma_f32_32x32x16_bf16 v[16:31], v[100:103], v[164:167], v[16:31]\n\t"
        "v_add_u32 v129, s46, v121\n\t"
        "ds_read_b64_tr_b16 v[100:101], v124 offset:6144\n\t"
        "ds_read_b64_tr_b16 v[102:103], v124 offset:9216\n\t"
        "s_waitcnt lgkmcnt(12)\n\t"
        "v_mfma_f32_32x32x16_bf16 v[32:47], v[104:107], v[164:167], v[32:47]\n\t"
        "ds_read_b64_tr_b16 v[104:105], v123 offset:6272\n\t"
        "ds_read_b64_tr_b16 v[106:107], v123 offset:9344\n\t"
        "buffer_load_dwordx4 v[160:163], v120, s[40:43], s44 offen offset:3072 sc1\n\t"
        "s_waitcnt lgkmcnt(12)\n\t"
        "v_mfma_f32_32x32x16_bf16 v[48:63], v[108:111], v[164:167], v[48:63]\n\t"
        "v_add_u32 v130, s46, v122\n\t"
        "ds_read_b64_tr_b16 v[108:109], v124 offset:6272\n\t"
        "ds_read_b64_tr_b16 v[110:111], v124 offset:9344\n\t"
        "s_waitcnt lgkmcnt(12)\n\t"
        "v_mfma_f32_32x32x16_bf16 v[64:79], v[164:167], v[112:115], v[64:79]\n\t"
        "ds_read_b64_tr_b16 v[112:113], v123 offset:6400\n\t"
        "ds_read_b64_tr_b16 v[114:115], v123 offset:9472\n\t"
        "s_waitcnt lgkmcnt(10)\n\t"
        "v_mfma_f32_32x32x16_bf16 v[80:95], v[164:167], v[116:119], v[80:95]\n\t"
        "v_cmp_gt_u32 vcc, s47, v126\n\t"
        "ds_read_b64_tr_b16 v[116:117], v124 offset:6400\n\t"
        "ds_read_b64_tr_b16 v[118:119], v124 offset:9472\n\t"
        "s_waitcnt vmcnt(14) lgkmcnt(10)\n\t"
        "v_mfma_f32_32x32x16_bf16 v[0:15], v[96:99], v[168:171], v[0:15]\n\t"
        "s_cbranch_vccnz 20f\n\t"
        "21:\n\t"
        "ds_read_b64_tr_b16 v[96:97], v129 offset:0\n\t"
        "ds_read_b64_tr_b16 v[98:99], v129 offset:3072\n\t"
        "s_waitcnt lgkmcnt(10)\n\t"
        "v_mfma_f32_32x32x16_bf16 v[16:31], v[100:103], v[168:171], v[16:31]\n\t"
        "v_add_u32 v128, 1, v128\n\t"
        "ds_read_b64_tr_b16 v[100:101], v130 offset:0\n\t"
        "ds_read_b64_tr_b16 v[102:103], v130 offset:3072\n\t"
        "s_waitcnt lgkmcnt(10)\n\t"
        "v_mfma_f32_32x32x16_bf16 v[32:47], v[104:107], v[168:171], v[32:47]\n\t"
        "ds_read_b64_tr_b16 v[104:105], v129 offset:128\n\t"
        "ds_read_b64_tr_b16 v[106:107], v129 offset:3200\n\t"
        "s_add_u32 s44, s44, 0x1000\n\t"
        "s_and_b32 s44, s44, 0xffff\n\t"
        "buffer_load_dwordx4 v[164:167], v120, s[40:43], s44 offen offset:0 sc1\n\t"
        "s_waitcnt lgkmcnt(10)\n\t"
        "v_mfma_f32_32x32x16_bf16 v[48:63], v[108:111], v[168:171], v[48:63]\n\t"
        "s_add_u32 s46, s46, 12288\n\t"
        "s_add_u32 s47, s47, 1\n\t"
        "ds_read_b64_tr_b16 v[108:109], v130 offset:128\n\t"
        "ds_read_b64_tr_b16 v[110:111], v130 offset:3200\n\t"
        "s_waitcnt lgkmcnt(10)\n\t"
        "v_mfma_f32_32x32x16_bf16 v[64:79], v[168:171], v[112:115], v[64:79]\n\t"
        "s_cmp_eq_u32 s46, 110592\n\t"
        "s_cselect_b32 s46, 0, s46\n\t"
        "ds_read_b64_tr_b16 v[112:113], v129 offset:256\n\t"
        "ds_read_b64_tr_b16 v[114:115], v129 offset:3328\n\t"
        "s_waitcnt lgkmcnt(10)\n\t"
        "v_mfma_f32_32x32x16_bf16 v[80:95], v[168:171], v[116:119], v[80:95]\n\t"
        "ds_write_b32 v127, v128\n\t"
        "ds_read_b64_tr_b16 v[116:117], v130 offset:256\n\t"
        "ds_read_b64_tr_b16 v[118:119], v130 offset:3328\n\t"
        "s_waitcnt vmcnt(14) lgkmcnt(11)\n\t"
        "v_mfma_f32_32x32x16_bf16 v[0:15], v[96:99], v[172:175], v[0:15]\n\t"
        "ds_read_b32 v126, v125\n\t"
        "ds_read_b64_tr_b16 v[96:97], v129 offset:6144\n\t"
        "ds_read_b64_tr_b16 v[98:99], v129 offset:9216\n\t"
        "s_waitcnt lgkmcnt(12)\n\t"
        "v_mfma_f32_32x32x16_bf16 v[16:31], v[100:103], v[172:175], v[16:31]\n\t"
        "v_add_u32 v123, s46, v121\n\t"
        "ds_read_b64_tr_b16 v[100:101], v130 offset:6144\n\t"
        "ds_read_b64_tr_b16 v[102:103], v130 offset:9216\n\t"
        "s_waitcnt lgkmcnt(12)\n\t"
        "v_mfma_f32_32x32x16_bf16 v[32:47], v[104:107], v[172:175], v[32:47]\n\t"
        "ds_read_b64_tr_b16 v[104:105], v129 offset:6272\n\t"
        "ds_read_b64_tr_b16 v[106:107], v129 offset:9344\n\t"
        "buffer_load_dwordx4 v[168:171], v120, s[40:43], s44 offen offset:1024 sc1\n\t"
        "s_waitcnt lgkmcnt(12)\n\t"
        "v_mfma_f32_32x32x16_bf16 v[48:63], v[108:111], v[172:175], v[48:63]\n\t"
        "v_add_u32 v124, s46, v122\n\t"
        "ds_read_b64_tr_b16 v[108:109], v130 offset:6272\n\t"
        "ds_read_b64_tr_b16 v[110:111], v130 offset:9344\n\t"
        "s_waitcnt lgkmcnt(12)\n\t"
        "v_mfma_f32_32x32x16_bf16 v[64:79], v[172:175], v[112:115], v[64:79]\n\t"
        "ds_read_b64_tr_b16 v[112:113], v129 offset:6400\n\t"
        "ds_read_b64_tr_b16 v[114:115], v129 offset:9472\n\t"
        "s_waitcnt lgkmcnt(10)\n\t"
        "v_mfma_f32_32x32x16_bf16 v[80:95], v[172:175], v[116:119], v[80:95]\n\t"
        "v_cmp_gt_u32 vcc, s47, v126\n\t"
        "ds_read_b64_tr_b16 v[116:117], v130 offset:6400\n\t"
        "ds_read_b64_tr_b16 v[118:119], v130 offset:9472\n\t"
        "s_waitcnt vmcnt(14) lgkmcnt(10)\n\t"
        "v_mfma_f32_32x32x16_bf16 v[0:15], v[96:99], v[176:179], v[0:15]\n\t"
        "s_cbranch_vccnz 22f\n\t"
        "23:\n\t"
        "ds_read_b64_tr_b16 v[96:97], v123 offset:0\n\t"
        "ds_read_b64_tr_b16 v[98:99], v123 offset:3072\n\t"
        "s_waitcnt lgkmcnt(10)\n\t"
        "v_mfma_f32_32x32x16_bf16 v[16:31], v[100:103], v[176:179], v[16:31]\n\t"
        "v_add_u32 v128, 1, v128\n\t"
        "ds_read_b64_tr_b16 v[100:101], v124 offset:0\n\t"
        "ds_read_b64_tr_b16 v[102:103], v124 offset:3072\n\t"
        "s_waitcnt lgkmcnt(10)\n\t"
        "v_mfma_f32_32x32x16_bf16 v[32:47], v[104:107], v[176:179], v[32:47]\n\t"
        "ds_read_b64_tr_b16 v[104:105], v123 offset:128\n\t"
        "ds_read_b64_tr_b16 v[106:107], v123 offset:3200\n\t"
        "buffer_load_dwordx4 v[172:175], v120, s[40:43], s44 offen offset:2048 sc1\n\t"
        "s_waitcnt lgkmcnt(10)\n\t"
        "v_mfma_f32_32x32x16_bf16 v[48:63], v[108:111], v[176:179], v[48:63]\n\t"
        "s_add_u32 s46, s46, 12288\n\t"
        "s_add_u32 s47, s47, 1\n\t"
        "ds_read_b64_tr_b16 v[108:109], v124 offset:128\n\t"
        "ds_read_b64_tr_b16 v[110:111], v124 offset:3200\n\t"
        "s_waitcnt lgkmcnt(10)\n\t"
        "v_mfma_f32_32x32x16_bf16 v[64:79], v[176:179], v[112:115], v[64:79]\n\t"
        "s_cmp_eq_u32 s46, 110592\n\t"
        "s_cselect_b32 s46, 0, s46\n\t"
        "ds_read_b64_tr_b16 v[112:113], v123 offset:256\n\t"
        "ds_read_b64_tr_b16 v[114:115], v123 offset:3328\n\t"
        "s_waitcnt lgkmcnt(10)\n\t"
        "v_mfma_f32_32x32x16_bf16 v[80:95], v[176:179], v[116:119], v[80:95]\n\t"
        "ds_write_b32 v127, v128\n\t"
        "ds_read_b64_tr_b16 v[116:117], v124 offset:256\n\t"
        "ds_read_b64_tr_b16 v[118:119], v124 offset:3328\n\t"
        "s_waitcnt vmcnt(14) lgkmcnt(11)\n\t"
        "v_mfma_f32_32x32x16_bf16 v[0:15], v[96:99], v[180:183], v[0:15]\n\t"
        "ds_read_b32 v126, v125\n\t"
        "ds_read_b64_tr_b16 v[96:97], v123 offset:6144\n\t"
        "ds_read_b64_tr_b16 v[98:99], v123 offset:9216\n\t"
        "s_waitcnt lgkmcnt(12)\n\t"
        "v_mfma_f32_32x32x16_bf16 v[16:31], v[100:103], v[180:183], v[16:31]\n\t"
        "v_add_u32 v129, s46, v121\n\t"
        "ds_read_b64_tr_b16 v[100:101], v124 offset:6144\n\t"
        "ds_read_b64_tr_b16 v[102:103], v124 offset:9216\n\t"
        "s_waitcnt lgkmcnt(12)\n\t"
        "v_mfma_f32_32x32x16_bf16 v[32:47], v[104:107], v[180:183], v[32:47]\n\t"
        "ds_read_b64_tr_b16 v[104:105], v123 offset:6272\n\t"
        "ds_read_b64_tr_b16 v[106:107], v123 offset:9344\n\t"
        "buffer_load_dwordx4 v[176:179], v120, s[40:43], s44 offen offset:3072 sc1\n\t"
        "s_waitcnt lgkmcnt(12)\n\t"
        "v_mfma_f32_32x32x16_bf16 v[48:63], v[108:111], v[180:183], v[48:63]\n\t"
        "v_add_u32 v130, s46, v122\n\t"
        "ds_read_b64_tr_b16 v[108:109], v124 offset:6272\n\t"
        "ds_read_b64_tr_b16 v[110:111], v124 offset:9344\n\t"
        "s_waitcnt lgkmcnt(12)\n\t"
        "v_mfma_f32_32x32x16_bf16 v[64:79], v[180:183], v[112:115], v[64:79]\n\t"
        "ds_read_b64_tr_b16 v[112:113], v123 offset:6400\n\t"
        "ds_read_b64_tr_b16 v[114:115], v123 offset:9472\n\t"
        "s_waitcnt lgkmcnt(10)\n\t"
        "v_mfma_f32_32x32x16_bf16 v[80:95], v[180:183], v[116:119], v[80:95]\n\t"
        "v_cmp_gt_u32 vcc, s47, v126\n\t"
        "ds_read_b64_tr_b16 v[116:117], v124 offset:6400\n\t"
        "ds_read_b64_tr_b16 v[118:119], v124 offset:9472\n\t"
        "s_waitcnt vmcnt(14) lgkmcnt(10)\n\t"
        "v_mfma_f32_32x32x16_bf16 v[0:15], v[96:99], v[184:187], v[0:15]\n\t"
        "s_cbranch_vccnz 24f\n\t"
        "25:\n\t"
        "ds_read_b64_tr_b16 v[96:97], v129 offset:0\n\t"
        "ds_read_b64_tr_b16 v[98:99], v129 offset:3072\n\t"
        "s_waitcnt lgkmcnt(10)\n\t"
        "v_mfma_f32_32x32x16_bf16 v[16:31], v[100:103], v[184:187], v[16:31]\n\t"
        "v_add_u32 v128, 1, v128\n\t"
        "ds_read_b64_tr_b16 v[100:101], v130 offset:0\n\t"
        "ds_read_b64_tr_b16 v[102:103], v130 offset:3072\n\t"
        "s_waitcnt lgkmcnt(10)\n\t"
        "v_mfma_f32_32x32x16_bf16 v[32:47], v[104:107], v[184:187], v[32:47]\n\t"
        "ds_read_b64_tr_b16 v[104:105], v129 offset:128\n\t"
        "ds_read_b64_tr_b16 v[106:107], v129 offset:3200\n\t"
        "s_add_u32 s44, s44, 0x1000\n\t"
        "s_and_b32 s44, s44, 0xffff\n\t"
        "buffer_load_dwordx4 v[180:183], v120, s[40:43], s44 offen offset:0 sc1\n\t"
        "s_waitcnt lgkmcnt(10)\n\t"
        "v_mfma_f32_32x32x16_bf16 v[48:63], v[108:111], v[184:187], v[48:63]\n\t"
        "s_add_u32 s46, s46, 12288\n\t"
        "s_add_u32 s47, s47, 1\n\t"
        "ds_read_b64_tr_b16 v[108:109], v130 offset:128\n\t"
        "ds_read_b64_tr_b16 v[110:111], v130 offset:3200\n\t"
        "s_waitcnt lgkmcnt(10)\n\t"
        "v_mfma_f32_32x32x16_bf16 v[64:79], v[184:187], v[112:115], v[64:79]\n\t"
        "s_cmp_eq_u32 s46, 110592\n\t"
        "s_cselect_b32 s46, 0, s46\n\t"
        "ds_read_b64_tr_b16 v[112:113], v129 offset:256\n\t"
        "ds_read_b64_tr_b16 v[114:115], v129 offset:3328\n\t"
        "s_waitcnt lgkmcnt(10)\n\t"
        "v_mfma_f32_32x32x16_bf16 v[80:95], v[184:187], v[116:119], v[80:95]\n\t"
        "ds_write_b32 v127, v128\n\t"
        "ds_read_b64_tr_b16 v[116:117], v130 offset:256\n\t"
        "ds_read_b64_tr_b16 v[118:119], v130 offset:3328\n\t"
        "s_waitcnt vmcnt(14) lgkmcnt(11)\n\t"
        "v_mfma_f32_32x32x16_bf16 v[0:15], v[96:99], v[188:191], v[0:15]\n\t"
        "ds_read_b32 v126, v125\n\t"
        "ds_read_b64_tr_b16 v[96:97], v129 offset:6144\n\t"
        "ds_read_b64_tr_b16 v[98:99], v129 offset:9216\n\t"
        "s_waitcnt lgkmcnt(12)\n\t"
        "v_mfma_f32_32x32x16_bf16 v[16:31], v[100:103], v[188:191], v[16:31]\n\t"
        "v_add_u32 v123, s46, v121\n\t"
        "ds_read_b64_tr_b16 v[100:101], v130 offset:6144\n\t"
        "ds_read_b64_tr_b16 v[102:103], v130 offset:9216\n\t"
        "s_waitcnt lgkmcnt(12)\n\t"
        "v_mfma_f32_32x32x16_bf16 v[32:47], v[104:107], v[188:191], v[32:47]\n\t"
        "ds_read_b64_tr_b16 v[104:105], v129 offset:6272\n\t"
        "ds_read_b64_tr_b16 v[106:107], v129 offset:9344\n\t"
        "buffer_load_dwordx4 v[184:187], v120, s[40:43], s44 offen offset:1024 sc1\n\t"
        "s_waitcnt lgkmcnt(12)\n\t"
        "v_mfma_f32_32x32x16_bf16 v[48:63], v[108:111], v[188:191], v[48:63]\n\t"
        "v_add_u32 v124, s46, v122\n\t"
        "ds_read_b64_tr_b16 v[108:109], v130 offset:6272\n\t"
        "ds_read_b64_tr_b16 v[110:111], v130 offset:9344\n\t"
        "s_waitcnt lgkmcnt(12)\n\t"
        "v_mfma_f32_32x32x16_bf16 v[64:79], v[188:191], v[112:115], v[64:79]\n\t"
        "ds_read_b64_tr_b16 v[112:113], v129 offset:6400\n\t"
        "ds_read_b64_tr_b16 v[114:115], v129 offset:9472\n\t"
        "s_waitcnt lgkmcnt(10)\n\t"
        "v_mfma_f32_32x32x16_bf16 v[80:95], v[188:191], v[116:119], v[80:95]\n\t"
        "v_cmp_gt_u32 vcc, s47, v126\n\t"
        "ds_read_b64_tr_b16 v[116:117], v130 offset:6400\n\t"
        "ds_read_b64_tr_b16 v[118:119], v130 offset:9472\n\t"
        "s_waitcnt vmcnt(14) lgkmcnt(10)\n\t"
        "v_mfma_f32_32x32x16_bf16 v[0:15], v[96:99], v[192:195], v[0:15]\n\t"
        "s_cbranch_vccnz 26f\n\t"
        "27:\n\t"
        "ds_read_b64_tr_b16 v[96:97], v123 offset:0\n\t"
        "ds_read_b64_tr_b16 v[98:99], v123 offset:3072\n\t"
        "s_waitcnt lgkmcnt(10)\n\t"
        "v_mfma_f32_32x32x16_bf16 v[16:31], v[100:103], v[192:195], v[16:31]\n\t"
        "v_add_u32 v128, 1, v128\n\t"
        "ds_read_b64_tr_b16 v[100:101], v124 offset:0\n\t"
        "ds_read_b64_tr_b16 v[102:103], v124 offset:3072\n\t"
        "s_waitcnt lgkmcnt(10)\n\t"
        "v_mfma_f32_32x32x16_bf16 v[32:47], v[104:107], v[192:195], v[32:47]\n\t"
        "ds_read_b64_tr_b16 v[104:105], v123 offset:128\n\t"
        "ds_read_b64_tr_b16 v[106:107], v123 offset:3200\n\t"
        "buffer_load_dwordx4 v[188:191], v120, s[40:43], s44 offen offset:2048 sc1\n\t"
        "s_waitcnt lgkmcnt(10)\n\t"
        "v_mfma_f32_32x32x16_bf16 v[48:63], v[108:111], v[192:195], v[48:63]\n\t"
        "s_add_u32 s46, s46, 12288\n\t"
        "s_add_u32 s47, s47, 1\n\t"
        "ds_read_b64_tr_b16 v[108:109], v124 offset:128\n\t"
        "ds_read_b64_tr_b16 v[110:111], v124 offset:3200\n\t"
        "s_waitcnt lgkmcnt(10)\n\t"
        "v_mfma_f32_32x32x16_bf16 v[64:79], v[192:195], v[112:115], v[64:79]\n\t"
        "s_cmp_eq_u32 s46, 110592\n\t"
        "s_cselect_b32 s46, 0, s46\n\t"
        "ds_read_b64_tr_b16 v[112:113], v123 offset:256\n\t"
        "ds_read_b64_tr_b16 v[114:115], v123 offset:3328\n\t"
        "s_waitcnt lgkmcnt(10)\n\t"
        "v_mfma_f32_32x32x16_bf16 v[80:95], v[192:195], v[116:119], v[80:95]\n\t"
        "ds_write_b32 v127, v128\n\t"
        "ds_read_b64_tr_b16 v[116:117], v124 offset:256\n\t"
        "ds_read_b64_tr_b16 v[118:119], v124 offset:3328\n\t"
        "s_waitcnt vmcnt(14) lgkmcnt(11)\n\t"
        "v_mfma_f32_32x32x16_bf16 v[0:15], v[96:99], v[132:135], v[0:15]\n\t"
        "ds_read_b32 v126, v125\n\t"
        "ds_read_b64_tr_b16 v[96:97], v123 offset:6144\n\t"
        "ds_read_b64_tr_b16 v[98:99], v123 offset:9216\n\t"
        "s_waitcnt lgkmcnt(12)\n\t"
        "v_mfma_f32_32x32x16_bf16 v[16:31], v[100:103], v[132:135], v[16:31]\n\t"
        "v_add_u32 v129, s46, v121\n\t"
        "ds_read_b64_tr_b16 v[100:101], v124 offset:6144\n\t"
        "ds_read_b64_tr_b16 v[102:103], v124 offset:9216\n\t"
        "s_waitcnt lgkmcnt(12)\n\t"
        "v_mfma_f32_32x32x16_bf16 v[32:47], v[104:107], v[132:135], v[32:47]\n\t"
        "ds_read_b64_tr_b16 v[104:105], v123 offset:6272\n\t"
        "ds_read_b64_tr_b16 v[106:107], v123 offset:9344\n\t"
        "buffer_load_dwordx4 v[192:195], v120, s[40:43], s44 offen offset:3072 sc1\n\t"
        "s_waitcnt lgkmcnt(12)\n\t"
        "v_mfma_f32_32x32x16_bf16 v[48:63], v[108:111], v[132:135], v[48:63]\n\t"
        "v_add_u32 v130, s46, v122\n\t"
        "ds_read_b64_tr_b16 v[108:109], v124 offset:6272\n\t"
        "ds_read_b64_tr_b16 v[110:111], v124 offset:9344\n\t"
        "s_waitcnt lgkmcnt(12)\n\t"
        "v_mfma_f32_32x32x16_bf16 v[64:79], v[132:135], v[112:115], v[64:79]\n\t"
        "ds_read_b64_tr_b16 v[112:113], v123 offset:6400\n\t"
        "ds_read_b64_tr_b16 v[114:115], v123 offset:9472\n\t"
        "s_waitcnt lgkmcnt(10)\n\t"
        "v_mfma_f32_32x32x16_bf16 v[80:95], v[132:135], v[116:119], v[80:95]\n\t"
        "v_cmp_gt_u32 vcc, s47, v126\n\t"
        "ds_read_b64_tr_b16 v[116:117], v124 offset:6400\n\t"
        "ds_read_b64_tr_b16 v[118:119], v124 offset:9472\n\t"
        "s_waitcnt vmcnt(14) lgkmcnt(10)\n\t"
        "v_mfma_f32_32x32x16_bf16 v[0:15], v[96:99], v[136:139], v[0:15]\n\t"
        "s_cbranch_vccnz 28f\n\t"
        "29:\n\t"
        "ds_read_b64_tr_b16 v[96:97], v129 offset:0\n\t"
        "ds_read_b64_tr_b16 v[98:99], v129 offset:3072\n\t"
        "s_waitcnt lgkmcnt(10)\n\t"
        "v_mfma_f32_32x32x16_bf16 v[16:31], v[100:103], v[136:139], v[16:31]\n\t"
        "v_add_u32 v128, 1, v128\n\t"
        "ds_read_b64_tr_b16 v[100:101], v130 offset:0\n\t"
        "ds_read_b64_tr_b16 v[102:103], v130 offset:3072\n\t"
        "s_waitcnt lgkmcnt(10)\n\t"
        "v_mfma_f32_32x32x16_bf16 v[32:47], v[104:107], v[136:139], v[32:47]\n\t"
        "ds_read_b64_tr_b16 v[104:105], v129 offset:128\n\t"
        "ds_read_b64_tr_b16 v[106:107], v129 offset:3200\n\t"
        "s_add_u32 s44, s44, 0x1000\n\t"
        "s_and_b32 s44, s44, 0xffff\n\t"
        "buffer_load_dwordx4 v[132:135], v120, s[40:43], s44 offen offset:0 sc1\n\t"
        "s_waitcnt lgkmcnt(10)\n\t"
        "v_mfma_f32_32x32x16_bf16 v[48:63], v[108:111], v[136:139], v[48:63]\n\t"
        "s_add_u32 s46, s46, 12288\n\t"
        "s_add_u32 s47, s47, 1\n\t"
        "ds_read_b64_tr_b16 v[108:109], v130 offset:128\n\t"
        "ds_read_b64_tr_b16 v[110:111], v130 offset:3200\n\t"
        "s_waitcnt lgkmcnt(10)\n\t"
        "v_mfma_f32_32x32x16_bf16 v[64:79], v[136:139], v[112:115], v[64:79]\n\t"
        "s_cmp_eq_u32 s46, 110592\n\t"
        "s_cselect_b32 s46, 0, s46\n\t"
        "ds_read_b64_tr_b16 v[112:113], v129 offset:256\n\t"
        "ds_read_b64_tr_b16 v[114:115], v129 offset:3328\n\t"
        "s_waitcnt lgkmcnt(10)\n\t"
        "v_mfma_f32_32x32x16_bf16 v[80:95], v[136:139], v[116:119], v[80:95]\n\t"
        "ds_write_b32 v127, v128\n\t"
        "ds_read_b64_tr_b16 v[116:117], v130 offset:256\n\t"
        "ds_read_b64_tr_b16 v[118:119], v130 offset:3328\n\t"
        "s_waitcnt vmcnt(14) lgkmcnt(11)\n\t"
        "v_mfma_f32_32x32x16_bf16 v[0:15], v[96:99], v[140:143], v[0:15]\n\t"
        "ds_read_b32 v126, v125\n\t"
        "ds_read_b64_tr_b16 v[96:97], v129 offset:6144\n\t"
        "ds_read_b64_tr_b16 v[98:99], v129 offset:9216\n\t"
        "s_waitcnt lgkmcnt(12)\n\t"
        "v_mfma_f32_32x32x16_bf16 v[16:31], v[100:103], v[140:143], v[16:31]\n\t"
        "v_add_u32 v123, s46, v121\n\t"
        "ds_read_b64_tr_b16 v[100:101], v130 offset:6144\n\t"
        "ds_read_b64_tr_b16 v[102:103], v130 offset:9216\n\t"
        "s_waitcnt lgkmcnt(12)\n\t"
        "v_mfma_f32_32x32x16_bf16 v[32:47], v[104:107], v[140:143], v[32:47]\n\t"
        "ds_read_b64_tr_b16 v[104:105], v129 offset:6272\n\t"
        "ds_read_b64_tr_b16 v[106:107], v129 offset:9344\n\t"
        "buffer_load_dwordx4 v[136:139], v120, s[40:43], s44 offen offset:1024 sc1\n\t"
        "s_waitcnt lgkmcnt(12)\n\t"
        "v_mfma_f32_32x32x16_bf16 v[48:63], v[108:111], v[140:143], v[48:63]\n\t"
        "v_add_u32 v124, s46, v122\n\t"
        "ds_read_b64_tr_b16 v[108:109], v130 offset:6272\n\t"
        "ds_read_b64_tr_b16 v[110:111], v130 offset:9344\n\t"
        "s_waitcnt lgkmcnt(12)\n\t"
        "v_mfma_f32_32x32x16_bf16 v[64:79], v[140:143], v[112:115], v[64:79]\n\t"
        "ds_read_b64_tr_b16 v[112:113], v129 offset:6400\n\t"
        "ds_read_b64_tr_b16 v[114:115], v129 offset:9472\n\t"
        "s_waitcnt lgkmcnt(10)\n\t"
        "v_mfma_f32_32x32x16_bf16 v[80:95], v[140:143], v[116:119], v[80:95]\n\t"
        "v_cmp_gt_u32 vcc, s47, v126\n\t"
        "ds_read_b64_tr_b16 v[116:117], v130 offset:6400\n\t"
        "ds_read_b64_tr_b16 v[118:119], v130 offset:9472\n\t"
        "s_waitcnt vmcnt(14) lgkmcnt(10)\n\t"
        "v_mfma_f32_32x32x16_bf16 v[0:15], v[96:99], v[144:147], v[0:15]\n\t"
        "s_cbranch_vccnz 30f\n\t"
        "31:\n\t"
        "ds_read_b64_tr_b16 v[96:97], v123 offset:0\n\t"
        "ds_read_b64_tr_b16 v[98:99], v123 offset:3072\n\t"
        "s_waitcnt lgkmcnt(10)\n\t"
        "v_mfma_f32_32x32x16_bf16 v[16:31], v[100:103], v[144:147], v[16:31]\n\t"
        "v_add_u32 v128, 1, v128\n\t"
        "ds_read_b64_tr_b16 v[100:101], v124 offset:0\n\t"
        "ds_read_b64_tr_b16 v[102:103], v124 offset:3072\n\t"
        "s_waitcnt lgkmcnt(10)\n\t"
        "v_mfma_f32_32x32x16_bf16 v[32:47], v[104:107], v[144:147], v[32:47]\n\t"
        "ds_read_b64_tr_b16 v[104:105], v123 offset:128\n\t"
        "ds_read_b64_tr_b16 v[106:107], v123 offset:3200\n\t"
        "buffer_load_dwordx4 v[140:143], v120, s[40:43], s44 offen offset:2048 sc1\n\t"
        "s_waitcnt lgkmcnt(10)\n\t"
        "v_mfma_f32_32x32x16_bf16 v[48:63], v[108:111], v[144:147], v[48:63]\n\t"
        "s_add_u32 s46, s46, 12288\n\t"
        "s_add_u32 s47, s47, 1\n\t"
        "ds_read_b64_tr_b16 v[108:109], v124 offset:128\n\t"
        "ds_read_b64_tr_b16 v[110:111], v124 offset:3200\n\t"
        "s_waitcnt lgkmcnt(10)\n\t"
        "v_mfma_f32_32x32x16_bf16 v[64:79], v[144:147], v[112:115], v[64:79]\n\t"
        "s_cmp_eq_u32 s46, 110592\n\t"
        "s_cselect_b32 s46, 0, s46\n\t"
        "ds_read_b64_tr_b16 v[112:113], v123 offset:256\n\t"
        "ds_read_b64_tr_b16 v[114:115], v123 offset:3328\n\t"
        "s_waitcnt lgkmcnt(10)\n\t"
        "v_mfma_f32_32x32x16_bf16 v[80:95], v[144:147], v[116:119], v[80:95]\n\t"
        "ds_write_b32 v127, v128\n\t"
        "ds_read_b64_tr_b16 v[116:117], v124 offset:256\n\t"
        "ds_read_b64_tr_b16 v[118:119], v124 offset:3328\n\t"
        "s_sub_u32 s45, s45, 1\n\t"
        "s_cmp_lg_u32 s45, 0\n\t"
        "s_cbranch_scc1 1b\n\t"
        "s_waitcnt vmcnt(14) lgkmcnt(11)\n\t"
        "v_mfma_f32_32x32x16_bf16 v[0:15], v[96:99], v[148:151], v[0:15]\n\t"
        "ds_read_b32 v126, v125\n\t"
        "ds_read_b64_tr_b16 v[96:97], v123 offset:6144\n\t"
        "ds_read_b64_tr_b16 v[98:99], v123 offset:9216\n\t"
        "s_waitcnt lgkmcnt(12)\n\t"
        "v_mfma_f32_32x32x16_bf16 v[16:31], v[100:103], v[148:151], v[16:31]\n\t"
        "v_add_u32 v129, s46, v121\n\t"
        "ds_read_b64_tr_b16 v[100:101], v124 offset:6144\n\t"
        "ds_read_b64_tr_b16 v[102:103], v124 offset:9216\n\t"
        "s_waitcnt lgkmcnt(12)\n\t"
        "v_mfma_f32_32x32x16_bf16 v[32:47], v[104:107], v[148:151], v[32:47]\n\t"
        "ds_read_b64_tr_b16 v[104:105], v123 offset:6272\n\t"
        "ds_read_b64_tr_b16 v[106:107], v123 offset:9344\n\t"
        "s_waitcnt lgkmcnt(12)\n\t"
        "v_mfma_f32_32x32x16_bf16 v[48:63], v[108:111], v[148:151], v[48:63]\n\t"
        "v_add_u32 v130, s46, v122\n\t"
        "ds_read_b64_tr_b16 v[108:109], v124 offset:6272\n\t"
        "ds_read_b64_tr_b16 v[110:111], v124 offset:9344\n\t"
        "s_waitcnt lgkmcnt(12)\n\t"
        "v_mfma_f32_32x32x16_bf16 v[64:79], v[148:151], v[112:115], v[64:79]\n\t"
        "ds_read_b64_tr_b16 v[112:113], v123 offset:6400\n\t"
        "ds_read_b64_tr_b16 v[114:115], v123 offset:9472\n\t"
        "s_waitcnt lgkmcnt(10)\n\t"
        "v_mfma_f32_32x32x16_bf16 v[80:95], v[148:151], v[116:119], v[80:95]\n\t"
        "v_cmp_gt_u32 vcc, s47, v126\n\t"
        "ds_read_b64_tr_b16 v[116:117], v124 offset:6400\n\t"
        "ds_read_b64_tr_b16 v[118:119], v124 offset:9472\n\t"
        "s_waitcnt vmcnt(13) lgkmcnt(10)\n\t"
        "v_mfma_f32_32x32x16_bf16 v[0:15], v[96:99], v[152:155], v[0:15]\n\t"
        "s_cbranch_vccnz 64f\n\t"
        "65:\n\t"
        "ds_read_b64_tr_b16 v[96:97], v129 offset:0\n\t"
        "ds_read_b64_tr_b16 v[98:99], v129 offset:3072\n\t"
        "s_waitcnt lgkmcnt(10)\n\t"
        "v_mfma_f32_32x32x16_bf16 v[16:31], v[100:103], v[152:155], v[16:31]\n\t"
        "v_add_u32 v128, 1, v128\n\t"
        "ds_read_b64_tr_b16 v[100:101], v130 offset:0\n\t"
        "ds_read_b64_tr_b16 v[102:103], v130 offset:3072\n\t"
        "s_waitcnt lgkmcnt(10)\n\t"
        "v_mfma_f32_32x32x16_bf16 v[32:47], v[104:107], v[152:155], v[32:47]\n\t"
        "ds_read_b64_tr_b16 v[104:105], v129 offset:128\n\t"
        "ds_read_b64_tr_b16 v[106:107], v129 offset:3200\n\t"
        "s_waitcnt lgkmcnt(10)\n\t"
        "v_mfma_f32_32x32x16_bf16 v[48:63], v[108:111], v[152:155], v[48:63]\n\t"
        "s_add_u32 s46, s46, 12288\n\t"
        "s_add_u32 s47, s47, 1\n\t"
        "ds_read_b64_tr_b16 v[108:109], v130 offset:128\n\t"
        "ds_read_b64_tr_b16 v[110:111], v130 offset:3200\n\t"
        "s_waitcnt lgkmcnt(10)\n\t"
        "v_mfma_f32_32x32x16_bf16 v[64:79], v[152:155], v[112:115], v[64:79]\n\t"
        "s_cmp_eq_u32 s46, 110592\n\t"
        "s_cselect_b32 s46, 0, s46\n\t"
        "ds_read_b64_tr_b16 v[112:113], v129 offset:256\n\t"
        "ds_read_b64_tr_b16 v[114:115], v129 offset:3328\n\t"
        "s_waitcnt lgkmcnt(10)\n\t"
        "v_mfma_f32_32x32x16_bf16 v[80:95], v[152:155], v[116:119], v[80:95]\n\t"
        "ds_write_b32 v127, v128\n\t"
        "ds_read_b64_tr_b16 v[116:117], v130 offset:256\n\t"
        "ds_read_b64_tr_b16 v[118:119], v130 offset:3328\n\t"
        "s_waitcnt vmcnt(12) lgkmcnt(11)\n\t"
        "v_mfma_f32_32x32x16_bf16 v[0:15], v[96:99], v[156:159], v[0:15]\n\t"
        "ds_read_b32 v126, v125\n\t"
        "ds_read_b64_tr_b16 v[96:97], v129 offset:6144\n\t"
        "ds_read_b64_tr_b16 v[98:99], v129 offset:9216\n\t"
        "s_waitcnt lgkmcnt(12)\n\t"
        "v_mfma_f32_32x32x16_bf16 v[16:31], v[100:103], v[156:159], v[16:31]\n\t"
        "v_add_u32 v123, s46, v121\n\t"
        "ds_read_b64_tr_b16 v[100:101], v130 offset:6144\n\t"
        "ds_read_b64_tr_b16 v[102:103], v130 offset:9216\n\t"
        "s_waitcnt lgkmcnt(12)\n\t"
        "v_mfma_f32_32x32x16_bf16 v[32:47], v[104:107], v[156:159], v[32:47]\n\t"
        "ds_read_b64_tr_b16 v[104:105], v129 offset:6272\n\t"
        "ds_read_b64_tr_b16 v[106:107], v129 offset:9344\n\t"
        "s_waitcnt lgkmcnt(12)\n\t"
        "v_mfma_f32_32x32x16_bf16 v[48:63], v[108:111], v[156:159], v[48:63]\n\t"
        "v_add_u32 v124, s46, v122\n\t"
        "ds_read_b64_tr_b16 v[108:109], v130 offset:6272\n\t"
        "ds_read_b64_tr_b16 v[110:111], v130 offset:9344\n\t"
        "s_waitcnt lgkmcnt(12)\n\t"
        "v_mfma_f32_32x32x16_bf16 v[64:79], v[156:159], v[112:115], v[64:79]\n\t"
        "ds_read_b64_tr_b16 v[112:113], v129 offset:6400\n\t"
        "ds_read_b64_tr_b16 v[114:115], v129 offset:9472\n\t"
        "s_waitcnt lgkmcnt(10)\n\t"
        "v_mfma_f32_32x32x16_bf16 v[80:95], v[156:159], v[116:119], v[80:95]\n\t"
        "v_cmp_gt_u32 vcc, s47, v126\n\t"
        "ds_read_b64_tr_b16 v[116:117], v130 offset:6400\n\t"
        "ds_read_b64_tr_b16 v[118:119], v130 offset:9472\n\t"
        "s_waitcnt vmcnt(11) lgkmcnt(10)\n\t"
        "v_mfma_f32_32x32x16_bf16 v[0:15], v[96:99], v[160:163], v[0:15]\n\t"
        "s_cbranch_vccnz 66f\n\t"
        "67:\n\t"
        "ds_read_b64_tr_b16 v[96:97], v123 offset:0\n\t"
        "ds_read_b64_tr_b16 v[98:99], v123 offset:3072\n\t"
        "s_waitcnt lgkmcnt(10)\n\t"
        "v_mfma_f32_32x32x16_bf16 v[16:31], v[100:103], v[160:163], v[16:31]\n\t"
        "v_add_u32 v128, 1, v128\n\t"
        "ds_read_b64_tr_b16 v[100:101], v124 offset:0\n\t"
        "ds_read_b64_tr_b16 v[102:103], v124 offset:3072\n\t"
        "s_waitcnt lgkmcnt(10)\n\t"
        "v_mfma_f32_32x32x16_bf16 v[32:47], v[104:107], v[160:163], v[32:47]\n\t"
        "ds_read_b64_tr_b16 v[104:105], v123 offset:128\n\t"
        "ds_read_b64_tr_b16 v[106:107], v123 offset:3200\n\t"
        "s_waitcnt lgkmcnt(10)\n\t"
        "v_mfma_f32_32x32x16_bf16 v[48:63], v[108:111], v[160:163], v[48:63]\n\t"
        "s_add_u32 s46, s46, 12288\n\t"
        "s_add_u32 s47, s47, 1\n\t"
        "ds_read_b64_tr_b16 v[108:109], v124 offset:128\n\t"
        "ds_read_b64_tr_b16 v[110:111], v124 offset:3200\n\t"
        "s_waitcnt lgkmcnt(10)\n\t"
        "v_mfma_f32_32x32x16_bf16 v[64:79], v[160:163], v[112:115], v[64:79]\n\t"
        "s_cmp_eq_u32 s46, 110592\n\t"
        "s_cselect_b32 s46, 0, s46\n\t"
        "ds_read_b64_tr_b16 v[112:113], v123 offset:256\n\t"
        "ds_read_b64_tr_b16 v[114:115], v123 offset:3328\n\t"
        "s_waitcnt lgkmcnt(10)\n\t"
        "v_mfma_f32_32x32x16_bf16 v[80:95], v[160:163], v[116:119], v[80:95]\n\t"
        "ds_write_b32 v127, v128\n\t"
        "ds_read_b64_tr_b16 v[116:117], v124 offset:256\n\t"
        "ds_read_b64_tr_b16 v[118:119], v124 offset:3328\n\t"
        "s_waitcnt vmcnt(10) lgkmcnt(11)\n\t"
        "v_mfma_f32_32x32x16_bf16 v[0:15], v[96:99], v[164:167], v[0:15]\n\t"
        "ds_read_b32 v126, v125\n\t"
        "ds_read_b64_tr_b16 v[96:97], v123 offset:6144\n\t"
        "ds_read_b64_tr_b16 v[98:99], v123 offset:9216\n\t"
        "s_waitcnt lgkmcnt(12)\n\t"
        "v_mfma_f32_32x32x16_bf16 v[16:31], v[100:103], v[164:167], v[16:31]\n\t"
        "v_add_u32 v129, s46, v121\n\t"
        "ds_read_b64_tr_b16 v[100:101], v124 offset:6144\n\t"
        "ds_read_b64_tr_b16 v[102:103], v124 offset:9216\n\t"
        "s_waitcnt lgkmcnt(12)\n\t"
        "v_mfma_f32_32x32x16_bf16 v[32:47], v[104:107], v[164:167], v[32:47]\n\t"
        "ds_read_b64_tr_b16 v[104:105], v123 offset:6272\n\t"
        "ds_read_b64_tr_b16 v[106:107], v123 offset:9344\n\t"
        "s_waitcnt lgkmcnt(12)\n\t"
        "v_mfma_f32_32x32x16_bf16 v[48:63], v[108:111], v[164:167], v[48:63]\n\t"
        "v_add_u32 v130, s46, v122\n\t"
        "ds_read_b64_tr_b16 v[108:109], v124 offset:6272\n\t"
        "ds_read_b64_tr_b16 v[110:111], v124 offset:9344\n\t"
        "s_waitcnt lgkmcnt(12)\n\t"
        "v_mfma_f32_32x32x16_bf16 v[64:79], v[164:167], v[112:115], v[64:79]\n\t"
        "ds_read_b64_tr_b16 v[112:113], v123 offset:6400\n\t"
        "ds_read_b64_tr_b16 v[114:115], v123 offset:9472\n\t"
        "s_waitcnt lgkmcnt(10)\n\t"
        "v_mfma_f32_32x32x16_bf16 v[80:95], v[164:167], v[116:119], v[80:95]\n\t"
        "v_cmp_gt_u32 vcc, s47, v126\n\t"
        "ds_read_b64_tr_b16 v[116:117], v124 offset:6400\n\t"
        "ds_read_b64_tr_b16 v[118:119], v124 offset:9472\n\t"
        "s_waitcnt vmcnt(9) lgkmcnt(10)\n\t"
        "v_mfma_f32_32x32x16_bf16 v[0:15], v[96:99], v[168:171], v[0:15]\n\t"
        "s_cbranch_vccnz 68f\n\t"
        "69:\n\t"
        "ds_read_b64_tr_b16 v[96:97], v129 offset:0\n\t"
        "ds_read_b64_tr_b16 v[98:99], v129 offset:3072\n\t"
        "s_waitcnt lgkmcnt(10)\n\t"
        "v_mfma_f32_32x32x16_bf16 v[16:31], v[100:103], v[168:171], v[16:31]\n\t"
        "v_add_u32 v128, 1, v128\n\t"
        "ds_read_b64_tr_b16 v[100:101], v130 offset:0\n\t"
        "ds_read_b64_tr_b16 v[102:103], v130 offset:3072\n\t"
        "s_waitcnt lgkmcnt(10)\n\t"
        "v_mfma_f32_32x32x16_bf16 v[32:47], v[104:107], v[168:171], v[32:47]\n\t"
        "ds_read_b64_tr_b16 v[104:105], v129 offset:128\n\t"
        "ds_read_b64_tr_b16 v[106:107], v129 offset:3200\n\t"
        "s_waitcnt lgkmcnt(10)\n\t"
        "v_mfma_f32_32x32x16_bf16 v[48:63], v[108:111], v[168:171], v[48:63]\n\t"
        "s_add_u32 s46, s46, 12288\n\t"
        "s_add_u32 s47, s47, 1\n\t"
        "ds_read_b64_tr_b16 v[108:109], v130 offset:128\n\t"
        "ds_read_b64_tr_b16 v[110:111], v130 offset:3200\n\t"
        "s_waitcnt lgkmcnt(10)\n\t"
        "v_mfma_f32_32x32x16_bf16 v[64:79], v[168:171], v[112:115], v[64:79]\n\t"
        "s_cmp_eq_u32 s46, 110592\n\t"
        "s_cselect_b32 s46, 0, s46\n\t"
        "ds_read_b64_tr_b16 v[112:113], v129 offset:256\n\t"
        "ds_read_b64_tr_b16 v[114:115], v129 offset:3328\n\t"
        "s_waitcnt lgkmcnt(10)\n\t"
        "v_mfma_f32_32x32x16_bf16 v[80:95], v[168:171], v[116:119], v[80:95]\n\t"
        "ds_write_b32 v127, v128\n\t"
        "ds_read_b64_tr_b16 v[116:117], v130 offset:256\n\t"
        "ds_read_b64_tr_b16 v[118:119], v130 offset:3328\n\t"
        "s_waitcnt vmcnt(8) lgkmcnt(11)\n\t"
        "v_mfma_f32_32x32x16_bf16 v[0:15], v[96:99], v[172:175], v[0:15]\n\t"
        "ds_read_b32 v126, v125\n\t"
        "ds_read_b64_tr_b16 v[96:97], v129 offset:6144\n\t"
        "ds_read_b64_tr_b16 v[98:99], v129 offset:9216\n\t"
        "s_waitcnt lgkmcnt(12)\n\t"
        "v_mfma_f32_32x32x16_bf16 v[16:31], v[100:103], v[172:175], v[16:31]\n\t"
        "v_add_u32 v123, s46, v121\n\t"
        "ds_read_b64_tr_b16 v[100:101], v130 offset:6144\n\t"
        "ds_read_b64_tr_b16 v[102:103], v130 offset:9216\n\t"
        "s_waitcnt lgkmcnt(12)\n\t"
        "v_mfma_f32_32x32x16_bf16 v[32:47], v[104:107], v[172:175], v[32:47]\n\t"
        "ds_read_b64_tr_b16 v[104:105], v129 offset:6272\n\t"
        "ds_read_b64_tr_b16 v[106:107], v129 offset:9344\n\t"
        "s_waitcnt lgkmcnt(12)\n\t"
        "v_mfma_f32_32x32x16_bf16 v[48:63], v[108:111], v[172:175], v[48:63]\n\t"
        "v_add_u32 v124, s46, v122\n\t"
        "ds_read_b64_tr_b16 v[108:109], v130 offset:6272\n\t"
        "ds_read_b64_tr_b16 v[110:111], v130 offset:9344\n\t"
        "s_waitcnt lgkmcnt(12)\n\t"
        "v_mfma_f32_32x32x16_bf16 v[64:79], v[172:175], v[112:115], v[64:79]\n\t"
        "ds_read_b64_tr_b16 v[112:113], v129 offset:6400\n\t"
        "ds_read_b64_tr_b16 v[114:115], v129 offset:9472\n\t"
        "s_waitcnt lgkmcnt(10)\n\t"
        "v_mfma_f32_32x32x16_bf16 v[80:95], v[172:175], v[116:119], v[80:95]\n\t"
        "v_cmp_gt_u32 vcc, s47, v126\n\t"
        "ds_read_b64_tr_b16 v[116:117], v130 offset:6400\n\t"
        "ds_read_b64_tr_b16 v[118:119], v130 offset:9472\n\t"
        "s_waitcnt vmcnt(7) lgkmcnt(10)\n\t"
        "v_mfma_f32_32x32x16_bf16 v[0:15], v[96:99], v[176:179], v[0:15]\n\t"
        "s_cbranch_vccnz 70f\n\t"
        "71:\n\t"
        "ds_read_b64_tr_b16 v[96:97], v123 offset:0\n\t"
        "ds_read_b64_tr_b16 v[98:99], v123 offset:3072\n\t"
        "s_waitcnt lgkmcnt(10)\n\t"
        "v_mfma_f32_32x32x16_bf16 v[16:31], v[100:103], v[176:179], v[16:31]\n\t"
        "v_add_u32 v128, 1, v128\n\t"
        "ds_read_b64_tr_b16 v[100:101], v124 offset:0\n\t"
        "ds_read_b64_tr_b16 v[102:103], v124 offset:3072\n\t"
        "s_waitcnt lgkmcnt(10)\n\t"
        "v_mfma_f32_32x32x16_bf16 v[32:47], v[104:107], v[176:179], v[32:47]\n\t"
        "ds_read_b64_tr_b16 v[104:105], v123 offset:128\n\t"
        "ds_read_b64_tr_b16 v[106:107], v123 offset:3200\n\t"
        "s_waitcnt lgkmcnt(10)\n\t"
        "v_mfma_f32_32x32x16_bf16 v[48:63], v[108:111], v[176:179], v[48:63]\n\t"
        "s_add_u32 s46, s46, 12288\n\t"
        "s_add_u32 s47, s47, 1\n\t"
        "ds_read_b64_tr_b16 v[108:109], v124 offset:128\n\t"
        "ds_read_b64_tr_b16 v[110:111], v124 offset:3200\n\t"
        "s_waitcnt lgkmcnt(10)\n\t"
        "v_mfma_f32_32x32x16_bf16 v[64:79], v[176:179], v[112:115], v[64:79]\n\t"
        "s_cmp_eq_u32 s46, 110592\n\t"
        "s_cselect_b32 s46, 0, s46\n\t"
        "ds_read_b64_tr_b16 v[112:113], v123 offset:256\n\t"
        "ds_read_b64_tr_b16 v[114:115], v123 offset:3328\n\t"
        "s_waitcnt lgkmcnt(10)\n\t"
        "v_mfma_f32_32x32x16_bf16 v[80:95], v[176:179], v[116:119], v[80:95]\n\t"
        "ds_write_b32 v127, v128\n\t"
        "ds_read_b64_tr_b16 v[116:117], v124 offset:256\n\t"
        "ds_read_b64_tr_b16 v[118:119], v124 offset:3328\n\t"
        "s_waitcnt vmcnt(6) lgkmcnt(11)\n\t"
        "v_mfma_f32_32x32x16_bf16 v[0:15], v[96:99], v[180:183], v[0:15]\n\t"
        "ds_read_b32 v126, v125\n\t"
        "ds_read_b64_tr_b16 v[96:97], v123 offset:6144\n\t"
        "ds_read_b64_tr_b16 v[98:99], v123 offset:9216\n\t"
        "s_waitcnt lgkmcnt(12)\n\t"
        "v_mfma_f32_32x32x16_bf16 v[16:31], v[100:103], v[180:183], v[16:31]\n\t"
        "v_add_u32 v129, s46, v121\n\t"
        "ds_read_b64_tr_b16 v[100:101], v124 offset:6144\n\t"
        "ds_read_b64_tr_b16 v[102:103], v124 offset:9216\n\t"
        "s_waitcnt lgkmcnt(12)\n\t"
        "v_mfma_f32_32x32x16_bf16 v[32:47], v[104:107], v[180:183], v[32:47]\n\t"
        "ds_read_b64_tr_b16 v[104:105], v123 offset:6272\n\t"
        "ds_read_b64_tr_b16 v[106:107], v123 offset:9344\n\t"
        "s_waitcnt lgkmcnt(12)\n\t"
        "v_mfma_f32_32x32x16_bf16 v[48:63], v[108:111], v[180:183], v[48:63]\n\t"
        "v_add_u32 v130, s46, v122\n\t"
        "ds_read_b64_tr_b16 v[108:109], v124 offset:6272\n\t"
        "ds_read_b64_tr_b16 v[110:111], v124 offset:9344\n\t"
        "s_waitcnt lgkmcnt(12)\n\t"
        "v_mfma_f32_32x32x16_bf16 v[64:79], v[180:183], v[112:115], v[64:79]\n\t"
        "ds_read_b64_tr_b16 v[112:113], v123 offset:6400\n\t"
        "ds_read_b64_tr_b16 v[114:115], v123 offset:9472\n\t"
        "s_waitcnt lgkmcnt(10)\n\t"
        "v_mfma_f32_32x32x16_bf16 v[80:95], v[180:183], v[116:119], v[80:95]\n\t"
        "v_cmp_gt_u32 vcc, s47, v126\n\t"
        "ds_read_b64_tr_b16 v[116:117], v124 offset:6400\n\t"
        "ds_read_b64_tr_b16 v[118:119], v124 offset:9472\n\t"
        "s_waitcnt vmcnt(5) lgkmcnt(10)\n\t"
        "v_mfma_f32_32x32x16_bf16 v[0:15], v[96:99], v[184:187], v[0:15]\n\t"
        "s_cbranch_vccnz 72f\n\t"
        "73:\n\t"
        "ds_read_b64_tr_b16 v[96:97], v129 offset:0\n\t"
        "ds_read_b64_tr_b16 v[98:99], v129 offset:3072\n\t"
        "s_waitcnt lgkmcnt(10)\n\t"
        "v_mfma_f32_32x32x16_bf16 v[16:31], v[100:103], v[184:187], v[16:31]\n\t"
        "v_add_u32 v128, 1, v128\n\t"
        "ds_read_b64_tr_b16 v[100:101], v130 offset:0\n\t"
        "ds_read_b64_tr_b16 v[102:103], v130 offset:3072\n\t"
        "s_waitcnt lgkmcnt(10)\n\t"
        "v_mfma_f32_32x32x16_bf16 v[32:47], v[104:107], v[184:187], v[32:47]\n\t"
        "ds_read_b64_tr_b16 v[104:105], v129 offset:128\n\t"
        "ds_read_b64_tr_b16 v[106:107], v129 offset:3200\n\t"
        "s_waitcnt lgkmcnt(10)\n\t"
        "v_mfma_f32_32x32x16_bf16 v[48:63], v[108:111], v[184:187], v[48:63]\n\t"
        "s_add_u32 s46, s46, 12288\n\t"
        "s_add_u32 s47, s47, 1\n\t"
        "ds_read_b64_tr_b16 v[108:109], v130 offset:128\n\t"
        "ds_read_b64_tr_b16 v[110:111], v130 offset:3200\n\t"
        "s_waitcnt lgkmcnt(10)\n\t"
        "v_mfma_f32_32x32x16_bf16 v[64:79], v[184:187], v[112:115], v[64:79]\n\t"
        "s_cmp_eq_u32 s46, 110592\n\t"
        "s_cselect_b32 s46, 0, s46\n\t"
        "ds_read_b64_tr_b16 v[112:113], v129 offset:256\n\t"
        "ds_read_b64_tr_b16 v[114:115], v129 offset:3328\n\t"
        "s_waitcnt lgkmcnt(10)\n\t"
        "v_mfma_f32_32x32x16_bf16 v[80:95], v[184:187], v[116:119], v[80:95]\n\t"
        "ds_write_b32 v127, v128\n\t"
        "ds_read_b64_tr_b16 v[116:117], v130 offset:256\n\t"
        "ds_read_b64_tr_b16 v[118:119], v130 offset:3328\n\t"
        "s_waitcnt vmcnt(4) lgkmcnt(11)\n\t"
        "v_mfma_f32_32x32x16_bf16 v[0:15], v[96:99], v[188:191], v[0:15]\n\t"
        "ds_read_b64_tr_b16 v[96:97], v129 offset:6144\n\t"
        "ds_read_b64_tr_b16 v[98:99], v129 offset:9216\n\t"
        "s_waitcnt lgkmcnt(11)\n\t"
        "v_mfma_f32_32x32x16_bf16 v[16:31], v[100:103], v[188:191], v[16:31]\n\t"
        "ds_read_b64_tr_b16 v[100:101], v130 offset:6144\n\t"
        "ds_read_b64_tr_b16 v[102:103], v130 offset:9216\n\t"
        "s_waitcnt lgkmcnt(11)\n\t"
        "v_mfma_f32_32x32x16_bf16 v[32:47], v[104:107], v[188:191], v[32:47]\n\t"
        "ds_read_b64_tr_b16 v[104:105], v129 offset:6272\n\t"
        "ds_read_b64_tr_b16 v[106:107], v129 offset:9344\n\t"
        "s_waitcnt lgkmcnt(11)\n\t"
        "v_mfma_f32_32x32x16_bf16 v[48:63], v[108:111], v[188:191], v[48:63]\n\t"
        "ds_read_b64_tr_b16 v[108:109], v130 offset:6272\n\t"
        "ds_read_b64_tr_b16 v[110:111], v130 offset:9344\n\t"
        "s_waitcnt lgkmcnt(11)\n\t"
        "v_mfma_f32_32x32x16_bf16 v[64:79], v[188:191], v[112:115], v[64:79]\n\t"
        "ds_read_b64_tr_b16 v[112:113], v129 offset:6400\n\t"
        "ds_read_b64_tr_b16 v[114:115], v129 offset:9472\n\t"
        "s_waitcnt lgkmcnt(10)\n\t"
        "v_mfma_f32_32x32x16_bf16 v[80:95], v[188:191], v[116:119], v[80:95]\n\t"
        "ds_read_b64_tr_b16 v[116:117], v130 offset:6400\n\t"
        "ds_read_b64_tr_b16 v[118:119], v130 offset:9472\n\t"
        "s_waitcnt vmcnt(3) lgkmcnt(10)\n\t"
        "v_mfma_f32_32x32x16_bf16 v[0:15], v[96:99], v[192:195], v[0:15]\n\t"
        "s_waitcnt lgkmcnt(8)\n\t"
        "v_mfma_f32_32x32x16_bf16 v[16:31], v[100:103], v[192:195], v[16:31]\n\t"
        "s_waitcnt lgkmcnt(6)\n\t"
        "v_mfma_f32_32x32x16_bf16 v[32:47], v[104:107], v[192:195], v[32:47]\n\t"
        "s_waitcnt lgkmcnt(4)\n\t"
        "v_mfma_f32_32x32x16_bf16 v[48:63], v[108:111], v[192:195], v[48:63]\n\t"
        "s_waitcnt lgkmcnt(2)\n\t"
        "v_mfma_f32_32x32x16_bf16 v[64:79], v[192:195], v[112:115], v[64:79]\n\t"
        "s_waitcnt lgkmcnt(0)\n\t"
        "v_mfma_f32_32x32x16_bf16 v[80:95], v[192:195], v[116:119], v[80:95]\n\t"
        "s_waitcnt vmcnt(0)\n\t"
        "s_nop 10\n\t"
        "s_branch 9f\n\t"
        "16:\n\t"
        "s_mov_b32 s48, 0x10000\n\t"
        "8:\n\t"
        "ds_read_b32 v126, v125\n\t"
        "s_sub_u32 s48, s48, 1\n\t"
        "s_waitcnt lgkmcnt(0)\n\t"
        "s_cbranch_scc1 17b\n\t"
        "v_cmp_gt_u32 vcc, s47, v126\n\t"
        "s_cbranch_vccnz 8b\n\t"
        "s_branch 17b\n\t"
        "18:\n\t"
        "s_mov_b32 s48, 0x10000\n\t"
        "8:\n\t"
        "ds_read_b32 v126, v125\n\t"
        "s_sub_u32 s48, s48, 1\n\t"
        "s_waitcnt lgkmcnt(0)\n\t"
        "s_cbranch_scc1 19b\n\t"
        "v_cmp_gt_u32 vcc, s47, v126\n\t"
        "s_cbranch_vccnz 8b\n\t"
        "s_branch 19b\n\t"
        "20:\n\t"
        "s_mov_b32 s48, 0x10000\n\t"
        "8:\n\t"
        "ds_read_b32 v126, v125\n\t"
        "s_sub_u32 s48, s48, 1\n\t"
        "s_waitcnt lgkmcnt(0)\n\t"
        "s_cbranch_scc1 21b\n\t"
        "v_cmp_gt_u32 vcc, s47, v126\n\t"
        "s_cbranch_vccnz 8b\n\t"
        "s_branch 21b\n\t"
        "22:\n\t"
        "s_mov_b32 s48, 0x10000\n\t"
        "8:\n\t"
        "ds_read_b32 v126, v125\n\t"
        "s_sub_u32 s48, s48, 1\n\t"
        "s_waitcnt lgkmcnt(0)\n\t"
        "s_cbranch_scc1 23b\n\t"
        "v_cmp_gt_u32 vcc, s47, v126\n\t"
        "s_cbranch_vccnz 8b\n\t"
        "s_branch 23b\n\t"
        "24:\n\t"
        "s_mov_b32 s48, 0x10000\n\t"
        "8:\n\t"
        "ds_read_b32 v126, v125\n\t"
        "s_sub_u32 s48, s48, 1\n\t"
        "s_waitcnt lgkmcnt(0)\n\t"
        "s_cbranch_scc1 25b\n\t"
        "v_cmp_gt_u32 vcc, s47, v126\n\t"
        "s_cbranch_vccnz 8b\n\t"
        "s_branch 25b\n\t"
        "26:\n\t"
        "s_mov_b32 s48, 0x10000\n\t"
        "8:\n\t"
        "ds_read_b32 v126, v125\n\t"
        "s_sub_u32 s48, s48, 1\n\t"
        "s_waitcnt lgkmcnt(0)\n\t"
        "s_cbranch_scc1 27b\n\t"
        "v_cmp_gt_u32 vcc, s47, v126\n\t"
        "s_cbranch_vccnz 8b\n\t"
        "s_branch 27b\n\t"
        "28:\n\t"
        "s_mov_b32 s48, 0x10000\n\t"
        "8:\n\t"
        "ds_read_b32 v126, v125\n\t"
        "s_sub_u32 s48, s48, 1\n\t"
        "s_waitcnt lgkmcnt(0)\n\t"
        "s_cbranch_scc1 29b\n\t"
        "v_cmp_gt_u32 vcc, s47, v126\n\t"
        "s_cbranch_vccnz 8b\n\t"
        "s_branch 29b\n\t"
        "30:\n\t"
        "s_mov_b32 s48, 0x10000\n\t"
        "8:\n\t"
        "ds_read_b32 v126, v125\n\t"
        "s_sub_u32 s48, s48, 1\n\t"
        "s_waitcnt lgkmcnt(0)\n\t"
        "s_cbranch_scc1 31b\n\t"
        "v_cmp_gt_u32 vcc, s47, v126\n\t"
        "s_cbranch_vccnz 8b\n\t"
        "s_branch 31b\n\t"
        "64:\n\t"
        "s_mov_b32 s48, 0x10000\n\t"
        "8:\n\t"
        "ds_read_b32 v126, v125\n\t"
        "s_sub_u32 s48, s48, 1\n\t"
        "s_waitcnt lgkmcnt(0)\n\t"
        "s_cbranch_scc1 65b\n\t"
        "v_cmp_gt_u32 vcc, s47, v126\n\t"
        "s_cbranch_vccnz 8b\n\t"
        "s_branch 65b\n\t"
        "66:\n\t"
        "s_mov_b32 s48, 0x10000\n\t"
        "8:\n\t"
        "ds_read_b32 v126, v125\n\t"
        "s_sub_u32 s48, s48, 1\n\t"
        "s_waitcnt lgkmcnt(0)\n\t"
        "s_cbranch_scc1 67b\n\t"
        "v_cmp_gt_u32 vcc, s47, v126\n\t"
        "s_cbranch_vccnz 8b\n\t"
        "s_branch 67b\n\t"
        "68:\n\t"
        "s_mov_b32 s48, 0x10000\n\t"
        "8:\n\t"
        "ds_read_b32 v126, v125\n\t"
        "s_sub_u32 s48, s48, 1\n\t"
        "s_waitcnt lgkmcnt(0)\n\t"
        "s_cbranch_scc1 69b\n\t"
        "v_cmp_gt_u32 vcc, s47, v126\n\t"
        "s_cbranch_vccnz 8b\n\t"
        "s_branch 69b\n\t"
        "70:\n\t"
        "s_mov_b32 s48, 0x10000\n\t"
        "8:\n\t"
        "ds_read_b32 v126, v125\n\t"
        "s_sub_u32 s48, s48, 1\n\t"
        "s_waitcnt lgkmcnt(0)\n\t"
        "s_cbranch_scc1 71b\n\t"
        "v_cmp_gt_u32 vcc, s47, v126\n\t"
        "s_cbranch_vccnz 8b\n\t"
        "s_branch 71b\n\t"
        "72:\n\t"
        "s_mov_b32 s48, 0x10000\n\t"
        "8:\n\t"
        "ds_read_b32 v126, v125\n\t"
        "s_sub_u32 s48, s48, 1\n\t"
        "s_waitcnt lgkmcnt(0)\n\t"
        "s_cbranch_scc1 73b\n\t"
        "v_cmp_gt_u32 vcc, s47, v126\n\t"
        "s_cbranch_vccnz 8b\n\t"
        "s_branch 73b\n\t"
        "9:\n\t"
        : "+{v[0:15]}"(acc[0]), "+{v[16:31]}"(acc[1]), "+{v[32:47]}"(acc[2]), "+{v[48:63]}"(acc[3]), "+{v[64:79]}"(acc[4]), "+{v[80:95]}"(acc[5]),
          "+{v[96:111]}"(stA), "+{v[112:127]}"(stB), "+{v[128:131]}"(stC), "+{v[148:163]}"(xr1), "+{v[164:179]}"(xr2), "+{v[180:195]}"(xr3),
          "+{s44}"(soff), "+{s46}"(slot), "+{s47}"(need)
        : "{s[40:43]}"(srd) : "v132", "v133", "v134", "v135", "v136", "v137", "v138", "v139", "v140", "v141", "v142", "v143", "v144", "v145", "v146", "v147", "s45", "s48", "vcc", "scc", "memory");
      STAMPV(4 + 16 * L);
      PHASE_VARS;
      if (L == 1) {
        f32x4 av[4], bv[4], cs[4][4];
        float cv[2];
#pragma unroll
        for (int g = 0; g < 4; ++g) {
          av[g] = *reinterpret_cast<const f32x4*>(smem + L_AB + wid * 256 + (8 * g + 4 * hi) * 4);
          bv[g] = *reinterpret_cast<const f32x4*>(smem + L_AB + wid * 256 + 128 + (8 * g + 4 * hi) * 4);
        }
#pragma unroll
        for (int j = 5; j >= 0; --j) {
          const int cb = (j >> 1) * 64 + ((j & 1) ^ hh) * 32;
          if (j >= 4) cv[j - 4] = *reinterpret_cast<const float*>(smem + L_CS + (cb + r32) * 4);
          else {
#pragma unroll
            for (int g = 0; g < 4; ++g) cs[j][g] = *reinterpret_cast<const f32x4*>(smem + L_CS + (cb + 8 * g + 4 * hi) * 4);
          }
        }
        SBAR();
#pragma unroll
        for (int j = 5; j >= 0; --j)
#pragma unroll
          for (int g = 0; g < 4; ++g)
#pragma unroll
            for (int e = 0; e < 4; ++e)
              acc[j][4 * g + e] = j < 4 ? al * acc[j][4 * g + e] + be * cs[j & 3][g][e] : av[g][e] * acc[j][4 * g + e] + bv[g][e] * cv[j & 1];
        SBAR();
        lds_wait_ge(lbase + L_W + 4 + 4 * (lane & 1), 1);
        const int idx = (int)__builtin_amdgcn_readfirstlane(words[3]);
        const bool mine = (idx >> 5) == tile;
        if (mine) {
#pragma unroll
          for (int j = 0; j < 6; ++j) {
            const int cb = (j >> 1) * 64 + ((j & 1) ^ hh) * 32;
            if (j >= 4) cv[j - 4] = *reinterpret_cast<const float*>(smem + L_PATCH + (cb + r32) * 4);
            else {
#pragma unroll
              for (int g = 0; g < 4; ++g) cs[j][g] = *reinterpret_cast<const f32x4*>(smem + L_PATCH + (cb + 8 * g + 4 * hi) * 4);
            }
          }
          SBAR();
          const int rel = (idx & 31) - 4 * hi;
#pragma unroll
          for (int j = 0; j < 6; ++j)
#pragma unroll
            for (int e = 0; e < 16; ++e)
              acc[j][e] = j < 4 ? (r32 == (idx & 31) ? cs[j & 3][e >> 2][e & 3] : acc[j][e]) : (rel == 8 * (e >> 2) + (e & 3) ? cv[j & 1] : acc[j][e]);
        }
      }
      constexpr float QS = 0.17677669529663687f * 1.4426950408889634f;
      u32x4 own[4];
#pragma unroll
      for (int j = 5; j >= 0; --j) {
        u32x4 o[2];
#pragma unroll
        for (int e = 0; e < 8; ++e) {
          unsigned w = pack2(acc[j][2 * e], acc[j][2 * e + 1]);
          if (j < 2) w = pack2(lo16(w) * QS, hi16(w) * QS);
          o[e >> 2][e & 3] = w;
        }
        if (j >= 4) { o[0] = fo_swap(o[0]); o[1] = fo_swap(o[1]); }
        const int hs = (j & 1) ^ hh;
        if (j >= 2) {
          const unsigned dst = OFF_KV + L * (10 * MBY) + (tn * 2 + hs) * HS + tile * 4096 + ((j >> 1) - 1) * 2048 + lane * 16;
          stg(rs, dst, o[0]); stg(rs, dst + 1024, o[1]);
        }
        if (j >= 2 && !(j & 1)) { own[j - 2] = o[0]; own[j - 1] = o[1]; }
        else {
          char* p = smem + L_XCH + (j < 2 ? (hs * 4 + wid) * 2048 : 16384 + wid * 4096 + ((j >> 1) - 1) * 2048) + lane * 16;
          *reinterpret_cast<u32x4*>(p) = o[0]; *reinterpret_cast<u32x4*>(p + 1024) = o[1];
        }
        SBAR();
      }
      asm volatile(
        "v_mov_b64 v[0:1], 0\n\t"
        "v_mov_b64 v[2:3], 0\n\t"
        "v_mov_b64 v[4:5], 0\n\t"
        "v_mov_b64 v[6:7], 0\n\t"
        "v_mov_b64 v[8:9], 0\n\t"
        "v_mov_b64 v[10:11], 0\n\t"
        "v_mov_b64 v[12:13], 0\n\t"
        "v_mov_b64 v[14:15], 0\n\t"
        "v_mov_b64 v[16:17], 0\n\t"
        "v_mov_b64 v[18:19], 0\n\t"
        "v_mov_b64 v[20:21], 0\n\t"
        "v_mov_b64 v[22:23], 0\n\t"
        "v_mov_b64 v[24:25], 0\n\t"
        "v_mov_b64 v[26:27], 0\n\t"
        "v_mov_b64 v[28:29], 0\n\t"
        "v_mov_b64 v[30:31], 0\n\t"
        "v_mov_b64 v[32:33], 0\n\t"
        "v_mov_b64 v[34:35], 0\n\t"
        "v_mov_b64 v[36:37], 0\n\t"
        "v_mov_b64 v[38:39], 0\n\t"
        "v_mov_b64 v[40:41], 0\n\t"
        "v_mov_b64 v[42:43], 0\n\t"
        "v_mov_b64 v[44:45], 0\n\t"
        "v_mov_b64 v[46:47], 0\n\t"
        "v_mov_b64 v[48:49], 0\n\t"
        "v_mov_b64 v[50:51], 0\n\t"
        "v_mov_b64 v[52:53], 0\n\t"
        "v_mov_b64 v[54:55], 0\n\t"
        "v_mov_b64 v[56:57], 0\n\t"
        "v_mov_b64 v[58:59], 0\n\t"
        "v_mov_b64 v[60:61], 0\n\t"
        "v_mov_b64 v[62:63], 0\n\t"
        "v_mov_b64 v[128:129], 0\n\t"
        "v_mov_b64 v[130:131], 0\n\t"
        "v_mov_b64 v[132:133], 0\n\t"
        "v_mov_b64 v[134:135], 0\n\t"
        "v_mov_b64 v[136:137], 0\n\t"
        "v_mov_b64 v[138:139], 0\n\t"
        "v_mov_b64 v[140:141], 0\n\t"
        "v_mov_b64 v[142:143], 0\n\t"
        : "={v[0:15]}"(oz[0]), "={v[16:31]}"(oz[1]), "={v[32:47]}"(oz[2]), "={v[48:63]}"(oz[3]), "={v[128:143]}"(oz[4]));
      BAR_ALL();
      kva = cat4(own[0], own[1], own[2], own[3]);
      const u32x4* pk = reinterpret_cast<const u32x4*>(smem + L_XCH + 16384 + (wid ^ 1) * 4096 + lane * 16);
#pragma unroll
      for (int u = 0; u < 4; ++u) {
        const u32x4* p = reinterpret_cast<const u32x4*>(smem + L_XCH + (hh * 4 + ((2 * kh + 2 + u) & 3)) * 2048 + lane * 16);
        q[u] = __builtin_shufflevector(p[0], p[64], 0, 1, 2, 3, 4, 5, 6, 7);
      }
      kvb = cat4(pk[0], pk[64], pk[128], pk[192]);
    }
    STAMPV(5 + 16 * L);
    {
      u32x4 ones;
      unsigned kvoff;
      u32x8 fv, cv;
      asm volatile("" : "={v[184:191]}"(fv), "={v[216:223]}"(cv));
      { PHASE_VARS;
        const unsigned v = (lane & 3) == 0 ? 0x3F803F80u : 0u; const bool up = (lane & 4) != 0; ones[0] = ones[1] = up ? 0u : v; ones[2] = ones[3] = up ? v : 0u;
        kvoff = OFF_KV + L * (10 * MBY) + (tn * 2 + hh) * HS + kh * 8192 + lane * 16;
        fv[1] = lbase + L_FREE + wid * 256 + lane * 4; fv[2] = 32 * L + 32;
        cv[0] = lbase + L_XCH + wid * 8192 + lane * 16; cv[1] = lbase + L_XCH + (wid ^ 2) * 8192 + lane * 16;
        cv[2] = lbase + L_XCH + 32768 + wid * 512 + lane * 4; cv[3] = lbase + L_XCH + 32768 + (wid ^ 2) * 512 + lane * 4;
        cv[4] = lbase + L_W + 48 + wid * 4; cv[5] = lbase + L_W + 48 + (wid ^ 2) * 4; cv[6] = L + 1;
        cv[7] = ((tile * 32 + r32) * D_ + tn * 64 + hi * 8) * 2; }
      f32x16 o0 = oz[0], o1 = oz[1], o2 = oz[2], o3 = oz[3], lv = oz[4];
      unsigned soff = tm * 16384;
      STAMP_DECL;
      asm volatile(
        "s_nop 1\n\t"
        "v_mfma_f32_32x32x16_bf16 v[96:111], v[152:155], v[64:67], 0\n\t"
        "v_mfma_f32_32x32x16_bf16 v[96:111], v[156:159], v[68:71], v[96:111]\n\t"
        "s_mov_b32 s46, 0\n\t"
        "ds_write_b32 v185, v186\n\t"
        "s_nop 9\n\t"
        "v_exp_f32 v96, v96\n\t"
        "v_exp_f32 v97, v97\n\t"
        "v_exp_f32 v98, v98\n\t"
        "v_exp_f32 v99, v99\n\t"
        "v_exp_f32 v100, v100\n\t"
        "v_exp_f32 v101, v101\n\t"
        "v_mfma_f32_32x32x16_bf16 v[112:127], v[152:155], v[72:75], 0\n\t"
        "v_exp_f32 v102, v102\n\t"
        "v_exp_f32 v103, v103\n\t"
        "v_exp_f32 v104, v104\n\t"
        "v_mfma_f32_32x32x16_bf16 v[112:127], v[156:159], v[76:79], v[112:127]\n\t"
        "v_exp_f32 v105, v105\n\t"
        "v_exp_f32 v106, v106\n\t"
        "v_exp_f32 v107, v107\n\t"
        "v_smfmac_f32_32x32x32_bf16 v[0:15], v[160:163], v[96:103], v148\n\t"
        "v_exp_f32 v108, v108\n\t"
        "v_exp_f32 v109, v109\n\t"
        "v_exp_f32 v110, v110\n\t"
        "v_smfmac_f32_16x16x64_bf16 v[128:131], v[144:147], v[96:103], v148\n\t"
        "v_exp_f32 v111, v111\n\t"
        "v_exp_f32 v112, v112\n\t"
        "v_exp_f32 v113, v113\n\t"
        "v_smfmac_f32_32x32x32_bf16 v[0:15], v[164:167], v[104:111], v148\n\t"
        "v_exp_f32 v114, v114\n\t"
        "v_exp_f32 v115, v115\n\t"
        "v_exp_f32 v116, v116\n\t"
        "v_smfmac_f32_16x16x64_bf16 v[128:131], v[144:147], v[104:111], v148\n\t"
        "v_exp_f32 v117, v117\n\t"
        "v_mfma_f32_32x32x16_bf16 v[96:111], v[152:155], v[80:83], 0\n\t"
        "v_exp_f32 v118, v118\n\t"
        "v_exp_f32 v119, v119\n\t"
        "v_exp_f32 v120, v120\n\t"
        "v_mfma_f32_32x32x16_bf16 v[96:111], v[156:159], v[84:87], v[96:111]\n\t"
        "v_exp_f32 v121, v121\n\t"
        "v_exp_f32 v122, v122\n\t"
        "v_exp_f32 v123, v123\n\t"
        "v_smfmac_f32_32x32x32_bf16 v[16:31], v[160:163], v[112:119], v148\n\t"
        "v_exp_f32 v124, v124\n\t"
        "v_exp_f32 v125, v125\n\t"
        "v_exp_f32 v126, v126\n\t"
        "v_smfmac_f32_16x16x64_bf16 v[132:135], v[144:147], v[112:119], v148\n\t"
        "v_exp_f32 v127, v127\n\t"
        "v_exp_f32 v96, v96\n\t"
        "v_exp_f32 v97, v97\n\t"
        "v_smfmac_f32_32x32x32_bf16 v[16:31], v[164:167], v[120:127], v148\n\t"
        "v_exp_f32 v98, v98\n\t"
        "v_exp_f32 v99, v99\n\t"
        "v_exp_f32 v100, v100\n\t"
        "v_smfmac_f32_16x16x64_bf16 v[132:135], v[144:147], v[120:127], v148\n\t"
        "v_exp_f32 v101, v101\n\t"
        "v_mfma_f32_32x32x16_bf16 v[112:127], v[152:155], v[88:91], 0\n\t"
        "v_exp_f32 v102, v102\n\t"
        "v_exp_f32 v103, v103\n\t"
        "v_exp_f32 v104, v104\n\t"
        "v_mfma_f32_32x32x16_bf16 v[112:127], v[156:159], v[92:95], v[112:127]\n\t"
        "v_exp_f32 v105, v105\n\t"
        "v_exp_f32 v106, v106\n\t"
        "v_exp_f32 v107, v107\n\t"
        "v_smfmac_f32_32x32x32_bf16 v[32:47], v[160:163], v[96:103], v148\n\t"
        "v_exp_f32 v108, v108\n\t"
        "v_exp_f32 v109, v109\n\t"
        "v_exp_f32 v110, v110\n\t"
        "v_smfmac_f32_16x16x64_bf16 v[136:139], v[144:147], v[96:103], v148\n\t"
        "v_exp_f32 v111, v111\n\t"
        "v_exp_f32 v112, v112\n\t"
        "v_exp_f32 v113, v113\n\t"
        "v_smfmac_f32_32x32x32_bf16 v[32:47], v[164:167], v[104:111], v148\n\t"
        "v_exp_f32 v114, v114\n\t"
        "v_exp_f32 v115, v115\n\t"
        "v_exp_f32 v116, v116\n\t"
        "v_smfmac_f32_16x16x64_bf16 v[136:139], v[144:147], v[104:111], v148\n\t"
        "v_exp_f32 v117, v117\n\t"
        "v_mfma_f32_32x32x16_bf16 v[96:111], v[168:171], v[64:67], 0\n\t"
        "v_exp_f32 v118, v118\n\t"
        "v_exp_f32 v119, v119\n\t"
        "v_exp_f32 v120, v120\n\t"
        "v_mfma_f32_32x32x16_bf16 v[96:111], v[172:175], v[68:71], v[96:111]\n\t"
        "v_exp_f32 v121, v121\n\t"
        "v_exp_f32 v122, v122\n\t"
        "v_exp_f32 v123, v123\n\t"
        "v_smfmac_f32_32x32x32_bf16 v[48:63], v[160:163], v[112:119], v148\n\t"
        "v_exp_f32 v124, v124\n\t"
        "v_exp_f32 v125, v125\n\t"
        "v_exp_f32 v126, v126\n\t"
        "v_smfmac_f32_16x16x64_bf16 v[140:143], v[144:147], v[112:119], v148\n\t"
        "v_exp_f32 v127, v127\n\t"
        "v_exp_f32 v96, v96\n\t"
        "v_exp_f32 v97, v97\n\t"
        "v_smfmac_f32_32x32x32_bf16 v[48:63], v[164:167], v[120:127], v148\n\t"
        "v_exp_f32 v98, v98\n\t"
        "v_exp_f32 v99, v99\n\t"
        "v_exp_f32 v100, v100\n\t"
        "v_smfmac_f32_16x16x64_bf16 v[140:143], v[144:147], v[120:127], v148\n\t"
        "v_exp_f32 v101, v101\n\t"
        "v_mfma_f32_32x32x16_bf16 v[112:127], v[168:171], v[72:75], 0\n\t"
        "v_exp_f32 v102, v102\n\t"
        "v_exp_f32 v103, v103\n\t"
        "v_exp_f32 v104, v104\n\t"
        "ds_read_b32 v151, v224\n\t"
        "v_mfma_f32_32x32x16_bf16 v[112:127], v[172:175], v[76:79], v[112:127]\n\t"
        "v_exp_f32 v105, v105\n\t"
        "v_exp_f32 v106, v106\n\t"
        "v_exp_f32 v107, v107\n\t"
        "v_smfmac_f32_32x32x32_bf16 v[0:15], v[176:179], v[96:103], v148\n\t"
        "v_exp_f32 v108, v108\n\t"
        "v_exp_f32 v109, v109\n\t"
        "v_exp_f32 v110, v110\n\t"
        "v_smfmac_f32_16x16x64_bf16 v[128:131], v[144:147], v[96:103], v148\n\t"
        "v_exp_f32 v111, v111\n\t"
        "s_cmp_lg_u32 s46, 0\n\t"
        "s_cbranch_scc1 31f\n\t"
        "s_waitcnt lgkmcnt(0)\n\t"
        "v_cmp_ne_u32 vcc, v222, v151\n\t"
        "s_cbranch_vccz 30f\n\t"
        "31:\n\t"
        "v_exp_f32 v112, v112\n\t"
        "v_exp_f32 v113, v113\n\t"
        "v_smfmac_f32_32x32x32_bf16 v[0:15], v[180:183], v[104:111], v148\n\t"
        "v_exp_f32 v114, v114\n\t"
        "v_exp_f32 v115, v115\n\t"
        "v_exp_f32 v116, v116\n\t"
        "v_smfmac_f32_16x16x64_bf16 v[128:131], v[144:147], v[104:111], v148\n\t"
        "v_exp_f32 v117, v117\n\t"
        "v_mfma_f32_32x32x16_bf16 v[96:111], v[168:171], v[80:83], 0\n\t"
        "v_exp_f32 v118, v118\n\t"
        "v_exp_f32 v119, v119\n\t"
        "v_exp_f32 v120, v120\n\t"
        "ds_read_b32 v151, v224\n\t"
        "v_mfma_f32_32x32x16_bf16 v[96:111], v[172:175], v[84:87], v[96:111]\n\t"
        "v_exp_f32 v121, v121\n\t"
        "v_exp_f32 v122, v122\n\t"
        "v_exp_f32 v123, v123\n\t"
        "v_smfmac_f32_32x32x32_bf16 v[16:31], v[176:179], v[112:119], v148\n\t"
        "v_exp_f32 v124, v124\n\t"
        "v_exp_f32 v125, v125\n\t"
        "v_exp_f32 v126, v126\n\t"
        "v_smfmac_f32_16x16x64_bf16 v[132:135], v[144:147], v[112:119], v148\n\t"
        "v_exp_f32 v127, v127\n\t"
        "s_cmp_lg_u32 s46, 0\n\t"
        "s_cbranch_scc1 33f\n\t"
        "s_waitcnt lgkmcnt(0)\n\t"
        "v_cmp_ne_u32 vcc, v222, v151\n\t"
        "s_cbranch_vccz 32f\n\t"
        "33:\n\t"
        "v_exp_f32 v96, v96\n\t"
        "v_exp_f32 v97, v97\n\t"
        "v_smfmac_f32_32x32x32_bf16 v[16:31], v[180:183], v[120:127], v148\n\t"
        "v_exp_f32 v98, v98\n\t"
        "v_exp_f32 v99, v99\n\t"
        "v_exp_f32 v100, v100\n\t"
        "v_smfmac_f32_16x16x64_bf16 v[132:135], v[144:147], v[120:127], v148\n\t"
        "v_exp_f32 v101, v101\n\t"
        "v_mfma_f32_32x32x16_bf16 v[112:127], v[168:171], v[88:91], 0\n\t"
        "v_exp_f32 v102, v102\n\t"
        "v_exp_f32 v103, v103\n\t"
        "v_exp_f32 v104, v104\n\t"
        "v_mfma_f32_32x32x16_bf16 v[112:127], v[172:175], v[92:95], v[112:127]\n\t"
        "v_exp_f32 v105, v105\n\t"
        "v_exp_f32 v106, v106\n\t"
        "v_exp_f32 v107, v107\n\t"
        "v_smfmac_f32_32x32x32_bf16 v[32:47], v[176:179], v[96:103], v148\n\t"
        "v_exp_f32 v108, v108\n\t"
        "v_exp_f32 v109, v109\n\t"
        "v_exp_f32 v110, v110\n\t"
        "v_smfmac_f32_16x16x64_bf16 v[136:139], v[144:147], v[96:103], v148\n\t"
        "v_exp_f32 v111, v111\n\t"
        "s_cmp_lg_u32 s46, 0\n\t"
        "s_cbranch_scc1 5f\n\t"
        "s_mov_b32 s45, 0x200000\n\t"
        "2:\n\t"
        "ds_read_b32 v151, v224\n\t"
        "s_sub_u32 s45, s45, 1\n\t"
        "s_waitcnt lgkmcnt(0)\n\t"
        "s_cbranch_scc1 3f\n\t"
        "v_cmp_ne_u32 vcc, v222, v151\n\t"
        "s_cbranch_vccnz 2b\n\t"
        "3:\n\t"
        "s_add_u32 s44, s44, 0x4000\n\t"
        "s_and_b32 s44, s44, 0x3ffff\n\t"
        "buffer_load_dwordx4 v[184:187], v149, s[40:43], s44 offen offset:0 sc1\n\t"
        "buffer_load_dwordx4 v[188:191], v149, s[40:43], s44 offen offset:1024 sc1\n\t"
        "buffer_load_dwordx4 v[192:195], v149, s[40:43], s44 offen offset:2048 sc1\n\t"
        "buffer_load_dwordx4 v[196:199], v149, s[40:43], s44 offen offset:3072 sc1\n\t"
        "buffer_load_dwordx4 v[200:203], v150, s[40:43], s44 offen offset:0 sc1\n\t"
        "buffer_load_dwordx4 v[204:207], v150, s[40:43], s44 offen offset:1024 sc1\n\t"
        "buffer_load_dwordx4 v[208:211], v150, s[40:43], s44 offen offset:2048 sc1\n\t"
        "buffer_load_dwordx4 v[212:215], v150, s[40:43], s44 offen offset:3072 sc1\n\t"
        "s_add_u32 s44, s44, 0x4000\n\t"
        "s_and_b32 s44, s44, 0x3ffff\n\t"
        "buffer_load_dwordx4 v[152:155], v149, s[40:43], s44 offen offset:0 sc1\n\t"
        "buffer_load_dwordx4 v[156:159], v149, s[40:43], s44 offen offset:1024 sc1\n\t"
        "buffer_load_dwordx4 v[160:163], v149, s[40:43], s44 offen offset:2048 sc1\n\t"
        "buffer_load_dwordx4 v[164:167], v149, s[40:43], s44 offen offset:3072 sc1\n\t"
        "5:\n\t"
        "s_mov_b32 s45, 7\n\t"
        "1:\n\t"
        "v_exp_f32 v112, v112\n\t"
        "v_exp_f32 v113, v113\n\t"
        "v_smfmac_f32_32x32x32_bf16 v[32:47], v[180:183], v[104:111], v148\n\t"
        "v_exp_f32 v114, v114\n\t"
        "v_exp_f32 v115, v115\n\t"
        "v_exp_f32 v116, v116\n\t"
        "v_smfmac_f32_16x16x64_bf16 v[136:139], v[144:147], v[104:111], v148\n\t"
        "v_exp_f32 v117, v117\n\t"
        "s_waitcnt vmcnt(8)\n\t"
        "v_mfma_f32_32x32x16_bf16 v[96:111], v[184:187], v[64:67], 0\n\t"
        "v_exp_f32 v118, v118\n\t"
        "v_exp_f32 v119, v119\n\t"
        "v_exp_f32 v120, v120\n\t"
        "v_mfma_f32_32x32x16_bf16 v[96:111], v[188:191], v[68:71], v[96:111]\n\t"
        "v_exp_f32 v121, v121\n\t"
        "v_exp_f32 v122, v122\n\t"
        "v_exp_f32 v123, v123\n\t"
        "v_smfmac_f32_32x32x32_bf16 v[48:63], v[176:179], v[112:119], v148\n\t"
        "v_exp_f32 v124, v124\n\t"
        "v_exp_f32 v125, v125\n\t"
        "v_exp_f32 v126, v126\n\t"
        "v_smfmac_f32_16x16x64_bf16 v[140:143], v[144:147], v[112:119], v148\n\t"
        "buffer_load_dwordx4 v[168:171], v150, s[40:43], s44 offen offset:0 sc1\n\t"
        "v_exp_f32 v127, v127\n\t"
        "v_exp_f32 v96, v96\n\t"
        "v_exp_f32 v97, v97\n\t"
        "v_smfmac_f32_32x32x32_bf16 v[48:63], v[180:183], v[120:127], v148\n\t"
        "v_exp_f32 v98, v98\n\t"
        "v_exp_f32 v99, v99\n\t"
        "v_exp_f32 v100, v100\n\t"
        "v_smfmac_f32_16x16x64_bf16 v[140:143], v[144:147], v[120:127], v148\n\t"
        "v_exp_f32 v101, v101\n\t"
        "v_mfma_f32_32x32x16_bf16 v[112:127], v[184:187], v[72:75], 0\n\t"
        "v_exp_f32 v102, v102\n\t"
        "v_exp_f32 v103, v103\n\t"
        "v_exp_f32 v104, v104\n\t"
        "v_mfma_f32_32x32x16_bf16 v[112:127], v[188:191], v[76:79], v[112:127]\n\t"
        "v_exp_f32 v105, v105\n\t"
        "v_exp_f32 v106, v106\n\t"
        "v_exp_f32 v107, v107\n\t"
        "v_smfmac_f32_32x32x32_bf16 v[0:15], v[192:195], v[96:103], v148\n\t"
        "v_exp_f32 v108, v108\n\t"
        "v_exp_f32 v109, v109\n\t"
        "v_exp_f32 v110, v110\n\t"
        "v_smfmac_f32_16x16x64_bf16 v[128:131], v[144:147], v[96:103], v148\n\t"
        "buffer_load_dwordx4 v[172:175], v150, s[40:43], s44 offen offset:1024 sc1\n\t"
        "v_exp_f32 v111, v111\n\t"
        "v_exp_f32 v112, v112\n\t"
        "v_exp_f32 v113, v113\n\t"
        "v_smfmac_f32_32x32x32_bf16 v[0:15], v[196:199], v[104:111], v148\n\t"
        "v_exp_f32 v114, v114\n\t"
        "v_exp_f32 v115, v115\n\t"
        "v_exp_f32 v116, v116\n\t"
        "v_smfmac_f32_16x16x64_bf16 v[128:131], v[144:147], v[104:111], v148\n\t"
        "v_exp_f32 v117, v117\n\t"
        "v_mfma_f32_32x32x16_bf16 v[96:111], v[184:187], v[80:83], 0\n\t"
        "v_exp_f32 v118, v118\n\t"
        "v_exp_f32 v119, v119\n\t"
        "v_exp_f32 v120, v120\n\t"
        "v_mfma_f32_32x32x16_bf16 v[96:111], v[188:191], v[84:87], v[96:111]\n\t"
        "v_exp_f32 v121, v121\n\t"
        "v_exp_f32 v122, v122\n\t"
        "v_exp_f32 v123, v123\n\t"
        "v_smfmac_f32_32x32x32_bf16 v[16:31], v[192:195], v[112:119], v148\n\t"
        "v_exp_f32 v124, v124\n\t"
        "v_exp_f32 v125, v125\n\t"
        "v_exp_f32 v126, v126\n\t"
        "v_smfmac_f32_16x16x64_bf16 v[132:135], v[144:147], v[112:119], v148\n\t"
        "buffer_load_dwordx4 v[176:179], v150, s[40:43], s44 offen offset:2048 sc1\n\t"
        "v_exp_f32 v127, v127\n\t"
        "v_exp_f32 v96, v96\n\t"
        "v_exp_f32 v97, v97\n\t"
        "v_smfmac_f32_32x32x32_bf16 v[16:31], v[196:199], v[120:127], v148\n\t"
        "v_exp_f32 v98, v98\n\t"
        "v_exp_f32 v99, v99\n\t"
        "v_exp_f32 v100, v100\n\t"
        "v_smfmac_f32_16x16x64_bf16 v[132:135], v[144:147], v[120:127], v148\n\t"
        "v_exp_f32 v101, v101\n\t"
        "v_mfma_f32_32x32x16_bf16 v[112:127], v[184:187], v[88:91], 0\n\t"
        "v_exp_f32 v102, v102\n\t"
        "v_exp_f32 v103, v103\n\t"
        "v_exp_f32 v104, v104\n\t"
        "v_mfma_f32_32x32x16_bf16 v[112:127], v[188:191], v[92:95], v[112:127]\n\t"
        "v_exp_f32 v105, v105\n\t"
        "v_exp_f32 v106, v106\n\t"
        "v_exp_f32 v107, v107\n\t"
        "v_smfmac_f32_32x32x32_bf16 v[32:47], v[192:195], v[96:103], v148\n\t"
        "v_exp_f32 v108, v108\n\t"
        "v_exp_f32 v109, v109\n\t"
        "v_exp_f32 v110, v110\n\t"
        "v_smfmac_f32_16x16x64_bf16 v[136:139], v[144:147], v[96:103], v148\n\t"
        "buffer_load_dwordx4 v[180:183], v150, s[40:43], s44 offen offset:3072 sc1\n\t"
        "v_exp_f32 v111, v111\n\t"
        "v_exp_f32 v112, v112\n\t"
        "v_exp_f32 v113, v113\n\t"
        "v_smfmac_f32_32x32x32_bf16 v[32:47], v[196:199], v[104:111], v148\n\t"
        "s_add_u32 s44, s44, 0x4000\n\t"
        "s_and_b32 s44, s44, 0x3ffff\n\t"
        "v_exp_f32 v114, v114\n\t"
        "v_exp_f32 v115, v115\n\t"
        "v_exp_f32 v116, v116\n\t"
        "v_smfmac_f32_16x16x64_bf16 v[136:139], v[144:147], v[104:111], v148\n\t"
        "v_exp_f32 v117, v117\n\t"
        "s_waitcnt vmcnt(8)\n\t"
        "v_mfma_f32_32x32x16_bf16 v[96:111], v[200:203], v[64:67], 0\n\t"
        "v_exp_f32 v118, v118\n\t"
        "v_exp_f32 v119, v119\n\t"
        "v_exp_f32 v120, v120\n\t"
        "v_mfma_f32_32x32x16_bf16 v[96:111], v[204:207], v[68:71], v[96:111]\n\t"
        "v_exp_f32 v121, v121\n\t"
        "v_exp_f32 v122, v122\n\t"
        "v_exp_f32 v123, v123\n\t"
        "v_smfmac_f32_32x32x32_bf16 v[48:63], v[192:195], v[112:119], v148\n\t"
        "v_exp_f32 v124, v124\n\t"
        "v_exp_f32 v125, v125\n\t"
        "v_exp_f32 v126, v126\n\t"
        "v_smfmac_f32_16x16x64_bf16 v[140:143], v[144:147], v[112:119], v148\n\t"
        "buffer_load_dwordx4 v[184:187], v149, s[40:43], s44 offen offset:0 sc1\n\t"
        "v_exp_f32 v127, v127\n\t"
        "v_exp_f32 v96, v96\n\t"
        "v_exp_f32 v97, v97\n\t"
        "v_smfmac_f32_32x32x32_bf16 v[48:63], v[196:199], v[120:127], v148\n\t"
        "v_exp_f32 v98, v98\n\t"
        "v_exp_f32 v99, v99\n\t"
        "v_exp_f32 v100, v100\n\t"
        "v_smfmac_f32_16x16x64_bf16 v[140:143], v[144:147], v[120:127], v148\n\t"
        "v_exp_f32 v101, v101\n\t"
        "v_mfma_f32_32x32x16_bf16 v[112:127], v[200:203], v[72:75], 0\n\t"
        "v_exp_f32 v102, v102\n\t"
        "v_exp_f32 v103, v103\n\t"
        "v_exp_f32 v104, v104\n\t"
        "v_mfma_f32_32x32x16_bf16 v[112:127], v[204:207], v[76:79], v[112:127]\n\t"
        "v_exp_f32 v105, v105\n\t"
        "v_exp_f32 v106, v106\n\t"
        "v_exp_f32 v107, v107\n\t"
        "v_smfmac_f32_32x32x32_bf16 v[0:15], v[208:211], v[96:103], v148\n\t"
        "v_exp_f32 v108, v108\n\t"
        "v_exp_f32 v109, v109\n\t"
        "v_exp_f32 v110, v110\n\t"
        "v_smfmac_f32_16x16x64_bf16 v[128:131], v[144:147], v[96:103], v148\n\t"
        "buffer_load_dwordx4 v[188:191], v149, s[40:43], s44 offen offset:1024 sc1\n\t"
        "v_exp_f32 v111, v111\n\t"
        "v_exp_f32 v112, v112\n\t"
        "v_exp_f32 v113, v113\n\t"
        "v_smfmac_f32_32x32x32_bf16 v[0:15], v[212:215], v[104:111], v148\n\t"
        "v_exp_f32 v114, v114\n\t"
        "v_exp_f32 v115, v115\n\t"
        "v_exp_f32 v116, v116\n\t"
        "v_smfmac_f32_16x16x64_bf16 v[128:131], v[144:147], v[104:111], v148\n\t"
        "v_exp_f32 v117, v117\n\t"
        "v_mfma_f32_32x32x16_bf16 v[96:111], v[200:203], v[80:83], 0\n\t"
        "v_exp_f32 v118, v118\n\t"
        "v_exp_f32 v119, v119\n\t"
        "v_exp_f32 v120, v120\n\t"
        "v_mfma_f32_32x32x16_bf16 v[96:111], v[204:207], v[84:87], v[96:111]\n\t"
        "v_exp_f32 v121, v121\n\t"
        "v_exp_f32 v122, v122\n\t"
        "v_exp_f32 v123, v123\n\t"
        "v_smfmac_f32_32x32x32_bf16 v[16:31], v[208:211], v[112:119], v148\n\t"
        "v_exp_f32 v124, v124\n\t"
        "v_exp_f32 v125, v125\n\t"
        "v_exp_f32 v126, v126\n\t"
        "v_smfmac_f32_16x16x64_bf16 v[132:135], v[144:147], v[112:119], v148\n\t"
        "buffer_load_dwordx4 v[192:195], v149, s[40:43], s44 offen offset:2048 sc1\n\t"
        "v_exp_f32 v127, v127\n\t"
        "v_exp_f32 v96, v96\n\t"
        "v_exp_f32 v97, v97\n\t"
        "v_smfmac_f32_32x32x32_bf16 v[16:31], v[212:215], v[120:127], v148\n\t"
        "v_exp_f32 v98, v98\n\t"
        "v_exp_f32 v99, v99\n\t"
        "v_exp_f32 v100, v100\n\t"
        "v_smfmac_f32_16x16x64_bf16 v[132:135], v[144:147], v[120:127], v148\n\t"
        "v_exp_f32 v101, v101\n\t"
        "v_mfma_f32_32x32x16_bf16 v[112:127], v[200:203], v[88:91], 0\n\t"
        "v_exp_f32 v102, v102\n\t"
        "v_exp_f32 v103, v103\n\t"
        "v_exp_f32 v104, v104\n\t"
        "v_mfma_f32_32x32x16_bf16 v[112:127], v[204:207], v[92:95], v[112:127]\n\t"
        "v_exp_f32 v105, v105\n\t"
        "v_exp_f32 v106, v106\n\t"
        "v_exp_f32 v107, v107\n\t"
        "v_smfmac_f32_32x32x32_bf16 v[32:47], v[208:211], v[96:103], v148\n\t"
        "v_exp_f32 v108, v108\n\t"
        "v_exp_f32 v109, v109\n\t"
        "v_exp_f32 v110, v110\n\t"
        "v_smfmac_f32_16x16x64_bf16 v[136:139], v[144:147], v[96:103], v148\n\t"
        "buffer_load_dwordx4 v[196:199], v149, s[40:43], s44 offen offset:3072 sc1\n\t"
        "v_exp_f32 v111, v111\n\t"
        "v_exp_f32 v112, v112\n\t"
        "v_exp_f32 v113, v113\n\t"
        "v_smfmac_f32_32x32x32_bf16 v[32:47], v[212:215], v[104:111], v148\n\t"
        "v_exp_f32 v114, v114\n\t"
        "v_exp_f32 v115, v115\n\t"
        "v_exp_f32 v116, v116\n\t"
        "v_smfmac_f32_16x16x64_bf16 v[136:139], v[144:147], v[104:111], v148\n\t"
        "v_exp_f32 v117, v117\n\t"
        "s_waitcnt vmcnt(8)\n\t"
        "v_mfma_f32_32x32x16_bf16 v[96:111], v[152:155], v[64:67], 0\n\t"
        "v_exp_f32 v118, v118\n\t"
        "v_exp_f32 v119, v119\n\t"
        "v_exp_f32 v120, v120\n\t"
        "v_mfma_f32_32x32x16_bf16 v[96:111], v[156:159], v[68:71], v[96:111]\n\t"
        "v_exp_f32 v121, v121\n\t"
        "v_exp_f32 v122, v122\n\t"
        "v_exp_f32 v123, v123\n\t"
        "v_smfmac_f32_32x32x32_bf16 v[48:63], v[208:211], v[112:119], v148\n\t"
        "v_exp_f32 v124, v124\n\t"
        "v_exp_f32 v125, v125\n\t"
        "v_exp_f32 v126, v126\n\t"
        "v_smfmac_f32_16x16x64_bf16 v[140:143], v[144:147], v[112:119], v148\n\t"
        "buffer_load_dwordx4 v[200:203], v150, s[40:43], s44 offen offset:0 sc1\n\t"
        "v_exp_f32 v127, v127\n\t"
        "v_exp_f32 v96, v96\n\t"
        "v_exp_f32 v97, v97\n\t"
        "v_smfmac_f32_32x32x32_bf16 v[48:63], v[212:215], v[120:127], v148\n\t"
        "v_exp_f32 v98, v98\n\t"
        "v_exp_f32 v99, v99\n\t"
        "v_exp_f32 v100, v100\n\t"
        "v_smfmac_f32_16x16x64_bf16 v[140:143], v[144:147], v[120:127], v148\n\t"
        "v_exp_f32 v101, v101\n\t"
        "v_mfma_f32_32x32x16_bf16 v[112:127], v[152:155], v[72:75], 0\n\t"
        "v_exp_f32 v102, v102\n\t"
        "v_exp_f32 v103, v103\n\t"
        "v_exp_f32 v104, v104\n\t"
        "v_mfma_f32_32x32x16_bf16 v[112:127], v[156:159], v[76:79], v[112:127]\n\t"
        "v_exp_f32 v105, v105\n\t"
        "v_exp_f32 v106, v106\n\t"
        "v_exp_f32 v107, v107\n\t"
        "v_smfmac_f32_32x32x32_bf16 v[0:15], v[160:163], v[96:103], v148\n\t"
        "v_exp_f32 v108, v108\n\t"
        "v_exp_f32 v109, v109\n\t"
        "v_exp_f32 v110, v110\n\t"
        "v_smfmac_f32_16x16x64_bf16 v[128:131], v[144:147], v[96:103], v148\n\t"
        "buffer_load_dwordx4 v[204:207], v150, s[40:43], s44 offen offset:1024 sc1\n\t"
        "v_exp_f32 v111, v111\n\t"
        "v_exp_f32 v112, v112\n\t"
        "v_exp_f32 v113, v113\n\t"
        "v_smfmac_f32_32x32x32_bf16 v[0:15], v[164:167], v[104:111], v148\n\t"
        "v_exp_f32 v114, v114\n\t"
        "v_exp_f32 v115, v115\n\t"
        "v_exp_f32 v116, v116\n\t"
        "v_smfmac_f32_16x16x64_bf16 v[128:131], v[144:147], v[104:111], v148\n\t"
        "v_exp_f32 v117, v117\n\t"
        "v_mfma_f32_32x32x16_bf16 v[96:111], v[152:155], v[80:83], 0\n\t"
        "v_exp_f32 v118, v118\n\t"
        "v_exp_f32 v119, v119\n\t"
        "v_exp_f32 v120, v120\n\t"
        "v_mfma_f32_32x32x16_bf16 v[96:111], v[156:159], v[84:87], v[96:111]\n\t"
        "v_exp_f32 v121, v121\n\t"
        "v_exp_f32 v122, v122\n\t"
        "v_exp_f32 v123, v123\n\t"
        "v_smfmac_f32_32x32x32_bf16 v[16:31], v[160:163], v[112:119], v148\n\t"
        "v_exp_f32 v124, v124\n\t"
        "v_exp_f32 v125, v125\n\t"
        "v_exp_f32 v126, v126\n\t"
        "v_smfmac_f32_16x16x64_bf16 v[132:135], v[144:147], v[112:119], v148\n\t"
        "buffer_load_dwordx4 v[208:211], v150, s[40:43], s44 offen offset:2048 sc1\n\t"
        "v_exp_f32 v127, v127\n\t"
        "v_exp_f32 v96, v96\n\t"
        "v_exp_f32 v97, v97\n\t"
        "v_smfmac_f32_32x32x32_bf16 v[16:31], v[164:167], v[120:127], v148\n\t"
        "v_exp_f32 v98, v98\n\t"
        "v_exp_f32 v99, v99\n\t"
        "v_exp_f32 v100, v100\n\t"
        "v_smfmac_f32_16x16x64_bf16 v[132:135], v[144:147], v[120:127], v148\n\t"
        "v_exp_f32 v101, v101\n\t"
        "v_mfma_f32_32x32x16_bf16 v[112:127], v[152:155], v[88:91], 0\n\t"
        "v_exp_f32 v102, v102\n\t"
        "v_exp_f32 v103, v103\n\t"
        "v_exp_f32 v104, v104\n\t"
        "v_mfma_f32_32x32x16_bf16 v[112:127], v[156:159], v[92:95], v[112:127]\n\t"
        "v_exp_f32 v105, v105\n\t"
        "v_exp_f32 v106, v106\n\t"
        "v_exp_f32 v107, v107\n\t"
        "v_smfmac_f32_32x32x32_bf16 v[32:47], v[160:163], v[96:103], v148\n\t"
        "v_exp_f32 v108, v108\n\t"
        "v_exp_f32 v109, v109\n\t"
        "v_exp_f32 v110, v110\n\t"
        "v_smfmac_f32_16x16x64_bf16 v[136:139], v[144:147], v[96:103], v148\n\t"
        "buffer_load_dwordx4 v[212:215], v150, s[40:43], s44 offen offset:3072 sc1\n\t"
        "v_exp_f32 v111, v111\n\t"
        "v_exp_f32 v112, v112\n\t"
        "v_exp_f32 v113, v113\n\t"
        "v_smfmac_f32_32x32x32_bf16 v[32:47], v[164:167], v[104:111], v148\n\t"
        "s_add_u32 s44, s44, 0x4000\n\t"
        "s_and_b32 s44, s44, 0x3ffff\n\t"
        "v_exp_f32 v114, v114\n\t"
        "v_exp_f32 v115, v115\n\t"
        "v_exp_f32 v116, v116\n\t"
        "v_smfmac_f32_16x16x64_bf16 v[136:139], v[144:147], v[104:111], v148\n\t"
        "v_exp_f32 v117, v117\n\t"
        "s_waitcnt vmcnt(8)\n\t"
        "v_mfma_f32_32x32x16_bf16 v[96:111], v[168:171], v[64:67], 0\n\t"
        "v_exp_f32 v118, v118\n\t"
        "v_exp_f32 v119, v119\n\t"
        "v_exp_f32 v120, v120\n\t"
        "v_mfma_f32_32x32x16_bf16 v[96:111], v[172:175], v[68:71], v[96:111]\n\t"
        "v_exp_f32 v121, v121\n\t"
        "v_exp_f32 v122, v122\n\t"
        "v_exp_f32 v123, v123\n\t"
        "v_smfmac_f32_32x32x32_bf16 v[48:63], v[160:163], v[112:119], v148\n\t"
        "v_exp_f32 v124, v124\n\t"
        "v_exp_f32 v125, v125\n\t"
        "v_exp_f32 v126, v126\n\t"
        "v_smfmac_f32_16x16x64_bf16 v[140:143], v[144:147], v[112:119], v148\n\t"
        "buffer_load_dwordx4 v[152:155], v149, s[40:43], s44 offen offset:0 sc1\n\t"
        "v_exp_f32 v127, v127\n\t"
        "v_exp_f32 v96, v96\n\t"
        "v_exp_f32 v97, v97\n\t"
        "v_smfmac_f32_32x32x32_bf16 v[48:63], v[164:167], v[120:127], v148\n\t"
        "v_exp_f32 v98, v98\n\t"
        "v_exp_f32 v99, v99\n\t"
        "v_exp_f32 v100, v100\n\t"
        "v_smfmac_f32_16x16x64_bf16 v[140:143], v[144:147], v[120:127], v148\n\t"
        "v_exp_f32 v101, v101\n\t"
        "v_mfma_f32_32x32x16_bf16 v[112:127], v[168:171], v[72:75], 0\n\t"
        "v_exp_f32 v102, v102\n\t"
        "v_exp_f32 v103, v103\n\t"
        "v_exp_f32 v104, v104\n\t"
        "v_mfma_f32_32x32x16_bf16 v[112:127], v[172:175], v[76:79], v[112:127]\n\t"
        "v_exp_f32 v105, v105\n\t"
        "v_exp_f32 v106, v106\n\t"
        "v_exp_f32 v107, v107\n\t"
        "v_smfmac_f32_32x32x32_bf16 v[0:15], v[176:179], v[96:103], v148\n\t"
        "v_exp_f32 v108, v108\n\t"
        "v_exp_f32 v109, v109\n\t"
        "v_exp_f32 v110, v110\n\t"
        "v_smfmac_f32_16x16x64_bf16 v[128:131], v[144:147], v[96:103], v148\n\t"
        "buffer_load_dwordx4 v[156:159], v149, s[40:43], s44 offen offset:1024 sc1\n\t"
        "v_exp_f32 v111, v111\n\t"
        "v_exp_f32 v112, v112\n\t"
        "v_exp_f32 v113, v113\n\t"
        "v_smfmac_f32_32x32x32_bf16 v[0:15], v[180:183], v[104:111], v148\n\t"
        "v_exp_f32 v114, v114\n\t"
        "v_exp_f32 v115, v115\n\t"
        "v_exp_f32 v116, v116\n\t"
        "v_smfmac_f32_16x16x64_bf16 v[128:131], v[144:147], v[104:111], v148\n\t"
        "v_exp_f32 v117, v117\n\t"
        "v_mfma_f32_32x32x16_bf16 v[96:111], v[168:171], v[80:83], 0\n\t"
        "v_exp_f32 v118, v118\n\t"
        "v_exp_f32 v119, v119\n\t"
        "v_exp_f32 v120, v120\n\t"
        "v_mfma_f32_32x32x16_bf16 v[96:111], v[172:175], v[84:87], v[96:111]\n\t"
        "v_exp_f32 v121, v121\n\t"
        "v_exp_f32 v122, v122\n\t"
        "v_exp_f32 v123, v123\n\t"
        "v_smfmac_f32_32x32x32_bf16 v[16:31], v[176:179], v[112:119], v148\n\t"
        "v_exp_f32 v124, v124\n\t"
        "v_exp_f32 v125, v125\n\t"
        "v_exp_f32 v126, v126\n\t"
        "v_smfmac_f32_16x16x64_bf16 v[132:135], v[144:147], v[112:119], v148\n\t"
        "buffer_load_dwordx4 v[160:163], v149, s[40:43], s44 offen offset:2048 sc1\n\t"
        "v_exp_f32 v127, v127\n\t"
        "v_exp_f32 v96, v96\n\t"
        "v_exp_f32 v97, v97\n\t"
        "v_smfmac_f32_32x32x32_bf16 v[16:31], v[180:183], v[120:127], v148\n\t"
        "v_exp_f32 v98, v98\n\t"
        "v_exp_f32 v99, v99\n\t"
        "v_exp_f32 v100, v100\n\t"
        "v_smfmac_f32_16x16x64_bf16 v[132:135], v[144:147], v[120:127], v148\n\t"
        "v_exp_f32 v101, v101\n\t"
        "v_mfma_f32_32x32x16_bf16 v[112:127], v[168:171], v[88:91], 0\n\t"
        "v_exp_f32 v102, v102\n\t"
        "v_exp_f32 v103, v103\n\t"
        "v_exp_f32 v104, v104\n\t"
        "v_mfma_f32_32x32x16_bf16 v[112:127], v[172:175], v[92:95], v[112:127]\n\t"
        "v_exp_f32 v105, v105\n\t"
        "v_exp_f32 v106, v106\n\t"
        "v_exp_f32 v107, v107\n\t"
        "v_smfmac_f32_32x32x32_bf16 v[32:47], v[176:179], v[96:103], v148\n\t"
        "v_exp_f32 v108, v108\n\t"
        "v_exp_f32 v109, v109\n\t"
        "v_exp_f32 v110, v110\n\t"
        "v_smfmac_f32_16x16x64_bf16 v[136:139], v[144:147], v[96:103], v148\n\t"
        "buffer_load_dwordx4 v[164:167], v149, s[40:43], s44 offen offset:3072 sc1\n\t"
        "v_exp_f32 v111, v111\n\t"
        "s_sub_u32 s45, s45, 1\n\t"
        "s_cmp_lg_u32 s45, 0\n\t"
        "s_cbranch_scc1 1b\n\t"
        "v_exp_f32 v112, v112\n\t"
        "v_exp_f32 v113, v113\n\t"
        "v_smfmac_f32_32x32x32_bf16 v[32:47], v[180:183], v[104:111], v148\n\t"
        "v_exp_f32 v114, v114\n\t"
        "v_exp_f32 v115, v115\n\t"
        "v_exp_f32 v116, v116\n\t"
        "v_smfmac_f32_16x16x64_bf16 v[136:139], v[144:147], v[104:111], v148\n\t"
        "v_exp_f32 v117, v117\n\t"
        "s_waitcnt vmcnt(8)\n\t"
        "v_mfma_f32_32x32x16_bf16 v[96:111], v[184:187], v[64:67], 0\n\t"
        "v_exp_f32 v118, v118\n\t"
        "v_exp_f32 v119, v119\n\t"
        "v_exp_f32 v120, v120\n\t"
        "v_mfma_f32_32x32x16_bf16 v[96:111], v[188:191], v[68:71], v[96:111]\n\t"
        "v_exp_f32 v121, v121\n\t"
        "v_exp_f32 v122, v122\n\t"
        "v_exp_f32 v123, v123\n\t"
        "v_smfmac_f32_32x32x32_bf16 v[48:63], v[176:179], v[112:119], v148\n\t"
        "v_exp_f32 v124, v124\n\t"
        "v_exp_f32 v125, v125\n\t"
        "v_exp_f32 v126, v126\n\t"
        "v_smfmac_f32_16x16x64_bf16 v[140:143], v[144:147], v[112:119], v148\n\t"
        "v_exp_f32 v127, v127\n\t"
        "v_exp_f32 v96, v96\n\t"
        "v_exp_f32 v97, v97\n\t"
        "v_smfmac_f32_32x32x32_bf16 v[48:63], v[180:183], v[120:127], v148\n\t"
        "v_exp_f32 v98, v98\n\t"
        "v_exp_f32 v99, v99\n\t"
        "v_exp_f32 v100, v100\n\t"
        "v_smfmac_f32_16x16x64_bf16 v[140:143], v[144:147], v[120:127], v148\n\t"
        "v_exp_f32 v101, v101\n\t"
        "v_mfma_f32_32x32x16_bf16 v[112:127], v[184:187], v[72:75], 0\n\t"
        "v_exp_f32 v102, v102\n\t"
        "v_exp_f32 v103, v103\n\t"
        "v_exp_f32 v104, v104\n\t"
        "v_mfma_f32_32x32x16_bf16 v[112:127], v[188:191], v[76:79], v[112:127]\n\t"
        "v_exp_f32 v105, v105\n\t"
        "v_exp_f32 v106, v106\n\t"
        "v_exp_f32 v107, v107\n\t"
        "v_smfmac_f32_32x32x32_bf16 v[0:15], v[192:195], v[96:103], v148\n\t"
        "v_exp_f32 v108, v108\n\t"
        "v_exp_f32 v109, v109\n\t"
        "v_exp_f32 v110, v110\n\t"
        "v_smfmac_f32_16x16x64_bf16 v[128:131], v[144:147], v[96:103], v148\n\t"
        "v_exp_f32 v111, v111\n\t"
        "v_exp_f32 v112, v112\n\t"
        "v_exp_f32 v113, v113\n\t"
        "v_smfmac_f32_32x32x32_bf16 v[0:15], v[196:199], v[104:111], v148\n\t"
        "v_exp_f32 v114, v114\n\t"
        "v_exp_f32 v115, v115\n\t"
        "v_exp_f32 v116, v116\n\t"
        "v_smfmac_f32_16x16x64_bf16 v[128:131], v[144:147], v[104:111], v148\n\t"
        "v_exp_f32 v117, v117\n\t"
        "s_waitcnt vmcnt(4)\n\t"
        "v_mfma_f32_32x32x16_bf16 v[96:111], v[200:203], v[64:67], 0\n\t"
        "v_exp_f32 v118, v118\n\t"
        "v_exp_f32 v119, v119\n\t"
        "v_exp_f32 v120, v120\n\t"
        "v_mfma_f32_32x32x16_bf16 v[96:111], v[204:207], v[68:71], v[96:111]\n\t"
        "v_exp_f32 v121, v121\n\t"
        "v_exp_f32 v122, v122\n\t"
        "v_exp_f32 v123, v123\n\t"
        "v_smfmac_f32_32x32x32_bf16 v[16:31], v[192:195], v[112:119], v148\n\t"
        "v_exp_f32 v124, v124\n\t"
        "v_exp_f32 v125, v125\n\t"
        "v_exp_f32 v126, v126\n\t"
        "v_smfmac_f32_16x16x64_bf16 v[132:135], v[144:147], v[112:119], v148\n\t"
        "v_exp_f32 v127, v127\n\t"
        "s_cmp_lg_u32 s47, 0\n\t"
        "s_cbranch_scc1 4f\n\t"
        "global_load_dwordx4 %[r0], v223, %[xb] offset:0\n\t"
        "global_load_dwordx4 %[r1], v223, %[xb] offset:32\n\t"
        "global_load_dwordx4 %[r2], v223, %[xb] offset:64\n\t"
        "global_load_dwordx4 %[r3], v223, %[xb] offset:96\n\t"
        "4:\n\t"
        "v_exp_f32 v96, v96\n\t"
        "v_exp_f32 v97, v97\n\t"
        "v_smfmac_f32_32x32x32_bf16 v[16:31], v[196:199], v[120:127], v148\n\t"
        "v_exp_f32 v98, v98\n\t"
        "v_exp_f32 v99, v99\n\t"
        "v_exp_f32 v100, v100\n\t"
        "v_smfmac_f32_16x16x64_bf16 v[132:135], v[144:147], v[120:127], v148\n\t"
        "v_exp_f32 v101, v101\n\t"
        "v_mfma_f32_32x32x16_bf16 v[112:127], v[200:203], v[72:75], 0\n\t"
        "v_exp_f32 v102, v102\n\t"
        "v_exp_f32 v103, v103\n\t"
        "v_exp_f32 v104, v104\n\t"
        "v_mfma_f32_32x32x16_bf16 v[112:127], v[204:207], v[76:79], v[112:127]\n\t"
        "v_exp_f32 v105, v105\n\t"
        "v_exp_f32 v106, v106\n\t"
        "v_exp_f32 v107, v107\n\t"
        "v_smfmac_f32_32x32x32_bf16 v[0:15], v[208:211], v[96:103], v148\n\t"
        "v_exp_f32 v108, v108\n\t"
        "v_exp_f32 v109, v109\n\t"
        "v_exp_f32 v110, v110\n\t"
        "v_smfmac_f32_16x16x64_bf16 v[128:131], v[144:147], v[96:103], v148\n\t"
        "v_exp_f32 v111, v111\n\t"
        "v_exp_f32 v112, v112\n\t"
        "v_exp_f32 v113, v113\n\t"
        "v_smfmac_f32_32x32x32_bf16 v[0:15], v[212:215], v[104:111], v148\n\t"
        "v_exp_f32 v114, v114\n\t"
        "v_exp_f32 v115, v115\n\t"
        "v_exp_f32 v116, v116\n\t"
        "v_smfmac_f32_16x16x64_bf16 v[128:131], v[144:147], v[104:111], v148\n\t"
        "v_exp_f32 v117, v117\n\t"
        "v_mfma_f32_32x32x16_bf16 v[96:111], v[184:187], v[80:83], 0\n\t"
        "v_exp_f32 v118, v118\n\t"
        "v_exp_f32 v119, v119\n\t"
        "v_exp_f32 v120, v120\n\t"
        "ds_write_b128 v216, v[0:3] offset:0\n\t"
        "v_mfma_f32_32x32x16_bf16 v[96:111], v[188:191], v[84:87], v[96:111]\n\t"
        "v_exp_f32 v121, v121\n\t"
        "ds_write_b128 v216, v[4:7] offset:1024\n\t"
        "v_exp_f32 v122, v122\n\t"
        "ds_write_b128 v216, v[8:11] offset:2048\n\t"
        "v_exp_f32 v123, v123\n\t"
        "ds_write_b128 v216, v[12:15] offset:3072\n\t"
        "v_smfmac_f32_32x32x32_bf16 v[16:31], v[208:211], v[112:119], v148\n\t"
        "v_exp_f32 v124, v124\n\t"
        "ds_write_b32 v218, v128 offset:0\n\t"
        "v_exp_f32 v125, v125\n\t"
        "v_exp_f32 v126, v126\n\t"
        "v_smfmac_f32_16x16x64_bf16 v[132:135], v[144:147], v[112:119], v148\n\t"
        "v_exp_f32 v127, v127\n\t"
        "v_exp_f32 v96, v96\n\t"
        "v_exp_f32 v97, v97\n\t"
        "v_smfmac_f32_32x32x32_bf16 v[16:31], v[212:215], v[120:127], v148\n\t"
        "v_exp_f32 v98, v98\n\t"
        "v_exp_f32 v99, v99\n\t"
        "v_exp_f32 v100, v100\n\t"
        "v_smfmac_f32_16x16x64_bf16 v[132:135], v[144:147], v[120:127], v148\n\t"
        "v_exp_f32 v101, v101\n\t"
        "v_mfma_f32_32x32x16_bf16 v[112:127], v[184:187], v[88:91], 0\n\t"
        "v_exp_f32 v102, v102\n\t"
        "v_exp_f32 v103, v103\n\t"
        "v_exp_f32 v104, v104\n\t"
        "ds_write_b128 v216, v[16:19] offset:4096\n\t"
        "v_mfma_f32_32x32x16_bf16 v[112:127], v[188:191], v[92:95], v[112:127]\n\t"
        "v_exp_f32 v105, v105\n\t"
        "ds_write_b128 v216, v[20:23] offset:5120\n\t"
        "v_exp_f32 v106, v106\n\t"
        "ds_write_b128 v216, v[24:27] offset:6144\n\t"
        "v_exp_f32 v107, v107\n\t"
        "ds_write_b128 v216, v[28:31] offset:7168\n\t"
        "v_smfmac_f32_32x32x32_bf16 v[32:47], v[192:195], v[96:103], v148\n\t"
        "v_exp_f32 v108, v108\n\t"
        "ds_write_b32 v218, v132 offset:256\n\t"
        "v_exp_f32 v109, v109\n\t"
        "ds_write_b32 v220, v222\n\t"
        "v_exp_f32 v110, v110\n\t"
        "v_smfmac_f32_16x16x64_bf16 v[136:139], v[144:147], v[96:103], v148\n\t"
        "v_exp_f32 v111, v111\n\t"
        "v_exp_f32 v112, v112\n\t"
        "v_exp_f32 v113, v113\n\t"
        "v_smfmac_f32_32x32x32_bf16 v[32:47], v[196:199], v[104:111], v148\n\t"
        "v_exp_f32 v114, v114\n\t"
        "v_exp_f32 v115, v115\n\t"
        "v_exp_f32 v116, v116\n\t"
        "v_smfmac_f32_16x16x64_bf16 v[136:139], v[144:147], v[104:111], v148\n\t"
        "v_exp_f32 v117, v117\n\t"
        "v_mfma_f32_32x32x16_bf16 v[96:111], v[200:203], v[80:83], 0\n\t"
        "v_exp_f32 v118, v118\n\t"
        "v_exp_f32 v119, v119\n\t"
        "v_exp_f32 v120, v120\n\t"
        "v_mfma_f32_32x32x16_bf16 v[96:111], v[204:207], v[84:87], v[96:111]\n\t"
        "v_exp_f32 v121, v121\n\t"
        "v_exp_f32 v122, v122\n\t"
        "v_exp_f32 v123, v123\n\t"
        "v_smfmac_f32_32x32x32_bf16 v[48:63], v[192:195], v[112:119], v148\n\t"
        "v_exp_f32 v124, v124\n\t"
        "v_exp_f32 v125, v125\n\t"
        "v_exp_f32 v126, v126\n\t"
        "v_smfmac_f32_16x16x64_bf16 v[140:143], v[144:147], v[112:119], v148\n\t"
        "v_exp_f32 v127, v127\n\t"
        "v_exp_f32 v96, v96\n\t"
        "v_exp_f32 v97, v97\n\t"
        "v_smfmac_f32_32x32x32_bf16 v[48:63], v[196:199], v[120:127], v148\n\t"
        "v_exp_f32 v98, v98\n\t"
        "v_exp_f32 v99, v99\n\t"
        "v_exp_f32 v100, v100\n\t"
        "v_smfmac_f32_16x16x64_bf16 v[140:143], v[144:147], v[120:127], v148\n\t"
        "v_exp_f32 v101, v101\n\t"
        "v_mfma_f32_32x32x16_bf16 v[112:127], v[200:203], v[88:91], 0\n\t"
        "v_exp_f32 v102, v102\n\t"
        "v_exp_f32 v103, v103\n\t"
        "v_exp_f32 v104, v104\n\t"
        "ds_read_b32 v151, v221\n\t"
        "v_mfma_f32_32x32x16_bf16 v[112:127], v[204:207], v[92:95], v[112:127]\n\t"
        "v_exp_f32 v105, v105\n\t"
        "v_exp_f32 v106, v106\n\t"
        "v_exp_f32 v107, v107\n\t"
        "v_smfmac_f32_32x32x32_bf16 v[32:47], v[208:211], v[96:103], v148\n\t"
        "v_exp_f32 v108, v108\n\t"
        "v_exp_f32 v109, v109\n\t"
        "v_exp_f32 v110, v110\n\t"
        "v_smfmac_f32_16x16x64_bf16 v[136:139], v[144:147], v[96:103], v148\n\t"
        "v_exp_f32 v111, v111\n\t"
        "s_waitcnt lgkmcnt(0)\n\t"
        "v_cmp_ne_u32 vcc, v222, v151\n\t"
        "s_cbranch_vccnz 40f\n\t"
        "41:\n\t"
        "v_exp_f32 v112, v112\n\t"
        "ds_read_b128 v[0:3], v217 offset:0\n\t"
        "v_exp_f32 v113, v113\n\t"
        "ds_read_b128 v[4:7], v217 offset:1024\n\t"
        "v_smfmac_f32_32x32x32_bf16 v[32:47], v[212:215], v[104:111], v148\n\t"
        "v_exp_f32 v114, v114\n\t"
        "ds_read_b128 v[8:11], v217 offset:2048\n\t"
        "v_exp_f32 v115, v115\n\t"
        "ds_read_b128 v[12:15], v217 offset:3072\n\t"
        "v_exp_f32 v116, v116\n\t"
        "ds_read_b32 v128, v219 offset:0\n\t"
        "v_smfmac_f32_16x16x64_bf16 v[136:139], v[144:147], v[104:111], v148\n\t"
        "v_exp_f32 v117, v117\n\t"
        "ds_read_b128 v[16:19], v217 offset:4096\n\t"
        "v_exp_f32 v118, v118\n\t"
        "ds_read_b128 v[20:23], v217 offset:5120\n\t"
        "v_exp_f32 v119, v119\n\t"
        "ds_read_b128 v[24:27], v217 offset:6144\n\t"
        "v_exp_f32 v120, v120\n\t"
        "ds_read_b128 v[28:31], v217 offset:7168\n\t"
        "v_exp_f32 v121, v121\n\t"
        "ds_read_b32 v132, v219 offset:256\n\t"
        "v_exp_f32 v122, v122\n\t"
        "v_exp_f32 v123, v123\n\t"
        "v_smfmac_f32_32x32x32_bf16 v[48:63], v[208:211], v[112:119], v148\n\t"
        "v_exp_f32 v124, v124\n\t"
        "v_exp_f32 v125, v125\n\t"
        "v_exp_f32 v126, v126\n\t"
        "v_smfmac_f32_16x16x64_bf16 v[140:143], v[144:147], v[112:119], v148\n\t"
        "v_exp_f32 v127, v127\n\t"
        "s_nop 1\n\t"
        "v_smfmac_f32_32x32x32_bf16 v[48:63], v[212:215], v[120:127], v148\n\t"
        "v_smfmac_f32_16x16x64_bf16 v[140:143], v[144:147], v[120:127], v148\n\t"
        "s_waitcnt vmcnt(0) lgkmcnt(0)\n\t"
        "s_nop 6\n\t"
        "s_branch 9f\n\t"
        "30:\n\t"
        "s_add_u32 s44, s44, 0x4000\n\t"
        "s_and_b32 s44, s44, 0x3ffff\n\t"
        "buffer_load_dwordx4 v[184:187], v149, s[40:43], s44 offen offset:0 sc1\n\t"
        "buffer_load_dwordx4 v[188:191], v149, s[40:43], s44 offen offset:1024 sc1\n\t"
        "buffer_load_dwordx4 v[192:195], v149, s[40:43], s44 offen offset:2048 sc1\n\t"
        "buffer_load_dwordx4 v[196:199], v149, s[40:43], s44 offen offset:3072 sc1\n\t"
        "buffer_load_dwordx4 v[200:203], v150, s[40:43], s44 offen offset:0 sc1\n\t"
        "buffer_load_dwordx4 v[204:207], v150, s[40:43], s44 offen offset:1024 sc1\n\t"
        "buffer_load_dwordx4 v[208:211], v150, s[40:43], s44 offen offset:2048 sc1\n\t"
        "buffer_load_dwordx4 v[212:215], v150, s[40:43], s44 offen offset:3072 sc1\n\t"
        "s_add_u32 s44, s44, 0x4000\n\t"
        "s_and_b32 s44, s44, 0x3ffff\n\t"
        "buffer_load_dwordx4 v[152:155], v149, s[40:43], s44 offen offset:0 sc1\n\t"
        "buffer_load_dwordx4 v[156:159], v149, s[40:43], s44 offen offset:1024 sc1\n\t"
        "buffer_load_dwordx4 v[160:163], v149, s[40:43], s44 offen offset:2048 sc1\n\t"
        "buffer_load_dwordx4 v[164:167], v149, s[40:43], s44 offen offset:3072 sc1\n\t"
        "s_mov_b32 s46, 1\n\t"
        "s_branch 31b\n\t"
        "32:\n\t"
        "s_add_u32 s44, s44, 0x4000\n\t"
        "s_and_b32 s44, s44, 0x3ffff\n\t"
        "buffer_load_dwordx4 v[184:187], v149, s[40:43], s44 offen offset:0 sc1\n\t"
        "buffer_load_dwordx4 v[188:191], v149, s[40:43], s44 offen offset:1024 sc1\n\t"
        "buffer_load_dwordx4 v[192:195], v149, s[40:43], s44 offen offset:2048 sc1\n\t"
        "buffer_load_dwordx4 v[196:199], v149, s[40:43], s44 offen offset:3072 sc1\n\t"
        "buffer_load_dwordx4 v[200:203], v150, s[40:43], s44 offen offset:0 sc1\n\t"
        "buffer_load_dwordx4 v[204:207], v150, s[40:43], s44 offen offset:1024 sc1\n\t"
        "buffer_load_dwordx4 v[208:211], v150, s[40:43], s44 offen offset:2048 sc1\n\t"
        "buffer_load_dwordx4 v[212:215], v150, s[40:43], s44 offen offset:3072 sc1\n\t"
        "s_add_u32 s44, s44, 0x4000\n\t"
        "s_and_b32 s44, s44, 0x3ffff\n\t"
        "buffer_load_dwordx4 v[152:155], v149, s[40:43], s44 offen offset:0 sc1\n\t"
        "buffer_load_dwordx4 v[156:159], v149, s[40:43], s44 offen offset:1024 sc1\n\t"
        "buffer_load_dwordx4 v[160:163], v149, s[40:43], s44 offen offset:2048 sc1\n\t"
        "buffer_load_dwordx4 v[164:167], v149, s[40:43], s44 offen offset:3072 sc1\n\t"
        "s_mov_b32 s46, 1\n\t"
        "s_branch 33b\n\t"
        "40:\n\t"
        "s_mov_b32 s45, 0x100000\n\t"
        "42:\n\t"
        "ds_read_b32 v151, v221\n\t"
        "s_sub_u32 s45, s45, 1\n\t"
        "s_waitcnt lgkmcnt(0)\n\t"
        "s_cbranch_scc1 41b\n\t"
        "v_cmp_ne_u32 vcc, v222, v151\n\t"
        "s_cbranch_vccnz 42b\n\t"
        "s_branch 41b\n\t"
        "9:\n\t"
        : "+{v[0:15]}"(o0), "+{v[16:31]}"(o1), "+{v[32:47]}"(o2), "+{v[48:63]}"(o3), "+{v[128:143]}"(lv), "+{s44}"(soff), "+{v[152:167]}"(kva), "+{v[168:183]}"(kvb), "+{v[184:191]}"(fv),
          [r0] "+v"(x1p[0]), [r1] "+v"(x1p[1]), [r2] "+v"(x1p[2]), [r3] "+v"(x1p[3]) STAMP_OUTS
        : "{v[64:71]}"(q[0]), "{v[72:79]}"(q[1]), "{v[80:87]}"(q[2]), "{v[88:95]}"(q[3]), "{v[144:147]}"(ones), "{v148}"(0xddddu), "{v149}"(kvoff), "{v150}"(kvoff + 4096),
          "{v[216:223]}"(cv), "{v224}"(lbase + L_W + 16), "{s[40:43]}"(srd), "{s47}"(L), [xb] "s"(x0)
        : "v96", "v97", "v98", "v99", "v100", "v101", "v102", "v103", "v104", "v105", "v106", "v107", "v108", "v109", "v110", "v111", "v112", "v113", "v114", "v115", "v116", "v117", "v118", "v119", "v120", "v121", "v122", "v123", "v124", "v125", "v126", "v127", "v151", "v192", "v193", "v194", "v195", "v196", "v197", "v198", "v199", "v200", "v201", "v202", "v203", "v204", "v205", "v206", "v207", "v208", "v209", "v210", "v211", "v212", "v213", "v214", "v215", "s45", "s46", "vcc", "scc", "memory");
      STAMP_PUT(6 + 16 * L);
      STAMPV(9 + 16 * L);
      PHASE_VARS;
      const unsigned dst = OFF_OF + L * (5 * MBY) + (tm * 4 + 2 * kh) * TS + (tn * 2 + hh) * 2048 + lane * 16;
      auto put = [&](int u, const f32x16& o) {
#pragma unroll
        for (int c = 0; c < 2; ++c) {
          u32x4 w;
#pragma unroll
          for (int e = 0; e < 4; ++e) w[e] = pack2(o[8 * c + 2 * e], o[8 * c + 2 * e + 1]);
          stg(rs, dst + u * TS + c * 1024, w);
        }
      };
      const float la = lv[8] + lv[0], lb = lv[12] + lv[4];
      o2 = (o2 + o0) * RCP(la); put(0, o2);
      SBAR();
      o3 = (o3 + o1) * RCP(lb); put(1, o3);
      SBAR();
      {
        const f32x16 t = o2 + o3;
        float sum = 0.f;
#pragma unroll
        for (int e = 0; e < 16; e += 4) sum += (t[e] + t[e + 1]) + (t[e + 2] + t[e + 3]);
        if (__any(!(fabsf(sum) < 1e37f) | !(fmaxf(la, lb) < 1e27f) | !(fminf(la, lb) > 1e-9f) | !(la + lb == la + lb))) {
          DRAIN();
          attn_safe(rs, kvoff - kh * 8192, q[2], ones, o2); put(0, o2);
          attn_safe(rs, kvoff - kh * 8192, q[3], ones, o3); put(1, o3);
        }
      }
    }
    STAMPV(10 + 16 * L);
    {
      f32x16 acc[2];
      u32x4 res[4];
      u32x2 wrc[8];
      STAMP_DECL;
      { PHASE_VARS;
      BAR_ALL();
      STAMPV(11 + 16 * L);
#pragma unroll
      for (int g = 0; g < 8; ++g) wrc[g] = *reinterpret_cast<const u32x2*>(smem + L_WR + g * 16 + hi * 8);
      if (L == 0) {
#pragma unroll
        for (int i = 0; i < 4; ++i) res[i] = fo_swap(x1p[i]);
      } else {
        const bool routed = (int)words[3] == tile * 32 + r32;
        const float mb1 = reinterpret_cast<const float*>(smem + L_AB)[256 + wid * 64 + r32], rd1 = reinterpret_cast<const float*>(smem + L_AB)[256 + wid * 64 + 32 + r32];
#pragma unroll
        for (int i = 0; i < 4; ++i) {
          const u32x4 pr = *reinterpret_cast<const u32x4*>(smem + L_X2P + (i * 2 + hi) * 16);
#pragma unroll
          for (int e = 0; e < 4; ++e) {
            const unsigned h2 = ln_pair(x1p[i][e], mb1, rd1);
            unsigned t = pack2(lo16(x1p[i][e]) + lo16(h2), hi16(x1p[i][e]) + hi16(h2));
            PIN(t);
            res[i][e] = routed ? pr[e] : t;
          }
        }
      }
#pragma unroll
      for (int g = 0; g < 8; ++g) PIN(wrc[g]);
#pragma unroll
      for (int i = 0; i < 4; ++i) PIN(res[i]);
      unsigned soff = tn * 4096;
      const unsigned a0 = lbase + wrow * 128 + ((wch ^ wsw) << 4) + 8 * (tp & 1), a1 = lbase + wrow * 128 + (((4 + wch) ^ wsw) << 4) + 8 * (tp & 1);
      asm volatile(
        "ds_read_b64_tr_b16 v[32:33], v193 offset:0\n\t"
        "ds_read_b64_tr_b16 v[34:35], v193 offset:1024\n\t"
        "ds_read_b64_tr_b16 v[36:37], v194 offset:0\n\t"
        "ds_read_b64_tr_b16 v[38:39], v194 offset:1024\n\t"
        "ds_read_b64_tr_b16 v[40:41], v193 offset:2048\n\t"
        "ds_read_b64_tr_b16 v[42:43], v193 offset:3072\n\t"
        "ds_read_b64_tr_b16 v[44:45], v194 offset:2048\n\t"
        "ds_read_b64_tr_b16 v[46:47], v194 offset:3072\n\t"
        "ds_read_b64_tr_b16 v[48:49], v193 offset:4096\n\t"
        "ds_read_b64_tr_b16 v[50:51], v193 offset:5120\n\t"
        "ds_read_b64_tr_b16 v[52:53], v194 offset:4096\n\t"
        "ds_read_b64_tr_b16 v[54:55], v194 offset:5120\n\t"
        "ds_read_b64_tr_b16 v[56:57], v193 offset:6144\n\t"
        "ds_read_b64_tr_b16 v[58:59], v193 offset:7168\n\t"
        "ds_read_b64_tr_b16 v[60:61], v194 offset:6144\n\t"
        "ds_read_b64_tr_b16 v[62:63], v194 offset:7168\n\t"
        "s_mov_b32 s45, 0x200000\n\t"
        "2:\n\t"
        "ds_read_b32 v200, v198\n\t"
        "s_sub_u32 s45, s45, 1\n\t"
        "s_waitcnt lgkmcnt(0)\n\t"
        "s_cbranch_scc1 3f\n\t"
        "v_cmp_ne_u32 vcc, v199, v200\n\t"
        "s_cbranch_vccnz 2b\n\t"
        "3:\n\t"
        "buffer_load_dwordx4 v[64:67], v192, s[40:43], s44 offen offset:0 sc1\n\t"
        "buffer_load_dwordx4 v[68:71], v192, s[40:43], s44 offen offset:1024 sc1\n\t"
        "buffer_load_dwordx4 v[72:75], v192, s[40:43], s44 offen offset:2048 sc1\n\t"
        "buffer_load_dwordx4 v[76:79], v192, s[40:43], s44 offen offset:3072 sc1\n\t"
        "s_add_u32 s44, s44, 0x1000\n\t"
        "s_and_b32 s44, s44, 0xffff\n\t"
        "buffer_load_dwordx4 v[80:83], v192, s[40:43], s44 offen offset:0 sc1\n\t"
        "buffer_load_dwordx4 v[84:87], v192, s[40:43], s44 offen offset:1024 sc1\n\t"
        "buffer_load_dwordx4 v[88:91], v192, s[40:43], s44 offen offset:2048 sc1\n\t"
        "buffer_load_dwordx4 v[92:95], v192, s[40:43], s44 offen offset:3072 sc1\n\t"
        "s_add_u32 s44, s44, 0x1000\n\t"
        "s_and_b32 s44, s44, 0xffff\n\t"
        "buffer_load_dwordx4 v[96:99], v192, s[40:43], s44 offen offset:0 sc1\n\t"
        "buffer_load_dwordx4 v[100:103], v192, s[40:43], s44 offen offset:1024 sc1\n\t"
        "buffer_load_dwordx4 v[104:107], v192, s[40:43], s44 offen offset:2048 sc1\n\t"
        "buffer_load_dwordx4 v[108:111], v192, s[40:43], s44 offen offset:3072 sc1\n\t"
        "s_waitcnt vmcnt(11) lgkmcnt(14)\n\t"
        "v_mfma_f32_32x32x16_bf16 v[0:15], v[32:35], v[64:67], 0\n\t"
        "ds_read_b64_tr_b16 v[32:33], v193 offset:8192\n\t"
        "ds_read_b64_tr_b16 v[34:35], v193 offset:9216\n\t"
        "s_waitcnt lgkmcnt(14)\n\t"
        "v_mfma_f32_32x32x16_bf16 v[16:31], v[36:39], v[64:67], 0\n\t"
        "ds_read_b64_tr_b16 v[36:37], v194 offset:8192\n\t"
        "ds_read_b64_tr_b16 v[38:39], v194 offset:9216\n\t"
        "s_add_u32 s44, s44, 0x1000\n\t"
        "s_and_b32 s44, s44, 0xffff\n\t"
        "buffer_load_dwordx4 v[112:115], v192, s[40:43], s44 offen offset:0 sc1\n\t"
        "s_waitcnt vmcnt(11) lgkmcnt(14)\n\t"
        "v_mfma_f32_32x32x16_bf16 v[0:15], v[40:43], v[68:71], v[0:15]\n\t"
        "ds_read_b64_tr_b16 v[40:41], v193 offset:10240\n\t"
        "ds_read_b64_tr_b16 v[42:43], v193 offset:11264\n\t"
        "s_waitcnt lgkmcnt(14)\n\t"
        "v_mfma_f32_32x32x16_bf16 v[16:31], v[44:47], v[68:71], v[16:31]\n\t"
        "ds_read_b64_tr_b16 v[44:45], v194 offset:10240\n\t"
        "ds_read_b64_tr_b16 v[46:47], v194 offset:11264\n\t"
        "buffer_load_dwordx4 v[116:119], v192, s[40:43], s44 offen offset:1024 sc1\n\t"
        "s_waitcnt vmcnt(11) lgkmcnt(14)\n\t"
        "v_mfma_f32_32x32x16_bf16 v[0:15], v[48:51], v[72:75], v[0:15]\n\t"
        "ds_read_b64_tr_b16 v[48:49], v193 offset:12288\n\t"
        "ds_read_b64_tr_b16 v[50:51], v193 offset:13312\n\t"
        "s_waitcnt lgkmcnt(14)\n\t"
        "v_mfma_f32_32x32x16_bf16 v[16:31], v[52:55], v[72:75], v[16:31]\n\t"
        "ds_read_b64_tr_b16 v[52:53], v194 offset:12288\n\t"
        "ds_read_b64_tr_b16 v[54:55], v194 offset:13312\n\t"
        "buffer_load_dwordx4 v[120:123], v192, s[40:43], s44 offen offset:2048 sc1\n\t"
        "s_waitcnt vmcnt(11) lgkmcnt(14)\n\t"
        "v_mfma_f32_32x32x16_bf16 v[0:15], v[56:59], v[76:79], v[0:15]\n\t"
        "ds_read_b64_tr_b16 v[56:57], v193 offset:14336\n\t"
        "ds_read_b64_tr_b16 v[58:59], v193 offset:15360\n\t"
        "s_waitcnt lgkmcnt(14)\n\t"
        "v_mfma_f32_32x32x16_bf16 v[16:31], v[60:63], v[76:79], v[16:31]\n\t"
        "ds_read_b64_tr_b16 v[60:61], v194 offset:14336\n\t"
        "ds_read_b64_tr_b16 v[62:63], v194 offset:15360\n\t"
        "buffer_load_dwordx4 v[124:127], v192, s[40:43], s44 offen offset:3072 sc1\n\t"
        "s_waitcnt vmcnt(11) lgkmcnt(14)\n\t"
        "v_mfma_f32_32x32x16_bf16 v[0:15], v[32:35], v[80:83], v[0:15]\n\t"
        "ds_read_b64_tr_b16 v[32:33], v193 offset:16384\n\t"
        "ds_read_b64_tr_b16 v[34:35], v193 offset:17408\n\t"
        "s_waitcnt lgkmcnt(14)\n\t"
        "v_mfma_f32_32x32x16_bf16 v[16:31], v[36:39], v[80:83], v[16:31]\n\t"
        "ds_read_b64_tr_b16 v[36:37], v194 offset:16384\n\t"
        "ds_read_b64_tr_b16 v[38:39], v194 offset:17408\n\t"
        "s_add_u32 s44, s44, 0x1000\n\t"
        "s_and_b32 s44, s44, 0xffff\n\t"
        "buffer_load_dwordx4 v[128:131], v192, s[40:43], s44 offen offset:0 sc1\n\t"
        "s_waitcnt vmcnt(11) lgkmcnt(14)\n\t"
        "v_mfma_f32_32x32x16_bf16 v[0:15], v[40:43], v[84:87], v[0:15]\n\t"
        "ds_read_b64_tr_b16 v[40:41], v193 offset:18432\n\t"
        "ds_read_b64_tr_b16 v[42:43], v193 offset:19456\n\t"
        "s_waitcnt lgkmcnt(14)\n\t"
        "v_mfma_f32_32x32x16_bf16 v[16:31], v[44:47], v[84:87], v[16:31]\n\t"
        "ds_read_b64_tr_b16 v[44:45], v194 offset:18432\n\t"
        "ds_read_b64_tr_b16 v[46:47], v194 offset:19456\n\t"
        "buffer_load_dwordx4 v[132:135], v192, s[40:43], s44 offen offset:1024 sc1\n\t"
        "s_waitcnt vmcnt(11) lgkmcnt(14)\n\t"
        "v_mfma_f32_32x32x16_bf16 v[0:15], v[48:51], v[88:91], v[0:15]\n\t"
        "ds_read_b64_tr_b16 v[48:49], v193 offset:20480\n\t"
        "ds_read_b64_tr_b16 v[50:51], v193 offset:21504\n\t"
        "s_waitcnt lgkmcnt(14)\n\t"
        "v_mfma_f32_32x32x16_bf16 v[16:31], v[52:55], v[88:91], v[16:31]\n\t"
        "ds_read_b64_tr_b16 v[52:53], v194 offset:20480\n\t"
        "ds_read_b64_tr_b16 v[54:55], v194 offset:21504\n\t"
        "buffer_load_dwordx4 v[136:139], v192, s[40:43], s44 offen offset:2048 sc1\n\t"
        "s_waitcnt vmcnt(11) lgkmcnt(14)\n\t"
        "v_mfma_f32_32x32x16_bf16 v[0:15], v[56:59], v[92:95], v[0:15]\n\t"
        "ds_read_b64_tr_b16 v[56:57], v193 offset:22528\n\t"
        "ds_read_b64_tr_b16 v[58:59], v193 offset:23552\n\t"
        "s_waitcnt lgkmcnt(14)\n\t"
        "v_mfma_f32_32x32x16_bf16 v[16:31], v[60:63], v[92:95], v[16:31]\n\t"
        "ds_read_b64_tr_b16 v[60:61], v194 offset:22528\n\t"
        "ds_read_b64_tr_b16 v[62:63], v194 offset:23552\n\t"
        "buffer_load_dwordx4 v[140:143], v192, s[40:43], s44 offen offset:3072 sc1\n\t"
        "s_waitcnt vmcnt(11) lgkmcnt(14)\n\t"
        "v_mfma_f32_32x32x16_bf16 v[0:15], v[32:35], v[96:99], v[0:15]\n\t"
        "ds_read_b64_tr_b16 v[32:33], v193 offset:24576\n\t"
        "ds_read_b64_tr_b16 v[34:35], v193 offset:25600\n\t"
        "s_waitcnt lgkmcnt(14)\n\t"
        "v_mfma_f32_32x32x16_bf16 v[16:31], v[36:39], v[96:99], v[16:31]\n\t"
        "ds_read_b64_tr_b16 v[36:37], v194 offset:24576\n\t"
        "ds_read_b64_tr_b16 v[38:39], v194 offset:25600\n\t"
        "s_add_u32 s44, s44, 0x1000\n\t"
        "s_and_b32 s44, s44, 0xffff\n\t"
        "buffer_load_dwordx4 v[144:147], v192, s[40:43], s44 offen offset:0 sc1\n\t"
        "s_waitcnt vmcnt(11) lgkmcnt(14)\n\t"
        "v_mfma_f32_32x32x16_bf16 v[0:15], v[40:43], v[100:103], v[0:15]\n\t"
        "ds_read_b64_tr_b16 v[40:41], v193 offset:26624\n\t"
        "ds_read_b64_tr_b16 v[42:43], v193 offset:27648\n\t"
        "s_waitcnt lgkmcnt(14)\n\t"
        "v_mfma_f32_32x32x16_bf16 v[16:31], v[44:47], v[100:103], v[16:31]\n\t"
        "ds_read_b64_tr_b16 v[44:45], v194 offset:26624\n\t"
        "ds_read_b64_tr_b16 v[46:47], v194 offset:27648\n\t"
        "buffer_load_dwordx4 v[148:151], v192, s[40:43], s44 offen offset:1024 sc1\n\t"
        "s_waitcnt vmcnt(11) lgkmcnt(14)\n\t"
        "v_mfma_f32_32x32x16_bf16 v[0:15], v[48:51], v[104:107], v[0:15]\n\t"
        "ds_read_b64_tr_b16 v[48:49], v193 offset:28672\n\t"
        "ds_read_b64_tr_b16 v[50:51], v193 offset:29696\n\t"
        "s_waitcnt lgkmcnt(14)\n\t"
        "v_mfma_f32_32x32x16_bf16 v[16:31], v[52:55], v[104:107], v[16:31]\n\t"
        "ds_read_b64_tr_b16 v[52:53], v194 offset:28672\n\t"
        "ds_read_b64_tr_b16 v[54:55], v194 offset:29696\n\t"
        "buffer_load_dwordx4 v[152:155], v192, s[40:43], s44 offen offset:2048 sc1\n\t"
        "s_waitcnt vmcnt(11) lgkmcnt(14)\n\t"
        "v_mfma_f32_32x32x16_bf16 v[0:15], v[56:59], v[108:111], v[0:15]\n\t"
        "ds_read_b64_tr_b16 v[56:57], v193 offset:30720\n\t"
        "ds_read_b64_tr_b16 v[58:59], v193 offset:31744\n\t"
        "s_waitcnt lgkmcnt(14)\n\t"
        "v_mfma_f32_32x32x16_bf16 v[16:31], v[60:63], v[108:111], v[16:31]\n\t"
        "ds_read_b64_tr_b16 v[60:61], v194 offset:30720\n\t"
        "ds_read_b64_tr_b16 v[62:63], v194 offset:31744\n\t"
        "buffer_load_dwordx4 v[156:159], v192, s[40:43], s44 offen offset:3072 sc1\n\t"
        "s_waitcnt vmcnt(11) lgkmcnt(14)\n\t"
        "v_mfma_f32_32x32x16_bf16 v[0:15], v[32:35], v[112:115], v[0:15]\n\t"
        "ds_read_b64_tr_b16 v[32:33], v193 offset:32768\n\t"
        "ds_read_b64_tr_b16 v[34:35], v193 offset:33792\n\t"
        "s_waitcnt lgkmcnt(14)\n\t"
        "v_mfma_f32_32x32x16_bf16 v[16:31], v[36:39], v[112:115], v[16:31]\n\t"
        "ds_read_b64_tr_b16 v[36:37], v194 offset:32768\n\t"
        "ds_read_b64_tr_b16 v[38:39], v194 offset:33792\n\t"
        "s_add_u32 s44, s44, 0x1000\n\t"
        "s_and_b32 s44, s44, 0xffff\n\t"
        "buffer_load_dwordx4 v[160:163], v192, s[40:43], s44 offen offset:0 sc1\n\t"
        "s_waitcnt vmcnt(11) lgkmcnt(14)\n\t"
        "v_mfma_f32_32x32x16_bf16 v[0:15], v[40:43], v[116:119], v[0:15]\n\t"
        "ds_read_b64_tr_b16 v[40:41], v193 offset:34816\n\t"
        "ds_read_b64_tr_b16 v[42:43], v193 offset:35840\n\t"
        "s_waitcnt lgkmcnt(14)\n\t"
        "v_mfma_f32_32x32x16_bf16 v[16:31], v[44:47], v[116:119], v[16:31]\n\t"
        "ds_read_b64_tr_b16 v[44:45], v194 offset:34816\n\t"
        "ds_read_b64_tr_b16 v[46:47], v194 offset:35840\n\t"
        "buffer_load_dwordx4 v[164:167], v192, s[40:43], s44 offen offset:1024 sc1\n\t"
        "s_waitcnt vmcnt(11) lgkmcnt(14)\n\t"
        "v_mfma_f32_32x32x16_bf16 v[0:15], v[48:51], v[120:123], v[0:15]\n\t"
        "ds_read_b64_tr_b16 v[48:49], v193 offset:36864\n\t"
        "ds_read_b64_tr_b16 v[50:51], v193 offset:37888\n\t"
        "s_waitcnt lgkmcnt(14)\n\t"
        "v_mfma_f32_32x32x16_bf16 v[16:31], v[52:55], v[120:123], v[16:31]\n\t"
        "ds_read_b64_tr_b16 v[52:53], v194 offset:36864\n\t"
        "ds_read_b64_tr_b16 v[54:55], v194 offset:37888\n\t"
        "buffer_load_dwordx4 v[168:171], v192, s[40:43], s44 offen offset:2048 sc1\n\t"
        "s_waitcnt vmcnt(11) lgkmcnt(14)\n\t"
        "v_mfma_f32_32x32x16_bf16 v[0:15], v[56:59], v[124:127], v[0:15]\n\t"
        "ds_read_b64_tr_b16 v[56:57], v193 offset:38912\n\t"
        "ds_read_b64_tr_b16 v[58:59], v193 offset:39936\n\t"
        "s_waitcnt lgkmcnt(14)\n\t"
        "v_mfma_f32_32x32x16_bf16 v[16:31], v[60:63], v[124:127], v[16:31]\n\t"
        "ds_read_b64_tr_b16 v[60:61], v194 offset:38912\n\t"
        "ds_read_b64_tr_b16 v[62:63], v194 offset:39936\n\t"
        "buffer_load_dwordx4 v[172:175], v192, s[40:43], s44 offen offset:3072 sc1\n\t"
        "s_waitcnt vmcnt(11) lgkmcnt(14)\n\t"
        "v_mfma_f32_32x32x16_bf16 v[0:15], v[32:35], v[128:131], v[0:15]\n\t"
        "ds_read_b64_tr_b16 v[32:33], v193 offset:40960\n\t"
        "ds_read_b64_tr_b16 v[34:35], v193 offset:41984\n\t"
        "s_waitcnt lgkmcnt(14)\n\t"
        "v_mfma_f32_32x32x16_bf16 v[16:31], v[36:39], v[128:131], v[16:31]\n\t"
        "ds_read_b64_tr_b16 v[36:37], v194 offset:40960\n\t"
        "ds_read_b64_tr_b16 v[38:39], v194 offset:41984\n\t"
        "s_add_u32 s44, s44, 0x1000\n\t"
        "s_and_b32 s44, s44, 0xffff\n\t"
        "buffer_load_dwordx4 v[176:179], v192, s[40:43], s44 offen offset:0 sc1\n\t"
        "s_waitcnt vmcnt(11) lgkmcnt(14)\n\t"
        "v_mfma_f32_32x32x16_bf16 v[0:15], v[40:43], v[132:135], v[0:15]\n\t"
        "ds_read_b64_tr_b16 v[40:41], v193 offset:43008\n\t"
        "ds_read_b64_tr_b16 v[42:43], v193 offset:44032\n\t"
        "s_waitcnt lgkmcnt(14)\n\t"
        "v_mfma_f32_32x32x16_bf16 v[16:31], v[44:47], v[132:135], v[16:31]\n\t"
        "ds_read_b64_tr_b16 v[44:45], v194 offset:43008\n\t"
        "ds_read_b64_tr_b16 v[46:47], v194 offset:44032\n\t"
        "buffer_load_dwordx4 v[180:183], v192, s[40:43], s44 offen offset:1024 sc1\n\t"
        "s_waitcnt vmcnt(11) lgkmcnt(14)\n\t"
        "v_mfma_f32_32x32x16_bf16 v[0:15], v[48:51], v[136:139], v[0:15]\n\t"
        "ds_read_b64_tr_b16 v[48:49], v193 offset:45056\n\t"
        "ds_read_b64_tr_b16 v[50:51], v193 offset:46080\n\t"
        "s_waitcnt lgkmcnt(14)\n\t"
        "v_mfma_f32_32x32x16_bf16 v[16:31], v[52:55], v[136:139], v[16:31]\n\t"
        "ds_read_b64_tr_b16 v[52:53], v194 offset:45056\n\t"
        "ds_read_b64_tr_b16 v[54:55], v194 offset:46080\n\t"
        "buffer_load_dwordx4 v[184:187], v192, s[40:43], s44 offen offset:2048 sc1\n\t"
        "s_waitcnt vmcnt(11) lgkmcnt(14)\n\t"
        "v_mfma_f32_32x32x16_bf16 v[0:15], v[56:59], v[140:143], v[0:15]\n\t"
        "ds_read_b64_tr_b16 v[56:57], v193 offset:47104\n\t"
        "ds_read_b64_tr_b16 v[58:59], v193 offset:48128\n\t"
        "s_waitcnt lgkmcnt(14)\n\t"
        "v_mfma_f32_32x32x16_bf16 v[16:31], v[60:63], v[140:143], v[16:31]\n\t"
        "ds_read_b64_tr_b16 v[60:61], v194 offset:47104\n\t"
        "ds_read_b64_tr_b16 v[62:63], v194 offset:48128\n\t"
        "buffer_load_dwordx4 v[188:191], v192, s[40:43], s44 offen offset:3072 sc1\n\t"
        "s_waitcnt vmcnt(11) lgkmcnt(14)\n\t"
        "v_mfma_f32_32x32x16_bf16 v[0:15], v[32:35], v[144:147], v[0:15]\n\t"
        "ds_read_b64_tr_b16 v[32:33], v193 offset:49152\n\t"
        "ds_read_b64_tr_b16 v[34:35], v193 offset:50176\n\t"
        "s_waitcnt lgkmcnt(14)\n\t"
        "v_mfma_f32_32x32x16_bf16 v[16:31], v[36:39], v[144:147], v[16:31]\n\t"
        "ds_read_b64_tr_b16 v[36:37], v194 offset:49152\n\t"
        "ds_read_b64_tr_b16 v[38:39], v194 offset:50176\n\t"
        "s_add_u32 s44, s44, 0x1000\n\t"
        "s_and_b32 s44, s44, 0xffff\n\t"
        "buffer_load_dwordx4 v[64:67], v192, s[40:43], s44 offen offset:0 sc1\n\t"
        "s_waitcnt vmcnt(11) lgkmcnt(14)\n\t"
        "v_mfma_f32_32x32x16_bf16 v[0:15], v[40:43], v[148:151], v[0:15]\n\t"
        "ds_read_b64_tr_b16 v[40:41], v193 offset:51200\n\t"
        "ds_read_b64_tr_b16 v[42:43], v193 offset:52224\n\t"
        "s_waitcnt lgkmcnt(14)\n\t"
        "v_mfma_f32_32x32x16_bf16 v[16:31], v[44:47], v[148:151], v[16:31]\n\t"
        "ds_read_b64_tr_b16 v[44:45], v194 offset:51200\n\t"
        "ds_read_b64_tr_b16 v[46:47], v194 offset:52224\n\t"
        "buffer_load_dwordx4 v[68:71], v192, s[40:43], s44 offen offset:1024 sc1\n\t"
        "s_waitcnt vmcnt(11) lgkmcnt(14)\n\t"
        "v_mfma_f32_32x32x16_bf16 v[0:15], v[48:51], v[152:155], v[0:15]\n\t"
        "ds_read_b64_tr_b16 v[48:49], v193 offset:53248\n\t"
        "ds_read_b64_tr_b16 v[50:51], v193 offset:54272\n\t"
        "s_waitcnt lgkmcnt(14)\n\t"
        "v_mfma_f32_32x32x16_bf16 v[16:31], v[52:55], v[152:155], v[16:31]\n\t"
        "ds_read_b64_tr_b16 v[52:53], v194 offset:53248\n\t"
        "ds_read_b64_tr_b16 v[54:55], v194 offset:54272\n\t"
        "buffer_load_dwordx4 v[72:75], v192, s[40:43], s44 offen offset:2048 sc1\n\t"
        "s_waitcnt vmcnt(11) lgkmcnt(14)\n\t"
        "v_mfma_f32_32x32x16_bf16 v[0:15], v[56:59], v[156:159], v[0:15]\n\t"
        "ds_read_b64_tr_b16 v[56:57], v193 offset:55296\n\t"
        "ds_read_b64_tr_b16 v[58:59], v193 offset:56320\n\t"
        "s_waitcnt lgkmcnt(14)\n\t"
        "v_mfma_f32_32x32x16_bf16 v[16:31], v[60:63], v[156:159], v[16:31]\n\t"
        "ds_read_b64_tr_b16 v[60:61], v194 offset:55296\n\t"
        "ds_read_b64_tr_b16 v[62:63], v194 offset:56320\n\t"
        "buffer_load_dwordx4 v[76:79], v192, s[40:43], s44 offen offset:3072 sc1\n\t"
        "s_waitcnt vmcnt(11) lgkmcnt(14)\n\t"
        "v_mfma_f32_32x32x16_bf16 v[0:15], v[32:35], v[160:163], v[0:15]\n\t"
        "ds_read_b64_tr_b16 v[32:33], v193 offset:57344\n\t"
        "ds_read_b64_tr_b16 v[34:35], v193 offset:58368\n\t"
        "s_waitcnt lgkmcnt(14)\n\t"
        "v_mfma_f32_32x32x16_bf16 v[16:31], v[36:39], v[160:163], v[16:31]\n\t"
        "ds_read_b64_tr_b16 v[36:37], v194 offset:57344\n\t"
        "ds_read_b64_tr_b16 v[38:39], v194 offset:58368\n\t"
        "s_add_u32 s44, s44, 0x1000\n\t"
        "s_and_b32 s44, s44, 0xffff\n\t"
        "buffer_load_dwordx4 v[80:83], v192, s[40:43], s44 offen offset:0 sc1\n\t"
        "s_waitcnt vmcnt(11) lgkmcnt(14)\n\t"
        "v_mfma_f32_32x32x16_bf16 v[0:15], v[40:43], v[164:167], v[0:15]\n\t"
        "ds_read_b64_tr_b16 v[40:41], v193 offset:59392\n\t"
        "ds_read_b64_tr_b16 v[42:43], v193 offset:60416\n\t"
        "s_waitcnt lgkmcnt(14)\n\t"
        "v_mfma_f32_32x32x16_bf16 v[16:31], v[44:47], v[164:167], v[16:31]\n\t"
        "ds_read_b64_tr_b16 v[44:45], v194 offset:59392\n\t"
        "ds_read_b64_tr_b16 v[46:47], v194 offset:60416\n\t"
        "buffer_load_dwordx4 v[84:87], v192, s[40:43], s44 offen offset:1024 sc1\n\t"
        "s_waitcnt vmcnt(11) lgkmcnt(14)\n\t"
        "v_mfma_f32_32x32x16_bf16 v[0:15], v[48:51], v[168:171], v[0:15]\n\t"
        "ds_read_b64_tr_b16 v[48:49], v193 offset:61440\n\t"
        "ds_read_b64_tr_b16 v[50:51], v193 offset:62464\n\t"
        "s_waitcnt lgkmcnt(14)\n\t"
        "v_mfma_f32_32x32x16_bf16 v[16:31], v[52:55], v[168:171], v[16:31]\n\t"
        "ds_read_b64_tr_b16 v[52:53], v194 offset:61440\n\t"
        "ds_read_b64_tr_b16 v[54:55], v194 offset:62464\n\t"
        "buffer_load_dwordx4 v[88:91], v192, s[40:43], s44 offen offset:2048 sc1\n\t"
        "s_waitcnt vmcnt(11) lgkmcnt(14)\n\t"
        "v_mfma_f32_32x32x16_bf16 v[0:15], v[56:59], v[172:175], v[0:15]\n\t"
        "ds_read_b64_tr_b16 v[56:57], v193 offset:63488\n\t"
        "ds_read_b64_tr_b16 v[58:59], v193 offset:64512\n\t"
        "s_waitcnt lgkmcnt(14)\n\t"
        "v_mfma_f32_32x32x16_bf16 v[16:31], v[60:63], v[172:175], v[16:31]\n\t"
        "ds_read_b64_tr_b16 v[60:61], v194 offset:63488\n\t"
        "ds_read_b64_tr_b16 v[62:63], v194 offset:64512\n\t"
        "buffer_load_dwordx4 v[92:95], v192, s[40:43], s44 offen offset:3072 sc1\n\t"
        "s_waitcnt vmcnt(11) lgkmcnt(14)\n\t"
        "v_mfma_f32_32x32x16_bf16 v[0:15], v[32:35], v[176:179], v[0:15]\n\t"
        "ds_read_b64_tr_b16 v[32:33], v195 offset:0\n\t"
        "ds_read_b64_tr_b16 v[34:35], v195 offset:1024\n\t"
        "s_waitcnt lgkmcnt(14)\n\t"
        "v_mfma_f32_32x32x16_bf16 v[16:31], v[36:39], v[176:179], v[16:31]\n\t"
        "ds_read_b64_tr_b16 v[36:37], v196 offset:0\n\t"
        "ds_read_b64_tr_b16 v[38:39], v196 offset:1024\n\t"
        "s_add_u32 s44, s44, 0x1000\n\t"
        "s_and_b32 s44, s44, 0xffff\n\t"
        "buffer_load_dwordx4 v[96:99], v192, s[40:43], s44 offen offset:0 sc1\n\t"
        "s_waitcnt vmcnt(11) lgkmcnt(14)\n\t"
        "v_mfma_f32_32x32x16_bf16 v[0:15], v[40:43], v[180:183], v[0:15]\n\t"
        "ds_read_b64_tr_b16 v[40:41], v195 offset:2048\n\t"
        "ds_read_b64_tr_b16 v[42:43], v195 offset:3072\n\t"
        "s_waitcnt lgkmcnt(14)\n\t"
        "v_mfma_f32_32x32x16_bf16 v[16:31], v[44:47], v[180:183], v[16:31]\n\t"
        "ds_read_b64_tr_b16 v[44:45], v196 offset:2048\n\t"
        "ds_read_b64_tr_b16 v[46:47], v196 offset:3072\n\t"
        "buffer_load_dwordx4 v[100:103], v192, s[40:43], s44 offen offset:1024 sc1\n\t"
        "s_waitcnt vmcnt(11) lgkmcnt(14)\n\t"
        "v_mfma_f32_32x32x16_bf16 v[0:15], v[48:51], v[184:187], v[0:15]\n\t"
        "ds_read_b64_tr_b16 v[48:49], v195 offset:4096\n\t"
        "ds_read_b64_tr_b16 v[50:51], v195 offset:5120\n\t"
        "s_waitcnt lgkmcnt(14)\n\t"
        "v_mfma_f32_32x32x16_bf16 v[16:31], v[52:55], v[184:187], v[16:31]\n\t"
        "ds_read_b64_tr_b16 v[52:53], v196 offset:4096\n\t"
        "ds_read_b64_tr_b16 v[54:55], v196 offset:5120\n\t"
        "buffer_load_dwordx4 v[104:107], v192, s[40:43], s44 offen offset:2048 sc1\n\t"
        "s_waitcnt vmcnt(11) lgkmcnt(14)\n\t"
        "v_mfma_f32_32x32x16_bf16 v[0:15], v[56:59], v[188:191], v[0:15]\n\t"
        "ds_read_b64_tr_b16 v[56:57], v195 offset:6144\n\t"
        "ds_read_b64_tr_b16 v[58:59], v195 offset:7168\n\t"
        "s_waitcnt lgkmcnt(14)\n\t"
        "v_mfma_f32_32x32x16_bf16 v[16:31], v[60:63], v[188:191], v[16:31]\n\t"
        "ds_read_b64_tr_b16 v[60:61], v196 offset:6144\n\t"
        "ds_read_b64_tr_b16 v[62:63], v196 offset:7168\n\t"
        "buffer_load_dwordx4 v[108:111], v192, s[40:43], s44 offen offset:3072 sc1\n\t"
        "s_waitcnt vmcnt(11) lgkmcnt(14)\n\t"
        "v_mfma_f32_32x32x16_bf16 v[0:15], v[32:35], v[64:67], v[0:15]\n\t"
        "ds_read_b64_tr_b16 v[32:33], v195 offset:8192\n\t"
        "ds_read_b64_tr_b16 v[34:35], v195 offset:9216\n\t"
        "s_waitcnt lgkmcnt(14)\n\t"
        "v_mfma_f32_32x32x16_bf16 v[16:31], v[36:39], v[64:67], v[16:31]\n\t"
        "ds_read_b64_tr_b16 v[36:37], v196 offset:8192\n\t"
        "ds_read_b64_tr_b16 v[38:39], v196 offset:9216\n\t"
        "s_add_u32 s44, s44, 0x1000\n\t"
        "s_and_b32 s44, s44, 0xffff\n\t"
        "buffer_load_dwordx4 v[112:115], v192, s[40:43], s44 offen offset:0 sc1\n\t"
        "s_waitcnt vmcnt(11) lgkmcnt(14)\n\t"
        "v_mfma_f32_32x32x16_bf16 v[0:15], v[40:43], v[68:71], v[0:15]\n\t"
        "ds_read_b64_tr_b16 v[40:41], v195 offset:10240\n\t"
        "ds_read_b64_tr_b16 v[42:43], v195 offset:11264\n\t"
        "s_waitcnt lgkmcnt(14)\n\t"
        "v_mfma_f32_32x32x16_bf16 v[16:31], v[44:47], v[68:71], v[16:31]\n\t"
        "ds_read_b64_tr_b16 v[44:45], v196 offset:10240\n\t"
        "ds_read_b64_tr_b16 v[46:47], v196 offset:11264\n\t"
        "buffer_load_dwordx4 v[116:119], v192, s[40:43], s44 offen offset:1024 sc1\n\t"
        "s_waitcnt vmcnt(11) lgkmcnt(14)\n\t"
        "v_mfma_f32_32x32x16_bf16 v[0:15], v[48:51], v[72:75], v[0:15]\n\t"
        "ds_read_b64_tr_b16 v[48:49], v195 offset:12288\n\t"
        "ds_read_b64_tr_b16 v[50:51], v195 offset:13312\n\t"
        "s_waitcnt lgkmcnt(14)\n\t"
        "v_mfma_f32_32x32x16_bf16 v[16:31], v[52:55], v[72:75], v[16:31]\n\t"
        "ds_read_b64_tr_b16 v[52:53], v196 offset:12288\n\t"
        "ds_read_b64_tr_b16 v[54:55], v196 offset:13312\n\t"
        "buffer_load_dwordx4 v[120:123], v192, s[40:43], s44 offen offset:2048 sc1\n\t"
        "s_waitcnt vmcnt(11) lgkmcnt(14)\n\t"
        "v_mfma_f32_32x32x16_bf16 v[0:15], v[56:59], v[76:79], v[0:15]\n\t"
        "ds_read_b64_tr_b16 v[56:57], v195 offset:14336\n\t"
        "ds_read_b64_tr_b16 v[58:59], v195 offset:15360\n\t"
        "s_waitcnt lgkmcnt(14)\n\t"
        "v_mfma_f32_32x32x16_bf16 v[16:31], v[60:63], v[76:79], v[16:31]\n\t"
        "ds_read_b64_tr_b16 v[60:61], v196 offset:14336\n\t"
        "ds_read_b64_tr_b16 v[62:63], v196 offset:15360\n\t"
        "buffer_load_dwordx4 v[124:127], v192, s[40:43], s44 offen offset:3072 sc1\n\t"
        "s_waitcnt vmcnt(11) lgkmcnt(14)\n\t"
        "v_mfma_f32_32x32x16_bf16 v[0:15], v[32:35], v[80:83], v[0:15]\n\t"
        "ds_read_b64_tr_b16 v[32:33], v195 offset:16384\n\t"
        "ds_read_b64_tr_b16 v[34:35], v195 offset:17408\n\t"
        "s_waitcnt lgkmcnt(14)\n\t"
        "v_mfma_f32_32x32x16_bf16 v[16:31], v[36:39], v[80:83], v[16:31]\n\t"
        "ds_read_b64_tr_b16 v[36:37], v196 offset:16384\n\t"
        "ds_read_b64_tr_b16 v[38:39], v196 offset:17408\n\t"
        "s_add_u32 s44, s44, 0x1000\n\t"
        "s_and_b32 s44, s44, 0xffff\n\t"
        "buffer_load_dwordx4 v[128:131], v192, s[40:43], s44 offen offset:0 sc1\n\t"
        "s_waitcnt vmcnt(11) lgkmcnt(14)\n\t"
        "v_mfma_f32_32x32x16_bf16 v[0:15], v[40:43], v[84:87], v[0:15]\n\t"
        "ds_read_b64_tr_b16 v[40:41], v195 offset:18432\n\t"
        "ds_read_b64_tr_b16 v[42:43], v195 offset:19456\n\t"
        "s_waitcnt lgkmcnt(14)\n\t"
        "v_mfma_f32_32x32x16_bf16 v[16:31], v[44:47], v[84:87], v[16:31]\n\t"
        "ds_read_b64_tr_b16 v[44:45], v196 offset:18432\n\t"
        "ds_read_b64_tr_b16 v[46:47], v196 offset:19456\n\t"
        "buffer_load_dwordx4 v[132:135], v192, s[40:43], s44 offen offset:1024 sc1\n\t"
        "s_waitcnt vmcnt(11) lgkmcnt(14)\n\t"
        "v_mfma_f32_32x32x16_bf16 v[0:15], v[48:51], v[88:91], v[0:15]\n\t"
        "ds_read_b64_tr_b16 v[48:49], v195 offset:20480\n\t"
        "ds_read_b64_tr_b16 v[50:51], v195 offset:21504\n\t"
        "s_waitcnt lgkmcnt(14)\n\t"
        "v_mfma_f32_32x32x16_bf16 v[16:31], v[52:55], v[88:91], v[16:31]\n\t"
        "ds_read_b64_tr_b16 v[52:53], v196 offset:20480\n\t"
        "ds_read_b64_tr_b16 v[54:55], v196 offset:21504\n\t"
        "buffer_load_dwordx4 v[136:139], v192, s[40:43], s44 offen offset:2048 sc1\n\t"
        "s_waitcnt vmcnt(11) lgkmcnt(14)\n\t"
        "v_mfma_f32_32x32x16_bf16 v[0:15], v[56:59], v[92:95], v[0:15]\n\t"
        "ds_read_b64_tr_b16 v[56:57], v195 offset:22528\n\t"
        "ds_read_b64_tr_b16 v[58:59], v195 offset:23552\n\t"
        "s_waitcnt lgkmcnt(14)\n\t"
        "v_mfma_f32_32x32x16_bf16 v[16:31], v[60:63], v[92:95], v[16:31]\n\t"
        "ds_read_b64_tr_b16 v[60:61], v196 offset:22528\n\t"
        "ds_read_b64_tr_b16 v[62:63], v196 offset:23552\n\t"
        "buffer_load_dwordx4 v[140:143], v192, s[40:43], s44 offen offset:3072 sc1\n\t"
        "s_waitcnt vmcnt(11) lgkmcnt(14)\n\t"
        "v_mfma_f32_32x32x16_bf16 v[0:15], v[32:35], v[96:99], v[0:15]\n\t"
        "ds_read_b64_tr_b16 v[32:33], v195 offset:24576\n\t"
        "ds_read_b64_tr_b16 v[34:35], v195 offset:25600\n\t"
        "s_waitcnt lgkmcnt(14)\n\t"
        "v_mfma_f32_32x32x16_bf16 v[16:31], v[36:39], v[96:99], v[16:31]\n\t"
        "ds_read_b64_tr_b16 v[36:37], v196 offset:24576\n\t"
        "ds_read_b64_tr_b16 v[38:39], v196 offset:25600\n\t"
        "s_add_u32 s44, s44, 0x1000\n\t"
        "s_and_b32 s44, s44, 0xffff\n\t"
        "buffer_load_dwordx4 v[144:147], v192, s[40:43], s44 offen offset:0 sc1\n\t"
        "s_waitcnt vmcnt(11) lgkmcnt(14)\n\t"
        "v_mfma_f32_32x32x16_bf16 v[0:15], v[40:43], v[100:103], v[0:15]\n\t"
        "ds_read_b64_tr_b16 v[40:41], v195 offset:26624\n\t"
        "ds_read_b64_tr_b16 v[42:43], v195 offset:27648\n\t"
        "s_waitcnt lgkmcnt(14)\n\t"
        "v_mfma_f32_32x32x16_bf16 v[16:31], v[44:47], v[100:103], v[16:31]\n\t"
        "ds_read_b64_tr_b16 v[44:45], v196 offset:26624\n\t"
        "ds_read_b64_tr_b16 v[46:47], v196 offset:27648\n\t"
        "buffer_load_dwordx4 v[148:151], v192, s[40:43], s44 offen offset:1024 sc1\n\t"
        "s_waitcnt vmcnt(11) lgkmcnt(14)\n\t"
        "v_mfma_f32_32x32x16_bf16 v[0:15], v[48:51], v[104:107], v[0:15]\n\t"
        "ds_read_b64_tr_b16 v[48:49], v195 offset:28672\n\t"
        "ds_read_b64_tr_b16 v[50:51], v195 offset:29696\n\t"
        "s_waitcnt lgkmcnt(14)\n\t"
        "v_mfma_f32_32x32x16_bf16 v[16:31], v[52:55], v[104:107], v[16:31]\n\t"
        "ds_read_b64_tr_b16 v[52:53], v196 offset:28672\n\t"
        "ds_read_b64_tr_b16 v[54:55], v196 offset:29696\n\t"
        "buffer_load_dwordx4 v[152:155], v192, s[40:43], s44 offen offset:2048 sc1\n\t"
        "s_waitcnt vmcnt(11) lgkmcnt(14)\n\t"
        "v_mfma_f32_32x32x16_bf16 v[0:15], v[56:59], v[108:111], v[0:15]\n\t"
        "ds_read_b64_tr_b16 v[56:57], v195 offset:30720\n\t"
        "ds_read_b64_tr_b16 v[58:59], v195 offset:31744\n\t"
        "s_waitcnt lgkmcnt(14)\n\t"
        "v_mfma_f32_32x32x16_bf16 v[16:31], v[60:63], v[108:111], v[16:31]\n\t"
        "ds_read_b64_tr_b16 v[60:61], v196 offset:30720\n\t"
        "ds_read_b64_tr_b16 v[62:63], v196 offset:31744\n\t"
        "buffer_load_dwordx4 v[156:159], v192, s[40:43], s44 offen offset:3072 sc1\n\t"
        "s_waitcnt vmcnt(11) lgkmcnt(14)\n\t"
        "v_mfma_f32_32x32x16_bf16 v[0:15], v[32:35], v[112:115], v[0:15]\n\t"
        "ds_read_b64_tr_b16 v[32:33], v195 offset:32768\n\t"
        "ds_read_b64_tr_b16 v[34:35], v195 offset:33792\n\t"
        "s_waitcnt lgkmcnt(14)\n\t"
        "v_mfma_f32_32x32x16_bf16 v[16:31], v[36:39], v[112:115], v[16:31]\n\t"
        "ds_read_b64_tr_b16 v[36:37], v196 offset:32768\n\t"
        "ds_read_b64_tr_b16 v[38:39], v196 offset:33792\n\t"
        "s_add_u32 s44, s44, 0x1000\n\t"
        "s_and_b32 s44, s44, 0xffff\n\t"
        "buffer_load_dwordx4 v[160:163], v192, s[40:43], s44 offen offset:0 sc1\n\t"
        "s_waitcnt vmcnt(11) lgkmcnt(14)\n\t"
        "v_mfma_f32_32x32x16_bf16 v[0:15], v[40:43], v[116:119], v[0:15]\n\t"
        "ds_read_b64_tr_b16 v[40:41], v195 offset:34816\n\t"
        "ds_read_b64_tr_b16 v[42:43], v195 offset:35840\n\t"
        "s_waitcnt lgkmcnt(14)\n\t"
        "v_mfma_f32_32x32x16_bf16 v[16:31], v[44:47], v[116:119], v[16:31]\n\t"
        "ds_read_b64_tr_b16 v[44:45], v196 offset:34816\n\t"
        "ds_read_b64_tr_b16 v[46:47], v196 offset:35840\n\t"
        "buffer_load_dwordx4 v[164:167], v192, s[40:43], s44 offen offset:1024 sc1\n\t"
        "s_waitcnt vmcnt(11) lgkmcnt(14)\n\t"
        "v_mfma_f32_32x32x16_bf16 v[0:15], v[48:51], v[120:123], v[0:15]\n\t"
        "ds_read_b64_tr_b16 v[48:49], v195 offset:36864\n\t"
        "ds_read_b64_tr_b16 v[50:51], v195 offset:37888\n\t"
        "s_waitcnt lgkmcnt(14)\n\t"
        "v_mfma_f32_32x32x16_bf16 v[16:31], v[52:55], v[120:123], v[16:31]\n\t"
        "ds_read_b64_tr_b16 v[52:53], v196 offset:36864\n\t"
        "ds_read_b64_tr_b16 v[54:55], v196 offset:37888\n\t"
        "buffer_load_dwordx4 v[168:171], v192, s[40:43], s44 offen offset:2048 sc1\n\t"
        "s_waitcnt vmcnt(11) lgkmcnt(14)\n\t"
        "v_mfma_f32_32x32x16_bf16 v[0:15], v[56:59], v[124:127], v[0:15]\n\t"
        "ds_read_b64_tr_b16 v[56:57], v195 offset:38912\n\t"
        "ds_read_b64_tr_b16 v[58:59], v195 offset:39936\n\t"
        "s_waitcnt lgkmcnt(14)\n\t"
        "v_mfma_f32_32x32x16_bf16 v[16:31], v[60:63], v[124:127], v[16:31]\n\t"
        "ds_read_b64_tr_b16 v[60:61], v196 offset:38912\n\t"
        "ds_read_b64_tr_b16 v[62:63], v196 offset:39936\n\t"
        "buffer_load_dwordx4 v[172:175], v192, s[40:43], s44 offen offset:3072 sc1\n\t"
        "s_waitcnt vmcnt(11) lgkmcnt(14)\n\t"
        "v_mfma_f32_32x32x16_bf16 v[0:15], v[32:35], v[128:131], v[0:15]\n\t"
        "ds_read_b64_tr_b16 v[32:33], v195 offset:40960\n\t"
        "ds_read_b64_tr_b16 v[34:35], v195 offset:41984\n\t"
        "s_waitcnt lgkmcnt(14)\n\t"
        "v_mfma_f32_32x32x16_bf16 v[16:31], v[36:39], v[128:131], v[16:31]\n\t"
        "ds_read_b64_tr_b16 v[36:37], v196 offset:40960\n\t"
        "ds_read_b64_tr_b16 v[38:39], v196 offset:41984\n\t"
        "s_add_u32 s44, s44, 0x1000\n\t"
        "s_and_b32 s44, s44, 0xffff\n\t"
        "buffer_load_dwordx4 v[176:179], v192, s[40:43], s44 offen offset:0 sc1\n\t"
        "s_waitcnt vmcnt(11) lgkmcnt(14)\n\t"
        "v_mfma_f32_32x32x16_bf16 v[0:15], v[40:43], v[132:135], v[0:15]\n\t"
        "ds_read_b64_tr_b16 v[40:41], v195 offset:43008\n\t"
        "ds_read_b64_tr_b16 v[42:43], v195 offset:44032\n\t"
        "s_waitcnt lgkmcnt(14)\n\t"
        "v_mfma_f32_32x32x16_bf16 v[16:31], v[44:47], v[132:135], v[16:31]\n\t"
        "ds_read_b64_tr_b16 v[44:45], v196 offset:43008\n\t"
        "ds_read_b64_tr_b16 v[46:47], v196 offset:44032\n\t"
        "buffer_load_dwordx4 v[180:183], v192, s[40:43], s44 offen offset:1024 sc1\n\t"
        "s_waitcnt vmcnt(11) lgkmcnt(14)\n\t"
        "v_mfma_f32_32x32x16_bf16 v[0:15], v[48:51], v[136:139], v[0:15]\n\t"
        "ds_read_b64_tr_b16 v[48:49], v195 offset:45056\n\t"
        "ds_read_b64_tr_b16 v[50:51], v195 offset:46080\n\t"
        "s_waitcnt lgkmcnt(14)\n\t"
        "v_mfma_f32_32x32x16_bf16 v[16:31], v[52:55], v[136:139], v[16:31]\n\t"
        "ds_read_b64_tr_b16 v[52:53], v196 offset:45056\n\t"
        "ds_read_b64_tr_b16 v[54:55], v196 offset:46080\n\t"
        "buffer_load_dwordx4 v[184:187], v192, s[40:43], s44 offen offset:2048 sc1\n\t"
        "s_waitcnt vmcnt(11) lgkmcnt(14)\n\t"
        "v_mfma_f32_32x32x16_bf16 v[0:15], v[56:59], v[140:143], v[0:15]\n\t"
        "ds_read_b64_tr_b16 v[56:57], v195 offset:47104\n\t"
        "ds_read_b64_tr_b16 v[58:59], v195 offset:48128\n\t"
        "s_waitcnt lgkmcnt(14)\n\t"
        "v_mfma_f32_32x32x16_bf16 v[16:31], v[60:63], v[140:143], v[16:31]\n\t"
        "ds_read_b64_tr_b16 v[60:61], v196 offset:47104\n\t"
        "ds_read_b64_tr_b16 v[62:63], v196 offset:48128\n\t"
        "buffer_load_dwordx4 v[188:191], v192, s[40:43], s44 offen offset:3072 sc1\n\t"
        "s_waitcnt vmcnt(11) lgkmcnt(14)\n\t"
        "v_mfma_f32_32x32x16_bf16 v[0:15], v[32:35], v[144:147], v[0:15]\n\t"
        "ds_read_b64_tr_b16 v[32:33], v195 offset:49152\n\t"
        "ds_read_b64_tr_b16 v[34:35], v195 offset:50176\n\t"
        "s_waitcnt lgkmcnt(14)\n\t"
        "v_mfma_f32_32x32x16_bf16 v[16:31], v[36:39], v[144:147], v[16:31]\n\t"
        "ds_read_b64_tr_b16 v[36:37], v196 offset:49152\n\t"
        "ds_read_b64_tr_b16 v[38:39], v196 offset:50176\n\t"
        "s_waitcnt vmcnt(10) lgkmcnt(14)\n\t"
        "v_mfma_f32_32x32x16_bf16 v[0:15], v[40:43], v[148:151], v[0:15]\n\t"
        "ds_read_b64_tr_b16 v[40:41], v195 offset:51200\n\t"
        "ds_read_b64_tr_b16 v[42:43], v195 offset:52224\n\t"
        "s_waitcnt lgkmcnt(14)\n\t"
        "v_mfma_f32_32x32x16_bf16 v[16:31], v[44:47], v[148:151], v[16:31]\n\t"
        "ds_read_b64_tr_b16 v[44:45], v196 offset:51200\n\t"
        "ds_read_b64_tr_b16 v[46:47], v196 offset:52224\n\t"
        "s_waitcnt vmcnt(9) lgkmcnt(14)\n\t"
        "v_mfma_f32_32x32x16_bf16 v[0:15], v[48:51], v[152:155], v[0:15]\n\t"
        "ds_read_b64_tr_b16 v[48:49], v195 offset:53248\n\t"
        "ds_read_b64_tr_b16 v[50:51], v195 offset:54272\n\t"
        "s_waitcnt lgkmcnt(14)\n\t"
        "v_mfma_f32_32x32x16_bf16 v[16:31], v[52:55], v[152:155], v[16:31]\n\t"
        "ds_read_b64_tr_b16 v[52:53], v196 offset:53248\n\t"
        "ds_read_b64_tr_b16 v[54:55], v196 offset:54272\n\t"
        "s_waitcnt vmcnt(8) lgkmcnt(14)\n\t"
        "v_mfma_f32_32x32x16_bf16 v[0:15], v[56:59], v[156:159], v[0:15]\n\t"
        "ds_read_b64_tr_b16 v[56:57], v195 offset:55296\n\t"
        "ds_read_b64_tr_b16 v[58:59], v195 offset:56320\n\t"
        "s_waitcnt lgkmcnt(14)\n\t"
        "v_mfma_f32_32x32x16_bf16 v[16:31], v[60:63], v[156:159], v[16:31]\n\t"
        "ds_read_b64_tr_b16 v[60:61], v196 offset:55296\n\t"
        "ds_read_b64_tr_b16 v[62:63], v196 offset:56320\n\t"
        "s_waitcnt vmcnt(7) lgkmcnt(14)\n\t"
        "v_mfma_f32_32x32x16_bf16 v[0:15], v[32:35], v[160:163], v[0:15]\n\t"
        "s_barrier\n\t"
        "ds_read_b64_tr_b16 v[32:33], v195 offset:57344\n\t"
        "ds_read_b64_tr_b16 v[34:35], v195 offset:58368\n\t"
        "s_waitcnt lgkmcnt(14)\n\t"
        "v_mfma_f32_32x32x16_bf16 v[16:31], v[36:39], v[160:163], v[16:31]\n\t"
        "ds_read_b64_tr_b16 v[36:37], v196 offset:57344\n\t"
        "ds_read_b64_tr_b16 v[38:39], v196 offset:58368\n\t"
        "s_waitcnt vmcnt(6) lgkmcnt(14)\n\t"
        "v_mfma_f32_32x32x16_bf16 v[0:15], v[40:43], v[164:167], v[0:15]\n\t"
        "ds_read_b64_tr_b16 v[40:41], v195 offset:59392\n\t"
        "ds_read_b64_tr_b16 v[42:43], v195 offset:60416\n\t"
        "s_waitcnt lgkmcnt(14)\n\t"
        "v_mfma_f32_32x32x16_bf16 v[16:31], v[44:47], v[164:167], v[16:31]\n\t"
        "ds_read_b64_tr_b16 v[44:45], v196 offset:59392\n\t"
        "ds_read_b64_tr_b16 v[46:47], v196 offset:60416\n\t"
        "s_waitcnt vmcnt(5) lgkmcnt(14)\n\t"
        "v_mfma_f32_32x32x16_bf16 v[0:15], v[48:51], v[168:171], v[0:15]\n\t"
        "ds_read_b64_tr_b16 v[48:49], v195 offset:61440\n\t"
        "ds_read_b64_tr_b16 v[50:51], v195 offset:62464\n\t"
        "s_waitcnt lgkmcnt(14)\n\t"
        "v_mfma_f32_32x32x16_bf16 v[16:31], v[52:55], v[168:171], v[16:31]\n\t"
        "ds_read_b64_tr_b16 v[52:53], v196 offset:61440\n\t"
        "ds_read_b64_tr_b16 v[54:55], v196 offset:62464\n\t"
        "s_waitcnt vmcnt(4) lgkmcnt(14)\n\t"
        "v_mfma_f32_32x32x16_bf16 v[0:15], v[56:59], v[172:175], v[0:15]\n\t"
        "ds_read_b64_tr_b16 v[56:57], v195 offset:63488\n\t"
        "ds_read_b64_tr_b16 v[58:59], v195 offset:64512\n\t"
        "s_waitcnt lgkmcnt(14)\n\t"
        "v_mfma_f32_32x32x16_bf16 v[16:31], v[60:63], v[172:175], v[16:31]\n\t"
        "ds_read_b64_tr_b16 v[60:61], v196 offset:63488\n\t"
        "ds_read_b64_tr_b16 v[62:63], v196 offset:64512\n\t"
        "s_waitcnt vmcnt(3) lgkmcnt(14)\n\t"
        "v_mfma_f32_32x32x16_bf16 v[0:15], v[32:35], v[176:179], v[0:15]\n\t"
        "s_waitcnt lgkmcnt(12)\n\t"
        "v_mfma_f32_32x32x16_bf16 v[16:31], v[36:39], v[176:179], v[16:31]\n\t"
        "s_waitcnt vmcnt(2) lgkmcnt(10)\n\t"
        "v_mfma_f32_32x32x16_bf16 v[0:15], v[40:43], v[180:183], v[0:15]\n\t"
        "s_waitcnt lgkmcnt(8)\n\t"
        "v_mfma_f32_32x32x16_bf16 v[16:31], v[44:47], v[180:183], v[16:31]\n\t"
        "s_waitcnt vmcnt(1) lgkmcnt(6)\n\t"
        "v_mfma_f32_32x32x16_bf16 v[0:15], v[48:51], v[184:187], v[0:15]\n\t"
        "s_waitcnt lgkmcnt(4)\n\t"
        "v_mfma_f32_32x32x16_bf16 v[16:31], v[52:55], v[184:187], v[16:31]\n\t"
        "s_waitcnt vmcnt(0) lgkmcnt(2)\n\t"
        "v_mfma_f32_32x32x16_bf16 v[0:15], v[56:59], v[188:191], v[0:15]\n\t"
        "s_waitcnt lgkmcnt(0)\n\t"
        "v_mfma_f32_32x32x16_bf16 v[16:31], v[60:63], v[188:191], v[16:31]\n\t"
        "s_nop 11\n\t"
        : "={v[0:15]}"(acc[0]), "={v[16:31]}"(acc[1]), "+{s44}"(soff) STAMP_OUTS
        : "{v192}"(OFF_OF + L * (5 * MBY) + tile * TS + lane * 16), "{v193}"(a0), "{v194}"(a1), "{v195}"(a0 + 65536), "{v196}"(a1 + 65536),
          "{v198}"(lbase + L_W + 20), "{v199}"(L + 1), "{s[40:43]}"(srd)
        : "v32", "v33", "v34", "v35", "v36", "v37", "v38", "v39", "v40", "v41", "v42", "v43", "v44", "v45", "v46", "v47", "v48", "v49", "v50", "v51", "v52", "v53", "v54", "v55", "v56", "v57", "v58", "v59", "v60", "v61", "v62", "v63", "v64", "v65", "v66", "v67", "v68", "v69", "v70", "v71", "v72", "v73", "v74", "v75", "v76", "v77", "v78", "v79", "v80", "v81", "v82", "v83", "v84", "v85", "v86", "v87", "v88", "v89", "v90", "v91", "v92", "v93", "v94", "v95", "v96", "v97", "v98", "v99", "v100", "v101", "v102", "v103", "v104", "v105", "v106", "v107", "v108", "v109", "v110", "v111", "v112", "v113", "v114", "v115", "v116", "v117", "v118", "v119", "v120", "v121", "v122", "v123", "v124", "v125", "v126", "v127", "v128", "v129", "v130", "v131", "v132", "v133", "v134", "v135", "v136", "v137", "v138", "v139", "v140", "v141", "v142", "v143", "v144", "v145", "v146", "v147", "v148", "v149", "v150", "v151", "v152", "v153", "v154", "v155", "v156", "v157", "v158", "v159", "v160", "v161", "v162", "v163", "v164", "v165", "v166", "v167", "v168", "v169", "v170", "v171", "v172", "v173", "v174", "v175", "v176", "v177", "v178", "v179", "v180", "v181", "v182", "v183", "v184", "v185", "v186", "v187", "v188", "v189", "v190", "v191", "v200", "s45", "vcc", "scc", "memory");
      }
      PHASE_VARS;
      STAMP_PUT(12 + 16 * L);
      STAMPV(15 + 16 * L);
      float s = 0.f, sq = 0.f, d = 0.f;
      const unsigned xdst = (L ? OOB : OFF_X1 + tile * TS + tn * 4096) + lane * 16;
#pragma unroll
      for (int g = 0; g < 8; ++g) {
#pragma unroll
        for (int e = 0; e < 2; ++e) {
          const unsigned rr = res[g >> 1][2 * (g & 1) + e];
          const unsigned t = pack2(acc[g >> 2][4 * (g & 3) + 2 * e], acc[g >> 2][4 * (g & 3) + 2 * e + 1]);
          x1p[g >> 1][2 * (g & 1) + e] = pack2(lo16(rr) + lo16(t), hi16(rr) + hi16(t));
        }
        if (g & 1) { stg(rs, xdst + (g >> 1) * 1024, x1p[g >> 1]); SBAR(); }
      }
#pragma unroll
      for (int g = 0; g < 8; ++g)
#pragma unroll
        for (int e = 0; e < 2; ++e) {
          const unsigned o = x1p[g >> 1][2 * (g & 1) + e];
          s = dot2(o, 0x3f803f80u, s); sq = dot2(o, o, sq); d = dot2(o, wrc[g][e], d);
        }
      stg(rs, stats_dst(sx, tile, tn, lane), u32x4{nzf(half_swap_sum(s)), nzf(half_swap_sum(sq)), nzf(half_swap_sum(d)), 0u});
    }
    STAMPV(16 + 16 * L);
  }
  {
    PHASE_VARS;
    float S, Q, Dd, mean, var, mb1, rd1;
    __builtin_amdgcn_s_sleep(10);
    row_stats_sum(rs, OFF_SX + SX_LAYER, tile, lane, S, Q, Dd);
    STAMPV(33);
    row_stats_fin(smem, 2, tile, lane, S, Q, Dd, U2F(words[0]), x1p, mean, var, mb1, rd1);
    STAMPV(34);
    u32x4 xo[4];
#pragma unroll
    for (int i = 0; i < 4; ++i) {
#pragma unroll
      for (int e = 0; e < 4; ++e) {
        const unsigned h2 = ln_pair(x1p[i][e], mb1, rd1);
        xo[i][e] = pack2(lo16(x1p[i][e]) + lo16(h2), hi16(x1p[i][e]) + hi16(h2));
      }
      *reinterpret_cast<u32x4*>(smem + wid * 4096 + r32 * 128 + ((2 * i + hi) ^ (r32 & 7)) * 16) = fo_swap(xo[i]);
    }
    asm volatile("s_waitcnt lgkmcnt(0)" ::: "memory");
#pragma unroll
    for (int i = 0; i < 4; ++i) { xo[i] = *reinterpret_cast<const u32x4*>(smem + wid * 4096 + i * 1024 + (lane >> 3) * 128 + ((lane ^ (lane >> 3)) & 7) * 16); PIN(xo[i]); }
    u16* dst = out + (size_t)(tile * 32 + (lane >> 3)) * D_ + tn * 64 + (lane & 7) * 8;
    lds_wait_ge(lbase + L_W + 4 + 4 * (lane & 1), 2);
    const int rel = (int)words[3] - tile * 32 - (lane >> 3);
#pragma unroll
    for (int i = 0; i < 4; ++i)
      if (rel != 8 * i) st16(dst + (size_t)i * 8 * D_, xo[i]);
    STAMPV(35);
    STAMP_END;
  }
}

extern "C" __global__ __launch_bounds__(512) void prep_kernel(const u16* __restrict__ x0, const u16* __restrict__ wqkv, char* __restrict__ ws) {
  __shared__ __attribute__((aligned(16))) char sm[128 * 144];
  const int tid = threadIdx.x, lane = tid & 63, wid = __builtin_amdgcn_readfirstlane(tid >> 6), b = blockIdx.x;
  if (b >= 256) {
    const int xcd = b & 7, slot = (b - 256) >> 3;
    if (tid < (int)(FL_COUNT / 256)) { u32x4 z = {0u, 0u, 0u, 0u}; st16(ws + OFF_FL + ((b - 256) * (FL_COUNT / 256) + tid) * 128, z); }
    unsigned acc = 0;
    {
      const u16* p = wqkv + (size_t)(slot * 32 + (tid >> 5)) * (3 * D_) + ((xcd & 3) * 4 + ((tid >> 3) & 3)) * 64 + (tid & 7) * 8;
#pragma unroll
      for (int i = 0; i < 6; ++i) {
        const u32x4 v = *reinterpret_cast<const u32x4*>(p + (size_t)(i & 1) * 16 * (3 * D_) + (i >> 1) * D_);
        acc |= v[0] ^ v[3];
      }
    }
    if (slot < 8) {
      const char *c0, *c1;
      CODE_ADDR(mega_kernel, c0); CODE_ADDR(prep_kernel, c1);
      const long long o = (slot * 512 + tid) * 16;
      if (o + 16 <= c1 - c0) acc |= (*reinterpret_cast<const __attribute__((address_space(1))) u32x4*>((uintptr_t)(c0 + o)))[0];
    }
    asm volatile("" :: "v"(acc));
    return;
  }
  u32x4 v[2];
  asm volatile("global_load_dwordx4 %0, %2, off\n\tglobal_load_dwordx4 %1, %2, off offset:1024"
               : "=&v"(v[0]), "=&v"(v[1]) : "v"(x0 + (size_t)(b * 8 + wid) * D_ + lane * 8) : "memory");
  int pass;
  asm volatile("s_mov_b32 %0, %1" : "=s"(pass) : "n"(0));
#pragma unroll 1
  for (;;) {
    asm volatile("s_cmp_eq_u32 %2, 0\n\ts_cbranch_scc1 1f\n\ts_waitcnt vmcnt(0)\n1:" : "+v"(v[0]), "+v"(v[1]) : "s"(pass) : "memory", "scc");
    float s = 0.f;
#pragma unroll
    for (int i = 0; i < 2; ++i)
#pragma unroll
      for (int e = 0; e < 4; ++e) s = dot2(v[i][e], 0x3f803f80u, s);
    const float mean = wave_sum(s) * (1.0f / 1024.0f);
    float m2 = 0.f;
#pragma unroll
    for (int i = 0; i < 2; ++i)
#pragma unroll
      for (int e = 0; e < 4; ++e) { const float a = lo16(v[i][e]) - mean, c = hi16(v[i][e]) - mean; m2 += a * a + c * c; }
    float mb, den;
    ln_scalars(mean, wave_sum(m2) * (1.0f / 1024.0f), mb, den);
    const float rd = 1.0f / den;
#pragma unroll
    for (int i = 0; i < 2; ++i) {
      char* p = sm + ((32 * i + (lane >> 1)) * 2) * 144 + wid * 16 + (lane & 1) * 8;
      u32x2 lo = {ln_pair(v[i][0], mb, rd), ln_pair(v[i][1], mb, rd)}, hi = {ln_pair(v[i][2], mb, rd), ln_pair(v[i][3], mb, rd)};
      *reinterpret_cast<u32x2*>(p) = lo;
      *reinterpret_cast<u32x2*>(p + 144) = hi;
    }
    if (pass) break;
    asm volatile("s_mov_b32 %0, 1" : "=s"(pass));
  }
  __syncthreads();
  char* dst = ws + OFF_HF + (size_t)(b >> 2) * TS + (b & 3) * 128;
#pragma unroll
  for (int i = 0; i < 2; ++i) {
    const int q = i * 512 + tid, pc = q >> 3, tk = q & 7;
    st16(dst + pc * 512 + tk * 16, *reinterpret_cast<const u32x4*>(sm + pc * 144 + tk * 16));
  }
}

extern "C" void kernel_launch(void* const* d_in, const int* in_sizes, int n_in, void* d_out, int out_size,
                              void* d_ws, size_t ws_size, hipStream_t stream) {
  char* ws = (char*)d_ws;
  prep_kernel<<<512, 512, 0, stream>>>((const u16*)d_in[0], (const u16*)d_in[1], ws);
  mega_kernel<<<256, 512, 0, stream>>>((const u16*)d_in[0], (const u16*)d_in[1], (const u16*)d_in[2], (const u16*)d_in[4], (const u16*)d_in[5],
                                       (const u16*)d_in[6], (const u16*)d_in[7], ws, (u16*)d_out);
}
```

```cpp
#include <hip/hip_runtime.h>
#include <hip/hip_bf16.h>
#include <stdint.h>

typedef unsigned short u16;
typedef short s16x4 __attribute__((ext_vector_type(4)));
typedef short s16x8 __attribute__((ext_vector_type(8)));
typedef float f32x16 __attribute__((ext_vector_type(16)));
typedef float f32x8 __attribute__((ext_vector_type(8)));
typedef float f32x4 __attribute__((ext_vector_type(4)));
typedef float f32x2 __attribute__((ext_vector_type(2)));
typedef unsigned int u32x16 __attribute__((ext_vector_type(16)));
typedef unsigned int u32x8 __attribute__((ext_vector_type(8)));
typedef unsigned int u32x4 __attribute__((ext_vector_type(4)));
typedef unsigned int u32x2 __attribute__((ext_vector_type(2)));
typedef __bf16 bf16x2 __attribute__((ext_vector_type(2)));
typedef __bf16 bf16x8 __attribute__((ext_vector_type(8)));
typedef __bf16 bf16x16 __attribute__((ext_vector_type(16)));

#define DI __device__ __forceinline__
#define LDS_PTR(T, p) ((__attribute__((address_space(3))) T*)(p))
#define BARRIER() asm volatile("s_barrier" ::: "memory")
#define BAR_LGKM() asm volatile("s_waitcnt lgkmcnt(0)\n\ts_barrier" ::: "memory")
#define BAR_ALL() asm volatile("s_waitcnt vmcnt(0) lgkmcnt(0)\n\ts_barrier" ::: "memory")
#define DRAIN() asm volatile("s_waitcnt vmcnt(0)" ::: "memory")
#define PACE() ({ asm volatile("s_sleep 1\n\ts_waitcnt vmcnt(3)" ::: "memory"); })
#define MFMA32(a, b, c) __builtin_amdgcn_mfma_f32_32x32x16_bf16(a, b, c, 0, 0, 0)
#define MFMA16(a, b, c) __builtin_amdgcn_mfma_f32_16x16x32_bf16(a, b, c, 0, 0, 0)
#define PIN(x) asm volatile("" : "+v"(x))
#define EX2(x) __builtin_amdgcn_exp2f(x)
#define RCP(x) __builtin_amdgcn_rcpf(x)
#define SBAR() __builtin_amdgcn_sched_barrier(0)

#define STAMP_INIT
#define STAMPV(i)
#define STAMP_END
#define STAMP_DECL
#define STAMP_OUTS
#define STAMP_PUT(i)
constexpr int D_ = 1024, F_ = 3584;

constexpr unsigned MBY = 1u << 20;
constexpr unsigned TS = 65536 + 4096, HS = 262144 + 4096;
constexpr unsigned OFF_HF = 0, OFF_X1 = 5 * MBY;
constexpr unsigned OFF_OF = 10 * MBY;
constexpr unsigned OFF_KV = 20 * MBY;
constexpr unsigned OFF_FL = 40 * MBY;
constexpr unsigned FL_EPOCH = OFF_FL;
constexpr unsigned FL_KV = OFF_FL + 4096;
constexpr unsigned FL_O = FL_KV + 65536;
constexpr unsigned FL_X = FL_O + 65536;
constexpr unsigned OFF_CS = 41 * MBY;
constexpr unsigned OFF_SX = 42 * MBY;
constexpr unsigned SX_ST = 0;
constexpr unsigned SX_ROW = 512 * 1024;
constexpr unsigned SX_CAND = SX_ROW + 32 * 1024;
constexpr unsigned SX_GP = SX_CAND + 32 * 1024;
constexpr unsigned SX_YP = SX_GP + 224 * 1024;
constexpr unsigned SX_LAYER = SX_YP + 224 * 1024;
constexpr unsigned SX_QP = 2 * SX_LAYER;
constexpr unsigned SX_BYTES = SX_QP + 512 * 1024;
static_assert(SX_BYTES <= 5 * 256 * 128 * 16, "sentinel fill");
constexpr unsigned WS_BYTES = 48 * MBY, OOB = 0x80000000u;
constexpr unsigned SENT = 0xFFFFFFFFu;

constexpr int NS = 9, DD = 3, PF = 6, SLOTB = 12288;
constexpr int L_HB = 110592, L_XB = L_HB + 2048;
constexpr int L_XCH = 122880;
constexpr int L_SL = L_XCH + 8192;
constexpr int L_CAND = L_XCH + 32768;
constexpr int L_AB = L_XCH + 34816;
constexpr int L_FULL = 159744, L_FREE = L_FULL + 512;
constexpr int L_CS = L_FREE + 1024, L_PATCH = L_CS + 768;
constexpr int L_GU = L_PATCH + 768, L_TAG = L_GU + 256, L_WR = L_TAG + 256;
constexpr int L_X2F = L_WR + 128, L_X2P = L_X2F + 128;
constexpr int L_W = L_X2P + 128;
constexpr int LDS_BYTES = 163840;
static_assert(NS * SLOTB <= L_HB && L_W + 64 <= LDS_BYTES && L_AB + 2048 <= L_FULL, "LDS map");

DI int fresh_lane() { int l; asm volatile("v_mbcnt_lo_u32_b32 %0, -1, 0\n\tv_mbcnt_hi_u32_b32 %0, -1, %0" : "=v"(l)); return l; }
#define LANE_VARS const int lane = fresh_lane(), r32 = lane & 31, hi = lane >> 5, tq = (lane & 15) >> 2, tp = lane & 3, tblk = (lane >> 4) & 1, \
  wrow = 4 * hi + tq, wsw = 4 * ((wrow >> 1) & 1), wch = 2 * tblk + (tp >> 1), c24 = r32 < 24 ? r32 : 23; \
  (void)r32; (void)hi; (void)wsw; (void)wch; (void)c24
DI int fresh_s(int v) { asm volatile("" : "+s"(v)); return v; }
#define PHASE_VARS const int tm = fresh_s(tm_), tn = fresh_s(tn_), wid = fresh_s(wid_), tile = tm * 4 + wid, hh = wid & 1, kh = wid >> 1; (void)tile; (void)hh; (void)kh; LANE_VARS
DI float bf2f(u16 v) { return __uint_as_float(((unsigned)v) << 16); }
DI unsigned pack2(float lo, float hi) { f32x2 v = {lo, hi}; return __builtin_bit_cast(unsigned, __builtin_convertvector(v, bf16x2)); }
DI float rbf(float f) { return __uint_as_float(pack2(f, 0.f) << 16); }
DI float lo16(unsigned u) { return __uint_as_float(u << 16); }
DI float hi16(unsigned u) { return __uint_as_float(u & 0xffff0000u); }
DI float opq(float r) { asm("" : "+v"(r)); return r; }
DI float fadd(float a, float b) { return opq(a + b); }
DI float fmul(float a, float b) { return opq(a * b); }
DI float ffma(float a, float b, float c) { return opq(__builtin_fmaf(a, b, c)); }
DI float U2F(unsigned u) { return __uint_as_float(u); }
DI float half_swap_sum(float v) {
  auto r = __builtin_amdgcn_permlane32_swap(__float_as_uint(v), __float_as_uint(v), false, false);
  return fadd(U2F(r[0]), U2F(r[1]));
}
DI float half_swap_max(float v) {
  auto r = __builtin_amdgcn_permlane32_swap(__float_as_uint(v), __float_as_uint(v), false, false);
  return fmaxf(__uint_as_float(r[0]), __uint_as_float(r[1]));
}
template <int CTRL> DI unsigned dpp(unsigned v) { return (unsigned)__builtin_amdgcn_update_dpp(0, (int)v, CTRL, 0xf, 0xf, false); }
DI float row_swap_sum(float v) {
  auto r = __builtin_amdgcn_permlane16_swap(__float_as_uint(v), __float_as_uint(v), false, false);
  return fadd(U2F(r[0]), U2F(r[1]));
}
DI float half_sum(float v) {
  v = fadd(v, U2F(dpp<0xB1>(__float_as_uint(v)))); v = fadd(v, U2F(dpp<0x4E>(__float_as_uint(v))));
  v = fadd(v, U2F(dpp<0x141>(__float_as_uint(v)))); v = fadd(v, U2F(dpp<0x140>(__float_as_uint(v))));
  return row_swap_sum(v);
}
DI float wave_sum(float v) { return half_swap_sum(half_sum(v)); }
DI unsigned half_max_u32(unsigned v) {
  v = max(v, dpp<0xB1>(v)); v = max(v, dpp<0x4E>(v)); v = max(v, dpp<0x141>(v)); v = max(v, dpp<0x140>(v));
  auto r = __builtin_amdgcn_permlane16_swap(v, v, false, false);
  return max(r[0], r[1]);
}
DI unsigned wave_max_u32(unsigned v) {
  v = half_max_u32(v);
  auto r = __builtin_amdgcn_permlane32_swap(v, v, false, false);
  return max(r[0], r[1]);
}
DI u32x4 fo_swap(u32x4 v) {
  auto a = __builtin_amdgcn_permlane32_swap(v[0], v[2], false, false);
  auto b = __builtin_amdgcn_permlane32_swap(v[1], v[3], false, false);
  u32x4 o; o[0] = a[0]; o[1] = b[0]; o[2] = a[1]; o[3] = b[1];
  return o;
}
DI void glds16(const void* gsrc, unsigned lds_dst) {
  unsigned keep;
  asm volatile("s_mov_b32 %0, m0\n\ts_mov_b32 m0, %2\n\ts_nop 0\n\tglobal_load_lds_dwordx4 %1, off\n\ts_mov_b32 m0, %0"
               : "=&s"(keep) : "v"(gsrc), "s"(lds_dst) : "memory");
}
#define GL_ "global_load_lds_dwordx4 "
DI void glds_v4(const void* sb, const unsigned (&v)[4], unsigned m0v) {
  unsigned keep;
  asm volatile("s_mov_b32 %0, m0\n\ts_mov_b32 m0, %6\n\ts_nop 0\n\t" GL_ "%1, %5\n\t" GL_ "%2, %5 offset:1024\n\t" GL_ "%3, %5 offset:2048\n\t"
               GL_ "%4, %5 offset:3072\n\ts_mov_b32 m0, %0" : "=&s"(keep) : "v"(v[0]), "v"(v[1]), "v"(v[2]), "v"(v[3]), "s"(sb), "s"(m0v) : "memory");
}
#define GP_ "\n\ts_sleep %8\n\ts_waitcnt vmcnt(%7)\n\t"
template <int TH, int SL> DI void glds_v4t(const void* sb, const unsigned (&v)[4], unsigned m0v) {
  unsigned keep;
  asm volatile("s_mov_b32 %0, m0\n\ts_mov_b32 m0, %6\n\ts_nop 0\n\t" GL_ "%1, %5" GP_ GL_ "%2, %5 offset:1024" GP_ GL_ "%3, %5 offset:2048" GP_ GL_ "%4, %5 offset:3072" GP_ "s_mov_b32 m0, %0"
               : "=&s"(keep) : "v"(v[0]), "v"(v[1]), "v"(v[2]), "v"(v[3]), "s"(sb), "s"(m0v), "n"(TH), "n"(SL) : "memory");
}
DI void glds_v3(const void* sb, const unsigned (&v)[3], unsigned m0v) {
  unsigned keep;
  asm volatile("s_mov_b32 %0, m0\n\ts_mov_b32 m0, %5\n\ts_nop 0\n\t" GL_ "%1, %4\n\t" GL_ "%2, %4 offset:1024\n\t" GL_ "%3, %4 offset:2048\n\t"
               "s_mov_b32 m0, %0" : "=&s"(keep) : "v"(v[0]), "v"(v[1]), "v"(v[2]), "s"(sb), "s"(m0v) : "memory");
}
DI float dot2(unsigned a, unsigned b, float c) {
  return __builtin_amdgcn_fdot2_f32_bf16(__builtin_bit_cast(bf16x2, a), __builtin_bit_cast(bf16x2, b), c, false);
}
#define CODE_ADDR(sym, out) do { unsigned long long pc_; unsigned off_; \
  asm volatile("s_getpc_b64 %0\n1:\n\ts_mov_b32 %1, " #sym "-1b" : "=s"(pc_), "=s"(off_)); out = (const char*)(pc_ + (long long)(int)off_); } while (0)
DI void st16(void* p, u32x4 v) { asm volatile("global_store_dwordx4 %0, %1, off sc0 sc1\n\ts_nop 1" :: "v"(p), "v"(v) : "memory"); }
DI void ln_scalars(float mean, float var, float& mb, float& den) { mb = rbf(mean); den = rbf(__builtin_amdgcn_sqrtf(rbf(rbf(var) + 1e-5f))); }
DI unsigned ln_pair(unsigned v, float mb, float rd) {
  const unsigned c = pack2(lo16(v) - mb, hi16(v) - mb);
  return pack2(lo16(c) * rd, hi16(c) * rd);
}
DI unsigned ln_pair_s(unsigned v, float nmb, float rd) {
  const unsigned c = pack2(fadd(lo16(v), nmb), fadd(hi16(v), nmb));
  return pack2(fmul(lo16(c), rd), fmul(hi16(c), rd));
}
template <int N, int M> DI f32x4 sum4(const u32x4 (&v)[M]) {
  f32x4 t;
#pragma unroll
  for (int e = 0; e < 4; ++e) {
    t[e] = U2F(v[0][e]);
#pragma unroll
    for (int i = 1; i < N; ++i) t[e] = fadd(t[e], U2F(v[i][e]));
  }
  return t;
}
#define PERM_LO(x, y) __builtin_amdgcn_perm(y, x, 0x05040100u)
#define PERM_HI(x, y) __builtin_amdgcn_perm(y, x, 0x07060302u)
constexpr unsigned SPIN_CAP = 1u << 15;
DI unsigned nz(unsigned v) { return min(v, SENT - 1u); }
DI unsigned nzf(float v) { return nz(__float_as_uint(v)); }
DI unsigned mx4(unsigned m, const u32x4& v) { return max(max(max(m, v[0]), v[1]), max(v[2], v[3])); }
template <int N> DI bool none_sent(const u32x4 (&v)[N]) {
  unsigned m = 0u;
#pragma unroll
  for (int i = 0; i < N; ++i) m = mx4(m, v[i]);
  return m != SENT;
}
typedef __amdgpu_buffer_rsrc_t rsrc_t;
DI u32x4 ldg(rsrc_t rs, unsigned off) { return __builtin_amdgcn_raw_buffer_load_b128(rs, (int)off, 0, 16); }
DI void stg(rsrc_t rs, unsigned off, u32x4 v) { __builtin_amdgcn_raw_buffer_store_b128(v, rs, (int)off, 0, 16); }
DI void poll_pause(bool slow) { if (slow) __builtin_amdgcn_s_sleep(4); asm volatile("s_sleep 1" ::: "memory"); }
template <int N, class LD, class OKF> DI void poll_all(u32x4 (&out)[N], bool slow, LD ld, OKF okf) {
  for (unsigned spins = 0;; ++spins) {
    ld(out);
    SBAR();
    if (__all(okf(out)) || spins > SPIN_CAP) break;
    poll_pause(slow);
  }
}
DI void lds_set(unsigned addr, unsigned v) { asm volatile("s_waitcnt lgkmcnt(0)\n\tds_write_b32 %0, %1" :: "v"(addr), "v"(v) : "memory"); }
template <int SLEEP = 1> DI void lds_wait_ge(unsigned addr, unsigned need) {
  unsigned spin = 0x100000, v;
  asm volatile("1:\n\tds_read_b32 %0, %2\n\ts_sub_u32 %1, %1, 1\n\ts_waitcnt lgkmcnt(0)\n\ts_cbranch_scc1 2f\n\tv_cmp_gt_u32 vcc, %3, %0\n\ts_cbranch_vccz 2f\n\ts_sleep %4\n\ts_branch 1b\n2:"
               : "=&v"(v), "+s"(spin) : "v"(addr), "s"(need), "n"(SLEEP) : "vcc", "scc", "memory");
}
DI s16x8 tr2(const char* p, int second) {
  s16x4 lo = __builtin_amdgcn_ds_read_tr16_b64_v4i16(LDS_PTR(s16x4, p)), hi = __builtin_amdgcn_ds_read_tr16_b64_v4i16(LDS_PTR(s16x4, p + second));
  return __builtin_shufflevector(lo, hi, 0, 1, 2, 3, 4, 5, 6, 7);
}
DI u32x16 cat4(const u32x4& a, const u32x4& b, const u32x4& c, const u32x4& d) {
  const u32x8 x = __builtin_shufflevector(a, b, 0, 1, 2, 3, 4, 5, 6, 7), y = __builtin_shufflevector(c, d, 0, 1, 2, 3, 4, 5, 6, 7);
  return __builtin_shufflevector(x, y, 0, 1, 2, 3, 4, 5, 6, 7, 8, 9, 10, 11, 12, 13, 14, 15);
}

DI unsigned stats_dst(unsigned sx, int tile, int tn, int lane) { return lane < 32 ? sx + SX_ST + tile * 8192 + tn * 512 + lane * 16 : OOB; }
DI void row_stats_sum(rsrc_t rs, unsigned sx, int tile, int lane, float& S, float& Q, float& Dd) {
  const unsigned exb = sx + SX_ST + tile * 8192 + (lane & 31) * 16;
  u32x4 pv[8];
  poll_all<8>(pv, false, [&](u32x4 (&v)[8]) {
#pragma unroll
    for (int i = 0; i < 8; ++i) v[i] = ldg(rs, exb + ((lane >> 5) * 8 + i) * 512);
  }, [&](const u32x4 (&v)[8]) { return none_sent(v); });
  S = 0.f; Q = 0.f; Dd = 0.f;
#pragma unroll
  for (int i = 0; i < 8; ++i) { S += __uint_as_float(pv[i][0]); Q += __uint_as_float(pv[i][1]); Dd += __uint_as_float(pv[i][2]); }
}
DI void row_stats_fin(char* smem, int epoch, int tile, int lane, float S, float Q, float Dd, float wsum, const u32x4 (&x1p)[4],
                      float& mean, float& var, float& mb, float& rd) {
  S = half_swap_sum(S); Q = half_swap_sum(Q); Dd = half_swap_sum(Dd);
  mean = S * (1.0f / 1024.0f); var = fmaxf(Q * (1.0f / 1024.0f) - mean * mean, 0.f);
  float den;
  ln_scalars(mean, var, mb, den);
  rd = RCP(den);
  const unsigned rb = pack2((Dd - mb * wsum) * rd, 0.f) & 0xffffu;
  const unsigned mine = (((rb & 0x8000u) ? (~rb & 0xffffu) : (rb | 0x8000u)) << 16) | (unsigned)(2047 - (tile * 32 + (lane & 31)));
  const bool best = half_max_u32(mine) == mine;
  if (best) {
    char* c = smem + L_CAND + (tile & 3) * 144;
#pragma unroll
    for (int i = 0; i < 4; ++i) *reinterpret_cast<u32x4*>(c + (2 * i + (lane >> 5)) * 16) = x1p[i];
    *reinterpret_cast<u32x4*>(c + 128) = u32x4{mine, __float_as_uint(mb), __float_as_uint(rd), 0u};
  }
  lds_set((unsigned)(uintptr_t)smem + L_W + 32 + (tile & 3) * 4, epoch);
}

DI void attn_safe(rsrc_t rs, unsigned kvoff, const u32x8& q, const u32x4& ones, f32x16& o) {
  const s16x8 q0 = __builtin_bit_cast(s16x8, __builtin_shufflevector(q, q, 0, 1, 2, 3)), q1 = __builtin_bit_cast(s16x8, __builtin_shufflevector(q, q, 4, 5, 6, 7));
  f32x4 l = {0.f, 0.f, 0.f, 0.f};
#pragma unroll
  for (int e = 0; e < 16; ++e) o[e] = 0.f;
  float m = -INFINITY;
#pragma unroll 1
  for (int kt = 0; kt < 64; ++kt) {
    const unsigned p = kvoff + kt * 4096;
    const u32x4 k0 = ldg(rs, p), k1 = ldg(rs, p + 1024), v0 = ldg(rs, p + 2048), v1 = ldg(rs, p + 3072);
    f32x16 s = {};
    s = MFMA32(__builtin_bit_cast(s16x8, k0), q0, s);
    s = MFMA32(__builtin_bit_cast(s16x8, k1), q1, s);
    float mt = fmaxf(s[0], s[1]);
#pragma unroll
    for (int e = 2; e < 16; ++e) mt = fmaxf(mt, s[e]);
    const float mn = fmaxf(m, half_swap_max(mt));
    const float alpha = EX2(m - mn);
    m = mn;
#pragma unroll
    for (int e = 0; e < 16; ++e) { s[e] = EX2(s[e] - mn); o[e] *= alpha; }
    l[0] *= alpha;
    const bf16x16 pa = __builtin_bit_cast(bf16x16, __builtin_shufflevector(s, s, 0, 1, 2, 3, 4, 5, 6, 7));
    const bf16x16 pb = __builtin_bit_cast(bf16x16, __builtin_shufflevector(s, s, 8, 9, 10, 11, 12, 13, 14, 15));
    o = __builtin_amdgcn_smfmac_f32_32x32x32_bf16(__builtin_bit_cast(bf16x8, v0), pa, o, 0xdddd, 0, 0);
    l = __builtin_amdgcn_smfmac_f32_16x16x64_bf16(__builtin_bit_cast(bf16x8, ones), pa, l, 0xdddd, 0, 0);
    o = __builtin_amdgcn_smfmac_f32_32x32x32_bf16(__builtin_bit_cast(bf16x8, v1), pb, o, 0xdddd, 0, 0);
    l = __builtin_amdgcn_smfmac_f32_16x16x64_bf16(__builtin_bit_cast(bf16x8, ones), pb, l, 0xdddd, 0, 0);
  }
  o *= 1.0f / l[0];
}

extern "C" __global__ __launch_bounds__(512) void mega_kernel(const u16* __restrict__ x0, const u16* __restrict__ wqkv, const u16* __restrict__ wo,
                                                              const u16* __restrict__ w_r, const u16* __restrict__ wg, const u16* __restrict__ wu,
                                                              const u16* __restrict__ wd, char* ws, u16* __restrict__ out) {
  __shared__ __attribute__((aligned(16))) char smem[LDS_BYTES];
  const int wid = __builtin_amdgcn_readfirstlane(threadIdx.x >> 6);
  STAMP_INIT;
  STAMPV(0);
  const int bid = blockIdx.x, xcd = bid & 7, loc = bid >> 3;
  const int tm = 8 * (xcd >> 2) + (loc >> 2), tn = 4 * (xcd & 3) + (loc & 3);
  const unsigned lbase = (unsigned)(uintptr_t)smem;
  const rsrc_t rs = __builtin_amdgcn_make_buffer_rsrc(ws, 0, (int)WS_BYTES, 0x00020000);
  volatile __attribute__((address_space(3))) unsigned* words = LDS_PTR(volatile unsigned, smem + L_W);
  const int lw = wid & 1;
  const char* wb = reinterpret_cast<const char*>(wqkv + (size_t)(16 * lw) * (3 * D_) + tn * 64);
  auto stage_lanes = [](unsigned (&wv)[3]) {
    const int lane = fresh_lane();
#pragma unroll
    for (int i = 0; i < 3; ++i) {
      const int li = lane + 64 * i, k = li / 24, cp = li % 24, c = cp ^ (4 * ((k >> 1) & 1));
      wv[i] = (unsigned)(k * (3 * D_ * 2) + (c >> 3) * (D_ * 2) + (c & 7) * 16 - i * 1024);
    }
  };
  auto stage = [&](const unsigned (&wv)[3], int s, int slot) {
    const char* p = wb + (size_t)((s + 2 * tn) & 31) * (32 * 3 * D_ * 2);
    glds_v3(p, wv, lbase + slot * SLOTB + lw * 6144);
    glds_v3(p + 8 * (3 * D_ * 2), wv, lbase + slot * SLOTB + lw * 6144 + 3072);
  };
  {
    u32x2 tag;
    const int tid = threadIdx.x, lane = tid & 63;
    char* sm = smem + L_XCH;
    u32x4 v[2];
    asm volatile("global_load_dwordx4 %0, %3, off\n\tglobal_load_dwordx4 %1, %3, off offset:1024\n\tglobal_load_dwordx2 %2, %4, %5 sc1"
                 : "=&v"(v[0]), "=&v"(v[1]), "=&v"(tag) : "v"(x0 + (size_t)(tm * 128 + tn * 8 + wid) * D_ + lane * 8), "v"(FL_EPOCH), "s"(ws) : "memory");
    const int ldr = (wid >> 2) & ~(wid >> 1) & 1;
    if (ldr) {
      unsigned wv[3];
      stage_lanes(wv);
#pragma unroll 1
      for (int s = 0; s < PF; ++s) stage(wv, s, s);
    }
    *reinterpret_cast<unsigned*>(smem + L_FULL + tid * 4) = 0u;
    if (tid < 16) words[tid] = 0u;
    int pass = fresh_s(ldr);
#pragma unroll 1
    for (;;) {
      asm volatile("s_cmp_eq_u32 %3, 0\n\ts_cbranch_scc1 1f\n\ts_waitcnt vmcnt(%5)\n\ts_cmp_lg_u32 %4, 0\n\ts_cbranch_scc1 1f\n\ts_waitcnt vmcnt(0)\n1:"
                   : "+v"(v[0]), "+v"(v[1]), "+v"(tag) : "s"(pass), "s"(ldr), "n"(PF * 6) : "memory", "scc");
      float s = 0.f;
#pragma unroll
      for (int i = 0; i < 2; ++i)
#pragma unroll
        for (int e = 0; e < 4; ++e) s = dot2(v[i][e], 0x3f803f80u, s);
      const float mean = wave_sum(s) * (1.0f / 1024.0f);
      float m2 = 0.f;
#pragma unroll
      for (int i = 0; i < 2; ++i)
#pragma unroll
        for (int e = 0; e < 4; ++e) { const float a = lo16(v[i][e]) - mean, c = hi16(v[i][e]) - mean; m2 += a * a + c * c; }
      float mb, den;
      ln_scalars(mean, wave_sum(m2) * (1.0f / 1024.0f), mb, den);
      const float rd = 1.0f / den;
#pragma unroll
      for (int i = 0; i < 2; ++i) {
        char* p = sm + ((32 * i + (lane >> 1)) * 2) * 144 + wid * 16 + (lane & 1) * 8;
        u32x2 lo = {ln_pair(v[i][0], mb, rd), ln_pair(v[i][1], mb, rd)}, hi = {ln_pair(v[i][2], mb, rd), ln_pair(v[i][3], mb, rd)};
        *reinterpret_cast<u32x2*>(p) = lo;
        *reinterpret_cast<u32x2*>(p + 144) = hi;
      }
      if (pass) break;
      pass = fresh_s(1);
    }
    *reinterpret_cast<u32x2*>(smem + L_TAG) = u32x2{tag[0] + 1u, tag[1] + 0x9E3779B9u};
    STAMPV(37);
    BAR_LGKM();
    if (wid < 4) {
      u32x4 t[4];
#pragma unroll
      for (int i = 0; i < 4; ++i) t[i] = *reinterpret_cast<const u32x4*>(sm + (i * 32 + (tid >> 3)) * 144 + (tid & 7) * 16);
      SBAR();
#pragma unroll
      for (int i = 0; i < 4; ++i) stg(rs, OFF_HF + (tm * 4 + (tn >> 2)) * TS + (tn & 3) * 128 + (i * 32 + (tid >> 3)) * 512 + (tid & 7) * 16, t[i]);
      DRAIN();
    }
    BARRIER();
    STAMPV(38);
  }
  auto exchange = [&](const u32x2& tag, unsigned line0, int me) {
    const int lane = fresh_lane();
    __builtin_amdgcn_raw_buffer_store_b64(tag, rs, lane == 0 ? line0 + me * 128 : OOB, 0, 16);
    __builtin_amdgcn_s_sleep(8);
    for (unsigned spins = 0;; ++spins) {
      const u32x2 v = __builtin_amdgcn_raw_buffer_load_b64(rs, line0 + (lane & 15) * 128, 0, 16);
      if (__all((v[0] == tag[0]) & (v[1] == tag[1])) || spins > SPIN_CAP) break;
      asm volatile("s_sleep 1" ::: "memory");
    }
  };
  if (wid >= 4) {
    auto wo_rows = [&](int g0, int n, int gs) {
      const int lane = fresh_lane();
      unsigned wov[4];
#pragma unroll
      for (int i = 0; i < 4; ++i) wov[i] = (unsigned)((8 * i + (lane >> 3)) * (D_ * 2) + ((lane & 7) ^ (4 * ((lane >> 4) & 1))) * 16 - i * 1024);
#pragma unroll 1
      for (int i = 0, k = (2 * (tm & 7)) % n, g; i < n && !0; ++i, k = k + 1 == n ? 0 : k + 1) g = g0 + gs * k, glds_v4t<3, 1>(reinterpret_cast<const char*>(wo) + (size_t)((64 * tn + 32 * g) & 1023) * (D_ * 2) + tn * 128, wov, lbase + g * 4096);
    };
    if (wid < 6) {
      const int lane = fresh_lane();
      unsigned wv[3];
      stage_lanes(wv);
      u32x2 tag = *reinterpret_cast<const u32x2*>(smem + L_TAG);
      PIN(tag);
      __builtin_amdgcn_s_setprio(3);
      const unsigned fra = lbase + L_FREE + (lane & 3) * 256, fla = lbase + L_FULL + lw * 256 + lane * 4;
      const rsrc_t rw = __builtin_amdgcn_make_buffer_rsrc(const_cast<char*>(wb), 0, 0 ? 0 : 6 * MBY - (16 * lw * 3 * D_ + tn * 64) * 2, 0x00020000);
#pragma unroll 1
      for (int L = 0; L < 2; ++L) {
        const int s0 = L ? NS : PF, lead = L ? 8 : NS - 1;
        int slot = L ? 0 : PF % NS;
        if (L == 0) {
#pragma unroll
          for (int s = 0; s < PF - DD; ++s) asm volatile("s_waitcnt vmcnt(%0)\n\tds_write_b32 %1, %2" :: "n"((PF - 1 - s) * 6), "v"(fla), "v"(s + 1) : "memory");
        }
#pragma unroll 1
        for (int s = s0; s < 32; ++s) {
          if (s >= NS) lds_wait_ge(fra, 32 * L + s - lead);
          stage(wv, s, slot);
          slot = (slot == NS - 1) ? 0 : slot + 1;
          if (s >= (L ? NS : 0) + DD) asm volatile("s_waitcnt vmcnt(%0)\n\tds_write_b32 %1, %2" :: "n"(DD * 6), "v"(fla), "v"(32 * L + s - DD + 1) : "memory");
        }
#pragma unroll
        for (int d = DD - 1; d >= 0; --d) asm volatile("s_waitcnt vmcnt(%0)\n\tds_write_b32 %1, %2" :: "n"(d * 6), "v"(fla), "v"(32 * L + 32 - d) : "memory");
        BAR_LGKM();
        if (lw == 0) {
          exchange(tag, FL_KV + (L * 16 + tn) * 2048, tm);
          lds_wait_ge(fra, 32 * L + 32);
          lds_set(lbase + L_W + 16, L + 1);
        } else lds_wait_ge<8>(lbase + L_W + 16, L + 1);
        __builtin_amdgcn_s_sleep(24);
        wo_rows(lw, 15, 2);
        u32x4 pre[NS * 6];
        if (L == 0) {
#pragma unroll
          for (int s = 0; s < NS; ++s) {
#pragma unroll
            for (int i = 0; i < 6; ++i)
              pre[s * 6 + i] = __builtin_amdgcn_raw_buffer_load_b128(rw, wv[i % 3], ((s + 2 * tn) & 31) * (32 * 3 * D_ * 2) + (i / 3) * (8 * 3 * D_ * 2) + (i % 3) * 1024, 0), PACE();
          }
        } else {
#pragma unroll
          for (int i = 0; i < NS * 6; ++i) asm volatile("" : "=v"(pre[i]));
        }
        BAR_ALL();
        if (lw == 0) {
          exchange(tag, FL_O + (L * 16 + tm) * 2048, tn); lds_set(lbase + L_W + 20, L + 1);
          __builtin_amdgcn_raw_buffer_store_b64(tag, rs, (bid == 0 && lane == 0) ? FL_EPOCH : OOB, 0, 16);
        }
        else wo_rows(30, 2, 1);
        BAR_ALL();
        if (L == 0) {
          char* d = smem + lw * 6144 + fresh_lane() * 16;
#pragma unroll
          for (int s = 0; s < NS; ++s) {
#pragma unroll
            for (int i = 0; i < 6; ++i) *reinterpret_cast<u32x4*>(d + s * SLOTB + i * 1024) = pre[s * 6 + i];
            lds_set(fla, 32 + s + 1);
          }
          if (lw == 0) { lds_wait_ge(fra, 33); exchange(tag, FL_X + (16 + tm) * 2048, tn); lds_set(lbase + L_W + 24, 2); }
          else lds_wait_ge<2>(lbase + L_W + 24, 2);
        }
      }
      return;
    }
    const int a = wid - 6;
    const bool ffn = loc < 28;
    const int grp = xcd * 7 + (ffn ? loc >> 2 : 0), qa = (loc & 3) * 2 + a;
    const int kr = loc, cg = xcd;
    const unsigned wa = lbase + L_W;
    if (a) { const u32x2 tag = *reinterpret_cast<const u32x2*>(smem + L_TAG); exchange(tag, FL_X + tm * 2048, tn); lds_set(wa + 24, 1); } else lds_wait_ge<2>(wa + 24, 1);
    __builtin_amdgcn_s_sleep(16);
    auto transpose8 = [](u32x4* v) {
      u32x4 t[8];
#pragma unroll
      for (int j = 0; j < 8; ++j)
#pragma unroll
        for (int e = 0; e < 4; ++e) t[j][e] = (j & 1) ? PERM_HI(v[2 * e][j >> 1], v[2 * e + 1][j >> 1]) : PERM_LO(v[2 * e][j >> 1], v[2 * e + 1][j >> 1]);
#pragma unroll
      for (int j = 0; j < 8; ++j) { v[j] = t[j]; PIN(v[j]); }
    };
    u32x4 gf[32];
    u32x4 wdf[16];
    auto wait_go = [&](int L) {
      lds_wait_ge<8>(lbase + L_W + 16, L + 1);
      __builtin_amdgcn_s_sleep(24);
    };
    auto load_wd = [&](unsigned dv) {
      const rsrc_t rd = __builtin_amdgcn_make_buffer_rsrc(const_cast<u16*>(wd) + (size_t)grp * 64 * D_ + 128 * qa, 0, 0 ? 0 : 64 * D_ * 2 - 256 * qa, 0x00020000);
#pragma unroll
      for (int i = 0; i < 16; ++i) {
        wdf[i] = __builtin_amdgcn_raw_buffer_load_b128(rd, dv, (32 * (i >> 3) + (i & 7)) * (D_ * 2), 0);
        asm volatile("s_sleep 2\n\ts_waitcnt vmcnt(3)" ::: "memory");
      }
    };
    {
      LANE_VARS;
      const u32x4 sv = {SENT, SENT, SENT, SENT};
#pragma unroll
      for (int i = 0; i < 5; ++i) {
        const unsigned o = ((i * 256 + bid) * 128 + a * 64 + lane) * 16;
        stg(rs, o < SX_BYTES ? OFF_SX + o : OOB, sv);
      }
      if (a == 0) {
        const u32x4 w0 = *reinterpret_cast<const u32x4*>(w_r + lane * 8), w1 = *reinterpret_cast<const u32x4*>(w_r + 512 + lane * 8);
        float t = 0.f;
#pragma unroll
        for (int e = 0; e < 4; ++e) t = fadd(fadd(t, fadd(lo16(w0[e]), hi16(w0[e]))), fadd(lo16(w1[e]), hi16(w1[e])));
        words[0] = __float_as_uint(wave_sum(t));
        if ((lane >> 3) == (tn & 7)) *reinterpret_cast<u32x4*>(smem + L_WR + (lane & 7) * 16) = tn < 8 ? w0 : w1;
      }
      const u16* p = wqkv + (size_t)(64 * tm + 32 * a + hi) * (3 * D_) + (c24 >> 3) * D_ + tn * 64 + (c24 & 7) * 8;
      float cs[8] = {0.f, 0.f, 0.f, 0.f, 0.f, 0.f, 0.f, 0.f};
      u32x4 v[16];
#pragma unroll
      for (int i = 0; i < 16; ++i) v[i] = *reinterpret_cast<const u32x4*>(p + (size_t)i * (2 * 3 * D_));
      SBAR();
#pragma unroll
      for (int i = 0; i < 16; ++i)
#pragma unroll
        for (int e = 0; e < 4; ++e) { cs[2 * e] = fadd(cs[2 * e], lo16(v[i][e])); cs[2 * e + 1] = fadd(cs[2 * e + 1], hi16(v[i][e])); }
      u32x4 o;
#pragma unroll
      for (int e = 0; e < 4; ++e) { const float x = half_swap_sum(cs[e]), y = half_swap_sum(cs[4 + e]); o[e] = __float_as_uint(hi ? y : x); }
      stg(rs, r32 < 24 ? OFF_CS + (tn * 32 + tm * 2 + a) * 768 + r32 * 32 + hi * 16 : OOB, o);
    }
    {
      BAR_ALL();
      LANE_VARS;
      const char* gp = reinterpret_cast<const char*>(((lane & 8) ? wu : wg) + (size_t)(128 * qa + 16 * hi + 4 * tblk) * F_ + 64 * grp + (lane & 7) * 8);
      unsigned dv = (lane >> 4) * (8 * D_ * 2) + (lane & 15) * 16;
      PIN(gp); PIN(dv);
      unsigned sv[3];
#pragma unroll
      for (int i = 0; i < 3; ++i) { const int li = lane + 64 * i, k = li / 24, c = li % 24; sv[i] = (unsigned)(k * (3 * D_ * 2) + (c >> 3) * (D_ * 2) + (c & 7) * 16 - i * 1024); PIN(sv[i]); }
      wait_go(0);
#pragma unroll
      for (int i = 0; i < 32; ++i) {
        gf[i] = *reinterpret_cast<const u32x4*>(gp + (size_t)(0 ? 0 : 32 * (i >> 3) + 8 * ((i >> 2) & 1) + (i & 3)) * (F_ * 2));
        asm volatile("s_sleep 2\n\ts_waitcnt vmcnt(3)" ::: "memory");
      }
      load_wd(dv);
      STAMPV(7);
      BARRIER();
      __builtin_amdgcn_s_sleep(2);
#pragma unroll
      for (int g = 0; g < 4; ++g) glds_v3(wqkv + (size_t)(32 * kr + 8 * g) * (3 * D_) + (2 * cg + a) * 64, sv, lbase + L_SL + a * 12288 + g * 3072);
      transpose8(gf); transpose8(gf + 8); transpose8(gf + 16); transpose8(gf + 24);
      transpose8(wdf); transpose8(wdf + 8);
      {
        LANE_VARS;
        f32x4 t[2];
#pragma unroll
        for (int c = 0; c < 2; ++c) {
          u32x4 v[8];
#pragma unroll
          for (int i = 0; i < 8; ++i) v[i] = ldg(rs, OFF_CS + (tn * 32 + 16 * c + 2 * i + hi) * 768 + a * 384 + c24 * 16);
          SBAR();
          t[c] = sum4<8>(v);
          SBAR();
        }
#pragma unroll
        for (int e = 0; e < 4; ++e) t[0][e] = half_swap_sum(fadd(t[0][e], t[1][e]));
        if (lane < 24) *reinterpret_cast<f32x4*>(smem + L_CS + a * 384 + lane * 16) = t[0];
      }
      BAR_LGKM();
    }
#pragma unroll 1
    for (int L = fresh_s(0);; ++L) {
      const unsigned sx = OFF_SX + L * SX_LAYER;
      STAMPV(1 + 16 * L);
      LANE_VARS;
      if (0 && L == 0) { words[3] = 5000u; lds_set(wa + 4 + 4 * a, 1); } else {
      unsigned cdst = lane < 8 ? sx + SX_ROW + tm * 2048 + tn * 128 + lane * 16 : sx + SX_CAND + (tn * 16 + tm) * 128;
      cdst = (a == 0 && lane < 9) ? cdst : OOB;
      PIN(cdst);
      lds_wait_ge(wa + 32 + 4 * tp, L + 1);
      unsigned key;
      {
        const unsigned k4 = *reinterpret_cast<const unsigned*>(smem + L_CAND + tp * 144 + 128);
        key = max(k4, dpp<0xB1>(k4)); key = max(key, dpp<0x4E>(key));
        const u32x4 c = *reinterpret_cast<const u32x4*>(smem + L_CAND + (((2047 - (int)(key & 2047u)) >> 5) & 3) * 144 + (lane < 8 ? lane : 8) * 16);
        const u32x4 o = {nz(c[0]), nz(c[1]), nz(c[2]), nz(c[3])};
        stg(rs, cdst, o);
      }
      STAMPV(8 + 16 * L);
      __builtin_amdgcn_s_sleep(10);
      if (L == 0) __builtin_amdgcn_s_sleep(0);
      u32x4 v[2];
      const unsigned rpc = (tblk ? 4 * kr + tp : 16 * qa + (lane & 15)) * 16;
      poll_all<2>(v, L == 0, [&](u32x4 (&t)[2]) {
        t[0] = ldg(rs, sx + SX_CAND + (tn * 16 + (lane & 15)) * 128);
        t[1] = ldg(rs, sx + SX_ROW + ((2047 - (int)(key & 2047u)) >> 7) * 2048 + rpc);
        t[0][3] = key;
      }, [&](const u32x4 (&t)[2]) {
        const bool ok = mx4(0u, t[0]) != SENT;
        unsigned k = ok ? t[0][0] : 0u;
        k = max(k, dpp<0xB1>(k)); k = max(k, dpp<0x4E>(k)); k = max(k, dpp<0x141>(k)); k = max(k, dpp<0x140>(k));
        key = max(key, k);
        return ok & (mx4(0u, t[1]) != SENT) & (key == t[0][3]);
      });
      key = __builtin_amdgcn_readfirstlane(key);
      const int idx = 2047 - (int)(key & 2047u);
      const bool owner = (idx >> 7) == tm;
      STAMPV(2 + 16 * L);
      {
        const float mb = __uint_as_float(__builtin_amdgcn_readlane(v[0][1], idx >> 7)), rd = __uint_as_float(__builtin_amdgcn_readlane(v[0][2], idx >> 7));
        u32x4 hb;
#pragma unroll
        for (int e = 0; e < 4; ++e) hb[e] = ln_pair_s(v[1][e], -mb, rd);
        *reinterpret_cast<u32x4*>(smem + L_HB + a * 1024 + lane * 16) = hb;
        *reinterpret_cast<u32x4*>(smem + L_XB + a * 1024 + lane * 16) = v[1];
        words[3] = (unsigned)idx;
        if (L == 1) lds_set(wa + 4 + 4 * a, 2);
      }
      auto pick2 = [&](const f32x4 (&d)[8]) {
        u32x2 o;
#pragma unroll
        for (int e = 0; e < 2; ++e) {
          const float x = tblk ? d[2 + e][0] : d[e][0], y = tblk ? d[6 + e][0] : d[4 + e][0];
          o[e] = nzf(hi ? y : x);
        }
        return o;
      };
      if (ffn) {
        const f32x4 zero = {0.f, 0.f, 0.f, 0.f};
        const unsigned ppos = (lane & 15) * 32 + (lane >> 4) * 8;
        f32x4 d[8];
        {
          s16x8 hf[4];
#pragma unroll
          for (int t = 0; t < 4; ++t) hf[t] = *reinterpret_cast<const s16x8*>(smem + L_HB + a * 1024 + (lane >> 4) * 16 + t * 64);
          SBAR();
          __builtin_amdgcn_s_setprio(3);
#pragma unroll
          for (int t = 0; t < 32; ++t) d[t & 7] = MFMA16(hf[t >> 3], __builtin_bit_cast(s16x8, gf[t]), t < 8 ? zero : d[t & 7]);
          SBAR();
          __builtin_amdgcn_s_setprio(0);
          __builtin_amdgcn_raw_buffer_store_b64(pick2(d), rs, sx + SX_GP + (grp * 8 + qa) * 512 + ppos, 0, 16);
        }
        STAMPV(9 + 16 * L);
        __builtin_amdgcn_s_sleep(10);
        if (L == 0) __builtin_amdgcn_s_sleep(0);
        u32x2 gu[8];
        for (unsigned spins = 0;; ++spins) {
#pragma unroll
          for (int i = 0; i < 8; ++i) gu[i] = __builtin_amdgcn_raw_buffer_load_b64(rs, sx + SX_GP + (grp * 8 + i) * 512 + lane * 8, 0, 16);
          SBAR();
          unsigned m = 0u;
#pragma unroll
          for (int i = 0; i < 8; ++i) m = max(max(m, gu[i][0]), gu[i][1]);
          if (__all(m != SENT) || spins > SPIN_CAP) break;
          poll_pause(L == 0);
        }
        STAMPV(10 + 16 * L);
        float t0 = opq(U2F(gu[0][0])), t1 = opq(U2F(gu[0][1]));
#pragma unroll
        for (int i = 1; i < 8; ++i) { t0 = fadd(t0, U2F(gu[i][0])); t1 = fadd(t1, U2F(gu[i][1])); }
        auto sw = __builtin_amdgcn_permlane32_swap(__float_as_uint(t0), __float_as_uint(t1), false, false);
        const float gv = rbf(U2F(sw[0])), uv = rbf(U2F(sw[1]));
        reinterpret_cast<u16*>(smem + L_GU + a * 128)[2 * r32 + hi] = (u16)pack2(fmul(rbf(fmul(gv, RCP(fadd(1.0f, __expf(-gv))))), uv), 0.f);
        asm volatile("s_waitcnt lgkmcnt(0)" ::: "memory");
        const s16x8 a0 = *reinterpret_cast<const s16x8*>(smem + L_GU + a * 128 + (lane >> 4) * 16), a1 = *reinterpret_cast<const s16x8*>(smem + L_GU + a * 128 + 64 + (lane >> 4) * 16);
        SBAR();
        __builtin_amdgcn_s_setprio(3);
#pragma unroll
        for (int t = 0; t < 16; ++t) d[t & 7] = MFMA16(t < 8 ? a0 : a1, __builtin_bit_cast(s16x8, wdf[t]), t < 8 ? zero : d[t & 7]);
        SBAR();
        __builtin_amdgcn_s_setprio(0);
        __builtin_amdgcn_raw_buffer_store_b64(pick2(d), rs, sx + SX_YP + grp * 4096 + qa * 512 + ppos, 0, 16);
      }
      STAMPV(3 + 16 * L);
      if (L == 1 && (cg != (kr & 7) || a)) { STAMP_END; __builtin_amdgcn_endpgm(); }
      s16x8 wpf[12];
#pragma unroll
      for (int t = 0; t < 12; ++t) wpf[t] = tr2(smem + L_SL + a * 12288 + (16 * hi + 4 * tblk + tq) * 384 + t * 32 + tp * 8, 8 * 384);
#pragma unroll
      for (int t = 0; t < 12; ++t) PIN(wpf[t]);
      if (L == 0 && !owner) lds_set(wa + 4 + 4 * a, 1);
      __builtin_amdgcn_s_sleep(10 - 4);
      if (L == 0) __builtin_amdgcn_s_sleep(0);
      if (L == 0 && !ffn) __builtin_amdgcn_s_sleep(64);
      const unsigned rk = key >> 16;
      float rsig = rbf(RCP(fadd(1.0f, __expf(-lo16((rk & 0x8000u) ? (rk & 0x7fffu) : (~rk & 0xffffu))))));
      PIN(rsig);
      const int n = 4 * (lane & 7) + ((lane >> 3) & 3);
      const int fo = ((n >> 4) * 2 + ((n >> 2) & 1)) * 8 + ((n >> 3) & 1) * 4 + (n & 3);
      float xv = bf2f(reinterpret_cast<const u16*>(smem + L_XB + a * 1024 + 256)[fo]), hv = bf2f(reinterpret_cast<const u16*>(smem + L_HB + a * 1024 + 256)[fo]);
      PIN(xv); PIN(hv);
      u32x4 pv[7];
      poll_all<7>(pv, L == 0, [&](u32x4 (&t)[7]) {
#pragma unroll
        for (int i = 0; i < 7; ++i) t[i] = ldg(rs, sx + SX_YP + (8 * i + (lane >> 3)) * 4096 + kr * 128 + (lane & 7) * 16);
      }, [&](const u32x4 (&t)[7]) { return none_sent(t); });
      STAMPV(4 + 16 * L);
      float ys;
      {
        f32x4 t = sum4<7>(pv);
#pragma unroll
        for (int e = 0; e < 4; ++e) {
          t[e] = fadd(t[e], U2F(dpp<0x128>(__float_as_uint(t[e]))));
          t[e] = half_swap_sum(row_swap_sum(t[e]));
        }
        const float x = (lane & 8) ? t[1] : t[0], y = (lane & 8) ? t[3] : t[2];
        ys = tblk ? y : x;
      }
      const unsigned ob = pack2(fadd(xv, rbf(fadd(hv, rbf(fmul(rbf(ys), rsig))))), 0.f) & 0xffffu;
      if (L == 1) {
        if (lane < 32) out[(size_t)idx * D_ + kr * 32 + n] = (u16)ob;
        STAMPV(5 + 16 * L);
        STAMP_END;
        __builtin_amdgcn_endpgm();
      }
      reinterpret_cast<u16*>(smem + L_X2F + a * 64)[fo] = (u16)ob;
      asm volatile("s_waitcnt lgkmcnt(0)" ::: "memory");
      {
        const s16x8 xa = *reinterpret_cast<const s16x8*>(smem + L_X2F + a * 64 + (lane >> 4) * 16);
        const u32x4 xf = *reinterpret_cast<const u32x4*>(smem + L_X2F + a * 64 + tp * 16);
        f32x4 z[12];
        const f32x4 zero = {0.f, 0.f, 0.f, 0.f};
        SBAR();
        __builtin_amdgcn_s_setprio(3);
#pragma unroll
        for (int t = 0; t < 12; ++t) z[t] = MFMA16(xa, wpf[t], zero);
        SBAR();
        __builtin_amdgcn_s_setprio(0);
        const float x2 = lo16(ob), s1 = half_sum(x2), s2 = half_sum(fmul(x2, x2));
        u32x4 o;
#pragma unroll
        for (int e = 0; e < 4; ++e) {
          const unsigned sp = lane < 52 ? (e == 0 ? __float_as_uint(s1) : __float_as_uint(s2)) : xf[e];
          const unsigned x = __float_as_uint(tblk ? z[4 + e][0] : z[e][0]), y = tblk ? sp : __float_as_uint(z[8 + e][0]);
          o[e] = nz(hi ? y : x);
        }
        const unsigned pos = lane < 24 ? lane * 16 : lane < 48 ? lane * 16 + 128 : lane < 52 ? (lane & 1) * 512 + 384 : 400 + tp * 16;
        stg(rs, lane < 56 ? OFF_SX + SX_QP + ((2 * cg + a) * 32 + kr) * 1024 + pos : OOB, o);
      }
      STAMPV(5 + 16 * L);
      if (owner) {
        __builtin_amdgcn_s_sleep(10);
        const int c25 = r32 < 25 ? r32 : 24, pl = 24 * a + c24, col = 64 * (pl >> 4) + (pl & 15);
        float cs[4];
#pragma unroll
        for (int e = 0; e < 4; ++e) { cs[e] = reinterpret_cast<const float*>(smem + L_CS)[col + 16 * e]; PIN(cs[e]); }
        u32x4 qv[17];
        poll_all<17>(qv, true, [&](u32x4 (&t)[17]) {
#pragma unroll
          for (int i = 0; i < 16; ++i) t[i] = ldg(rs, OFF_SX + SX_QP + (tn * 32 + 2 * i + hi) * 1024 + a * 512 + c25 * 16);
          t[16] = ldg(rs, OFF_SX + SX_QP + (tn * 34 + hi) * 1024 + 400 + tp * 16);
        }, [&](const u32x4 (&t)[17]) { return none_sent(t); });
        *reinterpret_cast<u32x4*>(smem + L_X2P + hi * 64 + tp * 16) = qv[16];
        f32x4 t = sum4<16>(qv);
#pragma unroll
        for (int e = 0; e < 4; ++e) t[e] = half_swap_sum(t[e]);
        const float mean = fmul(U2F(__builtin_amdgcn_readlane(__float_as_uint(t[0]), 24)), 1.0f / 1024.0f);
        const float var = fmaxf(ffma(-mean, mean, fmul(U2F(__builtin_amdgcn_readlane(__float_as_uint(t[1]), 24)), 1.0f / 1024.0f)), 0.f);
        float mb, den;
        ln_scalars(mean, var, mb, den);
        const float rd = RCP(den);
        if (lane < 24) {
#pragma unroll
          for (int e = 0; e < 4; ++e) reinterpret_cast<float*>(smem + L_PATCH)[col + 16 * e] = fmul(ffma(-mb, cs[e], t[e]), rd);
        }
        lds_set(wa + 4 + 4 * a, L + 1);
      }
      STAMPV(6 + 16 * L);
      }
      BAR_ALL();
      unsigned dv = (lane >> 4) * (8 * D_ * 2) + (lane & 15) * 16;
      PIN(dv);
      wait_go(1);
      load_wd(dv);
      STAMPV(23);
      BARRIER();
      transpose8(wdf); transpose8(wdf + 8);
      BARRIER();
    }
  }
  const int tm_ = tm, tn_ = tn, wid_ = wid;
  const u32x4 srd = {(unsigned)(uintptr_t)ws, (unsigned)((uintptr_t)ws >> 32) & 0xffffu, WS_BYTES, 0x00020000u};
  u32x4 x1p[4];
  asm volatile("" : "=v"(x1p[0]), "=v"(x1p[1]), "=v"(x1p[2]), "=v"(x1p[3]));
#pragma unroll 1
  for (int L = 0; L < 2; ++L) {
    const unsigned sx = OFF_SX + L * SX_LAYER;
    u32x8 q[4];
    u32x16 kva, kvb;
    f32x16 oz[5];
    {
      f32x16 acc[6];
      u32x16 stA, stB, xr0, xr1, xr2, xr3;
      u32x4 stC;
      asm volatile("" : "={v[112:127]}"(stB), "={v[128:131]}"(stC));
      { PHASE_VARS;
      stB[8] = lane * 16 + (L ? OFF_X1 : OFF_HF) + tile * TS;
      stB[9] = lbase + wrow * 384 + (((4 * hh + wch) ^ wsw) << 4) + 8 * (tp & 1);
      stB[10] = lbase + wrow * 384 + (((4 * (hh ^ 1) + wch) ^ wsw) << 4) + 8 * (tp & 1);
      stB[13] = lbase + L_FULL + (lane & 1) * 256;
      stB[15] = lbase + L_FREE + wid * 256 + lane * 4;
      stC[0] = 32 * L; }
      unsigned soff, slot, need = 32 * L + 1;
      if (L == 0) {
        lds_wait_ge<0>(lbase + L_W + 24, 1);
        STAMPV(39);
        soff = tn * 4096;
        asm volatile(
          "buffer_load_dwordx4 v[132:135], v120, s[40:43], s44 offen offset:0 sc1\n\t"
          "buffer_load_dwordx4 v[136:139], v120, s[40:43], s44 offen offset:1024 sc1\n\t"
          "buffer_load_dwordx4 v[140:143], v120, s[40:43], s44 offen offset:2048 sc1\n\t"
          "buffer_load_dwordx4 v[144:147], v120, s[40:43], s44 offen offset:3072 sc1\n\t"
          "s_add_u32 s44, s44, 0x1000\n\t"
          "s_and_b32 s44, s44, 0xffff\n\t"
          "buffer_load_dwordx4 v[148:151], v120, s[40:43], s44 offen offset:0 sc1\n\t"
          "buffer_load_dwordx4 v[152:155], v120, s[40:43], s44 offen offset:1024 sc1\n\t"
          "buffer_load_dwordx4 v[156:159], v120, s[40:43], s44 offen offset:2048 sc1\n\t"
          "buffer_load_dwordx4 v[160:163], v120, s[40:43], s44 offen offset:3072 sc1\n\t"
          "s_add_u32 s44, s44, 0x1000\n\t"
          "s_and_b32 s44, s44, 0xffff\n\t"
          "buffer_load_dwordx4 v[164:167], v120, s[40:43], s44 offen offset:0 sc1\n\t"
          "buffer_load_dwordx4 v[168:171], v120, s[40:43], s44 offen offset:1024 sc1\n\t"
          "buffer_load_dwordx4 v[172:175], v120, s[40:43], s44 offen offset:2048 sc1\n\t"
          "buffer_load_dwordx4 v[176:179], v120, s[40:43], s44 offen offset:3072 sc1\n\t"
          "s_add_u32 s44, s44, 0x1000\n\t"
          "s_and_b32 s44, s44, 0xffff\n\t"
          "buffer_load_dwordx4 v[180:183], v120, s[40:43], s44 offen offset:0 sc1\n\t"
          "buffer_load_dwordx4 v[184:187], v120, s[40:43], s44 offen offset:1024 sc1\n\t"
          "buffer_load_dwordx4 v[188:191], v120, s[40:43], s44 offen offset:2048 sc1\n\t"
          "buffer_load_dwordx4 v[192:195], v120, s[40:43], s44 offen offset:3072 sc1\n\t"
          : "={v[132:147]}"(xr0), "={v[148:163]}"(xr1), "={v[164:179]}"(xr2), "={v[180:195]}"(xr3), "+{s44}"(soff) : "{v[112:127]}"(stB), "{s[40:43]}"(srd) : "scc", "memory");
      } else {
        xr0 = cat4(x1p[0], x1p[1], x1p[2], x1p[3]);
        asm volatile("" : "={v[148:163]}"(xr1), "={v[164:179]}"(xr2), "={v[180:195]}"(xr3));
      }
      STAMPV(1 + 16 * L);
      asm volatile(
        "v_mov_b32 v123, v121\n\t"
        "v_mov_b32 v124, v122\n\t"
        "s_mov_b64 vcc, -1\n\t"
        "s_cbranch_vccnz 10f\n\t"
        "11:\n\t"
        "s_mov_b32 s46, 12288\n\t"
        "s_add_u32 s47, s47, 1\n\t"
        "ds_read_b64_tr_b16 v[96:97], v123 offset:0\n\t"
        "ds_read_b64_tr_b16 v[98:99], v123 offset:3072\n\t"
        "ds_read_b64_tr_b16 v[100:101], v124 offset:0\n\t"
        "ds_read_b64_tr_b16 v[102:103], v124 offset:3072\n\t"
        "ds_read_b64_tr_b16 v[104:105], v123 offset:128\n\t"
        "ds_read_b64_tr_b16 v[106:107], v123 offset:3200\n\t"
        "ds_read_b64_tr_b16 v[108:109], v124 offset:128\n\t"
        "ds_read_b64_tr_b16 v[110:111], v124 offset:3200\n\t"
        "ds_read_b64_tr_b16 v[112:113], v123 offset:256\n\t"
        "ds_read_b64_tr_b16 v[114:115], v123 offset:3328\n\t"
        "ds_write_b32 v127, v128\n\t"
        "ds_read_b64_tr_b16 v[116:117], v124 offset:256\n\t"
        "ds_read_b64_tr_b16 v[118:119], v124 offset:3328\n\t"
        "s_waitcnt vmcnt(15) lgkmcnt(11)\n\t"
        "v_mfma_f32_32x32x16_bf16 v[0:15], v[96:99], v[132:135], 0\n\t"
        "ds_read_b32 v126, v125\n\t"
        "ds_read_b64_tr_b16 v[96:97], v123 offset:6144\n\t"
        "ds_read_b64_tr_b16 v[98:99], v123 offset:9216\n\t"
        "s_waitcnt lgkmcnt(12)\n\t"
        "v_mfma_f32_32x32x16_bf16 v[16:31], v[100:103], v[132:135], 0\n\t"
        "v_add_u32 v129, s46, v121\n\t"
        "ds_read_b64_tr_b16 v[100:101], v124 offset:6144\n\t"
        "ds_read_b64_tr_b16 v[102:103], v124 offset:9216\n\t"
        "s_waitcnt lgkmcnt(12)\n\t"
        "v_mfma_f32_32x32x16_bf16 v[32:47], v[104:107], v[132:135], 0\n\t"
        "ds_read_b64_tr_b16 v[104:105], v123 offset:6272\n\t"
        "ds_read_b64_tr_b16 v[106:107], v123 offset:9344\n\t"
        "s_waitcnt lgkmcnt(12)\n\t"
        "v_mfma_f32_32x32x16_bf16 v[48:63], v[108:111], v[132:135], 0\n\t"
        "v_add_u32 v130, s46, v122\n\t"
        "ds_read_b64_tr_b16 v[108:109], v124 offset:6272\n\t"
        "ds_read_b64_tr_b16 v[110:111], v124 offset:9344\n\t"
        "s_waitcnt lgkmcnt(12)\n\t"
        "v_mfma_f32_32x32x16_bf16 v[64:79], v[132:135], v[112:115], 0\n\t"
        "ds_read_b64_tr_b16 v[112:113], v123 offset:6400\n\t"
        "ds_read_b64_tr_b16 v[114:115], v123 offset:9472\n\t"
        "s_waitcnt lgkmcnt(10)\n\t"
        "v_mfma_f32_32x32x16_bf16 v[80:95], v[132:135], v[116:119], 0\n\t"
        "v_cmp_gt_u32 vcc, s47, v126\n\t"
        "ds_read_b64_tr_b16 v[116:117], v124 offset:6400\n\t"
        "ds_read_b64_tr_b16 v[118:119], v124 offset:9472\n\t"
        "s_waitcnt vmcnt(14) lgkmcnt(10)\n\t"
        "v_mfma_f32_32x32x16_bf16 v[0:15], v[96:99], v[136:139], v[0:15]\n\t"
        "s_cbranch_vccnz 12f\n\t"
        "13:\n\t"
        "ds_read_b64_tr_b16 v[96:97], v129 offset:0\n\t"
        "ds_read_b64_tr_b16 v[98:99], v129 offset:3072\n\t"
        "s_waitcnt lgkmcnt(10)\n\t"
        "v_mfma_f32_32x32x16_bf16 v[16:31], v[100:103], v[136:139], v[16:31]\n\t"
        "v_add_u32 v128, 1, v128\n\t"
        "ds_read_b64_tr_b16 v[100:101], v130 offset:0\n\t"
        "ds_read_b64_tr_b16 v[102:103], v130 offset:3072\n\t"
        "s_waitcnt lgkmcnt(10)\n\t"
        "v_mfma_f32_32x32x16_bf16 v[32:47], v[104:107], v[136:139], v[32:47]\n\t"
        "ds_read_b64_tr_b16 v[104:105], v129 offset:128\n\t"
        "ds_read_b64_tr_b16 v[106:107], v129 offset:3200\n\t"
        "s_waitcnt lgkmcnt(10)\n\t"
        "v_mfma_f32_32x32x16_bf16 v[48:63], v[108:111], v[136:139], v[48:63]\n\t"
        "s_add_u32 s46, s46, 12288\n\t"
        "s_add_u32 s47, s47, 1\n\t"
        "ds_read_b64_tr_b16 v[108:109], v130 offset:128\n\t"
        "ds_read_b64_tr_b16 v[110:111], v130 offset:3200\n\t"
        "s_waitcnt lgkmcnt(10)\n\t"
        "v_mfma_f32_32x32x16_bf16 v[64:79], v[136:139], v[112:115], v[64:79]\n\t"
        "s_cmp_eq_u32 s46, 110592\n\t"
        "s_cselect_b32 s46, 0, s46\n\t"
        "ds_read_b64_tr_b16 v[112:113], v129 offset:256\n\t"
        "ds_read_b64_tr_b16 v[114:115], v129 offset:3328\n\t"
        "s_waitcnt lgkmcnt(10)\n\t"
        "v_mfma_f32_32x32x16_bf16 v[80:95], v[136:139], v[116:119], v[80:95]\n\t"
        "s_cmp_lt_u32 s47, 32\n\t"
        "s_cbranch_scc1 4f\n\t"
        "s_waitcnt vmcnt(0)\n\t"
        "4:\n\t"
        "ds_write_b32 v127, v128\n\t"
        "ds_read_b64_tr_b16 v[116:117], v130 offset:256\n\t"
        "ds_read_b64_tr_b16 v[118:119], v130 offset:3328\n\t"
        "s_waitcnt vmcnt(13) lgkmcnt(11)\n\t"
        "v_mfma_f32_32x32x16_bf16 v[0:15], v[96:99], v[140:143], v[0:15]\n\t"
        "ds_read_b32 v126, v125\n\t"
        "ds_read_b64_tr_b16 v[96:97], v129 offset:6144\n\t"
        "ds_read_b64_tr_b16 v[98:99], v129 offset:9216\n\t"
        "s_waitcnt lgkmcnt(12)\n\t"
        "v_mfma_f32_32x32x16_bf16 v[16:31], v[100:103], v[140:143], v[16:31]\n\t"
        "v_add_u32 v123, s46, v121\n\t"
        "ds_read_b64_tr_b16 v[100:101], v130 offset:6144\n\t"
        "ds_read_b64_tr_b16 v[102:103], v130 offset:9216\n\t"
        "s_waitcnt lgkmcnt(12)\n\t"
        "v_mfma_f32_32x32x16_bf16 v[32:47], v[104:107], v[140:143], v[32:47]\n\t"
        "ds_read_b64_tr_b16 v[104:105], v129 offset:6272\n\t"
        "ds_read_b64_tr_b16 v[106:107], v129 offset:9344\n\t"
        "s_waitcnt lgkmcnt(12)\n\t"
        "v_mfma_f32_32x32x16_bf16 v[48:63], v[108:111], v[140:143], v[48:63]\n\t"
        "v_add_u32 v124, s46, v122\n\t"
        "ds_read_b64_tr_b16 v[108:109], v130 offset:6272\n\t"
        "ds_read_b64_tr_b16 v[110:111], v130 offset:9344\n\t"
        "s_waitcnt lgkmcnt(12)\n\t"
        "v_mfma_f32_32x32x16_bf16 v[64:79], v[140:143], v[112:115], v[64:79]\n\t"
        "ds_read_b64_tr_b16 v[112:113], v129 offset:6400\n\t"
        "ds_read_b64_tr_b16 v[114:115], v129 offset:9472\n\t"
        "s_waitcnt lgkmcnt(10)\n\t"
        "v_mfma_f32_32x32x16_bf16 v[80:95], v[140:143], v[116:119], v[80:95]\n\t"
        "v_cmp_gt_u32 vcc, s47, v126\n\t"
        "ds_read_b64_tr_b16 v[116:117], v130 offset:6400\n\t"
        "ds_read_b64_tr_b16 v[118:119], v130 offset:9472\n\t"
        "s_waitcnt vmcnt(12) lgkmcnt(10)\n\t"
        "v_mfma_f32_32x32x16_bf16 v[0:15], v[96:99], v[144:147], v[0:15]\n\t"
        "s_cbranch_vccnz 14f\n\t"
        "15:\n\t"
        "ds_read_b64_tr_b16 v[96:97], v123 offset:0\n\t"
        "ds_read_b64_tr_b16 v[98:99], v123 offset:3072\n\t"
        "s_waitcnt lgkmcnt(10)\n\t"
        "v_mfma_f32_32x32x16_bf16 v[16:31], v[100:103], v[144:147], v[16:31]\n\t"
        "v_add_u32 v128, 1, v128\n\t"
        "ds_read_b64_tr_b16 v[100:101], v124 offset:0\n\t"
        "ds_read_b64_tr_b16 v[102:103], v124 offset:3072\n\t"
        "s_waitcnt lgkmcnt(10)\n\t"
        "v_mfma_f32_32x32x16_bf16 v[32:47], v[104:107], v[144:147], v[32:47]\n\t"
        "ds_read_b64_tr_b16 v[104:105], v123 offset:128\n\t"
        "ds_read_b64_tr_b16 v[106:107], v123 offset:3200\n\t"
        "s_waitcnt lgkmcnt(10)\n\t"
        "v_mfma_f32_32x32x16_bf16 v[48:63], v[108:111], v[144:147], v[48:63]\n\t"
        "s_add_u32 s46, s46, 12288\n\t"
        "s_add_u32 s47, s47, 1\n\t"
        "ds_read_b64_tr_b16 v[108:109], v124 offset:128\n\t"
        "ds_read_b64_tr_b16 v[110:111], v124 offset:3200\n\t"
        "s_waitcnt lgkmcnt(10)\n\t"
        "v_mfma_f32_32x32x16_bf16 v[64:79], v[144:147], v[112:115], v[64:79]\n\t"
        "s_cmp_eq_u32 s46, 110592\n\t"
        "s_cselect_b32 s46, 0, s46\n\t"
        "ds_read_b64_tr_b16 v[112:113], v123 offset:256\n\t"
        "ds_read_b64_tr_b16 v[114:115], v123 offset:3328\n\t"
        "s_waitcnt lgkmcnt(10)\n\t"
        "v_mfma_f32_32x32x16_bf16 v[80:95], v[144:147], v[116:119], v[80:95]\n\t"
        "ds_write_b32 v127, v128\n\t"
        "ds_read_b64_tr_b16 v[116:117], v124 offset:256\n\t"
        "ds_read_b64_tr_b16 v[118:119], v124 offset:3328\n\t"
        "s_waitcnt lgkmcnt(0)\n\t"
        "s_nop 7\n\t"
        "s_branch 9f\n\t"
        "10:\n\t"
        "s_mov_b32 s48, 0x10000\n\t"
        "8:\n\t"
        "ds_read_b32 v126, v125\n\t"
        "s_sub_u32 s48, s48, 1\n\t"
        "s_waitcnt lgkmcnt(0)\n\t"
        "s_cbranch_scc1 11b\n\t"
        "v_cmp_gt_u32 vcc, s47, v126\n\t"
        "s_cbranch_vccnz 8b\n\t"
        "s_branch 11b\n\t"
        "12:\n\t"
        "s_mov_b32 s48, 0x10000\n\t"
        "8:\n\t"
        "ds_read_b32 v126, v125\n\t"
        "s_sub_u32 s48, s48, 1\n\t"
        "s_waitcnt lgkmcnt(0)\n\t"
        "s_cbranch_scc1 13b\n\t"
        "v_cmp_gt_u32 vcc, s47, v126\n\t"
        "s_cbranch_vccnz 8b\n\t"
        "s_branch 13b\n\t"
        "14:\n\t"
        "s_mov_b32 s48, 0x10000\n\t"
        "8:\n\t"
        "ds_read_b32 v126, v125\n\t"
        "s_sub_u32 s48, s48, 1\n\t"
        "s_waitcnt lgkmcnt(0)\n\t"
        "s_cbranch_scc1 15b\n\t"
        "v_cmp_gt_u32 vcc, s47, v126\n\t"
        "s_cbranch_vccnz 8b\n\t"
        "s_branch 15b\n\t"
        "9:\n\t"
        : "={v[0:15]}"(acc[0]), "={v[16:31]}"(acc[1]), "={v[32:47]}"(acc[2]), "={v[48:63]}"(acc[3]), "={v[64:79]}"(acc[4]), "={v[80:95]}"(acc[5]),
          "={v[96:111]}"(stA), "+{v[112:127]}"(stB), "+{v[128:131]}"(stC), "={s46}"(slot), "+{s47}"(need)
        : "{v[132:147]}"(xr0) : "s48", "vcc", "scc", "memory");
      STAMPV(2 + 16 * L);
      float al = 1.f, be = 0.f;
      if (L == 1) {
        PHASE_VARS;
        float S, Q, Dd;
        row_stats_sum(rs, OFF_SX, tile, lane, S, Q, Dd);
        float mean, var, mb1, rd1, mb2, den2;
        row_stats_fin(smem, 1, tile, lane, S, Q, Dd, U2F(words[0]), x1p, mean, var, mb1, rd1);
        ln_scalars(mean + (mean - mb1) * rd1, var * (1.0f + rd1) * (1.0f + rd1), mb2, den2);
        const float rd2 = RCP(den2);
        al = (1.0f + rd1) * rd2; be = -(mb1 * rd1 + mb2) * rd2;
        reinterpret_cast<float*>(smem + L_AB)[wid * 64 + lane] = hi ? be : al;
        reinterpret_cast<float*>(smem + L_AB)[256 + wid * 64 + lane] = hi ? rd1 : mb1;
        STAMPV(36);
        lds_wait_ge<0>(lbase + L_W + 24, 2);
        soff = ((tn + 1) & 15) * 4096;
        asm volatile(
          "buffer_load_dwordx4 v[148:151], v120, s[40:43], s44 offen offset:0 sc1\n\t"
          "buffer_load_dwordx4 v[152:155], v120, s[40:43], s44 offen offset:1024 sc1\n\t"
          "buffer_load_dwordx4 v[156:159], v120, s[40:43], s44 offen offset:2048 sc1\n\t"
          "buffer_load_dwordx4 v[160:163], v120, s[40:43], s44 offen offset:3072 sc1\n\t"
          "s_add_u32 s44, s44, 0x1000\n\t"
          "s_and_b32 s44, s44, 0xffff\n\t"
          "buffer_load_dwordx4 v[164:167], v120, s[40:43], s44 offen offset:0 sc1\n\t"
          "buffer_load_dwordx4 v[168:171], v120, s[40:43], s44 offen offset:1024 sc1\n\t"
          "buffer_load_dwordx4 v[172:175], v120, s[40:43], s44 offen offset:2048 sc1\n\t"
          "buffer_load_dwordx4 v[176:179], v120, s[40:43], s44 offen offset:3072 sc1\n\t"
          "s_add_u32 s44, s44, 0x1000\n\t"
          "s_and_b32 s44, s44, 0xffff\n\t"
          "buffer_load_dwordx4 v[180:183], v120, s[40:43], s44 offen offset:0 sc1\n\t"
          "buffer_load_dwordx4 v[184:187], v120, s[40:43], s44 offen offset:1024 sc1\n\t"
          "buffer_load_dwordx4 v[188:191], v120, s[40:43], s44 offen offset:2048 sc1\n\t"
          "buffer_load_dwordx4 v[192:195], v120, s[40:43], s44 offen offset:3072 sc1\n\t"
          : "={v[148:163]}"(xr1), "={v[164:179]}"(xr2), "={v[180:195]}"(xr3), "+{s44}"(soff) : "{v[112:127]}"(stB), "{s[40:43]}"(srd) : "scc", "memory");
      }
      STAMPV(3 + 16 * L);
      soff = ((tn + 1 + 2) & 15) * 4096;
      asm volatile(
        "s_nop 0\n\t"
        "s_add_u32 s44, s44, 0x1000\n\t"
        "s_and_b32 s44, s44, 0xffff\n\t"
        "buffer_load_dwordx4 v[132:135], v120, s[40:43], s44 offen offset:0 sc1\n\t"
        "buffer_load_dwordx4 v[136:139], v120, s[40:43], s44 offen offset:1024 sc1\n\t"
        "buffer_load_dwordx4 v[140:143], v120, s[40:43], s44 offen offset:2048 sc1\n\t"
        "s_mov_b32 s45, 3\n\t"
        "1:\n\t"
        "s_waitcnt vmcnt(14) lgkmcnt(11)\n\t"
        "v_mfma_f32_32x32x16_bf16 v[0:15], v[96:99], v[148:151], v[0:15]\n\t"
        "ds_read_b32 v126, v125\n\t"
        "ds_read_b64_tr_b16 v[96:97], v123 offset:6144\n\t"
        "ds_read_b64_tr_b16 v[98:99], v123 offset:9216\n\t"
        "s_waitcnt lgkmcnt(12)\n\t"
        "v_mfma_f32_32x32x16_bf16 v[16:31], v[100:103], v[148:151], v[16:31]\n\t"
        "v_add_u32 v129, s46, v121\n\t"
        "ds_read_b64_tr_b16 v[100:101], v124 offset:6144\n\t"
        "ds_read_b64_tr_b16 v[102:103], v124 offset:9216\n\t"
        "s_waitcnt lgkmcnt(12)\n\t"
        "v_mfma_f32_32x32x16_bf16 v[32:47], v[104:107], v[148:151], v[32:47]\n\t"
        "ds_read_b64_tr_b16 v[104:105], v123 offset:6272\n\t"
        "ds_read_b64_tr_b16 v[106:107], v123 offset:9344\n\t"
        "buffer_load_dwordx4 v[144:147], v120, s[40:43], s44 offen offset:3072 sc1\n\t"
        "s_waitcnt lgkmcnt(12)\n\t"
        "v_mfma_f32_32x32x16_bf16 v[48:63], v[108:111], v[148:151], v[48:63]\n\t"
        "v_add_u32 v130, s46, v122\n\t"
        "ds_read_b64_tr_b16 v[108:109], v124 offset:6272\n\t"
        "ds_read_b64_tr_b16 v[110:111], v124 offset:9344\n\t"
        "s_waitcnt lgkmcnt(12)\n\t"
        "v_mfma_f32_32x32x16_bf16 v[64:79], v[148:151], v[112:115], v[64:79]\n\t"
        "ds_read_b64_tr_b16 v[112:113], v123 offset:6400\n\t"
        "ds_read_b64_tr_b16 v[114:115], v123 offset:9472\n\t"
        "s_waitcnt lgkmcnt(10)\n\t"
        "v_mfma_f32_32x32x16_bf16 v[80:95], v[148:151], v[116:119], v[80:95]\n\t"
        "v_cmp_gt_u32 vcc, s47, v126\n\t"
        "ds_read_b64_tr_b16 v[116:117], v124 offset:6400\n\t"
        "ds_read_b64_tr_b16 v[118:119], v124 offset:9472\n\t"
        "s_waitcnt vmcnt(14) lgkmcnt(10)\n\t"
        "v_mfma_f32_32x32x16_bf16 v[0:15], v[96:99], v[152:155], v[0:15]\n\t"
        "s_cbranch_vccnz 16f\n\t"
        "17:\n\t"
        "ds_read_b64_tr_b16 v[96:97], v129 offset:0\n\t"
        "ds_read_b64_tr_b16 v[98:99], v129 offset:3072\n\t"
        "s_waitcnt lgkmcnt(10)\n\t"
        "v_mfma_f32_32x32x16_bf16 v[16:31], v[100:103], v[152:155], v[16:31]\n\t"
        "v_add_u32 v128, 1, v128\n\t"
        "ds_read_b64_tr_b16 v[100:101], v130 offset:0\n\t"
        "ds_read_b64_tr_b16 v[102:103], v130 offset:3072\n\t"
        "s_waitcnt lgkmcnt(10)\n\t"
        "v_mfma_f32_32x32x16_bf16 v[32:47], v[104:107], v[152:155], v[32:47]\n\t"
        "ds_read_b64_tr_b16 v[104:105], v129 offset:128\n\t"
        "ds_read_b64_tr_b16 v[106:107], v129 offset:3200\n\t"
        "s_add_u32 s44, s44, 0x1000\n\t"
        "s_and_b32 s44, s44, 0xffff\n\t"
        "buffer_load_dwordx4 v[148:151], v120, s[40:43], s44 offen offset:0 sc1\n\t"
        "s_waitcnt lgkmcnt(10)\n\t"
        "v_mfma_f32_32x32x16_bf16 v[48:63], v[108:111], v[152:155], v[48:63]\n\t"
        "s_add_u32 s46, s46, 12288\n\t"
        "s_add_u32 s47, s47, 1\n\t"
        "ds_read_b64_tr_b16 v[108:109], v130 offset:128\n\t"
        "ds_read_b64_tr_b16 v[110:111], v130 offset:3200\n\t"
        "s_waitcnt lgkmcnt(10)\n\t"
        "v_mfma_f32_32x32x16_bf16 v[64:79], v[152:155], v[112:115], v[64:79]\n\t"
        "s_cmp_eq_u32 s46, 110592\n\t"
        "s_cselect_b32 s46, 0, s46\n\t"
        "ds_read_b64_tr_b16 v[112:113], v129 offset:256\n\t"
        "ds_read_b64_tr_b16 v[114:115], v129 offset:3328\n\t"
        "s_waitcnt lgkmcnt(10)\n\t"
        "v_mfma_f32_32x32x16_bf16 v[80:95], v[152:155], v[116:119], v[80:95]\n\t"
        "ds_write_b32 v127, v128\n\t"
        "ds_read_b64_tr_b16 v[116:117], v130 offset:256\n\t"
        "ds_read_b64_tr_b16 v[118:119], v130 offset:3328\n\t"
        "s_waitcnt vmcnt(14) lgkmcnt(11)\n\t"
        "v_mfma_f32_32x32x16_bf16 v[0:15], v[96:99], v[156:159], v[0:15]\n\t"
        "ds_read_b32 v126, v125\n\t"
        "ds_read_b64_tr_b16 v[96:97], v129 offset:6144\n\t"
        "ds_read_b64_tr_b16 v[98:99], v129 offset:9216\n\t"
        "s_waitcnt lgkmcnt(12)\n\t"
        "v_mfma_f32_32x32x16_bf16 v[16:31], v[100:103], v[156:159], v[16:31]\n\t"
        "v_add_u32 v123, s46, v121\n\t"
        "ds_read_b64_tr_b16 v[100:101], v130 offset:6144\n\t"
        "ds_read_b64_tr_b16 v[102:103], v130 offset:9216\n\t"
        "s_waitcnt lgkmcnt(12)\n\t"
        "v_mfma_f32_32x32x16_bf16 v[32:47], v[104:107], v[156:159], v[32:47]\n\t"
        "ds_read_b64_tr_b16 v[104:105], v129 offset:6272\n\t"
        "ds_read_b64_tr_b16 v[106:107], v129 offset:9344\n\t"
        "buffer_load_dwordx4 v[152:155], v120, s[40:43], s44 offen offset:1024 sc1\n\t"
        "s_waitcnt lgkmcnt(12)\n\t"
        "v_mfma_f32_32x32x16_bf16 v[48:63], v[108:111], v[156:159], v[48:63]\n\t"
        "v_add_u32 v124, s46, v122\n\t"
        "ds_read_b64_tr_b16 v[108:109], v130 offset:6272\n\t"
        "ds_read_b64_tr_b16 v[110:111], v130 offset:9344\n\t"
        "s_waitcnt lgkmcnt(12)\n\t"
        "v_mfma_f32_32x32x16_bf16 v[64:79], v[156:159], v[112:115], v[64:79]\n\t"
        "ds_read_b64_tr_b16 v[112:113], v129 offset:6400\n\t"
        "ds_read_b64_tr_b16 v[114:115], v129 offset:9472\n\t"
        "s_waitcnt lgkmcnt(10)\n\t"
        "v_mfma_f32_32x32x16_bf16 v[80:95], v[156:159], v[116:119], v[80:95]\n\t"
        "v_cmp_gt_u32 vcc, s47, v126\n\t"
        "ds_read_b64_tr_b16 v[116:117], v130 offset:6400\n\t"
        "ds_read_b64_tr_b16 v[118:119], v130 offset:9472\n\t"
        "s_waitcnt vmcnt(14) lgkmcnt(10)\n\t"
        "v_mfma_f32_32x32x16_bf16 v[0:15], v[96:99], v[160:163], v[0:15]\n\t"
        "s_cbranch_vccnz 18f\n\t"
        "19:\n\t"
        "ds_read_b64_tr_b16 v[96:97], v123 offset:0\n\t"
        "ds_read_b64_tr_b16 v[98:99], v123 offset:3072\n\t"
        "s_waitcnt lgkmcnt(10)\n\t"
        "v_mfma_f32_32x32x16_bf16 v[16:31], v[100:103], v[160:163], v[16:31]\n\t"
        "v_add_u32 v128, 1, v128\n\t"
        "ds_read_b64_tr_b16 v[100:101], v124 offset:0\n\t"
        "ds_read_b64_tr_b16 v[102:103], v124 offset:3072\n\t"
        "s_waitcnt lgkmcnt(10)\n\t"
        "v_mfma_f32_32x32x16_bf16 v[32:47], v[104:107], v[160:163], v[32:47]\n\t"
        "ds_read_b64_tr_b16 v[104:105], v123 offset:128\n\t"
        "ds_read_b64_tr_b16 v[106:107], v123 offset:3200\n\t"
        "buffer_load_dwordx4 v[156:159], v120, s[40:43], s44 offen offset:2048 sc1\n\t"
        "s_waitcnt lgkmcnt(10)\n\t"
        "v_mfma_f32_32x32x16_bf16 v[48:63], v[108:111], v[160:163], v[48:63]\n\t"
        "s_add_u32 s46, s46, 12288\n\t"
        "s_add_u32 s47, s47, 1\n\t"
        "ds_read_b64_tr_b16 v[108:109], v124 offset:128\n\t"
        "ds_read_b64_tr_b16 v[110:111], v124 offset:3200\n\t"
        "s_waitcnt lgkmcnt(10)\n\t"
        "v_mfma_f32_32x32x16_bf16 v[64:79], v[160:163], v[112:115], v[64:79]\n\t"
        "s_cmp_eq_u32 s46, 110592\n\t"
        "s_cselect_b32 s46, 0, s46\n\t"
        "ds_read_b64_tr_b16 v[112:113], v123 offset:256\n\t"
        "ds_read_b64_tr_b16 v[114:115], v123 offset:3328\n\t"
        "s_waitcnt lgkmcnt(10)\n\t"
        "v_mfma_f32_32x32x16_bf16 v[80:95], v[160:163], v[116:119], v[80:95]\n\t"
        "ds_write_b32 v127, v128\n\t"
        "ds_read_b64_tr_b16 v[116:117], v124 offset:256\n\t"
        "ds_read_b64_tr_b16 v[118:119], v124 offset:3328\n\t"
        "s_waitcnt vmcnt(14) lgkmcnt(11)\n\t"
        "v_mfma_f32_32x32x16_bf16 v[0:15], v[96:99], v[164:167], v[0:15]\n\t"
        "ds_read_b32 v126, v125\n\t"
        "ds_read_b64_tr_b16 v[96:97], v123 offset:6144\n\t"
        "ds_read_b64_tr_b16 v[98:99], v123 offset:9216\n\t"
        "s_waitcnt lgkmcnt(12)\n\t"
        "v_mfma_f32_32x32x16_bf16 v[16:31], v[100:103], v[164:167], v[16:31]\n\t"
        "v_add_u32 v129, s46, v121\n\t"
        "ds_read_b64_tr_b16 v[100:101], v124 offset:6144\n\t"
        "ds_read_b64_tr_b16 v[102:103], v124 offset:9216\n\t"
        "s_waitcnt lgkmcnt(12)\n\t"
        "v_mfma_f32_32x32x16_bf16 v[32:47], v[104:107], v[164:167], v[32:47]\n\t"
        "ds_read_b64_tr_b16 v[104:105], v123 offset:6272\n\t"
        "ds_read_b64_tr_b16 v[106:107], v123 offset:9344\n\t"
        "buffer_load_dwordx4 v[160:163], v120, s[40:43], s44 offen offset:3072 sc1\n\t"
        "s_waitcnt lgkmcnt(12)\n\t"
        "v_mfma_f32_32x32x16_bf16 v[48:63], v[108:111], v[164:167], v[48:63]\n\t"
        "v_add_u32 v130, s46, v122\n\t"
        "ds_read_b64_tr_b16 v[108:109], v124 offset:6272\n\t"
        "ds_read_b64_tr_b16 v[110:111], v124 offset:9344\n\t"
        "s_waitcnt lgkmcnt(12)\n\t"
        "v_mfma_f32_32x32x16_bf16 v[64:79], v[164:167], v[112:115], v[64:79]\n\t"
        "ds_read_b64_tr_b16 v[112:113], v123 offset:6400\n\t"
        "ds_read_b64_tr_b16 v[114:115], v123 offset:9472\n\t"
        "s_waitcnt lgkmcnt(10)\n\t"
        "v_mfma_f32_32x32x16_bf16 v[80:95], v[164:167], v[116:119], v[80:95]\n\t"
        "v_cmp_gt_u32 vcc, s47, v126\n\t"
        "ds_read_b64_tr_b16 v[116:117], v124 offset:6400\n\t"
        "ds_read_b64_tr_b16 v[118:119], v124 offset:9472\n\t"
        "s_waitcnt vmcnt(14) lgkmcnt(10)\n\t"
        "v_mfma_f32_32x32x16_bf16 v[0:15], v[96:99], v[168:171], v[0:15]\n\t"
        "s_cbranch_vccnz 20f\n\t"
        "21:\n\t"
        "ds_read_b64_tr_b16 v[96:97], v129 offset:0\n\t"
        "ds_read_b64_tr_b16 v[98:99], v129 offset:3072\n\t"
        "s_waitcnt lgkmcnt(10)\n\t"
        "v_mfma_f32_32x32x16_bf16 v[16:31], v[100:103], v[168:171], v[16:31]\n\t"
        "v_add_u32 v128, 1, v128\n\t"
        "ds_read_b64_tr_b16 v[100:101], v130 offset:0\n\t"
        "ds_read_b64_tr_b16 v[102:103], v130 offset:3072\n\t"
        "s_waitcnt lgkmcnt(10)\n\t"
        "v_mfma_f32_32x32x16_bf16 v[32:47], v[104:107], v[168:171], v[32:47]\n\t"
        "ds_read_b64_tr_b16 v[104:105], v129 offset:128\n\t"
        "ds_read_b64_tr_b16 v[106:107], v129 offset:3200\n\t"
        "s_add_u32 s44, s44, 0x1000\n\t"
        "s_and_b32 s44, s44, 0xffff\n\t"
        "buffer_load_dwordx4 v[164:167], v120, s[40:43], s44 offen offset:0 sc1\n\t"
        "s_waitcnt lgkmcnt(10)\n\t"
        "v_mfma_f32_32x32x16_bf16 v[48:63], v[108:111], v[168:171], v[48:63]\n\t"
        "s_add_u32 s46, s46, 12288\n\t"
        "s_add_u32 s47, s47, 1\n\t"
        "ds_read_b64_tr_b16 v[108:109], v130 offset:128\n\t"
        "ds_read_b64_tr_b16 v[110:111], v130 offset:3200\n\t"
        "s_waitcnt lgkmcnt(10)\n\t"
        "v_mfma_f32_32x32x16_bf16 v[64:79], v[168:171], v[112:115], v[64:79]\n\t"
        "s_cmp_eq_u32 s46, 110592\n\t"
        "s_cselect_b32 s46, 0, s46\n\t"
        "ds_read_b64_tr_b16 v[112:113], v129 offset:256\n\t"
        "ds_read_b64_tr_b16 v[114:115], v129 offset:3328\n\t"
        "s_waitcnt lgkmcnt(10)\n\t"
        "v_mfma_f32_32x32x16_bf16 v[80:95], v[168:171], v[116:119], v[80:95]\n\t"
        "ds_write_b32 v127, v128\n\t"
        "ds_read_b64_tr_b16 v[116:117], v130 offset:256\n\t"
        "ds_read_b64_tr_b16 v[118:119], v130 offset:3328\n\t"
        "s_waitcnt vmcnt(14) lgkmcnt(11)\n\t"
        "v_mfma_f32_32x32x16_bf16 v[0:15], v[96:99], v[172:175], v[0:15]\n\t"
        "ds_read_b32 v126, v125\n\t"
        "ds_read_b64_tr_b16 v[96:97], v129 offset:6144\n\t"
        "ds_read_b64_tr_b16 v[98:99], v129 offset:9216\n\t"
        "s_waitcnt lgkmcnt(12)\n\t"
        "v_mfma_f32_32x32x16_bf16 v[16:31], v[100:103], v[172:175], v[16:31]\n\t"
        "v_add_u32 v123, s46, v121\n\t"
        "ds_read_b64_tr_b16 v[100:101], v130 offset:6144\n\t"
        "ds_read_b64_tr_b16 v[102:103], v130 offset:9216\n\t"
        "s_waitcnt lgkmcnt(12)\n\t"
        "v_mfma_f32_32x32x16_bf16 v[32:47], v[104:107], v[172:175], v[32:47]\n\t"
        "ds_read_b64_tr_b16 v[104:105], v129 offset:6272\n\t"
        "ds_read_b64_tr_b16 v[106:107], v129 offset:9344\n\t"
        "buffer_load_dwordx4 v[168:171], v120, s[40:43], s44 offen offset:1024 sc1\n\t"
        "s_waitcnt lgkmcnt(12)\n\t"
        "v_mfma_f32_32x32x16_bf16 v[48:63], v[108:111], v[172:175], v[48:63]\n\t"
        "v_add_u32 v124, s46, v122\n\t"
        "ds_read_b64_tr_b16 v[108:109], v130 offset:6272\n\t"
        "ds_read_b64_tr_b16 v[110:111], v130 offset:9344\n\t"
        "s_waitcnt lgkmcnt(12)\n\t"
        "v_mfma_f32_32x32x16_bf16 v[64:79], v[172:175], v[112:115], v[64:79]\n\t"
        "ds_read_b64_tr_b16 v[112:113], v129 offset:6400\n\t"
        "ds_read_b64_tr_b16 v[114:115], v129 offset:9472\n\t"
        "s_waitcnt lgkmcnt(10)\n\t"
        "v_mfma_f32_32x32x16_bf16 v[80:95], v[172:175], v[116:119], v[80:95]\n\t"
        "v_cmp_gt_u32 vcc, s47, v126\n\t"
        "ds_read_b64_tr_b16 v[116:117], v130 offset:6400\n\t"
        "ds_read_b64_tr_b16 v[118:119], v130 offset:9472\n\t"
        "s_waitcnt vmcnt(14) lgkmcnt(10)\n\t"
        "v_mfma_f32_32x32x16_bf16 v[0:15], v[96:99], v[176:179], v[0:15]\n\t"
        "s_cbranch_vccnz 22f\n\t"
        "23:\n\t"
        "ds_read_b64_tr_b16 v[96:97], v123 offset:0\n\t"
        "ds_read_b64_tr_b16 v[98:99], v123 offset:3072\n\t"
        "s_waitcnt lgkmcnt(10)\n\t"
        "v_mfma_f32_32x32x16_bf16 v[16:31], v[100:103], v[176:179], v[16:31]\n\t"
        "v_add_u32 v128, 1, v128\n\t"
        "ds_read_b64_tr_b16 v[100:101], v124 offset:0\n\t"
        "ds_read_b64_tr_b16 v[102:103], v124 offset:3072\n\t"
        "s_waitcnt lgkmcnt(10)\n\t"
        "v_mfma_f32_32x32x16_bf16 v[32:47], v[104:107], v[176:179], v[32:47]\n\t"
        "ds_read_b64_tr_b16 v[104:105], v123 offset:128\n\t"
        "ds_read_b64_tr_b16 v[106:107], v123 offset:3200\n\t"
        "buffer_load_dwordx4 v[172:175], v120, s[40:43], s44 offen offset:2048 sc1\n\t"
        "s_waitcnt lgkmcnt(10)\n\t"
        "v_mfma_f32_32x32x16_bf16 v[48:63], v[108:111], v[176:179], v[48:63]\n\t"
        "s_add_u32 s46, s46, 12288\n\t"
        "s_add_u32 s47, s47, 1\n\t"
        "ds_read_b64_tr_b16 v[108:109], v124 offset:128\n\t"
        "ds_read_b64_tr_b16 v[110:111], v124 offset:3200\n\t"
        "s_waitcnt lgkmcnt(10)\n\t"
        "v_mfma_f32_32x32x16_bf16 v[64:79], v[176:179], v[112:115], v[64:79]\n\t"
        "s_cmp_eq_u32 s46, 110592\n\t"
        "s_cselect_b32 s46, 0, s46\n\t"
        "ds_read_b64_tr_b16 v[112:113], v123 offset:256\n\t"
        "ds_read_b64_tr_b16 v[114:115], v123 offset:3328\n\t"
        "s_waitcnt lgkmcnt(10)\n\t"
        "v_mfma_f32_32x32x16_bf16 v[80:95], v[176:179], v[116:119], v[80:95]\n\t"
        "ds_write_b32 v127, v128\n\t"
        "ds_read_b64_tr_b16 v[116:117], v124 offset:256\n\t"
        "ds_read_b64_tr_b16 v[118:119], v124 offset:3328\n\t"
        "s_waitcnt vmcnt(14) lgkmcnt(11)\n\t"
        "v_mfma_f32_32x32x16_bf16 v[0:15], v[96:99], v[180:183], v[0:15]\n\t"
        "ds_read_b32 v126, v125\n\t"
        "ds_read_b64_tr_b16 v[96:97], v123 offset:6144\n\t"
        "ds_read_b64_tr_b16 v[98:99], v123 offset:9216\n\t"
        "s_waitcnt lgkmcnt(12)\n\t"
        "v_mfma_f32_32x32x16_bf16 v[16:31], v[100:103], v[180:183], v[16:31]\n\t"
        "v_add_u32 v129, s46, v121\n\t"
        "ds_read_b64_tr_b16 v[100:101], v124 offset:6144\n\t"
        "ds_read_b64_tr_b16 v[102:103], v124 offset:9216\n\t"
        "s_waitcnt lgkmcnt(12)\n\t"
        "v_mfma_f32_32x32x16_bf16 v[32:47], v[104:107], v[180:183], v[32:47]\n\t"
        "ds_read_b64_tr_b16 v[104:105], v123 offset:6272\n\t"
        "ds_read_b64_tr_b16 v[106:107], v123 offset:9344\n\t"
        "buffer_load_dwordx4 v[176:179], v120, s[40:43], s44 offen offset:3072 sc1\n\t"
        "s_waitcnt lgkmcnt(12)\n\t"
        "v_mfma_f32_32x32x16_bf16 v[48:63], v[108:111], v[180:183], v[48:63]\n\t"
        "v_add_u32 v130, s46, v122\n\t"
        "ds_read_b64_tr_b16 v[108:109], v124 offset:6272\n\t"
        "ds_read_b64_tr_b16 v[110:111], v124 offset:9344\n\t"
        "s_waitcnt lgkmcnt(12)\n\t"
        "v_mfma_f32_32x32x16_bf16 v[64:79], v[180:183], v[112:115], v[64:79]\n\t"
        "ds_read_b64_tr_b16 v[112:113], v123 offset:6400\n\t"
        "ds_read_b64_tr_b16 v[114:115], v123 offset:9472\n\t"
        "s_waitcnt lgkmcnt(10)\n\t"
        "v_mfma_f32_32x32x16_bf16 v[80:95], v[180:183], v[116:119], v[80:95]\n\t"
        "v_cmp_gt_u32 vcc, s47, v126\n\t"
        "ds_read_b64_tr_b16 v[116:117], v124 offset:6400\n\t"
        "ds_read_b64_tr_b16 v[118:119], v124 offset:9472\n\t"
        "s_waitcnt vmcnt(14) lgkmcnt(10)\n\t"
        "v_mfma_f32_32x32x16_bf16 v[0:15], v[96:99], v[184:187], v[0:15]\n\t"
        "s_cbranch_vccnz 24f\n\t"
        "25:\n\t"
        "ds_read_b64_tr_b16 v[96:97], v129 offset:0\n\t"
        "ds_read_b64_tr_b16 v[98:99], v129 offset:3072\n\t"
        "s_waitcnt lgkmcnt(10)\n\t"
        "v_mfma_f32_32x32x16_bf16 v[16:31], v[100:103], v[184:187], v[16:31]\n\t"
        "v_add_u32 v128, 1, v128\n\t"
        "ds_read_b64_tr_b16 v[100:101], v130 offset:0\n\t"
        "ds_read_b64_tr_b16 v[102:103], v130 offset:3072\n\t"
        "s_waitcnt lgkmcnt(10)\n\t"
        "v_mfma_f32_32x32x16_bf16 v[32:47], v[104:107], v[184:187], v[32:47]\n\t"
        "ds_read_b64_tr_b16 v[104:105], v129 offset:128\n\t"
        "ds_read_b64_tr_b16 v[106:107], v129 offset:3200\n\t"
        "s_add_u32 s44, s44, 0x1000\n\t"
        "s_and_b32 s44, s44, 0xffff\n\t"
        "buffer_load_dwordx4 v[180:183], v120, s[40:43], s44 offen offset:0 sc1\n\t"
        "s_waitcnt lgkmcnt(10)\n\t"
        "v_mfma_f32_32x32x16_bf16 v[48:63], v[108:111], v[184:187], v[48:63]\n\t"
        "s_add_u32 s46, s46, 12288\n\t"
        "s_add_u32 s47, s47, 1\n\t"
        "ds_read_b64_tr_b16 v[108:109], v130 offset:128\n\t"
        "ds_read_b64_tr_b16 v[110:111], v130 offset:3200\n\t"
        "s_waitcnt lgkmcnt(10)\n\t"
        "v_mfma_f32_32x32x16_bf16 v[64:79], v[184:187], v[112:115], v[64:79]\n\t"
        "s_cmp_eq_u32 s46, 110592\n\t"
        "s_cselect_b32 s46, 0, s46\n\t"
        "ds_read_b64_tr_b16 v[112:113], v129 offset:256\n\t"
        "ds_read_b64_tr_b16 v[114:115], v129 offset:3328\n\t"
        "s_waitcnt lgkmcnt(10)\n\t"
        "v_mfma_f32_32x32x16_bf16 v[80:95], v[184:187], v[116:119], v[80:95]\n\t"
        "ds_write_b32 v127, v128\n\t"
        "ds_read_b64_tr_b16 v[116:117], v130 offset:256\n\t"
        "ds_read_b64_tr_b16 v[118:119], v130 offset:3328\n\t"
        "s_waitcnt vmcnt(14) lgkmcnt(11)\n\t"
        "v_mfma_f32_32x32x16_bf16 v[0:15], v[96:99], v[188:191], v[0:15]\n\t"
        "ds_read_b32 v126, v125\n\t"
        "ds_read_b64_tr_b16 v[96:97], v129 offset:6144\n\t"
        "ds_read_b64_tr_b16 v[98:99], v129 offset:9216\n\t"
        "s_waitcnt lgkmcnt(12)\n\t"
        "v_mfma_f32_32x32x16_bf16 v[16:31], v[100:103], v[188:191], v[16:31]\n\t"
        "v_add_u32 v123, s46, v121\n\t"
        "ds_read_b64_tr_b16 v[100:101], v130 offset:6144\n\t"
        "ds_read_b64_tr_b16 v[102:103], v130 offset:9216\n\t"
        "s_waitcnt lgkmcnt(12)\n\t"
        "v_mfma_f32_32x32x16_bf16 v[32:47], v[104:107], v[188:191], v[32:47]\n\t"
        "ds_read_b64_tr_b16 v[104:105], v129 offset:6272\n\t"
        "ds_read_b64_tr_b16 v[106:107], v129 offset:9344\n\t"
        "buffer_load_dwordx4 v[184:187], v120, s[40:43], s44 offen offset:1024 sc1\n\t"
        "s_waitcnt lgkmcnt(12)\n\t"
        "v_mfma_f32_32x32x16_bf16 v[48:63], v[108:111], v[188:191], v[48:63]\n\t"
        "v_add_u32 v124, s46, v122\n\t"
        "ds_read_b64_tr_b16 v[108:109], v130 offset:6272\n\t"
        "ds_read_b64_tr_b16 v[110:111], v130 offset:9344\n\t"
        "s_waitcnt lgkmcnt(12)\n\t"
        "v_mfma_f32_32x32x16_bf16 v[64:79], v[188:191], v[112:115], v[64:79]\n\t"
        "ds_read_b64_tr_b16 v[112:113], v129 offset:6400\n\t"
        "ds_read_b64_tr_b16 v[114:115], v129 offset:9472\n\t"
        "s_waitcnt lgkmcnt(10)\n\t"
        "v_mfma_f32_32x32x16_bf16 v[80:95], v[188:191], v[116:119], v[80:95]\n\t"
        "v_cmp_gt_u32 vcc, s47, v126\n\t"
        "ds_read_b64_tr_b16 v[116:117], v130 offset:6400\n\t"
        "ds_read_b64_tr_b16 v[118:119], v130 offset:9472\n\t"
        "s_waitcnt vmcnt(14) lgkmcnt(10)\n\t"
        "v_mfma_f32_32x32x16_bf16 v[0:15], v[96:99], v[192:195], v[0:15]\n\t"
        "s_cbranch_vccnz 26f\n\t"
        "27:\n\t"
        "ds_read_b64_tr_b16 v[96:97], v123 offset:0\n\t"
        "ds_read_b64_tr_b16 v[98:99], v123 offset:3072\n\t"
        "s_waitcnt lgkmcnt(10)\n\t"
        "v_mfma_f32_32x32x16_bf16 v[16:31], v[100:103], v[192:195], v[16:31]\n\t"
        "v_add_u32 v128, 1, v128\n\t"
        "ds_read_b64_tr_b16 v[100:101], v124 offset:0\n\t"
        "ds_read_b64_tr_b16 v[102:103], v124 offset:3072\n\t"
        "s_waitcnt lgkmcnt(10)\n\t"
        "v_mfma_f32_32x32x16_bf16 v[32:47], v[104:107], v[192:195], v[32:47]\n\t"
        "ds_read_b64_tr_b16 v[104:105], v123 offset:128\n\t"
        "ds_read_b64_tr_b16 v[106:107], v123 offset:3200\n\t"
        "buffer_load_dwordx4 v[188:191], v120, s[40:43], s44 offen offset:2048 sc1\n\t"
        "s_waitcnt lgkmcnt(10)\n\t"
        "v_mfma_f32_32x32x16_bf16 v[48:63], v[108:111], v[192:195], v[48:63]\n\t"
        "s_add_u32 s46, s46, 12288\n\t"
        "s_add_u32 s47, s47, 1\n\t"
        "ds_read_b64_tr_b16 v[108:109], v124 offset:128\n\t"
        "ds_read_b64_tr_b16 v[110:111], v124 offset:3200\n\t"
        "s_waitcnt lgkmcnt(10)\n\t"
        "v_mfma_f32_32x32x16_bf16 v[64:79], v[192:195], v[112:115], v[64:79]\n\t"
        "s_cmp_eq_u32 s46, 110592\n\t"
        "s_cselect_b32 s46, 0, s46\n\t"
        "ds_read_b64_tr_b16 v[112:113], v123 offset:256\n\t"
        "ds_read_b64_tr_b16 v[114:115], v123 offset:3328\n\t"
        "s_waitcnt lgkmcnt(10)\n\t"
        "v_mfma_f32_32x32x16_bf16 v[80:95], v[192:195], v[116:119], v[80:95]\n\t"
        "ds_write_b32 v127, v128\n\t"
        "ds_read_b64_tr_b16 v[116:117], v124 offset:256\n\t"
        "ds_read_b64_tr_b16 v[118:119], v124 offset:3328\n\t"
        "s_waitcnt vmcnt(14) lgkmcnt(11)\n\t"
        "v_mfma_f32_32x32x16_bf16 v[0:15], v[96:99], v[132:135], v[0:15]\n\t"
        "ds_read_b32 v126, v125\n\t"
        "ds_read_b64_tr_b16 v[96:97], v123 offset:6144\n\t"
        "ds_read_b64_tr_b16 v[98:99], v123 offset:9216\n\t"
        "s_waitcnt lgkmcnt(12)\n\t"
        "v_mfma_f32_32x32x16_bf16 v[16:31], v[100:103], v[132:135], v[16:31]\n\t"
        "v_add_u32 v129, s46, v121\n\t"
        "ds_read_b64_tr_b16 v[100:101], v124 offset:6144\n\t"
        "ds_read_b64_tr_b16 v[102:103], v124 offset:9216\n\t"
        "s_waitcnt lgkmcnt(12)\n\t"
        "v_mfma_f32_32x32x16_bf16 v[32:47], v[104:107], v[132:135], v[32:47]\n\t"
        "ds_read_b64_tr_b16 v[104:105], v123 offset:6272\n\t"
        "ds_read_b64_tr_b16 v[106:107], v123 offset:9344\n\t"
        "buffer_load_dwordx4 v[192:195], v120, s[40:43], s44 offen offset:3072 sc1\n\t"
        "s_waitcnt lgkmcnt(12)\n\t"
        "v_mfma_f32_32x32x16_bf16 v[48:63], v[108:111], v[132:135], v[48:63]\n\t"
        "v_add_u32 v130, s46, v122\n\t"
        "ds_read_b64_tr_b16 v[108:109], v124 offset:6272\n\t"
        "ds_read_b64_tr_b16 v[110:111], v124 offset:9344\n\t"
        "s_waitcnt lgkmcnt(12)\n\t"
        "v_mfma_f32_32x32x16_bf16 v[64:79], v[132:135], v[112:115], v[64:79]\n\t"
        "ds_read_b64_tr_b16 v[112:113], v123 offset:6400\n\t"
        "ds_read_b64_tr_b16 v[114:115], v123 offset:9472\n\t"
        "s_waitcnt lgkmcnt(10)\n\t"
        "v_mfma_f32_32x32x16_bf16 v[80:95], v[132:135], v[116:119], v[80:95]\n\t"
        "v_cmp_gt_u32 vcc, s47, v126\n\t"
        "ds_read_b64_tr_b16 v[116:117], v124 offset:6400\n\t"
        "ds_read_b64_tr_b16 v[118:119], v124 offset:9472\n\t"
        "s_waitcnt vmcnt(14) lgkmcnt(10)\n\t"
        "v_mfma_f32_32x32x16_bf16 v[0:15], v[96:99], v[136:139], v[0:15]\n\t"
        "s_cbranch_vccnz 28f\n\t"
        "29:\n\t"
        "ds_read_b64_tr_b16 v[96:97], v129 offset:0\n\t"
        "ds_read_b64_tr_b16 v[98:99], v129 offset:3072\n\t"
        "s_waitcnt lgkmcnt(10)\n\t"
        "v_mfma_f32_32x32x16_bf16 v[16:31], v[100:103], v[136:139], v[16:31]\n\t"
        "v_add_u32 v128, 1, v128\n\t"
        "ds_read_b64_tr_b16 v[100:101], v130 offset:0\n\t"
        "ds_read_b64_tr_b16 v[102:103], v130 offset:3072\n\t"
        "s_waitcnt lgkmcnt(10)\n\t"
        "v_mfma_f32_32x32x16_bf16 v[32:47], v[104:107], v[136:139], v[32:47]\n\t"
        "ds_read_b64_tr_b16 v[104:105], v129 offset:128\n\t"
        "ds_read_b64_tr_b16 v[106:107], v129 offset:3200\n\t"
        "s_add_u32 s44, s44, 0x1000\n\t"
        "s_and_b32 s44, s44, 0xffff\n\t"
        "buffer_load_dwordx4 v[132:135], v120, s[40:43], s44 offen offset:0 sc1\n\t"
        "s_waitcnt lgkmcnt(10)\n\t"
        "v_mfma_f32_32x32x16_bf16 v[48:63], v[108:111], v[136:139], v[48:63]\n\t"
        "s_add_u32 s46, s46, 12288\n\t"
        "s_add_u32 s47, s47, 1\n\t"
        "ds_read_b64_tr_b16 v[108:109], v130 offset:128\n\t"
        "ds_read_b64_tr_b16 v[110:111], v130 offset:3200\n\t"
        "s_waitcnt lgkmcnt(10)\n\t"
        "v_mfma_f32_32x32x16_bf16 v[64:79], v[136:139], v[112:115], v[64:79]\n\t"
        "s_cmp_eq_u32 s46, 110592\n\t"
        "s_cselect_b32 s46, 0, s46\n\t"
        "ds_read_b64_tr_b16 v[112:113], v129 offset:256\n\t"
        "ds_read_b64_tr_b16 v[114:115], v129 offset:3328\n\t"
        "s_waitcnt lgkmcnt(10)\n\t"
        "v_mfma_f32_32x32x16_bf16 v[80:95], v[136:139], v[116:119], v[80:95]\n\t"
        "ds_write_b32 v127, v128\n\t"
        "ds_read_b64_tr_b16 v[116:117], v130 offset:256\n\t"
        "ds_read_b64_tr_b16 v[118:119], v130 offset:3328\n\t"
        "s_waitcnt vmcnt(14) lgkmcnt(11)\n\t"
        "v_mfma_f32_32x32x16_bf16 v[0:15], v[96:99], v[140:143], v[0:15]\n\t"
        "ds_read_b32 v126, v125\n\t"
        "ds_read_b64_tr_b16 v[96:97], v129 offset:6144\n\t"
        "ds_read_b64_tr_b16 v[98:99], v129 offset:9216\n\t"
        "s_waitcnt lgkmcnt(12)\n\t"
        "v_mfma_f32_32x32x16_bf16 v[16:31], v[100:103], v[140:143], v[16:31]\n\t"
        "v_add_u32 v123, s46, v121\n\t"
        "ds_read_b64_tr_b16 v[100:101], v130 offset:6144\n\t"
        "ds_read_b64_tr_b16 v[102:103], v130 offset:9216\n\t"
        "s_waitcnt lgkmcnt(12)\n\t"
        "v_mfma_f32_32x32x16_bf16 v[32:47], v[104:107], v[140:143], v[32:47]\n\t"
        "ds_read_b64_tr_b16 v[104:105], v129 offset:6272\n\t"
        "ds_read_b64_tr_b16 v[106:107], v129 offset:9344\n\t"
        "buffer_load_dwordx4 v[136:139], v120, s[40:43], s44 offen offset:1024 sc1\n\t"
        "s_waitcnt lgkmcnt(12)\n\t"
        "v_mfma_f32_32x32x16_bf16 v[48:63], v[108:111], v[140:143], v[48:63]\n\t"
        "v_add_u32 v124, s46, v122\n\t"
        "ds_read_b64_tr_b16 v[108:109], v130 offset:6272\n\t"
        "ds_read_b64_tr_b16 v[110:111], v130 offset:9344\n\t"
        "s_waitcnt lgkmcnt(12)\n\t"
        "v_mfma_f32_32x32x16_bf16 v[64:79], v[140:143], v[112:115], v[64:79]\n\t"
        "ds_read_b64_tr_b16 v[112:113], v129 offset:6400\n\t"
        "ds_read_b64_tr_b16 v[114:115], v129 offset:9472\n\t"
        "s_waitcnt lgkmcnt(10)\n\t"
        "v_mfma_f32_32x32x16_bf16 v[80:95], v[140:143], v[116:119], v[80:95]\n\t"
        "v_cmp_gt_u32 vcc, s47, v126\n\t"
        "ds_read_b64_tr_b16 v[116:117], v130 offset:6400\n\t"
        "ds_read_b64_tr_b16 v[118:119], v130 offset:9472\n\t"
        "s_waitcnt vmcnt(14) lgkmcnt(10)\n\t"
        "v_mfma_f32_32x32x16_bf16 v[0:15], v[96:99], v[144:147], v[0:15]\n\t"
        "s_cbranch_vccnz 30f\n\t"
        "31:\n\t"
        "ds_read_b64_tr_b16 v[96:97], v123 offset:0\n\t"
        "ds_read_b64_tr_b16 v[98:99], v123 offset:3072\n\t"
        "s_waitcnt lgkmcnt(10)\n\t"
        "v_mfma_f32_32x32x16_bf16 v[16:31], v[100:103], v[144:147], v[16:31]\n\t"
        "v_add_u32 v128, 1, v128\n\t"
        "ds_read_b64_tr_b16 v[100:101], v124 offset:0\n\t"
        "ds_read_b64_tr_b16 v[102:103], v124 offset:3072\n\t"
        "s_waitcnt lgkmcnt(10)\n\t"
        "v_mfma_f32_32x32x16_bf16 v[32:47], v[104:107], v[144:147], v[32:47]\n\t"
        "ds_read_b64_tr_b16 v[104:105], v123 offset:128\n\t"
        "ds_read_b64_tr_b16 v[106:107], v123 offset:3200\n\t"
        "buffer_load_dwordx4 v[140:143], v120, s[40:43], s44 offen offset:2048 sc1\n\t"
        "s_waitcnt lgkmcnt(10)\n\t"
        "v_mfma_f32_32x32x16_bf16 v[48:63], v[108:111], v[144:147], v[48:63]\n\t"
        "s_add_u32 s46, s46, 12288\n\t"
        "s_add_u32 s47, s47, 1\n\t"
        "ds_read_b64_tr_b16 v[108:109], v124 offset:128\n\t"
        "ds_read_b64_tr_b16 v[110:111], v124 offset:3200\n\t"
        "s_waitcnt lgkmcnt(10)\n\t"
        "v_mfma_f32_32x32x16_bf16 v[64:79], v[144:147], v[112:115], v[64:79]\n\t"
        "s_cmp_eq_u32 s46, 110592\n\t"
        "s_cselect_b32 s46, 0, s46\n\t"
        "ds_read_b64_tr_b16 v[112:113], v123 offset:256\n\t"
        "ds_read_b64_tr_b16 v[114:115], v123 offset:3328\n\t"
        "s_waitcnt lgkmcnt(10)\n\t"
        "v_mfma_f32_32x32x16_bf16 v[80:95], v[144:147], v[116:119], v[80:95]\n\t"
        "ds_write_b32 v127, v128\n\t"
        "ds_read_b64_tr_b16 v[116:117], v124 offset:256\n\t"
        "ds_read_b64_tr_b16 v[118:119], v124 offset:3328\n\t"
        "s_sub_u32 s45, s45, 1\n\t"
        "s_cmp_lg_u32 s45, 0\n\t"
        "s_cbranch_scc1 1b\n\t"
        "s_waitcnt vmcnt(14) lgkmcnt(11)\n\t"
        "v_mfma_f32_32x32x16_bf16 v[0:15], v[96:99], v[148:151], v[0:15]\n\t"
        "ds_read_b32 v126, v125\n\t"
        "ds_read_b64_tr_b16 v[96:97], v123 offset:6144\n\t"
        "ds_read_b64_tr_b16 v[98:99], v123 offset:9216\n\t"
        "s_waitcnt lgkmcnt(12)\n\t"
        "v_mfma_f32_32x32x16_bf16 v[16:31], v[100:103], v[148:151], v[16:31]\n\t"
        "v_add_u32 v129, s46, v121\n\t"
        "ds_read_b64_tr_b16 v[100:101], v124 offset:6144\n\t"
        "ds_read_b64_tr_b16 v[102:103], v124 offset:9216\n\t"
        "s_waitcnt lgkmcnt(12)\n\t"
        "v_mfma_f32_32x32x16_bf16 v[32:47], v[104:107], v[148:151], v[32:47]\n\t"
        "ds_read_b64_tr_b16 v[104:105], v123 offset:6272\n\t"
        "ds_read_b64_tr_b16 v[106:107], v123 offset:9344\n\t"
        "s_waitcnt lgkmcnt(12)\n\t"
        "v_mfma_f32_32x32x16_bf16 v[48:63], v[108:111], v[148:151], v[48:63]\n\t"
        "v_add_u32 v130, s46, v122\n\t"
        "ds_read_b64_tr_b16 v[108:109], v124 offset:6272\n\t"
        "ds_read_b64_tr_b16 v[110:111], v124 offset:9344\n\t"
        "s_waitcnt lgkmcnt(12)\n\t"
        "v_mfma_f32_32x32x16_bf16 v[64:79], v[148:151], v[112:115], v[64:79]\n\t"
        "ds_read_b64_tr_b16 v[112:113], v123 offset:6400\n\t"
        "ds_read_b64_tr_b16 v[114:115], v123 offset:9472\n\t"
        "s_waitcnt lgkmcnt(10)\n\t"
        "v_mfma_f32_32x32x16_bf16 v[80:95], v[148:151], v[116:119], v[80:95]\n\t"
        "v_cmp_gt_u32 vcc, s47, v126\n\t"
        "ds_read_b64_tr_b16 v[116:117], v124 offset:6400\n\t"
        "ds_read_b64_tr_b16 v[118:119], v124 offset:9472\n\t"
        "s_waitcnt vmcnt(13) lgkmcnt(10)\n\t"
        "v_mfma_f32_32x32x16_bf16 v[0:15], v[96:99], v[152:155], v[0:15]\n\t"
        "s_cbranch_vccnz 64f\n\t"
        "65:\n\t"
        "ds_read_b64_tr_b16 v[96:97], v129 offset:0\n\t"
        "ds_read_b64_tr_b16 v[98:99], v129 offset:3072\n\t"
        "s_waitcnt lgkmcnt(10)\n\t"
        "v_mfma_f32_32x32x16_bf16 v[16:31], v[100:103], v[152:155], v[16:31]\n\t"
        "v_add_u32 v128, 1, v128\n\t"
        "ds_read_b64_tr_b16 v[100:101], v130 offset:0\n\t"
        "ds_read_b64_tr_b16 v[102:103], v130 offset:3072\n\t"
        "s_waitcnt lgkmcnt(10)\n\t"
        "v_mfma_f32_32x32x16_bf16 v[32:47], v[104:107], v[152:155], v[32:47]\n\t"
        "ds_read_b64_tr_b16 v[104:105], v129 offset:128\n\t"
        "ds_read_b64_tr_b16 v[106:107], v129 offset:3200\n\t"
        "s_waitcnt lgkmcnt(10)\n\t"
        "v_mfma_f32_32x32x16_bf16 v[48:63], v[108:111], v[152:155], v[48:63]\n\t"
        "s_add_u32 s46, s46, 12288\n\t"
        "s_add_u32 s47, s47, 1\n\t"
        "ds_read_b64_tr_b16 v[108:109], v130 offset:128\n\t"
        "ds_read_b64_tr_b16 v[110:111], v130 offset:3200\n\t"
        "s_waitcnt lgkmcnt(10)\n\t"
        "v_mfma_f32_32x32x16_bf16 v[64:79], v[152:155], v[112:115], v[64:79]\n\t"
        "s_cmp_eq_u32 s46, 110592\n\t"
        "s_cselect_b32 s46, 0, s46\n\t"
        "ds_read_b64_tr_b16 v[112:113], v129 offset:256\n\t"
        "ds_read_b64_tr_b16 v[114:115], v129 offset:3328\n\t"
        "s_waitcnt lgkmcnt(10)\n\t"
        "v_mfma_f32_32x32x16_bf16 v[80:95], v[152:155], v[116:119], v[80:95]\n\t"
        "ds_write_b32 v127, v128\n\t"
        "ds_read_b64_tr_b16 v[116:117], v130 offset:256\n\t"
        "ds_read_b64_tr_b16 v[118:119], v130 offset:3328\n\t"
        "s_waitcnt vmcnt(12) lgkmcnt(11)\n\t"
        "v_mfma_f32_32x32x16_bf16 v[0:15], v[96:99], v[156:159], v[0:15]\n\t"
        "ds_read_b32 v126, v125\n\t"
        "ds_read_b64_tr_b16 v[96:97], v129 offset:6144\n\t"
        "ds_read_b64_tr_b16 v[98:99], v129 offset:9216\n\t"
        "s_waitcnt lgkmcnt(12)\n\t"
        "v_mfma_f32_32x32x16_bf16 v[16:31], v[100:103], v[156:159], v[16:31]\n\t"
        "v_add_u32 v123, s46, v121\n\t"
        "ds_read_b64_tr_b16 v[100:101], v130 offset:6144\n\t"
        "ds_read_b64_tr_b16 v[102:103], v130 offset:9216\n\t"
        "s_waitcnt lgkmcnt(12)\n\t"
        "v_mfma_f32_32x32x16_bf16 v[32:47], v[104:107], v[156:159], v[32:47]\n\t"
        "ds_read_b64_tr_b16 v[104:105], v129 offset:6272\n\t"
        "ds_read_b64_tr_b16 v[106:107], v129 offset:9344\n\t"
        "s_waitcnt lgkmcnt(12)\n\t"
        "v_mfma_f32_32x32x16_bf16 v[48:63], v[108:111], v[156:159], v[48:63]\n\t"
        "v_add_u32 v124, s46, v122\n\t"
        "ds_read_b64_tr_b16 v[108:109], v130 offset:6272\n\t"
        "ds_read_b64_tr_b16 v[110:111], v130 offset:9344\n\t"
        "s_waitcnt lgkmcnt(12)\n\t"
        "v_mfma_f32_32x32x16_bf16 v[64:79], v[156:159], v[112:115], v[64:79]\n\t"
        "ds_read_b64_tr_b16 v[112:113], v129 offset:6400\n\t"
        "ds_read_b64_tr_b16 v[114:115], v129 offset:9472\n\t"
        "s_waitcnt lgkmcnt(10)\n\t"
        "v_mfma_f32_32x32x16_bf16 v[80:95], v[156:159], v[116:119], v[80:95]\n\t"
        "v_cmp_gt_u32 vcc, s47, v126\n\t"
        "ds_read_b64_tr_b16 v[116:117], v130 offset:6400\n\t"
        "ds_read_b64_tr_b16 v[118:119], v130 offset:9472\n\t"
        "s_waitcnt vmcnt(11) lgkmcnt(10)\n\t"
        "v_mfma_f32_32x32x16_bf16 v[0:15], v[96:99], v[160:163], v[0:15]\n\t"
        "s_cbranch_vccnz 66f\n\t"
        "67:\n\t"
        "ds_read_b64_tr_b16 v[96:97], v123 offset:0\n\t"
        "ds_read_b64_tr_b16 v[98:99], v123 offset:3072\n\t"
        "s_waitcnt lgkmcnt(10)\n\t"
        "v_mfma_f32_32x32x16_bf16 v[16:31], v[100:103], v[160:163], v[16:31]\n\t"
        "v_add_u32 v128, 1, v128\n\t"
        "ds_read_b64_tr_b16 v[100:101], v124 offset:0\n\t"
        "ds_read_b64_tr_b16 v[102:103], v124 offset:3072\n\t"
        "s_waitcnt lgkmcnt(10)\n\t"
        "v_mfma_f32_32x32x16_bf16 v[32:47], v[104:107], v[160:163], v[32:47]\n\t"
        "ds_read_b64_tr_b16 v[104:105], v123 offset:128\n\t"
        "ds_read_b64_tr_b16 v[106:107], v123 offset:3200\n\t"
        "s_waitcnt lgkmcnt(10)\n\t"
        "v_mfma_f32_32x32x16_bf16 v[48:63], v[108:111], v[160:163], v[48:63]\n\t"
        "s_add_u32 s46, s46, 12288\n\t"
        "s_add_u32 s47, s47, 1\n\t"
        "ds_read_b64_tr_b16 v[108:109], v124 offset:128\n\t"
        "ds_read_b64_tr_b16 v[110:111], v124 offset:3200\n\t"
        "s_waitcnt lgkmcnt(10)\n\t"
        "v_mfma_f32_32x32x16_bf16 v[64:79], v[160:163], v[112:115], v[64:79]\n\t"
        "s_cmp_eq_u32 s46, 110592\n\t"
        "s_cselect_b32 s46, 0, s46\n\t"
        "ds_read_b64_tr_b16 v[112:113], v123 offset:256\n\t"
        "ds_read_b64_tr_b16 v[114:115], v123 offset:3328\n\t"
        "s_waitcnt lgkmcnt(10)\n\t"
        "v_mfma_f32_32x32x16_bf16 v[80:95], v[160:163], v[116:119], v[80:95]\n\t"
        "ds_write_b32 v127, v128\n\t"
        "ds_read_b64_tr_b16 v[116:117], v124 offset:256\n\t"
        "ds_read_b64_tr_b16 v[118:119], v124 offset:3328\n\t"
        "s_waitcnt vmcnt(10) lgkmcnt(11)\n\t"
        "v_mfma_f32_32x32x16_bf16 v[0:15], v[96:99], v[164:167], v[0:15]\n\t"
        "ds_read_b32 v126, v125\n\t"
        "ds_read_b64_tr_b16 v[96:97], v123 offset:6144\n\t"
        "ds_read_b64_tr_b16 v[98:99], v123 offset:9216\n\t"
        "s_waitcnt lgkmcnt(12)\n\t"
        "v_mfma_f32_32x32x16_bf16 v[16:31], v[100:103], v[164:167], v[16:31]\n\t"
        "v_add_u32 v129, s46, v121\n\t"
        "ds_read_b64_tr_b16 v[100:101], v124 offset:6144\n\t"
        "ds_read_b64_tr_b16 v[102:103], v124 offset:9216\n\t"
        "s_waitcnt lgkmcnt(12)\n\t"
        "v_mfma_f32_32x32x16_bf16 v[32:47], v[104:107], v[164:167], v[32:47]\n\t"
        "ds_read_b64_tr_b16 v[104:105], v123 offset:6272\n\t"
        "ds_read_b64_tr_b16 v[106:107], v123 offset:9344\n\t"
        "s_waitcnt lgkmcnt(12)\n\t"
        "v_mfma_f32_32x32x16_bf16 v[48:63], v[108:111], v[164:167], v[48:63]\n\t"
        "v_add_u32 v130, s46, v122\n\t"
        "ds_read_b64_tr_b16 v[108:109], v124 offset:6272\n\t"
        "ds_read_b64_tr_b16 v[110:111], v124 offset:9344\n\t"
        "s_waitcnt lgkmcnt(12)\n\t"
        "v_mfma_f32_32x32x16_bf16 v[64:79], v[164:167], v[112:115], v[64:79]\n\t"
        "ds_read_b64_tr_b16 v[112:113], v123 offset:6400\n\t"
        "ds_read_b64_tr_b16 v[114:115], v123 offset:9472\n\t"
        "s_waitcnt lgkmcnt(10)\n\t"
        "v_mfma_f32_32x32x16_bf16 v[80:95], v[164:167], v[116:119], v[80:95]\n\t"
        "v_cmp_gt_u32 vcc, s47, v126\n\t"
        "ds_read_b64_tr_b16 v[116:117], v124 offset:6400\n\t"
        "ds_read_b64_tr_b16 v[118:119], v124 offset:9472\n\t"
        "s_waitcnt vmcnt(9) lgkmcnt(10)\n\t"
        "v_mfma_f32_32x32x16_bf16 v[0:15], v[96:99], v[168:171], v[0:15]\n\t"
        "s_cbranch_vccnz 68f\n\t"
        "69:\n\t"
        "ds_read_b64_tr_b16 v[96:97], v129 offset:0\n\t"
        "ds_read_b64_tr_b16 v[98:99], v129 offset:3072\n\t"
        "s_waitcnt lgkmcnt(10)\n\t"
        "v_mfma_f32_32x32x16_bf16 v[16:31], v[100:103], v[168:171], v[16:31]\n\t"
        "v_add_u32 v128, 1, v128\n\t"
        "ds_read_b64_tr_b16 v[100:101], v130 offset:0\n\t"
        "ds_read_b64_tr_b16 v[102:103], v130 offset:3072\n\t"
        "s_waitcnt lgkmcnt(10)\n\t"
        "v_mfma_f32_32x32x16_bf16 v[32:47], v[104:107], v[168:171], v[32:47]\n\t"
        "ds_read_b64_tr_b16 v[104:105], v129 offset:128\n\t"
        "ds_read_b64_tr_b16 v[106:107], v129 offset:3200\n\t"
        "s_waitcnt lgkmcnt(10)\n\t"
        "v_mfma_f32_32x32x16_bf16 v[48:63], v[108:111], v[168:171], v[48:63]\n\t"
        "s_add_u32 s46, s46, 12288\n\t"
        "s_add_u32 s47, s47, 1\n\t"
        "ds_read_b64_tr_b16 v[108:109], v130 offset:128\n\t"
        "ds_read_b64_tr_b16 v[110:111], v130 offset:3200\n\t"
        "s_waitcnt lgkmcnt(10)\n\t"
        "v_mfma_f32_32x32x16_bf16 v[64:79], v[168:171], v[112:115], v[64:79]\n\t"
        "s_cmp_eq_u32 s46, 110592\n\t"
        "s_cselect_b32 s46, 0, s46\n\t"
        "ds_read_b64_tr_b16 v[112:113], v129 offset:256\n\t"
        "ds_read_b64_tr_b16 v[114:115], v129 offset:3328\n\t"
        "s_waitcnt lgkmcnt(10)\n\t"
        "v_mfma_f32_32x32x16_bf16 v[80:95], v[168:171], v[116:119], v[80:95]\n\t"
        "ds_write_b32 v127, v128\n\t"
        "ds_read_b64_tr_b16 v[116:117], v130 offset:256\n\t"
        "ds_read_b64_tr_b16 v[118:119], v130 offset:3328\n\t"
        "s_waitcnt vmcnt(8) lgkmcnt(11)\n\t"
        "v_mfma_f32_32x32x16_bf16 v[0:15], v[96:99], v[172:175], v[0:15]\n\t"
        "ds_read_b32 v126, v125\n\t"
        "ds_read_b64_tr_b16 v[96:97], v129 offset:6144\n\t"
        "ds_read_b64_tr_b16 v[98:99], v129 offset:9216\n\t"
        "s_waitcnt lgkmcnt(12)\n\t"
        "v_mfma_f32_32x32x16_bf16 v[16:31], v[100:103], v[172:175], v[16:31]\n\t"
        "v_add_u32 v123, s46, v121\n\t"
        "ds_read_b64_tr_b16 v[100:101], v130 offset:6144\n\t"
        "ds_read_b64_tr_b16 v[102:103], v130 offset:9216\n\t"
        "s_waitcnt lgkmcnt(12)\n\t"
        "v_mfma_f32_32x32x16_bf16 v[32:47], v[104:107], v[172:175], v[32:47]\n\t"
        "ds_read_b64_tr_b16 v[104:105], v129 offset:6272\n\t"
        "ds_read_b64_tr_b16 v[106:107], v129 offset:9344\n\t"
        "s_waitcnt lgkmcnt(12)\n\t"
        "v_mfma_f32_32x32x16_bf16 v[48:63], v[108:111], v[172:175], v[48:63]\n\t"
        "v_add_u32 v124, s46, v122\n\t"
        "ds_read_b64_tr_b16 v[108:109], v130 offset:6272\n\t"
        "ds_read_b64_tr_b16 v[110:111], v130 offset:9344\n\t"
        "s_waitcnt lgkmcnt(12)\n\t"
        "v_mfma_f32_32x32x16_bf16 v[64:79], v[172:175], v[112:115], v[64:79]\n\t"
        "ds_read_b64_tr_b16 v[112:113], v129 offset:6400\n\t"
        "ds_read_b64_tr_b16 v[114:115], v129 offset:9472\n\t"
        "s_waitcnt lgkmcnt(10)\n\t"
        "v_mfma_f32_32x32x16_bf16 v[80:95], v[172:175], v[116:119], v[80:95]\n\t"
        "v_cmp_gt_u32 vcc, s47, v126\n\t"
        "ds_read_b64_tr_b16 v[116:117], v130 offset:6400\n\t"
        "ds_read_b64_tr_b16 v[118:119], v130 offset:9472\n\t"
        "s_waitcnt vmcnt(7) lgkmcnt(10)\n\t"
        "v_mfma_f32_32x32x16_bf16 v[0:15], v[96:99], v[176:179], v[0:15]\n\t"
        "s_cbranch_vccnz 70f\n\t"
        "71:\n\t"
        "ds_read_b64_tr_b16 v[96:97], v123 offset:0\n\t"
        "ds_read_b64_tr_b16 v[98:99], v123 offset:3072\n\t"
        "s_waitcnt lgkmcnt(10)\n\t"
        "v_mfma_f32_32x32x16_bf16 v[16:31], v[100:103], v[176:179], v[16:31]\n\t"
        "v_add_u32 v128, 1, v128\n\t"
        "ds_read_b64_tr_b16 v[100:101], v124 offset:0\n\t"
        "ds_read_b64_tr_b16 v[102:103], v124 offset:3072\n\t"
        "s_waitcnt lgkmcnt(10)\n\t"
        "v_mfma_f32_32x32x16_bf16 v[32:47], v[104:107], v[176:179], v[32:47]\n\t"
        "ds_read_b64_tr_b16 v[104:105], v123 offset:128\n\t"
        "ds_read_b64_tr_b16 v[106:107], v123 offset:3200\n\t"
        "s_waitcnt lgkmcnt(10)\n\t"
        "v_mfma_f32_32x32x16_bf16 v[48:63], v[108:111], v[176:179], v[48:63]\n\t"
        "s_add_u32 s46, s46, 12288\n\t"
        "s_add_u32 s47, s47, 1\n\t"
        "ds_read_b64_tr_b16 v[108:109], v124 offset:128\n\t"
        "ds_read_b64_tr_b16 v[110:111], v124 offset:3200\n\t"
        "s_waitcnt lgkmcnt(10)\n\t"
        "v_mfma_f32_32x32x16_bf16 v[64:79], v[176:179], v[112:115], v[64:79]\n\t"
        "s_cmp_eq_u32 s46, 110592\n\t"
        "s_cselect_b32 s46, 0, s46\n\t"
        "ds_read_b64_tr_b16 v[112:113], v123 offset:256\n\t"
        "ds_read_b64_tr_b16 v[114:115], v123 offset:3328\n\t"
        "s_waitcnt lgkmcnt(10)\n\t"
        "v_mfma_f32_32x32x16_bf16 v[80:95], v[176:179], v[116:119], v[80:95]\n\t"
        "ds_write_b32 v127, v128\n\t"
        "ds_read_b64_tr_b16 v[116:117], v124 offset:256\n\t"
        "ds_read_b64_tr_b16 v[118:119], v124 offset:3328\n\t"
        "s_waitcnt vmcnt(6) lgkmcnt(11)\n\t"
        "v_mfma_f32_32x32x16_bf16 v[0:15], v[96:99], v[180:183], v[0:15]\n\t"
        "ds_read_b32 v126, v125\n\t"
        "ds_read_b64_tr_b16 v[96:97], v123 offset:6144\n\t"
        "ds_read_b64_tr_b16 v[98:99], v123 offset:9216\n\t"
        "s_waitcnt lgkmcnt(12)\n\t"
        "v_mfma_f32_32x32x16_bf16 v[16:31], v[100:103], v[180:183], v[16:31]\n\t"
        "v_add_u32 v129, s46, v121\n\t"
        "ds_read_b64_tr_b16 v[100:101], v124 offset:6144\n\t"
        "ds_read_b64_tr_b16 v[102:103], v124 offset:9216\n\t"
        "s_waitcnt lgkmcnt(12)\n\t"
        "v_mfma_f32_32x32x16_bf16 v[32:47], v[104:107], v[180:183], v[32:47]\n\t"
        "ds_read_b64_tr_b16 v[104:105], v123 offset:6272\n\t"
        "ds_read_b64_tr_b16 v[106:107], v123 offset:9344\n\t"
        "s_waitcnt lgkmcnt(12)\n\t"
        "v_mfma_f32_32x32x16_bf16 v[48:63], v[108:111], v[180:183], v[48:63]\n\t"
        "v_add_u32 v130, s46, v122\n\t"
        "ds_read_b64_tr_b16 v[108:109], v124 offset:6272\n\t"
        "ds_read_b64_tr_b16 v[110:111], v124 offset:9344\n\t"
        "s_waitcnt lgkmcnt(12)\n\t"
        "v_mfma_f32_32x32x16_bf16 v[64:79], v[180:183], v[112:115], v[64:79]\n\t"
        "ds_read_b64_tr_b16 v[112:113], v123 offset:6400\n\t"
        "ds_read_b64_tr_b16 v[114:115], v123 offset:9472\n\t"
        "s_waitcnt lgkmcnt(10)\n\t"
        "v_mfma_f32_32x32x16_bf16 v[80:95], v[180:183], v[116:119], v[80:95]\n\t"
        "v_cmp_gt_u32 vcc, s47, v126\n\t"
        "ds_read_b64_tr_b16 v[116:117], v124 offset:6400\n\t"
        "ds_read_b64_tr_b16 v[118:119], v124 offset:9472\n\t"
        "s_waitcnt vmcnt(5) lgkmcnt(10)\n\t"
        "v_mfma_f32_32x32x16_bf16 v[0:15], v[96:99], v[184:187], v[0:15]\n\t"
        "s_cbranch_vccnz 72f\n\t"
        "73:\n\t"
        "ds_read_b64_tr_b16 v[96:97], v129 offset:0\n\t"
        "ds_read_b64_tr_b16 v[98:99], v129 offset:3072\n\t"
        "s_waitcnt lgkmcnt(10)\n\t"
        "v_mfma_f32_32x32x16_bf16 v[16:31], v[100:103], v[184:187], v[16:31]\n\t"
        "v_add_u32 v128, 1, v128\n\t"
        "ds_read_b64_tr_b16 v[100:101], v130 offset:0\n\t"
        "ds_read_b64_tr_b16 v[102:103], v130 offset:3072\n\t"
        "s_waitcnt lgkmcnt(10)\n\t"
        "v_mfma_f32_32x32x16_bf16 v[32:47], v[104:107], v[184:187], v[32:47]\n\t"
        "ds_read_b64_tr_b16 v[104:105], v129 offset:128\n\t"
        "ds_read_b64_tr_b16 v[106:107], v129 offset:3200\n\t"
        "s_waitcnt lgkmcnt(10)\n\t"
        "v_mfma_f32_32x32x16_bf16 v[48:63], v[108:111], v[184:187], v[48:63]\n\t"
        "s_add_u32 s46, s46, 12288\n\t"
        "s_add_u32 s47, s47, 1\n\t"
        "ds_read_b64_tr_b16 v[108:109], v130 offset:128\n\t"
        "ds_read_b64_tr_b16 v[110:111], v130 offset:3200\n\t"
        "s_waitcnt lgkmcnt(10)\n\t"
        "v_mfma_f32_32x32x16_bf16 v[64:79], v[184:187], v[112:115], v[64:79]\n\t"
        "s_cmp_eq_u32 s46, 110592\n\t"
        "s_cselect_b32 s46, 0, s46\n\t"
        "ds_read_b64_tr_b16 v[112:113], v129 offset:256\n\t"
        "ds_read_b64_tr_b16 v[114:115], v129 offset:3328\n\t"
        "s_waitcnt lgkmcnt(10)\n\t"
        "v_mfma_f32_32x32x16_bf16 v[80:95], v[184:187], v[116:119], v[80:95]\n\t"
        "ds_write_b32 v127, v128\n\t"
        "ds_read_b64_tr_b16 v[116:117], v130 offset:256\n\t"
        "ds_read_b64_tr_b16 v[118:119], v130 offset:3328\n\t"
        "s_waitcnt vmcnt(4) lgkmcnt(11)\n\t"
        "v_mfma_f32_32x32x16_bf16 v[0:15], v[96:99], v[188:191], v[0:15]\n\t"
        "ds_read_b64_tr_b16 v[96:97], v129 offset:6144\n\t"
        "ds_read_b64_tr_b16 v[98:99], v129 offset:9216\n\t"
        "s_waitcnt lgkmcnt(11)\n\t"
        "v_mfma_f32_32x32x16_bf16 v[16:31], v[100:103], v[188:191], v[16:31]\n\t"
        "ds_read_b64_tr_b16 v[100:101], v130 offset:6144\n\t"
        "ds_read_b64_tr_b16 v[102:103], v130 offset:9216\n\t"
        "s_waitcnt lgkmcnt(11)\n\t"
        "v_mfma_f32_32x32x16_bf16 v[32:47], v[104:107], v[188:191], v[32:47]\n\t"
        "ds_read_b64_tr_b16 v[104:105], v129 offset:6272\n\t"
        "ds_read_b64_tr_b16 v[106:107], v129 offset:9344\n\t"
        "s_waitcnt lgkmcnt(11)\n\t"
        "v_mfma_f32_32x32x16_bf16 v[48:63], v[108:111], v[188:191], v[48:63]\n\t"
        "ds_read_b64_tr_b16 v[108:109], v130 offset:6272\n\t"
        "ds_read_b64_tr_b16 v[110:111], v130 offset:9344\n\t"
        "s_waitcnt lgkmcnt(11)\n\t"
        "v_mfma_f32_32x32x16_bf16 v[64:79], v[188:191], v[112:115], v[64:79]\n\t"
        "ds_read_b64_tr_b16 v[112:113], v129 offset:6400\n\t"
        "ds_read_b64_tr_b16 v[114:115], v129 offset:9472\n\t"
        "s_waitcnt lgkmcnt(10)\n\t"
        "v_mfma_f32_32x32x16_bf16 v[80:95], v[188:191], v[116:119], v[80:95]\n\t"
        "ds_read_b64_tr_b16 v[116:117], v130 offset:6400\n\t"
        "ds_read_b64_tr_b16 v[118:119], v130 offset:9472\n\t"
        "s_waitcnt vmcnt(3) lgkmcnt(10)\n\t"
        "v_mfma_f32_32x32x16_bf16 v[0:15], v[96:99], v[192:195], v[0:15]\n\t"
        "s_waitcnt lgkmcnt(8)\n\t"
        "v_mfma_f32_32x32x16_bf16 v[16:31], v[100:103], v[192:195], v[16:31]\n\t"
        "s_waitcnt lgkmcnt(6)\n\t"
        "v_mfma_f32_32x32x16_bf16 v[32:47], v[104:107], v[192:195], v[32:47]\n\t"
        "s_waitcnt lgkmcnt(4)\n\t"
        "v_mfma_f32_32x32x16_bf16 v[48:63], v[108:111], v[192:195], v[48:63]\n\t"
        "s_waitcnt lgkmcnt(2)\n\t"
        "v_mfma_f32_32x32x16_bf16 v[64:79], v[192:195], v[112:115], v[64:79]\n\t"
        "s_waitcnt lgkmcnt(0)\n\t"
        "v_mfma_f32_32x32x16_bf16 v[80:95], v[192:195], v[116:119], v[80:95]\n\t"
        "s_waitcnt vmcnt(0)\n\t"
        "s_nop 10\n\t"
        "s_branch 9f\n\t"
        "16:\n\t"
        "s_mov_b32 s48, 0x10000\n\t"
        "8:\n\t"
        "ds_read_b32 v126, v125\n\t"
        "s_sub_u32 s48, s48, 1\n\t"
        "s_waitcnt lgkmcnt(0)\n\t"
        "s_cbranch_scc1 17b\n\t"
        "v_cmp_gt_u32 vcc, s47, v126\n\t"
        "s_cbranch_vccnz 8b\n\t"
        "s_branch 17b\n\t"
        "18:\n\t"
        "s_mov_b32 s48, 0x10000\n\t"
        "8:\n\t"
        "ds_read_b32 v126, v125\n\t"
        "s_sub_u32 s48, s48, 1\n\t"
        "s_waitcnt lgkmcnt(0)\n\t"
        "s_cbranch_scc1 19b\n\t"
        "v_cmp_gt_u32 vcc, s47, v126\n\t"
        "s_cbranch_vccnz 8b\n\t"
        "s_branch 19b\n\t"
        "20:\n\t"
        "s_mov_b32 s48, 0x10000\n\t"
        "8:\n\t"
        "ds_read_b32 v126, v125\n\t"
        "s_sub_u32 s48, s48, 1\n\t"
        "s_waitcnt lgkmcnt(0)\n\t"
        "s_cbranch_scc1 21b\n\t"
        "v_cmp_gt_u32 vcc, s47, v126\n\t"
        "s_cbranch_vccnz 8b\n\t"
        "s_branch 21b\n\t"
        "22:\n\t"
        "s_mov_b32 s48, 0x10000\n\t"
        "8:\n\t"
        "ds_read_b32 v126, v125\n\t"
        "s_sub_u32 s48, s48, 1\n\t"
        "s_waitcnt lgkmcnt(0)\n\t"
        "s_cbranch_scc1 23b\n\t"
        "v_cmp_gt_u32 vcc, s47, v126\n\t"
        "s_cbranch_vccnz 8b\n\t"
        "s_branch 23b\n\t"
        "24:\n\t"
        "s_mov_b32 s48, 0x10000\n\t"
        "8:\n\t"
        "ds_read_b32 v126, v125\n\t"
        "s_sub_u32 s48, s48, 1\n\t"
        "s_waitcnt lgkmcnt(0)\n\t"
        "s_cbranch_scc1 25b\n\t"
        "v_cmp_gt_u32 vcc, s47, v126\n\t"
        "s_cbranch_vccnz 8b\n\t"
        "s_branch 25b\n\t"
        "26:\n\t"
        "s_mov_b32 s48, 0x10000\n\t"
        "8:\n\t"
        "ds_read_b32 v126, v125\n\t"
        "s_sub_u32 s48, s48, 1\n\t"
        "s_waitcnt lgkmcnt(0)\n\t"
        "s_cbranch_scc1 27b\n\t"
        "v_cmp_gt_u32 vcc, s47, v126\n\t"
        "s_cbranch_vccnz 8b\n\t"
        "s_branch 27b\n\t"
        "28:\n\t"
        "s_mov_b32 s48, 0x10000\n\t"
        "8:\n\t"
        "ds_read_b32 v126, v125\n\t"
        "s_sub_u32 s48, s48, 1\n\t"
        "s_waitcnt lgkmcnt(0)\n\t"
        "s_cbranch_scc1 29b\n\t"
        "v_cmp_gt_u32 vcc, s47, v126\n\t"
        "s_cbranch_vccnz 8b\n\t"
        "s_branch 29b\n\t"
        "30:\n\t"
        "s_mov_b32 s48, 0x10000\n\t"
        "8:\n\t"
        "ds_read_b32 v126, v125\n\t"
        "s_sub_u32 s48, s48, 1\n\t"
        "s_waitcnt lgkmcnt(0)\n\t"
        "s_cbranch_scc1 31b\n\t"
        "v_cmp_gt_u32 vcc, s47, v126\n\t"
        "s_cbranch_vccnz 8b\n\t"
        "s_branch 31b\n\t"
        "64:\n\t"
        "s_mov_b32 s48, 0x10000\n\t"
        "8:\n\t"
        "ds_read_b32 v126, v125\n\t"
        "s_sub_u32 s48, s48, 1\n\t"
        "s_waitcnt lgkmcnt(0)\n\t"
        "s_cbranch_scc1 65b\n\t"
        "v_cmp_gt_u32 vcc, s47, v126\n\t"
        "s_cbranch_vccnz 8b\n\t"
        "s_branch 65b\n\t"
        "66:\n\t"
        "s_mov_b32 s48, 0x10000\n\t"
        "8:\n\t"
        "ds_read_b32 v126, v125\n\t"
        "s_sub_u32 s48, s48, 1\n\t"
        "s_waitcnt lgkmcnt(0)\n\t"
        "s_cbranch_scc1 67b\n\t"
        "v_cmp_gt_u32 vcc, s47, v126\n\t"
        "s_cbranch_vccnz 8b\n\t"
        "s_branch 67b\n\t"
        "68:\n\t"
        "s_mov_b32 s48, 0x10000\n\t"
        "8:\n\t"
        "ds_read_b32 v126, v125\n\t"
        "s_sub_u32 s48, s48, 1\n\t"
        "s_waitcnt lgkmcnt(0)\n\t"
        "s_cbranch_scc1 69b\n\t"
        "v_cmp_gt_u32 vcc, s47, v126\n\t"
        "s_cbranch_vccnz 8b\n\t"
        "s_branch 69b\n\t"
        "70:\n\t"
        "s_mov_b32 s48, 0x10000\n\t"
        "8:\n\t"
        "ds_read_b32 v126, v125\n\t"
        "s_sub_u32 s48, s48, 1\n\t"
        "s_waitcnt lgkmcnt(0)\n\t"
        "s_cbranch_scc1 71b\n\t"
        "v_cmp_gt_u32 vcc, s47, v126\n\t"
        "s_cbranch_vccnz 8b\n\t"
        "s_branch 71b\n\t"
        "72:\n\t"
        "s_mov_b32 s48, 0x10000\n\t"
        "8:\n\t"
        "ds_read_b32 v126, v125\n\t"
        "s_sub_u32 s48, s48, 1\n\t"
        "s_waitcnt lgkmcnt(0)\n\t"
        "s_cbranch_scc1 73b\n\t"
        "v_cmp_gt_u32 vcc, s47, v126\n\t"
        "s_cbranch_vccnz 8b\n\t"
        "s_branch 73b\n\t"
        "9:\n\t"
        : "+{v[0:15]}"(acc[0]), "+{v[16:31]}"(acc[1]), "+{v[32:47]}"(acc[2]), "+{v[48:63]}"(acc[3]), "+{v[64:79]}"(acc[4]), "+{v[80:95]}"(acc[5]),
          "+{v[96:111]}"(stA), "+{v[112:127]}"(stB), "+{v[128:131]}"(stC), "+{v[148:163]}"(xr1), "+{v[164:179]}"(xr2), "+{v[180:195]}"(xr3),
          "+{s44}"(soff), "+{s46}"(slot), "+{s47}"(need)
        : "{s[40:43]}"(srd) : "v132", "v133", "v134", "v135", "v136", "v137", "v138", "v139", "v140", "v141", "v142", "v143", "v144", "v145", "v146", "v147", "s45", "s48", "vcc", "scc", "memory");
      STAMPV(4 + 16 * L);
      PHASE_VARS;
      if (L == 1) {
        f32x4 av[4], bv[4], cs[4][4];
        float cv[2];
#pragma unroll
        for (int g = 0; g < 4; ++g) {
          av[g] = *reinterpret_cast<const f32x4*>(smem + L_AB + wid * 256 + (8 * g + 4 * hi) * 4);
          bv[g] = *reinterpret_cast<const f32x4*>(smem + L_AB + wid * 256 + 128 + (8 * g + 4 * hi) * 4);
        }
#pragma unroll
        for (int j = 5; j >= 0; --j) {
          const int cb = (j >> 1) * 64 + ((j & 1) ^ hh) * 32;
          if (j >= 4) cv[j - 4] = *reinterpret_cast<const float*>(smem + L_CS + (cb + r32) * 4);
          else {
#pragma unroll
            for (int g = 0; g < 4; ++g) cs[j][g] = *reinterpret_cast<const f32x4*>(smem + L_CS + (cb + 8 * g + 4 * hi) * 4);
          }
        }
        SBAR();
#pragma unroll
        for (int j = 5; j >= 0; --j)
#pragma unroll
          for (int g = 0; g < 4; ++g)
#pragma unroll
            for (int e = 0; e < 4; ++e)
              acc[j][4 * g + e] = j < 4 ? al * acc[j][4 * g + e] + be * cs[j & 3][g][e] : av[g][e] * acc[j][4 * g + e] + bv[g][e] * cv[j & 1];
        SBAR();
        lds_wait_ge(lbase + L_W + 4 + 4 * (lane & 1), 1);
        const int idx = (int)__builtin_amdgcn_readfirstlane(words[3]);
        const bool mine = (idx >> 5) == tile;
        if (mine) {
#pragma unroll
          for (int j = 0; j < 6; ++j) {
            const int cb = (j >> 1) * 64 + ((j & 1) ^ hh) * 32;
            if (j >= 4) cv[j - 4] = *reinterpret_cast<const float*>(smem + L_PATCH + (cb + r32) * 4);
            else {
#pragma unroll
              for (int g = 0; g < 4; ++g) cs[j][g] = *reinterpret_cast<const f32x4*>(smem + L_PATCH + (cb + 8 * g + 4 * hi) * 4);
            }
          }
          SBAR();
          const int rel = (idx & 31) - 4 * hi;
#pragma unroll
          for (int j = 0; j < 6; ++j)
#pragma unroll
            for (int e = 0; e < 16; ++e)
              acc[j][e] = j < 4 ? (r32 == (idx & 31) ? cs[j & 3][e >> 2][e & 3] : acc[j][e]) : (rel == 8 * (e >> 2) + (e & 3) ? cv[j & 1] : acc[j][e]);
        }
      }
      constexpr float QS = 0.17677669529663687f * 1.4426950408889634f;
      u32x4 own[4];
#pragma unroll
      for (int j = 5; j >= 0; --j) {
        u32x4 o[2];
#pragma unroll
        for (int e = 0; e < 8; ++e) {
          unsigned w = pack2(acc[j][2 * e], acc[j][2 * e + 1]);
          if (j < 2) w = pack2(lo16(w) * QS, hi16(w) * QS);
          o[e >> 2][e & 3] = w;
        }
        if (j >= 4) { o[0] = fo_swap(o[0]); o[1] = fo_swap(o[1]); }
        const int hs = (j & 1) ^ hh;
        if (j >= 2) {
          const unsigned dst = OFF_KV + L * (10 * MBY) + (tn * 2 + hs) * HS + tile * 4096 + ((j >> 1) - 1) * 2048 + lane * 16;
          stg(rs, dst, o[0]); stg(rs, dst + 1024, o[1]);
        }
        if (j >= 2 && !(j & 1)) { own[j - 2] = o[0]; own[j - 1] = o[1]; }
        else {
          char* p = smem + L_XCH + (j < 2 ? (hs * 4 + wid) * 2048 : 16384 + wid * 4096 + ((j >> 1) - 1) * 2048) + lane * 16;
          *reinterpret_cast<u32x4*>(p) = o[0]; *reinterpret_cast<u32x4*>(p + 1024) = o[1];
        }
        SBAR();
      }
      asm volatile(
        "v_mov_b64 v[0:1], 0\n\t"
        "v_mov_b64 v[2:3], 0\n\t"
        "v_mov_b64 v[4:5], 0\n\t"
        "v_mov_b64 v[6:7], 0\n\t"
        "v_mov_b64 v[8:9], 0\n\t"
        "v_mov_b64 v[10:11], 0\n\t"
        "v_mov_b64 v[12:13], 0\n\t"
        "v_mov_b64 v[14:15], 0\n\t"
        "v_mov_b64 v[16:17], 0\n\t"
        "v_mov_b64 v[18:19], 0\n\t"
        "v_mov_b64 v[20:21], 0\n\t"
        "v_mov_b64 v[22:23], 0\n\t"
        "v_mov_b64 v[24:25], 0\n\t"
        "v_mov_b64 v[26:27], 0\n\t"
        "v_mov_b64 v[28:29], 0\n\t"
        "v_mov_b64 v[30:31], 0\n\t"
        "v_mov_b64 v[32:33], 0\n\t"
        "v_mov_b64 v[34:35], 0\n\t"
        "v_mov_b64 v[36:37], 0\n\t"
        "v_mov_b64 v[38:39], 0\n\t"
        "v_mov_b64 v[40:41], 0\n\t"
        "v_mov_b64 v[42:43], 0\n\t"
        "v_mov_b64 v[44:45], 0\n\t"
        "v_mov_b64 v[46:47], 0\n\t"
        "v_mov_b64 v[48:49], 0\n\t"
        "v_mov_b64 v[50:51], 0\n\t"
        "v_mov_b64 v[52:53], 0\n\t"
        "v_mov_b64 v[54:55], 0\n\t"
        "v_mov_b64 v[56:57], 0\n\t"
        "v_mov_b64 v[58:59], 0\n\t"
        "v_mov_b64 v[60:61], 0\n\t"
        "v_mov_b64 v[62:63], 0\n\t"
        "v_mov_b64 v[128:129], 0\n\t"
        "v_mov_b64 v[130:131], 0\n\t"
        "v_mov_b64 v[132:133], 0\n\t"
        "v_mov_b64 v[134:135], 0\n\t"
        "v_mov_b64 v[136:137], 0\n\t"
        "v_mov_b64 v[138:139], 0\n\t"
        "v_mov_b64 v[140:141], 0\n\t"
        "v_mov_b64 v[142:143], 0\n\t"
        : "={v[0:15]}"(oz[0]), "={v[16:31]}"(oz[1]), "={v[32:47]}"(oz[2]), "={v[48:63]}"(oz[3]), "={v[128:143]}"(oz[4]));
      BAR_ALL();
      kva = cat4(own[0], own[1], own[2], own[3]);
      const u32x4* pk = reinterpret_cast<const u32x4*>(smem + L_XCH + 16384 + (wid ^ 1) * 4096 + lane * 16);
#pragma unroll
      for (int u = 0; u < 4; ++u) {
        const u32x4* p = reinterpret_cast<const u32x4*>(smem + L_XCH + (hh * 4 + ((2 * kh + 2 + u) & 3)) * 2048 + lane * 16);
        q[u] = __builtin_shufflevector(p[0], p[64], 0, 1, 2, 3, 4, 5, 6, 7);
      }
      kvb = cat4(pk[0], pk[64], pk[128], pk[192]);
    }
    STAMPV(5 + 16 * L);
    {
      u32x4 ones;
      unsigned kvoff;
      u32x8 fv, cv;
      asm volatile("" : "={v[184:191]}"(fv), "={v[216:223]}"(cv));
      { PHASE_VARS;
        const unsigned v = (lane & 3) == 0 ? 0x3F803F80u : 0u; const bool up = (lane & 4) != 0; ones[0] = ones[1] = up ? 0u : v; ones[2] = ones[3] = up ? v : 0u;
        kvoff = OFF_KV + L * (10 * MBY) + (tn * 2 + hh) * HS + kh * 8192 + lane * 16;
        fv[1] = lbase + L_FREE + wid * 256 + lane * 4; fv[2] = 32 * L + 32;
        cv[0] = lbase + L_XCH + wid * 8192 + lane * 16; cv[1] = lbase + L_XCH + (wid ^ 2) * 8192 + lane * 16;
        cv[2] = lbase + L_XCH + 32768 + wid * 512 + lane * 4; cv[3] = lbase + L_XCH + 32768 + (wid ^ 2) * 512 + lane * 4;
        cv[4] = lbase + L_W + 48 + wid * 4; cv[5] = lbase + L_W + 48 + (wid ^ 2) * 4; cv[6] = L + 1;
        cv[7] = ((tile * 32 + r32) * D_ + tn * 64 + hi * 8) * 2; }
      f32x16 o0 = oz[0], o1 = oz[1], o2 = oz[2], o3 = oz[3], lv = oz[4];
      unsigned soff = tm * 16384;
      STAMP_DECL;
      asm volatile(
        "s_nop 1\n\t"
        "v_mfma_f32_32x32x16_bf16 v[96:111], v[152:155], v[64:67], 0\n\t"
        "v_mfma_f32_32x32x16_bf16 v[96:111], v[156:159], v[68:71], v[96:111]\n\t"
        "s_mov_b32 s46, 0\n\t"
        "ds_write_b32 v185, v186\n\t"
        "s_nop 9\n\t"
        "v_exp_f32 v96, v96\n\t"
        "v_exp_f32 v97, v97\n\t"
        "v_exp_f32 v98, v98\n\t"
        "v_exp_f32 v99, v99\n\t"
        "v_exp_f32 v100, v100\n\t"
        "v_exp_f32 v101, v101\n\t"
        "v_mfma_f32_32x32x16_bf16 v[112:127], v[152:155], v[72:75], 0\n\t"
        "v_exp_f32 v102, v102\n\t"
        "v_exp_f32 v103, v103\n\t"
        "v_exp_f32 v104, v104\n\t"
        "v_mfma_f32_32x32x16_bf16 v[112:127], v[156:159], v[76:79], v[112:127]\n\t"
        "v_exp_f32 v105, v105\n\t"
        "v_exp_f32 v106, v106\n\t"
        "v_exp_f32 v107, v107\n\t"
        "v_smfmac_f32_32x32x32_bf16 v[0:15], v[160:163], v[96:103], v148\n\t"
        "v_exp_f32 v108, v108\n\t"
        "v_exp_f32 v109, v109\n\t"
        "v_exp_f32 v110, v110\n\t"
        "v_smfmac_f32_16x16x64_bf16 v[128:131], v[144:147], v[96:103], v148\n\t"
        "v_exp_f32 v111, v111\n\t"
        "v_exp_f32 v112, v112\n\t"
        "v_exp_f32 v113, v113\n\t"
        "v_smfmac_f32_32x32x32_bf16 v[0:15], v[164:167], v[104:111], v148\n\t"
        "v_exp_f32 v114, v114\n\t"
        "v_exp_f32 v115, v115\n\t"
        "v_exp_f32 v116, v116\n\t"
        "v_smfmac_f32_16x16x64_bf16 v[128:131], v[144:147], v[104:111], v148\n\t"
        "v_exp_f32 v117, v117\n\t"
        "v_mfma_f32_32x32x16_bf16 v[96:111], v[152:155], v[80:83], 0\n\t"
        "v_exp_f32 v118, v118\n\t"
        "v_exp_f32 v119, v119\n\t"
        "v_exp_f32 v120, v120\n\t"
        "v_mfma_f32_32x32x16_bf16 v[96:111], v[156:159], v[84:87], v[96:111]\n\t"
        "v_exp_f32 v121, v121\n\t"
        "v_exp_f32 v122, v122\n\t"
        "v_exp_f32 v123, v123\n\t"
        "v_smfmac_f32_32x32x32_bf16 v[16:31], v[160:163], v[112:119], v148\n\t"
        "v_exp_f32 v124, v124\n\t"
        "v_exp_f32 v125, v125\n\t"
        "v_exp_f32 v126, v126\n\t"
        "v_smfmac_f32_16x16x64_bf16 v[132:135], v[144:147], v[112:119], v148\n\t"
        "v_exp_f32 v127, v127\n\t"
        "v_exp_f32 v96, v96\n\t"
        "v_exp_f32 v97, v97\n\t"
        "v_smfmac_f32_32x32x32_bf16 v[16:31], v[164:167], v[120:127], v148\n\t"
        "v_exp_f32 v98, v98\n\t"
        "v_exp_f32 v99, v99\n\t"
        "v_exp_f32 v100, v100\n\t"
        "v_smfmac_f32_16x16x64_bf16 v[132:135], v[144:147], v[120:127], v148\n\t"
        "v_exp_f32 v101, v101\n\t"
        "v_mfma_f32_32x32x16_bf16 v[112:127], v[152:155], v[88:91], 0\n\t"
        "v_exp_f32 v102, v102\n\t"
        "v_exp_f32 v103, v103\n\t"
        "v_exp_f32 v104, v104\n\t"
        "v_mfma_f32_32x32x16_bf16 v[112:127], v[156:159], v[92:95], v[112:127]\n\t"
        "v_exp_f32 v105, v105\n\t"
        "v_exp_f32 v106, v106\n\t"
        "v_exp_f32 v107, v107\n\t"
        "v_smfmac_f32_32x32x32_bf16 v[32:47], v[160:163], v[96:103], v148\n\t"
        "v_exp_f32 v108, v108\n\t"
        "v_exp_f32 v109, v109\n\t"
        "v_exp_f32 v110, v110\n\t"
        "v_smfmac_f32_16x16x64_bf16 v[136:139], v[144:147], v[96:103], v148\n\t"
        "v_exp_f32 v111, v111\n\t"
        "v_exp_f32 v112, v112\n\t"
        "v_exp_f32 v113, v113\n\t"
        "v_smfmac_f32_32x32x32_bf16 v[32:47], v[164:167], v[104:111], v148\n\t"
        "v_exp_f32 v114, v114\n\t"
        "v_exp_f32 v115, v115\n\t"
        "v_exp_f32 v116, v116\n\t"
        "v_smfmac_f32_16x16x64_bf16 v[136:139], v[144:147], v[104:111], v148\n\t"
        "v_exp_f32 v117, v117\n\t"
        "v_mfma_f32_32x32x16_bf16 v[96:111], v[168:171], v[64:67], 0\n\t"
        "v_exp_f32 v118, v118\n\t"
        "v_exp_f32 v119, v119\n\t"
        "v_exp_f32 v120, v120\n\t"
        "v_mfma_f32_32x32x16_bf16 v[96:111], v[172:175], v[68:71], v[96:111]\n\t"
        "v_exp_f32 v121, v121\n\t"
        "v_exp_f32 v122, v122\n\t"
        "v_exp_f32 v123, v123\n\t"
        "v_smfmac_f32_32x32x32_bf16 v[48:63], v[160:163], v[112:119], v148\n\t"
        "v_exp_f32 v124, v124\n\t"
        "v_exp_f32 v125, v125\n\t"
        "v_exp_f32 v126, v126\n\t"
        "v_smfmac_f32_16x16x64_bf16 v[140:143], v[144:147], v[112:119], v148\n\t"
        "v_exp_f32 v127, v127\n\t"
        "v_exp_f32 v96, v96\n\t"
        "v_exp_f32 v97, v97\n\t"
        "v_smfmac_f32_32x32x32_bf16 v[48:63], v[164:167], v[120:127], v148\n\t"
        "v_exp_f32 v98, v98\n\t"
        "v_exp_f32 v99, v99\n\t"
        "v_exp_f32 v100, v100\n\t"
        "v_smfmac_f32_16x16x64_bf16 v[140:143], v[144:147], v[120:127], v148\n\t"
        "v_exp_f32 v101, v101\n\t"
        "v_mfma_f32_32x32x16_bf16 v[112:127], v[168:171], v[72:75], 0\n\t"
        "v_exp_f32 v102, v102\n\t"
        "v_exp_f32 v103, v103\n\t"
        "v_exp_f32 v104, v104\n\t"
        "ds_read_b32 v151, v224\n\t"
        "v_mfma_f32_32x32x16_bf16 v[112:127], v[172:175], v[76:79], v[112:127]\n\t"
        "v_exp_f32 v105, v105\n\t"
        "v_exp_f32 v106, v106\n\t"
        "v_exp_f32 v107, v107\n\t"
        "v_smfmac_f32_32x32x32_bf16 v[0:15], v[176:179], v[96:103], v148\n\t"
        "v_exp_f32 v108, v108\n\t"
        "v_exp_f32 v109, v109\n\t"
        "v_exp_f32 v110, v110\n\t"
        "v_smfmac_f32_16x16x64_bf16 v[128:131], v[144:147], v[96:103], v148\n\t"
        "v_exp_f32 v111, v111\n\t"
        "s_cmp_lg_u32 s46, 0\n\t"
        "s_cbranch_scc1 31f\n\t"
        "s_waitcnt lgkmcnt(0)\n\t"
        "v_cmp_ne_u32 vcc, v222, v151\n\t"
        "s_cbranch_vccz 30f\n\t"
        "31:\n\t"
        "v_exp_f32 v112, v112\n\t"
        "v_exp_f32 v113, v113\n\t"
        "v_smfmac_f32_32x32x32_bf16 v[0:15], v[180:183], v[104:111], v148\n\t"
        "v_exp_f32 v114, v114\n\t"
        "v_exp_f32 v115, v115\n\t"
        "v_exp_f32 v116, v116\n\t"
        "v_smfmac_f32_16x16x64_bf16 v[128:131], v[144:147], v[104:111], v148\n\t"
        "v_exp_f32 v117, v117\n\t"
        "v_mfma_f32_32x32x16_bf16 v[96:111], v[168:171], v[80:83], 0\n\t"
        "v_exp_f32 v118, v118\n\t"
        "v_exp_f32 v119, v119\n\t"
        "v_exp_f32 v120, v120\n\t"
        "ds_read_b32 v151, v224\n\t"
        "v_mfma_f32_32x32x16_bf16 v[96:111], v[172:175], v[84:87], v[96:111]\n\t"
        "v_exp_f32 v121, v121\n\t"
        "v_exp_f32 v122, v122\n\t"
        "v_exp_f32 v123, v123\n\t"
        "v_smfmac_f32_32x32x32_bf16 v[16:31], v[176:179], v[112:119], v148\n\t"
        "v_exp_f32 v124, v124\n\t"
        "v_exp_f32 v125, v125\n\t"
        "v_exp_f32 v126, v126\n\t"
        "v_smfmac_f32_16x16x64_bf16 v[132:135], v[144:147], v[112:119], v148\n\t"
        "v_exp_f32 v127, v127\n\t"
        "s_cmp_lg_u32 s46, 0\n\t"
        "s_cbranch_scc1 33f\n\t"
        "s_waitcnt lgkmcnt(0)\n\t"
        "v_cmp_ne_u32 vcc, v222, v151\n\t"
        "s_cbranch_vccz 32f\n\t"
        "33:\n\t"
        "v_exp_f32 v96, v96\n\t"
        "v_exp_f32 v97, v97\n\t"
        "v_smfmac_f32_32x32x32_bf16 v[16:31], v[180:183], v[120:127], v148\n\t"
        "v_exp_f32 v98, v98\n\t"
        "v_exp_f32 v99, v99\n\t"
        "v_exp_f32 v100, v100\n\t"
        "v_smfmac_f32_16x16x64_bf16 v[132:135], v[144:147], v[120:127], v148\n\t"
        "v_exp_f32 v101, v101\n\t"
        "v_mfma_f32_32x32x16_bf16 v[112:127], v[168:171], v[88:91], 0\n\t"
        "v_exp_f32 v102, v102\n\t"
        "v_exp_f32 v103, v103\n\t"
        "v_exp_f32 v104, v104\n\t"
        "v_mfma_f32_32x32x16_bf16 v[112:127], v[172:175], v[92:95], v[112:127]\n\t"
        "v_exp_f32 v105, v105\n\t"
        "v_exp_f32 v106, v106\n\t"
        "v_exp_f32 v107, v107\n\t"
        "v_smfmac_f32_32x32x32_bf16 v[32:47], v[176:179], v[96:103], v148\n\t"
        "v_exp_f32 v108, v108\n\t"
        "v_exp_f32 v109, v109\n\t"
        "v_exp_f32 v110, v110\n\t"
        "v_smfmac_f32_16x16x64_bf16 v[136:139], v[144:147], v[96:103], v148\n\t"
        "v_exp_f32 v111, v111\n\t"
        "s_cmp_lg_u32 s46, 0\n\t"
        "s_cbranch_scc1 5f\n\t"
        "s_mov_b32 s45, 0x200000\n\t"
        "2:\n\t"
        "ds_read_b32 v151, v224\n\t"
        "s_sub_u32 s45, s45, 1\n\t"
        "s_waitcnt lgkmcnt(0)\n\t"
        "s_cbranch_scc1 3f\n\t"
        "v_cmp_ne_u32 vcc, v222, v151\n\t"
        "s_cbranch_vccnz 2b\n\t"
        "3:\n\t"
        "s_add_u32 s44, s44, 0x4000\n\t"
        "s_and_b32 s44, s44, 0x3ffff\n\t"
        "buffer_load_dwordx4 v[184:187], v149, s[40:43], s44 offen offset:0 sc1\n\t"
        "buffer_load_dwordx4 v[188:191], v149, s[40:43], s44 offen offset:1024 sc1\n\t"
        "buffer_load_dwordx4 v[192:195], v149, s[40:43], s44 offen offset:2048 sc1\n\t"
        "buffer_load_dwordx4 v[196:199], v149, s[40:43], s44 offen offset:3072 sc1\n\t"
        "buffer_load_dwordx4 v[200:203], v150, s[40:43], s44 offen offset:0 sc1\n\t"
        "buffer_load_dwordx4 v[204:207], v150, s[40:43], s44 offen offset:1024 sc1\n\t"
        "buffer_load_dwordx4 v[208:211], v150, s[40:43], s44 offen offset:2048 sc1\n\t"
        "buffer_load_dwordx4 v[212:215], v150, s[40:43], s44 offen offset:3072 sc1\n\t"
        "s_add_u32 s44, s44, 0x4000\n\t"
        "s_and_b32 s44, s44, 0x3ffff\n\t"
        "buffer_load_dwordx4 v[152:155], v149, s[40:43], s44 offen offset:0 sc1\n\t"
        "buffer_load_dwordx4 v[156:159], v149, s[40:43], s44 offen offset:1024 sc1\n\t"
        "buffer_load_dwordx4 v[160:163], v149, s[40:43], s44 offen offset:2048 sc1\n\t"
        "buffer_load_dwordx4 v[164:167], v149, s[40:43], s44 offen offset:3072 sc1\n\t"
        "5:\n\t"
        "s_mov_b32 s45, 7\n\t"
        "1:\n\t"
        "v_exp_f32 v112, v112\n\t"
        "v_exp_f32 v113, v113\n\t"
        "v_smfmac_f32_32x32x32_bf16 v[32:47], v[180:183], v[104:111], v148\n\t"
        "v_exp_f32 v114, v114\n\t"
        "v_exp_f32 v115, v115\n\t"
        "v_exp_f32 v116, v116\n\t"
        "v_smfmac_f32_16x16x64_bf16 v[136:139], v[144:147], v[104:111], v148\n\t"
        "v_exp_f32 v117, v117\n\t"
        "s_waitcnt vmcnt(8)\n\t"
        "v_mfma_f32_32x32x16_bf16 v[96:111], v[184:187], v[64:67], 0\n\t"
        "v_exp_f32 v118, v118\n\t"
        "v_exp_f32 v119, v119\n\t"
        "v_exp_f32 v120, v120\n\t"
        "v_mfma_f32_32x32x16_bf16 v[96:111], v[188:191], v[68:71], v[96:111]\n\t"
        "v_exp_f32 v121, v121\n\t"
        "v_exp_f32 v122, v122\n\t"
        "v_exp_f32 v123, v123\n\t"
        "v_smfmac_f32_32x32x32_bf16 v[48:63], v[176:179], v[112:119], v148\n\t"
        "v_exp_f32 v124, v124\n\t"
        "v_exp_f32 v125, v125\n\t"
        "v_exp_f32 v126, v126\n\t"
        "v_smfmac_f32_16x16x64_bf16 v[140:143], v[144:147], v[112:119], v148\n\t"
        "buffer_load_dwordx4 v[168:171], v150, s[40:43], s44 offen offset:0 sc1\n\t"
        "v_exp_f32 v127, v127\n\t"
        "v_exp_f32 v96, v96\n\t"
        "v_exp_f32 v97, v97\n\t"
        "v_smfmac_f32_32x32x32_bf16 v[48:63], v[180:183], v[120:127], v148\n\t"
        "v_exp_f32 v98, v98\n\t"
        "v_exp_f32 v99, v99\n\t"
        "v_exp_f32 v100, v100\n\t"
        "v_smfmac_f32_16x16x64_bf16 v[140:143], v[144:147], v[120:127], v148\n\t"
        "v_exp_f32 v101, v101\n\t"
        "v_mfma_f32_32x32x16_bf16 v[112:127], v[184:187], v[72:75], 0\n\t"
        "v_exp_f32 v102, v102\n\t"
        "v_exp_f32 v103, v103\n\t"
        "v_exp_f32 v104, v104\n\t"
        "v_mfma_f32_32x32x16_bf16 v[112:127], v[188:191], v[76:79], v[112:127]\n\t"
        "v_exp_f32 v105, v105\n\t"
        "v_exp_f32 v106, v106\n\t"
        "v_exp_f32 v107, v107\n\t"
        "v_smfmac_f32_32x32x32_bf16 v[0:15], v[192:195], v[96:103], v148\n\t"
        "v_exp_f32 v108, v108\n\t"
        "v_exp_f32 v109, v109\n\t"
        "v_exp_f32 v110, v110\n\t"
        "v_smfmac_f32_16x16x64_bf16 v[128:131], v[144:147], v[96:103], v148\n\t"
        "buffer_load_dwordx4 v[172:175], v150, s[40:43], s44 offen offset:1024 sc1\n\t"
        "v_exp_f32 v111, v111\n\t"
        "v_exp_f32 v112, v112\n\t"
        "v_exp_f32 v113, v113\n\t"
        "v_smfmac_f32_32x32x32_bf16 v[0:15], v[196:199], v[104:111], v148\n\t"
        "v_exp_f32 v114, v114\n\t"
        "v_exp_f32 v115, v115\n\t"
        "v_exp_f32 v116, v116\n\t"
        "v_smfmac_f32_16x16x64_bf16 v[128:131], v[144:147], v[104:111], v148\n\t"
        "v_exp_f32 v117, v117\n\t"
        "v_mfma_f32_32x32x16_bf16 v[96:111], v[184:187], v[80:83], 0\n\t"
        "v_exp_f32 v118, v118\n\t"
        "v_exp_f32 v119, v119\n\t"
        "v_exp_f32 v120, v120\n\t"
        "v_mfma_f32_32x32x16_bf16 v[96:111], v[188:191], v[84:87], v[96:111]\n\t"
        "v_exp_f32 v121, v121\n\t"
        "v_exp_f32 v122, v122\n\t"
        "v_exp_f32 v123, v123\n\t"
        "v_smfmac_f32_32x32x32_bf16 v[16:31], v[192:195], v[112:119], v148\n\t"
        "v_exp_f32 v124, v124\n\t"
        "v_exp_f32 v125, v125\n\t"
        "v_exp_f32 v126, v126\n\t"
        "v_smfmac_f32_16x16x64_bf16 v[132:135], v[144:147], v[112:119], v148\n\t"
        "buffer_load_dwordx4 v[176:179], v150, s[40:43], s44 offen offset:2048 sc1\n\t"
        "v_exp_f32 v127, v127\n\t"
        "v_exp_f32 v96, v96\n\t"
        "v_exp_f32 v97, v97\n\t"
        "v_smfmac_f32_32x32x32_bf16 v[16:31], v[196:199], v[120:127], v148\n\t"
        "v_exp_f32 v98, v98\n\t"
        "v_exp_f32 v99, v99\n\t"
        "v_exp_f32 v100, v100\n\t"
        "v_smfmac_f32_16x16x64_bf16 v[132:135], v[144:147], v[120:127], v148\n\t"
        "v_exp_f32 v101, v101\n\t"
        "v_mfma_f32_32x32x16_bf16 v[112:127], v[184:187], v[88:91], 0\n\t"
        "v_exp_f32 v102, v102\n\t"
        "v_exp_f32 v103, v103\n\t"
        "v_exp_f32 v104, v104\n\t"
        "v_mfma_f32_32x32x16_bf16 v[112:127], v[188:191], v[92:95], v[112:127]\n\t"
        "v_exp_f32 v105, v105\n\t"
        "v_exp_f32 v106, v106\n\t"
        "v_exp_f32 v107, v107\n\t"
        "v_smfmac_f32_32x32x32_bf16 v[32:47], v[192:195], v[96:103], v148\n\t"
        "v_exp_f32 v108, v108\n\t"
        "v_exp_f32 v109, v109\n\t"
        "v_exp_f32 v110, v110\n\t"
        "v_smfmac_f32_16x16x64_bf16 v[136:139], v[144:147], v[96:103], v148\n\t"
        "buffer_load_dwordx4 v[180:183], v150, s[40:43], s44 offen offset:3072 sc1\n\t"
        "v_exp_f32 v111, v111\n\t"
        "v_exp_f32 v112, v112\n\t"
        "v_exp_f32 v113, v113\n\t"
        "v_smfmac_f32_32x32x32_bf16 v[32:47], v[196:199], v[104:111], v148\n\t"
        "s_add_u32 s44, s44, 0x4000\n\t"
        "s_and_b32 s44, s44, 0x3ffff\n\t"
        "v_exp_f32 v114, v114\n\t"
        "v_exp_f32 v115, v115\n\t"
        "v_exp_f32 v116, v116\n\t"
        "v_smfmac_f32_16x16x64_bf16 v[136:139], v[144:147], v[104:111], v148\n\t"
        "v_exp_f32 v117, v117\n\t"
        "s_waitcnt vmcnt(8)\n\t"
        "v_mfma_f32_32x32x16_bf16 v[96:111], v[200:203], v[64:67], 0\n\t"
        "v_exp_f32 v118, v118\n\t"
        "v_exp_f32 v119, v119\n\t"
        "v_exp_f32 v120, v120\n\t"
        "v_mfma_f32_32x32x16_bf16 v[96:111], v[204:207], v[68:71], v[96:111]\n\t"
        "v_exp_f32 v121, v121\n\t"
        "v_exp_f32 v122, v122\n\t"
        "v_exp_f32 v123, v123\n\t"
        "v_smfmac_f32_32x32x32_bf16 v[48:63], v[192:195], v[112:119], v148\n\t"
        "v_exp_f32 v124, v124\n\t"
        "v_exp_f32 v125, v125\n\t"
        "v_exp_f32 v126, v126\n\t"
        "v_smfmac_f32_16x16x64_bf16 v[140:143], v[144:147], v[112:119], v148\n\t"
        "buffer_load_dwordx4 v[184:187], v149, s[40:43], s44 offen offset:0 sc1\n\t"
        "v_exp_f32 v127, v127\n\t"
        "v_exp_f32 v96, v96\n\t"
        "v_exp_f32 v97, v97\n\t"
        "v_smfmac_f32_32x32x32_bf16 v[48:63], v[196:199], v[120:127], v148\n\t"
        "v_exp_f32 v98, v98\n\t"
        "v_exp_f32 v99, v99\n\t"
        "v_exp_f32 v100, v100\n\t"
        "v_smfmac_f32_16x16x64_bf16 v[140:143], v[144:147], v[120:127], v148\n\t"
        "v_exp_f32 v101, v101\n\t"
        "v_mfma_f32_32x32x16_bf16 v[112:127], v[200:203], v[72:75], 0\n\t"
        "v_exp_f32 v102, v102\n\t"
        "v_exp_f32 v103, v103\n\t"
        "v_exp_f32 v104, v104\n\t"
        "v_mfma_f32_32x32x16_bf16 v[112:127], v[204:207], v[76:79], v[112:127]\n\t"
        "v_exp_f32 v105, v105\n\t"
        "v_exp_f32 v106, v106\n\t"
        "v_exp_f32 v107, v107\n\t"
        "v_smfmac_f32_32x32x32_bf16 v[0:15], v[208:211], v[96:103], v148\n\t"
        "v_exp_f32 v108, v108\n\t"
        "v_exp_f32 v109, v109\n\t"
        "v_exp_f32 v110, v110\n\t"
        "v_smfmac_f32_16x16x64_bf16 v[128:131], v[144:147], v[96:103], v148\n\t"
        "buffer_load_dwordx4 v[188:191], v149, s[40:43], s44 offen offset:1024 sc1\n\t"
        "v_exp_f32 v111, v111\n\t"
        "v_exp_f32 v112, v112\n\t"
        "v_exp_f32 v113, v113\n\t"
        "v_smfmac_f32_32x32x32_bf16 v[0:15], v[212:215], v[104:111], v148\n\t"
        "v_exp_f32 v114, v114\n\t"
        "v_exp_f32 v115, v115\n\t"
        "v_exp_f32 v116, v116\n\t"
        "v_smfmac_f32_16x16x64_bf16 v[128:131], v[144:147], v[104:111], v148\n\t"
        "v_exp_f32 v117, v117\n\t"
        "v_mfma_f32_32x32x16_bf16 v[96:111], v[200:203], v[80:83], 0\n\t"
        "v_exp_f32 v118, v118\n\t"
        "v_exp_f32 v119, v119\n\t"
        "v_exp_f32 v120, v120\n\t"
        "v_mfma_f32_32x32x16_bf16 v[96:111], v[204:207], v[84:87], v[96:111]\n\t"
        "v_exp_f32 v121, v121\n\t"
        "v_exp_f32 v122, v122\n\t"
        "v_exp_f32 v123, v123\n\t"
        "v_smfmac_f32_32x32x32_bf16 v[16:31], v[208:211], v[112:119], v148\n\t"
        "v_exp_f32 v124, v124\n\t"
        "v_exp_f32 v125, v125\n\t"
        "v_exp_f32 v126, v126\n\t"
        "v_smfmac_f32_16x16x64_bf16 v[132:135], v[144:147], v[112:119], v148\n\t"
        "buffer_load_dwordx4 v[192:195], v149, s[40:43], s44 offen offset:2048 sc1\n\t"
        "v_exp_f32 v127, v127\n\t"
        "v_exp_f32 v96, v96\n\t"
        "v_exp_f32 v97, v97\n\t"
        "v_smfmac_f32_32x32x32_bf16 v[16:31], v[212:215], v[120:127], v148\n\t"
        "v_exp_f32 v98, v98\n\t"
        "v_exp_f32 v99, v99\n\t"
        "v_exp_f32 v100, v100\n\t"
        "v_smfmac_f32_16x16x64_bf16 v[132:135], v[144:147], v[120:127], v148\n\t"
        "v_exp_f32 v101, v101\n\t"
        "v_mfma_f32_32x32x16_bf16 v[112:127], v[200:203], v[88:91], 0\n\t"
        "v_exp_f32 v102, v102\n\t"
        "v_exp_f32 v103, v103\n\t"
        "v_exp_f32 v104, v104\n\t"
        "v_mfma_f32_32x32x16_bf16 v[112:127], v[204:207], v[92:95], v[112:127]\n\t"
        "v_exp_f32 v105, v105\n\t"
        "v_exp_f32 v106, v106\n\t"
        "v_exp_f32 v107, v107\n\t"
        "v_smfmac_f32_32x32x32_bf16 v[32:47], v[208:211], v[96:103], v148\n\t"
        "v_exp_f32 v108, v108\n\t"
        "v_exp_f32 v109, v109\n\t"
        "v_exp_f32 v110, v110\n\t"
        "v_smfmac_f32_16x16x64_bf16 v[136:139], v[144:147], v[96:103], v148\n\t"
        "buffer_load_dwordx4 v[196:199], v149, s[40:43], s44 offen offset:3072 sc1\n\t"
        "v_exp_f32 v111, v111\n\t"
        "v_exp_f32 v112, v112\n\t"
        "v_exp_f32 v113, v113\n\t"
        "v_smfmac_f32_32x32x32_bf16 v[32:47], v[212:215], v[104:111], v148\n\t"
        "v_exp_f32 v114, v114\n\t"
        "v_exp_f32 v115, v115\n\t"
        "v_exp_f32 v116, v116\n\t"
        "v_smfmac_f32_16x16x64_bf16 v[136:139], v[144:147], v[104:111], v148\n\t"
        "v_exp_f32 v117, v117\n\t"
        "s_waitcnt vmcnt(8)\n\t"
        "v_mfma_f32_32x32x16_bf16 v[96:111], v[152:155], v[64:67], 0\n\t"
        "v_exp_f32 v118, v118\n\t"
        "v_exp_f32 v119, v119\n\t"
        "v_exp_f32 v120, v120\n\t"
        "v_mfma_f32_32x32x16_bf16 v[96:111], v[156:159], v[68:71], v[96:111]\n\t"
        "v_exp_f32 v121, v121\n\t"
        "v_exp_f32 v122, v122\n\t"
        "v_exp_f32 v123, v123\n\t"
        "v_smfmac_f32_32x32x32_bf16 v[48:63], v[208:211], v[112:119], v148\n\t"
        "v_exp_f32 v124, v124\n\t"
        "v_exp_f32 v125, v125\n\t"
        "v_exp_f32 v126, v126\n\t"
        "v_smfmac_f32_16x16x64_bf16 v[140:143], v[144:147], v[112:119], v148\n\t"
        "buffer_load_dwordx4 v[200:203], v150, s[40:43], s44 offen offset:0 sc1\n\t"
        "v_exp_f32 v127, v127\n\t"
        "v_exp_f32 v96, v96\n\t"
        "v_exp_f32 v97, v97\n\t"
        "v_smfmac_f32_32x32x32_bf16 v[48:63], v[212:215], v[120:127], v148\n\t"
        "v_exp_f32 v98, v98\n\t"
        "v_exp_f32 v99, v99\n\t"
        "v_exp_f32 v100, v100\n\t"
        "v_smfmac_f32_16x16x64_bf16 v[140:143], v[144:147], v[120:127], v148\n\t"
        "v_exp_f32 v101, v101\n\t"
        "v_mfma_f32_32x32x16_bf16 v[112:127], v[152:155], v[72:75], 0\n\t"
        "v_exp_f32 v102, v102\n\t"
        "v_exp_f32 v103, v103\n\t"
        "v_exp_f32 v104, v104\n\t"
        "v_mfma_f32_32x32x16_bf16 v[112:127], v[156:159], v[76:79], v[112:127]\n\t"
        "v_exp_f32 v105, v105\n\t"
        "v_exp_f32 v106, v106\n\t"
        "v_exp_f32 v107, v107\n\t"
        "v_smfmac_f32_32x32x32_bf16 v[0:15], v[160:163], v[96:103], v148\n\t"
        "v_exp_f32 v108, v108\n\t"
        "v_exp_f32 v109, v109\n\t"
        "v_exp_f32 v110, v110\n\t"
        "v_smfmac_f32_16x16x64_bf16 v[128:131], v[144:147], v[96:103], v148\n\t"
        "buffer_load_dwordx4 v[204:207], v150, s[40:43], s44 offen offset:1024 sc1\n\t"
        "v_exp_f32 v111, v111\n\t"
        "v_exp_f32 v112, v112\n\t"
        "v_exp_f32 v113, v113\n\t"
        "v_smfmac_f32_32x32x32_bf16 v[0:15], v[164:167], v[104:111], v148\n\t"
        "v_exp_f32 v114, v114\n\t"
        "v_exp_f32 v115, v115\n\t"
        "v_exp_f32 v116, v116\n\t"
        "v_smfmac_f32_16x16x64_bf16 v[128:131], v[144:147], v[104:111], v148\n\t"
        "v_exp_f32 v117, v117\n\t"
        "v_mfma_f32_32x32x16_bf16 v[96:111], v[152:155], v[80:83], 0\n\t"
        "v_exp_f32 v118, v118\n\t"
        "v_exp_f32 v119, v119\n\t"
        "v_exp_f32 v120, v120\n\t"
        "v_mfma_f32_32x32x16_bf16 v[96:111], v[156:159], v[84:87], v[96:111]\n\t"
        "v_exp_f32 v121, v121\n\t"
        "v_exp_f32 v122, v122\n\t"
        "v_exp_f32 v123, v123\n\t"
        "v_smfmac_f32_32x32x32_bf16 v[16:31], v[160:163], v[112:119], v148\n\t"
        "v_exp_f32 v124, v124\n\t"
        "v_exp_f32 v125, v125\n\t"
        "v_exp_f32 v126, v126\n\t"
        "v_smfmac_f32_16x16x64_bf16 v[132:135], v[144:147], v[112:119], v148\n\t"
        "buffer_load_dwordx4 v[208:211], v150, s[40:43], s44 offen offset:2048 sc1\n\t"
        "v_exp_f32 v127, v127\n\t"
        "v_exp_f32 v96, v96\n\t"
        "v_exp_f32 v97, v97\n\t"
        "v_smfmac_f32_32x32x32_bf16 v[16:31], v[164:167], v[120:127], v148\n\t"
        "v_exp_f32 v98, v98\n\t"
        "v_exp_f32 v99, v99\n\t"
        "v_exp_f32 v100, v100\n\t"
        "v_smfmac_f32_16x16x64_bf16 v[132:135], v[144:147], v[120:127], v148\n\t"
        "v_exp_f32 v101, v101\n\t"
        "v_mfma_f32_32x32x16_bf16 v[112:127], v[152:155], v[88:91], 0\n\t"
        "v_exp_f32 v102, v102\n\t"
        "v_exp_f32 v103, v103\n\t"
        "v_exp_f32 v104, v104\n\t"
        "v_mfma_f32_32x32x16_bf16 v[112:127], v[156:159], v[92:95], v[112:127]\n\t"
        "v_exp_f32 v105, v105\n\t"
        "v_exp_f32 v106, v106\n\t"
        "v_exp_f32 v107, v107\n\t"
        "v_smfmac_f32_32x32x32_bf16 v[32:47], v[160:163], v[96:103], v148\n\t"
        "v_exp_f32 v108, v108\n\t"
        "v_exp_f32 v109, v109\n\t"
        "v_exp_f32 v110, v110\n\t"
        "v_smfmac_f32_16x16x64_bf16 v[136:139], v[144:147], v[96:103], v148\n\t"
        "buffer_load_dwordx4 v[212:215], v150, s[40:43], s44 offen offset:3072 sc1\n\t"
        "v_exp_f32 v111, v111\n\t"
        "v_exp_f32 v112, v112\n\t"
        "v_exp_f32 v113, v113\n\t"
        "v_smfmac_f32_32x32x32_bf16 v[32:47], v[164:167], v[104:111], v148\n\t"
        "s_add_u32 s44, s44, 0x4000\n\t"
        "s_and_b32 s44, s44, 0x3ffff\n\t"
        "v_exp_f32 v114, v114\n\t"
        "v_exp_f32 v115, v115\n\t"
        "v_exp_f32 v116, v116\n\t"
        "v_smfmac_f32_16x16x64_bf16 v[136:139], v[144:147], v[104:111], v148\n\t"
        "v_exp_f32 v117, v117\n\t"
        "s_waitcnt vmcnt(8)\n\t"
        "v_mfma_f32_32x32x16_bf16 v[96:111], v[168:171], v[64:67], 0\n\t"
        "v_exp_f32 v118, v118\n\t"
        "v_exp_f32 v119, v119\n\t"
        "v_exp_f32 v120, v120\n\t"
        "v_mfma_f32_32x32x16_bf16 v[96:111], v[172:175], v[68:71], v[96:111]\n\t"
        "v_exp_f32 v121, v121\n\t"
        "v_exp_f32 v122, v122\n\t"
        "v_exp_f32 v123, v123\n\t"
        "v_smfmac_f32_32x32x32_bf16 v[48:63], v[160:163], v[112:119], v148\n\t"
        "v_exp_f32 v124, v124\n\t"
        "v_exp_f32 v125, v125\n\t"
        "v_exp_f32 v126, v126\n\t"
        "v_smfmac_f32_16x16x64_bf16 v[140:143], v[144:147], v[112:119], v148\n\t"
        "buffer_load_dwordx4 v[152:155], v149, s[40:43], s44 offen offset:0 sc1\n\t"
        "v_exp_f32 v127, v127\n\t"
        "v_exp_f32 v96, v96\n\t"
        "v_exp_f32 v97, v97\n\t"
        "v_smfmac_f32_32x32x32_bf16 v[48:63], v[164:167], v[120:127], v148\n\t"
        "v_exp_f32 v98, v98\n\t"
        "v_exp_f32 v99, v99\n\t"
        "v_exp_f32 v100, v100\n\t"
        "v_smfmac_f32_16x16x64_bf16 v[140:143], v[144:147], v[120:127], v148\n\t"
        "v_exp_f32 v101, v101\n\t"
        "v_mfma_f32_32x32x16_bf16 v[112:127], v[168:171], v[72:75], 0\n\t"
        "v_exp_f32 v102, v102\n\t"
        "v_exp_f32 v103, v103\n\t"
        "v_exp_f32 v104, v104\n\t"
        "v_mfma_f32_32x32x16_bf16 v[112:127], v[172:175], v[76:79], v[112:127]\n\t"
        "v_exp_f32 v105, v105\n\t"
        "v_exp_f32 v106, v106\n\t"
        "v_exp_f32 v107, v107\n\t"
        "v_smfmac_f32_32x32x32_bf16 v[0:15], v[176:179], v[96:103], v148\n\t"
        "v_exp_f32 v108, v108\n\t"
        "v_exp_f32 v109, v109\n\t"
        "v_exp_f32 v110, v110\n\t"
        "v_smfmac_f32_16x16x64_bf16 v[128:131], v[144:147], v[96:103], v148\n\t"
        "buffer_load_dwordx4 v[156:159], v149, s[40:43], s44 offen offset:1024 sc1\n\t"
        "v_exp_f32 v111, v111\n\t"
        "v_exp_f32 v112, v112\n\t"
        "v_exp_f32 v113, v113\n\t"
        "v_smfmac_f32_32x32x32_bf16 v[0:15], v[180:183], v[104:111], v148\n\t"
        "v_exp_f32 v114, v114\n\t"
        "v_exp_f32 v115, v115\n\t"
        "v_exp_f32 v116, v116\n\t"
        "v_smfmac_f32_16x16x64_bf16 v[128:131], v[144:147], v[104:111], v148\n\t"
        "v_exp_f32 v117, v117\n\t"
        "v_mfma_f32_32x32x16_bf16 v[96:111], v[168:171], v[80:83], 0\n\t"
        "v_exp_f32 v118, v118\n\t"
        "v_exp_f32 v119, v119\n\t"
        "v_exp_f32 v120, v120\n\t"
        "v_mfma_f32_32x32x16_bf16 v[96:111], v[172:175], v[84:87], v[96:111]\n\t"
        "v_exp_f32 v121, v121\n\t"
        "v_exp_f32 v122, v122\n\t"
        "v_exp_f32 v123, v123\n\t"
        "v_smfmac_f32_32x32x32_bf16 v[16:31], v[176:179], v[112:119], v148\n\t"
        "v_exp_f32 v124, v124\n\t"
        "v_exp_f32 v125, v125\n\t"
        "v_exp_f32 v126, v126\n\t"
        "v_smfmac_f32_16x16x64_bf16 v[132:135], v[144:147], v[112:119], v148\n\t"
        "buffer_load_dwordx4 v[160:163], v149, s[40:43], s44 offen offset:2048 sc1\n\t"
        "v_exp_f32 v127, v127\n\t"
        "v_exp_f32 v96, v96\n\t"
        "v_exp_f32 v97, v97\n\t"
        "v_smfmac_f32_32x32x32_bf16 v[16:31], v[180:183], v[120:127], v148\n\t"
        "v_exp_f32 v98, v98\n\t"
        "v_exp_f32 v99, v99\n\t"
        "v_exp_f32 v100, v100\n\t"
        "v_smfmac_f32_16x16x64_bf16 v[132:135], v[144:147], v[120:127], v148\n\t"
        "v_exp_f32 v101, v101\n\t"
        "v_mfma_f32_32x32x16_bf16 v[112:127], v[168:171], v[88:91], 0\n\t"
        "v_exp_f32 v102, v102\n\t"
        "v_exp_f32 v103, v103\n\t"
        "v_exp_f32 v104, v104\n\t"
        "v_mfma_f32_32x32x16_bf16 v[112:127], v[172:175], v[92:95], v[112:127]\n\t"
        "v_exp_f32 v105, v105\n\t"
        "v_exp_f32 v106, v106\n\t"
        "v_exp_f32 v107, v107\n\t"
        "v_smfmac_f32_32x32x32_bf16 v[32:47], v[176:179], v[96:103], v148\n\t"
        "v_exp_f32 v108, v108\n\t"
        "v_exp_f32 v109, v109\n\t"
        "v_exp_f32 v110, v110\n\t"
        "v_smfmac_f32_16x16x64_bf16 v[136:139], v[144:147], v[96:103], v148\n\t"
        "buffer_load_dwordx4 v[164:167], v149, s[40:43], s44 offen offset:3072 sc1\n\t"
        "v_exp_f32 v111, v111\n\t"
        "s_sub_u32 s45, s45, 1\n\t"
        "s_cmp_lg_u32 s45, 0\n\t"
        "s_cbranch_scc1 1b\n\t"
        "v_exp_f32 v112, v112\n\t"
        "v_exp_f32 v113, v113\n\t"
        "v_smfmac_f32_32x32x32_bf16 v[32:47], v[180:183], v[104:111], v148\n\t"
        "v_exp_f32 v114, v114\n\t"
        "v_exp_f32 v115, v115\n\t"
        "v_exp_f32 v116, v116\n\t"
        "v_smfmac_f32_16x16x64_bf16 v[136:139], v[144:147], v[104:111], v148\n\t"
        "v_exp_f32 v117, v117\n\t"
        "s_waitcnt vmcnt(8)\n\t"
        "v_mfma_f32_32x32x16_bf16 v[96:111], v[184:187], v[64:67], 0\n\t"
        "v_exp_f32 v118, v118\n\t"
        "v_exp_f32 v119, v119\n\t"
        "v_exp_f32 v120, v120\n\t"
        "v_mfma_f32_32x32x16_bf16 v[96:111], v[188:191], v[68:71], v[96:111]\n\t"
        "v_exp_f32 v121, v121\n\t"
        "v_exp_f32 v122, v122\n\t"
        "v_exp_f32 v123, v123\n\t"
        "v_smfmac_f32_32x32x32_bf16 v[48:63], v[176:179], v[112:119], v148\n\t"
        "v_exp_f32 v124, v124\n\t"
        "v_exp_f32 v125, v125\n\t"
        "v_exp_f32 v126, v126\n\t"
        "v_smfmac_f32_16x16x64_bf16 v[140:143], v[144:147], v[112:119], v148\n\t"
        "v_exp_f32 v127, v127\n\t"
        "v_exp_f32 v96, v96\n\t"
        "v_exp_f32 v97, v97\n\t"
        "v_smfmac_f32_32x32x32_bf16 v[48:63], v[180:183], v[120:127], v148\n\t"
        "v_exp_f32 v98, v98\n\t"
        "v_exp_f32 v99, v99\n\t"
        "v_exp_f32 v100, v100\n\t"
        "v_smfmac_f32_16x16x64_bf16 v[140:143], v[144:147], v[120:127], v148\n\t"
        "v_exp_f32 v101, v101\n\t"
        "v_mfma_f32_32x32x16_bf16 v[112:127], v[184:187], v[72:75], 0\n\t"
        "v_exp_f32 v102, v102\n\t"
        "v_exp_f32 v103, v103\n\t"
        "v_exp_f32 v104, v104\n\t"
        "v_mfma_f32_32x32x16_bf16 v[112:127], v[188:191], v[76:79], v[112:127]\n\t"
        "v_exp_f32 v105, v105\n\t"
        "v_exp_f32 v106, v106\n\t"
        "v_exp_f32 v107, v107\n\t"
        "v_smfmac_f32_32x32x32_bf16 v[0:15], v[192:195], v[96:103], v148\n\t"
        "v_exp_f32 v108, v108\n\t"
        "v_exp_f32 v109, v109\n\t"
        "v_exp_f32 v110, v110\n\t"
        "v_smfmac_f32_16x16x64_bf16 v[128:131], v[144:147], v[96:103], v148\n\t"
        "v_exp_f32 v111, v111\n\t"
        "v_exp_f32 v112, v112\n\t"
        "v_exp_f32 v113, v113\n\t"
        "v_smfmac_f32_32x32x32_bf16 v[0:15], v[196:199], v[104:111], v148\n\t"
        "v_exp_f32 v114, v114\n\t"
        "v_exp_f32 v115, v115\n\t"
        "v_exp_f32 v116, v116\n\t"
        "v_smfmac_f32_16x16x64_bf16 v[128:131], v[144:147], v[104:111], v148\n\t"
        "v_exp_f32 v117, v117\n\t"
        "s_waitcnt vmcnt(4)\n\t"
        "v_mfma_f32_32x32x16_bf16 v[96:111], v[200:203], v[64:67], 0\n\t"
        "v_exp_f32 v118, v118\n\t"
        "v_exp_f32 v119, v119\n\t"
        "v_exp_f32 v120, v120\n\t"
        "v_mfma_f32_32x32x16_bf16 v[96:111], v[204:207], v[68:71], v[96:111]\n\t"
        "v_exp_f32 v121, v121\n\t"
        "v_exp_f32 v122, v122\n\t"
        "v_exp_f32 v123, v123\n\t"
        "v_smfmac_f32_32x32x32_bf16 v[16:31], v[192:195], v[112:119], v148\n\t"
        "v_exp_f32 v124, v124\n\t"
        "v_exp_f32 v125, v125\n\t"
        "v_exp_f32 v126, v126\n\t"
        "v_smfmac_f32_16x16x64_bf16 v[132:135], v[144:147], v[112:119], v148\n\t"
        "v_exp_f32 v127, v127\n\t"
        "s_cmp_lg_u32 s47, 0\n\t"
        "s_cbranch_scc1 4f\n\t"
        "global_load_dwordx4 %[r0], v223, %[xb] offset:0\n\t"
        "global_load_dwordx4 %[r1], v223, %[xb] offset:32\n\t"
        "global_load_dwordx4 %[r2], v223, %[xb] offset:64\n\t"
        "global_load_dwordx4 %[r3], v223, %[xb] offset:96\n\t"
        "4:\n\t"
        "v_exp_f32 v96, v96\n\t"
        "v_exp_f32 v97, v97\n\t"
        "v_smfmac_f32_32x32x32_bf16 v[16:31], v[196:199], v[120:127], v148\n\t"
        "v_exp_f32 v98, v98\n\t"
        "v_exp_f32 v99, v99\n\t"
        "v_exp_f32 v100, v100\n\t"
        "v_smfmac_f32_16x16x64_bf16 v[132:135], v[144:147], v[120:127], v148\n\t"
        "v_exp_f32 v101, v101\n\t"
        "v_mfma_f32_32x32x16_bf16 v[112:127], v[200:203], v[72:75], 0\n\t"
        "v_exp_f32 v102, v102\n\t"
        "v_exp_f32 v103, v103\n\t"
        "v_exp_f32 v104, v104\n\t"
        "v_mfma_f32_32x32x16_bf16 v[112:127], v[204:207], v[76:79], v[112:127]\n\t"
        "v_exp_f32 v105, v105\n\t"
        "v_exp_f32 v106, v106\n\t"
        "v_exp_f32 v107, v107\n\t"
        "v_smfmac_f32_32x32x32_bf16 v[0:15], v[208:211], v[96:103], v148\n\t"
        "v_exp_f32 v108, v108\n\t"
        "v_exp_f32 v109, v109\n\t"
        "v_exp_f32 v110, v110\n\t"
        "v_smfmac_f32_16x16x64_bf16 v[128:131], v[144:147], v[96:103], v148\n\t"
        "v_exp_f32 v111, v111\n\t"
        "v_exp_f32 v112, v112\n\t"
        "v_exp_f32 v113, v113\n\t"
        "v_smfmac_f32_32x32x32_bf16 v[0:15], v[212:215], v[104:111], v148\n\t"
        "v_exp_f32 v114, v114\n\t"
        "v_exp_f32 v115, v115\n\t"
        "v_exp_f32 v116, v116\n\t"
        "v_smfmac_f32_16x16x64_bf16 v[128:131], v[144:147], v[104:111], v148\n\t"
        "v_exp_f32 v117, v117\n\t"
        "v_mfma_f32_32x32x16_bf16 v[96:111], v[184:187], v[80:83], 0\n\t"
        "v_exp_f32 v118, v118\n\t"
        "v_exp_f32 v119, v119\n\t"
        "v_exp_f32 v120, v120\n\t"
        "ds_write_b128 v216, v[0:3] offset:0\n\t"
        "v_mfma_f32_32x32x16_bf16 v[96:111], v[188:191], v[84:87], v[96:111]\n\t"
        "v_exp_f32 v121, v121\n\t"
        "ds_write_b128 v216, v[4:7] offset:1024\n\t"
        "v_exp_f32 v122, v122\n\t"
        "ds_write_b128 v216, v[8:11] offset:2048\n\t"
        "v_exp_f32 v123, v123\n\t"
        "ds_write_b128 v216, v[12:15] offset:3072\n\t"
        "v_smfmac_f32_32x32x32_bf16 v[16:31], v[208:211], v[112:119], v148\n\t"
        "v_exp_f32 v124, v124\n\t"
        "ds_write_b32 v218, v128 offset:0\n\t"
        "v_exp_f32 v125, v125\n\t"
        "v_exp_f32 v126, v126\n\t"
        "v_smfmac_f32_16x16x64_bf16 v[132:135], v[144:147], v[112:119], v148\n\t"
        "v_exp_f32 v127, v127\n\t"
        "v_exp_f32 v96, v96\n\t"
        "v_exp_f32 v97, v97\n\t"
        "v_smfmac_f32_32x32x32_bf16 v[16:31], v[212:215], v[120:127], v148\n\t"
        "v_exp_f32 v98, v98\n\t"
        "v_exp_f32 v99, v99\n\t"
        "v_exp_f32 v100, v100\n\t"
        "v_smfmac_f32_16x16x64_bf16 v[132:135], v[144:147], v[120:127], v148\n\t"
        "v_exp_f32 v101, v101\n\t"
        "v_mfma_f32_32x32x16_bf16 v[112:127], v[184:187], v[88:91], 0\n\t"
        "v_exp_f32 v102, v102\n\t"
        "v_exp_f32 v103, v103\n\t"
        "v_exp_f32 v104, v104\n\t"
        "ds_write_b128 v216, v[16:19] offset:4096\n\t"
        "v_mfma_f32_32x32x16_bf16 v[112:127], v[188:191], v[92:95], v[112:127]\n\t"
        "v_exp_f32 v105, v105\n\t"
        "ds_write_b128 v216, v[20:23] offset:5120\n\t"
        "v_exp_f32 v106, v106\n\t"
        "ds_write_b128 v216, v[24:27] offset:6144\n\t"
        "v_exp_f32 v107, v107\n\t"
        "ds_write_b128 v216, v[28:31] offset:7168\n\t"
        "v_smfmac_f32_32x32x32_bf16 v[32:47], v[192:195], v[96:103], v148\n\t"
        "v_exp_f32 v108, v108\n\t"
        "ds_write_b32 v218, v132 offset:256\n\t"
        "v_exp_f32 v109, v109\n\t"
        "ds_write_b32 v220, v222\n\t"
        "v_exp_f32 v110, v110\n\t"
        "v_smfmac_f32_16x16x64_bf16 v[136:139], v[144:147], v[96:103], v148\n\t"
        "v_exp_f32 v111, v111\n\t"
        "v_exp_f32 v112, v112\n\t"
        "v_exp_f32 v113, v113\n\t"
        "v_smfmac_f32_32x32x32_bf16 v[32:47], v[196:199], v[104:111], v148\n\t"
        "v_exp_f32 v114, v114\n\t"
        "v_exp_f32 v115, v115\n\t"
        "v_exp_f32 v116, v116\n\t"
        "v_smfmac_f32_16x16x64_bf16 v[136:139], v[144:147], v[104:111], v148\n\t"
        "v_exp_f32 v117, v117\n\t"
        "v_mfma_f32_32x32x16_bf16 v[96:111], v[200:203], v[80:83], 0\n\t"
        "v_exp_f32 v118, v118\n\t"
        "v_exp_f32 v119, v119\n\t"
        "v_exp_f32 v120, v120\n\t"
        "v_mfma_f32_32x32x16_bf16 v[96:111], v[204:207], v[84:87], v[96:111]\n\t"
        "v_exp_f32 v121, v121\n\t"
        "v_exp_f32 v122, v122\n\t"
        "v_exp_f32 v123, v123\n\t"
        "v_smfmac_f32_32x32x32_bf16 v[48:63], v[192:195], v[112:119], v148\n\t"
        "v_exp_f32 v124, v124\n\t"
        "v_exp_f32 v125, v125\n\t"
        "v_exp_f32 v126, v126\n\t"
        "v_smfmac_f32_16x16x64_bf16 v[140:143], v[144:147], v[112:119], v148\n\t"
        "v_exp_f32 v127, v127\n\t"
        "v_exp_f32 v96, v96\n\t"
        "v_exp_f32 v97, v97\n\t"
        "v_smfmac_f32_32x32x32_bf16 v[48:63], v[196:199], v[120:127], v148\n\t"
        "v_exp_f32 v98, v98\n\t"
        "v_exp_f32 v99, v99\n\t"
        "v_exp_f32 v100, v100\n\t"
        "v_smfmac_f32_16x16x64_bf16 v[140:143], v[144:147], v[120:127], v148\n\t"
        "v_exp_f32 v101, v101\n\t"
        "v_mfma_f32_32x32x16_bf16 v[112:127], v[200:203], v[88:91], 0\n\t"
        "v_exp_f32 v102, v102\n\t"
        "v_exp_f32 v103, v103\n\t"
        "v_exp_f32 v104, v104\n\t"
        "ds_read_b32 v151, v221\n\t"
        "v_mfma_f32_32x32x16_bf16 v[112:127], v[204:207], v[92:95], v[112:127]\n\t"
        "v_exp_f32 v105, v105\n\t"
        "v_exp_f32 v106, v106\n\t"
        "v_exp_f32 v107, v107\n\t"
        "v_smfmac_f32_32x32x32_bf16 v[32:47], v[208:211], v[96:103], v148\n\t"
        "v_exp_f32 v108, v108\n\t"
        "v_exp_f32 v109, v109\n\t"
        "v_exp_f32 v110, v110\n\t"
        "v_smfmac_f32_16x16x64_bf16 v[136:139], v[144:147], v[96:103], v148\n\t"
        "v_exp_f32 v111, v111\n\t"
        "s_waitcnt lgkmcnt(0)\n\t"
        "v_cmp_ne_u32 vcc, v222, v151\n\t"
        "s_cbranch_vccnz 40f\n\t"
        "41:\n\t"
        "v_exp_f32 v112, v112\n\t"
        "ds_read_b128 v[0:3], v217 offset:0\n\t"
        "v_exp_f32 v113, v113\n\t"
        "ds_read_b128 v[4:7], v217 offset:1024\n\t"
        "v_smfmac_f32_32x32x32_bf16 v[32:47], v[212:215], v[104:111], v148\n\t"
        "v_exp_f32 v114, v114\n\t"
        "ds_read_b128 v[8:11], v217 offset:2048\n\t"
        "v_exp_f32 v115, v115\n\t"
        "ds_read_b128 v[12:15], v217 offset:3072\n\t"
        "v_exp_f32 v116, v116\n\t"
        "ds_read_b32 v128, v219 offset:0\n\t"
        "v_smfmac_f32_16x16x64_bf16 v[136:139], v[144:147], v[104:111], v148\n\t"
        "v_exp_f32 v117, v117\n\t"
        "ds_read_b128 v[16:19], v217 offset:4096\n\t"
        "v_exp_f32 v118, v118\n\t"
        "ds_read_b128 v[20:23], v217 offset:5120\n\t"
        "v_exp_f32 v119, v119\n\t"
        "ds_read_b128 v[24:27], v217 offset:6144\n\t"
        "v_exp_f32 v120, v120\n\t"
        "ds_read_b128 v[28:31], v217 offset:7168\n\t"
        "v_exp_f32 v121, v121\n\t"
        "ds_read_b32 v132, v219 offset:256\n\t"
        "v_exp_f32 v122, v122\n\t"
        "v_exp_f32 v123, v123\n\t"
        "v_smfmac_f32_32x32x32_bf16 v[48:63], v[208:211], v[112:119], v148\n\t"
        "v_exp_f32 v124, v124\n\t"
        "v_exp_f32 v125, v125\n\t"
        "v_exp_f32 v126, v126\n\t"
        "v_smfmac_f32_16x16x64_bf16 v[140:143], v[144:147], v[112:119], v148\n\t"
        "v_exp_f32 v127, v127\n\t"
        "s_nop 1\n\t"
        "v_smfmac_f32_32x32x32_bf16 v[48:63], v[212:215], v[120:127], v148\n\t"
        "v_smfmac_f32_16x16x64_bf16 v[140:143], v[144:147], v[120:127], v148\n\t"
        "s_waitcnt vmcnt(0) lgkmcnt(0)\n\t"
        "s_nop 6\n\t"
        "s_branch 9f\n\t"
        "30:\n\t"
        "s_add_u32 s44, s44, 0x4000\n\t"
        "s_and_b32 s44, s44, 0x3ffff\n\t"
        "buffer_load_dwordx4 v[184:187], v149, s[40:43], s44 offen offset:0 sc1\n\t"
        "buffer_load_dwordx4 v[188:191], v149, s[40:43], s44 offen offset:1024 sc1\n\t"
        "buffer_load_dwordx4 v[192:195], v149, s[40:43], s44 offen offset:2048 sc1\n\t"
        "buffer_load_dwordx4 v[196:199], v149, s[40:43], s44 offen offset:3072 sc1\n\t"
        "buffer_load_dwordx4 v[200:203], v150, s[40:43], s44 offen offset:0 sc1\n\t"
        "buffer_load_dwordx4 v[204:207], v150, s[40:43], s44 offen offset:1024 sc1\n\t"
        "buffer_load_dwordx4 v[208:211], v150, s[40:43], s44 offen offset:2048 sc1\n\t"
        "buffer_load_dwordx4 v[212:215], v150, s[40:43], s44 offen offset:3072 sc1\n\t"
        "s_add_u32 s44, s44, 0x4000\n\t"
        "s_and_b32 s44, s44, 0x3ffff\n\t"
        "buffer_load_dwordx4 v[152:155], v149, s[40:43], s44 offen offset:0 sc1\n\t"
        "buffer_load_dwordx4 v[156:159], v149, s[40:43], s44 offen offset:1024 sc1\n\t"
        "buffer_load_dwordx4 v[160:163], v149, s[40:43], s44 offen offset:2048 sc1\n\t"
        "buffer_load_dwordx4 v[164:167], v149, s[40:43], s44 offen offset:3072 sc1\n\t"
        "s_mov_b32 s46, 1\n\t"
        "s_branch 31b\n\t"
        "32:\n\t"
        "s_add_u32 s44, s44, 0x4000\n\t"
        "s_and_b32 s44, s44, 0x3ffff\n\t"
        "buffer_load_dwordx4 v[184:187], v149, s[40:43], s44 offen offset:0 sc1\n\t"
        "buffer_load_dwordx4 v[188:191], v149, s[40:43], s44 offen offset:1024 sc1\n\t"
        "buffer_load_dwordx4 v[192:195], v149, s[40:43], s44 offen offset:2048 sc1\n\t"
        "buffer_load_dwordx4 v[196:199], v149, s[40:43], s44 offen offset:3072 sc1\n\t"
        "buffer_load_dwordx4 v[200:203], v150, s[40:43], s44 offen offset:0 sc1\n\t"
        "buffer_load_dwordx4 v[204:207], v150, s[40:43], s44 offen offset:1024 sc1\n\t"
        "buffer_load_dwordx4 v[208:211], v150, s[40:43], s44 offen offset:2048 sc1\n\t"
        "buffer_load_dwordx4 v[212:215], v150, s[40:43], s44 offen offset:3072 sc1\n\t"
        "s_add_u32 s44, s44, 0x4000\n\t"
        "s_and_b32 s44, s44, 0x3ffff\n\t"
        "buffer_load_dwordx4 v[152:155], v149, s[40:43], s44 offen offset:0 sc1\n\t"
        "buffer_load_dwordx4 v[156:159], v149, s[40:43], s44 offen offset:1024 sc1\n\t"
        "buffer_load_dwordx4 v[160:163], v149, s[40:43], s44 offen offset:2048 sc1\n\t"
        "buffer_load_dwordx4 v[164:167], v149, s[40:43], s44 offen offset:3072 sc1\n\t"
        "s_mov_b32 s46, 1\n\t"
        "s_branch 33b\n\t"
        "40:\n\t"
        "s_mov_b32 s45, 0x100000\n\t"
        "42:\n\t"
        "ds_read_b32 v151, v221\n\t"
        "s_sub_u32 s45, s45, 1\n\t"
        "s_waitcnt lgkmcnt(0)\n\t"
        "s_cbranch_scc1 41b\n\t"
        "v_cmp_ne_u32 vcc, v222, v151\n\t"
        "s_cbranch_vccnz 42b\n\t"
        "s_branch 41b\n\t"
        "9:\n\t"
        : "+{v[0:15]}"(o0), "+{v[16:31]}"(o1), "+{v[32:47]}"(o2), "+{v[48:63]}"(o3), "+{v[128:143]}"(lv), "+{s44}"(soff), "+{v[152:167]}"(kva), "+{v[168:183]}"(kvb), "+{v[184:191]}"(fv),
          [r0] "+v"(x1p[0]), [r1] "+v"(x1p[1]), [r2] "+v"(x1p[2]), [r3] "+v"(x1p[3]) STAMP_OUTS
        : "{v[64:71]}"(q[0]), "{v[72:79]}"(q[1]), "{v[80:87]}"(q[2]), "{v[88:95]}"(q[3]), "{v[144:147]}"(ones), "{v148}"(0xddddu), "{v149}"(kvoff), "{v150}"(kvoff + 4096),
          "{v[216:223]}"(cv), "{v224}"(lbase + L_W + 16), "{s[40:43]}"(srd), "{s47}"(L), [xb] "s"(x0)
        : "v96", "v97", "v98", "v99", "v100", "v101", "v102", "v103", "v104", "v105", "v106", "v107", "v108", "v109", "v110", "v111", "v112", "v113", "v114", "v115", "v116", "v117", "v118", "v119", "v120", "v121", "v122", "v123", "v124", "v125", "v126", "v127", "v151", "v192", "v193", "v194", "v195", "v196", "v197", "v198", "v199", "v200", "v201", "v202", "v203", "v204", "v205", "v206", "v207", "v208", "v209", "v210", "v211", "v212", "v213", "v214", "v215", "s45", "s46", "vcc", "scc", "memory");
      STAMP_PUT(6 + 16 * L);
      STAMPV(9 + 16 * L);
      PHASE_VARS;
      const unsigned dst = OFF_OF + L * (5 * MBY) + (tm * 4 + 2 * kh) * TS + (tn * 2 + hh) * 2048 + lane * 16;
      auto put = [&](int u, const f32x16& o) {
#pragma unroll
        for (int c = 0; c < 2; ++c) {
          u32x4 w;
#pragma unroll
          for (int e = 0; e < 4; ++e) w[e] = pack2(o[8 * c + 2 * e], o[8 * c + 2 * e + 1]);
          stg(rs, dst + u * TS + c * 1024, w);
        }
      };
      const float la = lv[8] + lv[0], lb = lv[12] + lv[4];
      o2 = (o2 + o0) * RCP(la); put(0, o2);
      SBAR();
      o3 = (o3 + o1) * RCP(lb); put(1, o3);
      SBAR();
      {
        const f32x16 t = o2 + o3;
        float sum = 0.f;
#pragma unroll
        for (int e = 0; e < 16; e += 4) sum += (t[e] + t[e + 1]) + (t[e + 2] + t[e + 3]);
        if (__any(!(fabsf(sum) < 1e37f) | !(fmaxf(la, lb) < 1e27f) | !(fminf(la, lb) > 1e-9f) | !(la + lb == la + lb))) {
          DRAIN();
          attn_safe(rs, kvoff - kh * 8192, q[2], ones, o2); put(0, o2);
          attn_safe(rs, kvoff - kh * 8192, q[3], ones, o3); put(1, o3);
        }
      }
    }
    STAMPV(10 + 16 * L);
    {
      f32x16 acc[2];
      u32x4 res[4];
      u32x2 wrc[8];
      STAMP_DECL;
      { PHASE_VARS;
      BAR_ALL();
      STAMPV(11 + 16 * L);
#pragma unroll
      for (int g = 0; g < 8; ++g) wrc[g] = *reinterpret_cast<const u32x2*>(smem + L_WR + g * 16 + hi * 8);
      if (L == 0) {
#pragma unroll
        for (int i = 0; i < 4; ++i) res[i] = fo_swap(x1p[i]);
      } else {
        const bool routed = (int)words[3] == tile * 32 + r32;
        const float mb1 = reinterpret_cast<const float*>(smem + L_AB)[256 + wid * 64 + r32], rd1 = reinterpret_cast<const float*>(smem + L_AB)[256 + wid * 64 + 32 + r32];
#pragma unroll
        for (int i = 0; i < 4; ++i) {
          const u32x4 pr = *reinterpret_cast<const u32x4*>(smem + L_X2P + (i * 2 + hi) * 16);
#pragma unroll
          for (int e = 0; e < 4; ++e) {
            const unsigned h2 = ln_pair(x1p[i][e], mb1, rd1);
            unsigned t = pack2(lo16(x1p[i][e]) + lo16(h2), hi16(x1p[i][e]) + hi16(h2));
            PIN(t);
            res[i][e] = routed ? pr[e] : t;
          }
        }
      }
#pragma unroll
      for (int g = 0; g < 8; ++g) PIN(wrc[g]);
#pragma unroll
      for (int i = 0; i < 4; ++i) PIN(res[i]);
      unsigned soff = tn * 4096;
      const unsigned a0 = lbase + wrow * 128 + ((wch ^ wsw) << 4) + 8 * (tp & 1), a1 = lbase + wrow * 128 + (((4 + wch) ^ wsw) << 4) + 8 * (tp & 1);
      asm volatile(
        "ds_read_b64_tr_b16 v[32:33], v193 offset:0\n\t"
        "ds_read_b64_tr_b16 v[34:35], v193 offset:1024\n\t"
        "ds_read_b64_tr_b16 v[36:37], v194 offset:0\n\t"
        "ds_read_b64_tr_b16 v[38:39], v194 offset:1024\n\t"
        "ds_read_b64_tr_b16 v[40:41], v193 offset:2048\n\t"
        "ds_read_b64_tr_b16 v[42:43], v193 offset:3072\n\t"
        "ds_read_b64_tr_b16 v[44:45], v194 offset:2048\n\t"
        "ds_read_b64_tr_b16 v[46:47], v194 offset:3072\n\t"
        "ds_read_b64_tr_b16 v[48:49], v193 offset:4096\n\t"
        "ds_read_b64_tr_b16 v[50:51], v193 offset:5120\n\t"
        "ds_read_b64_tr_b16 v[52:53], v194 offset:4096\n\t"
        "ds_read_b64_tr_b16 v[54:55], v194 offset:5120\n\t"
        "ds_read_b64_tr_b16 v[56:57], v193 offset:6144\n\t"
        "ds_read_b64_tr_b16 v[58:59], v193 offset:7168\n\t"
        "ds_read_b64_tr_b16 v[60:61], v194 offset:6144\n\t"
        "ds_read_b64_tr_b16 v[62:63], v194 offset:7168\n\t"
        "s_mov_b32 s45, 0x200000\n\t"
        "2:\n\t"
        "ds_read_b32 v200, v198\n\t"
        "s_sub_u32 s45, s45, 1\n\t"
        "s_waitcnt lgkmcnt(0)\n\t"
        "s_cbranch_scc1 3f\n\t"
        "v_cmp_ne_u32 vcc, v199, v200\n\t"
        "s_cbranch_vccnz 2b\n\t"
        "3:\n\t"
        "buffer_load_dwordx4 v[64:67], v192, s[40:43], s44 offen offset:0 sc1\n\t"
        "buffer_load_dwordx4 v[68:71], v192, s[40:43], s44 offen offset:1024 sc1\n\t"
        "buffer_load_dwordx4 v[72:75], v192, s[40:43], s44 offen offset:2048 sc1\n\t"
        "buffer_load_dwordx4 v[76:79], v192, s[40:43], s44 offen offset:3072 sc1\n\t"
        "s_add_u32 s44, s44, 0x1000\n\t"
        "s_and_b32 s44, s44, 0xffff\n\t"
        "buffer_load_dwordx4 v[80:83], v192, s[40:43], s44 offen offset:0 sc1\n\t"
        "buffer_load_dwordx4 v[84:87], v192, s[40:43], s44 offen offset:1024 sc1\n\t"
        "buffer_load_dwordx4 v[88:91], v192, s[40:43], s44 offen offset:2048 sc1\n\t"
        "buffer_load_dwordx4 v[92:95], v192, s[40:43], s44 offen offset:3072 sc1\n\t"
        "s_add_u32 s44, s44, 0x1000\n\t"
        "s_and_b32 s44, s44, 0xffff\n\t"
        "buffer_load_dwordx4 v[96:99], v192, s[40:43], s44 offen offset:0 sc1\n\t"
        "buffer_load_dwordx4 v[100:103], v192, s[40:43], s44 offen offset:1024 sc1\n\t"
        "buffer_load_dwordx4 v[104:107], v192, s[40:43], s44 offen offset:2048 sc1\n\t"
        "buffer_load_dwordx4 v[108:111], v192, s[40:43], s44 offen offset:3072 sc1\n\t"
        "s_waitcnt vmcnt(11) lgkmcnt(14)\n\t"
        "v_mfma_f32_32x32x16_bf16 v[0:15], v[32:35], v[64:67], 0\n\t"
        "ds_read_b64_tr_b16 v[32:33], v193 offset:8192\n\t"
        "ds_read_b64_tr_b16 v[34:35], v193 offset:9216\n\t"
        "s_waitcnt lgkmcnt(14)\n\t"
        "v_mfma_f32_32x32x16_bf16 v[16:31], v[36:39], v[64:67], 0\n\t"
        "ds_read_b64_tr_b16 v[36:37], v194 offset:8192\n\t"
        "ds_read_b64_tr_b16 v[38:39], v194 offset:9216\n\t"
        "s_add_u32 s44, s44, 0x1000\n\t"
        "s_and_b32 s44, s44, 0xffff\n\t"
        "buffer_load_dwordx4 v[112:115], v192, s[40:43], s44 offen offset:0 sc1\n\t"
        "s_waitcnt vmcnt(11) lgkmcnt(14)\n\t"
        "v_mfma_f32_32x32x16_bf16 v[0:15], v[40:43], v[68:71], v[0:15]\n\t"
        "ds_read_b64_tr_b16 v[40:41], v193 offset:10240\n\t"
        "ds_read_b64_tr_b16 v[42:43], v193 offset:11264\n\t"
        "s_waitcnt lgkmcnt(14)\n\t"
        "v_mfma_f32_32x32x16_bf16 v[16:31], v[44:47], v[68:71], v[16:31]\n\t"
        "ds_read_b64_tr_b16 v[44:45], v194 offset:10240\n\t"
        "ds_read_b64_tr_b16 v[46:47], v194 offset:11264\n\t"
        "buffer_load_dwordx4 v[116:119], v192, s[40:43], s44 offen offset:1024 sc1\n\t"
        "s_waitcnt vmcnt(11) lgkmcnt(14)\n\t"
        "v_mfma_f32_32x32x16_bf16 v[0:15], v[48:51], v[72:75], v[0:15]\n\t"
        "ds_read_b64_tr_b16 v[48:49], v193 offset:12288\n\t"
        "ds_read_b64_tr_b16 v[50:51], v193 offset:13312\n\t"
        "s_waitcnt lgkmcnt(14)\n\t"
        "v_mfma_f32_32x32x16_bf16 v[16:31], v[52:55], v[72:75], v[16:31]\n\t"
        "ds_read_b64_tr_b16 v[52:53], v194 offset:12288\n\t"
        "ds_read_b64_tr_b16 v[54:55], v194 offset:13312\n\t"
        "buffer_load_dwordx4 v[120:123], v192, s[40:43], s44 offen offset:2048 sc1\n\t"
        "s_waitcnt vmcnt(11) lgkmcnt(14)\n\t"
        "v_mfma_f32_32x32x16_bf16 v[0:15], v[56:59], v[76:79], v[0:15]\n\t"
        "ds_read_b64_tr_b16 v[56:57], v193 offset:14336\n\t"
        "ds_read_b64_tr_b16 v[58:59], v193 offset:15360\n\t"
        "s_waitcnt lgkmcnt(14)\n\t"
        "v_mfma_f32_32x32x16_bf16 v[16:31], v[60:63], v[76:79], v[16:31]\n\t"
        "ds_read_b64_tr_b16 v[60:61], v194 offset:14336\n\t"
        "ds_read_b64_tr_b16 v[62:63], v194 offset:15360\n\t"
        "buffer_load_dwordx4 v[124:127], v192, s[40:43], s44 offen offset:3072 sc1\n\t"
        "s_waitcnt vmcnt(11) lgkmcnt(14)\n\t"
        "v_mfma_f32_32x32x16_bf16 v[0:15], v[32:35], v[80:83], v[0:15]\n\t"
        "ds_read_b64_tr_b16 v[32:33], v193 offset:16384\n\t"
        "ds_read_b64_tr_b16 v[34:35], v193 offset:17408\n\t"
        "s_waitcnt lgkmcnt(14)\n\t"
        "v_mfma_f32_32x32x16_bf16 v[16:31], v[36:39], v[80:83], v[16:31]\n\t"
        "ds_read_b64_tr_b16 v[36:37], v194 offset:16384\n\t"
        "ds_read_b64_tr_b16 v[38:39], v194 offset:17408\n\t"
        "s_add_u32 s44, s44, 0x1000\n\t"
        "s_and_b32 s44, s44, 0xffff\n\t"
        "buffer_load_dwordx4 v[128:131], v192, s[40:43], s44 offen offset:0 sc1\n\t"
        "s_waitcnt vmcnt(11) lgkmcnt(14)\n\t"
        "v_mfma_f32_32x32x16_bf16 v[0:15], v[40:43], v[84:87], v[0:15]\n\t"
        "ds_read_b64_tr_b16 v[40:41], v193 offset:18432\n\t"
        "ds_read_b64_tr_b16 v[42:43], v193 offset:19456\n\t"
        "s_waitcnt lgkmcnt(14)\n\t"
        "v_mfma_f32_32x32x16_bf16 v[16:31], v[44:47], v[84:87], v[16:31]\n\t"
        "ds_read_b64_tr_b16 v[44:45], v194 offset:18432\n\t"
        "ds_read_b64_tr_b16 v[46:47], v194 offset:19456\n\t"
        "buffer_load_dwordx4 v[132:135], v192, s[40:43], s44 offen offset:1024 sc1\n\t"
        "s_waitcnt vmcnt(11) lgkmcnt(14)\n\t"
        "v_mfma_f32_32x32x16_bf16 v[0:15], v[48:51], v[88:91], v[0:15]\n\t"
        "ds_read_b64_tr_b16 v[48:49], v193 offset:20480\n\t"
        "ds_read_b64_tr_b16 v[50:51], v193 offset:21504\n\t"
        "s_waitcnt lgkmcnt(14)\n\t"
        "v_mfma_f32_32x32x16_bf16 v[16:31], v[52:55], v[88:91], v[16:31]\n\t"
        "ds_read_b64_tr_b16 v[52:53], v194 offset:20480\n\t"
        "ds_read_b64_tr_b16 v[54:55], v194 offset:21504\n\t"
        "buffer_load_dwordx4 v[136:139], v192, s[40:43], s44 offen offset:2048 sc1\n\t"
        "s_waitcnt vmcnt(11) lgkmcnt(14)\n\t"
        "v_mfma_f32_32x32x16_bf16 v[0:15], v[56:59], v[92:95], v[0:15]\n\t"
        "ds_read_b64_tr_b16 v[56:57], v193 offset:22528\n\t"
        "ds_read_b64_tr_b16 v[58:59], v193 offset:23552\n\t"
        "s_waitcnt lgkmcnt(14)\n\t"
        "v_mfma_f32_32x32x16_bf16 v[16:31], v[60:63], v[92:95], v[16:31]\n\t"
        "ds_read_b64_tr_b16 v[60:61], v194 offset:22528\n\t"
        "ds_read_b64_tr_b16 v[62:63], v194 offset:23552\n\t"
        "buffer_load_dwordx4 v[140:143], v192, s[40:43], s44 offen offset:3072 sc1\n\t"
        "s_waitcnt vmcnt(11) lgkmcnt(14)\n\t"
        "v_mfma_f32_32x32x16_bf16 v[0:15], v[32:35], v[96:99], v[0:15]\n\t"
        "ds_read_b64_tr_b16 v[32:33], v193 offset:24576\n\t"
        "ds_read_b64_tr_b16 v[34:35], v193 offset:25600\n\t"
        "s_waitcnt lgkmcnt(14)\n\t"
        "v_mfma_f32_32x32x16_bf16 v[16:31], v[36:39], v[96:99], v[16:31]\n\t"
        "ds_read_b64_tr_b16 v[36:37], v194 offset:24576\n\t"
        "ds_read_b64_tr_b16 v[38:39], v194 offset:25600\n\t"
        "s_add_u32 s44, s44, 0x1000\n\t"
        "s_and_b32 s44, s44, 0xffff\n\t"
        "buffer_load_dwordx4 v[144:147], v192, s[40:43], s44 offen offset:0 sc1\n\t"
        "s_waitcnt vmcnt(11) lgkmcnt(14)\n\t"
        "v_mfma_f32_32x32x16_bf16 v[0:15], v[40:43], v[100:103], v[0:15]\n\t"
        "ds_read_b64_tr_b16 v[40:41], v193 offset:26624\n\t"
        "ds_read_b64_tr_b16 v[42:43], v193 offset:27648\n\t"
        "s_waitcnt lgkmcnt(14)\n\t"
        "v_mfma_f32_32x32x16_bf16 v[16:31], v[44:47], v[100:103], v[16:31]\n\t"
        "ds_read_b64_tr_b16 v[44:45], v194 offset:26624\n\t"
        "ds_read_b64_tr_b16 v[46:47], v194 offset:27648\n\t"
        "buffer_load_dwordx4 v[148:151], v192, s[40:43], s44 offen offset:1024 sc1\n\t"
        "s_waitcnt vmcnt(11) lgkmcnt(14)\n\t"
        "v_mfma_f32_32x32x16_bf16 v[0:15], v[48:51], v[104:107], v[0:15]\n\t"
        "ds_read_b64_tr_b16 v[48:49], v193 offset:28672\n\t"
        "ds_read_b64_tr_b16 v[50:51], v193 offset:29696\n\t"
        "s_waitcnt lgkmcnt(14)\n\t"
        "v_mfma_f32_32x32x16_bf16 v[16:31], v[52:55], v[104:107], v[16:31]\n\t"
        "ds_read_b64_tr_b16 v[52:53], v194 offset:28672\n\t"
        "ds_read_b64_tr_b16 v[54:55], v194 offset:29696\n\t"
        "buffer_load_dwordx4 v[152:155], v192, s[40:43], s44 offen offset:2048 sc1\n\t"
        "s_waitcnt vmcnt(11) lgkmcnt(14)\n\t"
        "v_mfma_f32_32x32x16_bf16 v[0:15], v[56:59], v[108:111], v[0:15]\n\t"
        "ds_read_b64_tr_b16 v[56:57], v193 offset:30720\n\t"
        "ds_read_b64_tr_b16 v[58:59], v193 offset:31744\n\t"
        "s_waitcnt lgkmcnt(14)\n\t"
        "v_mfma_f32_32x32x16_bf16 v[16:31], v[60:63], v[108:111], v[16:31]\n\t"
        "ds_read_b64_tr_b16 v[60:61], v194 offset:30720\n\t"
        "ds_read_b64_tr_b16 v[62:63], v194 offset:31744\n\t"
        "buffer_load_dwordx4 v[156:159], v192, s[40:43], s44 offen offset:3072 sc1\n\t"
        "s_waitcnt vmcnt(11) lgkmcnt(14)\n\t"
        "v_mfma_f32_32x32x16_bf16 v[0:15], v[32:35], v[112:115], v[0:15]\n\t"
        "ds_read_b64_tr_b16 v[32:33], v193 offset:32768\n\t"
        "ds_read_b64_tr_b16 v[34:35], v193 offset:33792\n\t"
        "s_waitcnt lgkmcnt(14)\n\t"
        "v_mfma_f32_32x32x16_bf16 v[16:31], v[36:39], v[112:115], v[16:31]\n\t"
        "ds_read_b64_tr_b16 v[36:37], v194 offset:32768\n\t"
        "ds_read_b64_tr_b16 v[38:39], v194 offset:33792\n\t"
        "s_add_u32 s44, s44, 0x1000\n\t"
        "s_and_b32 s44, s44, 0xffff\n\t"
        "buffer_load_dwordx4 v[160:163], v192, s[40:43], s44 offen offset:0 sc1\n\t"
        "s_waitcnt vmcnt(11) lgkmcnt(14)\n\t"
        "v_mfma_f32_32x32x16_bf16 v[0:15], v[40:43], v[116:119], v[0:15]\n\t"
        "ds_read_b64_tr_b16 v[40:41], v193 offset:34816\n\t"
        "ds_read_b64_tr_b16 v[42:43], v193 offset:35840\n\t"
        "s_waitcnt lgkmcnt(14)\n\t"
        "v_mfma_f32_32x32x16_bf16 v[16:31], v[44:47], v[116:119], v[16:31]\n\t"
        "ds_read_b64_tr_b16 v[44:45], v194 offset:34816\n\t"
        "ds_read_b64_tr_b16 v[46:47], v194 offset:35840\n\t"
        "buffer_load_dwordx4 v[164:167], v192, s[40:43], s44 offen offset:1024 sc1\n\t"
        "s_waitcnt vmcnt(11) lgkmcnt(14)\n\t"
        "v_mfma_f32_32x32x16_bf16 v[0:15], v[48:51], v[120:123], v[0:15]\n\t"
        "ds_read_b64_tr_b16 v[48:49], v193 offset:36864\n\t"
        "ds_read_b64_tr_b16 v[50:51], v193 offset:37888\n\t"
        "s_waitcnt lgkmcnt(14)\n\t"
        "v_mfma_f32_32x32x16_bf16 v[16:31], v[52:55], v[120:123], v[16:31]\n\t"
        "ds_read_b64_tr_b16 v[52:53], v194 offset:36864\n\t"
        "ds_read_b64_tr_b16 v[54:55], v194 offset:37888\n\t"
        "buffer_load_dwordx4 v[168:171], v192, s[40:43], s44 offen offset:2048 sc1\n\t"
        "s_waitcnt vmcnt(11) lgkmcnt(14)\n\t"
        "v_mfma_f32_32x32x16_bf16 v[0:15], v[56:59], v[124:127], v[0:15]\n\t"
        "ds_read_b64_tr_b16 v[56:57], v193 offset:38912\n\t"
        "ds_read_b64_tr_b16 v[58:59], v193 offset:39936\n\t"
        "s_waitcnt lgkmcnt(14)\n\t"
        "v_mfma_f32_32x32x16_bf16 v[16:31], v[60:63], v[124:127], v[16:31]\n\t"
        "ds_read_b64_tr_b16 v[60:61], v194 offset:38912\n\t"
        "ds_read_b64_tr_b16 v[62:63], v194 offset:39936\n\t"
        "buffer_load_dwordx4 v[172:175], v192, s[40:43], s44 offen offset:3072 sc1\n\t"
        "s_waitcnt vmcnt(11) lgkmcnt(14)\n\t"
        "v_mfma_f32_32x32x16_bf16 v[0:15], v[32:35], v[128:131], v[0:15]\n\t"
        "ds_read_b64_tr_b16 v[32:33], v193 offset:40960\n\t"
        "ds_read_b64_tr_b16 v[34:35], v193 offset:41984\n\t"
        "s_waitcnt lgkmcnt(14)\n\t"
        "v_mfma_f32_32x32x16_bf16 v[16:31], v[36:39], v[128:131], v[16:31]\n\t"
        "ds_read_b64_tr_b16 v[36:37], v194 offset:40960\n\t"
        "ds_read_b64_tr_b16 v[38:39], v194 offset:41984\n\t"
        "s_add_u32 s44, s44, 0x1000\n\t"
        "s_and_b32 s44, s44, 0xffff\n\t"
        "buffer_load_dwordx4 v[176:179], v192, s[40:43], s44 offen offset:0 sc1\n\t"
        "s_waitcnt vmcnt(11) lgkmcnt(14)\n\t"
        "v_mfma_f32_32x32x16_bf16 v[0:15], v[40:43], v[132:135], v[0:15]\n\t"
        "ds_read_b64_tr_b16 v[40:41], v193 offset:43008\n\t"
        "ds_read_b64_tr_b16 v[42:43], v193 offset:44032\n\t"
        "s_waitcnt lgkmcnt(14)\n\t"
        "v_mfma_f32_32x32x16_bf16 v[16:31], v[44:47], v[132:135], v[16:31]\n\t"
        "ds_read_b64_tr_b16 v[44:45], v194 offset:43008\n\t"
        "ds_read_b64_tr_b16 v[46:47], v194 offset:44032\n\t"
        "buffer_load_dwordx4 v[180:183], v192, s[40:43], s44 offen offset:1024 sc1\n\t"
        "s_waitcnt vmcnt(11) lgkmcnt(14)\n\t"
        "v_mfma_f32_32x32x16_bf16 v[0:15], v[48:51], v[136:139], v[0:15]\n\t"
        "ds_read_b64_tr_b16 v[48:49], v193 offset:45056\n\t"
        "ds_read_b64_tr_b16 v[50:51], v193 offset:46080\n\t"
        "s_waitcnt lgkmcnt(14)\n\t"
        "v_mfma_f32_32x32x16_bf16 v[16:31], v[52:55], v[136:139], v[16:31]\n\t"
        "ds_read_b64_tr_b16 v[52:53], v194 offset:45056\n\t"
        "ds_read_b64_tr_b16 v[54:55], v194 offset:46080\n\t"
        "buffer_load_dwordx4 v[184:187], v192, s[40:43], s44 offen offset:2048 sc1\n\t"
        "s_waitcnt vmcnt(11) lgkmcnt(14)\n\t"
        "v_mfma_f32_32x32x16_bf16 v[0:15], v[56:59], v[140:143], v[0:15]\n\t"
        "ds_read_b64_tr_b16 v[56:57], v193 offset:47104\n\t"
        "ds_read_b64_tr_b16 v[58:59], v193 offset:48128\n\t"
        "s_waitcnt lgkmcnt(14)\n\t"
        "v_mfma_f32_32x32x16_bf16 v[16:31], v[60:63], v[140:143], v[16:31]\n\t"
        "ds_read_b64_tr_b16 v[60:61], v194 offset:47104\n\t"
        "ds_read_b64_tr_b16 v[62:63], v194 offset:48128\n\t"
        "buffer_load_dwordx4 v[188:191], v192, s[40:43], s44 offen offset:3072 sc1\n\t"
        "s_waitcnt vmcnt(11) lgkmcnt(14)\n\t"
        "v_mfma_f32_32x32x16_bf16 v[0:15], v[32:35], v[144:147], v[0:15]\n\t"
        "ds_read_b64_tr_b16 v[32:33], v193 offset:49152\n\t"
        "ds_read_b64_tr_b16 v[34:35], v193 offset:50176\n\t"
        "s_waitcnt lgkmcnt(14)\n\t"
        "v_mfma_f32_32x32x16_bf16 v[16:31], v[36:39], v[144:147], v[16:31]\n\t"
        "ds_read_b64_tr_b16 v[36:37], v194 offset:49152\n\t"
        "ds_read_b64_tr_b16 v[38:39], v194 offset:50176\n\t"
        "s_add_u32 s44, s44, 0x1000\n\t"
        "s_and_b32 s44, s44, 0xffff\n\t"
        "buffer_load_dwordx4 v[64:67], v192, s[40:43], s44 offen offset:0 sc1\n\t"
        "s_waitcnt vmcnt(11) lgkmcnt(14)\n\t"
        "v_mfma_f32_32x32x16_bf16 v[0:15], v[40:43], v[148:151], v[0:15]\n\t"
        "ds_read_b64_tr_b16 v[40:41], v193 offset:51200\n\t"
        "ds_read_b64_tr_b16 v[42:43], v193 offset:52224\n\t"
        "s_waitcnt lgkmcnt(14)\n\t"
        "v_mfma_f32_32x32x16_bf16 v[16:31], v[44:47], v[148:151], v[16:31]\n\t"
        "ds_read_b64_tr_b16 v[44:45], v194 offset:51200\n\t"
        "ds_read_b64_tr_b16 v[46:47], v194 offset:52224\n\t"
        "buffer_load_dwordx4 v[68:71], v192, s[40:43], s44 offen offset:1024 sc1\n\t"
        "s_waitcnt vmcnt(11) lgkmcnt(14)\n\t"
        "v_mfma_f32_32x32x16_bf16 v[0:15], v[48:51], v[152:155], v[0:15]\n\t"
        "ds_read_b64_tr_b16 v[48:49], v193 offset:53248\n\t"
        "ds_read_b64_tr_b16 v[50:51], v193 offset:54272\n\t"
        "s_waitcnt lgkmcnt(14)\n\t"
        "v_mfma_f32_32x32x16_bf16 v[16:31], v[52:55], v[152:155], v[16:31]\n\t"
        "ds_read_b64_tr_b16 v[52:53], v194 offset:53248\n\t"
        "ds_read_b64_tr_b16 v[54:55], v194 offset:54272\n\t"
        "buffer_load_dwordx4 v[72:75], v192, s[40:43], s44 offen offset:2048 sc1\n\t"
        "s_waitcnt vmcnt(11) lgkmcnt(14)\n\t"
        "v_mfma_f32_32x32x16_bf16 v[0:15], v[56:59], v[156:159], v[0:15]\n\t"
        "ds_read_b64_tr_b16 v[56:57], v193 offset:55296\n\t"
        "ds_read_b64_tr_b16 v[58:59], v193 offset:56320\n\t"
        "s_waitcnt lgkmcnt(14)\n\t"
        "v_mfma_f32_32x32x16_bf16 v[16:31], v[60:63], v[156:159], v[16:31]\n\t"
        "ds_read_b64_tr_b16 v[60:61], v194 offset:55296\n\t"
        "ds_read_b64_tr_b16 v[62:63], v194 offset:56320\n\t"
        "buffer_load_dwordx4 v[76:79], v192, s[40:43], s44 offen offset:3072 sc1\n\t"
        "s_waitcnt vmcnt(11) lgkmcnt(14)\n\t"
        "v_mfma_f32_32x32x16_bf16 v[0:15], v[32:35], v[160:163], v[0:15]\n\t"
        "ds_read_b64_tr_b16 v[32:33], v193 offset:57344\n\t"
        "ds_read_b64_tr_b16 v[34:35], v193 offset:58368\n\t"
        "s_waitcnt lgkmcnt(14)\n\t"
        "v_mfma_f32_32x32x16_bf16 v[16:31], v[36:39], v[160:163], v[16:31]\n\t"
        "ds_read_b64_tr_b16 v[36:37], v194 offset:57344\n\t"
        "ds_read_b64_tr_b16 v[38:39], v194 offset:58368\n\t"
        "s_add_u32 s44, s44, 0x1000\n\t"
        "s_and_b32 s44, s44, 0xffff\n\t"
        "buffer_load_dwordx4 v[80:83], v192, s[40:43], s44 offen offset:0 sc1\n\t"
        "s_waitcnt vmcnt(11) lgkmcnt(14)\n\t"
        "v_mfma_f32_32x32x16_bf16 v[0:15], v[40:43], v[164:167], v[0:15]\n\t"
        "ds_read_b64_tr_b16 v[40:41], v193 offset:59392\n\t"
        "ds_read_b64_tr_b16 v[42:43], v193 offset:60416\n\t"
        "s_waitcnt lgkmcnt(14)\n\t"
        "v_mfma_f32_32x32x16_bf16 v[16:31], v[44:47], v[164:167], v[16:31]\n\t"
        "ds_read_b64_tr_b16 v[44:45], v194 offset:59392\n\t"
        "ds_read_b64_tr_b16 v[46:47], v194 offset:60416\n\t"
        "buffer_load_dwordx4 v[84:87], v192, s[40:43], s44 offen offset:1024 sc1\n\t"
        "s_waitcnt vmcnt(11) lgkmcnt(14)\n\t"
        "v_mfma_f32_32x32x16_bf16 v[0:15], v[48:51], v[168:171], v[0:15]\n\t"
        "ds_read_b64_tr_b16 v[48:49], v193 offset:61440\n\t"
        "ds_read_b64_tr_b16 v[50:51], v193 offset:62464\n\t"
        "s_waitcnt lgkmcnt(14)\n\t"
        "v_mfma_f32_32x32x16_bf16 v[16:31], v[52:55], v[168:171], v[16:31]\n\t"
        "ds_read_b64_tr_b16 v[52:53], v194 offset:61440\n\t"
        "ds_read_b64_tr_b16 v[54:55], v194 offset:62464\n\t"
        "buffer_load_dwordx4 v[88:91], v192, s[40:43], s44 offen offset:2048 sc1\n\t"
        "s_waitcnt vmcnt(11) lgkmcnt(14)\n\t"
        "v_mfma_f32_32x32x16_bf16 v[0:15], v[56:59], v[172:175], v[0:15]\n\t"
        "ds_read_b64_tr_b16 v[56:57], v193 offset:63488\n\t"
        "ds_read_b64_tr_b16 v[58:59], v193 offset:64512\n\t"
        "s_waitcnt lgkmcnt(14)\n\t"
        "v_mfma_f32_32x32x16_bf16 v[16:31], v[60:63], v[172:175], v[16:31]\n\t"
        "ds_read_b64_tr_b16 v[60:61], v194 offset:63488\n\t"
        "ds_read_b64_tr_b16 v[62:63], v194 offset:64512\n\t"
        "buffer_load_dwordx4 v[92:95], v192, s[40:43], s44 offen offset:3072 sc1\n\t"
        "s_waitcnt vmcnt(11) lgkmcnt(14)\n\t"
        "v_mfma_f32_32x32x16_bf16 v[0:15], v[32:35], v[176:179], v[0:15]\n\t"
        "ds_read_b64_tr_b16 v[32:33], v195 offset:0\n\t"
        "ds_read_b64_tr_b16 v[34:35], v195 offset:1024\n\t"
        "s_waitcnt lgkmcnt(14)\n\t"
        "v_mfma_f32_32x32x16_bf16 v[16:31], v[36:39], v[176:179], v[16:31]\n\t"
        "ds_read_b64_tr_b16 v[36:37], v196 offset:0\n\t"
        "ds_read_b64_tr_b16 v[38:39], v196 offset:1024\n\t"
        "s_add_u32 s44, s44, 0x1000\n\t"
        "s_and_b32 s44, s44, 0xffff\n\t"
        "buffer_load_dwordx4 v[96:99], v192, s[40:43], s44 offen offset:0 sc1\n\t"
        "s_waitcnt vmcnt(11) lgkmcnt(14)\n\t"
        "v_mfma_f32_32x32x16_bf16 v[0:15], v[40:43], v[180:183], v[0:15]\n\t"
        "ds_read_b64_tr_b16 v[40:41], v195 offset:2048\n\t"
        "ds_read_b64_tr_b16 v[42:43], v195 offset:3072\n\t"
        "s_waitcnt lgkmcnt(14)\n\t"
        "v_mfma_f32_32x32x16_bf16 v[16:31], v[44:47], v[180:183], v[16:31]\n\t"
        "ds_read_b64_tr_b16 v[44:45], v196 offset:2048\n\t"
        "ds_read_b64_tr_b16 v[46:47], v196 offset:3072\n\t"
        "buffer_load_dwordx4 v[100:103], v192, s[40:43], s44 offen offset:1024 sc1\n\t"
        "s_waitcnt vmcnt(11) lgkmcnt(14)\n\t"
        "v_mfma_f32_32x32x16_bf16 v[0:15], v[48:51], v[184:187], v[0:15]\n\t"
        "ds_read_b64_tr_b16 v[48:49], v195 offset:4096\n\t"
        "ds_read_b64_tr_b16 v[50:51], v195 offset:5120\n\t"
        "s_waitcnt lgkmcnt(14)\n\t"
        "v_mfma_f32_32x32x16_bf16 v[16:31], v[52:55], v[184:187], v[16:31]\n\t"
        "ds_read_b64_tr_b16 v[52:53], v196 offset:4096\n\t"
        "ds_read_b64_tr_b16 v[54:55], v196 offset:5120\n\t"
        "buffer_load_dwordx4 v[104:107], v192, s[40:43], s44 offen offset:2048 sc1\n\t"
        "s_waitcnt vmcnt(11) lgkmcnt(14)\n\t"
        "v_mfma_f32_32x32x16_bf16 v[0:15], v[56:59], v[188:191], v[0:15]\n\t"
        "ds_read_b64_tr_b16 v[56:57], v195 offset:6144\n\t"
        "ds_read_b64_tr_b16 v[58:59], v195 offset:7168\n\t"
        "s_waitcnt lgkmcnt(14)\n\t"
        "v_mfma_f32_32x32x16_bf16 v[16:31], v[60:63], v[188:191], v[16:31]\n\t"
        "ds_read_b64_tr_b16 v[60:61], v196 offset:6144\n\t"
        "ds_read_b64_tr_b16 v[62:63], v196 offset:7168\n\t"
        "buffer_load_dwordx4 v[108:111], v192, s[40:43], s44 offen offset:3072 sc1\n\t"
        "s_waitcnt vmcnt(11) lgkmcnt(14)\n\t"
        "v_mfma_f32_32x32x16_bf16 v[0:15], v[32:35], v[64:67], v[0:15]\n\t"
        "ds_read_b64_tr_b16 v[32:33], v195 offset:8192\n\t"
        "ds_read_b64_tr_b16 v[34:35], v195 offset:9216\n\t"
        "s_waitcnt lgkmcnt(14)\n\t"
        "v_mfma_f32_32x32x16_bf16 v[16:31], v[36:39], v[64:67], v[16:31]\n\t"
        "ds_read_b64_tr_b16 v[36:37], v196 offset:8192\n\t"
        "ds_read_b64_tr_b16 v[38:39], v196 offset:9216\n\t"
        "s_add_u32 s44, s44, 0x1000\n\t"
        "s_and_b32 s44, s44, 0xffff\n\t"
        "buffer_load_dwordx4 v[112:115], v192, s[40:43], s44 offen offset:0 sc1\n\t"
        "s_waitcnt vmcnt(11) lgkmcnt(14)\n\t"
        "v_mfma_f32_32x32x16_bf16 v[0:15], v[40:43], v[68:71], v[0:15]\n\t"
        "ds_read_b64_tr_b16 v[40:41], v195 offset:10240\n\t"
        "ds_read_b64_tr_b16 v[42:43], v195 offset:11264\n\t"
        "s_waitcnt lgkmcnt(14)\n\t"
        "v_mfma_f32_32x32x16_bf16 v[16:31], v[44:47], v[68:71], v[16:31]\n\t"
        "ds_read_b64_tr_b16 v[44:45], v196 offset:10240\n\t"
        "ds_read_b64_tr_b16 v[46:47], v196 offset:11264\n\t"
        "buffer_load_dwordx4 v[116:119], v192, s[40:43], s44 offen offset:1024 sc1\n\t"
        "s_waitcnt vmcnt(11) lgkmcnt(14)\n\t"
        "v_mfma_f32_32x32x16_bf16 v[0:15], v[48:51], v[72:75], v[0:15]\n\t"
        "ds_read_b64_tr_b16 v[48:49], v195 offset:12288\n\t"
        "ds_read_b64_tr_b16 v[50:51], v195 offset:13312\n\t"
        "s_waitcnt lgkmcnt(14)\n\t"
        "v_mfma_f32_32x32x16_bf16 v[16:31], v[52:55], v[72:75], v[16:31]\n\t"
        "ds_read_b64_tr_b16 v[52:53], v196 offset:12288\n\t"
        "ds_read_b64_tr_b16 v[54:55], v196 offset:13312\n\t"
        "buffer_load_dwordx4 v[120:123], v192, s[40:43], s44 offen offset:2048 sc1\n\t"
        "s_waitcnt vmcnt(11) lgkmcnt(14)\n\t"
        "v_mfma_f32_32x32x16_bf16 v[0:15], v[56:59], v[76:79], v[0:15]\n\t"
        "ds_read_b64_tr_b16 v[56:57], v195 offset:14336\n\t"
        "ds_read_b64_tr_b16 v[58:59], v195 offset:15360\n\t"
        "s_waitcnt lgkmcnt(14)\n\t"
        "v_mfma_f32_32x32x16_bf16 v[16:31], v[60:63], v[76:79], v[16:31]\n\t"
        "ds_read_b64_tr_b16 v[60:61], v196 offset:14336\n\t"
        "ds_read_b64_tr_b16 v[62:63], v196 offset:15360\n\t"
        "buffer_load_dwordx4 v[124:127], v192, s[40:43], s44 offen offset:3072 sc1\n\t"
        "s_waitcnt vmcnt(11) lgkmcnt(14)\n\t"
        "v_mfma_f32_32x32x16_bf16 v[0:15], v[32:35], v[80:83], v[0:15]\n\t"
        "ds_read_b64_tr_b16 v[32:33], v195 offset:16384\n\t"
        "ds_read_b64_tr_b16 v[34:35], v195 offset:17408\n\t"
        "s_waitcnt lgkmcnt(14)\n\t"
        "v_mfma_f32_32x32x16_bf16 v[16:31], v[36:39], v[80:83], v[16:31]\n\t"
        "ds_read_b64_tr_b16 v[36:37], v196 offset:16384\n\t"
        "ds_read_b64_tr_b16 v[38:39], v196 offset:17408\n\t"
        "s_add_u32 s44, s44, 0x1000\n\t"
        "s_and_b32 s44, s44, 0xffff\n\t"
        "buffer_load_dwordx4 v[128:131], v192, s[40:43], s44 offen offset:0 sc1\n\t"
        "s_waitcnt vmcnt(11) lgkmcnt(14)\n\t"
        "v_mfma_f32_32x32x16_bf16 v[0:15], v[40:43], v[84:87], v[0:15]\n\t"
        "ds_read_b64_tr_b16 v[40:41], v195 offset:18432\n\t"
        "ds_read_b64_tr_b16 v[42:43], v195 offset:19456\n\t"
        "s_waitcnt lgkmcnt(14)\n\t"
        "v_mfma_f32_32x32x16_bf16 v[16:31], v[44:47], v[84:87], v[16:31]\n\t"
        "ds_read_b64_tr_b16 v[44:45], v196 offset:18432\n\t"
        "ds_read_b64_tr_b16 v[46:47], v196 offset:19456\n\t"
        "buffer_load_dwordx4 v[132:135], v192, s[40:43], s44 offen offset:1024 sc1\n\t"
        "s_waitcnt vmcnt(11) lgkmcnt(14)\n\t"
        "v_mfma_f32_32x32x16_bf16 v[0:15], v[48:51], v[88:91], v[0:15]\n\t"
        "ds_read_b64_tr_b16 v[48:49], v195 offset:20480\n\t"
        "ds_read_b64_tr_b16 v[50:51], v195 offset:21504\n\t"
        "s_waitcnt lgkmcnt(14)\n\t"
        "v_mfma_f32_32x32x16_bf16 v[16:31], v[52:55], v[88:91], v[16:31]\n\t"
        "ds_read_b64_tr_b16 v[52:53], v196 offset:20480\n\t"
        "ds_read_b64_tr_b16 v[54:55], v196 offset:21504\n\t"
        "buffer_load_dwordx4 v[136:139], v192, s[40:43], s44 offen offset:2048 sc1\n\t"
        "s_waitcnt vmcnt(11) lgkmcnt(14)\n\t"
        "v_mfma_f32_32x32x16_bf16 v[0:15], v[56:59], v[92:95], v[0:15]\n\t"
        "ds_read_b64_tr_b16 v[56:57], v195 offset:22528\n\t"
        "ds_read_b64_tr_b16 v[58:59], v195 offset:23552\n\t"
        "s_waitcnt lgkmcnt(14)\n\t"
        "v_mfma_f32_32x32x16_bf16 v[16:31], v[60:63], v[92:95], v[16:31]\n\t"
        "ds_read_b64_tr_b16 v[60:61], v196 offset:22528\n\t"
        "ds_read_b64_tr_b16 v[62:63], v196 offset:23552\n\t"
        "buffer_load_dwordx4 v[140:143], v192, s[40:43], s44 offen offset:3072 sc1\n\t"
        "s_waitcnt vmcnt(11) lgkmcnt(14)\n\t"
        "v_mfma_f32_32x32x16_bf16 v[0:15], v[32:35], v[96:99], v[0:15]\n\t"
        "ds_read_b64_tr_b16 v[32:33], v195 offset:24576\n\t"
        "ds_read_b64_tr_b16 v[34:35], v195 offset:25600\n\t"
        "s_waitcnt lgkmcnt(14)\n\t"
        "v_mfma_f32_32x32x16_bf16 v[16:31], v[36:39], v[96:99], v[16:31]\n\t"
        "ds_read_b64_tr_b16 v[36:37], v196 offset:24576\n\t"
        "ds_read_b64_tr_b16 v[38:39], v196 offset:25600\n\t"
        "s_add_u32 s44, s44, 0x1000\n\t"
        "s_and_b32 s44, s44, 0xffff\n\t"
        "buffer_load_dwordx4 v[144:147], v192, s[40:43], s44 offen offset:0 sc1\n\t"
        "s_waitcnt vmcnt(11) lgkmcnt(14)\n\t"
        "v_mfma_f32_32x32x16_bf16 v[0:15], v[40:43], v[100:103], v[0:15]\n\t"
        "ds_read_b64_tr_b16 v[40:41], v195 offset:26624\n\t"
        "ds_read_b64_tr_b16 v[42:43], v195 offset:27648\n\t"
        "s_waitcnt lgkmcnt(14)\n\t"
        "v_mfma_f32_32x32x16_bf16 v[16:31], v[44:47], v[100:103], v[16:31]\n\t"
        "ds_read_b64_tr_b16 v[44:45], v196 offset:26624\n\t"
        "ds_read_b64_tr_b16 v[46:47], v196 offset:27648\n\t"
        "buffer_load_dwordx4 v[148:151], v192, s[40:43], s44 offen offset:1024 sc1\n\t"
        "s_waitcnt vmcnt(11) lgkmcnt(14)\n\t"
        "v_mfma_f32_32x32x16_bf16 v[0:15], v[48:51], v[104:107], v[0:15]\n\t"
        "ds_read_b64_tr_b16 v[48:49], v195 offset:28672\n\t"
        "ds_read_b64_tr_b16 v[50:51], v195 offset:29696\n\t"
        "s_waitcnt lgkmcnt(14)\n\t"
        "v_mfma_f32_32x32x16_bf16 v[16:31], v[52:55], v[104:107], v[16:31]\n\t"
        "ds_read_b64_tr_b16 v[52:53], v196 offset:28672\n\t"
        "ds_read_b64_tr_b16 v[54:55], v196 offset:29696\n\t"
        "buffer_load_dwordx4 v[152:155], v192, s[40:43], s44 offen offset:2048 sc1\n\t"
        "s_waitcnt vmcnt(11) lgkmcnt(14)\n\t"
        "v_mfma_f32_32x32x16_bf16 v[0:15], v[56:59], v[108:111], v[0:15]\n\t"
        "ds_read_b64_tr_b16 v[56:57], v195 offset:30720\n\t"
        "ds_read_b64_tr_b16 v[58:59], v195 offset:31744\n\t"
        "s_waitcnt lgkmcnt(14)\n\t"
        "v_mfma_f32_32x32x16_bf16 v[16:31], v[60:63], v[108:111], v[16:31]\n\t"
        "ds_read_b64_tr_b16 v[60:61], v196 offset:30720\n\t"
        "ds_read_b64_tr_b16 v[62:63], v196 offset:31744\n\t"
        "buffer_load_dwordx4 v[156:159], v192, s[40:43], s44 offen offset:3072 sc1\n\t"
        "s_waitcnt vmcnt(11) lgkmcnt(14)\n\t"
        "v_mfma_f32_32x32x16_bf16 v[0:15], v[32:35], v[112:115], v[0:15]\n\t"
        "ds_read_b64_tr_b16 v[32:33], v195 offset:32768\n\t"
        "ds_read_b64_tr_b16 v[34:35], v195 offset:33792\n\t"
        "s_waitcnt lgkmcnt(14)\n\t"
        "v_mfma_f32_32x32x16_bf16 v[16:31], v[36:39], v[112:115], v[16:31]\n\t"
        "ds_read_b64_tr_b16 v[36:37], v196 offset:32768\n\t"
        "ds_read_b64_tr_b16 v[38:39], v196 offset:33792\n\t"
        "s_add_u32 s44, s44, 0x1000\n\t"
        "s_and_b32 s44, s44, 0xffff\n\t"
        "buffer_load_dwordx4 v[160:163], v192, s[40:43], s44 offen offset:0 sc1\n\t"
        "s_waitcnt vmcnt(11) lgkmcnt(14)\n\t"
        "v_mfma_f32_32x32x16_bf16 v[0:15], v[40:43], v[116:119], v[0:15]\n\t"
        "ds_read_b64_tr_b16 v[40:41], v195 offset:34816\n\t"
        "ds_read_b64_tr_b16 v[42:43], v195 offset:35840\n\t"
        "s_waitcnt lgkmcnt(14)\n\t"
        "v_mfma_f32_32x32x16_bf16 v[16:31], v[44:47], v[116:119], v[16:31]\n\t"
        "ds_read_b64_tr_b16 v[44:45], v196 offset:34816\n\t"
        "ds_read_b64_tr_b16 v[46:47], v196 offset:35840\n\t"
        "buffer_load_dwordx4 v[164:167], v192, s[40:43], s44 offen offset:1024 sc1\n\t"
        "s_waitcnt vmcnt(11) lgkmcnt(14)\n\t"
        "v_mfma_f32_32x32x16_bf16 v[0:15], v[48:51], v[120:123], v[0:15]\n\t"
        "ds_read_b64_tr_b16 v[48:49], v195 offset:36864\n\t"
        "ds_read_b64_tr_b16 v[50:51], v195 offset:37888\n\t"
        "s_waitcnt lgkmcnt(14)\n\t"
        "v_mfma_f32_32x32x16_bf16 v[16:31], v[52:55], v[120:123], v[16:31]\n\t"
        "ds_read_b64_tr_b16 v[52:53], v196 offset:36864\n\t"
        "ds_read_b64_tr_b16 v[54:55], v196 offset:37888\n\t"
        "buffer_load_dwordx4 v[168:171], v192, s[40:43], s44 offen offset:2048 sc1\n\t"
        "s_waitcnt vmcnt(11) lgkmcnt(14)\n\t"
        "v_mfma_f32_32x32x16_bf16 v[0:15], v[56:59], v[124:127], v[0:15]\n\t"
        "ds_read_b64_tr_b16 v[56:57], v195 offset:38912\n\t"
        "ds_read_b64_tr_b16 v[58:59], v195 offset:39936\n\t"
        "s_waitcnt lgkmcnt(14)\n\t"
        "v_mfma_f32_32x32x16_bf16 v[16:31], v[60:63], v[124:127], v[16:31]\n\t"
        "ds_read_b64_tr_b16 v[60:61], v196 offset:38912\n\t"
        "ds_read_b64_tr_b16 v[62:63], v196 offset:39936\n\t"
        "buffer_load_dwordx4 v[172:175], v192, s[40:43], s44 offen offset:3072 sc1\n\t"
        "s_waitcnt vmcnt(11) lgkmcnt(14)\n\t"
        "v_mfma_f32_32x32x16_bf16 v[0:15], v[32:35], v[128:131], v[0:15]\n\t"
        "ds_read_b64_tr_b16 v[32:33], v195 offset:40960\n\t"
        "ds_read_b64_tr_b16 v[34:35], v195 offset:41984\n\t"
        "s_waitcnt lgkmcnt(14)\n\t"
        "v_mfma_f32_32x32x16_bf16 v[16:31], v[36:39], v[128:131], v[16:31]\n\t"
        "ds_read_b64_tr_b16 v[36:37], v196 offset:40960\n\t"
        "ds_read_b64_tr_b16 v[38:39], v196 offset:41984\n\t"
        "s_add_u32 s44, s44, 0x1000\n\t"
        "s_and_b32 s44, s44, 0xffff\n\t"
        "buffer_load_dwordx4 v[176:179], v192, s[40:43], s44 offen offset:0 sc1\n\t"
        "s_waitcnt vmcnt(11) lgkmcnt(14)\n\t"
        "v_mfma_f32_32x32x16_bf16 v[0:15], v[40:43], v[132:135], v[0:15]\n\t"
        "ds_read_b64_tr_b16 v[40:41], v195 offset:43008\n\t"
        "ds_read_b64_tr_b16 v[42:43], v195 offset:44032\n\t"
        "s_waitcnt lgkmcnt(14)\n\t"
        "v_mfma_f32_32x32x16_bf16 v[16:31], v[44:47], v[132:135], v[16:31]\n\t"
        "ds_read_b64_tr_b16 v[44:45], v196 offset:43008\n\t"
        "ds_read_b64_tr_b16 v[46:47], v196 offset:44032\n\t"
        "buffer_load_dwordx4 v[180:183], v192, s[40:43], s44 offen offset:1024 sc1\n\t"
        "s_waitcnt vmcnt(11) lgkmcnt(14)\n\t"
        "v_mfma_f32_32x32x16_bf16 v[0:15], v[48:51], v[136:139], v[0:15]\n\t"
        "ds_read_b64_tr_b16 v[48:49], v195 offset:45056\n\t"
        "ds_read_b64_tr_b16 v[50:51], v195 offset:46080\n\t"
        "s_waitcnt lgkmcnt(14)\n\t"
        "v_mfma_f32_32x32x16_bf16 v[16:31], v[52:55], v[136:139], v[16:31]\n\t"
        "ds_read_b64_tr_b16 v[52:53], v196 offset:45056\n\t"
        "ds_read_b64_tr_b16 v[54:55], v196 offset:46080\n\t"
        "buffer_load_dwordx4 v[184:187], v192, s[40:43], s44 offen offset:2048 sc1\n\t"
        "s_waitcnt vmcnt(11) lgkmcnt(14)\n\t"
        "v_mfma_f32_32x32x16_bf16 v[0:15], v[56:59], v[140:143], v[0:15]\n\t"
        "ds_read_b64_tr_b16 v[56:57], v195 offset:47104\n\t"
        "ds_read_b64_tr_b16 v[58:59], v195 offset:48128\n\t"
        "s_waitcnt lgkmcnt(14)\n\t"
        "v_mfma_f32_32x32x16_bf16 v[16:31], v[60:63], v[140:143], v[16:31]\n\t"
        "ds_read_b64_tr_b16 v[60:61], v196 offset:47104\n\t"
        "ds_read_b64_tr_b16 v[62:63], v196 offset:48128\n\t"
        "buffer_load_dwordx4 v[188:191], v192, s[40:43], s44 offen offset:3072 sc1\n\t"
        "s_waitcnt vmcnt(11) lgkmcnt(14)\n\t"
        "v_mfma_f32_32x32x16_bf16 v[0:15], v[32:35], v[144:147], v[0:15]\n\t"
        "ds_read_b64_tr_b16 v[32:33], v195 offset:49152\n\t"
        "ds_read_b64_tr_b16 v[34:35], v195 offset:50176\n\t"
        "s_waitcnt lgkmcnt(14)\n\t"
        "v_mfma_f32_32x32x16_bf16 v[16:31], v[36:39], v[144:147], v[16:31]\n\t"
        "ds_read_b64_tr_b16 v[36:37], v196 offset:49152\n\t"
        "ds_read_b64_tr_b16 v[38:39], v196 offset:50176\n\t"
        "s_waitcnt vmcnt(10) lgkmcnt(14)\n\t"
        "v_mfma_f32_32x32x16_bf16 v[0:15], v[40:43], v[148:151], v[0:15]\n\t"
        "ds_read_b64_tr_b16 v[40:41], v195 offset:51200\n\t"
        "ds_read_b64_tr_b16 v[42:43], v195 offset:52224\n\t"
        "s_waitcnt lgkmcnt(14)\n\t"
        "v_mfma_f32_32x32x16_bf16 v[16:31], v[44:47], v[148:151], v[16:31]\n\t"
        "ds_read_b64_tr_b16 v[44:45], v196 offset:51200\n\t"
        "ds_read_b64_tr_b16 v[46:47], v196 offset:52224\n\t"
        "s_waitcnt vmcnt(9) lgkmcnt(14)\n\t"
        "v_mfma_f32_32x32x16_bf16 v[0:15], v[48:51], v[152:155], v[0:15]\n\t"
        "ds_read_b64_tr_b16 v[48:49], v195 offset:53248\n\t"
        "ds_read_b64_tr_b16 v[50:51], v195 offset:54272\n\t"
        "s_waitcnt lgkmcnt(14)\n\t"
        "v_mfma_f32_32x32x16_bf16 v[16:31], v[52:55], v[152:155], v[16:31]\n\t"
        "ds_read_b64_tr_b16 v[52:53], v196 offset:53248\n\t"
        "ds_read_b64_tr_b16 v[54:55], v196 offset:54272\n\t"
        "s_waitcnt vmcnt(8) lgkmcnt(14)\n\t"
        "v_mfma_f32_32x32x16_bf16 v[0:15], v[56:59], v[156:159], v[0:15]\n\t"
        "ds_read_b64_tr_b16 v[56:57], v195 offset:55296\n\t"
        "ds_read_b64_tr_b16 v[58:59], v195 offset:56320\n\t"
        "s_waitcnt lgkmcnt(14)\n\t"
        "v_mfma_f32_32x32x16_bf16 v[16:31], v[60:63], v[156:159], v[16:31]\n\t"
        "ds_read_b64_tr_b16 v[60:61], v196 offset:55296\n\t"
        "ds_read_b64_tr_b16 v[62:63], v196 offset:56320\n\t"
        "s_waitcnt vmcnt(7) lgkmcnt(14)\n\t"
        "v_mfma_f32_32x32x16_bf16 v[0:15], v[32:35], v[160:163], v[0:15]\n\t"
        "s_barrier\n\t"
        "ds_read_b64_tr_b16 v[32:33], v195 offset:57344\n\t"
        "ds_read_b64_tr_b16 v[34:35], v195 offset:58368\n\t"
        "s_waitcnt lgkmcnt(14)\n\t"
        "v_mfma_f32_32x32x16_bf16 v[16:31], v[36:39], v[160:163], v[16:31]\n\t"
        "ds_read_b64_tr_b16 v[36:37], v196 offset:57344\n\t"
        "ds_read_b64_tr_b16 v[38:39], v196 offset:58368\n\t"
        "s_waitcnt vmcnt(6) lgkmcnt(14)\n\t"
        "v_mfma_f32_32x32x16_bf16 v[0:15], v[40:43], v[164:167], v[0:15]\n\t"
        "ds_read_b64_tr_b16 v[40:41], v195 offset:59392\n\t"
        "ds_read_b64_tr_b16 v[42:43], v195 offset:60416\n\t"
        "s_waitcnt lgkmcnt(14)\n\t"
        "v_mfma_f32_32x32x16_bf16 v[16:31], v[44:47], v[164:167], v[16:31]\n\t"
        "ds_read_b64_tr_b16 v[44:45], v196 offset:59392\n\t"
        "ds_read_b64_tr_b16 v[46:47], v196 offset:60416\n\t"
        "s_waitcnt vmcnt(5) lgkmcnt(14)\n\t"
        "v_mfma_f32_32x32x16_bf16 v[0:15], v[48:51], v[168:171], v[0:15]\n\t"
        "ds_read_b64_tr_b16 v[48:49], v195 offset:61440\n\t"
        "ds_read_b64_tr_b16 v[50:51], v195 offset:62464\n\t"
        "s_waitcnt lgkmcnt(14)\n\t"
        "v_mfma_f32_32x32x16_bf16 v[16:31], v[52:55], v[168:171], v[16:31]\n\t"
        "ds_read_b64_tr_b16 v[52:53], v196 offset:61440\n\t"
        "ds_read_b64_tr_b16 v[54:55], v196 offset:62464\n\t"
        "s_waitcnt vmcnt(4) lgkmcnt(14)\n\t"
        "v_mfma_f32_32x32x16_bf16 v[0:15], v[56:59], v[172:175], v[0:15]\n\t"
        "ds_read_b64_tr_b16 v[56:57], v195 offset:63488\n\t"
        "ds_read_b64_tr_b16 v[58:59], v195 offset:64512\n\t"
        "s_waitcnt lgkmcnt(14)\n\t"
        "v_mfma_f32_32x32x16_bf16 v[16:31], v[60:63], v[172:175], v[16:31]\n\t"
        "ds_read_b64_tr_b16 v[60:61], v196 offset:63488\n\t"
        "ds_read_b64_tr_b16 v[62:63], v196 offset:64512\n\t"
        "s_waitcnt vmcnt(3) lgkmcnt(14)\n\t"
        "v_mfma_f32_32x32x16_bf16 v[0:15], v[32:35], v[176:179], v[0:15]\n\t"
        "s_waitcnt lgkmcnt(12)\n\t"
        "v_mfma_f32_32x32x16_bf16 v[16:31], v[36:39], v[176:179], v[16:31]\n\t"
        "s_waitcnt vmcnt(2) lgkmcnt(10)\n\t"
        "v_mfma_f32_32x32x16_bf16 v[0:15], v[40:43], v[180:183], v[0:15]\n\t"
        "s_waitcnt lgkmcnt(8)\n\t"
        "v_mfma_f32_32x32x16_bf16 v[16:31], v[44:47], v[180:183], v[16:31]\n\t"
        "s_waitcnt vmcnt(1) lgkmcnt(6)\n\t"
        "v_mfma_f32_32x32x16_bf16 v[0:15], v[48:51], v[184:187], v[0:15]\n\t"
        "s_waitcnt lgkmcnt(4)\n\t"
        "v_mfma_f32_32x32x16_bf16 v[16:31], v[52:55], v[184:187], v[16:31]\n\t"
        "s_waitcnt vmcnt(0) lgkmcnt(2)\n\t"
        "v_mfma_f32_32x32x16_bf16 v[0:15], v[56:59], v[188:191], v[0:15]\n\t"
        "s_waitcnt lgkmcnt(0)\n\t"
        "v_mfma_f32_32x32x16_bf16 v[16:31], v[60:63], v[188:191], v[16:31]\n\t"
        "s_nop 11\n\t"
        : "={v[0:15]}"(acc[0]), "={v[16:31]}"(acc[1]), "+{s44}"(soff) STAMP_OUTS
        : "{v192}"(OFF_OF + L * (5 * MBY) + tile * TS + lane * 16), "{v193}"(a0), "{v194}"(a1), "{v195}"(a0 + 65536), "{v196}"(a1 + 65536),
          "{v198}"(lbase + L_W + 20), "{v199}"(L + 1), "{s[40:43]}"(srd)
        : "v32", "v33", "v34", "v35", "v36", "v37", "v38", "v39", "v40", "v41", "v42", "v43", "v44", "v45", "v46", "v47", "v48", "v49", "v50", "v51", "v52", "v53", "v54", "v55", "v56", "v57", "v58", "v59", "v60", "v61", "v62", "v63", "v64", "v65", "v66", "v67", "v68", "v69", "v70", "v71", "v72", "v73", "v74", "v75", "v76", "v77", "v78", "v79", "v80", "v81", "v82", "v83", "v84", "v85", "v86", "v87", "v88", "v89", "v90", "v91", "v92", "v93", "v94", "v95", "v96", "v97", "v98", "v99", "v100", "v101", "v102", "v103", "v104", "v105", "v106", "v107", "v108", "v109", "v110", "v111", "v112", "v113", "v114", "v115", "v116", "v117", "v118", "v119", "v120", "v121", "v122", "v123", "v124", "v125", "v126", "v127", "v128", "v129", "v130", "v131", "v132", "v133", "v134", "v135", "v136", "v137", "v138", "v139", "v140", "v141", "v142", "v143", "v144", "v145", "v146", "v147", "v148", "v149", "v150", "v151", "v152", "v153", "v154", "v155", "v156", "v157", "v158", "v159", "v160", "v161", "v162", "v163", "v164", "v165", "v166", "v167", "v168", "v169", "v170", "v171", "v172", "v173", "v174", "v175", "v176", "v177", "v178", "v179", "v180", "v181", "v182", "v183", "v184", "v185", "v186", "v187", "v188", "v189", "v190", "v191", "v200", "s45", "vcc", "scc", "memory");
      }
      PHASE_VARS;
      STAMP_PUT(12 + 16 * L);
      STAMPV(15 + 16 * L);
      float s = 0.f, sq = 0.f, d = 0.f;
      const unsigned xdst = (L ? OOB : OFF_X1 + tile * TS + tn * 4096) + lane * 16;
#pragma unroll
      for (int g = 0; g < 8; ++g) {
#pragma unroll
        for (int e = 0; e < 2; ++e) {
          const unsigned rr = res[g >> 1][2 * (g & 1) + e];
          const unsigned t = pack2(acc[g >> 2][4 * (g & 3) + 2 * e], acc[g >> 2][4 * (g & 3) + 2 * e + 1]);
          x1p[g >> 1][2 * (g & 1) + e] = pack2(lo16(rr) + lo16(t), hi16(rr) + hi16(t));
        }
        if (g & 1) { stg(rs, xdst + (g >> 1) * 1024, x1p[g >> 1]); SBAR(); }
      }
#pragma unroll
      for (int g = 0; g < 8; ++g)
#pragma unroll
        for (int e = 0; e < 2; ++e) {
          const unsigned o = x1p[g >> 1][2 * (g & 1) + e];
          s = dot2(o, 0x3f803f80u, s); sq = dot2(o, o, sq); d = dot2(o, wrc[g][e], d);
        }
      stg(rs, stats_dst(sx, tile, tn, lane), u32x4{nzf(half_swap_sum(s)), nzf(half_swap_sum(sq)), nzf(half_swap_sum(d)), 0u});
    }
    STAMPV(16 + 16 * L);
  }
  {
    PHASE_VARS;
    float S, Q, Dd, mean, var, mb1, rd1;
    __builtin_amdgcn_s_sleep(10);
    row_stats_sum(rs, OFF_SX + SX_LAYER, tile, lane, S, Q, Dd);
    STAMPV(33);
    row_stats_fin(smem, 2, tile, lane, S, Q, Dd, U2F(words[0]), x1p, mean, var, mb1, rd1);
    STAMPV(34);
    u32x4 xo[4];
#pragma unroll
    for (int i = 0; i < 4; ++i) {
#pragma unroll
      for (int e = 0; e < 4; ++e) {
        const unsigned h2 = ln_pair(x1p[i][e], mb1, rd1);
        xo[i][e] = pack2(lo16(x1p[i][e]) + lo16(h2), hi16(x1p[i][e]) + hi16(h2));
      }
      *reinterpret_cast<u32x4*>(smem + wid * 4096 + r32 * 128 + ((2 * i + hi) ^ (r32 & 7)) * 16) = fo_swap(xo[i]);
    }
    asm volatile("s_waitcnt lgkmcnt(0)" ::: "memory");
#pragma unroll
    for (int i = 0; i < 4; ++i) { xo[i] = *reinterpret_cast<const u32x4*>(smem + wid * 4096 + i * 1024 + (lane >> 3) * 128 + ((lane ^ (lane >> 3)) & 7) * 16); PIN(xo[i]); }
    u16* dst = out + (size_t)(tile * 32 + (lane >> 3)) * D_ + tn * 64 + (lane & 7) * 8;
    lds_wait_ge(lbase + L_W + 4 + 4 * (lane & 1), 2);
    const int rel = (int)words[3] - tile * 32 - (lane >> 3);
#pragma unroll
    for (int i = 0; i < 4; ++i)
      if (rel != 8 * i) st16(dst + (size_t)i * 8 * D_, xo[i]);
    STAMPV(35);
    STAMP_END;
  }
}

extern "C" void kernel_launch(void* const* d_in, const int* in_sizes, int n_in, void* d_out, int out_size,
                              void* d_ws, size_t ws_size, hipStream_t stream) {
  char* ws = (char*)d_ws;
  mega_kernel<<<256, 512, 0, stream>>>((const u16*)d_in[0], (const u16*)d_in[1], (const u16*)d_in[2], (const u16*)d_in[4], (const u16*)d_in[5],
                                       (const u16*)d_in[6], (const u16*)d_in[7], ws, (u16*)d_out);
}
```

```cpp
#include <hip/hip_runtime.h>
#include <hip/hip_bf16.h>
#include <stdint.h>

typedef unsigned short u16;
typedef short s16x4 __attribute__((ext_vector_type(4)));
typedef short s16x8 __attribute__((ext_vector_type(8)));
typedef float f32x16 __attribute__((ext_vector_type(16)));
typedef float f32x8 __attribute__((ext_vector_type(8)));
typedef float f32x4 __attribute__((ext_vector_type(4)));
typedef float f32x2 __attribute__((ext_vector_type(2)));
typedef unsigned int u32x16 __attribute__((ext_vector_type(16)));
typedef unsigned int u32x8 __attribute__((ext_vector_type(8)));
typedef unsigned int u32x4 __attribute__((ext_vector_type(4)));
typedef unsigned int u32x2 __attribute__((ext_vector_type(2)));
typedef __bf16 bf16x2 __attribute__((ext_vector_type(2)));
typedef __bf16 bf16x8 __attribute__((ext_vector_type(8)));
typedef __bf16 bf16x16 __attribute__((ext_vector_type(16)));

#define DI __device__ __forceinline__
#define LDS_PTR(T, p) ((__attribute__((address_space(3))) T*)(p))
#define BARRIER() asm volatile("s_barrier" ::: "memory")
#define BAR_LGKM() asm volatile("s_waitcnt lgkmcnt(0)\n\ts_barrier" ::: "memory")
#define BAR_ALL() asm volatile("s_waitcnt vmcnt(0) lgkmcnt(0)\n\ts_barrier" ::: "memory")
#define DRAIN() asm volatile("s_waitcnt vmcnt(0)" ::: "memory")
#define PACE() ({ asm volatile("s_sleep 1\n\ts_waitcnt vmcnt(3)" ::: "memory"); })
#define MFMA32(a, b, c) __builtin_amdgcn_mfma_f32_32x32x16_bf16(a, b, c, 0, 0, 0)
#define MFMA16(a, b, c) __builtin_amdgcn_mfma_f32_16x16x32_bf16(a, b, c, 0, 0, 0)
#define PIN(x) asm volatile("" : "+v"(x))
#define EX2(x) __builtin_amdgcn_exp2f(x)
#define RCP(x) __builtin_amdgcn_rcpf(x)
#define SBAR() __builtin_amdgcn_sched_barrier(0)

#define STAMP_INIT
#define STAMPV(i)
#define STAMP_END
#define STAMP_DECL
#define STAMP_OUTS
#define STAMP_PUT(i)
constexpr int D_ = 1024, F_ = 3584;

constexpr unsigned MBY = 1u << 20;
constexpr unsigned TS = 65536 + 4096, HS = 262144 + 4096;
constexpr unsigned OFF_HF = 0, OFF_X1 = 5 * MBY;
constexpr unsigned OFF_OF = 10 * MBY;
constexpr unsigned OFF_KV = 20 * MBY;
constexpr unsigned OFF_FL = 40 * MBY;
constexpr unsigned FL_EPOCH = OFF_FL;
constexpr unsigned FL_KV = OFF_FL + 4096;
constexpr unsigned FL_O = FL_KV + 65536;
constexpr unsigned FL_X = FL_O + 65536;
constexpr unsigned OFF_CS = 41 * MBY;
constexpr unsigned OFF_SX = 42 * MBY;
constexpr unsigned SX_ST = 0;
constexpr unsigned SX_ROW = 512 * 1024;
constexpr unsigned SX_CAND = SX_ROW + 32 * 1024;
constexpr unsigned SX_GP = SX_CAND + 32 * 1024;
constexpr unsigned SX_YP = SX_GP + 224 * 1024;
constexpr unsigned SX_LAYER = SX_YP + 224 * 1024;
constexpr unsigned SX_QP = 2 * SX_LAYER;
constexpr unsigned SX_BYTES = SX_QP + 512 * 1024;
static_assert(SX_BYTES <= 5 * 256 * 128 * 16, "sentinel fill");
constexpr unsigned WS_BYTES = 48 * MBY, OOB = 0x80000000u;
constexpr unsigned SENT = 0xFFFFFFFFu;

constexpr int NS = 9, DD = 3, SLOTB = 12288;
constexpr int L_HB = 110592, L_XB = L_HB + 2048;
constexpr int L_XCH = 122880;
constexpr int L_SL = L_XCH + 8192;
constexpr int L_CAND = L_XCH + 32768;
constexpr int L_AB = L_XCH + 34816;
constexpr int L_FULL = 159744, L_FREE = L_FULL + 512;
constexpr int L_CS = L_FREE + 1024, L_PATCH = L_CS + 768;
constexpr int L_GU = L_PATCH + 768, L_TAG = L_GU + 256, L_WR = L_TAG + 256;
constexpr int L_X2F = L_WR + 128, L_X2P = L_X2F + 128;
constexpr int L_W = L_X2P + 128;
constexpr int LDS_BYTES = 163840;
static_assert(NS * SLOTB <= L_HB && L_W + 128 <= LDS_BYTES && (NS + 1) * SLOTB <= L_XCH && L_AB + 2048 <= L_FULL, "LDS map");

DI int fresh_lane() { int l; asm volatile("v_mbcnt_lo_u32_b32 %0, -1, 0\n\tv_mbcnt_hi_u32_b32 %0, -1, %0" : "=v"(l)); return l; }
#define LANE_VARS const int lane = fresh_lane(), r32 = lane & 31, hi = lane >> 5, tq = (lane & 15) >> 2, tp = lane & 3, tblk = (lane >> 4) & 1, \
  wrow = 4 * hi + tq, wsw = 4 * ((wrow >> 1) & 1), wch = 2 * tblk + (tp >> 1), c24 = r32 < 24 ? r32 : 23; \
  (void)r32; (void)hi; (void)wsw; (void)wch; (void)c24
DI int fresh_s(int v) { asm volatile("" : "+s"(v)); return v; }
#define PHASE_VARS const int tm = fresh_s(tm_), tn = fresh_s(tn_), wid = fresh_s(wid_), tile = tm * 4 + wid, hh = wid & 1, kh = wid >> 1; (void)tile; (void)hh; (void)kh; LANE_VARS
DI float bf2f(u16 v) { return __uint_as_float(((unsigned)v) << 16); }
DI unsigned pack2(float lo, float hi) { f32x2 v = {lo, hi}; return __builtin_bit_cast(unsigned, __builtin_convertvector(v, bf16x2)); }
DI float rbf(float f) { return __uint_as_float(pack2(f, 0.f) << 16); }
DI float lo16(unsigned u) { return __uint_as_float(u << 16); }
DI float hi16(unsigned u) { return __uint_as_float(u & 0xffff0000u); }
DI float opq(float r) { asm("" : "+v"(r)); return r; }
DI float fadd(float a, float b) { return opq(a + b); }
DI float fmul(float a, float b) { return opq(a * b); }
DI float ffma(float a, float b, float c) { return opq(__builtin_fmaf(a, b, c)); }
DI float U2F(unsigned u) { return __uint_as_float(u); }
DI float half_swap_sum(float v) {
  auto r = __builtin_amdgcn_permlane32_swap(__float_as_uint(v), __float_as_uint(v), false, false);
  return fadd(U2F(r[0]), U2F(r[1]));
}
DI float half_swap_max(float v) {
  auto r = __builtin_amdgcn_permlane32_swap(__float_as_uint(v), __float_as_uint(v), false, false);
  return fmaxf(__uint_as_float(r[0]), __uint_as_float(r[1]));
}
template <int CTRL> DI unsigned dpp(unsigned v) { return (unsigned)__builtin_amdgcn_update_dpp(0, (int)v, CTRL, 0xf, 0xf, false); }
DI float row_swap_sum(float v) {
  auto r = __builtin_amdgcn_permlane16_swap(__float_as_uint(v), __float_as_uint(v), false, false);
  return fadd(U2F(r[0]), U2F(r[1]));
}
DI float half_sum(float v) {
  v = fadd(v, U2F(dpp<0xB1>(__float_as_uint(v)))); v = fadd(v, U2F(dpp<0x4E>(__float_as_uint(v))));
  v = fadd(v, U2F(dpp<0x141>(__float_as_uint(v)))); v = fadd(v, U2F(dpp<0x140>(__float_as_uint(v))));
  return row_swap_sum(v);
}
DI float wave_sum(float v) { return half_swap_sum(half_sum(v)); }
DI unsigned half_max_u32(unsigned v) {
  v = max(v, dpp<0xB1>(v)); v = max(v, dpp<0x4E>(v)); v = max(v, dpp<0x141>(v)); v = max(v, dpp<0x140>(v));
  auto r = __builtin_amdgcn_permlane16_swap(v, v, false, false);
  return max(r[0], r[1]);
}
DI unsigned wave_max_u32(unsigned v) {
  v = half_max_u32(v);
  auto r = __builtin_amdgcn_permlane32_swap(v, v, false, false);
  return max(r[0], r[1]);
}
DI u32x4 fo_swap(u32x4 v) {
  auto a = __builtin_amdgcn_permlane32_swap(v[0], v[2], false, false);
  auto b = __builtin_amdgcn_permlane32_swap(v[1], v[3], false, false);
  u32x4 o; o[0] = a[0]; o[1] = b[0]; o[2] = a[1]; o[3] = b[1];
  return o;
}
DI void glds16(const void* gsrc, unsigned lds_dst) {
  unsigned keep;
  asm volatile("s_mov_b32 %0, m0\n\ts_mov_b32 m0, %2\n\ts_nop 0\n\tglobal_load_lds_dwordx4 %1, off\n\ts_mov_b32 m0, %0"
               : "=&s"(keep) : "v"(gsrc), "s"(lds_dst) : "memory");
}
#define GL_ "global_load_lds_dwordx4 "
DI void glds_v4(const void* sb, const unsigned (&v)[4], unsigned m0v) {
  unsigned keep;
  asm volatile("s_mov_b32 %0, m0\n\ts_mov_b32 m0, %6\n\ts_nop 0\n\t" GL_ "%1, %5\n\t" GL_ "%2, %5 offset:1024\n\t" GL_ "%3, %5 offset:2048\n\t"
               GL_ "%4, %5 offset:3072\n\ts_mov_b32 m0, %0" : "=&s"(keep) : "v"(v[0]), "v"(v[1]), "v"(v[2]), "v"(v[3]), "s"(sb), "s"(m0v) : "memory");
}
#define GP_ "\n\ts_sleep %8\n\ts_waitcnt vmcnt(%7)\n\t"
template <int TH, int SL> DI void glds_v4t(const void* sb, const unsigned (&v)[4], unsigned m0v) {
  unsigned keep;
  asm volatile("s_mov_b32 %0, m0\n\ts_mov_b32 m0, %6\n\ts_nop 0\n\t" GL_ "%1, %5" GP_ GL_ "%2, %5 offset:1024" GP_ GL_ "%3, %5 offset:2048" GP_ GL_ "%4, %5 offset:3072" GP_ "s_mov_b32 m0, %0"
               : "=&s"(keep) : "v"(v[0]), "v"(v[1]), "v"(v[2]), "v"(v[3]), "s"(sb), "s"(m0v), "n"(TH), "n"(SL) : "memory");
}
DI void glds_v3(const void* sb, const unsigned (&v)[3], unsigned m0v) {
  unsigned keep;
  asm volatile("s_mov_b32 %0, m0\n\ts_mov_b32 m0, %5\n\ts_nop 0\n\t" GL_ "%1, %4\n\t" GL_ "%2, %4 offset:1024\n\t" GL_ "%3, %4 offset:2048\n\t"
               "s_mov_b32 m0, %0" : "=&s"(keep) : "v"(v[0]), "v"(v[1]), "v"(v[2]), "s"(sb), "s"(m0v) : "memory");
}
DI float dot2(unsigned a, unsigned b, float c) {
  return __builtin_amdgcn_fdot2_f32_bf16(__builtin_bit_cast(bf16x2, a), __builtin_bit_cast(bf16x2, b), c, false);
}
#define CODE_ADDR(sym, out) do { unsigned long long pc_; unsigned off_; \
  asm volatile("s_getpc_b64 %0\n1:\n\ts_mov_b32 %1, " #sym "-1b" : "=s"(pc_), "=s"(off_)); out = (const char*)(pc_ + (long long)(int)off_); } while (0)
DI void st16(void* p, u32x4 v) { asm volatile("global_store_dwordx4 %0, %1, off sc0 sc1\n\ts_nop 1" :: "v"(p), "v"(v) : "memory"); }
DI void ln_scalars(float mean, float var, float& mb, float& den) { mb = rbf(mean); den = rbf(__builtin_amdgcn_sqrtf(rbf(rbf(var) + 1e-5f))); }
DI unsigned ln_pair(unsigned v, float mb, float rd) {
  const unsigned c = pack2(lo16(v) - mb, hi16(v) - mb);
  return pack2(lo16(c) * rd, hi16(c) * rd);
}
DI unsigned ln_pair_s(unsigned v, float nmb, float rd) {
  const unsigned c = pack2(fadd(lo16(v), nmb), fadd(hi16(v), nmb));
  return pack2(fmul(lo16(c), rd), fmul(hi16(c), rd));
}
template <int N, int M> DI f32x4 sum4(const u32x4 (&v)[M]) {
  f32x4 t;
#pragma unroll
  for (int e = 0; e < 4; ++e) {
    t[e] = U2F(v[0][e]);
#pragma unroll
    for (int i = 1; i < N; ++i) t[e] = fadd(t[e], U2F(v[i][e]));
  }
  return t;
}
#define PERM_LO(x, y) __builtin_amdgcn_perm(y, x, 0x05040100u)
#define PERM_HI(x, y) __builtin_amdgcn_perm(y, x, 0x07060302u)
constexpr unsigned SPIN_CAP = 1u << 15;
DI unsigned nz(unsigned v) { return min(v, SENT - 1u); }
DI unsigned nzf(float v) { return nz(__float_as_uint(v)); }
DI unsigned mx4(unsigned m, const u32x4& v) { return max(max(max(m, v[0]), v[1]), max(v[2], v[3])); }
template <int N> DI bool none_sent(const u32x4 (&v)[N]) {
  unsigned m = 0u;
#pragma unroll
  for (int i = 0; i < N; ++i) m = mx4(m, v[i]);
  return m != SENT;
}
typedef __amdgpu_buffer_rsrc_t rsrc_t;
DI u32x4 ldg(rsrc_t rs, unsigned off) { return __builtin_amdgcn_raw_buffer_load_b128(rs, (int)off, 0, 16); }
DI void stg(rsrc_t rs, unsigned off, u32x4 v) { __builtin_amdgcn_raw_buffer_store_b128(v, rs, (int)off, 0, 16); }
DI void poll_pause(bool slow) { if (slow) __builtin_amdgcn_s_sleep(4); asm volatile("s_sleep 1" ::: "memory"); }
template <int N, class LD, class OKF> DI void poll_all(u32x4 (&out)[N], bool slow, LD ld, OKF okf) {
  for (unsigned spins = 0;; ++spins) {
    ld(out);
    SBAR();
    if (__all(okf(out)) || spins > SPIN_CAP) break;
    poll_pause(slow);
  }
}
DI void lds_set(unsigned addr, unsigned v) { asm volatile("s_waitcnt lgkmcnt(0)\n\tds_write_b32 %0, %1" :: "v"(addr), "v"(v) : "memory"); }
template <int SLEEP = 1> DI void lds_wait_ge(unsigned addr, unsigned need) {
  unsigned spin = 0x100000, v;
  asm volatile("1:\n\tds_read_b32 %0, %2\n\ts_sub_u32 %1, %1, 1\n\ts_waitcnt lgkmcnt(0)\n\ts_cbranch_scc1 2f\n\tv_cmp_gt_u32 vcc, %3, %0\n\ts_cbranch_vccz 2f\n\ts_sleep %4\n\ts_branch 1b\n2:"
               : "=&v"(v), "+s"(spin) : "v"(addr), "s"(need), "n"(SLEEP) : "vcc", "scc", "memory");
}
DI s16x8 tr2(const char* p, int second) {
  s16x4 lo = __builtin_amdgcn_ds_read_tr16_b64_v4i16(LDS_PTR(s16x4, p)), hi = __builtin_amdgcn_ds_read_tr16_b64_v4i16(LDS_PTR(s16x4, p + second));
  return __builtin_shufflevector(lo, hi, 0, 1, 2, 3, 4, 5, 6, 7);
}
DI u32x16 cat4(const u32x4& a, const u32x4& b, const u32x4& c, const u32x4& d) {
  const u32x8 x = __builtin_shufflevector(a, b, 0, 1, 2, 3, 4, 5, 6, 7), y = __builtin_shufflevector(c, d, 0, 1, 2, 3, 4, 5, 6, 7);
  return __builtin_shufflevector(x, y, 0, 1, 2, 3, 4, 5, 6, 7, 8, 9, 10, 11, 12, 13, 14, 15);
}

DI unsigned stats_dst(unsigned sx, int tile, int tn, int lane) { return lane < 32 ? sx + SX_ST + tile * 8192 + tn * 512 + lane * 16 : OOB; }
DI void row_stats_sum(rsrc_t rs, unsigned sx, int tile, int lane, float& S, float& Q, float& Dd) {
  const unsigned exb = sx + SX_ST + tile * 8192 + (lane & 31) * 16;
  u32x4 pv[8];
  poll_all<8>(pv, false, [&](u32x4 (&v)[8]) {
#pragma unroll
    for (int i = 0; i < 8; ++i) v[i] = ldg(rs, exb + ((lane >> 5) * 8 + i) * 512);
  }, [&](const u32x4 (&v)[8]) { return none_sent(v); });
  S = 0.f; Q = 0.f; Dd = 0.f;
#pragma unroll
  for (int i = 0; i < 8; ++i) { S += __uint_as_float(pv[i][0]); Q += __uint_as_float(pv[i][1]); Dd += __uint_as_float(pv[i][2]); }
}
DI void row_stats_fin(char* smem, int epoch, int tile, int lane, float S, float Q, float Dd, float wsum, const u32x4 (&x1p)[4],
                      float& mean, float& var, float& mb, float& rd) {
  S = half_swap_sum(S); Q = half_swap_sum(Q); Dd = half_swap_sum(Dd);
  mean = S * (1.0f / 1024.0f); var = fmaxf(Q * (1.0f / 1024.0f) - mean * mean, 0.f);
  float den;
  ln_scalars(mean, var, mb, den);
  rd = RCP(den);
  const unsigned rb = pack2((Dd - mb * wsum) * rd, 0.f) & 0xffffu;
  const unsigned mine = (((rb & 0x8000u) ? (~rb & 0xffffu) : (rb | 0x8000u)) << 16) | (unsigned)(2047 - (tile * 32 + (lane & 31)));
  const bool best = half_max_u32(mine) == mine;
  if (best) {
    char* c = smem + L_CAND + (tile & 3) * 144;
#pragma unroll
    for (int i = 0; i < 4; ++i) *reinterpret_cast<u32x4*>(c + (2 * i + (lane >> 5)) * 16) = x1p[i];
    *reinterpret_cast<u32x4*>(c + 128) = u32x4{mine, __float_as_uint(mb), __float_as_uint(rd), 0u};
  }
  lds_set((unsigned)(uintptr_t)smem + L_W + 32 + (tile & 3) * 4, epoch);
}

DI void attn_safe(rsrc_t rs, unsigned kvoff, const u32x8& q, const u32x4& ones, f32x16& o) {
  const s16x8 q0 = __builtin_bit_cast(s16x8, __builtin_shufflevector(q, q, 0, 1, 2, 3)), q1 = __builtin_bit_cast(s16x8, __builtin_shufflevector(q, q, 4, 5, 6, 7));
  f32x4 l = {0.f, 0.f, 0.f, 0.f};
#pragma unroll
  for (int e = 0; e < 16; ++e) o[e] = 0.f;
  float m = -INFINITY;
#pragma unroll 1
  for (int kt = 0; kt < 64; ++kt) {
    const unsigned p = kvoff + kt * 4096;
    const u32x4 k0 = ldg(rs, p), k1 = ldg(rs, p + 1024), v0 = ldg(rs, p + 2048), v1 = ldg(rs, p + 3072);
    f32x16 s = {};
    s = MFMA32(__builtin_bit_cast(s16x8, k0), q0, s);
    s = MFMA32(__builtin_bit_cast(s16x8, k1), q1, s);
    float mt = fmaxf(s[0], s[1]);
#pragma unroll
    for (int e = 2; e < 16; ++e) mt = fmaxf(mt, s[e]);
    const float mn = fmaxf(m, half_swap_max(mt));
    const float alpha = EX2(m - mn);
    m = mn;
#pragma unroll
    for (int e = 0; e < 16; ++e) { s[e] = EX2(s[e] - mn); o[e] *= alpha; }
    l[0] *= alpha;
    const bf16x16 pa = __builtin_bit_cast(bf16x16, __builtin_shufflevector(s, s, 0, 1, 2, 3, 4, 5, 6, 7));
    const bf16x16 pb = __builtin_bit_cast(bf16x16, __builtin_shufflevector(s, s, 8, 9, 10, 11, 12, 13, 14, 15));
    o = __builtin_amdgcn_smfmac_f32_32x32x32_bf16(__builtin_bit_cast(bf16x8, v0), pa, o, 0xdddd, 0, 0);
    l = __builtin_amdgcn_smfmac_f32_16x16x64_bf16(__builtin_bit_cast(bf16x8, ones), pa, l, 0xdddd, 0, 0);
    o = __builtin_amdgcn_smfmac_f32_32x32x32_bf16(__builtin_bit_cast(bf16x8, v1), pb, o, 0xdddd, 0, 0);
    l = __builtin_amdgcn_smfmac_f32_16x16x64_bf16(__builtin_bit_cast(bf16x8, ones), pb, l, 0xdddd, 0, 0);
  }
  o *= 1.0f / l[0];
}

extern "C" __global__ __launch_bounds__(512) void mega_kernel(const u16* __restrict__ x0, const u16* __restrict__ wqkv, const u16* __restrict__ wo,
                                                              const u16* __restrict__ w_r, const u16* __restrict__ wg, const u16* __restrict__ wu,
                                                              const u16* __restrict__ wd, char* ws, u16* __restrict__ out) {
  __shared__ __attribute__((aligned(16))) char smem[LDS_BYTES];
  const int wid = __builtin_amdgcn_readfirstlane(threadIdx.x >> 6);
  STAMP_INIT;
  STAMPV(0);
  const int bid = blockIdx.x, xcd = bid & 7, loc = bid >> 3;
  const int tm = 8 * (xcd >> 2) + (loc >> 2), tn = 4 * (xcd & 3) + (loc & 3);
  const unsigned lbase = (unsigned)(uintptr_t)smem;
  const rsrc_t rs = __builtin_amdgcn_make_buffer_rsrc(ws, 0, (int)WS_BYTES, 0x00020000);
  volatile __attribute__((address_space(3))) unsigned* words = LDS_PTR(volatile unsigned, smem + L_W);
  *reinterpret_cast<unsigned*>(smem + L_FULL + threadIdx.x * 4) = 0u;
  words[threadIdx.x & 31] = 0u;
  BAR_LGKM();
  const int lw = wid & 1;
  const char* wb = reinterpret_cast<const char*>(wqkv + (size_t)(16 * lw) * (3 * D_) + tn * 64);
  auto stage_lanes = [](unsigned (&wv)[3]) {
    const int lane = fresh_lane();
#pragma unroll
    for (int i = 0; i < 3; ++i) {
      const int li = lane + 64 * i, k = li / 24, cp = li % 24, c = cp ^ (4 * ((k >> 1) & 1));
      wv[i] = (unsigned)(k * (3 * D_ * 2) + (c >> 3) * (D_ * 2) + (c & 7) * 16 - i * 1024);
    }
  };
  auto stage = [&](const unsigned (&wv)[3], int s, int slot) {
    const char* p = wb + (size_t)((s + 2 * tn) & 31) * (32 * 3 * D_ * 2);
    glds_v3(p, wv, lbase + slot * SLOTB + lw * 6144);
    glds_v3(p + 8 * (3 * D_ * 2), wv, lbase + slot * SLOTB + lw * 6144 + 3072);
  };
  auto load_tag = [&]() {
    u32x2 t = __builtin_amdgcn_raw_buffer_load_b64(rs, FL_EPOCH, 0, 16);
    t[0] += 1u; t[1] += 0x9E3779B9u;
    PIN(t);
    return t;
  };
  auto exchange = [&](const u32x2& tag, unsigned line0, int me) {
    const int lane = fresh_lane();
    __builtin_amdgcn_raw_buffer_store_b64(tag, rs, lane == 0 ? line0 + me * 128 : OOB, 0, 16);
    __builtin_amdgcn_s_sleep(8);
    for (unsigned spins = 0;; ++spins) {
      const u32x2 v = __builtin_amdgcn_raw_buffer_load_b64(rs, line0 + (lane & 15) * 128, 0, 16);
      if (__all((v[0] == tag[0]) & (v[1] == tag[1])) || spins > SPIN_CAP) break;
      asm volatile("s_sleep 1" ::: "memory");
    }
  };
  if (wid >= 4) {
    auto wo_rows = [&](int g0, int n, int gs) {
      const int lane = fresh_lane();
      unsigned wov[4];
#pragma unroll
      for (int i = 0; i < 4; ++i) wov[i] = (unsigned)((8 * i + (lane >> 3)) * (D_ * 2) + ((lane & 7) ^ (4 * ((lane >> 4) & 1))) * 16 - i * 1024);
#pragma unroll 1
      for (int i = 0, k = (2 * (tm & 7)) % n, g; i < n && !0; ++i, k = k + 1 == n ? 0 : k + 1) g = g0 + gs * k, glds_v4t<3, 1>(reinterpret_cast<const char*>(wo) + (size_t)((64 * tn + 32 * g) & 1023) * (D_ * 2) + tn * 128, wov, lbase + g * 4096);
    };
    if (wid < 6) {
      const int lane = fresh_lane();
      unsigned wv[3];
      stage_lanes(wv);
      __builtin_amdgcn_s_setprio(3);
      const unsigned fra = lbase + L_FREE + (lane & 3) * 256, fla = lbase + L_FULL + lw * 256 + lane * 4;
      const rsrc_t rw = __builtin_amdgcn_make_buffer_rsrc(const_cast<char*>(wb), 0, 0 ? 0 : 6 * MBY - (16 * lw * 3 * D_ + tn * 64) * 2, 0x00020000);
#pragma unroll 1
      for (int L = 0; L < 2; ++L) {
        const int s0 = NS, lead = L ? 8 : NS - 1;
        int slot = 0;
        if (L == 0) {
#pragma unroll 1
          for (int i = 0, s = tm & 7; i < 2 * NS; ++i) {
            glds_v3(wb + (size_t)((s + 2 * tn) & 31) * (32 * 3 * D_ * 2) + (i & 1) * (8 * 3 * D_ * 2), wv, lbase + s * SLOTB + lw * 6144 + (i & 1) * 3072);
            asm volatile("s_waitcnt vmcnt(6)" ::: "memory");
            if (i & 1) s = s + 1 == NS ? 0 : s + 1;
          }
          asm volatile("s_waitcnt vmcnt(0)\n\tds_write_b32 %0, %1" :: "v"(fla), "v"(NS) : "memory");
          STAMPV(1);
        }
#pragma unroll 1
        for (int s = NS; s < 32; ++s) {
          lds_wait_ge(fra, 32 * L + s - lead);
          stage(wv, s, slot);
          slot = (slot == NS - 1) ? 0 : slot + 1;
          if (s >= s0 + DD) asm volatile("s_waitcnt vmcnt(%0)\n\tds_write_b32 %1, %2" :: "n"(DD * 6), "v"(fla), "v"(32 * L + s - DD + 1) : "memory");
        }
#pragma unroll
        for (int d = DD - 1; d >= 0; --d) asm volatile("s_waitcnt vmcnt(%0)\n\tds_write_b32 %1, %2" :: "n"(d * 6), "v"(fla), "v"(32 * L + 32 - d) : "memory");
        u32x2 tag = *reinterpret_cast<const u32x2*>(smem + L_TAG);
        PIN(tag);
        BAR_LGKM();
        if (lw == 0) {
          exchange(tag, FL_KV + (L * 16 + tn) * 2048, tm);
          lds_wait_ge(fra, 32 * L + 32);
          lds_set(lbase + L_W + 16, L + 1);
        } else lds_wait_ge<8>(lbase + L_W + 16, L + 1);
        __builtin_amdgcn_s_sleep(24);
        wo_rows(lw, 15, 2);
        u32x4 pre[NS * 6];
        if (L == 0) {
#pragma unroll
          for (int s = 0; s < NS; ++s) {
#pragma unroll
            for (int i = 0; i < 6; ++i)
              pre[s * 6 + i] = __builtin_amdgcn_raw_buffer_load_b128(rw, wv[i % 3], ((s + 2 * tn) & 31) * (32 * 3 * D_ * 2) + (i / 3) * (8 * 3 * D_ * 2) + (i % 3) * 1024, 0), PACE();
          }
        } else {
#pragma unroll
          for (int i = 0; i < NS * 6; ++i) asm volatile("" : "=v"(pre[i]));
        }
        BAR_ALL();
        if (lw == 0) {
          exchange(tag, FL_O + (L * 16 + tm) * 2048, tn); lds_set(lbase + L_W + 20, L + 1);
          __builtin_amdgcn_raw_buffer_store_b64(tag, rs, (bid == 0 && lane == 0) ? FL_EPOCH : OOB, 0, 16);
        }
        else wo_rows(30, 2, 1);
        BAR_ALL();
        if (L == 0) {
          char* d = smem + lw * 6144 + fresh_lane() * 16;
#pragma unroll
          for (int s = 0; s < NS; ++s) {
#pragma unroll
            for (int i = 0; i < 6; ++i) *reinterpret_cast<u32x4*>(d + s * SLOTB + i * 1024) = pre[s * 6 + i];
            lds_set(fla, 32 + s + 1);
          }
          if (lw == 0) { lds_wait_ge(fra, 33); exchange(tag, FL_X + (16 + tm) * 2048, tn); lds_set(lbase + L_W + 24, 2); }
          else lds_wait_ge<2>(lbase + L_W + 24, 2);
        }
      }
      STAMP_END;
      return;
    }
    const int a = wid - 6;
    const bool ffn = loc < 28;
    const int grp = xcd * 7 + (ffn ? loc >> 2 : 0), qa = (loc & 3) * 2 + a;
    const int kr = loc, cg = xcd;
    const unsigned wa = lbase + L_W;
    if (a) {
      const int lane = fresh_lane();
      unsigned t = __builtin_amdgcn_raw_buffer_load_b32(rs, 1 ? lane * MBY + MBY - 128 : OOB, 0, 16);
      if (1) {
        const char *c0, *c1;
        CODE_ADDR(mega_kernel, c0); CODE_ADDR(end_marker, c1);
        const long long o = (loc * 64 + lane) * 32;
        t |= *reinterpret_cast<const __attribute__((address_space(1))) unsigned*>((uintptr_t)(c0 + (o + 4 <= c1 - c0 ? o : 0)));
      }
      const u32x2 tag = load_tag();
      *reinterpret_cast<u32x2*>(smem + L_TAG) = tag;
      PIN(t);
      lds_wait_ge<0>(wa + 64 + 4 * (lane & 3), 2);
      exchange(tag, FL_X + tm * 2048, tn);
      lds_set(wa + 24, 1);
    } else {
      unsigned wv[3];
      stage_lanes(wv);
#pragma unroll 1
      for (int i = 0; i < 4 * 0; ++i) {
        glds_v3(reinterpret_cast<const char*>(wqkv + tn * 64) + (size_t)((4 * (NS + 2 * tn + 3 * (tm & 7)) + i) & 127) * (8 * 3 * D_ * 2), wv, lbase + NS * SLOTB);
        asm volatile("s_waitcnt vmcnt(6)" ::: "memory");
      }
      STAMPV(11);
      lds_wait_ge<2>(wa + 24, 1);
    }
    __builtin_amdgcn_s_sleep(16);
    auto transpose8 = [](u32x4* v) {
      u32x4 t[8];
#pragma unroll
      for (int j = 0; j < 8; ++j)
#pragma unroll
        for (int e = 0; e < 4; ++e) t[j][e] = (j & 1) ? PERM_HI(v[2 * e][j >> 1], v[2 * e + 1][j >> 1]) : PERM_LO(v[2 * e][j >> 1], v[2 * e + 1][j >> 1]);
#pragma unroll
      for (int j = 0; j < 8; ++j) { v[j] = t[j]; PIN(v[j]); }
    };
    u32x4 gf[32];
    u32x4 wdf[16];
    auto wait_go = [&](int L) {
      lds_wait_ge<8>(lbase + L_W + 16, L + 1);
      __builtin_amdgcn_s_sleep(24);
    };
    auto load_wd = [&](unsigned dv) {
      const rsrc_t rd = __builtin_amdgcn_make_buffer_rsrc(const_cast<u16*>(wd) + (size_t)grp * 64 * D_ + 128 * qa, 0, 0 ? 0 : 64 * D_ * 2 - 256 * qa, 0x00020000);
#pragma unroll
      for (int i = 0; i < 16; ++i) {
        wdf[i] = __builtin_amdgcn_raw_buffer_load_b128(rd, dv, (32 * (i >> 3) + (i & 7)) * (D_ * 2), 0);
        asm volatile("s_sleep 2\n\ts_waitcnt vmcnt(3)" ::: "memory");
      }
    };
    {
      LANE_VARS;
      const u32x4 sv = {SENT, SENT, SENT, SENT};
#pragma unroll
      for (int i = 0; i < 5; ++i) {
        const unsigned o = ((i * 256 + bid) * 128 + a * 64 + lane) * 16;
        stg(rs, o < SX_BYTES ? OFF_SX + o : OOB, sv);
      }
      if (a == 0) {
        const u32x4 w0 = *reinterpret_cast<const u32x4*>(w_r + lane * 8), w1 = *reinterpret_cast<const u32x4*>(w_r + 512 + lane * 8);
        float t = 0.f;
#pragma unroll
        for (int e = 0; e < 4; ++e) t = fadd(fadd(t, fadd(lo16(w0[e]), hi16(w0[e]))), fadd(lo16(w1[e]), hi16(w1[e])));
        words[0] = __float_as_uint(wave_sum(t));
        if ((lane >> 3) == (tn & 7)) *reinterpret_cast<u32x4*>(smem + L_WR + (lane & 7) * 16) = tn < 8 ? w0 : w1;
      }
      const u16* p = wqkv + (size_t)(64 * tm + 32 * a + hi) * (3 * D_) + (c24 >> 3) * D_ + tn * 64 + (c24 & 7) * 8;
      float cs[8] = {0.f, 0.f, 0.f, 0.f, 0.f, 0.f, 0.f, 0.f};
      u32x4 v[16];
#pragma unroll
      for (int i = 0; i < 16; ++i) v[i] = *reinterpret_cast<const u32x4*>(p + (size_t)i * (2 * 3 * D_));
      SBAR();
#pragma unroll
      for (int i = 0; i < 16; ++i)
#pragma unroll
        for (int e = 0; e < 4; ++e) { cs[2 * e] = fadd(cs[2 * e], lo16(v[i][e])); cs[2 * e + 1] = fadd(cs[2 * e + 1], hi16(v[i][e])); }
      u32x4 o;
#pragma unroll
      for (int e = 0; e < 4; ++e) { const float x = half_swap_sum(cs[e]), y = half_swap_sum(cs[4 + e]); o[e] = __float_as_uint(hi ? y : x); }
      stg(rs, r32 < 24 ? OFF_CS + (tn * 32 + tm * 2 + a) * 768 + r32 * 32 + hi * 16 : OOB, o);
    }
    {
      BAR_ALL();
      LANE_VARS;
      const char* gp = reinterpret_cast<const char*>(((lane & 8) ? wu : wg) + (size_t)(128 * qa + 16 * hi + 4 * tblk) * F_ + 64 * grp + (lane & 7) * 8);
      unsigned dv = (lane >> 4) * (8 * D_ * 2) + (lane & 15) * 16;
      PIN(gp); PIN(dv);
      unsigned sv[3];
#pragma unroll
      for (int i = 0; i < 3; ++i) { const int li = lane + 64 * i, k = li / 24, c = li % 24; sv[i] = (unsigned)(k * (3 * D_ * 2) + (c >> 3) * (D_ * 2) + (c & 7) * 16 - i * 1024); PIN(sv[i]); }
      wait_go(0);
#pragma unroll
      for (int i = 0; i < 32; ++i) {
        gf[i] = *reinterpret_cast<const u32x4*>(gp + (size_t)(0 ? 0 : 32 * (i >> 3) + 8 * ((i >> 2) & 1) + (i & 3)) * (F_ * 2));
        asm volatile("s_sleep 2\n\ts_waitcnt vmcnt(3)" ::: "memory");
      }
      load_wd(dv);
      STAMPV(7);
      BARRIER();
      __builtin_amdgcn_s_sleep(2);
#pragma unroll
      for (int g = 0; g < 4; ++g) glds_v3(wqkv + (size_t)(32 * kr + 8 * g) * (3 * D_) + (2 * cg + a) * 64, sv, lbase + L_SL + a * 12288 + g * 3072);
      transpose8(gf); transpose8(gf + 8); transpose8(gf + 16); transpose8(gf + 24);
      transpose8(wdf); transpose8(wdf + 8);
      {
        LANE_VARS;
        f32x4 t[2];
#pragma unroll
        for (int c = 0; c < 2; ++c) {
          u32x4 v[8];
#pragma unroll
          for (int i = 0; i < 8; ++i) v[i] = ldg(rs, OFF_CS + (tn * 32 + 16 * c + 2 * i + hi) * 768 + a * 384 + c24 * 16);
          SBAR();
          t[c] = sum4<8>(v);
          SBAR();
        }
#pragma unroll
        for (int e = 0; e < 4; ++e) t[0][e] = half_swap_sum(fadd(t[0][e], t[1][e]));
        if (lane < 24) *reinterpret_cast<f32x4*>(smem + L_CS + a * 384 + lane * 16) = t[0];
      }
      BAR_LGKM();
    }
#pragma unroll 1
    for (int L = fresh_s(0);; ++L) {
      const unsigned sx = OFF_SX + L * SX_LAYER;
      STAMPV(1 + 16 * L);
      LANE_VARS;
      if (0 && L == 0) { words[3] = 5000u; lds_set(wa + 4 + 4 * a, 1); } else {
      unsigned cdst = lane < 8 ? sx + SX_ROW + tm * 2048 + tn * 128 + lane * 16 : sx + SX_CAND + (tn * 16 + tm) * 128;
      cdst = (a == 0 && lane < 9) ? cdst : OOB;
      PIN(cdst);
      lds_wait_ge(wa + 32 + 4 * tp, L + 1);
      unsigned key;
      {
        const unsigned k4 = *reinterpret_cast<const unsigned*>(smem + L_CAND + tp * 144 + 128);
        key = max(k4, dpp<0xB1>(k4)); key = max(key, dpp<0x4E>(key));
        const u32x4 c = *reinterpret_cast<const u32x4*>(smem + L_CAND + (((2047 - (int)(key & 2047u)) >> 5) & 3) * 144 + (lane < 8 ? lane : 8) * 16);
        const u32x4 o = {nz(c[0]), nz(c[1]), nz(c[2]), nz(c[3])};
        stg(rs, cdst, o);
      }
      STAMPV(8 + 16 * L);
      __builtin_amdgcn_s_sleep(10);
      if (L == 0) __builtin_amdgcn_s_sleep(0);
      u32x4 v[2];
      const unsigned rpc = (tblk ? 4 * kr + tp : 16 * qa + (lane & 15)) * 16;
      poll_all<2>(v, L == 0, [&](u32x4 (&t)[2]) {
        t[0] = ldg(rs, sx + SX_CAND + (tn * 16 + (lane & 15)) * 128);
        t[1] = ldg(rs, sx + SX_ROW + ((2047 - (int)(key & 2047u)) >> 7) * 2048 + rpc);
        t[0][3] = key;
      }, [&](const u32x4 (&t)[2]) {
        const bool ok = mx4(0u, t[0]) != SENT;
        unsigned k = ok ? t[0][0] : 0u;
        k = max(k, dpp<0xB1>(k)); k = max(k, dpp<0x4E>(k)); k = max(k, dpp<0x141>(k)); k = max(k, dpp<0x140>(k));
        key = max(key, k);
        return ok & (mx4(0u, t[1]) != SENT) & (key == t[0][3]);
      });
      key = __builtin_amdgcn_readfirstlane(key);
      const int idx = 2047 - (int)(key & 2047u);
      const bool owner = (idx >> 7) == tm;
      STAMPV(2 + 16 * L);
      {
        const float mb = __uint_as_float(__builtin_amdgcn_readlane(v[0][1], idx >> 7)), rd = __uint_as_float(__builtin_amdgcn_readlane(v[0][2], idx >> 7));
        u32x4 hb;
#pragma unroll
        for (int e = 0; e < 4; ++e) hb[e] = ln_pair_s(v[1][e], -mb, rd);
        *reinterpret_cast<u32x4*>(smem + L_HB + a * 1024 + lane * 16) = hb;
        *reinterpret_cast<u32x4*>(smem + L_XB + a * 1024 + lane * 16) = v[1];
        words[3] = (unsigned)idx;
        if (L == 1) lds_set(wa + 4 + 4 * a, 2);
      }
      auto pick2 = [&](const f32x4 (&d)[8]) {
        u32x2 o;
#pragma unroll
        for (int e = 0; e < 2; ++e) {
          const float x = tblk ? d[2 + e][0] : d[e][0], y = tblk ? d[6 + e][0] : d[4 + e][0];
          o[e] = nzf(hi ? y : x);
        }
        return o;
      };
      if (ffn) {
        const f32x4 zero = {0.f, 0.f, 0.f, 0.f};
        const unsigned ppos = (lane & 15) * 32 + (lane >> 4) * 8;
        f32x4 d[8];
        {
          s16x8 hf[4];
#pragma unroll
          for (int t = 0; t < 4; ++t) hf[t] = *reinterpret_cast<const s16x8*>(smem + L_HB + a * 1024 + (lane >> 4) * 16 + t * 64);
          SBAR();
          __builtin_amdgcn_s_setprio(3);
#pragma unroll
          for (int t = 0; t < 32; ++t) d[t & 7] = MFMA16(hf[t >> 3], __builtin_bit_cast(s16x8, gf[t]), t < 8 ? zero : d[t & 7]);
          SBAR();
          __builtin_amdgcn_s_setprio(0);
          __builtin_amdgcn_raw_buffer_store_b64(pick2(d), rs, sx + SX_GP + (grp * 8 + qa) * 512 + ppos, 0, 16);
        }
        STAMPV(9 + 16 * L);
        __builtin_amdgcn_s_sleep(10);
        if (L == 0) __builtin_amdgcn_s_sleep(0);
        u32x2 gu[8];
        for (unsigned spins = 0;; ++spins) {
#pragma unroll
          for (int i = 0; i < 8; ++i) gu[i] = __builtin_amdgcn_raw_buffer_load_b64(rs, sx + SX_GP + (grp * 8 + i) * 512 + lane * 8, 0, 16);
          SBAR();
          unsigned m = 0u;
#pragma unroll
          for (int i = 0; i < 8; ++i) m = max(max(m, gu[i][0]), gu[i][1]);
          if (__all(m != SENT) || spins > SPIN_CAP) break;
          poll_pause(L == 0);
        }
        STAMPV(10 + 16 * L);
        float t0 = opq(U2F(gu[0][0])), t1 = opq(U2F(gu[0][1]));
#pragma unroll
        for (int i = 1; i < 8; ++i) { t0 = fadd(t0, U2F(gu[i][0])); t1 = fadd(t1, U2F(gu[i][1])); }
        auto sw = __builtin_amdgcn_permlane32_swap(__float_as_uint(t0), __float_as_uint(t1), false, false);
        const float gv = rbf(U2F(sw[0])), uv = rbf(U2F(sw[1]));
        reinterpret_cast<u16*>(smem + L_GU + a * 128)[2 * r32 + hi] = (u16)pack2(fmul(rbf(fmul(gv, RCP(fadd(1.0f, __expf(-gv))))), uv), 0.f);
        asm volatile("s_waitcnt lgkmcnt(0)" ::: "memory");
        const s16x8 a0 = *reinterpret_cast<const s16x8*>(smem + L_GU + a * 128 + (lane >> 4) * 16), a1 = *reinterpret_cast<const s16x8*>(smem + L_GU + a * 128 + 64 + (lane >> 4) * 16);
        SBAR();
        __builtin_amdgcn_s_setprio(3);
#pragma unroll
        for (int t = 0; t < 16; ++t) d[t & 7] = MFMA16(t < 8 ? a0 : a1, __builtin_bit_cast(s16x8, wdf[t]), t < 8 ? zero : d[t & 7]);
        SBAR();
        __builtin_amdgcn_s_setprio(0);
        __builtin_amdgcn_raw_buffer_store_b64(pick2(d), rs, sx + SX_YP + grp * 4096 + qa * 512 + ppos, 0, 16);
      }
      STAMPV(3 + 16 * L);
      if (L == 1 && (cg != (kr & 7) || a)) { STAMP_END; __builtin_amdgcn_endpgm(); }
      s16x8 wpf[12];
#pragma unroll
      for (int t = 0; t < 12; ++t) wpf[t] = tr2(smem + L_SL + a * 12288 + (16 * hi + 4 * tblk + tq) * 384 + t * 32 + tp * 8, 8 * 384);
#pragma unroll
      for (int t = 0; t < 12; ++t) PIN(wpf[t]);
      if (L == 0 && !owner) lds_set(wa + 4 + 4 * a, 1);
      __builtin_amdgcn_s_sleep(10 - 4);
      if (L == 0) __builtin_amdgcn_s_sleep(0);
      if (L == 0 && !ffn) __builtin_amdgcn_s_sleep(64);
      const unsigned rk = key >> 16;
      float rsig = rbf(RCP(fadd(1.0f, __expf(-lo16((rk & 0x8000u) ? (rk & 0x7fffu) : (~rk & 0xffffu))))));
      PIN(rsig);
      const int n = 4 * (lane & 7) + ((lane >> 3) & 3);
      const int fo = ((n >> 4) * 2 + ((n >> 2) & 1)) * 8 + ((n >> 3) & 1) * 4 + (n & 3);
      float xv = bf2f(reinterpret_cast<const u16*>(smem + L_XB + a * 1024 + 256)[fo]), hv = bf2f(reinterpret_cast<const u16*>(smem + L_HB + a * 1024 + 256)[fo]);
      PIN(xv); PIN(hv);
      u32x4 pv[7];
      poll_all<7>(pv, L == 0, [&](u32x4 (&t)[7]) {
#pragma unroll
        for (int i = 0; i < 7; ++i) t[i] = ldg(rs, sx + SX_YP + (8 * i + (lane >> 3)) * 4096 + kr * 128 + (lane & 7) * 16);
      }, [&](const u32x4 (&t)[7]) { return none_sent(t); });
      STAMPV(4 + 16 * L);
      float ys;
      {
        f32x4 t = sum4<7>(pv);
#pragma unroll
        for (int e = 0; e < 4; ++e) {
          t[e] = fadd(t[e], U2F(dpp<0x128>(__float_as_uint(t[e]))));
          t[e] = half_swap_sum(row_swap_sum(t[e]));
        }
        const float x = (lane & 8) ? t[1] : t[0], y = (lane & 8) ? t[3] : t[2];
        ys = tblk ? y : x;
      }
      const unsigned ob = pack2(fadd(xv, rbf(fadd(hv, rbf(fmul(rbf(ys), rsig))))), 0.f) & 0xffffu;
      if (L == 1) {
        if (lane < 32) out[(size_t)idx * D_ + kr * 32 + n] = (u16)ob;
        STAMPV(5 + 16 * L);
        STAMP_END;
        __builtin_amdgcn_endpgm();
      }
      reinterpret_cast<u16*>(smem + L_X2F + a * 64)[fo] = (u16)ob;
      asm volatile("s_waitcnt lgkmcnt(0)" ::: "memory");
      {
        const s16x8 xa = *reinterpret_cast<const s16x8*>(smem + L_X2F + a * 64 + (lane >> 4) * 16);
        const u32x4 xf = *reinterpret_cast<const u32x4*>(smem + L_X2F + a * 64 + tp * 16);
        f32x4 z[12];
        const f32x4 zero = {0.f, 0.f, 0.f, 0.f};
        SBAR();
        __builtin_amdgcn_s_setprio(3);
#pragma unroll
        for (int t = 0; t < 12; ++t) z[t] = MFMA16(xa, wpf[t], zero);
        SBAR();
        __builtin_amdgcn_s_setprio(0);
        const float x2 = lo16(ob), s1 = half_sum(x2), s2 = half_sum(fmul(x2, x2));
        u32x4 o;
#pragma unroll
        for (int e = 0; e < 4; ++e) {
          const unsigned sp = lane < 52 ? (e == 0 ? __float_as_uint(s1) : __float_as_uint(s2)) : xf[e];
          const unsigned x = __float_as_uint(tblk ? z[4 + e][0] : z[e][0]), y = tblk ? sp : __float_as_uint(z[8 + e][0]);
          o[e] = nz(hi ? y : x);
        }
        const unsigned pos = lane < 24 ? lane * 16 : lane < 48 ? lane * 16 + 128 : lane < 52 ? (lane & 1) * 512 + 384 : 400 + tp * 16;
        stg(rs, lane < 56 ? OFF_SX + SX_QP + ((2 * cg + a) * 32 + kr) * 1024 + pos : OOB, o);
      }
      STAMPV(5 + 16 * L);
      if (owner) {
        __builtin_amdgcn_s_sleep(10);
        const int c25 = r32 < 25 ? r32 : 24, pl = 24 * a + c24, col = 64 * (pl >> 4) + (pl & 15);
        float cs[4];
#pragma unroll
        for (int e = 0; e < 4; ++e) { cs[e] = reinterpret_cast<const float*>(smem + L_CS)[col + 16 * e]; PIN(cs[e]); }
        u32x4 qv[17];
        poll_all<17>(qv, true, [&](u32x4 (&t)[17]) {
#pragma unroll
          for (int i = 0; i < 16; ++i) t[i] = ldg(rs, OFF_SX + SX_QP + (tn * 32 + 2 * i + hi) * 1024 + a * 512 + c25 * 16);
          t[16] = ldg(rs, OFF_SX + SX_QP + (tn * 34 + hi) * 1024 + 400 + tp * 16);
        }, [&](const u32x4 (&t)[17]) { return none_sent(t); });
        *reinterpret_cast<u32x4*>(smem + L_X2P + hi * 64 + tp * 16) = qv[16];
        f32x4 t = sum4<16>(qv);
#pragma unroll
        for (int e = 0; e < 4; ++e) t[e] = half_swap_sum(t[e]);
        const float mean = fmul(U2F(__builtin_amdgcn_readlane(__float_as_uint(t[0]), 24)), 1.0f / 1024.0f);
        const float var = fmaxf(ffma(-mean, mean, fmul(U2F(__builtin_amdgcn_readlane(__float_as_uint(t[1]), 24)), 1.0f / 1024.0f)), 0.f);
        float mb, den;
        ln_scalars(mean, var, mb, den);
        const float rd = RCP(den);
        if (lane < 24) {
#pragma unroll
          for (int e = 0; e < 4; ++e) reinterpret_cast<float*>(smem + L_PATCH)[col + 16 * e] = fmul(ffma(-mb, cs[e], t[e]), rd);
        }
        lds_set(wa + 4 + 4 * a, L + 1);
      }
      STAMPV(6 + 16 * L);
      }
      BAR_ALL();
      unsigned dv = (lane >> 4) * (8 * D_ * 2) + (lane & 15) * 16;
      PIN(dv);
      wait_go(1);
      load_wd(dv);
      STAMPV(23);
      BARRIER();
      transpose8(wdf); transpose8(wdf + 8);
      BARRIER();
    }
  }
  const int tm_ = tm, tn_ = tn, wid_ = wid;
  {
    const int tid = threadIdx.x, lane = tid & 63, r32 = lane & 31, row = 2 * wid + (lane >> 5);
    char* sm = smem + L_XCH;
    u32x4 v[4];
    asm volatile("global_load_dwordx4 %0, %4, off\n\tglobal_load_dwordx4 %1, %4, off offset:512\n\tglobal_load_dwordx4 %2, %4, off offset:1024\n\tglobal_load_dwordx4 %3, %4, off offset:1536"
                 : "=&v"(v[0]), "=&v"(v[1]), "=&v"(v[2]), "=&v"(v[3]) : "v"(x0 + (size_t)(tm * 128 + tn * 8 + row) * D_ + r32 * 8) : "memory");
    int pass = fresh_s(0);
#pragma unroll 1
    for (;;) {
      asm volatile("s_cmp_eq_u32 %4, 0\n\ts_cbranch_scc1 1f\n\ts_waitcnt vmcnt(0)\n1:" : "+v"(v[0]), "+v"(v[1]), "+v"(v[2]), "+v"(v[3]) : "s"(pass) : "memory", "scc");
      float s = 0.f, s2 = 0.f;
#pragma unroll
      for (int i = 0; i < 4; ++i)
#pragma unroll
        for (int e = 0; e < 4; e += 2) { s = dot2(v[i][e], 0x3f803f80u, s); s2 = dot2(v[i][e + 1], 0x3f803f80u, s2); }
      const float mean = half_sum(s + s2) * (1.0f / 1024.0f);
      f32x2 m2 = {0.f, 0.f};
#pragma unroll
      for (int i = 0; i < 4; ++i)
#pragma unroll
        for (int e = 0; e < 4; ++e) { const f32x2 d = f32x2{lo16(v[i][e]), hi16(v[i][e])} - mean; m2 += d * d; }
      float mb, den;
      ln_scalars(mean, half_sum(m2[0] + m2[1]) * (1.0f / 1024.0f), mb, den);
      const float rd = 1.0f / den;
#pragma unroll
      for (int i = 0; i < 4; ++i) {
        char* p = sm + ((16 * i + (r32 >> 1)) * 2) * 144 + row * 16 + (r32 & 1) * 8;
        u32x2 lo = {ln_pair(v[i][0], mb, rd), ln_pair(v[i][1], mb, rd)}, hi = {ln_pair(v[i][2], mb, rd), ln_pair(v[i][3], mb, rd)};
        *reinterpret_cast<u32x2*>(p) = lo;
        *reinterpret_cast<u32x2*>(p + 144) = hi;
      }
      STAMPV(37);
      lds_set(lbase + L_W + 64 + 4 * wid, pass);
      lds_wait_ge<0>(lbase + L_W + 64 + 4 * (lane & 3), pass);
      u32x4 t[4];
#pragma unroll
      for (int i = 0; i < 4; ++i) t[i] = *reinterpret_cast<const u32x4*>(sm + (i * 32 + (tid >> 3)) * 144 + (tid & 7) * 16);
      SBAR();
      unsigned dst = OFF_HF + (tm * 4 + (tn >> 2)) * TS + (tn & 3) * 128 + (tid >> 3) * 512 + (tid & 7) * 16;
      PIN(dst);
      if (pass) {
#pragma unroll
        for (int i = 0; i < 4; ++i) stg(rs, dst + i * 16384, t[i]);
        break;
      }
#pragma unroll
      for (int i = 0; i < 4; ++i) PIN(t[i]);
      pass = fresh_s(1);
    }
    DRAIN();
    lds_set(lbase + L_W + 64 + 4 * wid, 2);
    STAMPV(38);
  }
  const u32x4 srd = {(unsigned)(uintptr_t)ws, (unsigned)((uintptr_t)ws >> 32) & 0xffffu, WS_BYTES, 0x00020000u};
  u32x4 x1p[4];
  asm volatile("" : "=v"(x1p[0]), "=v"(x1p[1]), "=v"(x1p[2]), "=v"(x1p[3]));
#pragma unroll 1
  for (int L = 0; L < 2; ++L) {
    const unsigned sx = OFF_SX + L * SX_LAYER;
    u32x8 q[4];
    u32x16 kva, kvb;
    f32x16 oz[5];
    {
      f32x16 acc[6];
      u32x16 stA, stB, xr0, xr1, xr2, xr3;
      u32x4 stC;
      asm volatile("" : "={v[112:127]}"(stB), "={v[128:131]}"(stC));
      { PHASE_VARS;
      stB[8] = lane * 16 + (L ? OFF_X1 : OFF_HF) + tile * TS;
      stB[9] = lbase + wrow * 384 + (((4 * hh + wch) ^ wsw) << 4) + 8 * (tp & 1);
      stB[10] = lbase + wrow * 384 + (((4 * (hh ^ 1) + wch) ^ wsw) << 4) + 8 * (tp & 1);
      stB[13] = lbase + L_FULL + (lane & 1) * 256;
      stB[15] = lbase + L_FREE + wid * 256 + lane * 4;
      stC[0] = 32 * L; }
      unsigned soff, slot, need = 32 * L + 1;
      if (L == 0) {
        lds_wait_ge<0>(lbase + L_W + 24, 1);
        STAMPV(39);
        soff = tn * 4096;
        asm volatile(
          "buffer_load_dwordx4 v[132:135], v120, s[40:43], s44 offen offset:0 sc1\n\t"
          "buffer_load_dwordx4 v[136:139], v120, s[40:43], s44 offen offset:1024 sc1\n\t"
          "buffer_load_dwordx4 v[140:143], v120, s[40:43], s44 offen offset:2048 sc1\n\t"
          "buffer_load_dwordx4 v[144:147], v120, s[40:43], s44 offen offset:3072 sc1\n\t"
          "s_add_u32 s44, s44, 0x1000\n\t"
          "s_and_b32 s44, s44, 0xffff\n\t"
          "buffer_load_dwordx4 v[148:151], v120, s[40:43], s44 offen offset:0 sc1\n\t"
          "buffer_load_dwordx4 v[152:155], v120, s[40:43], s44 offen offset:1024 sc1\n\t"
          "buffer_load_dwordx4 v[156:159], v120, s[40:43], s44 offen offset:2048 sc1\n\t"
          "buffer_load_dwordx4 v[160:163], v120, s[40:43], s44 offen offset:3072 sc1\n\t"
          "s_add_u32 s44, s44, 0x1000\n\t"
          "s_and_b32 s44, s44, 0xffff\n\t"
          "buffer_load_dwordx4 v[164:167], v120, s[40:43], s44 offen offset:0 sc1\n\t"
          "buffer_load_dwordx4 v[168:171], v120, s[40:43], s44 offen offset:1024 sc1\n\t"
          "buffer_load_dwordx4 v[172:175], v120, s[40:43], s44 offen offset:2048 sc1\n\t"
          "buffer_load_dwordx4 v[176:179], v120, s[40:43], s44 offen offset:3072 sc1\n\t"
          "s_add_u32 s44, s44, 0x1000\n\t"
          "s_and_b32 s44, s44, 0xffff\n\t"
          "buffer_load_dwordx4 v[180:183], v120, s[40:43], s44 offen offset:0 sc1\n\t"
          "buffer_load_dwordx4 v[184:187], v120, s[40:43], s44 offen offset:1024 sc1\n\t"
          "buffer_load_dwordx4 v[188:191], v120, s[40:43], s44 offen offset:2048 sc1\n\t"
          "buffer_load_dwordx4 v[192:195], v120, s[40:43], s44 offen offset:3072 sc1\n\t"
          : "={v[132:147]}"(xr0), "={v[148:163]}"(xr1), "={v[164:179]}"(xr2), "={v[180:195]}"(xr3), "+{s44}"(soff) : "{v[112:127]}"(stB), "{s[40:43]}"(srd) : "scc", "memory");
      } else {
        xr0 = cat4(x1p[0], x1p[1], x1p[2], x1p[3]);
        asm volatile("" : "={v[148:163]}"(xr1), "={v[164:179]}"(xr2), "={v[180:195]}"(xr3));
      }
      STAMPV(1 + 16 * L);
      asm volatile(
        "v_mov_b32 v123, v121\n\t"
        "v_mov_b32 v124, v122\n\t"
        "s_mov_b64 vcc, -1\n\t"
        "s_cbranch_vccnz 10f\n\t"
        "11:\n\t"
        "s_mov_b32 s46, 12288\n\t"
        "s_add_u32 s47, s47, 1\n\t"
        "ds_read_b64_tr_b16 v[96:97], v123 offset:0\n\t"
        "ds_read_b64_tr_b16 v[98:99], v123 offset:3072\n\t"
        "ds_read_b64_tr_b16 v[100:101], v124 offset:0\n\t"
        "ds_read_b64_tr_b16 v[102:103], v124 offset:3072\n\t"
        "ds_read_b64_tr_b16 v[104:105], v123 offset:128\n\t"
        "ds_read_b64_tr_b16 v[106:107], v123 offset:3200\n\t"
        "ds_read_b64_tr_b16 v[108:109], v124 offset:128\n\t"
        "ds_read_b64_tr_b16 v[110:111], v124 offset:3200\n\t"
        "ds_read_b64_tr_b16 v[112:113], v123 offset:256\n\t"
        "ds_read_b64_tr_b16 v[114:115], v123 offset:3328\n\t"
        "ds_write_b32 v127, v128\n\t"
        "ds_read_b64_tr_b16 v[116:117], v124 offset:256\n\t"
        "ds_read_b64_tr_b16 v[118:119], v124 offset:3328\n\t"
        "s_waitcnt vmcnt(15) lgkmcnt(11)\n\t"
        "v_mfma_f32_32x32x16_bf16 v[0:15], v[96:99], v[132:135], 0\n\t"
        "ds_read_b32 v126, v125\n\t"
        "ds_read_b64_tr_b16 v[96:97], v123 offset:6144\n\t"
        "ds_read_b64_tr_b16 v[98:99], v123 offset:9216\n\t"
        "s_waitcnt lgkmcnt(12)\n\t"
        "v_mfma_f32_32x32x16_bf16 v[16:31], v[100:103], v[132:135], 0\n\t"
        "v_add_u32 v129, s46, v121\n\t"
        "ds_read_b64_tr_b16 v[100:101], v124 offset:6144\n\t"
        "ds_read_b64_tr_b16 v[102:103], v124 offset:9216\n\t"
        "s_waitcnt lgkmcnt(12)\n\t"
        "v_mfma_f32_32x32x16_bf16 v[32:47], v[104:107], v[132:135], 0\n\t"
        "ds_read_b64_tr_b16 v[104:105], v123 offset:6272\n\t"
        "ds_read_b64_tr_b16 v[106:107], v123 offset:9344\n\t"
        "s_waitcnt lgkmcnt(12)\n\t"
        "v_mfma_f32_32x32x16_bf16 v[48:63], v[108:111], v[132:135], 0\n\t"
        "v_add_u32 v130, s46, v122\n\t"
        "ds_read_b64_tr_b16 v[108:109], v124 offset:6272\n\t"
        "ds_read_b64_tr_b16 v[110:111], v124 offset:9344\n\t"
        "s_waitcnt lgkmcnt(12)\n\t"
        "v_mfma_f32_32x32x16_bf16 v[64:79], v[132:135], v[112:115], 0\n\t"
        "ds_read_b64_tr_b16 v[112:113], v123 offset:6400\n\t"
        "ds_read_b64_tr_b16 v[114:115], v123 offset:9472\n\t"
        "s_waitcnt lgkmcnt(10)\n\t"
        "v_mfma_f32_32x32x16_bf16 v[80:95], v[132:135], v[116:119], 0\n\t"
        "v_cmp_gt_u32 vcc, s47, v126\n\t"
        "ds_read_b64_tr_b16 v[116:117], v124 offset:6400\n\t"
        "ds_read_b64_tr_b16 v[118:119], v124 offset:9472\n\t"
        "s_waitcnt vmcnt(14) lgkmcnt(10)\n\t"
        "v_mfma_f32_32x32x16_bf16 v[0:15], v[96:99], v[136:139], v[0:15]\n\t"
        "s_cbranch_vccnz 12f\n\t"
        "13:\n\t"
        "ds_read_b64_tr_b16 v[96:97], v129 offset:0\n\t"
        "ds_read_b64_tr_b16 v[98:99], v129 offset:3072\n\t"
        "s_waitcnt lgkmcnt(10)\n\t"
        "v_mfma_f32_32x32x16_bf16 v[16:31], v[100:103], v[136:139], v[16:31]\n\t"
        "v_add_u32 v128, 1, v128\n\t"
        "ds_read_b64_tr_b16 v[100:101], v130 offset:0\n\t"
        "ds_read_b64_tr_b16 v[102:103], v130 offset:3072\n\t"
        "s_waitcnt lgkmcnt(10)\n\t"
        "v_mfma_f32_32x32x16_bf16 v[32:47], v[104:107], v[136:139], v[32:47]\n\t"
        "ds_read_b64_tr_b16 v[104:105], v129 offset:128\n\t"
        "ds_read_b64_tr_b16 v[106:107], v129 offset:3200\n\t"
        "s_waitcnt lgkmcnt(10)\n\t"
        "v_mfma_f32_32x32x16_bf16 v[48:63], v[108:111], v[136:139], v[48:63]\n\t"
        "s_add_u32 s46, s46, 12288\n\t"
        "s_add_u32 s47, s47, 1\n\t"
        "ds_read_b64_tr_b16 v[108:109], v130 offset:128\n\t"
        "ds_read_b64_tr_b16 v[110:111], v130 offset:3200\n\t"
        "s_waitcnt lgkmcnt(10)\n\t"
        "v_mfma_f32_32x32x16_bf16 v[64:79], v[136:139], v[112:115], v[64:79]\n\t"
        "s_cmp_eq_u32 s46, 110592\n\t"
        "s_cselect_b32 s46, 0, s46\n\t"
        "ds_read_b64_tr_b16 v[112:113], v129 offset:256\n\t"
        "ds_read_b64_tr_b16 v[114:115], v129 offset:3328\n\t"
        "s_waitcnt lgkmcnt(10)\n\t"
        "v_mfma_f32_32x32x16_bf16 v[80:95], v[136:139], v[116:119], v[80:95]\n\t"
        "s_cmp_lt_u32 s47, 32\n\t"
        "s_cbranch_scc1 4f\n\t"
        "s_waitcnt vmcnt(0)\n\t"
        "4:\n\t"
        "ds_write_b32 v127, v128\n\t"
        "ds_read_b64_tr_b16 v[116:117], v130 offset:256\n\t"
        "ds_read_b64_tr_b16 v[118:119], v130 offset:3328\n\t"
        "s_waitcnt vmcnt(13) lgkmcnt(11)\n\t"
        "v_mfma_f32_32x32x16_bf16 v[0:15], v[96:99], v[140:143], v[0:15]\n\t"
        "ds_read_b32 v126, v125\n\t"
        "ds_read_b64_tr_b16 v[96:97], v129 offset:6144\n\t"
        "ds_read_b64_tr_b16 v[98:99], v129 offset:9216\n\t"
        "s_waitcnt lgkmcnt(12)\n\t"
        "v_mfma_f32_32x32x16_bf16 v[16:31], v[100:103], v[140:143], v[16:31]\n\t"
        "v_add_u32 v123, s46, v121\n\t"
        "ds_read_b64_tr_b16 v[100:101], v130 offset:6144\n\t"
        "ds_read_b64_tr_b16 v[102:103], v130 offset:9216\n\t"
        "s_waitcnt lgkmcnt(12)\n\t"
        "v_mfma_f32_32x32x16_bf16 v[32:47], v[104:107], v[140:143], v[32:47]\n\t"
        "ds_read_b64_tr_b16 v[104:105], v129 offset:6272\n\t"
        "ds_read_b64_tr_b16 v[106:107], v129 offset:9344\n\t"
        "s_waitcnt lgkmcnt(12)\n\t"
        "v_mfma_f32_32x32x16_bf16 v[48:63], v[108:111], v[140:143], v[48:63]\n\t"
        "v_add_u32 v124, s46, v122\n\t"
        "ds_read_b64_tr_b16 v[108:109], v130 offset:6272\n\t"
        "ds_read_b64_tr_b16 v[110:111], v130 offset:9344\n\t"
        "s_waitcnt lgkmcnt(12)\n\t"
        "v_mfma_f32_32x32x16_bf16 v[64:79], v[140:143], v[112:115], v[64:79]\n\t"
        "ds_read_b64_tr_b16 v[112:113], v129 offset:6400\n\t"
        "ds_read_b64_tr_b16 v[114:115], v129 offset:9472\n\t"
        "s_waitcnt lgkmcnt(10)\n\t"
        "v_mfma_f32_32x32x16_bf16 v[80:95], v[140:143], v[116:119], v[80:95]\n\t"
        "v_cmp_gt_u32 vcc, s47, v126\n\t"
        "ds_read_b64_tr_b16 v[116:117], v130 offset:6400\n\t"
        "ds_read_b64_tr_b16 v[118:119], v130 offset:9472\n\t"
        "s_waitcnt vmcnt(12) lgkmcnt(10)\n\t"
        "v_mfma_f32_32x32x16_bf16 v[0:15], v[96:99], v[144:147], v[0:15]\n\t"
        "s_cbranch_vccnz 14f\n\t"
        "15:\n\t"
        "ds_read_b64_tr_b16 v[96:97], v123 offset:0\n\t"
        "ds_read_b64_tr_b16 v[98:99], v123 offset:3072\n\t"
        "s_waitcnt lgkmcnt(10)\n\t"
        "v_mfma_f32_32x32x16_bf16 v[16:31], v[100:103], v[144:147], v[16:31]\n\t"
        "v_add_u32 v128, 1, v128\n\t"
        "ds_read_b64_tr_b16 v[100:101], v124 offset:0\n\t"
        "ds_read_b64_tr_b16 v[102:103], v124 offset:3072\n\t"
        "s_waitcnt lgkmcnt(10)\n\t"
        "v_mfma_f32_32x32x16_bf16 v[32:47], v[104:107], v[144:147], v[32:47]\n\t"
        "ds_read_b64_tr_b16 v[104:105], v123 offset:128\n\t"
        "ds_read_b64_tr_b16 v[106:107], v123 offset:3200\n\t"
        "s_waitcnt lgkmcnt(10)\n\t"
        "v_mfma_f32_32x32x16_bf16 v[48:63], v[108:111], v[144:147], v[48:63]\n\t"
        "s_add_u32 s46, s46, 12288\n\t"
        "s_add_u32 s47, s47, 1\n\t"
        "ds_read_b64_tr_b16 v[108:109], v124 offset:128\n\t"
        "ds_read_b64_tr_b16 v[110:111], v124 offset:3200\n\t"
        "s_waitcnt lgkmcnt(10)\n\t"
        "v_mfma_f32_32x32x16_bf16 v[64:79], v[144:147], v[112:115], v[64:79]\n\t"
        "s_cmp_eq_u32 s46, 110592\n\t"
        "s_cselect_b32 s46, 0, s46\n\t"
        "ds_read_b64_tr_b16 v[112:113], v123 offset:256\n\t"
        "ds_read_b64_tr_b16 v[114:115], v123 offset:3328\n\t"
        "s_waitcnt lgkmcnt(10)\n\t"
        "v_mfma_f32_32x32x16_bf16 v[80:95], v[144:147], v[116:119], v[80:95]\n\t"
        "ds_write_b32 v127, v128\n\t"
        "ds_read_b64_tr_b16 v[116:117], v124 offset:256\n\t"
        "ds_read_b64_tr_b16 v[118:119], v124 offset:3328\n\t"
        "s_waitcnt lgkmcnt(0)\n\t"
        "s_nop 7\n\t"
        "s_branch 9f\n\t"
        "10:\n\t"
        "s_mov_b32 s48, 0x10000\n\t"
        "8:\n\t"
        "ds_read_b32 v126, v125\n\t"
        "s_sub_u32 s48, s48, 1\n\t"
        "s_waitcnt lgkmcnt(0)\n\t"
        "s_cbranch_scc1 11b\n\t"
        "v_cmp_gt_u32 vcc, s47, v126\n\t"
        "s_cbranch_vccnz 8b\n\t"
        "s_branch 11b\n\t"
        "12:\n\t"
        "s_mov_b32 s48, 0x10000\n\t"
        "8:\n\t"
        "ds_read_b32 v126, v125\n\t"
        "s_sub_u32 s48, s48, 1\n\t"
        "s_waitcnt lgkmcnt(0)\n\t"
        "s_cbranch_scc1 13b\n\t"
        "v_cmp_gt_u32 vcc, s47, v126\n\t"
        "s_cbranch_vccnz 8b\n\t"
        "s_branch 13b\n\t"
        "14:\n\t"
        "s_mov_b32 s48, 0x10000\n\t"
        "8:\n\t"
        "ds_read_b32 v126, v125\n\t"
        "s_sub_u32 s48, s48, 1\n\t"
        "s_waitcnt lgkmcnt(0)\n\t"
        "s_cbranch_scc1 15b\n\t"
        "v_cmp_gt_u32 vcc, s47, v126\n\t"
        "s_cbranch_vccnz 8b\n\t"
        "s_branch 15b\n\t"
        "9:\n\t"
        : "={v[0:15]}"(acc[0]), "={v[16:31]}"(acc[1]), "={v[32:47]}"(acc[2]), "={v[48:63]}"(acc[3]), "={v[64:79]}"(acc[4]), "={v[80:95]}"(acc[5]),
          "={v[96:111]}"(stA), "+{v[112:127]}"(stB), "+{v[128:131]}"(stC), "={s46}"(slot), "+{s47}"(need)
        : "{v[132:147]}"(xr0) : "s48", "vcc", "scc", "memory");
      STAMPV(2 + 16 * L);
      float al = 1.f, be = 0.f;
      if (L == 1) {
        PHASE_VARS;
        float S, Q, Dd;
        row_stats_sum(rs, OFF_SX, tile, lane, S, Q, Dd);
        float mean, var, mb1, rd1, mb2, den2;
        row_stats_fin(smem, 1, tile, lane, S, Q, Dd, U2F(words[0]), x1p, mean, var, mb1, rd1);
        ln_scalars(mean + (mean - mb1) * rd1, var * (1.0f + rd1) * (1.0f + rd1), mb2, den2);
        const float rd2 = RCP(den2);
        al = (1.0f + rd1) * rd2; be = -(mb1 * rd1 + mb2) * rd2;
        reinterpret_cast<float*>(smem + L_AB)[wid * 64 + lane] = hi ? be : al;
        reinterpret_cast<float*>(smem + L_AB)[256 + wid * 64 + lane] = hi ? rd1 : mb1;
        STAMPV(36);
        lds_wait_ge<0>(lbase + L_W + 24, 2);
        soff = ((tn + 1) & 15) * 4096;
        asm volatile(
          "buffer_load_dwordx4 v[148:151], v120, s[40:43], s44 offen offset:0 sc1\n\t"
          "buffer_load_dwordx4 v[152:155], v120, s[40:43], s44 offen offset:1024 sc1\n\t"
          "buffer_load_dwordx4 v[156:159], v120, s[40:43], s44 offen offset:2048 sc1\n\t"
          "buffer_load_dwordx4 v[160:163], v120, s[40:43], s44 offen offset:3072 sc1\n\t"
          "s_add_u32 s44, s44, 0x1000\n\t"
          "s_and_b32 s44, s44, 0xffff\n\t"
          "buffer_load_dwordx4 v[164:167], v120, s[40:43], s44 offen offset:0 sc1\n\t"
          "buffer_load_dwordx4 v[168:171], v120, s[40:43], s44 offen offset:1024 sc1\n\t"
          "buffer_load_dwordx4 v[172:175], v120, s[40:43], s44 offen offset:2048 sc1\n\t"
          "buffer_load_dwordx4 v[176:179], v120, s[40:43], s44 offen offset:3072 sc1\n\t"
          "s_add_u32 s44, s44, 0x1000\n\t"
          "s_and_b32 s44, s44, 0xffff\n\t"
          "buffer_load_dwordx4 v[180:183], v120, s[40:43], s44 offen offset:0 sc1\n\t"
          "buffer_load_dwordx4 v[184:187], v120, s[40:43], s44 offen offset:1024 sc1\n\t"
          "buffer_load_dwordx4 v[188:191], v120, s[40:43], s44 offen offset:2048 sc1\n\t"
          "buffer_load_dwordx4 v[192:195], v120, s[40:43], s44 offen offset:3072 sc1\n\t"
          : "={v[148:163]}"(xr1), "={v[164:179]}"(xr2), "={v[180:195]}"(xr3), "+{s44}"(soff) : "{v[112:127]}"(stB), "{s[40:43]}"(srd) : "scc", "memory");
      }
      STAMPV(3 + 16 * L);
      soff = ((tn + 1 + 2) & 15) * 4096;
      asm volatile(
        "s_nop 0\n\t"
        "s_add_u32 s44, s44, 0x1000\n\t"
        "s_and_b32 s44, s44, 0xffff\n\t"
        "buffer_load_dwordx4 v[132:135], v120, s[40:43], s44 offen offset:0 sc1\n\t"
        "buffer_load_dwordx4 v[136:139], v120, s[40:43], s44 offen offset:1024 sc1\n\t"
        "buffer_load_dwordx4 v[140:143], v120, s[40:43], s44 offen offset:2048 sc1\n\t"
        "s_mov_b32 s45, 3\n\t"
        "1:\n\t"
        "s_waitcnt vmcnt(14) lgkmcnt(11)\n\t"
        "v_mfma_f32_32x32x16_bf16 v[0:15], v[96:99], v[148:151], v[0:15]\n\t"
        "ds_read_b32 v126, v125\n\t"
        "ds_read_b64_tr_b16 v[96:97], v123 offset:6144\n\t"
        "ds_read_b64_tr_b16 v[98:99], v123 offset:9216\n\t"
        "s_waitcnt lgkmcnt(12)\n\t"
        "v_mfma_f32_32x32x16_bf16 v[16:31], v[100:103], v[148:151], v[16:31]\n\t"
        "v_add_u32 v129, s46, v121\n\t"
        "ds_read_b64_tr_b16 v[100:101], v124 offset:6144\n\t"
        "ds_read_b64_tr_b16 v[102:103], v124 offset:9216\n\t"
        "s_waitcnt lgkmcnt(12)\n\t"
        "v_mfma_f32_32x32x16_bf16 v[32:47], v[104:107], v[148:151], v[32:47]\n\t"
        "ds_read_b64_tr_b16 v[104:105], v123 offset:6272\n\t"
        "ds_read_b64_tr_b16 v[106:107], v123 offset:9344\n\t"
        "buffer_load_dwordx4 v[144:147], v120, s[40:43], s44 offen offset:3072 sc1\n\t"
        "s_waitcnt lgkmcnt(12)\n\t"
        "v_mfma_f32_32x32x16_bf16 v[48:63], v[108:111], v[148:151], v[48:63]\n\t"
        "v_add_u32 v130, s46, v122\n\t"
        "ds_read_b64_tr_b16 v[108:109], v124 offset:6272\n\t"
        "ds_read_b64_tr_b16 v[110:111], v124 offset:9344\n\t"
        "s_waitcnt lgkmcnt(12)\n\t"
        "v_mfma_f32_32x32x16_bf16 v[64:79], v[148:151], v[112:115], v[64:79]\n\t"
        "ds_read_b64_tr_b16 v[112:113], v123 offset:6400\n\t"
        "ds_read_b64_tr_b16 v[114:115], v123 offset:9472\n\t"
        "s_waitcnt lgkmcnt(10)\n\t"
        "v_mfma_f32_32x32x16_bf16 v[80:95], v[148:151], v[116:119], v[80:95]\n\t"
        "v_cmp_gt_u32 vcc, s47, v126\n\t"
        "ds_read_b64_tr_b16 v[116:117], v124 offset:6400\n\t"
        "ds_read_b64_tr_b16 v[118:119], v124 offset:9472\n\t"
        "s_waitcnt vmcnt(14) lgkmcnt(10)\n\t"
        "v_mfma_f32_32x32x16_bf16 v[0:15], v[96:99], v[152:155], v[0:15]\n\t"
        "s_cbranch_vccnz 16f\n\t"
        "17:\n\t"
        "ds_read_b64_tr_b16 v[96:97], v129 offset:0\n\t"
        "ds_read_b64_tr_b16 v[98:99], v129 offset:3072\n\t"
        "s_waitcnt lgkmcnt(10)\n\t"
        "v_mfma_f32_32x32x16_bf16 v[16:31], v[100:103], v[152:155], v[16:31]\n\t"
        "v_add_u32 v128, 1, v128\n\t"
        "ds_read_b64_tr_b16 v[100:101], v130 offset:0\n\t"
        "ds_read_b64_tr_b16 v[102:103], v130 offset:3072\n\t"
        "s_waitcnt lgkmcnt(10)\n\t"
        "v_mfma_f32_32x32x16_bf16 v[32:47], v[104:107], v[152:155], v[32:47]\n\t"
        "ds_read_b64_tr_b16 v[104:105], v129 offset:128\n\t"
        "ds_read_b64_tr_b16 v[106:107], v129 offset:3200\n\t"
        "s_add_u32 s44, s44, 0x1000\n\t"
        "s_and_b32 s44, s44, 0xffff\n\t"
        "buffer_load_dwordx4 v[148:151], v120, s[40:43], s44 offen offset:0 sc1\n\t"
        "s_waitcnt lgkmcnt(10)\n\t"
        "v_mfma_f32_32x32x16_bf16 v[48:63], v[108:111], v[152:155], v[48:63]\n\t"
        "s_add_u32 s46, s46, 12288\n\t"
        "s_add_u32 s47, s47, 1\n\t"
        "ds_read_b64_tr_b16 v[108:109], v130 offset:128\n\t"
        "ds_read_b64_tr_b16 v[110:111], v130 offset:3200\n\t"
        "s_waitcnt lgkmcnt(10)\n\t"
        "v_mfma_f32_32x32x16_bf16 v[64:79], v[152:155], v[112:115], v[64:79]\n\t"
        "s_cmp_eq_u32 s46, 110592\n\t"
        "s_cselect_b32 s46, 0, s46\n\t"
        "ds_read_b64_tr_b16 v[112:113], v129 offset:256\n\t"
        "ds_read_b64_tr_b16 v[114:115], v129 offset:3328\n\t"
        "s_waitcnt lgkmcnt(10)\n\t"
        "v_mfma_f32_32x32x16_bf16 v[80:95], v[152:155], v[116:119], v[80:95]\n\t"
        "ds_write_b32 v127, v128\n\t"
        "ds_read_b64_tr_b16 v[116:117], v130 offset:256\n\t"
        "ds_read_b64_tr_b16 v[118:119], v130 offset:3328\n\t"
        "s_waitcnt vmcnt(14) lgkmcnt(11)\n\t"
        "v_mfma_f32_32x32x16_bf16 v[0:15], v[96:99], v[156:159], v[0:15]\n\t"
        "ds_read_b32 v126, v125\n\t"
        "ds_read_b64_tr_b16 v[96:97], v129 offset:6144\n\t"
        "ds_read_b64_tr_b16 v[98:99], v129 offset:9216\n\t"
        "s_waitcnt lgkmcnt(12)\n\t"
        "v_mfma_f32_32x32x16_bf16 v[16:31], v[100:103], v[156:159], v[16:31]\n\t"
        "v_add_u32 v123, s46, v121\n\t"
        "ds_read_b64_tr_b16 v[100:101], v130 offset:6144\n\t"
        "ds_read_b64_tr_b16 v[102:103], v130 offset:9216\n\t"
        "s_waitcnt lgkmcnt(12)\n\t"
        "v_mfma_f32_32x32x16_bf16 v[32:47], v[104:107], v[156:159], v[32:47]\n\t"
        "ds_read_b64_tr_b16 v[104:105], v129 offset:6272\n\t"
        "ds_read_b64_tr_b16 v[106:107], v129 offset:9344\n\t"
        "buffer_load_dwordx4 v[152:155], v120, s[40:43], s44 offen offset:1024 sc1\n\t"
        "s_waitcnt lgkmcnt(12)\n\t"
        "v_mfma_f32_32x32x16_bf16 v[48:63], v[108:111], v[156:159], v[48:63]\n\t"
        "v_add_u32 v124, s46, v122\n\t"
        "ds_read_b64_tr_b16 v[108:109], v130 offset:6272\n\t"
        "ds_read_b64_tr_b16 v[110:111], v130 offset:9344\n\t"
        "s_waitcnt lgkmcnt(12)\n\t"
        "v_mfma_f32_32x32x16_bf16 v[64:79], v[156:159], v[112:115], v[64:79]\n\t"
        "ds_read_b64_tr_b16 v[112:113], v129 offset:6400\n\t"
        "ds_read_b64_tr_b16 v[114:115], v129 offset:9472\n\t"
        "s_waitcnt lgkmcnt(10)\n\t"
        "v_mfma_f32_32x32x16_bf16 v[80:95], v[156:159], v[116:119], v[80:95]\n\t"
        "v_cmp_gt_u32 vcc, s47, v126\n\t"
        "ds_read_b64_tr_b16 v[116:117], v130 offset:6400\n\t"
        "ds_read_b64_tr_b16 v[118:119], v130 offset:9472\n\t"
        "s_waitcnt vmcnt(14) lgkmcnt(10)\n\t"
        "v_mfma_f32_32x32x16_bf16 v[0:15], v[96:99], v[160:163], v[0:15]\n\t"
        "s_cbranch_vccnz 18f\n\t"
        "19:\n\t"
        "ds_read_b64_tr_b16 v[96:97], v123 offset:0\n\t"
        "ds_read_b64_tr_b16 v[98:99], v123 offset:3072\n\t"
        "s_waitcnt lgkmcnt(10)\n\t"
        "v_mfma_f32_32x32x16_bf16 v[16:31], v[100:103], v[160:163], v[16:31]\n\t"
        "v_add_u32 v128, 1, v128\n\t"
        "ds_read_b64_tr_b16 v[100:101], v124 offset:0\n\t"
        "ds_read_b64_tr_b16 v[102:103], v124 offset:3072\n\t"
        "s_waitcnt lgkmcnt(10)\n\t"
        "v_mfma_f32_32x32x16_bf16 v[32:47], v[104:107], v[160:163], v[32:47]\n\t"
        "ds_read_b64_tr_b16 v[104:105], v123 offset:128\n\t"
        "ds_read_b64_tr_b16 v[106:107], v123 offset:3200\n\t"
        "buffer_load_dwordx4 v[156:159], v120, s[40:43], s44 offen offset:2048 sc1\n\t"
        "s_waitcnt lgkmcnt(10)\n\t"
        "v_mfma_f32_32x32x16_bf16 v[48:63], v[108:111], v[160:163], v[48:63]\n\t"
        "s_add_u32 s46, s46, 12288\n\t"
        "s_add_u32 s47, s47, 1\n\t"
        "ds_read_b64_tr_b16 v[108:109], v124 offset:128\n\t"
        "ds_read_b64_tr_b16 v[110:111], v124 offset:3200\n\t"
        "s_waitcnt lgkmcnt(10)\n\t"
        "v_mfma_f32_32x32x16_bf16 v[64:79], v[160:163], v[112:115], v[64:79]\n\t"
        "s_cmp_eq_u32 s46, 110592\n\t"
        "s_cselect_b32 s46, 0, s46\n\t"
        "ds_read_b64_tr_b16 v[112:113], v123 offset:256\n\t"
        "ds_read_b64_tr_b16 v[114:115], v123 offset:3328\n\t"
        "s_waitcnt lgkmcnt(10)\n\t"
        "v_mfma_f32_32x32x16_bf16 v[80:95], v[160:163], v[116:119], v[80:95]\n\t"
        "ds_write_b32 v127, v128\n\t"
        "ds_read_b64_tr_b16 v[116:117], v124 offset:256\n\t"
        "ds_read_b64_tr_b16 v[118:119], v124 offset:3328\n\t"
        "s_waitcnt vmcnt(14) lgkmcnt(11)\n\t"
        "v_mfma_f32_32x32x16_bf16 v[0:15], v[96:99], v[164:167], v[0:15]\n\t"
        "ds_read_b32 v126, v125\n\t"
        "ds_read_b64_tr_b16 v[96:97], v123 offset:6144\n\t"
        "ds_read_b64_tr_b16 v[98:99], v123 offset:9216\n\t"
        "s_waitcnt lgkmcnt(12)\n\t"
        "v_mfma_f32_32x32x16_bf16 v[16:31], v[100:103], v[164:167], v[16:31]\n\t"
        "v_add_u32 v129, s46, v121\n\t"
        "ds_read_b64_tr_b16 v[100:101], v124 offset:6144\n\t"
        "ds_read_b64_tr_b16 v[102:103], v124 offset:9216\n\t"
        "s_waitcnt lgkmcnt(12)\n\t"
        "v_mfma_f32_32x32x16_bf16 v[32:47], v[104:107], v[164:167], v[32:47]\n\t"
        "ds_read_b64_tr_b16 v[104:105], v123 offset:6272\n\t"
        "ds_read_b64_tr_b16 v[106:107], v123 offset:9344\n\t"
        "buffer_load_dwordx4 v[160:163], v120, s[40:43], s44 offen offset:3072 sc1\n\t"
        "s_waitcnt lgkmcnt(12)\n\t"
        "v_mfma_f32_32x32x16_bf16 v[48:63], v[108:111], v[164:167], v[48:63]\n\t"
        "v_add_u32 v130, s46, v122\n\t"
        "ds_read_b64_tr_b16 v[108:109], v124 offset:6272\n\t"
        "ds_read_b64_tr_b16 v[110:111], v124 offset:9344\n\t"
        "s_waitcnt lgkmcnt(12)\n\t"
        "v_mfma_f32_32x32x16_bf16 v[64:79], v[164:167], v[112:115], v[64:79]\n\t"
        "ds_read_b64_tr_b16 v[112:113], v123 offset:6400\n\t"
        "ds_read_b64_tr_b16 v[114:115], v123 offset:9472\n\t"
        "s_waitcnt lgkmcnt(10)\n\t"
        "v_mfma_f32_32x32x16_bf16 v[80:95], v[164:167], v[116:119], v[80:95]\n\t"
        "v_cmp_gt_u32 vcc, s47, v126\n\t"
        "ds_read_b64_tr_b16 v[116:117], v124 offset:6400\n\t"
        "ds_read_b64_tr_b16 v[118:119], v124 offset:9472\n\t"
        "s_waitcnt vmcnt(14) lgkmcnt(10)\n\t"
        "v_mfma_f32_32x32x16_bf16 v[0:15], v[96:99], v[168:171], v[0:15]\n\t"
        "s_cbranch_vccnz 20f\n\t"
        "21:\n\t"
        "ds_read_b64_tr_b16 v[96:97], v129 offset:0\n\t"
        "ds_read_b64_tr_b16 v[98:99], v129 offset:3072\n\t"
        "s_waitcnt lgkmcnt(10)\n\t"
        "v_mfma_f32_32x32x16_bf16 v[16:31], v[100:103], v[168:171], v[16:31]\n\t"
        "v_add_u32 v128, 1, v128\n\t"
        "ds_read_b64_tr_b16 v[100:101], v130 offset:0\n\t"
        "ds_read_b64_tr_b16 v[102:103], v130 offset:3072\n\t"
        "s_waitcnt lgkmcnt(10)\n\t"
        "v_mfma_f32_32x32x16_bf16 v[32:47], v[104:107], v[168:171], v[32:47]\n\t"
        "ds_read_b64_tr_b16 v[104:105], v129 offset:128\n\t"
        "ds_read_b64_tr_b16 v[106:107], v129 offset:3200\n\t"
        "s_add_u32 s44, s44, 0x1000\n\t"
        "s_and_b32 s44, s44, 0xffff\n\t"
        "buffer_load_dwordx4 v[164:167], v120, s[40:43], s44 offen offset:0 sc1\n\t"
        "s_waitcnt lgkmcnt(10)\n\t"
        "v_mfma_f32_32x32x16_bf16 v[48:63], v[108:111], v[168:171], v[48:63]\n\t"
        "s_add_u32 s46, s46, 12288\n\t"
        "s_add_u32 s47, s47, 1\n\t"
        "ds_read_b64_tr_b16 v[108:109], v130 offset:128\n\t"
        "ds_read_b64_tr_b16 v[110:111], v130 offset:3200\n\t"
        "s_waitcnt lgkmcnt(10)\n\t"
        "v_mfma_f32_32x32x16_bf16 v[64:79], v[168:171], v[112:115], v[64:79]\n\t"
        "s_cmp_eq_u32 s46, 110592\n\t"
        "s_cselect_b32 s46, 0, s46\n\t"
        "ds_read_b64_tr_b16 v[112:113], v129 offset:256\n\t"
        "ds_read_b64_tr_b16 v[114:115], v129 offset:3328\n\t"
        "s_waitcnt lgkmcnt(10)\n\t"
        "v_mfma_f32_32x32x16_bf16 v[80:95], v[168:171], v[116:119], v[80:95]\n\t"
        "ds_write_b32 v127, v128\n\t"
        "ds_read_b64_tr_b16 v[116:117], v130 offset:256\n\t"
        "ds_read_b64_tr_b16 v[118:119], v130 offset:3328\n\t"
        "s_waitcnt vmcnt(14) lgkmcnt(11)\n\t"
        "v_mfma_f32_32x32x16_bf16 v[0:15], v[96:99], v[172:175], v[0:15]\n\t"
        "ds_read_b32 v126, v125\n\t"
        "ds_read_b64_tr_b16 v[96:97], v129 offset:6144\n\t"
        "ds_read_b64_tr_b16 v[98:99], v129 offset:9216\n\t"
        "s_waitcnt lgkmcnt(12)\n\t"
        "v_mfma_f32_32x32x16_bf16 v[16:31], v[100:103], v[172:175], v[16:31]\n\t"
        "v_add_u32 v123, s46, v121\n\t"
        "ds_read_b64_tr_b16 v[100:101], v130 offset:6144\n\t"
        "ds_read_b64_tr_b16 v[102:103], v130 offset:9216\n\t"
        "s_waitcnt lgkmcnt(12)\n\t"
        "v_mfma_f32_32x32x16_bf16 v[32:47], v[104:107], v[172:175], v[32:47]\n\t"
        "ds_read_b64_tr_b16 v[104:105], v129 offset:6272\n\t"
        "ds_read_b64_tr_b16 v[106:107], v129 offset:9344\n\t"
        "buffer_load_dwordx4 v[168:171], v120, s[40:43], s44 offen offset:1024 sc1\n\t"
        "s_waitcnt lgkmcnt(12)\n\t"
        "v_mfma_f32_32x32x16_bf16 v[48:63], v[108:111], v[172:175], v[48:63]\n\t"
        "v_add_u32 v124, s46, v122\n\t"
        "ds_read_b64_tr_b16 v[108:109], v130 offset:6272\n\t"
        "ds_read_b64_tr_b16 v[110:111], v130 offset:9344\n\t"
        "s_waitcnt lgkmcnt(12)\n\t"
        "v_mfma_f32_32x32x16_bf16 v[64:79], v[172:175], v[112:115], v[64:79]\n\t"
        "ds_read_b64_tr_b16 v[112:113], v129 offset:6400\n\t"
        "ds_read_b64_tr_b16 v[114:115], v129 offset:9472\n\t"
        "s_waitcnt lgkmcnt(10)\n\t"
        "v_mfma_f32_32x32x16_bf16 v[80:95], v[172:175], v[116:119], v[80:95]\n\t"
        "v_cmp_gt_u32 vcc, s47, v126\n\t"
        "ds_read_b64_tr_b16 v[116:117], v130 offset:6400\n\t"
        "ds_read_b64_tr_b16 v[118:119], v130 offset:9472\n\t"
        "s_waitcnt vmcnt(14) lgkmcnt(10)\n\t"
        "v_mfma_f32_32x32x16_bf16 v[0:15], v[96:99], v[176:179], v[0:15]\n\t"
        "s_cbranch_vccnz 22f\n\t"
        "23:\n\t"
        "ds_read_b64_tr_b16 v[96:97], v123 offset:0\n\t"
        "ds_read_b64_tr_b16 v[98:99], v123 offset:3072\n\t"
        "s_waitcnt lgkmcnt(10)\n\t"
        "v_mfma_f32_32x32x16_bf16 v[16:31], v[100:103], v[176:179], v[16:31]\n\t"
        "v_add_u32 v128, 1, v128\n\t"
        "ds_read_b64_tr_b16 v[100:101], v124 offset:0\n\t"
        "ds_read_b64_tr_b16 v[102:103], v124 offset:3072\n\t"
        "s_waitcnt lgkmcnt(10)\n\t"
        "v_mfma_f32_32x32x16_bf16 v[32:47], v[104:107], v[176:179], v[32:47]\n\t"
        "ds_read_b64_tr_b16 v[104:105], v123 offset:128\n\t"
        "ds_read_b64_tr_b16 v[106:107], v123 offset:3200\n\t"
        "buffer_load_dwordx4 v[172:175], v120, s[40:43], s44 offen offset:2048 sc1\n\t"
        "s_waitcnt lgkmcnt(10)\n\t"
        "v_mfma_f32_32x32x16_bf16 v[48:63], v[108:111], v[176:179], v[48:63]\n\t"
        "s_add_u32 s46, s46, 12288\n\t"
        "s_add_u32 s47, s47, 1\n\t"
        "ds_read_b64_tr_b16 v[108:109], v124 offset:128\n\t"
        "ds_read_b64_tr_b16 v[110:111], v124 offset:3200\n\t"
        "s_waitcnt lgkmcnt(10)\n\t"
        "v_mfma_f32_32x32x16_bf16 v[64:79], v[176:179], v[112:115], v[64:79]\n\t"
        "s_cmp_eq_u32 s46, 110592\n\t"
        "s_cselect_b32 s46, 0, s46\n\t"
        "ds_read_b64_tr_b16 v[112:113], v123 offset:256\n\t"
        "ds_read_b64_tr_b16 v[114:115], v123 offset:3328\n\t"
        "s_waitcnt lgkmcnt(10)\n\t"
        "v_mfma_f32_32x32x16_bf16 v[80:95], v[176:179], v[116:119], v[80:95]\n\t"
        "ds_write_b32 v127, v128\n\t"
        "ds_read_b64_tr_b16 v[116:117], v124 offset:256\n\t"
        "ds_read_b64_tr_b16 v[118:119], v124 offset:3328\n\t"
        "s_waitcnt vmcnt(14) lgkmcnt(11)\n\t"
        "v_mfma_f32_32x32x16_bf16 v[0:15], v[96:99], v[180:183], v[0:15]\n\t"
        "ds_read_b32 v126, v125\n\t"
        "ds_read_b64_tr_b16 v[96:97], v123 offset:6144\n\t"
        "ds_read_b64_tr_b16 v[98:99], v123 offset:9216\n\t"
        "s_waitcnt lgkmcnt(12)\n\t"
        "v_mfma_f32_32x32x16_bf16 v[16:31], v[100:103], v[180:183], v[16:31]\n\t"
        "v_add_u32 v129, s46, v121\n\t"
        "ds_read_b64_tr_b16 v[100:101], v124 offset:6144\n\t"
        "ds_read_b64_tr_b16 v[102:103], v124 offset:9216\n\t"
        "s_waitcnt lgkmcnt(12)\n\t"
        "v_mfma_f32_32x32x16_bf16 v[32:47], v[104:107], v[180:183], v[32:47]\n\t"
        "ds_read_b64_tr_b16 v[104:105], v123 offset:6272\n\t"
        "ds_read_b64_tr_b16 v[106:107], v123 offset:9344\n\t"
        "buffer_load_dwordx4 v[176:179], v120, s[40:43], s44 offen offset:3072 sc1\n\t"
        "s_waitcnt lgkmcnt(12)\n\t"
        "v_mfma_f32_32x32x16_bf16 v[48:63], v[108:111], v[180:183], v[48:63]\n\t"
        "v_add_u32 v130, s46, v122\n\t"
        "ds_read_b64_tr_b16 v[108:109], v124 offset:6272\n\t"
        "ds_read_b64_tr_b16 v[110:111], v124 offset:9344\n\t"
        "s_waitcnt lgkmcnt(12)\n\t"
        "v_mfma_f32_32x32x16_bf16 v[64:79], v[180:183], v[112:115], v[64:79]\n\t"
        "ds_read_b64_tr_b16 v[112:113], v123 offset:6400\n\t"
        "ds_read_b64_tr_b16 v[114:115], v123 offset:9472\n\t"
        "s_waitcnt lgkmcnt(10)\n\t"
        "v_mfma_f32_32x32x16_bf16 v[80:95], v[180:183], v[116:119], v[80:95]\n\t"
        "v_cmp_gt_u32 vcc, s47, v126\n\t"
        "ds_read_b64_tr_b16 v[116:117], v124 offset:6400\n\t"
        "ds_read_b64_tr_b16 v[118:119], v124 offset:9472\n\t"
        "s_waitcnt vmcnt(14) lgkmcnt(10)\n\t"
        "v_mfma_f32_32x32x16_bf16 v[0:15], v[96:99], v[184:187], v[0:15]\n\t"
        "s_cbranch_vccnz 24f\n\t"
        "25:\n\t"
        "ds_read_b64_tr_b16 v[96:97], v129 offset:0\n\t"
        "ds_read_b64_tr_b16 v[98:99], v129 offset:3072\n\t"
        "s_waitcnt lgkmcnt(10)\n\t"
        "v_mfma_f32_32x32x16_bf16 v[16:31], v[100:103], v[184:187], v[16:31]\n\t"
        "v_add_u32 v128, 1, v128\n\t"
        "ds_read_b64_tr_b16 v[100:101], v130 offset:0\n\t"
        "ds_read_b64_tr_b16 v[102:103], v130 offset:3072\n\t"
        "s_waitcnt lgkmcnt(10)\n\t"
        "v_mfma_f32_32x32x16_bf16 v[32:47], v[104:107], v[184:187], v[32:47]\n\t"
        "ds_read_b64_tr_b16 v[104:105], v129 offset:128\n\t"
        "ds_read_b64_tr_b16 v[106:107], v129 offset:3200\n\t"
        "s_add_u32 s44, s44, 0x1000\n\t"
        "s_and_b32 s44, s44, 0xffff\n\t"
        "buffer_load_dwordx4 v[180:183], v120, s[40:43], s44 offen offset:0 sc1\n\t"
        "s_waitcnt lgkmcnt(10)\n\t"
        "v_mfma_f32_32x32x16_bf16 v[48:63], v[108:111], v[184:187], v[48:63]\n\t"
        "s_add_u32 s46, s46, 12288\n\t"
        "s_add_u32 s47, s47, 1\n\t"
        "ds_read_b64_tr_b16 v[108:109], v130 offset:128\n\t"
        "ds_read_b64_tr_b16 v[110:111], v130 offset:3200\n\t"
        "s_waitcnt lgkmcnt(10)\n\t"
        "v_mfma_f32_32x32x16_bf16 v[64:79], v[184:187], v[112:115], v[64:79]\n\t"
        "s_cmp_eq_u32 s46, 110592\n\t"
        "s_cselect_b32 s46, 0, s46\n\t"
        "ds_read_b64_tr_b16 v[112:113], v129 offset:256\n\t"
        "ds_read_b64_tr_b16 v[114:115], v129 offset:3328\n\t"
        "s_waitcnt lgkmcnt(10)\n\t"
        "v_mfma_f32_32x32x16_bf16 v[80:95], v[184:187], v[116:119], v[80:95]\n\t"
        "ds_write_b32 v127, v128\n\t"
        "ds_read_b64_tr_b16 v[116:117], v130 offset:256\n\t"
        "ds_read_b64_tr_b16 v[118:119], v130 offset:3328\n\t"
        "s_waitcnt vmcnt(14) lgkmcnt(11)\n\t"
        "v_mfma_f32_32x32x16_bf16 v[0:15], v[96:99], v[188:191], v[0:15]\n\t"
        "ds_read_b32 v126, v125\n\t"
        "ds_read_b64_tr_b16 v[96:97], v129 offset:6144\n\t"
        "ds_read_b64_tr_b16 v[98:99], v129 offset:9216\n\t"
        "s_waitcnt lgkmcnt(12)\n\t"
        "v_mfma_f32_32x32x16_bf16 v[16:31], v[100:103], v[188:191], v[16:31]\n\t"
        "v_add_u32 v123, s46, v121\n\t"
        "ds_read_b64_tr_b16 v[100:101], v130 offset:6144\n\t"
        "ds_read_b64_tr_b16 v[102:103], v130 offset:9216\n\t"
        "s_waitcnt lgkmcnt(12)\n\t"
        "v_mfma_f32_32x32x16_bf16 v[32:47], v[104:107], v[188:191], v[32:47]\n\t"
        "ds_read_b64_tr_b16 v[104:105], v129 offset:6272\n\t"
        "ds_read_b64_tr_b16 v[106:107], v129 offset:9344\n\t"
        "buffer_load_dwordx4 v[184:187], v120, s[40:43], s44 offen offset:1024 sc1\n\t"
        "s_waitcnt lgkmcnt(12)\n\t"
        "v_mfma_f32_32x32x16_bf16 v[48:63], v[108:111], v[188:191], v[48:63]\n\t"
        "v_add_u32 v124, s46, v122\n\t"
        "ds_read_b64_tr_b16 v[108:109], v130 offset:6272\n\t"
        "ds_read_b64_tr_b16 v[110:111], v130 offset:9344\n\t"
        "s_waitcnt lgkmcnt(12)\n\t"
        "v_mfma_f32_32x32x16_bf16 v[64:79], v[188:191], v[112:115], v[64:79]\n\t"
        "ds_read_b64_tr_b16 v[112:113], v129 offset:6400\n\t"
        "ds_read_b64_tr_b16 v[114:115], v129 offset:9472\n\t"
        "s_waitcnt lgkmcnt(10)\n\t"
        "v_mfma_f32_32x32x16_bf16 v[80:95], v[188:191], v[116:119], v[80:95]\n\t"
        "v_cmp_gt_u32 vcc, s47, v126\n\t"
        "ds_read_b64_tr_b16 v[116:117], v130 offset:6400\n\t"
        "ds_read_b64_tr_b16 v[118:119], v130 offset:9472\n\t"
        "s_waitcnt vmcnt(14) lgkmcnt(10)\n\t"
        "v_mfma_f32_32x32x16_bf16 v[0:15], v[96:99], v[192:195], v[0:15]\n\t"
        "s_cbranch_vccnz 26f\n\t"
        "27:\n\t"
        "ds_read_b64_tr_b16 v[96:97], v123 offset:0\n\t"
        "ds_read_b64_tr_b16 v[98:99], v123 offset:3072\n\t"
        "s_waitcnt lgkmcnt(10)\n\t"
        "v_mfma_f32_32x32x16_bf16 v[16:31], v[100:103], v[192:195], v[16:31]\n\t"
        "v_add_u32 v128, 1, v128\n\t"
        "ds_read_b64_tr_b16 v[100:101], v124 offset:0\n\t"
        "ds_read_b64_tr_b16 v[102:103], v124 offset:3072\n\t"
        "s_waitcnt lgkmcnt(10)\n\t"
        "v_mfma_f32_32x32x16_bf16 v[32:47], v[104:107], v[192:195], v[32:47]\n\t"
        "ds_read_b64_tr_b16 v[104:105], v123 offset:128\n\t"
        "ds_read_b64_tr_b16 v[106:107], v123 offset:3200\n\t"
        "buffer_load_dwordx4 v[188:191], v120, s[40:43], s44 offen offset:2048 sc1\n\t"
        "s_waitcnt lgkmcnt(10)\n\t"
        "v_mfma_f32_32x32x16_bf16 v[48:63], v[108:111], v[192:195], v[48:63]\n\t"
        "s_add_u32 s46, s46, 12288\n\t"
        "s_add_u32 s47, s47, 1\n\t"
        "ds_read_b64_tr_b16 v[108:109], v124 offset:128\n\t"
        "ds_read_b64_tr_b16 v[110:111], v124 offset:3200\n\t"
        "s_waitcnt lgkmcnt(10)\n\t"
        "v_mfma_f32_32x32x16_bf16 v[64:79], v[192:195], v[112:115], v[64:79]\n\t"
        "s_cmp_eq_u32 s46, 110592\n\t"
        "s_cselect_b32 s46, 0, s46\n\t"
        "ds_read_b64_tr_b16 v[112:113], v123 offset:256\n\t"
        "ds_read_b64_tr_b16 v[114:115], v123 offset:3328\n\t"
        "s_waitcnt lgkmcnt(10)\n\t"
        "v_mfma_f32_32x32x16_bf16 v[80:95], v[192:195], v[116:119], v[80:95]\n\t"
        "ds_write_b32 v127, v128\n\t"
        "ds_read_b64_tr_b16 v[116:117], v124 offset:256\n\t"
        "ds_read_b64_tr_b16 v[118:119], v124 offset:3328\n\t"
        "s_waitcnt vmcnt(14) lgkmcnt(11)\n\t"
        "v_mfma_f32_32x32x16_bf16 v[0:15], v[96:99], v[132:135], v[0:15]\n\t"
        "ds_read_b32 v126, v125\n\t"
        "ds_read_b64_tr_b16 v[96:97], v123 offset:6144\n\t"
        "ds_read_b64_tr_b16 v[98:99], v123 offset:9216\n\t"
        "s_waitcnt lgkmcnt(12)\n\t"
        "v_mfma_f32_32x32x16_bf16 v[16:31], v[100:103], v[132:135], v[16:31]\n\t"
        "v_add_u32 v129, s46, v121\n\t"
        "ds_read_b64_tr_b16 v[100:101], v124 offset:6144\n\t"
        "ds_read_b64_tr_b16 v[102:103], v124 offset:9216\n\t"
        "s_waitcnt lgkmcnt(12)\n\t"
        "v_mfma_f32_32x32x16_bf16 v[32:47], v[104:107], v[132:135], v[32:47]\n\t"
        "ds_read_b64_tr_b16 v[104:105], v123 offset:6272\n\t"
        "ds_read_b64_tr_b16 v[106:107], v123 offset:9344\n\t"
        "buffer_load_dwordx4 v[192:195], v120, s[40:43], s44 offen offset:3072 sc1\n\t"
        "s_waitcnt lgkmcnt(12)\n\t"
        "v_mfma_f32_32x32x16_bf16 v[48:63], v[108:111], v[132:135], v[48:63]\n\t"
        "v_add_u32 v130, s46, v122\n\t"
        "ds_read_b64_tr_b16 v[108:109], v124 offset:6272\n\t"
        "ds_read_b64_tr_b16 v[110:111], v124 offset:9344\n\t"
        "s_waitcnt lgkmcnt(12)\n\t"
        "v_mfma_f32_32x32x16_bf16 v[64:79], v[132:135], v[112:115], v[64:79]\n\t"
        "ds_read_b64_tr_b16 v[112:113], v123 offset:6400\n\t"
        "ds_read_b64_tr_b16 v[114:115], v123 offset:9472\n\t"
        "s_waitcnt lgkmcnt(10)\n\t"
        "v_mfma_f32_32x32x16_bf16 v[80:95], v[132:135], v[116:119], v[80:95]\n\t"
        "v_cmp_gt_u32 vcc, s47, v126\n\t"
        "ds_read_b64_tr_b16 v[116:117], v124 offset:6400\n\t"
        "ds_read_b64_tr_b16 v[118:119], v124 offset:9472\n\t"
        "s_waitcnt vmcnt(14) lgkmcnt(10)\n\t"
        "v_mfma_f32_32x32x16_bf16 v[0:15], v[96:99], v[136:139], v[0:15]\n\t"
        "s_cbranch_vccnz 28f\n\t"
        "29:\n\t"
        "ds_read_b64_tr_b16 v[96:97], v129 offset:0\n\t"
        "ds_read_b64_tr_b16 v[98:99], v129 offset:3072\n\t"
        "s_waitcnt lgkmcnt(10)\n\t"
        "v_mfma_f32_32x32x16_bf16 v[16:31], v[100:103], v[136:139], v[16:31]\n\t"
        "v_add_u32 v128, 1, v128\n\t"
        "ds_read_b64_tr_b16 v[100:101], v130 offset:0\n\t"
        "ds_read_b64_tr_b16 v[102:103], v130 offset:3072\n\t"
        "s_waitcnt lgkmcnt(10)\n\t"
        "v_mfma_f32_32x32x16_bf16 v[32:47], v[104:107], v[136:139], v[32:47]\n\t"
        "ds_read_b64_tr_b16 v[104:105], v129 offset:128\n\t"
        "ds_read_b64_tr_b16 v[106:107], v129 offset:3200\n\t"
        "s_add_u32 s44, s44, 0x1000\n\t"
        "s_and_b32 s44, s44, 0xffff\n\t"
        "buffer_load_dwordx4 v[132:135], v120, s[40:43], s44 offen offset:0 sc1\n\t"
        "s_waitcnt lgkmcnt(10)\n\t"
        "v_mfma_f32_32x32x16_bf16 v[48:63], v[108:111], v[136:139], v[48:63]\n\t"
        "s_add_u32 s46, s46, 12288\n\t"
        "s_add_u32 s47, s47, 1\n\t"
        "ds_read_b64_tr_b16 v[108:109], v130 offset:128\n\t"
        "ds_read_b64_tr_b16 v[110:111], v130 offset:3200\n\t"
        "s_waitcnt lgkmcnt(10)\n\t"
        "v_mfma_f32_32x32x16_bf16 v[64:79], v[136:139], v[112:115], v[64:79]\n\t"
        "s_cmp_eq_u32 s46, 110592\n\t"
        "s_cselect_b32 s46, 0, s46\n\t"
        "ds_read_b64_tr_b16 v[112:113], v129 offset:256\n\t"
        "ds_read_b64_tr_b16 v[114:115], v129 offset:3328\n\t"
        "s_waitcnt lgkmcnt(10)\n\t"
        "v_mfma_f32_32x32x16_bf16 v[80:95], v[136:139], v[116:119], v[80:95]\n\t"
        "ds_write_b32 v127, v128\n\t"
        "ds_read_b64_tr_b16 v[116:117], v130 offset:256\n\t"
        "ds_read_b64_tr_b16 v[118:119], v130 offset:3328\n\t"
        "s_waitcnt vmcnt(14) lgkmcnt(11)\n\t"
        "v_mfma_f32_32x32x16_bf16 v[0:15], v[96:99], v[140:143], v[0:15]\n\t"
        "ds_read_b32 v126, v125\n\t"
        "ds_read_b64_tr_b16 v[96:97], v129 offset:6144\n\t"
        "ds_read_b64_tr_b16 v[98:99], v129 offset:9216\n\t"
        "s_waitcnt lgkmcnt(12)\n\t"
        "v_mfma_f32_32x32x16_bf16 v[16:31], v[100:103], v[140:143], v[16:31]\n\t"
        "v_add_u32 v123, s46, v121\n\t"
        "ds_read_b64_tr_b16 v[100:101], v130 offset:6144\n\t"
        "ds_read_b64_tr_b16 v[102:103], v130 offset:9216\n\t"
        "s_waitcnt lgkmcnt(12)\n\t"
        "v_mfma_f32_32x32x16_bf16 v[32:47], v[104:107], v[140:143], v[32:47]\n\t"
        "ds_read_b64_tr_b16 v[104:105], v129 offset:6272\n\t"
        "ds_read_b64_tr_b16 v[106:107], v129 offset:9344\n\t"
        "buffer_load_dwordx4 v[136:139], v120, s[40:43], s44 offen offset:1024 sc1\n\t"
        "s_waitcnt lgkmcnt(12)\n\t"
        "v_mfma_f32_32x32x16_bf16 v[48:63], v[108:111], v[140:143], v[48:63]\n\t"
        "v_add_u32 v124, s46, v122\n\t"
        "ds_read_b64_tr_b16 v[108:109], v130 offset:6272\n\t"
        "ds_read_b64_tr_b16 v[110:111], v130 offset:9344\n\t"
        "s_waitcnt lgkmcnt(12)\n\t"
        "v_mfma_f32_32x32x16_bf16 v[64:79], v[140:143], v[112:115], v[64:79]\n\t"
        "ds_read_b64_tr_b16 v[112:113], v129 offset:6400\n\t"
        "ds_read_b64_tr_b16 v[114:115], v129 offset:9472\n\t"
        "s_waitcnt lgkmcnt(10)\n\t"
        "v_mfma_f32_32x32x16_bf16 v[80:95], v[140:143], v[116:119], v[80:95]\n\t"
        "v_cmp_gt_u32 vcc, s47, v126\n\t"
        "ds_read_b64_tr_b16 v[116:117], v130 offset:6400\n\t"
        "ds_read_b64_tr_b16 v[118:119], v130 offset:9472\n\t"
        "s_waitcnt vmcnt(14) lgkmcnt(10)\n\t"
        "v_mfma_f32_32x32x16_bf16 v[0:15], v[96:99], v[144:147], v[0:15]\n\t"
        "s_cbranch_vccnz 30f\n\t"
        "31:\n\t"
        "ds_read_b64_tr_b16 v[96:97], v123 offset:0\n\t"
        "ds_read_b64_tr_b16 v[98:99], v123 offset:3072\n\t"
        "s_waitcnt lgkmcnt(10)\n\t"
        "v_mfma_f32_32x32x16_bf16 v[16:31], v[100:103], v[144:147], v[16:31]\n\t"
        "v_add_u32 v128, 1, v128\n\t"
        "ds_read_b64_tr_b16 v[100:101], v124 offset:0\n\t"
        "ds_read_b64_tr_b16 v[102:103], v124 offset:3072\n\t"
        "s_waitcnt lgkmcnt(10)\n\t"
        "v_mfma_f32_32x32x16_bf16 v[32:47], v[104:107], v[144:147], v[32:47]\n\t"
        "ds_read_b64_tr_b16 v[104:105], v123 offset:128\n\t"
        "ds_read_b64_tr_b16 v[106:107], v123 offset:3200\n\t"
        "buffer_load_dwordx4 v[140:143], v120, s[40:43], s44 offen offset:2048 sc1\n\t"
        "s_waitcnt lgkmcnt(10)\n\t"
        "v_mfma_f32_32x32x16_bf16 v[48:63], v[108:111], v[144:147], v[48:63]\n\t"
        "s_add_u32 s46, s46, 12288\n\t"
        "s_add_u32 s47, s47, 1\n\t"
        "ds_read_b64_tr_b16 v[108:109], v124 offset:128\n\t"
        "ds_read_b64_tr_b16 v[110:111], v124 offset:3200\n\t"
        "s_waitcnt lgkmcnt(10)\n\t"
        "v_mfma_f32_32x32x16_bf16 v[64:79], v[144:147], v[112:115], v[64:79]\n\t"
        "s_cmp_eq_u32 s46, 110592\n\t"
        "s_cselect_b32 s46, 0, s46\n\t"
        "ds_read_b64_tr_b16 v[112:113], v123 offset:256\n\t"
        "ds_read_b64_tr_b16 v[114:115], v123 offset:3328\n\t"
        "s_waitcnt lgkmcnt(10)\n\t"
        "v_mfma_f32_32x32x16_bf16 v[80:95], v[144:147], v[116:119], v[80:95]\n\t"
        "ds_write_b32 v127, v128\n\t"
        "ds_read_b64_tr_b16 v[116:117], v124 offset:256\n\t"
        "ds_read_b64_tr_b16 v[118:119], v124 offset:3328\n\t"
        "s_sub_u32 s45, s45, 1\n\t"
        "s_cmp_lg_u32 s45, 0\n\t"
        "s_cbranch_scc1 1b\n\t"
        "s_waitcnt vmcnt(14) lgkmcnt(11)\n\t"
        "v_mfma_f32_32x32x16_bf16 v[0:15], v[96:99], v[148:151], v[0:15]\n\t"
        "ds_read_b32 v126, v125\n\t"
        "ds_read_b64_tr_b16 v[96:97], v123 offset:6144\n\t"
        "ds_read_b64_tr_b16 v[98:99], v123 offset:9216\n\t"
        "s_waitcnt lgkmcnt(12)\n\t"
        "v_mfma_f32_32x32x16_bf16 v[16:31], v[100:103], v[148:151], v[16:31]\n\t"
        "v_add_u32 v129, s46, v121\n\t"
        "ds_read_b64_tr_b16 v[100:101], v124 offset:6144\n\t"
        "ds_read_b64_tr_b16 v[102:103], v124 offset:9216\n\t"
        "s_waitcnt lgkmcnt(12)\n\t"
        "v_mfma_f32_32x32x16_bf16 v[32:47], v[104:107], v[148:151], v[32:47]\n\t"
        "ds_read_b64_tr_b16 v[104:105], v123 offset:6272\n\t"
        "ds_read_b64_tr_b16 v[106:107], v123 offset:9344\n\t"
        "s_waitcnt lgkmcnt(12)\n\t"
        "v_mfma_f32_32x32x16_bf16 v[48:63], v[108:111], v[148:151], v[48:63]\n\t"
        "v_add_u32 v130, s46, v122\n\t"
        "ds_read_b64_tr_b16 v[108:109], v124 offset:6272\n\t"
        "ds_read_b64_tr_b16 v[110:111], v124 offset:9344\n\t"
        "s_waitcnt lgkmcnt(12)\n\t"
        "v_mfma_f32_32x32x16_bf16 v[64:79], v[148:151], v[112:115], v[64:79]\n\t"
        "ds_read_b64_tr_b16 v[112:113], v123 offset:6400\n\t"
        "ds_read_b64_tr_b16 v[114:115], v123 offset:9472\n\t"
        "s_waitcnt lgkmcnt(10)\n\t"
        "v_mfma_f32_32x32x16_bf16 v[80:95], v[148:151], v[116:119], v[80:95]\n\t"
        "v_cmp_gt_u32 vcc, s47, v126\n\t"
        "ds_read_b64_tr_b16 v[116:117], v124 offset:6400\n\t"
        "ds_read_b64_tr_b16 v[118:119], v124 offset:9472\n\t"
        "s_waitcnt vmcnt(13) lgkmcnt(10)\n\t"
        "v_mfma_f32_32x32x16_bf16 v[0:15], v[96:99], v[152:155], v[0:15]\n\t"
        "s_cbranch_vccnz 64f\n\t"
        "65:\n\t"
        "ds_read_b64_tr_b16 v[96:97], v129 offset:0\n\t"
        "ds_read_b64_tr_b16 v[98:99], v129 offset:3072\n\t"
        "s_waitcnt lgkmcnt(10)\n\t"
        "v_mfma_f32_32x32x16_bf16 v[16:31], v[100:103], v[152:155], v[16:31]\n\t"
        "v_add_u32 v128, 1, v128\n\t"
        "ds_read_b64_tr_b16 v[100:101], v130 offset:0\n\t"
        "ds_read_b64_tr_b16 v[102:103], v130 offset:3072\n\t"
        "s_waitcnt lgkmcnt(10)\n\t"
        "v_mfma_f32_32x32x16_bf16 v[32:47], v[104:107], v[152:155], v[32:47]\n\t"
        "ds_read_b64_tr_b16 v[104:105], v129 offset:128\n\t"
        "ds_read_b64_tr_b16 v[106:107], v129 offset:3200\n\t"
        "s_waitcnt lgkmcnt(10)\n\t"
        "v_mfma_f32_32x32x16_bf16 v[48:63], v[108:111], v[152:155], v[48:63]\n\t"
        "s_add_u32 s46, s46, 12288\n\t"
        "s_add_u32 s47, s47, 1\n\t"
        "ds_read_b64_tr_b16 v[108:109], v130 offset:128\n\t"
        "ds_read_b64_tr_b16 v[110:111], v130 offset:3200\n\t"
        "s_waitcnt lgkmcnt(10)\n\t"
        "v_mfma_f32_32x32x16_bf16 v[64:79], v[152:155], v[112:115], v[64:79]\n\t"
        "s_cmp_eq_u32 s46, 110592\n\t"
        "s_cselect_b32 s46, 0, s46\n\t"
        "ds_read_b64_tr_b16 v[112:113], v129 offset:256\n\t"
        "ds_read_b64_tr_b16 v[114:115], v129 offset:3328\n\t"
        "s_waitcnt lgkmcnt(10)\n\t"
        "v_mfma_f32_32x32x16_bf16 v[80:95], v[152:155], v[116:119], v[80:95]\n\t"
        "ds_write_b32 v127, v128\n\t"
        "ds_read_b64_tr_b16 v[116:117], v130 offset:256\n\t"
        "ds_read_b64_tr_b16 v[118:119], v130 offset:3328\n\t"
        "s_waitcnt vmcnt(12) lgkmcnt(11)\n\t"
        "v_mfma_f32_32x32x16_bf16 v[0:15], v[96:99], v[156:159], v[0:15]\n\t"
        "ds_read_b32 v126, v125\n\t"
        "ds_read_b64_tr_b16 v[96:97], v129 offset:6144\n\t"
        "ds_read_b64_tr_b16 v[98:99], v129 offset:9216\n\t"
        "s_waitcnt lgkmcnt(12)\n\t"
        "v_mfma_f32_32x32x16_bf16 v[16:31], v[100:103], v[156:159], v[16:31]\n\t"
        "v_add_u32 v123, s46, v121\n\t"
        "ds_read_b64_tr_b16 v[100:101], v130 offset:6144\n\t"
        "ds_read_b64_tr_b16 v[102:103], v130 offset:9216\n\t"
        "s_waitcnt lgkmcnt(12)\n\t"
        "v_mfma_f32_32x32x16_bf16 v[32:47], v[104:107], v[156:159], v[32:47]\n\t"
        "ds_read_b64_tr_b16 v[104:105], v129 offset:6272\n\t"
        "ds_read_b64_tr_b16 v[106:107], v129 offset:9344\n\t"
        "s_waitcnt lgkmcnt(12)\n\t"
        "v_mfma_f32_32x32x16_bf16 v[48:63], v[108:111], v[156:159], v[48:63]\n\t"
        "v_add_u32 v124, s46, v122\n\t"
        "ds_read_b64_tr_b16 v[108:109], v130 offset:6272\n\t"
        "ds_read_b64_tr_b16 v[110:111], v130 offset:9344\n\t"
        "s_waitcnt lgkmcnt(12)\n\t"
        "v_mfma_f32_32x32x16_bf16 v[64:79], v[156:159], v[112:115], v[64:79]\n\t"
        "ds_read_b64_tr_b16 v[112:113], v129 offset:6400\n\t"
        "ds_read_b64_tr_b16 v[114:115], v129 offset:9472\n\t"
        "s_waitcnt lgkmcnt(10)\n\t"
        "v_mfma_f32_32x32x16_bf16 v[80:95], v[156:159], v[116:119], v[80:95]\n\t"
        "v_cmp_gt_u32 vcc, s47, v126\n\t"
        "ds_read_b64_tr_b16 v[116:117], v130 offset:6400\n\t"
        "ds_read_b64_tr_b16 v[118:119], v130 offset:9472\n\t"
        "s_waitcnt vmcnt(11) lgkmcnt(10)\n\t"
        "v_mfma_f32_32x32x16_bf16 v[0:15], v[96:99], v[160:163], v[0:15]\n\t"
        "s_cbranch_vccnz 66f\n\t"
        "67:\n\t"
        "ds_read_b64_tr_b16 v[96:97], v123 offset:0\n\t"
        "ds_read_b64_tr_b16 v[98:99], v123 offset:3072\n\t"
        "s_waitcnt lgkmcnt(10)\n\t"
        "v_mfma_f32_32x32x16_bf16 v[16:31], v[100:103], v[160:163], v[16:31]\n\t"
        "v_add_u32 v128, 1, v128\n\t"
        "ds_read_b64_tr_b16 v[100:101], v124 offset:0\n\t"
        "ds_read_b64_tr_b16 v[102:103], v124 offset:3072\n\t"
        "s_waitcnt lgkmcnt(10)\n\t"
        "v_mfma_f32_32x32x16_bf16 v[32:47], v[104:107], v[160:163], v[32:47]\n\t"
        "ds_read_b64_tr_b16 v[104:105], v123 offset:128\n\t"
        "ds_read_b64_tr_b16 v[106:107], v123 offset:3200\n\t"
        "s_waitcnt lgkmcnt(10)\n\t"
        "v_mfma_f32_32x32x16_bf16 v[48:63], v[108:111], v[160:163], v[48:63]\n\t"
        "s_add_u32 s46, s46, 12288\n\t"
        "s_add_u32 s47, s47, 1\n\t"
        "ds_read_b64_tr_b16 v[108:109], v124 offset:128\n\t"
        "ds_read_b64_tr_b16 v[110:111], v124 offset:3200\n\t"
        "s_waitcnt lgkmcnt(10)\n\t"
        "v_mfma_f32_32x32x16_bf16 v[64:79], v[160:163], v[112:115], v[64:79]\n\t"
        "s_cmp_eq_u32 s46, 110592\n\t"
        "s_cselect_b32 s46, 0, s46\n\t"
        "ds_read_b64_tr_b16 v[112:113], v123 offset:256\n\t"
        "ds_read_b64_tr_b16 v[114:115], v123 offset:3328\n\t"
        "s_waitcnt lgkmcnt(10)\n\t"
        "v_mfma_f32_32x32x16_bf16 v[80:95], v[160:163], v[116:119], v[80:95]\n\t"
        "ds_write_b32 v127, v128\n\t"
        "ds_read_b64_tr_b16 v[116:117], v124 offset:256\n\t"
        "ds_read_b64_tr_b16 v[118:119], v124 offset:3328\n\t"
        "s_waitcnt vmcnt(10) lgkmcnt(11)\n\t"
        "v_mfma_f32_32x32x16_bf16 v[0:15], v[96:99], v[164:167], v[0:15]\n\t"
        "ds_read_b32 v126, v125\n\t"
        "ds_read_b64_tr_b16 v[96:97], v123 offset:6144\n\t"
        "ds_read_b64_tr_b16 v[98:99], v123 offset:9216\n\t"
        "s_waitcnt lgkmcnt(12)\n\t"
        "v_mfma_f32_32x32x16_bf16 v[16:31], v[100:103], v[164:167], v[16:31]\n\t"
        "v_add_u32 v129, s46, v121\n\t"
        "ds_read_b64_tr_b16 v[100:101], v124 offset:6144\n\t"
        "ds_read_b64_tr_b16 v[102:103], v124 offset:9216\n\t"
        "s_waitcnt lgkmcnt(12)\n\t"
        "v_mfma_f32_32x32x16_bf16 v[32:47], v[104:107], v[164:167], v[32:47]\n\t"
        "ds_read_b64_tr_b16 v[104:105], v123 offset:6272\n\t"
        "ds_read_b64_tr_b16 v[106:107], v123 offset:9344\n\t"
        "s_waitcnt lgkmcnt(12)\n\t"
        "v_mfma_f32_32x32x16_bf16 v[48:63], v[108:111], v[164:167], v[48:63]\n\t"
        "v_add_u32 v130, s46, v122\n\t"
        "ds_read_b64_tr_b16 v[108:109], v124 offset:6272\n\t"
        "ds_read_b64_tr_b16 v[110:111], v124 offset:9344\n\t"
        "s_waitcnt lgkmcnt(12)\n\t"
        "v_mfma_f32_32x32x16_bf16 v[64:79], v[164:167], v[112:115], v[64:79]\n\t"
        "ds_read_b64_tr_b16 v[112:113], v123 offset:6400\n\t"
        "ds_read_b64_tr_b16 v[114:115], v123 offset:9472\n\t"
        "s_waitcnt lgkmcnt(10)\n\t"
        "v_mfma_f32_32x32x16_bf16 v[80:95], v[164:167], v[116:119], v[80:95]\n\t"
        "v_cmp_gt_u32 vcc, s47, v126\n\t"
        "ds_read_b64_tr_b16 v[116:117], v124 offset:6400\n\t"
        "ds_read_b64_tr_b16 v[118:119], v124 offset:9472\n\t"
        "s_waitcnt vmcnt(9) lgkmcnt(10)\n\t"
        "v_mfma_f32_32x32x16_bf16 v[0:15], v[96:99], v[168:171], v[0:15]\n\t"
        "s_cbranch_vccnz 68f\n\t"
        "69:\n\t"
        "ds_read_b64_tr_b16 v[96:97], v129 offset:0\n\t"
        "ds_read_b64_tr_b16 v[98:99], v129 offset:3072\n\t"
        "s_waitcnt lgkmcnt(10)\n\t"
        "v_mfma_f32_32x32x16_bf16 v[16:31], v[100:103], v[168:171], v[16:31]\n\t"
        "v_add_u32 v128, 1, v128\n\t"
        "ds_read_b64_tr_b16 v[100:101], v130 offset:0\n\t"
        "ds_read_b64_tr_b16 v[102:103], v130 offset:3072\n\t"
        "s_waitcnt lgkmcnt(10)\n\t"
        "v_mfma_f32_32x32x16_bf16 v[32:47], v[104:107], v[168:171], v[32:47]\n\t"
        "ds_read_b64_tr_b16 v[104:105], v129 offset:128\n\t"
        "ds_read_b64_tr_b16 v[106:107], v129 offset:3200\n\t"
        "s_waitcnt lgkmcnt(10)\n\t"
        "v_mfma_f32_32x32x16_bf16 v[48:63], v[108:111], v[168:171], v[48:63]\n\t"
        "s_add_u32 s46, s46, 12288\n\t"
        "s_add_u32 s47, s47, 1\n\t"
        "ds_read_b64_tr_b16 v[108:109], v130 offset:128\n\t"
        "ds_read_b64_tr_b16 v[110:111], v130 offset:3200\n\t"
        "s_waitcnt lgkmcnt(10)\n\t"
        "v_mfma_f32_32x32x16_bf16 v[64:79], v[168:171], v[112:115], v[64:79]\n\t"
        "s_cmp_eq_u32 s46, 110592\n\t"
        "s_cselect_b32 s46, 0, s46\n\t"
        "ds_read_b64_tr_b16 v[112:113], v129 offset:256\n\t"
        "ds_read_b64_tr_b16 v[114:115], v129 offset:3328\n\t"
        "s_waitcnt lgkmcnt(10)\n\t"
        "v_mfma_f32_32x32x16_bf16 v[80:95], v[168:171], v[116:119], v[80:95]\n\t"
        "ds_write_b32 v127, v128\n\t"
        "ds_read_b64_tr_b16 v[116:117], v130 offset:256\n\t"
        "ds_read_b64_tr_b16 v[118:119], v130 offset:3328\n\t"
        "s_waitcnt vmcnt(8) lgkmcnt(11)\n\t"
        "v_mfma_f32_32x32x16_bf16 v[0:15], v[96:99], v[172:175], v[0:15]\n\t"
        "ds_read_b32 v126, v125\n\t"
        "ds_read_b64_tr_b16 v[96:97], v129 offset:6144\n\t"
        "ds_read_b64_tr_b16 v[98:99], v129 offset:9216\n\t"
        "s_waitcnt lgkmcnt(12)\n\t"
        "v_mfma_f32_32x32x16_bf16 v[16:31], v[100:103], v[172:175], v[16:31]\n\t"
        "v_add_u32 v123, s46, v121\n\t"
        "ds_read_b64_tr_b16 v[100:101], v130 offset:6144\n\t"
        "ds_read_b64_tr_b16 v[102:103], v130 offset:9216\n\t"
        "s_waitcnt lgkmcnt(12)\n\t"
        "v_mfma_f32_32x32x16_bf16 v[32:47], v[104:107], v[172:175], v[32:47]\n\t"
        "ds_read_b64_tr_b16 v[104:105], v129 offset:6272\n\t"
        "ds_read_b64_tr_b16 v[106:107], v129 offset:9344\n\t"
        "s_waitcnt lgkmcnt(12)\n\t"
        "v_mfma_f32_32x32x16_bf16 v[48:63], v[108:111], v[172:175], v[48:63]\n\t"
        "v_add_u32 v124, s46, v122\n\t"
        "ds_read_b64_tr_b16 v[108:109], v130 offset:6272\n\t"
        "ds_read_b64_tr_b16 v[110:111], v130 offset:9344\n\t"
        "s_waitcnt lgkmcnt(12)\n\t"
        "v_mfma_f32_32x32x16_bf16 v[64:79], v[172:175], v[112:115], v[64:79]\n\t"
        "ds_read_b64_tr_b16 v[112:113], v129 offset:6400\n\t"
        "ds_read_b64_tr_b16 v[114:115], v129 offset:9472\n\t"
        "s_waitcnt lgkmcnt(10)\n\t"
        "v_mfma_f32_32x32x16_bf16 v[80:95], v[172:175], v[116:119], v[80:95]\n\t"
        "v_cmp_gt_u32 vcc, s47, v126\n\t"
        "ds_read_b64_tr_b16 v[116:117], v130 offset:6400\n\t"
        "ds_read_b64_tr_b16 v[118:119], v130 offset:9472\n\t"
        "s_waitcnt vmcnt(7) lgkmcnt(10)\n\t"
        "v_mfma_f32_32x32x16_bf16 v[0:15], v[96:99], v[176:179], v[0:15]\n\t"
        "s_cbranch_vccnz 70f\n\t"
        "71:\n\t"
        "ds_read_b64_tr_b16 v[96:97], v123 offset:0\n\t"
        "ds_read_b64_tr_b16 v[98:99], v123 offset:3072\n\t"
        "s_waitcnt lgkmcnt(10)\n\t"
        "v_mfma_f32_32x32x16_bf16 v[16:31], v[100:103], v[176:179], v[16:31]\n\t"
        "v_add_u32 v128, 1, v128\n\t"
        "ds_read_b64_tr_b16 v[100:101], v124 offset:0\n\t"
        "ds_read_b64_tr_b16 v[102:103], v124 offset:3072\n\t"
        "s_waitcnt lgkmcnt(10)\n\t"
        "v_mfma_f32_32x32x16_bf16 v[32:47], v[104:107], v[176:179], v[32:47]\n\t"
        "ds_read_b64_tr_b16 v[104:105], v123 offset:128\n\t"
        "ds_read_b64_tr_b16 v[106:107], v123 offset:3200\n\t"
        "s_waitcnt lgkmcnt(10)\n\t"
        "v_mfma_f32_32x32x16_bf16 v[48:63], v[108:111], v[176:179], v[48:63]\n\t"
        "s_add_u32 s46, s46, 12288\n\t"
        "s_add_u32 s47, s47, 1\n\t"
        "ds_read_b64_tr_b16 v[108:109], v124 offset:128\n\t"
        "ds_read_b64_tr_b16 v[110:111], v124 offset:3200\n\t"
        "s_waitcnt lgkmcnt(10)\n\t"
        "v_mfma_f32_32x32x16_bf16 v[64:79], v[176:179], v[112:115], v[64:79]\n\t"
        "s_cmp_eq_u32 s46, 110592\n\t"
        "s_cselect_b32 s46, 0, s46\n\t"
        "ds_read_b64_tr_b16 v[112:113], v123 offset:256\n\t"
        "ds_read_b64_tr_b16 v[114:115], v123 offset:3328\n\t"
        "s_waitcnt lgkmcnt(10)\n\t"
        "v_mfma_f32_32x32x16_bf16 v[80:95], v[176:179], v[116:119], v[80:95]\n\t"
        "ds_write_b32 v127, v128\n\t"
        "ds_read_b64_tr_b16 v[116:117], v124 offset:256\n\t"
        "ds_read_b64_tr_b16 v[118:119], v124 offset:3328\n\t"
        "s_waitcnt vmcnt(6) lgkmcnt(11)\n\t"
        "v_mfma_f32_32x32x16_bf16 v[0:15], v[96:99], v[180:183], v[0:15]\n\t"
        "ds_read_b32 v126, v125\n\t"
        "ds_read_b64_tr_b16 v[96:97], v123 offset:6144\n\t"
        "ds_read_b64_tr_b16 v[98:99], v123 offset:9216\n\t"
        "s_waitcnt lgkmcnt(12)\n\t"
        "v_mfma_f32_32x32x16_bf16 v[16:31], v[100:103], v[180:183], v[16:31]\n\t"
        "v_add_u32 v129, s46, v121\n\t"
        "ds_read_b64_tr_b16 v[100:101], v124 offset:6144\n\t"
        "ds_read_b64_tr_b16 v[102:103], v124 offset:9216\n\t"
        "s_waitcnt lgkmcnt(12)\n\t"
        "v_mfma_f32_32x32x16_bf16 v[32:47], v[104:107], v[180:183], v[32:47]\n\t"
        "ds_read_b64_tr_b16 v[104:105], v123 offset:6272\n\t"
        "ds_read_b64_tr_b16 v[106:107], v123 offset:9344\n\t"
        "s_waitcnt lgkmcnt(12)\n\t"
        "v_mfma_f32_32x32x16_bf16 v[48:63], v[108:111], v[180:183], v[48:63]\n\t"
        "v_add_u32 v130, s46, v122\n\t"
        "ds_read_b64_tr_b16 v[108:109], v124 offset:6272\n\t"
        "ds_read_b64_tr_b16 v[110:111], v124 offset:9344\n\t"
        "s_waitcnt lgkmcnt(12)\n\t"
        "v_mfma_f32_32x32x16_bf16 v[64:79], v[180:183], v[112:115], v[64:79]\n\t"
        "ds_read_b64_tr_b16 v[112:113], v123 offset:6400\n\t"
        "ds_read_b64_tr_b16 v[114:115], v123 offset:9472\n\t"
        "s_waitcnt lgkmcnt(10)\n\t"
        "v_mfma_f32_32x32x16_bf16 v[80:95], v[180:183], v[116:119], v[80:95]\n\t"
        "v_cmp_gt_u32 vcc, s47, v126\n\t"
        "ds_read_b64_tr_b16 v[116:117], v124 offset:6400\n\t"
        "ds_read_b64_tr_b16 v[118:119], v124 offset:9472\n\t"
        "s_waitcnt vmcnt(5) lgkmcnt(10)\n\t"
        "v_mfma_f32_32x32x16_bf16 v[0:15], v[96:99], v[184:187], v[0:15]\n\t"
        "s_cbranch_vccnz 72f\n\t"
        "73:\n\t"
        "ds_read_b64_tr_b16 v[96:97], v129 offset:0\n\t"
        "ds_read_b64_tr_b16 v[98:99], v129 offset:3072\n\t"
        "s_waitcnt lgkmcnt(10)\n\t"
        "v_mfma_f32_32x32x16_bf16 v[16:31], v[100:103], v[184:187], v[16:31]\n\t"
        "v_add_u32 v128, 1, v128\n\t"
        "ds_read_b64_tr_b16 v[100:101], v130 offset:0\n\t"
        "ds_read_b64_tr_b16 v[102:103], v130 offset:3072\n\t"
        "s_waitcnt lgkmcnt(10)\n\t"
        "v_mfma_f32_32x32x16_bf16 v[32:47], v[104:107], v[184:187], v[32:47]\n\t"
        "ds_read_b64_tr_b16 v[104:105], v129 offset:128\n\t"
        "ds_read_b64_tr_b16 v[106:107], v129 offset:3200\n\t"
        "s_waitcnt lgkmcnt(10)\n\t"
        "v_mfma_f32_32x32x16_bf16 v[48:63], v[108:111], v[184:187], v[48:63]\n\t"
        "s_add_u32 s46, s46, 12288\n\t"
        "s_add_u32 s47, s47, 1\n\t"
        "ds_read_b64_tr_b16 v[108:109], v130 offset:128\n\t"
        "ds_read_b64_tr_b16 v[110:111], v130 offset:3200\n\t"
        "s_waitcnt lgkmcnt(10)\n\t"
        "v_mfma_f32_32x32x16_bf16 v[64:79], v[184:187], v[112:115], v[64:79]\n\t"
        "s_cmp_eq_u32 s46, 110592\n\t"
        "s_cselect_b32 s46, 0, s46\n\t"
        "ds_read_b64_tr_b16 v[112:113], v129 offset:256\n\t"
        "ds_read_b64_tr_b16 v[114:115], v129 offset:3328\n\t"
        "s_waitcnt lgkmcnt(10)\n\t"
        "v_mfma_f32_32x32x16_bf16 v[80:95], v[184:187], v[116:119], v[80:95]\n\t"
        "ds_write_b32 v127, v128\n\t"
        "ds_read_b64_tr_b16 v[116:117], v130 offset:256\n\t"
        "ds_read_b64_tr_b16 v[118:119], v130 offset:3328\n\t"
        "s_waitcnt vmcnt(4) lgkmcnt(11)\n\t"
        "v_mfma_f32_32x32x16_bf16 v[0:15], v[96:99], v[188:191], v[0:15]\n\t"
        "ds_read_b64_tr_b16 v[96:97], v129 offset:6144\n\t"
        "ds_read_b64_tr_b16 v[98:99], v129 offset:9216\n\t"
        "s_waitcnt lgkmcnt(11)\n\t"
        "v_mfma_f32_32x32x16_bf16 v[16:31], v[100:103], v[188:191], v[16:31]\n\t"
        "ds_read_b64_tr_b16 v[100:101], v130 offset:6144\n\t"
        "ds_read_b64_tr_b16 v[102:103], v130 offset:9216\n\t"
        "s_waitcnt lgkmcnt(11)\n\t"
        "v_mfma_f32_32x32x16_bf16 v[32:47], v[104:107], v[188:191], v[32:47]\n\t"
        "ds_read_b64_tr_b16 v[104:105], v129 offset:6272\n\t"
        "ds_read_b64_tr_b16 v[106:107], v129 offset:9344\n\t"
        "s_waitcnt lgkmcnt(11)\n\t"
        "v_mfma_f32_32x32x16_bf16 v[48:63], v[108:111], v[188:191], v[48:63]\n\t"
        "ds_read_b64_tr_b16 v[108:109], v130 offset:6272\n\t"
        "ds_read_b64_tr_b16 v[110:111], v130 offset:9344\n\t"
        "s_waitcnt lgkmcnt(11)\n\t"
        "v_mfma_f32_32x32x16_bf16 v[64:79], v[188:191], v[112:115], v[64:79]\n\t"
        "ds_read_b64_tr_b16 v[112:113], v129 offset:6400\n\t"
        "ds_read_b64_tr_b16 v[114:115], v129 offset:9472\n\t"
        "s_waitcnt lgkmcnt(10)\n\t"
        "v_mfma_f32_32x32x16_bf16 v[80:95], v[188:191], v[116:119], v[80:95]\n\t"
        "ds_read_b64_tr_b16 v[116:117], v130 offset:6400\n\t"
        "ds_read_b64_tr_b16 v[118:119], v130 offset:9472\n\t"
        "s_waitcnt vmcnt(3) lgkmcnt(10)\n\t"
        "v_mfma_f32_32x32x16_bf16 v[0:15], v[96:99], v[192:195], v[0:15]\n\t"
        "s_waitcnt lgkmcnt(8)\n\t"
        "v_mfma_f32_32x32x16_bf16 v[16:31], v[100:103], v[192:195], v[16:31]\n\t"
        "s_waitcnt lgkmcnt(6)\n\t"
        "v_mfma_f32_32x32x16_bf16 v[32:47], v[104:107], v[192:195], v[32:47]\n\t"
        "s_waitcnt lgkmcnt(4)\n\t"
        "v_mfma_f32_32x32x16_bf16 v[48:63], v[108:111], v[192:195], v[48:63]\n\t"
        "s_waitcnt lgkmcnt(2)\n\t"
        "v_mfma_f32_32x32x16_bf16 v[64:79], v[192:195], v[112:115], v[64:79]\n\t"
        "s_waitcnt lgkmcnt(0)\n\t"
        "v_mfma_f32_32x32x16_bf16 v[80:95], v[192:195], v[116:119], v[80:95]\n\t"
        "s_waitcnt vmcnt(0)\n\t"
        "s_nop 10\n\t"
        "s_branch 9f\n\t"
        "16:\n\t"
        "s_mov_b32 s48, 0x10000\n\t"
        "8:\n\t"
        "ds_read_b32 v126, v125\n\t"
        "s_sub_u32 s48, s48, 1\n\t"
        "s_waitcnt lgkmcnt(0)\n\t"
        "s_cbranch_scc1 17b\n\t"
        "v_cmp_gt_u32 vcc, s47, v126\n\t"
        "s_cbranch_vccnz 8b\n\t"
        "s_branch 17b\n\t"
        "18:\n\t"
        "s_mov_b32 s48, 0x10000\n\t"
        "8:\n\t"
        "ds_read_b32 v126, v125\n\t"
        "s_sub_u32 s48, s48, 1\n\t"
        "s_waitcnt lgkmcnt(0)\n\t"
        "s_cbranch_scc1 19b\n\t"
        "v_cmp_gt_u32 vcc, s47, v126\n\t"
        "s_cbranch_vccnz 8b\n\t"
        "s_branch 19b\n\t"
        "20:\n\t"
        "s_mov_b32 s48, 0x10000\n\t"
        "8:\n\t"
        "ds_read_b32 v126, v125\n\t"
        "s_sub_u32 s48, s48, 1\n\t"
        "s_waitcnt lgkmcnt(0)\n\t"
        "s_cbranch_scc1 21b\n\t"
        "v_cmp_gt_u32 vcc, s47, v126\n\t"
        "s_cbranch_vccnz 8b\n\t"
        "s_branch 21b\n\t"
        "22:\n\t"
        "s_mov_b32 s48, 0x10000\n\t"
        "8:\n\t"
        "ds_read_b32 v126, v125\n\t"
        "s_sub_u32 s48, s48, 1\n\t"
        "s_waitcnt lgkmcnt(0)\n\t"
        "s_cbranch_scc1 23b\n\t"
        "v_cmp_gt_u32 vcc, s47, v126\n\t"
        "s_cbranch_vccnz 8b\n\t"
        "s_branch 23b\n\t"
        "24:\n\t"
        "s_mov_b32 s48, 0x10000\n\t"
        "8:\n\t"
        "ds_read_b32 v126, v125\n\t"
        "s_sub_u32 s48, s48, 1\n\t"
        "s_waitcnt lgkmcnt(0)\n\t"
        "s_cbranch_scc1 25b\n\t"
        "v_cmp_gt_u32 vcc, s47, v126\n\t"
        "s_cbranch_vccnz 8b\n\t"
        "s_branch 25b\n\t"
        "26:\n\t"
        "s_mov_b32 s48, 0x10000\n\t"
        "8:\n\t"
        "ds_read_b32 v126, v125\n\t"
        "s_sub_u32 s48, s48, 1\n\t"
        "s_waitcnt lgkmcnt(0)\n\t"
        "s_cbranch_scc1 27b\n\t"
        "v_cmp_gt_u32 vcc, s47, v126\n\t"
        "s_cbranch_vccnz 8b\n\t"
        "s_branch 27b\n\t"
        "28:\n\t"
        "s_mov_b32 s48, 0x10000\n\t"
        "8:\n\t"
        "ds_read_b32 v126, v125\n\t"
        "s_sub_u32 s48, s48, 1\n\t"
        "s_waitcnt lgkmcnt(0)\n\t"
        "s_cbranch_scc1 29b\n\t"
        "v_cmp_gt_u32 vcc, s47, v126\n\t"
        "s_cbranch_vccnz 8b\n\t"
        "s_branch 29b\n\t"
        "30:\n\t"
        "s_mov_b32 s48, 0x10000\n\t"
        "8:\n\t"
        "ds_read_b32 v126, v125\n\t"
        "s_sub_u32 s48, s48, 1\n\t"
        "s_waitcnt lgkmcnt(0)\n\t"
        "s_cbranch_scc1 31b\n\t"
        "v_cmp_gt_u32 vcc, s47, v126\n\t"
        "s_cbranch_vccnz 8b\n\t"
        "s_branch 31b\n\t"
        "64:\n\t"
        "s_mov_b32 s48, 0x10000\n\t"
        "8:\n\t"
        "ds_read_b32 v126, v125\n\t"
        "s_sub_u32 s48, s48, 1\n\t"
        "s_waitcnt lgkmcnt(0)\n\t"
        "s_cbranch_scc1 65b\n\t"
        "v_cmp_gt_u32 vcc, s47, v126\n\t"
        "s_cbranch_vccnz 8b\n\t"
        "s_branch 65b\n\t"
        "66:\n\t"
        "s_mov_b32 s48, 0x10000\n\t"
        "8:\n\t"
        "ds_read_b32 v126, v125\n\t"
        "s_sub_u32 s48, s48, 1\n\t"
        "s_waitcnt lgkmcnt(0)\n\t"
        "s_cbranch_scc1 67b\n\t"
        "v_cmp_gt_u32 vcc, s47, v126\n\t"
        "s_cbranch_vccnz 8b\n\t"
        "s_branch 67b\n\t"
        "68:\n\t"
        "s_mov_b32 s48, 0x10000\n\t"
        "8:\n\t"
        "ds_read_b32 v126, v125\n\t"
        "s_sub_u32 s48, s48, 1\n\t"
        "s_waitcnt lgkmcnt(0)\n\t"
        "s_cbranch_scc1 69b\n\t"
        "v_cmp_gt_u32 vcc, s47, v126\n\t"
        "s_cbranch_vccnz 8b\n\t"
        "s_branch 69b\n\t"
        "70:\n\t"
        "s_mov_b32 s48, 0x10000\n\t"
        "8:\n\t"
        "ds_read_b32 v126, v125\n\t"
        "s_sub_u32 s48, s48, 1\n\t"
        "s_waitcnt lgkmcnt(0)\n\t"
        "s_cbranch_scc1 71b\n\t"
        "v_cmp_gt_u32 vcc, s47, v126\n\t"
        "s_cbranch_vccnz 8b\n\t"
        "s_branch 71b\n\t"
        "72:\n\t"
        "s_mov_b32 s48, 0x10000\n\t"
        "8:\n\t"
        "ds_read_b32 v126, v125\n\t"
        "s_sub_u32 s48, s48, 1\n\t"
        "s_waitcnt lgkmcnt(0)\n\t"
        "s_cbranch_scc1 73b\n\t"
        "v_cmp_gt_u32 vcc, s47, v126\n\t"
        "s_cbranch_vccnz 8b\n\t"
        "s_branch 73b\n\t"
        "9:\n\t"
        : "+{v[0:15]}"(acc[0]), "+{v[16:31]}"(acc[1]), "+{v[32:47]}"(acc[2]), "+{v[48:63]}"(acc[3]), "+{v[64:79]}"(acc[4]), "+{v[80:95]}"(acc[5]),
          "+{v[96:111]}"(stA), "+{v[112:127]}"(stB), "+{v[128:131]}"(stC), "+{v[148:163]}"(xr1), "+{v[164:179]}"(xr2), "+{v[180:195]}"(xr3),
          "+{s44}"(soff), "+{s46}"(slot), "+{s47}"(need)
        : "{s[40:43]}"(srd) : "v132", "v133", "v134", "v135", "v136", "v137", "v138", "v139", "v140", "v141", "v142", "v143", "v144", "v145", "v146", "v147", "s45", "s48", "vcc", "scc", "memory");
      STAMPV(4 + 16 * L);
      PHASE_VARS;
      if (L == 1) {
        f32x4 av[4], bv[4], cs[4][4];
        float cv[2];
#pragma unroll
        for (int g = 0; g < 4; ++g) {
          av[g] = *reinterpret_cast<const f32x4*>(smem + L_AB + wid * 256 + (8 * g + 4 * hi) * 4);
          bv[g] = *reinterpret_cast<const f32x4*>(smem + L_AB + wid * 256 + 128 + (8 * g + 4 * hi) * 4);
        }
#pragma unroll
        for (int j = 5; j >= 0; --j) {
          const int cb = (j >> 1) * 64 + ((j & 1) ^ hh) * 32;
          if (j >= 4) cv[j - 4] = *reinterpret_cast<const float*>(smem + L_CS + (cb + r32) * 4);
          else {
#pragma unroll
            for (int g = 0; g < 4; ++g) cs[j][g] = *reinterpret_cast<const f32x4*>(smem + L_CS + (cb + 8 * g + 4 * hi) * 4);
          }
        }
        SBAR();
#pragma unroll
        for (int j = 5; j >= 0; --j)
#pragma unroll
          for (int g = 0; g < 4; ++g)
#pragma unroll
            for (int e = 0; e < 4; ++e)
              acc[j][4 * g + e] = j < 4 ? al * acc[j][4 * g + e] + be * cs[j & 3][g][e] : av[g][e] * acc[j][4 * g + e] + bv[g][e] * cv[j & 1];
        SBAR();
        lds_wait_ge(lbase + L_W + 4 + 4 * (lane & 1), 1);
        const int idx = (int)__builtin_amdgcn_readfirstlane(words[3]);
        const bool mine = (idx >> 5) == tile;
        if (mine) {
#pragma unroll
          for (int j = 0; j < 6; ++j) {
            const int cb = (j >> 1) * 64 + ((j & 1) ^ hh) * 32;
            if (j >= 4) cv[j - 4] = *reinterpret_cast<const float*>(smem + L_PATCH + (cb + r32) * 4);
            else {
#pragma unroll
              for (int g = 0; g < 4; ++g) cs[j][g] = *reinterpret_cast<const f32x4*>(smem + L_PATCH + (cb + 8 * g + 4 * hi) * 4);
            }
          }
          SBAR();
          const int rel = (idx & 31) - 4 * hi;
#pragma unroll
          for (int j = 0; j < 6; ++j)
#pragma unroll
            for (int e = 0; e < 16; ++e)
              acc[j][e] = j < 4 ? (r32 == (idx & 31) ? cs[j & 3][e >> 2][e & 3] : acc[j][e]) : (rel == 8 * (e >> 2) + (e & 3) ? cv[j & 1] : acc[j][e]);
        }
      }
      constexpr float QS = 0.17677669529663687f * 1.4426950408889634f;
      u32x4 own[4];
#pragma unroll
      for (int j = 5; j >= 0; --j) {
        u32x4 o[2];
#pragma unroll
        for (int e = 0; e < 8; ++e) {
          unsigned w = pack2(acc[j][2 * e], acc[j][2 * e + 1]);
          if (j < 2) w = pack2(lo16(w) * QS, hi16(w) * QS);
          o[e >> 2][e & 3] = w;
        }
        if (j >= 4) { o[0] = fo_swap(o[0]); o[1] = fo_swap(o[1]); }
        const int hs = (j & 1) ^ hh;
        if (j >= 2) {
          const unsigned dst = OFF_KV + L * (10 * MBY) + (tn * 2 + hs) * HS + tile * 4096 + ((j >> 1) - 1) * 2048 + lane * 16;
          stg(rs, dst, o[0]); stg(rs, dst + 1024, o[1]);
        }
        if (j >= 2 && !(j & 1)) { own[j - 2] = o[0]; own[j - 1] = o[1]; }
        else {
          char* p = smem + L_XCH + (j < 2 ? (hs * 4 + wid) * 2048 : 16384 + wid * 4096 + ((j >> 1) - 1) * 2048) + lane * 16;
          *reinterpret_cast<u32x4*>(p) = o[0]; *reinterpret_cast<u32x4*>(p + 1024) = o[1];
        }
        SBAR();
      }
      asm volatile(
        "v_mov_b64 v[0:1], 0\n\t"
        "v_mov_b64 v[2:3], 0\n\t"
        "v_mov_b64 v[4:5], 0\n\t"
        "v_mov_b64 v[6:7], 0\n\t"
        "v_mov_b64 v[8:9], 0\n\t"
        "v_mov_b64 v[10:11], 0\n\t"
        "v_mov_b64 v[12:13], 0\n\t"
        "v_mov_b64 v[14:15], 0\n\t"
        "v_mov_b64 v[16:17], 0\n\t"
        "v_mov_b64 v[18:19], 0\n\t"
        "v_mov_b64 v[20:21], 0\n\t"
        "v_mov_b64 v[22:23], 0\n\t"
        "v_mov_b64 v[24:25], 0\n\t"
        "v_mov_b64 v[26:27], 0\n\t"
        "v_mov_b64 v[28:29], 0\n\t"
        "v_mov_b64 v[30:31], 0\n\t"
        "v_mov_b64 v[32:33], 0\n\t"
        "v_mov_b64 v[34:35], 0\n\t"
        "v_mov_b64 v[36:37], 0\n\t"
        "v_mov_b64 v[38:39], 0\n\t"
        "v_mov_b64 v[40:41], 0\n\t"
        "v_mov_b64 v[42:43], 0\n\t"
        "v_mov_b64 v[44:45], 0\n\t"
        "v_mov_b64 v[46:47], 0\n\t"
        "v_mov_b64 v[48:49], 0\n\t"
        "v_mov_b64 v[50:51], 0\n\t"
        "v_mov_b64 v[52:53], 0\n\t"
        "v_mov_b64 v[54:55], 0\n\t"
        "v_mov_b64 v[56:57], 0\n\t"
        "v_mov_b64 v[58:59], 0\n\t"
        "v_mov_b64 v[60:61], 0\n\t"
        "v_mov_b64 v[62:63], 0\n\t"
        "v_mov_b64 v[128:129], 0\n\t"
        "v_mov_b64 v[130:131], 0\n\t"
        "v_mov_b64 v[132:133], 0\n\t"
        "v_mov_b64 v[134:135], 0\n\t"
        "v_mov_b64 v[136:137], 0\n\t"
        "v_mov_b64 v[138:139], 0\n\t"
        "v_mov_b64 v[140:141], 0\n\t"
        "v_mov_b64 v[142:143], 0\n\t"
        : "={v[0:15]}"(oz[0]), "={v[16:31]}"(oz[1]), "={v[32:47]}"(oz[2]), "={v[48:63]}"(oz[3]), "={v[128:143]}"(oz[4]));
      BAR_ALL();
      kva = cat4(own[0], own[1], own[2], own[3]);
      const u32x4* pk = reinterpret_cast<const u32x4*>(smem + L_XCH + 16384 + (wid ^ 1) * 4096 + lane * 16);
#pragma unroll
      for (int u = 0; u < 4; ++u) {
        const u32x4* p = reinterpret_cast<const u32x4*>(smem + L_XCH + (hh * 4 + ((2 * kh + 2 + u) & 3)) * 2048 + lane * 16);
        q[u] = __builtin_shufflevector(p[0], p[64], 0, 1, 2, 3, 4, 5, 6, 7);
      }
      kvb = cat4(pk[0], pk[64], pk[128], pk[192]);
    }
    STAMPV(5 + 16 * L);
    {
      u32x4 ones;
      unsigned kvoff;
      u32x8 fv, cv;
      asm volatile("" : "={v[184:191]}"(fv), "={v[216:223]}"(cv));
      { PHASE_VARS;
        const unsigned v = (lane & 3) == 0 ? 0x3F803F80u : 0u; const bool up = (lane & 4) != 0; ones[0] = ones[1] = up ? 0u : v; ones[2] = ones[3] = up ? v : 0u;
        kvoff = OFF_KV + L * (10 * MBY) + (tn * 2 + hh) * HS + kh * 8192 + lane * 16;
        fv[1] = lbase + L_FREE + wid * 256 + lane * 4; fv[2] = 32 * L + 32;
        cv[0] = lbase + L_XCH + wid * 8192 + lane * 16; cv[1] = lbase + L_XCH + (wid ^ 2) * 8192 + lane * 16;
        cv[2] = lbase + L_XCH + 32768 + wid * 512 + lane * 4; cv[3] = lbase + L_XCH + 32768 + (wid ^ 2) * 512 + lane * 4;
        cv[4] = lbase + L_W + 48 + wid * 4; cv[5] = lbase + L_W + 48 + (wid ^ 2) * 4; cv[6] = L + 1;
        cv[7] = ((tile * 32 + r32) * D_ + tn * 64 + hi * 8) * 2; }
      f32x16 o0 = oz[0], o1 = oz[1], o2 = oz[2], o3 = oz[3], lv = oz[4];
      unsigned soff = tm * 16384;
      STAMP_DECL;
      asm volatile(
        "s_nop 1\n\t"
        "v_mfma_f32_32x32x16_bf16 v[96:111], v[152:155], v[64:67], 0\n\t"
        "v_mfma_f32_32x32x16_bf16 v[96:111], v[156:159], v[68:71], v[96:111]\n\t"
        "s_mov_b32 s46, 0\n\t"
        "ds_write_b32 v185, v186\n\t"
        "s_nop 9\n\t"
        "v_exp_f32 v96, v96\n\t"
        "v_exp_f32 v97, v97\n\t"
        "v_exp_f32 v98, v98\n\t"
        "v_exp_f32 v99, v99\n\t"
        "v_exp_f32 v100, v100\n\t"
        "v_exp_f32 v101, v101\n\t"
        "v_mfma_f32_32x32x16_bf16 v[112:127], v[152:155], v[72:75], 0\n\t"
        "v_exp_f32 v102, v102\n\t"
        "v_exp_f32 v103, v103\n\t"
        "v_exp_f32 v104, v104\n\t"
        "v_mfma_f32_32x32x16_bf16 v[112:127], v[156:159], v[76:79], v[112:127]\n\t"
        "v_exp_f32 v105, v105\n\t"
        "v_exp_f32 v106, v106\n\t"
        "v_exp_f32 v107, v107\n\t"
        "v_smfmac_f32_32x32x32_bf16 v[0:15], v[160:163], v[96:103], v148\n\t"
        "v_exp_f32 v108, v108\n\t"
        "v_exp_f32 v109, v109\n\t"
        "v_exp_f32 v110, v110\n\t"
        "v_smfmac_f32_16x16x64_bf16 v[128:131], v[144:147], v[96:103], v148\n\t"
        "v_exp_f32 v111, v111\n\t"
        "v_exp_f32 v112, v112\n\t"
        "v_exp_f32 v113, v113\n\t"
        "v_smfmac_f32_32x32x32_bf16 v[0:15], v[164:167], v[104:111], v148\n\t"
        "v_exp_f32 v114, v114\n\t"
        "v_exp_f32 v115, v115\n\t"
        "v_exp_f32 v116, v116\n\t"
        "v_smfmac_f32_16x16x64_bf16 v[128:131], v[144:147], v[104:111], v148\n\t"
        "v_exp_f32 v117, v117\n\t"
        "v_mfma_f32_32x32x16_bf16 v[96:111], v[152:155], v[80:83], 0\n\t"
        "v_exp_f32 v118, v118\n\t"
        "v_exp_f32 v119, v119\n\t"
        "v_exp_f32 v120, v120\n\t"
        "v_mfma_f32_32x32x16_bf16 v[96:111], v[156:159], v[84:87], v[96:111]\n\t"
        "v_exp_f32 v121, v121\n\t"
        "v_exp_f32 v122, v122\n\t"
        "v_exp_f32 v123, v123\n\t"
        "v_smfmac_f32_32x32x32_bf16 v[16:31], v[160:163], v[112:119], v148\n\t"
        "v_exp_f32 v124, v124\n\t"
        "v_exp_f32 v125, v125\n\t"
        "v_exp_f32 v126, v126\n\t"
        "v_smfmac_f32_16x16x64_bf16 v[132:135], v[144:147], v[112:119], v148\n\t"
        "v_exp_f32 v127, v127\n\t"
        "v_exp_f32 v96, v96\n\t"
        "v_exp_f32 v97, v97\n\t"
        "v_smfmac_f32_32x32x32_bf16 v[16:31], v[164:167], v[120:127], v148\n\t"
        "v_exp_f32 v98, v98\n\t"
        "v_exp_f32 v99, v99\n\t"
        "v_exp_f32 v100, v100\n\t"
        "v_smfmac_f32_16x16x64_bf16 v[132:135], v[144:147], v[120:127], v148\n\t"
        "v_exp_f32 v101, v101\n\t"
        "v_mfma_f32_32x32x16_bf16 v[112:127], v[152:155], v[88:91], 0\n\t"
        "v_exp_f32 v102, v102\n\t"
        "v_exp_f32 v103, v103\n\t"
        "v_exp_f32 v104, v104\n\t"
        "v_mfma_f32_32x32x16_bf16 v[112:127], v[156:159], v[92:95], v[112:127]\n\t"
        "v_exp_f32 v105, v105\n\t"
        "v_exp_f32 v106, v106\n\t"
        "v_exp_f32 v107, v107\n\t"
        "v_smfmac_f32_32x32x32_bf16 v[32:47], v[160:163], v[96:103], v148\n\t"
        "v_exp_f32 v108, v108\n\t"
        "v_exp_f32 v109, v109\n\t"
        "v_exp_f32 v110, v110\n\t"
        "v_smfmac_f32_16x16x64_bf16 v[136:139], v[144:147], v[96:103], v148\n\t"
        "v_exp_f32 v111, v111\n\t"
        "v_exp_f32 v112, v112\n\t"
        "v_exp_f32 v113, v113\n\t"
        "v_smfmac_f32_32x32x32_bf16 v[32:47], v[164:167], v[104:111], v148\n\t"
        "v_exp_f32 v114, v114\n\t"
        "v_exp_f32 v115, v115\n\t"
        "v_exp_f32 v116, v116\n\t"
        "v_smfmac_f32_16x16x64_bf16 v[136:139], v[144:147], v[104:111], v148\n\t"
        "v_exp_f32 v117, v117\n\t"
        "v_mfma_f32_32x32x16_bf16 v[96:111], v[168:171], v[64:67], 0\n\t"
        "v_exp_f32 v118, v118\n\t"
        "v_exp_f32 v119, v119\n\t"
        "v_exp_f32 v120, v120\n\t"
        "v_mfma_f32_32x32x16_bf16 v[96:111], v[172:175], v[68:71], v[96:111]\n\t"
        "v_exp_f32 v121, v121\n\t"
        "v_exp_f32 v122, v122\n\t"
        "v_exp_f32 v123, v123\n\t"
        "v_smfmac_f32_32x32x32_bf16 v[48:63], v[160:163], v[112:119], v148\n\t"
        "v_exp_f32 v124, v124\n\t"
        "v_exp_f32 v125, v125\n\t"
        "v_exp_f32 v126, v126\n\t"
        "v_smfmac_f32_16x16x64_bf16 v[140:143], v[144:147], v[112:119], v148\n\t"
        "v_exp_f32 v127, v127\n\t"
        "v_exp_f32 v96, v96\n\t"
        "v_exp_f32 v97, v97\n\t"
        "v_smfmac_f32_32x32x32_bf16 v[48:63], v[164:167], v[120:127], v148\n\t"
        "v_exp_f32 v98, v98\n\t"
        "v_exp_f32 v99, v99\n\t"
        "v_exp_f32 v100, v100\n\t"
        "v_smfmac_f32_16x16x64_bf16 v[140:143], v[144:147], v[120:127], v148\n\t"
        "v_exp_f32 v101, v101\n\t"
        "v_mfma_f32_32x32x16_bf16 v[112:127], v[168:171], v[72:75], 0\n\t"
        "v_exp_f32 v102, v102\n\t"
        "v_exp_f32 v103, v103\n\t"
        "v_exp_f32 v104, v104\n\t"
        "ds_read_b32 v151, v224\n\t"
        "v_mfma_f32_32x32x16_bf16 v[112:127], v[172:175], v[76:79], v[112:127]\n\t"
        "v_exp_f32 v105, v105\n\t"
        "v_exp_f32 v106, v106\n\t"
        "v_exp_f32 v107, v107\n\t"
        "v_smfmac_f32_32x32x32_bf16 v[0:15], v[176:179], v[96:103], v148\n\t"
        "v_exp_f32 v108, v108\n\t"
        "v_exp_f32 v109, v109\n\t"
        "v_exp_f32 v110, v110\n\t"
        "v_smfmac_f32_16x16x64_bf16 v[128:131], v[144:147], v[96:103], v148\n\t"
        "v_exp_f32 v111, v111\n\t"
        "s_cmp_lg_u32 s46, 0\n\t"
        "s_cbranch_scc1 31f\n\t"
        "s_waitcnt lgkmcnt(0)\n\t"
        "v_cmp_ne_u32 vcc, v222, v151\n\t"
        "s_cbranch_vccz 30f\n\t"
        "31:\n\t"
        "v_exp_f32 v112, v112\n\t"
        "v_exp_f32 v113, v113\n\t"
        "v_smfmac_f32_32x32x32_bf16 v[0:15], v[180:183], v[104:111], v148\n\t"
        "v_exp_f32 v114, v114\n\t"
        "v_exp_f32 v115, v115\n\t"
        "v_exp_f32 v116, v116\n\t"
        "v_smfmac_f32_16x16x64_bf16 v[128:131], v[144:147], v[104:111], v148\n\t"
        "v_exp_f32 v117, v117\n\t"
        "v_mfma_f32_32x32x16_bf16 v[96:111], v[168:171], v[80:83], 0\n\t"
        "v_exp_f32 v118, v118\n\t"
        "v_exp_f32 v119, v119\n\t"
        "v_exp_f32 v120, v120\n\t"
        "ds_read_b32 v151, v224\n\t"
        "v_mfma_f32_32x32x16_bf16 v[96:111], v[172:175], v[84:87], v[96:111]\n\t"
        "v_exp_f32 v121, v121\n\t"
        "v_exp_f32 v122, v122\n\t"
        "v_exp_f32 v123, v123\n\t"
        "v_smfmac_f32_32x32x32_bf16 v[16:31], v[176:179], v[112:119], v148\n\t"
        "v_exp_f32 v124, v124\n\t"
        "v_exp_f32 v125, v125\n\t"
        "v_exp_f32 v126, v126\n\t"
        "v_smfmac_f32_16x16x64_bf16 v[132:135], v[144:147], v[112:119], v148\n\t"
        "v_exp_f32 v127, v127\n\t"
        "s_cmp_lg_u32 s46, 0\n\t"
        "s_cbranch_scc1 33f\n\t"
        "s_waitcnt lgkmcnt(0)\n\t"
        "v_cmp_ne_u32 vcc, v222, v151\n\t"
        "s_cbranch_vccz 32f\n\t"
        "33:\n\t"
        "v_exp_f32 v96, v96\n\t"
        "v_exp_f32 v97, v97\n\t"
        "v_smfmac_f32_32x32x32_bf16 v[16:31], v[180:183], v[120:127], v148\n\t"
        "v_exp_f32 v98, v98\n\t"
        "v_exp_f32 v99, v99\n\t"
        "v_exp_f32 v100, v100\n\t"
        "v_smfmac_f32_16x16x64_bf16 v[132:135], v[144:147], v[120:127], v148\n\t"
        "v_exp_f32 v101, v101\n\t"
        "v_mfma_f32_32x32x16_bf16 v[112:127], v[168:171], v[88:91], 0\n\t"
        "v_exp_f32 v102, v102\n\t"
        "v_exp_f32 v103, v103\n\t"
        "v_exp_f32 v104, v104\n\t"
        "v_mfma_f32_32x32x16_bf16 v[112:127], v[172:175], v[92:95], v[112:127]\n\t"
        "v_exp_f32 v105, v105\n\t"
        "v_exp_f32 v106, v106\n\t"
        "v_exp_f32 v107, v107\n\t"
        "v_smfmac_f32_32x32x32_bf16 v[32:47], v[176:179], v[96:103], v148\n\t"
        "v_exp_f32 v108, v108\n\t"
        "v_exp_f32 v109, v109\n\t"
        "v_exp_f32 v110, v110\n\t"
        "v_smfmac_f32_16x16x64_bf16 v[136:139], v[144:147], v[96:103], v148\n\t"
        "v_exp_f32 v111, v111\n\t"
        "s_cmp_lg_u32 s46, 0\n\t"
        "s_cbranch_scc1 5f\n\t"
        "s_mov_b32 s45, 0x200000\n\t"
        "2:\n\t"
        "ds_read_b32 v151, v224\n\t"
        "s_sub_u32 s45, s45, 1\n\t"
        "s_waitcnt lgkmcnt(0)\n\t"
        "s_cbranch_scc1 3f\n\t"
        "v_cmp_ne_u32 vcc, v222, v151\n\t"
        "s_cbranch_vccnz 2b\n\t"
        "3:\n\t"
        "s_add_u32 s44, s44, 0x4000\n\t"
        "s_and_b32 s44, s44, 0x3ffff\n\t"
        "buffer_load_dwordx4 v[184:187], v149, s[40:43], s44 offen offset:0 sc1\n\t"
        "buffer_load_dwordx4 v[188:191], v149, s[40:43], s44 offen offset:1024 sc1\n\t"
        "buffer_load_dwordx4 v[192:195], v149, s[40:43], s44 offen offset:2048 sc1\n\t"
        "buffer_load_dwordx4 v[196:199], v149, s[40:43], s44 offen offset:3072 sc1\n\t"
        "buffer_load_dwordx4 v[200:203], v150, s[40:43], s44 offen offset:0 sc1\n\t"
        "buffer_load_dwordx4 v[204:207], v150, s[40:43], s44 offen offset:1024 sc1\n\t"
        "buffer_load_dwordx4 v[208:211], v150, s[40:43], s44 offen offset:2048 sc1\n\t"
        "buffer_load_dwordx4 v[212:215], v150, s[40:43], s44 offen offset:3072 sc1\n\t"
        "s_add_u32 s44, s44, 0x4000\n\t"
        "s_and_b32 s44, s44, 0x3ffff\n\t"
        "buffer_load_dwordx4 v[152:155], v149, s[40:43], s44 offen offset:0 sc1\n\t"
        "buffer_load_dwordx4 v[156:159], v149, s[40:43], s44 offen offset:1024 sc1\n\t"
        "buffer_load_dwordx4 v[160:163], v149, s[40:43], s44 offen offset:2048 sc1\n\t"
        "buffer_load_dwordx4 v[164:167], v149, s[40:43], s44 offen offset:3072 sc1\n\t"
        "5:\n\t"
        "s_mov_b32 s45, 7\n\t"
        "1:\n\t"
        "v_exp_f32 v112, v112\n\t"
        "v_exp_f32 v113, v113\n\t"
        "v_smfmac_f32_32x32x32_bf16 v[32:47], v[180:183], v[104:111], v148\n\t"
        "v_exp_f32 v114, v114\n\t"
        "v_exp_f32 v115, v115\n\t"
        "v_exp_f32 v116, v116\n\t"
        "v_smfmac_f32_16x16x64_bf16 v[136:139], v[144:147], v[104:111], v148\n\t"
        "v_exp_f32 v117, v117\n\t"
        "s_waitcnt vmcnt(8)\n\t"
        "v_mfma_f32_32x32x16_bf16 v[96:111], v[184:187], v[64:67], 0\n\t"
        "v_exp_f32 v118, v118\n\t"
        "v_exp_f32 v119, v119\n\t"
        "v_exp_f32 v120, v120\n\t"
        "v_mfma_f32_32x32x16_bf16 v[96:111], v[188:191], v[68:71], v[96:111]\n\t"
        "v_exp_f32 v121, v121\n\t"
        "v_exp_f32 v122, v122\n\t"
        "v_exp_f32 v123, v123\n\t"
        "v_smfmac_f32_32x32x32_bf16 v[48:63], v[176:179], v[112:119], v148\n\t"
        "v_exp_f32 v124, v124\n\t"
        "v_exp_f32 v125, v125\n\t"
        "v_exp_f32 v126, v126\n\t"
        "v_smfmac_f32_16x16x64_bf16 v[140:143], v[144:147], v[112:119], v148\n\t"
        "buffer_load_dwordx4 v[168:171], v150, s[40:43], s44 offen offset:0 sc1\n\t"
        "v_exp_f32 v127, v127\n\t"
        "v_exp_f32 v96, v96\n\t"
        "v_exp_f32 v97, v97\n\t"
        "v_smfmac_f32_32x32x32_bf16 v[48:63], v[180:183], v[120:127], v148\n\t"
        "v_exp_f32 v98, v98\n\t"
        "v_exp_f32 v99, v99\n\t"
        "v_exp_f32 v100, v100\n\t"
        "v_smfmac_f32_16x16x64_bf16 v[140:143], v[144:147], v[120:127], v148\n\t"
        "v_exp_f32 v101, v101\n\t"
        "v_mfma_f32_32x32x16_bf16 v[112:127], v[184:187], v[72:75], 0\n\t"
        "v_exp_f32 v102, v102\n\t"
        "v_exp_f32 v103, v103\n\t"
        "v_exp_f32 v104, v104\n\t"
        "v_mfma_f32_32x32x16_bf16 v[112:127], v[188:191], v[76:79], v[112:127]\n\t"
        "v_exp_f32 v105, v105\n\t"
        "v_exp_f32 v106, v106\n\t"
        "v_exp_f32 v107, v107\n\t"
        "v_smfmac_f32_32x32x32_bf16 v[0:15], v[192:195], v[96:103], v148\n\t"
        "v_exp_f32 v108, v108\n\t"
        "v_exp_f32 v109, v109\n\t"
        "v_exp_f32 v110, v110\n\t"
        "v_smfmac_f32_16x16x64_bf16 v[128:131], v[144:147], v[96:103], v148\n\t"
        "buffer_load_dwordx4 v[172:175], v150, s[40:43], s44 offen offset:1024 sc1\n\t"
        "v_exp_f32 v111, v111\n\t"
        "v_exp_f32 v112, v112\n\t"
        "v_exp_f32 v113, v113\n\t"
        "v_smfmac_f32_32x32x32_bf16 v[0:15], v[196:199], v[104:111], v148\n\t"
        "v_exp_f32 v114, v114\n\t"
        "v_exp_f32 v115, v115\n\t"
        "v_exp_f32 v116, v116\n\t"
        "v_smfmac_f32_16x16x64_bf16 v[128:131], v[144:147], v[104:111], v148\n\t"
        "v_exp_f32 v117, v117\n\t"
        "v_mfma_f32_32x32x16_bf16 v[96:111], v[184:187], v[80:83], 0\n\t"
        "v_exp_f32 v118, v118\n\t"
        "v_exp_f32 v119, v119\n\t"
        "v_exp_f32 v120, v120\n\t"
        "v_mfma_f32_32x32x16_bf16 v[96:111], v[188:191], v[84:87], v[96:111]\n\t"
        "v_exp_f32 v121, v121\n\t"
        "v_exp_f32 v122, v122\n\t"
        "v_exp_f32 v123, v123\n\t"
        "v_smfmac_f32_32x32x32_bf16 v[16:31], v[192:195], v[112:119], v148\n\t"
        "v_exp_f32 v124, v124\n\t"
        "v_exp_f32 v125, v125\n\t"
        "v_exp_f32 v126, v126\n\t"
        "v_smfmac_f32_16x16x64_bf16 v[132:135], v[144:147], v[112:119], v148\n\t"
        "buffer_load_dwordx4 v[176:179], v150, s[40:43], s44 offen offset:2048 sc1\n\t"
        "v_exp_f32 v127, v127\n\t"
        "v_exp_f32 v96, v96\n\t"
        "v_exp_f32 v97, v97\n\t"
        "v_smfmac_f32_32x32x32_bf16 v[16:31], v[196:199], v[120:127], v148\n\t"
        "v_exp_f32 v98, v98\n\t"
        "v_exp_f32 v99, v99\n\t"
        "v_exp_f32 v100, v100\n\t"
        "v_smfmac_f32_16x16x64_bf16 v[132:135], v[144:147], v[120:127], v148\n\t"
        "v_exp_f32 v101, v101\n\t"
        "v_mfma_f32_32x32x16_bf16 v[112:127], v[184:187], v[88:91], 0\n\t"
        "v_exp_f32 v102, v102\n\t"
        "v_exp_f32 v103, v103\n\t"
        "v_exp_f32 v104, v104\n\t"
        "v_mfma_f32_32x32x16_bf16 v[112:127], v[188:191], v[92:95], v[112:127]\n\t"
        "v_exp_f32 v105, v105\n\t"
        "v_exp_f32 v106, v106\n\t"
        "v_exp_f32 v107, v107\n\t"
        "v_smfmac_f32_32x32x32_bf16 v[32:47], v[192:195], v[96:103], v148\n\t"
        "v_exp_f32 v108, v108\n\t"
        "v_exp_f32 v109, v109\n\t"
        "v_exp_f32 v110, v110\n\t"
        "v_smfmac_f32_16x16x64_bf16 v[136:139], v[144:147], v[96:103], v148\n\t"
        "buffer_load_dwordx4 v[180:183], v150, s[40:43], s44 offen offset:3072 sc1\n\t"
        "v_exp_f32 v111, v111\n\t"
        "v_exp_f32 v112, v112\n\t"
        "v_exp_f32 v113, v113\n\t"
        "v_smfmac_f32_32x32x32_bf16 v[32:47], v[196:199], v[104:111], v148\n\t"
        "s_add_u32 s44, s44, 0x4000\n\t"
        "s_and_b32 s44, s44, 0x3ffff\n\t"
        "v_exp_f32 v114, v114\n\t"
        "v_exp_f32 v115, v115\n\t"
        "v_exp_f32 v116, v116\n\t"
        "v_smfmac_f32_16x16x64_bf16 v[136:139], v[144:147], v[104:111], v148\n\t"
        "v_exp_f32 v117, v117\n\t"
        "s_waitcnt vmcnt(8)\n\t"
        "v_mfma_f32_32x32x16_bf16 v[96:111], v[200:203], v[64:67], 0\n\t"
        "v_exp_f32 v118, v118\n\t"
        "v_exp_f32 v119, v119\n\t"
        "v_exp_f32 v120, v120\n\t"
        "v_mfma_f32_32x32x16_bf16 v[96:111], v[204:207], v[68:71], v[96:111]\n\t"
        "v_exp_f32 v121, v121\n\t"
        "v_exp_f32 v122, v122\n\t"
        "v_exp_f32 v123, v123\n\t"
        "v_smfmac_f32_32x32x32_bf16 v[48:63], v[192:195], v[112:119], v148\n\t"
        "v_exp_f32 v124, v124\n\t"
        "v_exp_f32 v125, v125\n\t"
        "v_exp_f32 v126, v126\n\t"
        "v_smfmac_f32_16x16x64_bf16 v[140:143], v[144:147], v[112:119], v148\n\t"
        "buffer_load_dwordx4 v[184:187], v149, s[40:43], s44 offen offset:0 sc1\n\t"
        "v_exp_f32 v127, v127\n\t"
        "v_exp_f32 v96, v96\n\t"
        "v_exp_f32 v97, v97\n\t"
        "v_smfmac_f32_32x32x32_bf16 v[48:63], v[196:199], v[120:127], v148\n\t"
        "v_exp_f32 v98, v98\n\t"
        "v_exp_f32 v99, v99\n\t"
        "v_exp_f32 v100, v100\n\t"
        "v_smfmac_f32_16x16x64_bf16 v[140:143], v[144:147], v[120:127], v148\n\t"
        "v_exp_f32 v101, v101\n\t"
        "v_mfma_f32_32x32x16_bf16 v[112:127], v[200:203], v[72:75], 0\n\t"
        "v_exp_f32 v102, v102\n\t"
        "v_exp_f32 v103, v103\n\t"
        "v_exp_f32 v104, v104\n\t"
        "v_mfma_f32_32x32x16_bf16 v[112:127], v[204:207], v[76:79], v[112:127]\n\t"
        "v_exp_f32 v105, v105\n\t"
        "v_exp_f32 v106, v106\n\t"
        "v_exp_f32 v107, v107\n\t"
        "v_smfmac_f32_32x32x32_bf16 v[0:15], v[208:211], v[96:103], v148\n\t"
        "v_exp_f32 v108, v108\n\t"
        "v_exp_f32 v109, v109\n\t"
        "v_exp_f32 v110, v110\n\t"
        "v_smfmac_f32_16x16x64_bf16 v[128:131], v[144:147], v[96:103], v148\n\t"
        "buffer_load_dwordx4 v[188:191], v149, s[40:43], s44 offen offset:1024 sc1\n\t"
        "v_exp_f32 v111, v111\n\t"
        "v_exp_f32 v112, v112\n\t"
        "v_exp_f32 v113, v113\n\t"
        "v_smfmac_f32_32x32x32_bf16 v[0:15], v[212:215], v[104:111], v148\n\t"
        "v_exp_f32 v114, v114\n\t"
        "v_exp_f32 v115, v115\n\t"
        "v_exp_f32 v116, v116\n\t"
        "v_smfmac_f32_16x16x64_bf16 v[128:131], v[144:147], v[104:111], v148\n\t"
        "v_exp_f32 v117, v117\n\t"
        "v_mfma_f32_32x32x16_bf16 v[96:111], v[200:203], v[80:83], 0\n\t"
        "v_exp_f32 v118, v118\n\t"
        "v_exp_f32 v119, v119\n\t"
        "v_exp_f32 v120, v120\n\t"
        "v_mfma_f32_32x32x16_bf16 v[96:111], v[204:207], v[84:87], v[96:111]\n\t"
        "v_exp_f32 v121, v121\n\t"
        "v_exp_f32 v122, v122\n\t"
        "v_exp_f32 v123, v123\n\t"
        "v_smfmac_f32_32x32x32_bf16 v[16:31], v[208:211], v[112:119], v148\n\t"
        "v_exp_f32 v124, v124\n\t"
        "v_exp_f32 v125, v125\n\t"
        "v_exp_f32 v126, v126\n\t"
        "v_smfmac_f32_16x16x64_bf16 v[132:135], v[144:147], v[112:119], v148\n\t"
        "buffer_load_dwordx4 v[192:195], v149, s[40:43], s44 offen offset:2048 sc1\n\t"
        "v_exp_f32 v127, v127\n\t"
        "v_exp_f32 v96, v96\n\t"
        "v_exp_f32 v97, v97\n\t"
        "v_smfmac_f32_32x32x32_bf16 v[16:31], v[212:215], v[120:127], v148\n\t"
        "v_exp_f32 v98, v98\n\t"
        "v_exp_f32 v99, v99\n\t"
        "v_exp_f32 v100, v100\n\t"
        "v_smfmac_f32_16x16x64_bf16 v[132:135], v[144:147], v[120:127], v148\n\t"
        "v_exp_f32 v101, v101\n\t"
        "v_mfma_f32_32x32x16_bf16 v[112:127], v[200:203], v[88:91], 0\n\t"
        "v_exp_f32 v102, v102\n\t"
        "v_exp_f32 v103, v103\n\t"
        "v_exp_f32 v104, v104\n\t"
        "v_mfma_f32_32x32x16_bf16 v[112:127], v[204:207], v[92:95], v[112:127]\n\t"
        "v_exp_f32 v105, v105\n\t"
        "v_exp_f32 v106, v106\n\t"
        "v_exp_f32 v107, v107\n\t"
        "v_smfmac_f32_32x32x32_bf16 v[32:47], v[208:211], v[96:103], v148\n\t"
        "v_exp_f32 v108, v108\n\t"
        "v_exp_f32 v109, v109\n\t"
        "v_exp_f32 v110, v110\n\t"
        "v_smfmac_f32_16x16x64_bf16 v[136:139], v[144:147], v[96:103], v148\n\t"
        "buffer_load_dwordx4 v[196:199], v149, s[40:43], s44 offen offset:3072 sc1\n\t"
        "v_exp_f32 v111, v111\n\t"
        "v_exp_f32 v112, v112\n\t"
        "v_exp_f32 v113, v113\n\t"
        "v_smfmac_f32_32x32x32_bf16 v[32:47], v[212:215], v[104:111], v148\n\t"
        "v_exp_f32 v114, v114\n\t"
        "v_exp_f32 v115, v115\n\t"
        "v_exp_f32 v116, v116\n\t"
        "v_smfmac_f32_16x16x64_bf16 v[136:139], v[144:147], v[104:111], v148\n\t"
        "v_exp_f32 v117, v117\n\t"
        "s_waitcnt vmcnt(8)\n\t"
        "v_mfma_f32_32x32x16_bf16 v[96:111], v[152:155], v[64:67], 0\n\t"
        "v_exp_f32 v118, v118\n\t"
        "v_exp_f32 v119, v119\n\t"
        "v_exp_f32 v120, v120\n\t"
        "v_mfma_f32_32x32x16_bf16 v[96:111], v[156:159], v[68:71], v[96:111]\n\t"
        "v_exp_f32 v121, v121\n\t"
        "v_exp_f32 v122, v122\n\t"
        "v_exp_f32 v123, v123\n\t"
        "v_smfmac_f32_32x32x32_bf16 v[48:63], v[208:211], v[112:119], v148\n\t"
        "v_exp_f32 v124, v124\n\t"
        "v_exp_f32 v125, v125\n\t"
        "v_exp_f32 v126, v126\n\t"
        "v_smfmac_f32_16x16x64_bf16 v[140:143], v[144:147], v[112:119], v148\n\t"
        "buffer_load_dwordx4 v[200:203], v150, s[40:43], s44 offen offset:0 sc1\n\t"
        "v_exp_f32 v127, v127\n\t"
        "v_exp_f32 v96, v96\n\t"
        "v_exp_f32 v97, v97\n\t"
        "v_smfmac_f32_32x32x32_bf16 v[48:63], v[212:215], v[120:127], v148\n\t"
        "v_exp_f32 v98, v98\n\t"
        "v_exp_f32 v99, v99\n\t"
        "v_exp_f32 v100, v100\n\t"
        "v_smfmac_f32_16x16x64_bf16 v[140:143], v[144:147], v[120:127], v148\n\t"
        "v_exp_f32 v101, v101\n\t"
        "v_mfma_f32_32x32x16_bf16 v[112:127], v[152:155], v[72:75], 0\n\t"
        "v_exp_f32 v102, v102\n\t"
        "v_exp_f32 v103, v103\n\t"
        "v_exp_f32 v104, v104\n\t"
        "v_mfma_f32_32x32x16_bf16 v[112:127], v[156:159], v[76:79], v[112:127]\n\t"
        "v_exp_f32 v105, v105\n\t"
        "v_exp_f32 v106, v106\n\t"
        "v_exp_f32 v107, v107\n\t"
        "v_smfmac_f32_32x32x32_bf16 v[0:15], v[160:163], v[96:103], v148\n\t"
        "v_exp_f32 v108, v108\n\t"
        "v_exp_f32 v109, v109\n\t"
        "v_exp_f32 v110, v110\n\t"
        "v_smfmac_f32_16x16x64_bf16 v[128:131], v[144:147], v[96:103], v148\n\t"
        "buffer_load_dwordx4 v[204:207], v150, s[40:43], s44 offen offset:1024 sc1\n\t"
        "v_exp_f32 v111, v111\n\t"
        "v_exp_f32 v112, v112\n\t"
        "v_exp_f32 v113, v113\n\t"
        "v_smfmac_f32_32x32x32_bf16 v[0:15], v[164:167], v[104:111], v148\n\t"
        "v_exp_f32 v114, v114\n\t"
        "v_exp_f32 v115, v115\n\t"
        "v_exp_f32 v116, v116\n\t"
        "v_smfmac_f32_16x16x64_bf16 v[128:131], v[144:147], v[104:111], v148\n\t"
        "v_exp_f32 v117, v117\n\t"
        "v_mfma_f32_32x32x16_bf16 v[96:111], v[152:155], v[80:83], 0\n\t"
        "v_exp_f32 v118, v118\n\t"
        "v_exp_f32 v119, v119\n\t"
        "v_exp_f32 v120, v120\n\t"
        "v_mfma_f32_32x32x16_bf16 v[96:111], v[156:159], v[84:87], v[96:111]\n\t"
        "v_exp_f32 v121, v121\n\t"
        "v_exp_f32 v122, v122\n\t"
        "v_exp_f32 v123, v123\n\t"
        "v_smfmac_f32_32x32x32_bf16 v[16:31], v[160:163], v[112:119], v148\n\t"
        "v_exp_f32 v124, v124\n\t"
        "v_exp_f32 v125, v125\n\t"
        "v_exp_f32 v126, v126\n\t"
        "v_smfmac_f32_16x16x64_bf16 v[132:135], v[144:147], v[112:119], v148\n\t"
        "buffer_load_dwordx4 v[208:211], v150, s[40:43], s44 offen offset:2048 sc1\n\t"
        "v_exp_f32 v127, v127\n\t"
        "v_exp_f32 v96, v96\n\t"
        "v_exp_f32 v97, v97\n\t"
        "v_smfmac_f32_32x32x32_bf16 v[16:31], v[164:167], v[120:127], v148\n\t"
        "v_exp_f32 v98, v98\n\t"
        "v_exp_f32 v99, v99\n\t"
        "v_exp_f32 v100, v100\n\t"
        "v_smfmac_f32_16x16x64_bf16 v[132:135], v[144:147], v[120:127], v148\n\t"
        "v_exp_f32 v101, v101\n\t"
        "v_mfma_f32_32x32x16_bf16 v[112:127], v[152:155], v[88:91], 0\n\t"
        "v_exp_f32 v102, v102\n\t"
        "v_exp_f32 v103, v103\n\t"
        "v_exp_f32 v104, v104\n\t"
        "v_mfma_f32_32x32x16_bf16 v[112:127], v[156:159], v[92:95], v[112:127]\n\t"
        "v_exp_f32 v105, v105\n\t"
        "v_exp_f32 v106, v106\n\t"
        "v_exp_f32 v107, v107\n\t"
        "v_smfmac_f32_32x32x32_bf16 v[32:47], v[160:163], v[96:103], v148\n\t"
        "v_exp_f32 v108, v108\n\t"
        "v_exp_f32 v109, v109\n\t"
        "v_exp_f32 v110, v110\n\t"
        "v_smfmac_f32_16x16x64_bf16 v[136:139], v[144:147], v[96:103], v148\n\t"
        "buffer_load_dwordx4 v[212:215], v150, s[40:43], s44 offen offset:3072 sc1\n\t"
        "v_exp_f32 v111, v111\n\t"
        "v_exp_f32 v112, v112\n\t"
        "v_exp_f32 v113, v113\n\t"
        "v_smfmac_f32_32x32x32_bf16 v[32:47], v[164:167], v[104:111], v148\n\t"
        "s_add_u32 s44, s44, 0x4000\n\t"
        "s_and_b32 s44, s44, 0x3ffff\n\t"
        "v_exp_f32 v114, v114\n\t"
        "v_exp_f32 v115, v115\n\t"
        "v_exp_f32 v116, v116\n\t"
        "v_smfmac_f32_16x16x64_bf16 v[136:139], v[144:147], v[104:111], v148\n\t"
        "v_exp_f32 v117, v117\n\t"
        "s_waitcnt vmcnt(8)\n\t"
        "v_mfma_f32_32x32x16_bf16 v[96:111], v[168:171], v[64:67], 0\n\t"
        "v_exp_f32 v118, v118\n\t"
        "v_exp_f32 v119, v119\n\t"
        "v_exp_f32 v120, v120\n\t"
        "v_mfma_f32_32x32x16_bf16 v[96:111], v[172:175], v[68:71], v[96:111]\n\t"
        "v_exp_f32 v121, v121\n\t"
        "v_exp_f32 v122, v122\n\t"
        "v_exp_f32 v123, v123\n\t"
        "v_smfmac_f32_32x32x32_bf16 v[48:63], v[160:163], v[112:119], v148\n\t"
        "v_exp_f32 v124, v124\n\t"
        "v_exp_f32 v125, v125\n\t"
        "v_exp_f32 v126, v126\n\t"
        "v_smfmac_f32_16x16x64_bf16 v[140:143], v[144:147], v[112:119], v148\n\t"
        "buffer_load_dwordx4 v[152:155], v149, s[40:43], s44 offen offset:0 sc1\n\t"
        "v_exp_f32 v127, v127\n\t"
        "v_exp_f32 v96, v96\n\t"
        "v_exp_f32 v97, v97\n\t"
        "v_smfmac_f32_32x32x32_bf16 v[48:63], v[164:167], v[120:127], v148\n\t"
        "v_exp_f32 v98, v98\n\t"
        "v_exp_f32 v99, v99\n\t"
        "v_exp_f32 v100, v100\n\t"
        "v_smfmac_f32_16x16x64_bf16 v[140:143], v[144:147], v[120:127], v148\n\t"
        "v_exp_f32 v101, v101\n\t"
        "v_mfma_f32_32x32x16_bf16 v[112:127], v[168:171], v[72:75], 0\n\t"
        "v_exp_f32 v102, v102\n\t"
        "v_exp_f32 v103, v103\n\t"
        "v_exp_f32 v104, v104\n\t"
        "v_mfma_f32_32x32x16_bf16 v[112:127], v[172:175], v[76:79], v[112:127]\n\t"
        "v_exp_f32 v105, v105\n\t"
        "v_exp_f32 v106, v106\n\t"
        "v_exp_f32 v107, v107\n\t"
        "v_smfmac_f32_32x32x32_bf16 v[0:15], v[176:179], v[96:103], v148\n\t"
        "v_exp_f32 v108, v108\n\t"
        "v_exp_f32 v109, v109\n\t"
        "v_exp_f32 v110, v110\n\t"
        "v_smfmac_f32_16x16x64_bf16 v[128:131], v[144:147], v[96:103], v148\n\t"
        "buffer_load_dwordx4 v[156:159], v149, s[40:43], s44 offen offset:1024 sc1\n\t"
        "v_exp_f32 v111, v111\n\t"
        "v_exp_f32 v112, v112\n\t"
        "v_exp_f32 v113, v113\n\t"
        "v_smfmac_f32_32x32x32_bf16 v[0:15], v[180:183], v[104:111], v148\n\t"
        "v_exp_f32 v114, v114\n\t"
        "v_exp_f32 v115, v115\n\t"
        "v_exp_f32 v116, v116\n\t"
        "v_smfmac_f32_16x16x64_bf16 v[128:131], v[144:147], v[104:111], v148\n\t"
        "v_exp_f32 v117, v117\n\t"
        "v_mfma_f32_32x32x16_bf16 v[96:111], v[168:171], v[80:83], 0\n\t"
        "v_exp_f32 v118, v118\n\t"
        "v_exp_f32 v119, v119\n\t"
        "v_exp_f32 v120, v120\n\t"
        "v_mfma_f32_32x32x16_bf16 v[96:111], v[172:175], v[84:87], v[96:111]\n\t"
        "v_exp_f32 v121, v121\n\t"
        "v_exp_f32 v122, v122\n\t"
        "v_exp_f32 v123, v123\n\t"
        "v_smfmac_f32_32x32x32_bf16 v[16:31], v[176:179], v[112:119], v148\n\t"
        "v_exp_f32 v124, v124\n\t"
        "v_exp_f32 v125, v125\n\t"
        "v_exp_f32 v126, v126\n\t"
        "v_smfmac_f32_16x16x64_bf16 v[132:135], v[144:147], v[112:119], v148\n\t"
        "buffer_load_dwordx4 v[160:163], v149, s[40:43], s44 offen offset:2048 sc1\n\t"
        "v_exp_f32 v127, v127\n\t"
        "v_exp_f32 v96, v96\n\t"
        "v_exp_f32 v97, v97\n\t"
        "v_smfmac_f32_32x32x32_bf16 v[16:31], v[180:183], v[120:127], v148\n\t"
        "v_exp_f32 v98, v98\n\t"
        "v_exp_f32 v99, v99\n\t"
        "v_exp_f32 v100, v100\n\t"
        "v_smfmac_f32_16x16x64_bf16 v[132:135], v[144:147], v[120:127], v148\n\t"
        "v_exp_f32 v101, v101\n\t"
        "v_mfma_f32_32x32x16_bf16 v[112:127], v[168:171], v[88:91], 0\n\t"
        "v_exp_f32 v102, v102\n\t"
        "v_exp_f32 v103, v103\n\t"
        "v_exp_f32 v104, v104\n\t"
        "v_mfma_f32_32x32x16_bf16 v[112:127], v[172:175], v[92:95], v[112:127]\n\t"
        "v_exp_f32 v105, v105\n\t"
        "v_exp_f32 v106, v106\n\t"
        "v_exp_f32 v107, v107\n\t"
        "v_smfmac_f32_32x32x32_bf16 v[32:47], v[176:179], v[96:103], v148\n\t"
        "v_exp_f32 v108, v108\n\t"
        "v_exp_f32 v109, v109\n\t"
        "v_exp_f32 v110, v110\n\t"
        "v_smfmac_f32_16x16x64_bf16 v[136:139], v[144:147], v[96:103], v148\n\t"
        "buffer_load_dwordx4 v[164:167], v149, s[40:43], s44 offen offset:3072 sc1\n\t"
        "v_exp_f32 v111, v111\n\t"
        "s_sub_u32 s45, s45, 1\n\t"
        "s_cmp_lg_u32 s45, 0\n\t"
        "s_cbranch_scc1 1b\n\t"
        "v_exp_f32 v112, v112\n\t"
        "v_exp_f32 v113, v113\n\t"
        "v_smfmac_f32_32x32x32_bf16 v[32:47], v[180:183], v[104:111], v148\n\t"
        "v_exp_f32 v114, v114\n\t"
        "v_exp_f32 v115, v115\n\t"
        "v_exp_f32 v116, v116\n\t"
        "v_smfmac_f32_16x16x64_bf16 v[136:139], v[144:147], v[104:111], v148\n\t"
        "v_exp_f32 v117, v117\n\t"
        "s_waitcnt vmcnt(8)\n\t"
        "v_mfma_f32_32x32x16_bf16 v[96:111], v[184:187], v[64:67], 0\n\t"
        "v_exp_f32 v118, v118\n\t"
        "v_exp_f32 v119, v119\n\t"
        "v_exp_f32 v120, v120\n\t"
        "v_mfma_f32_32x32x16_bf16 v[96:111], v[188:191], v[68:71], v[96:111]\n\t"
        "v_exp_f32 v121, v121\n\t"
        "v_exp_f32 v122, v122\n\t"
        "v_exp_f32 v123, v123\n\t"
        "v_smfmac_f32_32x32x32_bf16 v[48:63], v[176:179], v[112:119], v148\n\t"
        "v_exp_f32 v124, v124\n\t"
        "v_exp_f32 v125, v125\n\t"
        "v_exp_f32 v126, v126\n\t"
        "v_smfmac_f32_16x16x64_bf16 v[140:143], v[144:147], v[112:119], v148\n\t"
        "v_exp_f32 v127, v127\n\t"
        "v_exp_f32 v96, v96\n\t"
        "v_exp_f32 v97, v97\n\t"
        "v_smfmac_f32_32x32x32_bf16 v[48:63], v[180:183], v[120:127], v148\n\t"
        "v_exp_f32 v98, v98\n\t"
        "v_exp_f32 v99, v99\n\t"
        "v_exp_f32 v100, v100\n\t"
        "v_smfmac_f32_16x16x64_bf16 v[140:143], v[144:147], v[120:127], v148\n\t"
        "v_exp_f32 v101, v101\n\t"
        "v_mfma_f32_32x32x16_bf16 v[112:127], v[184:187], v[72:75], 0\n\t"
        "v_exp_f32 v102, v102\n\t"
        "v_exp_f32 v103, v103\n\t"
        "v_exp_f32 v104, v104\n\t"
        "v_mfma_f32_32x32x16_bf16 v[112:127], v[188:191], v[76:79], v[112:127]\n\t"
        "v_exp_f32 v105, v105\n\t"
        "v_exp_f32 v106, v106\n\t"
        "v_exp_f32 v107, v107\n\t"
        "v_smfmac_f32_32x32x32_bf16 v[0:15], v[192:195], v[96:103], v148\n\t"
        "v_exp_f32 v108, v108\n\t"
        "v_exp_f32 v109, v109\n\t"
        "v_exp_f32 v110, v110\n\t"
        "v_smfmac_f32_16x16x64_bf16 v[128:131], v[144:147], v[96:103], v148\n\t"
        "v_exp_f32 v111, v111\n\t"
        "v_exp_f32 v112, v112\n\t"
        "v_exp_f32 v113, v113\n\t"
        "v_smfmac_f32_32x32x32_bf16 v[0:15], v[196:199], v[104:111], v148\n\t"
        "v_exp_f32 v114, v114\n\t"
        "v_exp_f32 v115, v115\n\t"
        "v_exp_f32 v116, v116\n\t"
        "v_smfmac_f32_16x16x64_bf16 v[128:131], v[144:147], v[104:111], v148\n\t"
        "v_exp_f32 v117, v117\n\t"
        "s_waitcnt vmcnt(4)\n\t"
        "v_mfma_f32_32x32x16_bf16 v[96:111], v[200:203], v[64:67], 0\n\t"
        "v_exp_f32 v118, v118\n\t"
        "v_exp_f32 v119, v119\n\t"
        "v_exp_f32 v120, v120\n\t"
        "v_mfma_f32_32x32x16_bf16 v[96:111], v[204:207], v[68:71], v[96:111]\n\t"
        "v_exp_f32 v121, v121\n\t"
        "v_exp_f32 v122, v122\n\t"
        "v_exp_f32 v123, v123\n\t"
        "v_smfmac_f32_32x32x32_bf16 v[16:31], v[192:195], v[112:119], v148\n\t"
        "v_exp_f32 v124, v124\n\t"
        "v_exp_f32 v125, v125\n\t"
        "v_exp_f32 v126, v126\n\t"
        "v_smfmac_f32_16x16x64_bf16 v[132:135], v[144:147], v[112:119], v148\n\t"
        "v_exp_f32 v127, v127\n\t"
        "s_cmp_lg_u32 s47, 0\n\t"
        "s_cbranch_scc1 4f\n\t"
        "global_load_dwordx4 %[r0], v223, %[xb] offset:0\n\t"
        "global_load_dwordx4 %[r1], v223, %[xb] offset:32\n\t"
        "global_load_dwordx4 %[r2], v223, %[xb] offset:64\n\t"
        "global_load_dwordx4 %[r3], v223, %[xb] offset:96\n\t"
        "4:\n\t"
        "v_exp_f32 v96, v96\n\t"
        "v_exp_f32 v97, v97\n\t"
        "v_smfmac_f32_32x32x32_bf16 v[16:31], v[196:199], v[120:127], v148\n\t"
        "v_exp_f32 v98, v98\n\t"
        "v_exp_f32 v99, v99\n\t"
        "v_exp_f32 v100, v100\n\t"
        "v_smfmac_f32_16x16x64_bf16 v[132:135], v[144:147], v[120:127], v148\n\t"
        "v_exp_f32 v101, v101\n\t"
        "v_mfma_f32_32x32x16_bf16 v[112:127], v[200:203], v[72:75], 0\n\t"
        "v_exp_f32 v102, v102\n\t"
        "v_exp_f32 v103, v103\n\t"
        "v_exp_f32 v104, v104\n\t"
        "v_mfma_f32_32x32x16_bf16 v[112:127], v[204:207], v[76:79], v[112:127]\n\t"
        "v_exp_f32 v105, v105\n\t"
        "v_exp_f32 v106, v106\n\t"
        "v_exp_f32 v107, v107\n\t"
        "v_smfmac_f32_32x32x32_bf16 v[0:15], v[208:211], v[96:103], v148\n\t"
        "v_exp_f32 v108, v108\n\t"
        "v_exp_f32 v109, v109\n\t"
        "v_exp_f32 v110, v110\n\t"
        "v_smfmac_f32_16x16x64_bf16 v[128:131], v[144:147], v[96:103], v148\n\t"
        "v_exp_f32 v111, v111\n\t"
        "v_exp_f32 v112, v112\n\t"
        "v_exp_f32 v113, v113\n\t"
        "v_smfmac_f32_32x32x32_bf16 v[0:15], v[212:215], v[104:111], v148\n\t"
        "v_exp_f32 v114, v114\n\t"
        "v_exp_f32 v115, v115\n\t"
        "v_exp_f32 v116, v116\n\t"
        "v_smfmac_f32_16x16x64_bf16 v[128:131], v[144:147], v[104:111], v148\n\t"
        "v_exp_f32 v117, v117\n\t"
        "v_mfma_f32_32x32x16_bf16 v[96:111], v[184:187], v[80:83], 0\n\t"
        "v_exp_f32 v118, v118\n\t"
        "v_exp_f32 v119, v119\n\t"
        "v_exp_f32 v120, v120\n\t"
        "ds_write_b128 v216, v[0:3] offset:0\n\t"
        "v_mfma_f32_32x32x16_bf16 v[96:111], v[188:191], v[84:87], v[96:111]\n\t"
        "v_exp_f32 v121, v121\n\t"
        "ds_write_b128 v216, v[4:7] offset:1024\n\t"
        "v_exp_f32 v122, v122\n\t"
        "ds_write_b128 v216, v[8:11] offset:2048\n\t"
        "v_exp_f32 v123, v123\n\t"
        "ds_write_b128 v216, v[12:15] offset:3072\n\t"
        "v_smfmac_f32_32x32x32_bf16 v[16:31], v[208:211], v[112:119], v148\n\t"
        "v_exp_f32 v124, v124\n\t"
        "ds_write_b32 v218, v128 offset:0\n\t"
        "v_exp_f32 v125, v125\n\t"
        "v_exp_f32 v126, v126\n\t"
        "v_smfmac_f32_16x16x64_bf16 v[132:135], v[144:147], v[112:119], v148\n\t"
        "v_exp_f32 v127, v127\n\t"
        "v_exp_f32 v96, v96\n\t"
        "v_exp_f32 v97, v97\n\t"
        "v_smfmac_f32_32x32x32_bf16 v[16:31], v[212:215], v[120:127], v148\n\t"
        "v_exp_f32 v98, v98\n\t"
        "v_exp_f32 v99, v99\n\t"
        "v_exp_f32 v100, v100\n\t"
        "v_smfmac_f32_16x16x64_bf16 v[132:135], v[144:147], v[120:127], v148\n\t"
        "v_exp_f32 v101, v101\n\t"
        "v_mfma_f32_32x32x16_bf16 v[112:127], v[184:187], v[88:91], 0\n\t"
        "v_exp_f32 v102, v102\n\t"
        "v_exp_f32 v103, v103\n\t"
        "v_exp_f32 v104, v104\n\t"
        "ds_write_b128 v216, v[16:19] offset:4096\n\t"
        "v_mfma_f32_32x32x16_bf16 v[112:127], v[188:191], v[92:95], v[112:127]\n\t"
        "v_exp_f32 v105, v105\n\t"
        "ds_write_b128 v216, v[20:23] offset:5120\n\t"
        "v_exp_f32 v106, v106\n\t"
        "ds_write_b128 v216, v[24:27] offset:6144\n\t"
        "v_exp_f32 v107, v107\n\t"
        "ds_write_b128 v216, v[28:31] offset:7168\n\t"
        "v_smfmac_f32_32x32x32_bf16 v[32:47], v[192:195], v[96:103], v148\n\t"
        "v_exp_f32 v108, v108\n\t"
        "ds_write_b32 v218, v132 offset:256\n\t"
        "v_exp_f32 v109, v109\n\t"
        "ds_write_b32 v220, v222\n\t"
        "v_exp_f32 v110, v110\n\t"
        "v_smfmac_f32_16x16x64_bf16 v[136:139], v[144:147], v[96:103], v148\n\t"
        "v_exp_f32 v111, v111\n\t"
        "v_exp_f32 v112, v112\n\t"
        "v_exp_f32 v113, v113\n\t"
        "v_smfmac_f32_32x32x32_bf16 v[32:47], v[196:199], v[104:111], v148\n\t"
        "v_exp_f32 v114, v114\n\t"
        "v_exp_f32 v115, v115\n\t"
        "v_exp_f32 v116, v116\n\t"
        "v_smfmac_f32_16x16x64_bf16 v[136:139], v[144:147], v[104:111], v148\n\t"
        "v_exp_f32 v117, v117\n\t"
        "v_mfma_f32_32x32x16_bf16 v[96:111], v[200:203], v[80:83], 0\n\t"
        "v_exp_f32 v118, v118\n\t"
        "v_exp_f32 v119, v119\n\t"
        "v_exp_f32 v120, v120\n\t"
        "v_mfma_f32_32x32x16_bf16 v[96:111], v[204:207], v[84:87], v[96:111]\n\t"
        "v_exp_f32 v121, v121\n\t"
        "v_exp_f32 v122, v122\n\t"
        "v_exp_f32 v123, v123\n\t"
        "v_smfmac_f32_32x32x32_bf16 v[48:63], v[192:195], v[112:119], v148\n\t"
        "v_exp_f32 v124, v124\n\t"
        "v_exp_f32 v125, v125\n\t"
        "v_exp_f32 v126, v126\n\t"
        "v_smfmac_f32_16x16x64_bf16 v[140:143], v[144:147], v[112:119], v148\n\t"
        "v_exp_f32 v127, v127\n\t"
        "v_exp_f32 v96, v96\n\t"
        "v_exp_f32 v97, v97\n\t"
        "v_smfmac_f32_32x32x32_bf16 v[48:63], v[196:199], v[120:127], v148\n\t"
        "v_exp_f32 v98, v98\n\t"
        "v_exp_f32 v99, v99\n\t"
        "v_exp_f32 v100, v100\n\t"
        "v_smfmac_f32_16x16x64_bf16 v[140:143], v[144:147], v[120:127], v148\n\t"
        "v_exp_f32 v101, v101\n\t"
        "v_mfma_f32_32x32x16_bf16 v[112:127], v[200:203], v[88:91], 0\n\t"
        "v_exp_f32 v102, v102\n\t"
        "v_exp_f32 v103, v103\n\t"
        "v_exp_f32 v104, v104\n\t"
        "ds_read_b32 v151, v221\n\t"
        "v_mfma_f32_32x32x16_bf16 v[112:127], v[204:207], v[92:95], v[112:127]\n\t"
        "v_exp_f32 v105, v105\n\t"
        "v_exp_f32 v106, v106\n\t"
        "v_exp_f32 v107, v107\n\t"
        "v_smfmac_f32_32x32x32_bf16 v[32:47], v[208:211], v[96:103], v148\n\t"
        "v_exp_f32 v108, v108\n\t"
        "v_exp_f32 v109, v109\n\t"
        "v_exp_f32 v110, v110\n\t"
        "v_smfmac_f32_16x16x64_bf16 v[136:139], v[144:147], v[96:103], v148\n\t"
        "v_exp_f32 v111, v111\n\t"
        "s_waitcnt lgkmcnt(0)\n\t"
        "v_cmp_ne_u32 vcc, v222, v151\n\t"
        "s_cbranch_vccnz 40f\n\t"
        "41:\n\t"
        "v_exp_f32 v112, v112\n\t"
        "ds_read_b128 v[0:3], v217 offset:0\n\t"
        "v_exp_f32 v113, v113\n\t"
        "ds_read_b128 v[4:7], v217 offset:1024\n\t"
        "v_smfmac_f32_32x32x32_bf16 v[32:47], v[212:215], v[104:111], v148\n\t"
        "v_exp_f32 v114, v114\n\t"
        "ds_read_b128 v[8:11], v217 offset:2048\n\t"
        "v_exp_f32 v115, v115\n\t"
        "ds_read_b128 v[12:15], v217 offset:3072\n\t"
        "v_exp_f32 v116, v116\n\t"
        "ds_read_b32 v128, v219 offset:0\n\t"
        "v_smfmac_f32_16x16x64_bf16 v[136:139], v[144:147], v[104:111], v148\n\t"
        "v_exp_f32 v117, v117\n\t"
        "ds_read_b128 v[16:19], v217 offset:4096\n\t"
        "v_exp_f32 v118, v118\n\t"
        "ds_read_b128 v[20:23], v217 offset:5120\n\t"
        "v_exp_f32 v119, v119\n\t"
        "ds_read_b128 v[24:27], v217 offset:6144\n\t"
        "v_exp_f32 v120, v120\n\t"
        "ds_read_b128 v[28:31], v217 offset:7168\n\t"
        "v_exp_f32 v121, v121\n\t"
        "ds_read_b32 v132, v219 offset:256\n\t"
        "v_exp_f32 v122, v122\n\t"
        "v_exp_f32 v123, v123\n\t"
        "v_smfmac_f32_32x32x32_bf16 v[48:63], v[208:211], v[112:119], v148\n\t"
        "v_exp_f32 v124, v124\n\t"
        "v_exp_f32 v125, v125\n\t"
        "v_exp_f32 v126, v126\n\t"
        "v_smfmac_f32_16x16x64_bf16 v[140:143], v[144:147], v[112:119], v148\n\t"
        "v_exp_f32 v127, v127\n\t"
        "s_nop 1\n\t"
        "v_smfmac_f32_32x32x32_bf16 v[48:63], v[212:215], v[120:127], v148\n\t"
        "v_smfmac_f32_16x16x64_bf16 v[140:143], v[144:147], v[120:127], v148\n\t"
        "s_waitcnt vmcnt(0) lgkmcnt(0)\n\t"
        "s_nop 6\n\t"
        "s_branch 9f\n\t"
        "30:\n\t"
        "s_add_u32 s44, s44, 0x4000\n\t"
        "s_and_b32 s44, s44, 0x3ffff\n\t"
        "buffer_load_dwordx4 v[184:187], v149, s[40:43], s44 offen offset:0 sc1\n\t"
        "buffer_load_dwordx4 v[188:191], v149, s[40:43], s44 offen offset:1024 sc1\n\t"
        "buffer_load_dwordx4 v[192:195], v149, s[40:43], s44 offen offset:2048 sc1\n\t"
        "buffer_load_dwordx4 v[196:199], v149, s[40:43], s44 offen offset:3072 sc1\n\t"
        "buffer_load_dwordx4 v[200:203], v150, s[40:43], s44 offen offset:0 sc1\n\t"
        "buffer_load_dwordx4 v[204:207], v150, s[40:43], s44 offen offset:1024 sc1\n\t"
        "buffer_load_dwordx4 v[208:211], v150, s[40:43], s44 offen offset:2048 sc1\n\t"
        "buffer_load_dwordx4 v[212:215], v150, s[40:43], s44 offen offset:3072 sc1\n\t"
        "s_add_u32 s44, s44, 0x4000\n\t"
        "s_and_b32 s44, s44, 0x3ffff\n\t"
        "buffer_load_dwordx4 v[152:155], v149, s[40:43], s44 offen offset:0 sc1\n\t"
        "buffer_load_dwordx4 v[156:159], v149, s[40:43], s44 offen offset:1024 sc1\n\t"
        "buffer_load_dwordx4 v[160:163], v149, s[40:43], s44 offen offset:2048 sc1\n\t"
        "buffer_load_dwordx4 v[164:167], v149, s[40:43], s44 offen offset:3072 sc1\n\t"
        "s_mov_b32 s46, 1\n\t"
        "s_branch 31b\n\t"
        "32:\n\t"
        "s_add_u32 s44, s44, 0x4000\n\t"
        "s_and_b32 s44, s44, 0x3ffff\n\t"
        "buffer_load_dwordx4 v[184:187], v149, s[40:43], s44 offen offset:0 sc1\n\t"
        "buffer_load_dwordx4 v[188:191], v149, s[40:43], s44 offen offset:1024 sc1\n\t"
        "buffer_load_dwordx4 v[192:195], v149, s[40:43], s44 offen offset:2048 sc1\n\t"
        "buffer_load_dwordx4 v[196:199], v149, s[40:43], s44 offen offset:3072 sc1\n\t"
        "buffer_load_dwordx4 v[200:203], v150, s[40:43], s44 offen offset:0 sc1\n\t"
        "buffer_load_dwordx4 v[204:207], v150, s[40:43], s44 offen offset:1024 sc1\n\t"
        "buffer_load_dwordx4 v[208:211], v150, s[40:43], s44 offen offset:2048 sc1\n\t"
        "buffer_load_dwordx4 v[212:215], v150, s[40:43], s44 offen offset:3072 sc1\n\t"
        "s_add_u32 s44, s44, 0x4000\n\t"
        "s_and_b32 s44, s44, 0x3ffff\n\t"
        "buffer_load_dwordx4 v[152:155], v149, s[40:43], s44 offen offset:0 sc1\n\t"
        "buffer_load_dwordx4 v[156:159], v149, s[40:43], s44 offen offset:1024 sc1\n\t"
        "buffer_load_dwordx4 v[160:163], v149, s[40:43], s44 offen offset:2048 sc1\n\t"
        "buffer_load_dwordx4 v[164:167], v149, s[40:43], s44 offen offset:3072 sc1\n\t"
        "s_mov_b32 s46, 1\n\t"
        "s_branch 33b\n\t"
        "40:\n\t"
        "s_mov_b32 s45, 0x100000\n\t"
        "42:\n\t"
        "ds_read_b32 v151, v221\n\t"
        "s_sub_u32 s45, s45, 1\n\t"
        "s_waitcnt lgkmcnt(0)\n\t"
        "s_cbranch_scc1 41b\n\t"
        "v_cmp_ne_u32 vcc, v222, v151\n\t"
        "s_cbranch_vccnz 42b\n\t"
        "s_branch 41b\n\t"
        "9:\n\t"
        : "+{v[0:15]}"(o0), "+{v[16:31]}"(o1), "+{v[32:47]}"(o2), "+{v[48:63]}"(o3), "+{v[128:143]}"(lv), "+{s44}"(soff), "+{v[152:167]}"(kva), "+{v[168:183]}"(kvb), "+{v[184:191]}"(fv),
          [r0] "+v"(x1p[0]), [r1] "+v"(x1p[1]), [r2] "+v"(x1p[2]), [r3] "+v"(x1p[3]) STAMP_OUTS
        : "{v[64:71]}"(q[0]), "{v[72:79]}"(q[1]), "{v[80:87]}"(q[2]), "{v[88:95]}"(q[3]), "{v[144:147]}"(ones), "{v148}"(0xddddu), "{v149}"(kvoff), "{v150}"(kvoff + 4096),
          "{v[216:223]}"(cv), "{v224}"(lbase + L_W + 16), "{s[40:43]}"(srd), "{s47}"(L), [xb] "s"(x0)
        : "v96", "v97", "v98", "v99", "v100", "v101", "v102", "v103", "v104", "v105", "v106", "v107", "v108", "v109", "v110", "v111", "v112", "v113", "v114", "v115", "v116", "v117", "v118", "v119", "v120", "v121", "v122", "v123", "v124", "v125", "v126", "v127", "v151", "v192", "v193", "v194", "v195", "v196", "v197", "v198", "v199", "v200", "v201", "v202", "v203", "v204", "v205", "v206", "v207", "v208", "v209", "v210", "v211", "v212", "v213", "v214", "v215", "s45", "s46", "vcc", "scc", "memory");
      STAMP_PUT(6 + 16 * L);
      STAMPV(9 + 16 * L);
      PHASE_VARS;
      const unsigned dst = OFF_OF + L * (5 * MBY) + (tm * 4 + 2 * kh) * TS + (tn * 2 + hh) * 2048 + lane * 16;
      auto put = [&](int u, const f32x16& o) {
#pragma unroll
        for (int c = 0; c < 2; ++c) {
          u32x4 w;
#pragma unroll
          for (int e = 0; e < 4; ++e) w[e] = pack2(o[8 * c + 2 * e], o[8 * c + 2 * e + 1]);
          stg(rs, dst + u * TS + c * 1024, w);
        }
      };
      const float la = lv[8] + lv[0], lb = lv[12] + lv[4];
      o2 = (o2 + o0) * RCP(la); put(0, o2);
      SBAR();
      o3 = (o3 + o1) * RCP(lb); put(1, o3);
      SBAR();
      {
        const f32x16 t = o2 + o3;
        float sum = 0.f;
#pragma unroll
        for (int e = 0; e < 16; e += 4) sum += (t[e] + t[e + 1]) + (t[e + 2] + t[e + 3]);
        if (__any(!(fabsf(sum) < 1e37f) | !(fmaxf(la, lb) < 1e27f) | !(fminf(la, lb) > 1e-9f) | !(la + lb == la + lb))) {
          DRAIN();
          attn_safe(rs, kvoff - kh * 8192, q[2], ones, o2); put(0, o2);
          attn_safe(rs, kvoff - kh * 8192, q[3], ones, o3); put(1, o3);
        }
      }
    }
    STAMPV(10 + 16 * L);
    {
      f32x16 acc[2];
      u32x4 res[4];
      u32x2 wrc[8];
      STAMP_DECL;
      { PHASE_VARS;
      BAR_ALL();
      STAMPV(11 + 16 * L);
#pragma unroll
      for (int g = 0; g < 8; ++g) wrc[g] = *reinterpret_cast<const u32x2*>(smem + L_WR + g * 16 + hi * 8);
      if (L == 0) {
#pragma unroll
        for (int i = 0; i < 4; ++i) res[i] = fo_swap(x1p[i]);
      } else {
        const bool routed = (int)words[3] == tile * 32 + r32;
        const float mb1 = reinterpret_cast<const float*>(smem + L_AB)[256 + wid * 64 + r32], rd1 = reinterpret_cast<const float*>(smem + L_AB)[256 + wid * 64 + 32 + r32];
#pragma unroll
        for (int i = 0; i < 4; ++i) {
          const u32x4 pr = *reinterpret_cast<const u32x4*>(smem + L_X2P + (i * 2 + hi) * 16);
#pragma unroll
          for (int e = 0; e < 4; ++e) {
            const unsigned h2 = ln_pair(x1p[i][e], mb1, rd1);
            unsigned t = pack2(lo16(x1p[i][e]) + lo16(h2), hi16(x1p[i][e]) + hi16(h2));
            PIN(t);
            res[i][e] = routed ? pr[e] : t;
          }
        }
      }
#pragma unroll
      for (int g = 0; g < 8; ++g) PIN(wrc[g]);
#pragma unroll
      for (int i = 0; i < 4; ++i) PIN(res[i]);
      unsigned soff = tn * 4096;
      const unsigned a0 = lbase + wrow * 128 + ((wch ^ wsw) << 4) + 8 * (tp & 1), a1 = lbase + wrow * 128 + (((4 + wch) ^ wsw) << 4) + 8 * (tp & 1);
      asm volatile(
        "ds_read_b64_tr_b16 v[32:33], v193 offset:0\n\t"
        "ds_read_b64_tr_b16 v[34:35], v193 offset:1024\n\t"
        "ds_read_b64_tr_b16 v[36:37], v194 offset:0\n\t"
        "ds_read_b64_tr_b16 v[38:39], v194 offset:1024\n\t"
        "ds_read_b64_tr_b16 v[40:41], v193 offset:2048\n\t"
        "ds_read_b64_tr_b16 v[42:43], v193 offset:3072\n\t"
        "ds_read_b64_tr_b16 v[44:45], v194 offset:2048\n\t"
        "ds_read_b64_tr_b16 v[46:47], v194 offset:3072\n\t"
        "ds_read_b64_tr_b16 v[48:49], v193 offset:4096\n\t"
        "ds_read_b64_tr_b16 v[50:51], v193 offset:5120\n\t"
        "ds_read_b64_tr_b16 v[52:53], v194 offset:4096\n\t"
        "ds_read_b64_tr_b16 v[54:55], v194 offset:5120\n\t"
        "ds_read_b64_tr_b16 v[56:57], v193 offset:6144\n\t"
        "ds_read_b64_tr_b16 v[58:59], v193 offset:7168\n\t"
        "ds_read_b64_tr_b16 v[60:61], v194 offset:6144\n\t"
        "ds_read_b64_tr_b16 v[62:63], v194 offset:7168\n\t"
        "s_mov_b32 s45, 0x200000\n\t"
        "2:\n\t"
        "ds_read_b32 v200, v198\n\t"
        "s_sub_u32 s45, s45, 1\n\t"
        "s_waitcnt lgkmcnt(0)\n\t"
        "s_cbranch_scc1 3f\n\t"
        "v_cmp_ne_u32 vcc, v199, v200\n\t"
        "s_cbranch_vccnz 2b\n\t"
        "3:\n\t"
        "buffer_load_dwordx4 v[64:67], v192, s[40:43], s44 offen offset:0 sc1\n\t"
        "buffer_load_dwordx4 v[68:71], v192, s[40:43], s44 offen offset:1024 sc1\n\t"
        "buffer_load_dwordx4 v[72:75], v192, s[40:43], s44 offen offset:2048 sc1\n\t"
        "buffer_load_dwordx4 v[76:79], v192, s[40:43], s44 offen offset:3072 sc1\n\t"
        "s_add_u32 s44, s44, 0x1000\n\t"
        "s_and_b32 s44, s44, 0xffff\n\t"
        "buffer_load_dwordx4 v[80:83], v192, s[40:43], s44 offen offset:0 sc1\n\t"
        "buffer_load_dwordx4 v[84:87], v192, s[40:43], s44 offen offset:1024 sc1\n\t"
        "buffer_load_dwordx4 v[88:91], v192, s[40:43], s44 offen offset:2048 sc1\n\t"
        "buffer_load_dwordx4 v[92:95], v192, s[40:43], s44 offen offset:3072 sc1\n\t"
        "s_add_u32 s44, s44, 0x1000\n\t"
        "s_and_b32 s44, s44, 0xffff\n\t"
        "buffer_load_dwordx4 v[96:99], v192, s[40:43], s44 offen offset:0 sc1\n\t"
        "buffer_load_dwordx4 v[100:103], v192, s[40:43], s44 offen offset:1024 sc1\n\t"
        "buffer_load_dwordx4 v[104:107], v192, s[40:43], s44 offen offset:2048 sc1\n\t"
        "buffer_load_dwordx4 v[108:111], v192, s[40:43], s44 offen offset:3072 sc1\n\t"
        "s_waitcnt vmcnt(11) lgkmcnt(14)\n\t"
        "v_mfma_f32_32x32x16_bf16 v[0:15], v[32:35], v[64:67], 0\n\t"
        "ds_read_b64_tr_b16 v[32:33], v193 offset:8192\n\t"
        "ds_read_b64_tr_b16 v[34:35], v193 offset:9216\n\t"
        "s_waitcnt lgkmcnt(14)\n\t"
        "v_mfma_f32_32x32x16_bf16 v[16:31], v[36:39], v[64:67], 0\n\t"
        "ds_read_b64_tr_b16 v[36:37], v194 offset:8192\n\t"
        "ds_read_b64_tr_b16 v[38:39], v194 offset:9216\n\t"
        "s_add_u32 s44, s44, 0x1000\n\t"
        "s_and_b32 s44, s44, 0xffff\n\t"
        "buffer_load_dwordx4 v[112:115], v192, s[40:43], s44 offen offset:0 sc1\n\t"
        "s_waitcnt vmcnt(11) lgkmcnt(14)\n\t"
        "v_mfma_f32_32x32x16_bf16 v[0:15], v[40:43], v[68:71], v[0:15]\n\t"
        "ds_read_b64_tr_b16 v[40:41], v193 offset:10240\n\t"
        "ds_read_b64_tr_b16 v[42:43], v193 offset:11264\n\t"
        "s_waitcnt lgkmcnt(14)\n\t"
        "v_mfma_f32_32x32x16_bf16 v[16:31], v[44:47], v[68:71], v[16:31]\n\t"
        "ds_read_b64_tr_b16 v[44:45], v194 offset:10240\n\t"
        "ds_read_b64_tr_b16 v[46:47], v194 offset:11264\n\t"
        "buffer_load_dwordx4 v[116:119], v192, s[40:43], s44 offen offset:1024 sc1\n\t"
        "s_waitcnt vmcnt(11) lgkmcnt(14)\n\t"
        "v_mfma_f32_32x32x16_bf16 v[0:15], v[48:51], v[72:75], v[0:15]\n\t"
        "ds_read_b64_tr_b16 v[48:49], v193 offset:12288\n\t"
        "ds_read_b64_tr_b16 v[50:51], v193 offset:13312\n\t"
        "s_waitcnt lgkmcnt(14)\n\t"
        "v_mfma_f32_32x32x16_bf16 v[16:31], v[52:55], v[72:75], v[16:31]\n\t"
        "ds_read_b64_tr_b16 v[52:53], v194 offset:12288\n\t"
        "ds_read_b64_tr_b16 v[54:55], v194 offset:13312\n\t"
        "buffer_load_dwordx4 v[120:123], v192, s[40:43], s44 offen offset:2048 sc1\n\t"
        "s_waitcnt vmcnt(11) lgkmcnt(14)\n\t"
        "v_mfma_f32_32x32x16_bf16 v[0:15], v[56:59], v[76:79], v[0:15]\n\t"
        "ds_read_b64_tr_b16 v[56:57], v193 offset:14336\n\t"
        "ds_read_b64_tr_b16 v[58:59], v193 offset:15360\n\t"
        "s_waitcnt lgkmcnt(14)\n\t"
        "v_mfma_f32_32x32x16_bf16 v[16:31], v[60:63], v[76:79], v[16:31]\n\t"
        "ds_read_b64_tr_b16 v[60:61], v194 offset:14336\n\t"
        "ds_read_b64_tr_b16 v[62:63], v194 offset:15360\n\t"
        "buffer_load_dwordx4 v[124:127], v192, s[40:43], s44 offen offset:3072 sc1\n\t"
        "s_waitcnt vmcnt(11) lgkmcnt(14)\n\t"
        "v_mfma_f32_32x32x16_bf16 v[0:15], v[32:35], v[80:83], v[0:15]\n\t"
        "ds_read_b64_tr_b16 v[32:33], v193 offset:16384\n\t"
        "ds_read_b64_tr_b16 v[34:35], v193 offset:17408\n\t"
        "s_waitcnt lgkmcnt(14)\n\t"
        "v_mfma_f32_32x32x16_bf16 v[16:31], v[36:39], v[80:83], v[16:31]\n\t"
        "ds_read_b64_tr_b16 v[36:37], v194 offset:16384\n\t"
        "ds_read_b64_tr_b16 v[38:39], v194 offset:17408\n\t"
        "s_add_u32 s44, s44, 0x1000\n\t"
        "s_and_b32 s44, s44, 0xffff\n\t"
        "buffer_load_dwordx4 v[128:131], v192, s[40:43], s44 offen offset:0 sc1\n\t"
        "s_waitcnt vmcnt(11) lgkmcnt(14)\n\t"
        "v_mfma_f32_32x32x16_bf16 v[0:15], v[40:43], v[84:87], v[0:15]\n\t"
        "ds_read_b64_tr_b16 v[40:41], v193 offset:18432\n\t"
        "ds_read_b64_tr_b16 v[42:43], v193 offset:19456\n\t"
        "s_waitcnt lgkmcnt(14)\n\t"
        "v_mfma_f32_32x32x16_bf16 v[16:31], v[44:47], v[84:87], v[16:31]\n\t"
        "ds_read_b64_tr_b16 v[44:45], v194 offset:18432\n\t"
        "ds_read_b64_tr_b16 v[46:47], v194 offset:19456\n\t"
        "buffer_load_dwordx4 v[132:135], v192, s[40:43], s44 offen offset:1024 sc1\n\t"
        "s_waitcnt vmcnt(11) lgkmcnt(14)\n\t"
        "v_mfma_f32_32x32x16_bf16 v[0:15], v[48:51], v[88:91], v[0:15]\n\t"
        "ds_read_b64_tr_b16 v[48:49], v193 offset:20480\n\t"
        "ds_read_b64_tr_b16 v[50:51], v193 offset:21504\n\t"
        "s_waitcnt lgkmcnt(14)\n\t"
        "v_mfma_f32_32x32x16_bf16 v[16:31], v[52:55], v[88:91], v[16:31]\n\t"
        "ds_read_b64_tr_b16 v[52:53], v194 offset:20480\n\t"
        "ds_read_b64_tr_b16 v[54:55], v194 offset:21504\n\t"
        "buffer_load_dwordx4 v[136:139], v192, s[40:43], s44 offen offset:2048 sc1\n\t"
        "s_waitcnt vmcnt(11) lgkmcnt(14)\n\t"
        "v_mfma_f32_32x32x16_bf16 v[0:15], v[56:59], v[92:95], v[0:15]\n\t"
        "ds_read_b64_tr_b16 v[56:57], v193 offset:22528\n\t"
        "ds_read_b64_tr_b16 v[58:59], v193 offset:23552\n\t"
        "s_waitcnt lgkmcnt(14)\n\t"
        "v_mfma_f32_32x32x16_bf16 v[16:31], v[60:63], v[92:95], v[16:31]\n\t"
        "ds_read_b64_tr_b16 v[60:61], v194 offset:22528\n\t"
        "ds_read_b64_tr_b16 v[62:63], v194 offset:23552\n\t"
        "buffer_load_dwordx4 v[140:143], v192, s[40:43], s44 offen offset:3072 sc1\n\t"
        "s_waitcnt vmcnt(11) lgkmcnt(14)\n\t"
        "v_mfma_f32_32x32x16_bf16 v[0:15], v[32:35], v[96:99], v[0:15]\n\t"
        "ds_read_b64_tr_b16 v[32:33], v193 offset:24576\n\t"
        "ds_read_b64_tr_b16 v[34:35], v193 offset:25600\n\t"
        "s_waitcnt lgkmcnt(14)\n\t"
        "v_mfma_f32_32x32x16_bf16 v[16:31], v[36:39], v[96:99], v[16:31]\n\t"
        "ds_read_b64_tr_b16 v[36:37], v194 offset:24576\n\t"
        "ds_read_b64_tr_b16 v[38:39], v194 offset:25600\n\t"
        "s_add_u32 s44, s44, 0x1000\n\t"
        "s_and_b32 s44, s44, 0xffff\n\t"
        "buffer_load_dwordx4 v[144:147], v192, s[40:43], s44 offen offset:0 sc1\n\t"
        "s_waitcnt vmcnt(11) lgkmcnt(14)\n\t"
        "v_mfma_f32_32x32x16_bf16 v[0:15], v[40:43], v[100:103], v[0:15]\n\t"
        "ds_read_b64_tr_b16 v[40:41], v193 offset:26624\n\t"
        "ds_read_b64_tr_b16 v[42:43], v193 offset:27648\n\t"
        "s_waitcnt lgkmcnt(14)\n\t"
        "v_mfma_f32_32x32x16_bf16 v[16:31], v[44:47], v[100:103], v[16:31]\n\t"
        "ds_read_b64_tr_b16 v[44:45], v194 offset:26624\n\t"
        "ds_read_b64_tr_b16 v[46:47], v194 offset:27648\n\t"
        "buffer_load_dwordx4 v[148:151], v192, s[40:43], s44 offen offset:1024 sc1\n\t"
        "s_waitcnt vmcnt(11) lgkmcnt(14)\n\t"
        "v_mfma_f32_32x32x16_bf16 v[0:15], v[48:51], v[104:107], v[0:15]\n\t"
        "ds_read_b64_tr_b16 v[48:49], v193 offset:28672\n\t"
        "ds_read_b64_tr_b16 v[50:51], v193 offset:29696\n\t"
        "s_waitcnt lgkmcnt(14)\n\t"
        "v_mfma_f32_32x32x16_bf16 v[16:31], v[52:55], v[104:107], v[16:31]\n\t"
        "ds_read_b64_tr_b16 v[52:53], v194 offset:28672\n\t"
        "ds_read_b64_tr_b16 v[54:55], v194 offset:29696\n\t"
        "buffer_load_dwordx4 v[152:155], v192, s[40:43], s44 offen offset:2048 sc1\n\t"
        "s_waitcnt vmcnt(11) lgkmcnt(14)\n\t"
        "v_mfma_f32_32x32x16_bf16 v[0:15], v[56:59], v[108:111], v[0:15]\n\t"
        "ds_read_b64_tr_b16 v[56:57], v193 offset:30720\n\t"
        "ds_read_b64_tr_b16 v[58:59], v193 offset:31744\n\t"
        "s_waitcnt lgkmcnt(14)\n\t"
        "v_mfma_f32_32x32x16_bf16 v[16:31], v[60:63], v[108:111], v[16:31]\n\t"
        "ds_read_b64_tr_b16 v[60:61], v194 offset:30720\n\t"
        "ds_read_b64_tr_b16 v[62:63], v194 offset:31744\n\t"
        "buffer_load_dwordx4 v[156:159], v192, s[40:43], s44 offen offset:3072 sc1\n\t"
        "s_waitcnt vmcnt(11) lgkmcnt(14)\n\t"
        "v_mfma_f32_32x32x16_bf16 v[0:15], v[32:35], v[112:115], v[0:15]\n\t"
        "ds_read_b64_tr_b16 v[32:33], v193 offset:32768\n\t"
        "ds_read_b64_tr_b16 v[34:35], v193 offset:33792\n\t"
        "s_waitcnt lgkmcnt(14)\n\t"
        "v_mfma_f32_32x32x16_bf16 v[16:31], v[36:39], v[112:115], v[16:31]\n\t"
        "ds_read_b64_tr_b16 v[36:37], v194 offset:32768\n\t"
        "ds_read_b64_tr_b16 v[38:39], v194 offset:33792\n\t"
        "s_add_u32 s44, s44, 0x1000\n\t"
        "s_and_b32 s44, s44, 0xffff\n\t"
        "buffer_load_dwordx4 v[160:163], v192, s[40:43], s44 offen offset:0 sc1\n\t"
        "s_waitcnt vmcnt(11) lgkmcnt(14)\n\t"
        "v_mfma_f32_32x32x16_bf16 v[0:15], v[40:43], v[116:119], v[0:15]\n\t"
        "ds_read_b64_tr_b16 v[40:41], v193 offset:34816\n\t"
        "ds_read_b64_tr_b16 v[42:43], v193 offset:35840\n\t"
        "s_waitcnt lgkmcnt(14)\n\t"
        "v_mfma_f32_32x32x16_bf16 v[16:31], v[44:47], v[116:119], v[16:31]\n\t"
        "ds_read_b64_tr_b16 v[44:45], v194 offset:34816\n\t"
        "ds_read_b64_tr_b16 v[46:47], v194 offset:35840\n\t"
        "buffer_load_dwordx4 v[164:167], v192, s[40:43], s44 offen offset:1024 sc1\n\t"
        "s_waitcnt vmcnt(11) lgkmcnt(14)\n\t"
        "v_mfma_f32_32x32x16_bf16 v[0:15], v[48:51], v[120:123], v[0:15]\n\t"
        "ds_read_b64_tr_b16 v[48:49], v193 offset:36864\n\t"
        "ds_read_b64_tr_b16 v[50:51], v193 offset:37888\n\t"
        "s_waitcnt lgkmcnt(14)\n\t"
        "v_mfma_f32_32x32x16_bf16 v[16:31], v[52:55], v[120:123], v[16:31]\n\t"
        "ds_read_b64_tr_b16 v[52:53], v194 offset:36864\n\t"
        "ds_read_b64_tr_b16 v[54:55], v194 offset:37888\n\t"
        "buffer_load_dwordx4 v[168:171], v192, s[40:43], s44 offen offset:2048 sc1\n\t"
        "s_waitcnt vmcnt(11) lgkmcnt(14)\n\t"
        "v_mfma_f32_32x32x16_bf16 v[0:15], v[56:59], v[124:127], v[0:15]\n\t"
        "ds_read_b64_tr_b16 v[56:57], v193 offset:38912\n\t"
        "ds_read_b64_tr_b16 v[58:59], v193 offset:39936\n\t"
        "s_waitcnt lgkmcnt(14)\n\t"
        "v_mfma_f32_32x32x16_bf16 v[16:31], v[60:63], v[124:127], v[16:31]\n\t"
        "ds_read_b64_tr_b16 v[60:61], v194 offset:38912\n\t"
        "ds_read_b64_tr_b16 v[62:63], v194 offset:39936\n\t"
        "buffer_load_dwordx4 v[172:175], v192, s[40:43], s44 offen offset:3072 sc1\n\t"
        "s_waitcnt vmcnt(11) lgkmcnt(14)\n\t"
        "v_mfma_f32_32x32x16_bf16 v[0:15], v[32:35], v[128:131], v[0:15]\n\t"
        "ds_read_b64_tr_b16 v[32:33], v193 offset:40960\n\t"
        "ds_read_b64_tr_b16 v[34:35], v193 offset:41984\n\t"
        "s_waitcnt lgkmcnt(14)\n\t"
        "v_mfma_f32_32x32x16_bf16 v[16:31], v[36:39], v[128:131], v[16:31]\n\t"
        "ds_read_b64_tr_b16 v[36:37], v194 offset:40960\n\t"
        "ds_read_b64_tr_b16 v[38:39], v194 offset:41984\n\t"
        "s_add_u32 s44, s44, 0x1000\n\t"
        "s_and_b32 s44, s44, 0xffff\n\t"
        "buffer_load_dwordx4 v[176:179], v192, s[40:43], s44 offen offset:0 sc1\n\t"
        "s_waitcnt vmcnt(11) lgkmcnt(14)\n\t"
        "v_mfma_f32_32x32x16_bf16 v[0:15], v[40:43], v[132:135], v[0:15]\n\t"
        "ds_read_b64_tr_b16 v[40:41], v193 offset:43008\n\t"
        "ds_read_b64_tr_b16 v[42:43], v193 offset:44032\n\t"
        "s_waitcnt lgkmcnt(14)\n\t"
        "v_mfma_f32_32x32x16_bf16 v[16:31], v[44:47], v[132:135], v[16:31]\n\t"
        "ds_read_b64_tr_b16 v[44:45], v194 offset:43008\n\t"
        "ds_read_b64_tr_b16 v[46:47], v194 offset:44032\n\t"
        "buffer_load_dwordx4 v[180:183], v192, s[40:43], s44 offen offset:1024 sc1\n\t"
        "s_waitcnt vmcnt(11) lgkmcnt(14)\n\t"
        "v_mfma_f32_32x32x16_bf16 v[0:15], v[48:51], v[136:139], v[0:15]\n\t"
        "ds_read_b64_tr_b16 v[48:49], v193 offset:45056\n\t"
        "ds_read_b64_tr_b16 v[50:51], v193 offset:46080\n\t"
        "s_waitcnt lgkmcnt(14)\n\t"
        "v_mfma_f32_32x32x16_bf16 v[16:31], v[52:55], v[136:139], v[16:31]\n\t"
        "ds_read_b64_tr_b16 v[52:53], v194 offset:45056\n\t"
        "ds_read_b64_tr_b16 v[54:55], v194 offset:46080\n\t"
        "buffer_load_dwordx4 v[184:187], v192, s[40:43], s44 offen offset:2048 sc1\n\t"
        "s_waitcnt vmcnt(11) lgkmcnt(14)\n\t"
        "v_mfma_f32_32x32x16_bf16 v[0:15], v[56:59], v[140:143], v[0:15]\n\t"
        "ds_read_b64_tr_b16 v[56:57], v193 offset:47104\n\t"
        "ds_read_b64_tr_b16 v[58:59], v193 offset:48128\n\t"
        "s_waitcnt lgkmcnt(14)\n\t"
        "v_mfma_f32_32x32x16_bf16 v[16:31], v[60:63], v[140:143], v[16:31]\n\t"
        "ds_read_b64_tr_b16 v[60:61], v194 offset:47104\n\t"
        "ds_read_b64_tr_b16 v[62:63], v194 offset:48128\n\t"
        "buffer_load_dwordx4 v[188:191], v192, s[40:43], s44 offen offset:3072 sc1\n\t"
        "s_waitcnt vmcnt(11) lgkmcnt(14)\n\t"
        "v_mfma_f32_32x32x16_bf16 v[0:15], v[32:35], v[144:147], v[0:15]\n\t"
        "ds_read_b64_tr_b16 v[32:33], v193 offset:49152\n\t"
        "ds_read_b64_tr_b16 v[34:35], v193 offset:50176\n\t"
        "s_waitcnt lgkmcnt(14)\n\t"
        "v_mfma_f32_32x32x16_bf16 v[16:31], v[36:39], v[144:147], v[16:31]\n\t"
        "ds_read_b64_tr_b16 v[36:37], v194 offset:49152\n\t"
        "ds_read_b64_tr_b16 v[38:39], v194 offset:50176\n\t"
        "s_add_u32 s44, s44, 0x1000\n\t"
        "s_and_b32 s44, s44, 0xffff\n\t"
        "buffer_load_dwordx4 v[64:67], v192, s[40:43], s44 offen offset:0 sc1\n\t"
        "s_waitcnt vmcnt(11) lgkmcnt(14)\n\t"
        "v_mfma_f32_32x32x16_bf16 v[0:15], v[40:43], v[148:151], v[0:15]\n\t"
        "ds_read_b64_tr_b16 v[40:41], v193 offset:51200\n\t"
        "ds_read_b64_tr_b16 v[42:43], v193 offset:52224\n\t"
        "s_waitcnt lgkmcnt(14)\n\t"
        "v_mfma_f32_32x32x16_bf16 v[16:31], v[44:47], v[148:151], v[16:31]\n\t"
        "ds_read_b64_tr_b16 v[44:45], v194 offset:51200\n\t"
        "ds_read_b64_tr_b16 v[46:47], v194 offset:52224\n\t"
        "buffer_load_dwordx4 v[68:71], v192, s[40:43], s44 offen offset:1024 sc1\n\t"
        "s_waitcnt vmcnt(11) lgkmcnt(14)\n\t"
        "v_mfma_f32_32x32x16_bf16 v[0:15], v[48:51], v[152:155], v[0:15]\n\t"
        "ds_read_b64_tr_b16 v[48:49], v193 offset:53248\n\t"
        "ds_read_b64_tr_b16 v[50:51], v193 offset:54272\n\t"
        "s_waitcnt lgkmcnt(14)\n\t"
        "v_mfma_f32_32x32x16_bf16 v[16:31], v[52:55], v[152:155], v[16:31]\n\t"
        "ds_read_b64_tr_b16 v[52:53], v194 offset:53248\n\t"
        "ds_read_b64_tr_b16 v[54:55], v194 offset:54272\n\t"
        "buffer_load_dwordx4 v[72:75], v192, s[40:43], s44 offen offset:2048 sc1\n\t"
        "s_waitcnt vmcnt(11) lgkmcnt(14)\n\t"
        "v_mfma_f32_32x32x16_bf16 v[0:15], v[56:59], v[156:159], v[0:15]\n\t"
        "ds_read_b64_tr_b16 v[56:57], v193 offset:55296\n\t"
        "ds_read_b64_tr_b16 v[58:59], v193 offset:56320\n\t"
        "s_waitcnt lgkmcnt(14)\n\t"
        "v_mfma_f32_32x32x16_bf16 v[16:31], v[60:63], v[156:159], v[16:31]\n\t"
        "ds_read_b64_tr_b16 v[60:61], v194 offset:55296\n\t"
        "ds_read_b64_tr_b16 v[62:63], v194 offset:56320\n\t"
        "buffer_load_dwordx4 v[76:79], v192, s[40:43], s44 offen offset:3072 sc1\n\t"
        "s_waitcnt vmcnt(11) lgkmcnt(14)\n\t"
        "v_mfma_f32_32x32x16_bf16 v[0:15], v[32:35], v[160:163], v[0:15]\n\t"
        "ds_read_b64_tr_b16 v[32:33], v193 offset:57344\n\t"
        "ds_read_b64_tr_b16 v[34:35], v193 offset:58368\n\t"
        "s_waitcnt lgkmcnt(14)\n\t"
        "v_mfma_f32_32x32x16_bf16 v[16:31], v[36:39], v[160:163], v[16:31]\n\t"
        "ds_read_b64_tr_b16 v[36:37], v194 offset:57344\n\t"
        "ds_read_b64_tr_b16 v[38:39], v194 offset:58368\n\t"
        "s_add_u32 s44, s44, 0x1000\n\t"
        "s_and_b32 s44, s44, 0xffff\n\t"
        "buffer_load_dwordx4 v[80:83], v192, s[40:43], s44 offen offset:0 sc1\n\t"
        "s_waitcnt vmcnt(11) lgkmcnt(14)\n\t"
        "v_mfma_f32_32x32x16_bf16 v[0:15], v[40:43], v[164:167], v[0:15]\n\t"
        "ds_read_b64_tr_b16 v[40:41], v193 offset:59392\n\t"
        "ds_read_b64_tr_b16 v[42:43], v193 offset:60416\n\t"
        "s_waitcnt lgkmcnt(14)\n\t"
        "v_mfma_f32_32x32x16_bf16 v[16:31], v[44:47], v[164:167], v[16:31]\n\t"
        "ds_read_b64_tr_b16 v[44:45], v194 offset:59392\n\t"
        "ds_read_b64_tr_b16 v[46:47], v194 offset:60416\n\t"
        "buffer_load_dwordx4 v[84:87], v192, s[40:43], s44 offen offset:1024 sc1\n\t"
        "s_waitcnt vmcnt(11) lgkmcnt(14)\n\t"
        "v_mfma_f32_32x32x16_bf16 v[0:15], v[48:51], v[168:171], v[0:15]\n\t"
        "ds_read_b64_tr_b16 v[48:49], v193 offset:61440\n\t"
        "ds_read_b64_tr_b16 v[50:51], v193 offset:62464\n\t"
        "s_waitcnt lgkmcnt(14)\n\t"
        "v_mfma_f32_32x32x16_bf16 v[16:31], v[52:55], v[168:171], v[16:31]\n\t"
        "ds_read_b64_tr_b16 v[52:53], v194 offset:61440\n\t"
        "ds_read_b64_tr_b16 v[54:55], v194 offset:62464\n\t"
        "buffer_load_dwordx4 v[88:91], v192, s[40:43], s44 offen offset:2048 sc1\n\t"
        "s_waitcnt vmcnt(11) lgkmcnt(14)\n\t"
        "v_mfma_f32_32x32x16_bf16 v[0:15], v[56:59], v[172:175], v[0:15]\n\t"
        "ds_read_b64_tr_b16 v[56:57], v193 offset:63488\n\t"
        "ds_read_b64_tr_b16 v[58:59], v193 offset:64512\n\t"
        "s_waitcnt lgkmcnt(14)\n\t"
        "v_mfma_f32_32x32x16_bf16 v[16:31], v[60:63], v[172:175], v[16:31]\n\t"
        "ds_read_b64_tr_b16 v[60:61], v194 offset:63488\n\t"
        "ds_read_b64_tr_b16 v[62:63], v194 offset:64512\n\t"
        "buffer_load_dwordx4 v[92:95], v192, s[40:43], s44 offen offset:3072 sc1\n\t"
        "s_waitcnt vmcnt(11) lgkmcnt(14)\n\t"
        "v_mfma_f32_32x32x16_bf16 v[0:15], v[32:35], v[176:179], v[0:15]\n\t"
        "ds_read_b64_tr_b16 v[32:33], v195 offset:0\n\t"
        "ds_read_b64_tr_b16 v[34:35], v195 offset:1024\n\t"
        "s_waitcnt lgkmcnt(14)\n\t"
        "v_mfma_f32_32x32x16_bf16 v[16:31], v[36:39], v[176:179], v[16:31]\n\t"
        "ds_read_b64_tr_b16 v[36:37], v196 offset:0\n\t"
        "ds_read_b64_tr_b16 v[38:39], v196 offset:1024\n\t"
        "s_add_u32 s44, s44, 0x1000\n\t"
        "s_and_b32 s44, s44, 0xffff\n\t"
        "buffer_load_dwordx4 v[96:99], v192, s[40:43], s44 offen offset:0 sc1\n\t"
        "s_waitcnt vmcnt(11) lgkmcnt(14)\n\t"
        "v_mfma_f32_32x32x16_bf16 v[0:15], v[40:43], v[180:183], v[0:15]\n\t"
        "ds_read_b64_tr_b16 v[40:41], v195 offset:2048\n\t"
        "ds_read_b64_tr_b16 v[42:43], v195 offset:3072\n\t"
        "s_waitcnt lgkmcnt(14)\n\t"
        "v_mfma_f32_32x32x16_bf16 v[16:31], v[44:47], v[180:183], v[16:31]\n\t"
        "ds_read_b64_tr_b16 v[44:45], v196 offset:2048\n\t"
        "ds_read_b64_tr_b16 v[46:47], v196 offset:3072\n\t"
        "buffer_load_dwordx4 v[100:103], v192, s[40:43], s44 offen offset:1024 sc1\n\t"
        "s_waitcnt vmcnt(11) lgkmcnt(14)\n\t"
        "v_mfma_f32_32x32x16_bf16 v[0:15], v[48:51], v[184:187], v[0:15]\n\t"
        "ds_read_b64_tr_b16 v[48:49], v195 offset:4096\n\t"
        "ds_read_b64_tr_b16 v[50:51], v195 offset:5120\n\t"
        "s_waitcnt lgkmcnt(14)\n\t"
        "v_mfma_f32_32x32x16_bf16 v[16:31], v[52:55], v[184:187], v[16:31]\n\t"
        "ds_read_b64_tr_b16 v[52:53], v196 offset:4096\n\t"
        "ds_read_b64_tr_b16 v[54:55], v196 offset:5120\n\t"
        "buffer_load_dwordx4 v[104:107], v192, s[40:43], s44 offen offset:2048 sc1\n\t"
        "s_waitcnt vmcnt(11) lgkmcnt(14)\n\t"
        "v_mfma_f32_32x32x16_bf16 v[0:15], v[56:59], v[188:191], v[0:15]\n\t"
        "ds_read_b64_tr_b16 v[56:57], v195 offset:6144\n\t"
        "ds_read_b64_tr_b16 v[58:59], v195 offset:7168\n\t"
        "s_waitcnt lgkmcnt(14)\n\t"
        "v_mfma_f32_32x32x16_bf16 v[16:31], v[60:63], v[188:191], v[16:31]\n\t"
        "ds_read_b64_tr_b16 v[60:61], v196 offset:6144\n\t"
        "ds_read_b64_tr_b16 v[62:63], v196 offset:7168\n\t"
        "buffer_load_dwordx4 v[108:111], v192, s[40:43], s44 offen offset:3072 sc1\n\t"
        "s_waitcnt vmcnt(11) lgkmcnt(14)\n\t"
        "v_mfma_f32_32x32x16_bf16 v[0:15], v[32:35], v[64:67], v[0:15]\n\t"
        "ds_read_b64_tr_b16 v[32:33], v195 offset:8192\n\t"
        "ds_read_b64_tr_b16 v[34:35], v195 offset:9216\n\t"
        "s_waitcnt lgkmcnt(14)\n\t"
        "v_mfma_f32_32x32x16_bf16 v[16:31], v[36:39], v[64:67], v[16:31]\n\t"
        "ds_read_b64_tr_b16 v[36:37], v196 offset:8192\n\t"
        "ds_read_b64_tr_b16 v[38:39], v196 offset:9216\n\t"
        "s_add_u32 s44, s44, 0x1000\n\t"
        "s_and_b32 s44, s44, 0xffff\n\t"
        "buffer_load_dwordx4 v[112:115], v192, s[40:43], s44 offen offset:0 sc1\n\t"
        "s_waitcnt vmcnt(11) lgkmcnt(14)\n\t"
        "v_mfma_f32_32x32x16_bf16 v[0:15], v[40:43], v[68:71], v[0:15]\n\t"
        "ds_read_b64_tr_b16 v[40:41], v195 offset:10240\n\t"
        "ds_read_b64_tr_b16 v[42:43], v195 offset:11264\n\t"
        "s_waitcnt lgkmcnt(14)\n\t"
        "v_mfma_f32_32x32x16_bf16 v[16:31], v[44:47], v[68:71], v[16:31]\n\t"
        "ds_read_b64_tr_b16 v[44:45], v196 offset:10240\n\t"
        "ds_read_b64_tr_b16 v[46:47], v196 offset:11264\n\t"
        "buffer_load_dwordx4 v[116:119], v192, s[40:43], s44 offen offset:1024 sc1\n\t"
        "s_waitcnt vmcnt(11) lgkmcnt(14)\n\t"
        "v_mfma_f32_32x32x16_bf16 v[0:15], v[48:51], v[72:75], v[0:15]\n\t"
        "ds_read_b64_tr_b16 v[48:49], v195 offset:12288\n\t"
        "ds_read_b64_tr_b16 v[50:51], v195 offset:13312\n\t"
        "s_waitcnt lgkmcnt(14)\n\t"
        "v_mfma_f32_32x32x16_bf16 v[16:31], v[52:55], v[72:75], v[16:31]\n\t"
        "ds_read_b64_tr_b16 v[52:53], v196 offset:12288\n\t"
        "ds_read_b64_tr_b16 v[54:55], v196 offset:13312\n\t"
        "buffer_load_dwordx4 v[120:123], v192, s[40:43], s44 offen offset:2048 sc1\n\t"
        "s_waitcnt vmcnt(11) lgkmcnt(14)\n\t"
        "v_mfma_f32_32x32x16_bf16 v[0:15], v[56:59], v[76:79], v[0:15]\n\t"
        "ds_read_b64_tr_b16 v[56:57], v195 offset:14336\n\t"
        "ds_read_b64_tr_b16 v[58:59], v195 offset:15360\n\t"
        "s_waitcnt lgkmcnt(14)\n\t"
        "v_mfma_f32_32x32x16_bf16 v[16:31], v[60:63], v[76:79], v[16:31]\n\t"
        "ds_read_b64_tr_b16 v[60:61], v196 offset:14336\n\t"
        "ds_read_b64_tr_b16 v[62:63], v196 offset:15360\n\t"
        "buffer_load_dwordx4 v[124:127], v192, s[40:43], s44 offen offset:3072 sc1\n\t"
        "s_waitcnt vmcnt(11) lgkmcnt(14)\n\t"
        "v_mfma_f32_32x32x16_bf16 v[0:15], v[32:35], v[80:83], v[0:15]\n\t"
        "ds_read_b64_tr_b16 v[32:33], v195 offset:16384\n\t"
        "ds_read_b64_tr_b16 v[34:35], v195 offset:17408\n\t"
        "s_waitcnt lgkmcnt(14)\n\t"
        "v_mfma_f32_32x32x16_bf16 v[16:31], v[36:39], v[80:83], v[16:31]\n\t"
        "ds_read_b64_tr_b16 v[36:37], v196 offset:16384\n\t"
        "ds_read_b64_tr_b16 v[38:39], v196 offset:17408\n\t"
        "s_add_u32 s44, s44, 0x1000\n\t"
        "s_and_b32 s44, s44, 0xffff\n\t"
        "buffer_load_dwordx4 v[128:131], v192, s[40:43], s44 offen offset:0 sc1\n\t"
        "s_waitcnt vmcnt(11) lgkmcnt(14)\n\t"
        "v_mfma_f32_32x32x16_bf16 v[0:15], v[40:43], v[84:87], v[0:15]\n\t"
        "ds_read_b64_tr_b16 v[40:41], v195 offset:18432\n\t"
        "ds_read_b64_tr_b16 v[42:43], v195 offset:19456\n\t"
        "s_waitcnt lgkmcnt(14)\n\t"
        "v_mfma_f32_32x32x16_bf16 v[16:31], v[44:47], v[84:87], v[16:31]\n\t"
        "ds_read_b64_tr_b16 v[44:45], v196 offset:18432\n\t"
        "ds_read_b64_tr_b16 v[46:47], v196 offset:19456\n\t"
        "buffer_load_dwordx4 v[132:135], v192, s[40:43], s44 offen offset:1024 sc1\n\t"
        "s_waitcnt vmcnt(11) lgkmcnt(14)\n\t"
        "v_mfma_f32_32x32x16_bf16 v[0:15], v[48:51], v[88:91], v[0:15]\n\t"
        "ds_read_b64_tr_b16 v[48:49], v195 offset:20480\n\t"
        "ds_read_b64_tr_b16 v[50:51], v195 offset:21504\n\t"
        "s_waitcnt lgkmcnt(14)\n\t"
        "v_mfma_f32_32x32x16_bf16 v[16:31], v[52:55], v[88:91], v[16:31]\n\t"
        "ds_read_b64_tr_b16 v[52:53], v196 offset:20480\n\t"
        "ds_read_b64_tr_b16 v[54:55], v196 offset:21504\n\t"
        "buffer_load_dwordx4 v[136:139], v192, s[40:43], s44 offen offset:2048 sc1\n\t"
        "s_waitcnt vmcnt(11) lgkmcnt(14)\n\t"
        "v_mfma_f32_32x32x16_bf16 v[0:15], v[56:59], v[92:95], v[0:15]\n\t"
        "ds_read_b64_tr_b16 v[56:57], v195 offset:22528\n\t"
        "ds_read_b64_tr_b16 v[58:59], v195 offset:23552\n\t"
        "s_waitcnt lgkmcnt(14)\n\t"
        "v_mfma_f32_32x32x16_bf16 v[16:31], v[60:63], v[92:95], v[16:31]\n\t"
        "ds_read_b64_tr_b16 v[60:61], v196 offset:22528\n\t"
        "ds_read_b64_tr_b16 v[62:63], v196 offset:23552\n\t"
        "buffer_load_dwordx4 v[140:143], v192, s[40:43], s44 offen offset:3072 sc1\n\t"
        "s_waitcnt vmcnt(11) lgkmcnt(14)\n\t"
        "v_mfma_f32_32x32x16_bf16 v[0:15], v[32:35], v[96:99], v[0:15]\n\t"
        "ds_read_b64_tr_b16 v[32:33], v195 offset:24576\n\t"
        "ds_read_b64_tr_b16 v[34:35], v195 offset:25600\n\t"
        "s_waitcnt lgkmcnt(14)\n\t"
        "v_mfma_f32_32x32x16_bf16 v[16:31], v[36:39], v[96:99], v[16:31]\n\t"
        "ds_read_b64_tr_b16 v[36:37], v196 offset:24576\n\t"
        "ds_read_b64_tr_b16 v[38:39], v196 offset:25600\n\t"
        "s_add_u32 s44, s44, 0x1000\n\t"
        "s_and_b32 s44, s44, 0xffff\n\t"
        "buffer_load_dwordx4 v[144:147], v192, s[40:43], s44 offen offset:0 sc1\n\t"
        "s_waitcnt vmcnt(11) lgkmcnt(14)\n\t"
        "v_mfma_f32_32x32x16_bf16 v[0:15], v[40:43], v[100:103], v[0:15]\n\t"
        "ds_read_b64_tr_b16 v[40:41], v195 offset:26624\n\t"
        "ds_read_b64_tr_b16 v[42:43], v195 offset:27648\n\t"
        "s_waitcnt lgkmcnt(14)\n\t"
        "v_mfma_f32_32x32x16_bf16 v[16:31], v[44:47], v[100:103], v[16:31]\n\t"
        "ds_read_b64_tr_b16 v[44:45], v196 offset:26624\n\t"
        "ds_read_b64_tr_b16 v[46:47], v196 offset:27648\n\t"
        "buffer_load_dwordx4 v[148:151], v192, s[40:43], s44 offen offset:1024 sc1\n\t"
        "s_waitcnt vmcnt(11) lgkmcnt(14)\n\t"
        "v_mfma_f32_32x32x16_bf16 v[0:15], v[48:51], v[104:107], v[0:15]\n\t"
        "ds_read_b64_tr_b16 v[48:49], v195 offset:28672\n\t"
        "ds_read_b64_tr_b16 v[50:51], v195 offset:29696\n\t"
        "s_waitcnt lgkmcnt(14)\n\t"
        "v_mfma_f32_32x32x16_bf16 v[16:31], v[52:55], v[104:107], v[16:31]\n\t"
        "ds_read_b64_tr_b16 v[52:53], v196 offset:28672\n\t"
        "ds_read_b64_tr_b16 v[54:55], v196 offset:29696\n\t"
        "buffer_load_dwordx4 v[152:155], v192, s[40:43], s44 offen offset:2048 sc1\n\t"
        "s_waitcnt vmcnt(11) lgkmcnt(14)\n\t"
        "v_mfma_f32_32x32x16_bf16 v[0:15], v[56:59], v[108:111], v[0:15]\n\t"
        "ds_read_b64_tr_b16 v[56:57], v195 offset:30720\n\t"
        "ds_read_b64_tr_b16 v[58:59], v195 offset:31744\n\t"
        "s_waitcnt lgkmcnt(14)\n\t"
        "v_mfma_f32_32x32x16_bf16 v[16:31], v[60:63], v[108:111], v[16:31]\n\t"
        "ds_read_b64_tr_b16 v[60:61], v196 offset:30720\n\t"
        "ds_read_b64_tr_b16 v[62:63], v196 offset:31744\n\t"
        "buffer_load_dwordx4 v[156:159], v192, s[40:43], s44 offen offset:3072 sc1\n\t"
        "s_waitcnt vmcnt(11) lgkmcnt(14)\n\t"
        "v_mfma_f32_32x32x16_bf16 v[0:15], v[32:35], v[112:115], v[0:15]\n\t"
        "ds_read_b64_tr_b16 v[32:33], v195 offset:32768\n\t"
        "ds_read_b64_tr_b16 v[34:35], v195 offset:33792\n\t"
        "s_waitcnt lgkmcnt(14)\n\t"
        "v_mfma_f32_32x32x16_bf16 v[16:31], v[36:39], v[112:115], v[16:31]\n\t"
        "ds_read_b64_tr_b16 v[36:37], v196 offset:32768\n\t"
        "ds_read_b64_tr_b16 v[38:39], v196 offset:33792\n\t"
        "s_add_u32 s44, s44, 0x1000\n\t"
        "s_and_b32 s44, s44, 0xffff\n\t"
        "buffer_load_dwordx4 v[160:163], v192, s[40:43], s44 offen offset:0 sc1\n\t"
        "s_waitcnt vmcnt(11) lgkmcnt(14)\n\t"
        "v_mfma_f32_32x32x16_bf16 v[0:15], v[40:43], v[116:119], v[0:15]\n\t"
        "ds_read_b64_tr_b16 v[40:41], v195 offset:34816\n\t"
        "ds_read_b64_tr_b16 v[42:43], v195 offset:35840\n\t"
        "s_waitcnt lgkmcnt(14)\n\t"
        "v_mfma_f32_32x32x16_bf16 v[16:31], v[44:47], v[116:119], v[16:31]\n\t"
        "ds_read_b64_tr_b16 v[44:45], v196 offset:34816\n\t"
        "ds_read_b64_tr_b16 v[46:47], v196 offset:35840\n\t"
        "buffer_load_dwordx4 v[164:167], v192, s[40:43], s44 offen offset:1024 sc1\n\t"
        "s_waitcnt vmcnt(11) lgkmcnt(14)\n\t"
        "v_mfma_f32_32x32x16_bf16 v[0:15], v[48:51], v[120:123], v[0:15]\n\t"
        "ds_read_b64_tr_b16 v[48:49], v195 offset:36864\n\t"
        "ds_read_b64_tr_b16 v[50:51], v195 offset:37888\n\t"
        "s_waitcnt lgkmcnt(14)\n\t"
        "v_mfma_f32_32x32x16_bf16 v[16:31], v[52:55], v[120:123], v[16:31]\n\t"
        "ds_read_b64_tr_b16 v[52:53], v196 offset:36864\n\t"
        "ds_read_b64_tr_b16 v[54:55], v196 offset:37888\n\t"
        "buffer_load_dwordx4 v[168:171], v192, s[40:43], s44 offen offset:2048 sc1\n\t"
        "s_waitcnt vmcnt(11) lgkmcnt(14)\n\t"
        "v_mfma_f32_32x32x16_bf16 v[0:15], v[56:59], v[124:127], v[0:15]\n\t"
        "ds_read_b64_tr_b16 v[56:57], v195 offset:38912\n\t"
        "ds_read_b64_tr_b16 v[58:59], v195 offset:39936\n\t"
        "s_waitcnt lgkmcnt(14)\n\t"
        "v_mfma_f32_32x32x16_bf16 v[16:31], v[60:63], v[124:127], v[16:31]\n\t"
        "ds_read_b64_tr_b16 v[60:61], v196 offset:38912\n\t"
        "ds_read_b64_tr_b16 v[62:63], v196 offset:39936\n\t"
        "buffer_load_dwordx4 v[172:175], v192, s[40:43], s44 offen offset:3072 sc1\n\t"
        "s_waitcnt vmcnt(11) lgkmcnt(14)\n\t"
        "v_mfma_f32_32x32x16_bf16 v[0:15], v[32:35], v[128:131], v[0:15]\n\t"
        "ds_read_b64_tr_b16 v[32:33], v195 offset:40960\n\t"
        "ds_read_b64_tr_b16 v[34:35], v195 offset:41984\n\t"
        "s_waitcnt lgkmcnt(14)\n\t"
        "v_mfma_f32_32x32x16_bf16 v[16:31], v[36:39], v[128:131], v[16:31]\n\t"
        "ds_read_b64_tr_b16 v[36:37], v196 offset:40960\n\t"
        "ds_read_b64_tr_b16 v[38:39], v196 offset:41984\n\t"
        "s_add_u32 s44, s44, 0x1000\n\t"
        "s_and_b32 s44, s44, 0xffff\n\t"
        "buffer_load_dwordx4 v[176:179], v192, s[40:43], s44 offen offset:0 sc1\n\t"
        "s_waitcnt vmcnt(11) lgkmcnt(14)\n\t"
        "v_mfma_f32_32x32x16_bf16 v[0:15], v[40:43], v[132:135], v[0:15]\n\t"
        "ds_read_b64_tr_b16 v[40:41], v195 offset:43008\n\t"
        "ds_read_b64_tr_b16 v[42:43], v195 offset:44032\n\t"
        "s_waitcnt lgkmcnt(14)\n\t"
        "v_mfma_f32_32x32x16_bf16 v[16:31], v[44:47], v[132:135], v[16:31]\n\t"
        "ds_read_b64_tr_b16 v[44:45], v196 offset:43008\n\t"
        "ds_read_b64_tr_b16 v[46:47], v196 offset:44032\n\t"
        "buffer_load_dwordx4 v[180:183], v192, s[40:43], s44 offen offset:1024 sc1\n\t"
        "s_waitcnt vmcnt(11) lgkmcnt(14)\n\t"
        "v_mfma_f32_32x32x16_bf16 v[0:15], v[48:51], v[136:139], v[0:15]\n\t"
        "ds_read_b64_tr_b16 v[48:49], v195 offset:45056\n\t"
        "ds_read_b64_tr_b16 v[50:51], v195 offset:46080\n\t"
        "s_waitcnt lgkmcnt(14)\n\t"
        "v_mfma_f32_32x32x16_bf16 v[16:31], v[52:55], v[136:139], v[16:31]\n\t"
        "ds_read_b64_tr_b16 v[52:53], v196 offset:45056\n\t"
        "ds_read_b64_tr_b16 v[54:55], v196 offset:46080\n\t"
        "buffer_load_dwordx4 v[184:187], v192, s[40:43], s44 offen offset:2048 sc1\n\t"
        "s_waitcnt vmcnt(11) lgkmcnt(14)\n\t"
        "v_mfma_f32_32x32x16_bf16 v[0:15], v[56:59], v[140:143], v[0:15]\n\t"
        "ds_read_b64_tr_b16 v[56:57], v195 offset:47104\n\t"
        "ds_read_b64_tr_b16 v[58:59], v195 offset:48128\n\t"
        "s_waitcnt lgkmcnt(14)\n\t"
        "v_mfma_f32_32x32x16_bf16 v[16:31], v[60:63], v[140:143], v[16:31]\n\t"
        "ds_read_b64_tr_b16 v[60:61], v196 offset:47104\n\t"
        "ds_read_b64_tr_b16 v[62:63], v196 offset:48128\n\t"
        "buffer_load_dwordx4 v[188:191], v192, s[40:43], s44 offen offset:3072 sc1\n\t"
        "s_waitcnt vmcnt(11) lgkmcnt(14)\n\t"
        "v_mfma_f32_32x32x16_bf16 v[0:15], v[32:35], v[144:147], v[0:15]\n\t"
        "ds_read_b64_tr_b16 v[32:33], v195 offset:49152\n\t"
        "ds_read_b64_tr_b16 v[34:35], v195 offset:50176\n\t"
        "s_waitcnt lgkmcnt(14)\n\t"
        "v_mfma_f32_32x32x16_bf16 v[16:31], v[36:39], v[144:147], v[16:31]\n\t"
        "ds_read_b64_tr_b16 v[36:37], v196 offset:49152\n\t"
        "ds_read_b64_tr_b16 v[38:39], v196 offset:50176\n\t"
        "s_waitcnt vmcnt(10) lgkmcnt(14)\n\t"
        "v_mfma_f32_32x32x16_bf16 v[0:15], v[40:43], v[148:151], v[0:15]\n\t"
        "ds_read_b64_tr_b16 v[40:41], v195 offset:51200\n\t"
        "ds_read_b64_tr_b16 v[42:43], v195 offset:52224\n\t"
        "s_waitcnt lgkmcnt(14)\n\t"
        "v_mfma_f32_32x32x16_bf16 v[16:31], v[44:47], v[148:151], v[16:31]\n\t"
        "ds_read_b64_tr_b16 v[44:45], v196 offset:51200\n\t"
        "ds_read_b64_tr_b16 v[46:47], v196 offset:52224\n\t"
        "s_waitcnt vmcnt(9) lgkmcnt(14)\n\t"
        "v_mfma_f32_32x32x16_bf16 v[0:15], v[48:51], v[152:155], v[0:15]\n\t"
        "ds_read_b64_tr_b16 v[48:49], v195 offset:53248\n\t"
        "ds_read_b64_tr_b16 v[50:51], v195 offset:54272\n\t"
        "s_waitcnt lgkmcnt(14)\n\t"
        "v_mfma_f32_32x32x16_bf16 v[16:31], v[52:55], v[152:155], v[16:31]\n\t"
        "ds_read_b64_tr_b16 v[52:53], v196 offset:53248\n\t"
        "ds_read_b64_tr_b16 v[54:55], v196 offset:54272\n\t"
        "s_waitcnt vmcnt(8) lgkmcnt(14)\n\t"
        "v_mfma_f32_32x32x16_bf16 v[0:15], v[56:59], v[156:159], v[0:15]\n\t"
        "ds_read_b64_tr_b16 v[56:57], v195 offset:55296\n\t"
        "ds_read_b64_tr_b16 v[58:59], v195 offset:56320\n\t"
        "s_waitcnt lgkmcnt(14)\n\t"
        "v_mfma_f32_32x32x16_bf16 v[16:31], v[60:63], v[156:159], v[16:31]\n\t"
        "ds_read_b64_tr_b16 v[60:61], v196 offset:55296\n\t"
        "ds_read_b64_tr_b16 v[62:63], v196 offset:56320\n\t"
        "s_waitcnt vmcnt(7) lgkmcnt(14)\n\t"
        "v_mfma_f32_32x32x16_bf16 v[0:15], v[32:35], v[160:163], v[0:15]\n\t"
        "s_barrier\n\t"
        "ds_read_b64_tr_b16 v[32:33], v195 offset:57344\n\t"
        "ds_read_b64_tr_b16 v[34:35], v195 offset:58368\n\t"
        "s_waitcnt lgkmcnt(14)\n\t"
        "v_mfma_f32_32x32x16_bf16 v[16:31], v[36:39], v[160:163], v[16:31]\n\t"
        "ds_read_b64_tr_b16 v[36:37], v196 offset:57344\n\t"
        "ds_read_b64_tr_b16 v[38:39], v196 offset:58368\n\t"
        "s_waitcnt vmcnt(6) lgkmcnt(14)\n\t"
        "v_mfma_f32_32x32x16_bf16 v[0:15], v[40:43], v[164:167], v[0:15]\n\t"
        "ds_read_b64_tr_b16 v[40:41], v195 offset:59392\n\t"
        "ds_read_b64_tr_b16 v[42:43], v195 offset:60416\n\t"
        "s_waitcnt lgkmcnt(14)\n\t"
        "v_mfma_f32_32x32x16_bf16 v[16:31], v[44:47], v[164:167], v[16:31]\n\t"
        "ds_read_b64_tr_b16 v[44:45], v196 offset:59392\n\t"
        "ds_read_b64_tr_b16 v[46:47], v196 offset:60416\n\t"
        "s_waitcnt vmcnt(5) lgkmcnt(14)\n\t"
        "v_mfma_f32_32x32x16_bf16 v[0:15], v[48:51], v[168:171], v[0:15]\n\t"
        "ds_read_b64_tr_b16 v[48:49], v195 offset:61440\n\t"
        "ds_read_b64_tr_b16 v[50:51], v195 offset:62464\n\t"
        "s_waitcnt lgkmcnt(14)\n\t"
        "v_mfma_f32_32x32x16_bf16 v[16:31], v[52:55], v[168:171], v[16:31]\n\t"
        "ds_read_b64_tr_b16 v[52:53], v196 offset:61440\n\t"
        "ds_read_b64_tr_b16 v[54:55], v196 offset:62464\n\t"
        "s_waitcnt vmcnt(4) lgkmcnt(14)\n\t"
        "v_mfma_f32_32x32x16_bf16 v[0:15], v[56:59], v[172:175], v[0:15]\n\t"
        "ds_read_b64_tr_b16 v[56:57], v195 offset:63488\n\t"
        "ds_read_b64_tr_b16 v[58:59], v195 offset:64512\n\t"
        "s_waitcnt lgkmcnt(14)\n\t"
        "v_mfma_f32_32x32x16_bf16 v[16:31], v[60:63], v[172:175], v[16:31]\n\t"
        "ds_read_b64_tr_b16 v[60:61], v196 offset:63488\n\t"
        "ds_read_b64_tr_b16 v[62:63], v196 offset:64512\n\t"
        "s_waitcnt vmcnt(3) lgkmcnt(14)\n\t"
        "v_mfma_f32_32x32x16_bf16 v[0:15], v[32:35], v[176:179], v[0:15]\n\t"
        "s_waitcnt lgkmcnt(12)\n\t"
        "v_mfma_f32_32x32x16_bf16 v[16:31], v[36:39], v[176:179], v[16:31]\n\t"
        "s_waitcnt vmcnt(2) lgkmcnt(10)\n\t"
        "v_mfma_f32_32x32x16_bf16 v[0:15], v[40:43], v[180:183], v[0:15]\n\t"
        "s_waitcnt lgkmcnt(8)\n\t"
        "v_mfma_f32_32x32x16_bf16 v[16:31], v[44:47], v[180:183], v[16:31]\n\t"
        "s_waitcnt vmcnt(1) lgkmcnt(6)\n\t"
        "v_mfma_f32_32x32x16_bf16 v[0:15], v[48:51], v[184:187], v[0:15]\n\t"
        "s_waitcnt lgkmcnt(4)\n\t"
        "v_mfma_f32_32x32x16_bf16 v[16:31], v[52:55], v[184:187], v[16:31]\n\t"
        "s_waitcnt vmcnt(0) lgkmcnt(2)\n\t"
        "v_mfma_f32_32x32x16_bf16 v[0:15], v[56:59], v[188:191], v[0:15]\n\t"
        "s_waitcnt lgkmcnt(0)\n\t"
        "v_mfma_f32_32x32x16_bf16 v[16:31], v[60:63], v[188:191], v[16:31]\n\t"
        "s_nop 11\n\t"
        : "={v[0:15]}"(acc[0]), "={v[16:31]}"(acc[1]), "+{s44}"(soff) STAMP_OUTS
        : "{v192}"(OFF_OF + L * (5 * MBY) + tile * TS + lane * 16), "{v193}"(a0), "{v194}"(a1), "{v195}"(a0 + 65536), "{v196}"(a1 + 65536),
          "{v198}"(lbase + L_W + 20), "{v199}"(L + 1), "{s[40:43]}"(srd)
        : "v32", "v33", "v34", "v35", "v36", "v37", "v38", "v39", "v40", "v41", "v42", "v43", "v44", "v45", "v46", "v47", "v48", "v49", "v50", "v51", "v52", "v53", "v54", "v55", "v56", "v57", "v58", "v59", "v60", "v61", "v62", "v63", "v64", "v65", "v66", "v67", "v68", "v69", "v70", "v71", "v72", "v73", "v74", "v75", "v76", "v77", "v78", "v79", "v80", "v81", "v82", "v83", "v84", "v85", "v86", "v87", "v88", "v89", "v90", "v91", "v92", "v93", "v94", "v95", "v96", "v97", "v98", "v99", "v100", "v101", "v102", "v103", "v104", "v105", "v106", "v107", "v108", "v109", "v110", "v111", "v112", "v113", "v114", "v115", "v116", "v117", "v118", "v119", "v120", "v121", "v122", "v123", "v124", "v125", "v126", "v127", "v128", "v129", "v130", "v131", "v132", "v133", "v134", "v135", "v136", "v137", "v138", "v139", "v140", "v141", "v142", "v143", "v144", "v145", "v146", "v147", "v148", "v149", "v150", "v151", "v152", "v153", "v154", "v155", "v156", "v157", "v158", "v159", "v160", "v161", "v162", "v163", "v164", "v165", "v166", "v167", "v168", "v169", "v170", "v171", "v172", "v173", "v174", "v175", "v176", "v177", "v178", "v179", "v180", "v181", "v182", "v183", "v184", "v185", "v186", "v187", "v188", "v189", "v190", "v191", "v200", "s45", "vcc", "scc", "memory");
      }
      PHASE_VARS;
      STAMP_PUT(12 + 16 * L);
      STAMPV(15 + 16 * L);
      float s = 0.f, sq = 0.f, d = 0.f;
      const unsigned xdst = (L ? OOB : OFF_X1 + tile * TS + tn * 4096) + lane * 16;
#pragma unroll
      for (int g = 0; g < 8; ++g) {
#pragma unroll
        for (int e = 0; e < 2; ++e) {
          const unsigned rr = res[g >> 1][2 * (g & 1) + e];
          const unsigned t = pack2(acc[g >> 2][4 * (g & 3) + 2 * e], acc[g >> 2][4 * (g & 3) + 2 * e + 1]);
          x1p[g >> 1][2 * (g & 1) + e] = pack2(lo16(rr) + lo16(t), hi16(rr) + hi16(t));
        }
        if (g & 1) { stg(rs, xdst + (g >> 1) * 1024, x1p[g >> 1]); SBAR(); }
      }
#pragma unroll
      for (int g = 0; g < 8; ++g)
#pragma unroll
        for (int e = 0; e < 2; ++e) {
          const unsigned o = x1p[g >> 1][2 * (g & 1) + e];
          s = dot2(o, 0x3f803f80u, s); sq = dot2(o, o, sq); d = dot2(o, wrc[g][e], d);
        }
      stg(rs, stats_dst(sx, tile, tn, lane), u32x4{nzf(half_swap_sum(s)), nzf(half_swap_sum(sq)), nzf(half_swap_sum(d)), 0u});
    }
    STAMPV(16 + 16 * L);
  }
  {
    PHASE_VARS;
    float S, Q, Dd, mean, var, mb1, rd1;
    __builtin_amdgcn_s_sleep(10);
    row_stats_sum(rs, OFF_SX + SX_LAYER, tile, lane, S, Q, Dd);
    STAMPV(33);
    row_stats_fin(smem, 2, tile, lane, S, Q, Dd, U2F(words[0]), x1p, mean, var, mb1, rd1);
    STAMPV(34);
    u32x4 xo[4];
#pragma unroll
    for (int i = 0; i < 4; ++i) {
#pragma unroll
      for (int e = 0; e < 4; ++e) {
        const unsigned h2 = ln_pair(x1p[i][e], mb1, rd1);
        xo[i][e] = pack2(lo16(x1p[i][e]) + lo16(h2), hi16(x1p[i][e]) + hi16(h2));
      }
      *reinterpret_cast<u32x4*>(smem + wid * 4096 + r32 * 128 + ((2 * i + hi) ^ (r32 & 7)) * 16) = fo_swap(xo[i]);
    }
    asm volatile("s_waitcnt lgkmcnt(0)" ::: "memory");
#pragma unroll
    for (int i = 0; i < 4; ++i) { xo[i] = *reinterpret_cast<const u32x4*>(smem + wid * 4096 + i * 1024 + (lane >> 3) * 128 + ((lane ^ (lane >> 3)) & 7) * 16); PIN(xo[i]); }
    u16* dst = out + (size_t)(tile * 32 + (lane >> 3)) * D_ + tn * 64 + (lane & 7) * 8;
    lds_wait_ge(lbase + L_W + 4 + 4 * (lane & 1), 2);
    const int rel = (int)words[3] - tile * 32 - (lane >> 3);
#pragma unroll
    for (int i = 0; i < 4; ++i)
      if (rel != 8 * i) st16(dst + (size_t)i * 8 * D_, xo[i]);
    STAMPV(35);
    STAMP_END;
  }
}

extern "C" __global__ void end_marker() {}

extern "C" void kernel_launch(void* const* d_in, const int* in_sizes, int n_in, void* d_out, int out_size,
                              void* d_ws, size_t ws_size, hipStream_t stream) {
  char* ws = (char*)d_ws;
  mega_kernel<<<256, 512, 0, stream>>>((const u16*)d_in[0], (const u16*)d_in[1], (const u16*)d_in[2], (const u16*)d_in[4], (const u16*)d_in[5],
                                       (const u16*)d_in[6], (const u16*)d_in[7], ws, (u16*)d_out);
}
```

```cpp
#include <hip/hip_runtime.h>
#include <hip/hip_bf16.h>
#include <stdint.h>

typedef unsigned short u16;
typedef short s16x4 __attribute__((ext_vector_type(4)));
typedef short s16x8 __attribute__((ext_vector_type(8)));
typedef float f32x16 __attribute__((ext_vector_type(16)));
typedef float f32x8 __attribute__((ext_vector_type(8)));
typedef float f32x4 __attribute__((ext_vector_type(4)));
typedef float f32x2 __attribute__((ext_vector_type(2)));
typedef unsigned int u32x16 __attribute__((ext_vector_type(16)));
typedef unsigned int u32x8 __attribute__((ext_vector_type(8)));
typedef unsigned int u32x4 __attribute__((ext_vector_type(4)));
typedef unsigned int u32x2 __attribute__((ext_vector_type(2)));
typedef __bf16 bf16x2 __attribute__((ext_vector_type(2)));
typedef __bf16 bf16x8 __attribute__((ext_vector_type(8)));
typedef __bf16 bf16x16 __attribute__((ext_vector_type(16)));

#define DI __device__ __forceinline__
#define LDS_PTR(T, p) ((__attribute__((address_space(3))) T*)(p))
#define BARRIER() asm volatile("s_barrier" ::: "memory")
#define BAR_LGKM() asm volatile("s_waitcnt lgkmcnt(0)\n\ts_barrier" ::: "memory")
#define BAR_ALL() asm volatile("s_waitcnt vmcnt(0) lgkmcnt(0)\n\ts_barrier" ::: "memory")
#define DRAIN() asm volatile("s_waitcnt vmcnt(0)" ::: "memory")
#define PACE() ({ asm volatile("s_sleep 1\n\ts_waitcnt vmcnt(3)" ::: "memory"); })
#define MFMA32(a, b, c) __builtin_amdgcn_mfma_f32_32x32x16_bf16(a, b, c, 0, 0, 0)
#define MFMA16(a, b, c) __builtin_amdgcn_mfma_f32_16x16x32_bf16(a, b, c, 0, 0, 0)
#define PIN(x) asm volatile("" : "+v"(x))
#define EX2(x) __builtin_amdgcn_exp2f(x)
#define RCP(x) __builtin_amdgcn_rcpf(x)
#define SBAR() __builtin_amdgcn_sched_barrier(0)

#define STAMP_INIT
#define STAMPV(i)
#define STAMP_END
#define STAMP_DECL
#define STAMP_OUTS
#define STAMP_PUT(i)
constexpr int D_ = 1024, F_ = 3584;

constexpr unsigned MBY = 1u << 20;
constexpr unsigned TS = 65536 + 4096, HS = 262144 + 4096;
constexpr unsigned OFF_HF = 0, OFF_X1 = 5 * MBY;
constexpr unsigned OFF_OF = 10 * MBY;
constexpr unsigned OFF_KV = 20 * MBY;
constexpr unsigned OFF_FL = 40 * MBY;
constexpr unsigned FL_EPOCH = OFF_FL;
constexpr unsigned FL_KV = OFF_FL + 4096;
constexpr unsigned FL_O = FL_KV + 65536;
constexpr unsigned FL_X = FL_O + 65536;
constexpr unsigned OFF_CS = 41 * MBY;
constexpr unsigned OFF_SX = 42 * MBY;
constexpr unsigned SX_ST = 0;
constexpr unsigned SX_ROW = 512 * 1024;
constexpr unsigned SX_CAND = SX_ROW + 64 * 1024;
constexpr unsigned SX_GP = SX_CAND + 32 * 1024;
constexpr unsigned SX_YP = SX_GP + 224 * 1024;
constexpr unsigned SX_LAYER = SX_YP + 224 * 1024;
constexpr unsigned SX_QP = 2 * SX_LAYER;
constexpr unsigned SX_BYTES = SX_QP + 512 * 1024;
static_assert(SX_BYTES <= 6 * 256 * 128 * 16, "sentinel fill");
constexpr unsigned WS_BYTES = 48 * MBY, OOB = 0x80000000u;
constexpr unsigned SENT = 0xFFFFFFFFu;

constexpr int NS = 9, DD = 3, SLOTB = 12288;
constexpr int L_XCH = 122880;
constexpr int L_SL = L_XCH + 8192;
constexpr int L_CAND = L_XCH + 32768;
constexpr int L_AB = L_XCH + 34816;
constexpr int L_FULL = 159744, L_FREE = L_FULL + 512;
constexpr int L_CS = L_FREE + 1024, L_PATCH = L_CS + 768;
constexpr int L_GU = L_PATCH + 768, L_TAG = L_GU + 256, L_WR = L_TAG + 256;
constexpr int L_X2F = L_WR + 128, L_X2P = L_X2F + 128;
constexpr int L_W = L_X2P + 128;
constexpr int LDS_BYTES = 163840;
static_assert(NS * SLOTB <= 54 * 2048 && L_W + 128 <= LDS_BYTES && (NS + 1) * SLOTB <= L_XCH && L_AB + 2048 <= L_FULL, "LDS map");

DI int fresh_lane() { int l; asm volatile("v_mbcnt_lo_u32_b32 %0, -1, 0\n\tv_mbcnt_hi_u32_b32 %0, -1, %0" : "=v"(l)); return l; }
#define LANE_VARS const int lane = fresh_lane(), r32 = lane & 31, hi = lane >> 5, tq = (lane & 15) >> 2, tp = lane & 3, tblk = (lane >> 4) & 1, \
  wrow = 4 * hi + tq, wsw = 4 * ((wrow >> 1) & 1), wch = 2 * tblk + (tp >> 1), c24 = r32 < 24 ? r32 : 23; \
  (void)r32; (void)hi; (void)wsw; (void)wch; (void)c24
DI int fresh_s(int v) { asm volatile("" : "+s"(v)); return v; }
#define PHASE_VARS const int tm = fresh_s(tm_), tn = fresh_s(tn_), wid = fresh_s(wid_), tile = tm * 4 + wid, hh = wid & 1, kh = wid >> 1; (void)tile; (void)hh; (void)kh; LANE_VARS
DI float bf2f(u16 v) { return __uint_as_float(((unsigned)v) << 16); }
DI unsigned pack2(float lo, float hi) { f32x2 v = {lo, hi}; return __builtin_bit_cast(unsigned, __builtin_convertvector(v, bf16x2)); }
DI float rbf(float f) { return __uint_as_float(pack2(f, 0.f) << 16); }
DI float lo16(unsigned u) { return __uint_as_float(u << 16); }
DI float hi16(unsigned u) { return __uint_as_float(u & 0xffff0000u); }
DI float opq(float r) { asm("" : "+v"(r)); return r; }
DI float fadd(float a, float b) { return opq(a + b); }
DI float fmul(float a, float b) { return opq(a * b); }
DI float ffma(float a, float b, float c) { return opq(__builtin_fmaf(a, b, c)); }
DI float U2F(unsigned u) { return __uint_as_float(u); }
DI float half_swap_sum(float v) {
  auto r = __builtin_amdgcn_permlane32_swap(__float_as_uint(v), __float_as_uint(v), false, false);
  return fadd(U2F(r[0]), U2F(r[1]));
}
DI float half_swap_max(float v) {
  auto r = __builtin_amdgcn_permlane32_swap(__float_as_uint(v), __float_as_uint(v), false, false);
  return fmaxf(__uint_as_float(r[0]), __uint_as_float(r[1]));
}
template <int CTRL> DI unsigned dpp(unsigned v) { return (unsigned)__builtin_amdgcn_update_dpp(0, (int)v, CTRL, 0xf, 0xf, false); }
DI float row_swap_sum(float v) {
  auto r = __builtin_amdgcn_permlane16_swap(__float_as_uint(v), __float_as_uint(v), false, false);
  return fadd(U2F(r[0]), U2F(r[1]));
}
DI float half_sum(float v) {
  v = fadd(v, U2F(dpp<0xB1>(__float_as_uint(v)))); v = fadd(v, U2F(dpp<0x4E>(__float_as_uint(v))));
  v = fadd(v, U2F(dpp<0x141>(__float_as_uint(v)))); v = fadd(v, U2F(dpp<0x140>(__float_as_uint(v))));
  return row_swap_sum(v);
}
DI float wave_sum(float v) { return half_swap_sum(half_sum(v)); }
DI unsigned half_max_u32(unsigned v) {
  v = max(v, dpp<0xB1>(v)); v = max(v, dpp<0x4E>(v)); v = max(v, dpp<0x141>(v)); v = max(v, dpp<0x140>(v));
  auto r = __builtin_amdgcn_permlane16_swap(v, v, false, false);
  return max(r[0], r[1]);
}
DI unsigned wave_max_u32(unsigned v) {
  v = half_max_u32(v);
  auto r = __builtin_amdgcn_permlane32_swap(v, v, false, false);
  return max(r[0], r[1]);
}
DI u32x4 fo_swap(u32x4 v) {
  auto a = __builtin_amdgcn_permlane32_swap(v[0], v[2], false, false);
  auto b = __builtin_amdgcn_permlane32_swap(v[1], v[3], false, false);
  u32x4 o; o[0] = a[0]; o[1] = b[0]; o[2] = a[1]; o[3] = b[1];
  return o;
}
DI void glds16(const void* gsrc, unsigned lds_dst) {
  unsigned keep;
  asm volatile("s_mov_b32 %0, m0\n\ts_mov_b32 m0, %2\n\ts_nop 0\n\tglobal_load_lds_dwordx4 %1, off\n\ts_mov_b32 m0, %0"
               : "=&s"(keep) : "v"(gsrc), "s"(lds_dst) : "memory");
}
#define GL_ "global_load_lds_dwordx4 "
DI void glds_v4(const void* sb, const unsigned (&v)[4], unsigned m0v) {
  unsigned keep;
  asm volatile("s_mov_b32 %0, m0\n\ts_mov_b32 m0, %6\n\ts_nop 0\n\t" GL_ "%1, %5\n\t" GL_ "%2, %5 offset:1024\n\t" GL_ "%3, %5 offset:2048\n\t"
               GL_ "%4, %5 offset:3072\n\ts_mov_b32 m0, %0" : "=&s"(keep) : "v"(v[0]), "v"(v[1]), "v"(v[2]), "v"(v[3]), "s"(sb), "s"(m0v) : "memory");
}
#define GP_ "\n\ts_sleep %8\n\ts_waitcnt vmcnt(%7)\n\t"
template <int TH, int SL> DI void glds_v4t(const void* sb, const unsigned (&v)[4], unsigned m0v) {
  unsigned keep;
  asm volatile("s_mov_b32 %0, m0\n\ts_mov_b32 m0, %6\n\ts_nop 0\n\t" GL_ "%1, %5" GP_ GL_ "%2, %5 offset:1024" GP_ GL_ "%3, %5 offset:2048" GP_ GL_ "%4, %5 offset:3072" GP_ "s_mov_b32 m0, %0"
               : "=&s"(keep) : "v"(v[0]), "v"(v[1]), "v"(v[2]), "v"(v[3]), "s"(sb), "s"(m0v), "n"(TH), "n"(SL) : "memory");
}
DI void glds_v3(const void* sb, const unsigned (&v)[3], unsigned m0v) {
  unsigned keep;
  asm volatile("s_mov_b32 %0, m0\n\ts_mov_b32 m0, %5\n\ts_nop 0\n\t" GL_ "%1, %4\n\t" GL_ "%2, %4 offset:1024\n\t" GL_ "%3, %4 offset:2048\n\t"
               "s_mov_b32 m0, %0" : "=&s"(keep) : "v"(v[0]), "v"(v[1]), "v"(v[2]), "s"(sb), "s"(m0v) : "memory");
}
DI float dot2(unsigned a, unsigned b, float c) {
  return __builtin_amdgcn_fdot2_f32_bf16(__builtin_bit_cast(bf16x2, a), __builtin_bit_cast(bf16x2, b), c, false);
}
#define CODE_ADDR(sym, out) do { unsigned long long pc_; unsigned off_; \
  asm volatile("s_getpc_b64 %0\n1:\n\ts_mov_b32 %1, " #sym "-1b" : "=s"(pc_), "=s"(off_)); out = (const char*)(pc_ + (long long)(int)off_); } while (0)
DI void st16(void* p, u32x4 v) { asm volatile("global_store_dwordx4 %0, %1, off sc0 sc1\n\ts_nop 1" :: "v"(p), "v"(v) : "memory"); }
DI void ln_scalars(float mean, float var, float& mb, float& den) { mb = rbf(mean); den = rbf(__builtin_amdgcn_sqrtf(rbf(rbf(var) + 1e-5f))); }
DI unsigned ln_pair(unsigned v, float mb, float rd) {
  const unsigned c = pack2(lo16(v) - mb, hi16(v) - mb);
  return pack2(lo16(c) * rd, hi16(c) * rd);
}
DI unsigned ln_pair_s(unsigned v, float nmb, float rd) {
  const unsigned c = pack2(fadd(lo16(v), nmb), fadd(hi16(v), nmb));
  return pack2(fmul(lo16(c), rd), fmul(hi16(c), rd));
}
template <int N, int M> DI f32x4 sum4(const u32x4 (&v)[M]) {
  f32x4 t;
#pragma unroll
  for (int e = 0; e < 4; ++e) {
    t[e] = U2F(v[0][e]);
#pragma unroll
    for (int i = 1; i < N; ++i) t[e] = fadd(t[e], U2F(v[i][e]));
  }
  return t;
}
#define PERM_LO(x, y) __builtin_amdgcn_perm(y, x, 0x05040100u)
#define PERM_HI(x, y) __builtin_amdgcn_perm(y, x, 0x07060302u)
constexpr unsigned SPIN_CAP = 1u << 15;
DI unsigned nz(unsigned v) { return min(v, SENT - 1u); }
DI unsigned nzf(float v) { return nz(__float_as_uint(v)); }
DI unsigned mx4(unsigned m, const u32x4& v) { return max(max(max(m, v[0]), v[1]), max(v[2], v[3])); }
template <int N> DI bool none_sent(const u32x4 (&v)[N]) {
  unsigned m = 0u;
#pragma unroll
  for (int i = 0; i < N; ++i) m = mx4(m, v[i]);
  return m != SENT;
}
typedef __amdgpu_buffer_rsrc_t rsrc_t;
DI u32x4 ldg(rsrc_t rs, unsigned off) { return __builtin_amdgcn_raw_buffer_load_b128(rs, (int)off, 0, 16); }
DI void stg(rsrc_t rs, unsigned off, u32x4 v) { __builtin_amdgcn_raw_buffer_store_b128(v, rs, (int)off, 0, 16); }
DI void poll_pause(bool slow) { if (slow) __builtin_amdgcn_s_sleep(4); asm volatile("s_sleep 1" ::: "memory"); }
template <int N, class LD, class OKF> DI void poll_all(u32x4 (&out)[N], bool slow, LD ld, OKF okf) {
  for (unsigned spins = 0;; ++spins) {
    ld(out);
    SBAR();
    if (__all(okf(out)) || spins > SPIN_CAP) break;
    poll_pause(slow);
  }
}
DI void lds_set(unsigned addr, unsigned v) { asm volatile("s_waitcnt lgkmcnt(0)\n\tds_write_b32 %0, %1" :: "v"(addr), "v"(v) : "memory"); }
template <int SLEEP = 1> DI void lds_wait_ge(unsigned addr, unsigned need) {
  unsigned spin = 0x100000, v;
  asm volatile("1:\n\tds_read_b32 %0, %2\n\ts_sub_u32 %1, %1, 1\n\ts_waitcnt lgkmcnt(0)\n\ts_cbranch_scc1 2f\n\tv_cmp_gt_u32 vcc, %3, %0\n\ts_cbranch_vccz 2f\n\ts_sleep %4\n\ts_branch 1b\n2:"
               : "=&v"(v), "+s"(spin) : "v"(addr), "s"(need), "n"(SLEEP) : "vcc", "scc", "memory");
}
DI s16x8 tr2(const char* p, int second) {
  s16x4 lo = __builtin_amdgcn_ds_read_tr16_b64_v4i16(LDS_PTR(s16x4, p)), hi = __builtin_amdgcn_ds_read_tr16_b64_v4i16(LDS_PTR(s16x4, p + second));
  return __builtin_shufflevector(lo, hi, 0, 1, 2, 3, 4, 5, 6, 7);
}
DI u32x16 cat4(const u32x4& a, const u32x4& b, const u32x4& c, const u32x4& d) {
  const u32x8 x = __builtin_shufflevector(a, b, 0, 1, 2, 3, 4, 5, 6, 7), y = __builtin_shufflevector(c, d, 0, 1, 2, 3, 4, 5, 6, 7);
  return __builtin_shufflevector(x, y, 0, 1, 2, 3, 4, 5, 6, 7, 8, 9, 10, 11, 12, 13, 14, 15);
}

DI unsigned stats_dst(unsigned sx, int tile, int tn, int lane) { return lane < 32 ? sx + SX_ST + tile * 8192 + tn * 512 + lane * 16 : OOB; }
DI void row_stats_sum(rsrc_t rs, unsigned sx, int tile, int lane, float& S, float& Q, float& Dd) {
  const unsigned exb = sx + SX_ST + tile * 8192 + (lane & 31) * 16;
  u32x4 pv[8];
  poll_all<8>(pv, false, [&](u32x4 (&v)[8]) {
#pragma unroll
    for (int i = 0; i < 8; ++i) v[i] = ldg(rs, exb + ((lane >> 5) * 8 + i) * 512);
  }, [&](const u32x4 (&v)[8]) { return none_sent(v); });
  S = 0.f; Q = 0.f; Dd = 0.f;
#pragma unroll
  for (int i = 0; i < 8; ++i) { S += __uint_as_float(pv[i][0]); Q += __uint_as_float(pv[i][1]); Dd += __uint_as_float(pv[i][2]); }
}
DI void row_stats_fin(char* smem, int epoch, int tile, int lane, float S, float Q, float Dd, float wsum, const u32x4 (&x1p)[4],
                      float& mean, float& var, float& mb, float& rd) {
  S = half_swap_sum(S); Q = half_swap_sum(Q); Dd = half_swap_sum(Dd);
  mean = S * (1.0f / 1024.0f); var = fmaxf(Q * (1.0f / 1024.0f) - mean * mean, 0.f);
  float den;
  ln_scalars(mean, var, mb, den);
  rd = RCP(den);
  const unsigned rb = pack2((Dd - mb * wsum) * rd, 0.f) & 0xffffu;
  const unsigned mine = (((rb & 0x8000u) ? (~rb & 0xffffu) : (rb | 0x8000u)) << 16) | (unsigned)(2047 - (tile * 32 + (lane & 31)));
  const bool best = half_max_u32(mine) == mine;
  if (best) {
    char* c = smem + L_CAND + (tile & 3) * 144;
#pragma unroll
    for (int i = 0; i < 4; ++i) *reinterpret_cast<u32x4*>(c + (2 * i + (lane >> 5)) * 16) = x1p[i];
    *reinterpret_cast<u32x4*>(c + 128) = u32x4{mine, __float_as_uint(mb), __float_as_uint(rd), 0u};
  }
  lds_set((unsigned)(uintptr_t)smem + L_W + 32 + (tile & 3) * 4, epoch);
}

DI void attn_safe(rsrc_t rs, unsigned kvoff, const u32x8& q, const u32x4& ones, f32x16& o) {
  const s16x8 q0 = __builtin_bit_cast(s16x8, __builtin_shufflevector(q, q, 0, 1, 2, 3)), q1 = __builtin_bit_cast(s16x8, __builtin_shufflevector(q, q, 4, 5, 6, 7));
  f32x4 l = {0.f, 0.f, 0.f, 0.f};
#pragma unroll
  for (int e = 0; e < 16; ++e) o[e] = 0.f;
  float m = -INFINITY;
#pragma unroll 1
  for (int kt = 0; kt < 64; ++kt) {
    const unsigned p = kvoff + kt * 4096;
    const u32x4 k0 = ldg(rs, p), k1 = ldg(rs, p + 1024), v0 = ldg(rs, p + 2048), v1 = ldg(rs, p + 3072);
    f32x16 s = {};
    s = MFMA32(__builtin_bit_cast(s16x8, k0), q0, s);
    s = MFMA32(__builtin_bit_cast(s16x8, k1), q1, s);
    float mt = fmaxf(s[0], s[1]);
#pragma unroll
    for (int e = 2; e < 16; ++e) mt = fmaxf(mt, s[e]);
    const float mn = fmaxf(m, half_swap_max(mt));
    const float alpha = EX2(m - mn);
    m = mn;
#pragma unroll
    for (int e = 0; e < 16; ++e) { s[e] = EX2(s[e] - mn); o[e] *= alpha; }
    l[0] *= alpha;
    const bf16x16 pa = __builtin_bit_cast(bf16x16, __builtin_shufflevector(s, s, 0, 1, 2, 3, 4, 5, 6, 7));
    const bf16x16 pb = __builtin_bit_cast(bf16x16, __builtin_shufflevector(s, s, 8, 9, 10, 11, 12, 13, 14, 15));
    o = __builtin_amdgcn_smfmac_f32_32x32x32_bf16(__builtin_bit_cast(bf16x8, v0), pa, o, 0xdddd, 0, 0);
    l = __builtin_amdgcn_smfmac_f32_16x16x64_bf16(__builtin_bit_cast(bf16x8, ones), pa, l, 0xdddd, 0, 0);
    o = __builtin_amdgcn_smfmac_f32_32x32x32_bf16(__builtin_bit_cast(bf16x8, v1), pb, o, 0xdddd, 0, 0);
    l = __builtin_amdgcn_smfmac_f32_16x16x64_bf16(__builtin_bit_cast(bf16x8, ones), pb, l, 0xdddd, 0, 0);
  }
  o *= 1.0f / l[0];
}

extern "C" __global__ __launch_bounds__(512) void mega_kernel(const u16* __restrict__ x0, const u16* __restrict__ wqkv, const u16* __restrict__ wo,
                                                              const u16* __restrict__ w_r, const u16* __restrict__ wg, const u16* __restrict__ wu,
                                                              const u16* __restrict__ wd, char* ws, u16* __restrict__ out) {
  __shared__ __attribute__((aligned(16))) char smem[LDS_BYTES];
  const int wid = __builtin_amdgcn_readfirstlane(threadIdx.x >> 6);
  STAMP_INIT;
  STAMPV(0);
  const int bid = blockIdx.x, xcd = bid & 7, loc = bid >> 3;
  const int tm = 8 * (xcd >> 2) + (loc >> 2), tn = 4 * (xcd & 3) + (loc & 3);
  const unsigned lbase = (unsigned)(uintptr_t)smem;
  const rsrc_t rs = __builtin_amdgcn_make_buffer_rsrc(ws, 0, (int)WS_BYTES, 0x00020000);
  volatile __attribute__((address_space(3))) unsigned* words = LDS_PTR(volatile unsigned, smem + L_W);
  *reinterpret_cast<unsigned*>(smem + L_FULL + threadIdx.x * 4) = 0u;
  words[threadIdx.x & 31] = 0u;
  BAR_LGKM();
  const int lw = wid & 1;
  const char* wb = reinterpret_cast<const char*>(wqkv + (size_t)(16 * lw) * (3 * D_) + tn * 64);
  auto stage_lanes = [](unsigned (&wv)[3]) {
    const int lane = fresh_lane();
#pragma unroll
    for (int i = 0; i < 3; ++i) {
      const int li = lane + 64 * i, k = li / 24, cp = li % 24, c = cp ^ (4 * ((k >> 1) & 1));
      wv[i] = (unsigned)(k * (3 * D_ * 2) + (c >> 3) * (D_ * 2) + (c & 7) * 16 - i * 1024);
    }
  };
  auto stage = [&](const unsigned (&wv)[3], int s, int slot) {
    const char* p = wb + (size_t)((s + 2 * tn) & 31) * (32 * 3 * D_ * 2);
    glds_v3(p, wv, lbase + slot * SLOTB + lw * 6144);
    glds_v3(p + 8 * (3 * D_ * 2), wv, lbase + slot * SLOTB + lw * 6144 + 3072);
  };
  auto load_tag = [&]() {
    u32x2 t = __builtin_amdgcn_raw_buffer_load_b64(rs, FL_EPOCH, 0, 16);
    t[0] += 1u; t[1] += 0x9E3779B9u;
    PIN(t);
    return t;
  };
  auto exchange = [&](const u32x2& tag, unsigned line0, int me) {
    const int lane = fresh_lane();
    __builtin_amdgcn_raw_buffer_store_b64(tag, rs, lane == 0 ? line0 + me * 128 : OOB, 0, 16);
    if (1) {
      unsigned po = lane < 16 ? line0 + lane * 128 : OOB;
      const u32x4 srd = {(unsigned)(uintptr_t)ws, (unsigned)((uintptr_t)ws >> 32) & 0xffffu, WS_BYTES, 0x00020000u};
      for (unsigned spins = 0;; ++spins) {
        u32x2 v = {0u, 0u};
        asm volatile("buffer_atomic_or_x2 %0, %1, %2, 0 offen sc0 sc1\n\ts_waitcnt vmcnt(0)" : "+v"(v) : "v"(po), "s"(srd) : "memory");
        po = ((v[0] == tag[0]) & (v[1] == tag[1])) ? OOB : po;
        if (__all(po == OOB) || spins > SPIN_CAP) break;
      }
      return;
    }
    __builtin_amdgcn_s_sleep(8);
    for (unsigned spins = 0;; ++spins) {
      const u32x2 v = __builtin_amdgcn_raw_buffer_load_b64(rs, line0 + (lane & 15) * 128, 0, 16);
      if (__all((v[0] == tag[0]) & (v[1] == tag[1])) || spins > SPIN_CAP) break;
      asm volatile("s_sleep 1" ::: "memory");
    }
  };
  if (wid >= 4) {
    auto wo_rows = [&](int g0, int n, int gs) {
      const int lane = fresh_lane();
      unsigned wov[4];
#pragma unroll
      for (int i = 0; i < 4; ++i) wov[i] = (unsigned)((8 * i + (lane >> 3)) * (D_ * 2) + ((lane & 7) ^ (4 * ((lane >> 4) & 1))) * 16 - i * 1024);
#pragma unroll 1
      for (int i = 0, k = (2 * (tm & 7)) % n, g; i < n && !0; ++i, k = k + 1 == n ? 0 : k + 1) g = g0 + gs * k, glds_v4t<3, 1>(reinterpret_cast<const char*>(wo) + (size_t)((64 * tn + 32 * g) & 1023) * (D_ * 2) + tn * 128, wov, lbase + g * 4096);
    };
    if (wid < 6) {
      const int lane = fresh_lane();
      unsigned wv[3];
      stage_lanes(wv);
      __builtin_amdgcn_s_setprio(3);
      const unsigned fra = lbase + L_FREE + (lane & 3) * 256, fla = lbase + L_FULL + lw * 256 + lane * 4;
      const rsrc_t rw = __builtin_amdgcn_make_buffer_rsrc(const_cast<char*>(wb), 0, 0 ? 0 : 6 * MBY - (16 * lw * 3 * D_ + tn * 64) * 2, 0x00020000);
#pragma unroll 1
      for (int L = 0; L < 2; ++L) {
        const int s0 = NS, lead = L ? 8 : NS - 1;
        int slot = 0;
        if (L == 0) {
#pragma unroll 1
          for (int i = 0, s = tm & 7; i < 2 * NS; ++i) {
            glds_v3(wb + (size_t)((s + 2 * tn) & 31) * (32 * 3 * D_ * 2) + (i & 1) * (8 * 3 * D_ * 2), wv, lbase + s * SLOTB + lw * 6144 + (i & 1) * 3072);
            asm volatile("s_waitcnt vmcnt(6)" ::: "memory");
            if (i & 1) s = s + 1 == NS ? 0 : s + 1;
          }
          asm volatile("s_waitcnt vmcnt(0)\n\tds_write_b32 %0, %1" :: "v"(fla), "v"(NS) : "memory");
          STAMPV(1);
        }
#pragma unroll 1
        for (int s = NS; s < 32; ++s) {
          lds_wait_ge(fra, 32 * L + s - lead);
          stage(wv, s, slot);
          slot = (slot == NS - 1) ? 0 : slot + 1;
          if (s >= s0 + DD) asm volatile("s_waitcnt vmcnt(%0)\n\tds_write_b32 %1, %2" :: "n"(DD * 6), "v"(fla), "v"(32 * L + s - DD + 1) : "memory");
        }
#pragma unroll
        for (int d = DD - 1; d >= 0; --d) asm volatile("s_waitcnt vmcnt(%0)\n\tds_write_b32 %1, %2" :: "n"(d * 6), "v"(fla), "v"(32 * L + 32 - d) : "memory");
        u32x2 tag = *reinterpret_cast<const u32x2*>(smem + L_TAG);
        PIN(tag);
        BAR_LGKM();
        if (lw == 0) {
          exchange(tag, FL_KV + (L * 16 + tn) * 2048, tm);
          lds_wait_ge(fra, 32 * L + 32);
          lds_set(lbase + L_W + 16, L + 1);
        } else lds_wait_ge<8>(lbase + L_W + 16, L + 1);
        __builtin_amdgcn_s_sleep(24);
        wo_rows(lw, 15, 2);
        u32x4 pre[NS * 6];
        if (L == 0) {
#pragma unroll
          for (int s = 0; s < NS; ++s) {
#pragma unroll
            for (int i = 0; i < 6; ++i)
              pre[s * 6 + i] = __builtin_amdgcn_raw_buffer_load_b128(rw, wv[i % 3], ((s + 2 * tn) & 31) * (32 * 3 * D_ * 2) + (i / 3) * (8 * 3 * D_ * 2) + (i % 3) * 1024, 0), PACE();
          }
        } else {
#pragma unroll
          for (int i = 0; i < NS * 6; ++i) asm volatile("" : "=v"(pre[i]));
        }
        BAR_ALL();
        if (lw == 0) {
          exchange(tag, FL_O + (L * 16 + tm) * 2048, tn); lds_set(lbase + L_W + 20, L + 1);
          __builtin_amdgcn_raw_buffer_store_b64(tag, rs, (bid == 0 && lane == 0) ? FL_EPOCH : OOB, 0, 16);
        }
        else wo_rows(30, 2, 1);
        BAR_ALL();
        if (L == 0) {
          char* d = smem + lw * 6144 + fresh_lane() * 16;
#pragma unroll
          for (int s = 0; s < NS; ++s) {
#pragma unroll
            for (int i = 0; i < 6; ++i) *reinterpret_cast<u32x4*>(d + s * SLOTB + i * 1024) = pre[s * 6 + i];
            lds_set(fla, 32 + s + 1);
          }
          if (lw == 0) { lds_wait_ge(fra, 33); exchange(tag, FL_X + (16 + tm) * 2048, tn); lds_set(lbase + L_W + 24, 2); }
          else lds_wait_ge<2>(lbase + L_W + 24, 2);
        }
      }
      STAMP_END;
      return;
    }
    const int a = wid - 6;
    const bool ffn = loc < 28;
    const int grp = xcd * 7 + (ffn ? loc >> 2 : 0), qa = (loc & 3) * 2 + a;
    const int kr = loc, cg = xcd;
    const unsigned wa = lbase + L_W;
    if (a) {
      const int lane = fresh_lane();
      unsigned t = __builtin_amdgcn_raw_buffer_load_b32(rs, 1 ? lane * MBY + MBY - 128 : OOB, 0, 16);
      if (1) {
        const char *c0, *c1;
        CODE_ADDR(mega_kernel, c0); CODE_ADDR(end_marker, c1);
        const long long o = (loc * 64 + lane) * 32;
        t |= *reinterpret_cast<const __attribute__((address_space(1))) unsigned*>((uintptr_t)(c0 + (o + 4 <= c1 - c0 ? o : 0)));
      }
      const u32x2 tag = load_tag();
      *reinterpret_cast<u32x2*>(smem + L_TAG) = tag;
      PIN(t);
      lds_wait_ge<0>(wa + 64 + 4 * (lane & 3), 2);
      exchange(tag, FL_X + tm * 2048, tn);
      lds_set(wa + 24, 1);
    } else {
      unsigned wv[3];
      stage_lanes(wv);
#pragma unroll 1
      for (int i = 0; i < 4 * 0; ++i) {
        glds_v3(reinterpret_cast<const char*>(wqkv + tn * 64) + (size_t)((4 * (NS + 2 * tn + 3 * (tm & 7)) + i) & 127) * (8 * 3 * D_ * 2), wv, lbase + NS * SLOTB);
        asm volatile("s_waitcnt vmcnt(6)" ::: "memory");
      }
      STAMPV(11);
      lds_wait_ge<2>(wa + 24, 1);
    }
    __builtin_amdgcn_s_sleep(16);
    auto transpose8 = [](u32x4* v) {
      u32x4 t[8];
#pragma unroll
      for (int j = 0; j < 8; ++j)
#pragma unroll
        for (int e = 0; e < 4; ++e) t[j][e] = (j & 1) ? PERM_HI(v[2 * e][j >> 1], v[2 * e + 1][j >> 1]) : PERM_LO(v[2 * e][j >> 1], v[2 * e + 1][j >> 1]);
#pragma unroll
      for (int j = 0; j < 8; ++j) { v[j] = t[j]; PIN(v[j]); }
    };
    u32x4 gf[32];
    u32x4 wdf[16];
    auto wait_go = [&](int L) {
      lds_wait_ge<8>(lbase + L_W + 16, L + 1);
      __builtin_amdgcn_s_sleep(24);
    };
    auto load_wd = [&](unsigned dv) {
      const rsrc_t rd = __builtin_amdgcn_make_buffer_rsrc(const_cast<u16*>(wd) + (size_t)grp * 64 * D_ + 128 * qa, 0, 0 ? 0 : 64 * D_ * 2 - 256 * qa, 0x00020000);
#pragma unroll
      for (int i = 0; i < 16; ++i) {
        wdf[i] = __builtin_amdgcn_raw_buffer_load_b128(rd, dv, (32 * (i >> 3) + (i & 7)) * (D_ * 2), 0);
        asm volatile("s_sleep 2\n\ts_waitcnt vmcnt(3)" ::: "memory");
      }
    };
    {
      LANE_VARS;
      const u32x4 sv = {SENT, SENT, SENT, SENT};
#pragma unroll
      for (int i = 0; i < 6; ++i) {
        const unsigned o = ((i * 256 + bid) * 128 + a * 64 + lane) * 16;
        stg(rs, o < SX_BYTES ? OFF_SX + o : OOB, sv);
      }
      if (a == 0) {
        const u32x4 w0 = *reinterpret_cast<const u32x4*>(w_r + lane * 8), w1 = *reinterpret_cast<const u32x4*>(w_r + 512 + lane * 8);
        float t = 0.f;
#pragma unroll
        for (int e = 0; e < 4; ++e) t = fadd(fadd(t, fadd(lo16(w0[e]), hi16(w0[e]))), fadd(lo16(w1[e]), hi16(w1[e])));
        words[0] = __float_as_uint(wave_sum(t));
        if ((lane >> 3) == (tn & 7)) *reinterpret_cast<u32x4*>(smem + L_WR + (lane & 7) * 16) = tn < 8 ? w0 : w1;
      }
      const u16* p = wqkv + (size_t)(64 * tm + 32 * a + hi) * (3 * D_) + (c24 >> 3) * D_ + tn * 64 + (c24 & 7) * 8;
      float cs[8] = {0.f, 0.f, 0.f, 0.f, 0.f, 0.f, 0.f, 0.f};
      u32x4 v[16];
#pragma unroll
      for (int i = 0; i < 16; ++i) v[i] = *reinterpret_cast<const u32x4*>(p + (size_t)i * (2 * 3 * D_));
      SBAR();
#pragma unroll
      for (int i = 0; i < 16; ++i)
#pragma unroll
        for (int e = 0; e < 4; ++e) { cs[2 * e] = fadd(cs[2 * e], lo16(v[i][e])); cs[2 * e + 1] = fadd(cs[2 * e + 1], hi16(v[i][e])); }
      u32x4 o;
#pragma unroll
      for (int e = 0; e < 4; ++e) { const float x = half_swap_sum(cs[e]), y = half_swap_sum(cs[4 + e]); o[e] = __float_as_uint(hi ? y : x); }
      stg(rs, r32 < 24 ? OFF_CS + (tn * 32 + tm * 2 + a) * 768 + r32 * 32 + hi * 16 : OOB, o);
    }
    {
      BAR_ALL();
      LANE_VARS;
      const char* gp = reinterpret_cast<const char*>(((lane & 8) ? wu : wg) + (size_t)(128 * qa + 16 * hi + 4 * tblk) * F_ + 64 * grp + (lane & 7) * 8);
      unsigned dv = (lane >> 4) * (8 * D_ * 2) + (lane & 15) * 16;
      PIN(gp); PIN(dv);
      unsigned sv[3];
#pragma unroll
      for (int i = 0; i < 3; ++i) { const int li = lane + 64 * i, k = li / 24, c = li % 24; sv[i] = (unsigned)(k * (3 * D_ * 2) + (c >> 3) * (D_ * 2) + (c & 7) * 16 - i * 1024); PIN(sv[i]); }
      wait_go(0);
#pragma unroll
      for (int i = 0; i < 32; ++i) {
        gf[i] = *reinterpret_cast<const u32x4*>(gp + (size_t)(0 ? 0 : 32 * (i >> 3) + 8 * ((i >> 2) & 1) + (i & 3)) * (F_ * 2));
        asm volatile("s_sleep 2\n\ts_waitcnt vmcnt(3)" ::: "memory");
      }
      load_wd(dv);
      STAMPV(7);
      BARRIER();
      __builtin_amdgcn_s_sleep(2);
#pragma unroll
      for (int g = 0; g < 4; ++g) glds_v3(wqkv + (size_t)(32 * kr + 8 * g) * (3 * D_) + (2 * cg + a) * 64, sv, lbase + L_SL + a * 12288 + g * 3072);
      transpose8(gf); transpose8(gf + 8); transpose8(gf + 16); transpose8(gf + 24);
      transpose8(wdf); transpose8(wdf + 8);
      {
        LANE_VARS;
        f32x4 t[2];
#pragma unroll
        for (int c = 0; c < 2; ++c) {
          u32x4 v[8];
#pragma unroll
          for (int i = 0; i < 8; ++i) v[i] = ldg(rs, OFF_CS + (tn * 32 + 16 * c + 2 * i + hi) * 768 + a * 384 + c24 * 16);
          SBAR();
          t[c] = sum4<8>(v);
          SBAR();
        }
#pragma unroll
        for (int e = 0; e < 4; ++e) t[0][e] = half_swap_sum(fadd(t[0][e], t[1][e]));
        if (lane < 24) *reinterpret_cast<f32x4*>(smem + L_CS + a * 384 + lane * 16) = t[0];
      }
      BAR_LGKM();
    }
#pragma unroll 1
    for (int L = fresh_s(0);; ++L) {
      const unsigned sx = OFF_SX + L * SX_LAYER;
      STAMPV(1 + 16 * L);
      if (0 && L == 0) { words[3] = 5000u; lds_set(wa + 4 + 4 * a, 1); } else {
      u32x4 hf[5];
      unsigned key;
      { LANE_VARS;
      {
        unsigned cdst = lane < 16 ? sx + SX_ROW + (lane >> 3) * 32768 + tm * 2048 + tn * 128 + (lane & 7) * 16 : sx + SX_CAND + (tn * 16 + tm) * 128;
        cdst = (a == 0 && lane < 17) ? cdst : OOB;
        PIN(cdst);
        lds_wait_ge<0>(wa + 32 + 4 * tp, L + 1);
        key = *reinterpret_cast<const unsigned*>(smem + L_CAND + tp * 144 + 128);
        key = max(key, dpp<0xB1>(key)); key = max(key, dpp<0x4E>(key));
        const char* cp = smem + L_CAND + (((2047 - (int)(key & 2047u)) >> 5) & 3) * 144;
        const u32x4 c = *reinterpret_cast<const u32x4*>(cp + (lane < 16 ? lane & 7 : 8) * 16), rec = *reinterpret_cast<const u32x4*>(cp + 128);
        u32x4 o;
#pragma unroll
        for (int e = 0; e < 4; ++e) { unsigned h = ln_pair_s(c[e], -U2F(rec[1]), U2F(rec[2])); PIN(h); o[e] = nz(lane < 8 ? h : c[e]); }
        stg(rs, cdst, o);
      }
      STAMPV(8 + 16 * L);
      __builtin_amdgcn_s_sleep(13);
      poll_all<5>(hf, L == 0, [&](u32x4 (&t)[5]) {
        t[4][0] = __builtin_amdgcn_raw_buffer_load_b32(rs, sx + SX_CAND + (tn * 16 + (lane & 15)) * 128, 0, 16);
#pragma unroll
        for (int i = 0; i < 4; ++i) t[i] = ldg(rs, sx + SX_ROW + ((2047 - (int)(key & 2047u)) >> 7) * 2048 + (16 * qa + 4 * i + (lane >> 4)) * 16);
        t[4][1] = t[4][2] = t[4][3] = key;
      }, [&](const u32x4 (&t)[5]) {
        unsigned k = t[4][0] == SENT ? 0u : t[4][0];
        k = max(k, dpp<0xB1>(k)); k = max(k, dpp<0x4E>(k)); k = max(k, dpp<0x141>(k)); k = max(k, dpp<0x140>(k));
        key = max(key, k);
        return none_sent(t) & (key == t[4][1]);
      });
      key = __builtin_amdgcn_readfirstlane(key);
      words[3] = 2047u - (key & 2047u);
      if (L == 1) lds_set(wa + 4 + 4 * a, 2);
      }
      STAMPV(2 + 16 * L);
      f32x4 d[8];
      const f32x4 zero = {0.f, 0.f, 0.f, 0.f};
      SBAR();
      __builtin_amdgcn_s_setprio(3);
#pragma unroll
      for (int t = 0; t < 32; ++t) d[t & 7] = MFMA16(__builtin_bit_cast(s16x8, hf[t >> 3]), __builtin_bit_cast(s16x8, gf[t]), t < 8 ? zero : d[t & 7]);
      SBAR();
      __builtin_amdgcn_s_setprio(0);
      LANE_VARS;
      const int idx = 2047 - (int)(key & 2047u), win = idx >> 7;
      const bool owner = win == tm;
      auto pick2 = [&](const f32x4 (&d)[8]) {
        u32x2 o;
#pragma unroll
        for (int e = 0; e < 2; ++e) {
          const float x = tblk ? d[2 + e][0] : d[e][0], y = tblk ? d[6 + e][0] : d[4 + e][0];
          o[e] = nzf(hi ? y : x);
        }
        return o;
      };
      if (ffn) {
        const unsigned ppos = (lane & 15) * 32 + (lane >> 4) * 8;
        __builtin_amdgcn_raw_buffer_store_b64(pick2(d), rs, sx + SX_GP + (grp * 8 + qa) * 512 + ppos, 0, 16);
        STAMPV(9 + 16 * L);
        __builtin_amdgcn_s_sleep(13);
        if (L == 0) __builtin_amdgcn_s_sleep(0);
        u32x2 gu[8];
        for (unsigned spins = 0;; ++spins) {
#pragma unroll
          for (int i = 0; i < 8; ++i) gu[i] = __builtin_amdgcn_raw_buffer_load_b64(rs, sx + SX_GP + (grp * 8 + i) * 512 + lane * 8, 0, 16);
          SBAR();
          unsigned m = 0u;
#pragma unroll
          for (int i = 0; i < 8; ++i) m = max(max(m, gu[i][0]), gu[i][1]);
          if (__all(m != SENT) || spins > SPIN_CAP) break;
          poll_pause(L == 0);
        }
        STAMPV(10 + 16 * L);
        float t0 = opq(U2F(gu[0][0])), t1 = opq(U2F(gu[0][1]));
#pragma unroll
        for (int i = 1; i < 8; ++i) { t0 = fadd(t0, U2F(gu[i][0])); t1 = fadd(t1, U2F(gu[i][1])); }
        auto sw = __builtin_amdgcn_permlane32_swap(__float_as_uint(t0), __float_as_uint(t1), false, false);
        const float gv = rbf(U2F(sw[0])), uv = rbf(U2F(sw[1]));
        reinterpret_cast<u16*>(smem + L_GU + a * 128)[2 * r32 + hi] = (u16)pack2(fmul(rbf(fmul(gv, RCP(fadd(1.0f, __expf(-gv))))), uv), 0.f);
        asm volatile("s_waitcnt lgkmcnt(0)" ::: "memory");
        const s16x8 a0 = *reinterpret_cast<const s16x8*>(smem + L_GU + a * 128 + (lane >> 4) * 16), a1 = *reinterpret_cast<const s16x8*>(smem + L_GU + a * 128 + 64 + (lane >> 4) * 16);
        SBAR();
        __builtin_amdgcn_s_setprio(3);
#pragma unroll
        for (int t = 0; t < 16; ++t) d[t & 7] = MFMA16(t < 8 ? a0 : a1, __builtin_bit_cast(s16x8, wdf[t]), t < 8 ? zero : d[t & 7]);
        SBAR();
        __builtin_amdgcn_s_setprio(0);
        __builtin_amdgcn_raw_buffer_store_b64(pick2(d), rs, sx + SX_YP + grp * 4096 + qa * 512 + ppos, 0, 16);
      }
      STAMPV(3 + 16 * L);
      if (L == 1 && (cg != (kr & 7) || a)) { STAMP_END; __builtin_amdgcn_endpgm(); }
      s16x8 wpf[12];
#pragma unroll
      for (int t = 0; t < 12; ++t) wpf[t] = tr2(smem + L_SL + a * 12288 + (16 * hi + 4 * tblk + tq) * 384 + t * 32 + tp * 8, 8 * 384);
#pragma unroll
      for (int t = 0; t < 12; ++t) PIN(wpf[t]);
      if (L == 0 && !owner) lds_set(wa + 4 + 4 * a, 1);
      __builtin_amdgcn_s_sleep(13 - 4);
      if (L == 0) __builtin_amdgcn_s_sleep(0);
      if (L == 0 && !ffn) __builtin_amdgcn_s_sleep(64);
      const unsigned rk = key >> 16;
      float rsig = rbf(RCP(fadd(1.0f, __expf(-lo16((rk & 0x8000u) ? (rk & 0x7fffu) : (~rk & 0xffffu))))));
      PIN(rsig);
      const int n = 4 * (lane & 7) + ((lane >> 3) & 3);
      const int fo = ((n >> 4) * 2 + ((n >> 2) & 1)) * 8 + ((n >> 3) & 1) * 4 + (n & 3);
      u32x4 pv[8];
      poll_all<8>(pv, L == 0, [&](u32x4 (&t)[8]) {
#pragma unroll
        for (int i = 0; i < 7; ++i) t[i] = ldg(rs, sx + SX_YP + (8 * i + (lane >> 3)) * 4096 + kr * 128 + (lane & 7) * 16);
        t[7][0] = t[7][2] = __builtin_amdgcn_raw_buffer_load_b32(rs, sx + SX_ROW + win * 2048 + kr * 64 + (fo >> 1) * 4, 0, 16);
        t[7][1] = t[7][3] = __builtin_amdgcn_raw_buffer_load_b32(rs, sx + SX_ROW + 32768 + win * 2048 + kr * 64 + (fo >> 1) * 4, 0, 16);
      }, [&](const u32x4 (&t)[8]) { return none_sent(t); });
      const float hv = (fo & 1) ? hi16(pv[7][0]) : lo16(pv[7][0]), xv = (fo & 1) ? hi16(pv[7][1]) : lo16(pv[7][1]);
      STAMPV(4 + 16 * L);
      float ys;
      {
        f32x4 t = sum4<7>(pv);
#pragma unroll
        for (int e = 0; e < 4; ++e) {
          t[e] = fadd(t[e], U2F(dpp<0x128>(__float_as_uint(t[e]))));
          t[e] = half_swap_sum(row_swap_sum(t[e]));
        }
        const float x = (lane & 8) ? t[1] : t[0], y = (lane & 8) ? t[3] : t[2];
        ys = tblk ? y : x;
      }
      const unsigned ob = pack2(fadd(xv, rbf(fadd(hv, rbf(fmul(rbf(ys), rsig))))), 0.f) & 0xffffu;
      if (L == 1) {
        if (lane < 32) out[(size_t)idx * D_ + kr * 32 + n] = (u16)ob;
        STAMPV(5 + 16 * L);
        STAMP_END;
        __builtin_amdgcn_endpgm();
      }
      reinterpret_cast<u16*>(smem + L_X2F + a * 64)[fo] = (u16)ob;
      asm volatile("s_waitcnt lgkmcnt(0)" ::: "memory");
      {
        const s16x8 xa = *reinterpret_cast<const s16x8*>(smem + L_X2F + a * 64 + (lane >> 4) * 16);
        const u32x4 xf = *reinterpret_cast<const u32x4*>(smem + L_X2F + a * 64 + tp * 16);
        f32x4 z[12];
        SBAR();
        __builtin_amdgcn_s_setprio(3);
#pragma unroll
        for (int t = 0; t < 12; ++t) z[t] = MFMA16(xa, wpf[t], zero);
        SBAR();
        __builtin_amdgcn_s_setprio(0);
        const float x2 = lo16(ob), s1 = half_sum(x2), s2 = half_sum(fmul(x2, x2));
        u32x4 o;
#pragma unroll
        for (int e = 0; e < 4; ++e) {
          const unsigned sp = lane < 52 ? (e == 0 ? __float_as_uint(s1) : __float_as_uint(s2)) : xf[e];
          const unsigned x = __float_as_uint(tblk ? z[4 + e][0] : z[e][0]), y = tblk ? sp : __float_as_uint(z[8 + e][0]);
          o[e] = nz(hi ? y : x);
        }
        const unsigned pos = lane < 24 ? lane * 16 : lane < 48 ? lane * 16 + 128 : lane < 52 ? (lane & 1) * 512 + 384 : 400 + tp * 16;
        stg(rs, lane < 56 ? OFF_SX + SX_QP + ((2 * cg + a) * 32 + kr) * 1024 + pos : OOB, o);
      }
      STAMPV(5 + 16 * L);
      if (owner) {
        __builtin_amdgcn_s_sleep(13);
        const int c25 = r32 < 25 ? r32 : 24, pl = 24 * a + c24, col = 64 * (pl >> 4) + (pl & 15);
        float cs[4];
#pragma unroll
        for (int e = 0; e < 4; ++e) { cs[e] = reinterpret_cast<const float*>(smem + L_CS)[col + 16 * e]; PIN(cs[e]); }
        u32x4 qv[17];
        poll_all<17>(qv, true, [&](u32x4 (&t)[17]) {
#pragma unroll
          for (int i = 0; i < 16; ++i) t[i] = ldg(rs, OFF_SX + SX_QP + (tn * 32 + 2 * i + hi) * 1024 + a * 512 + c25 * 16);
          t[16] = ldg(rs, OFF_SX + SX_QP + (tn * 34 + hi) * 1024 + 400 + tp * 16);
        }, [&](const u32x4 (&t)[17]) { return none_sent(t); });
        *reinterpret_cast<u32x4*>(smem + L_X2P + hi * 64 + tp * 16) = qv[16];
        f32x4 t = sum4<16>(qv);
#pragma unroll
        for (int e = 0; e < 4; ++e) t[e] = half_swap_sum(t[e]);
        const float mean = fmul(U2F(__builtin_amdgcn_readlane(__float_as_uint(t[0]), 24)), 1.0f / 1024.0f);
        const float var = fmaxf(ffma(-mean, mean, fmul(U2F(__builtin_amdgcn_readlane(__float_as_uint(t[1]), 24)), 1.0f / 1024.0f)), 0.f);
        float mb, den;
        ln_scalars(mean, var, mb, den);
        const float rd = RCP(den);
        if (lane < 24) {
#pragma unroll
          for (int e = 0; e < 4; ++e) reinterpret_cast<float*>(smem + L_PATCH)[col + 16 * e] = fmul(ffma(-mb, cs[e], t[e]), rd);
        }
        lds_set(wa + 4 + 4 * a, L + 1);
      }
      STAMPV(6 + 16 * L);
      }
      BAR_ALL();
      unsigned dv = fresh_lane();
      dv = (dv >> 4) * (8 * D_ * 2) + (dv & 15) * 16;
      PIN(dv);
      wait_go(1);
      load_wd(dv);
      STAMPV(23);
      BARRIER();
      transpose8(wdf); transpose8(wdf + 8);
      BARRIER();
    }
  }
  const int tm_ = tm, tn_ = tn, wid_ = wid;
  {
    const int tid = threadIdx.x, lane = tid & 63, r32 = lane & 31, row = 2 * wid + (lane >> 5);
    char* sm = smem + L_XCH;
    u32x4 v[4];
    asm volatile("global_load_dwordx4 %0, %4, off\n\tglobal_load_dwordx4 %1, %4, off offset:512\n\tglobal_load_dwordx4 %2, %4, off offset:1024\n\tglobal_load_dwordx4 %3, %4, off offset:1536"
                 : "=&v"(v[0]), "=&v"(v[1]), "=&v"(v[2]), "=&v"(v[3]) : "v"(x0 + (size_t)(tm * 128 + tn * 8 + row) * D_ + r32 * 8) : "memory");
    int pass = fresh_s(0);
#pragma unroll 1
    for (;;) {
      asm volatile("s_cmp_eq_u32 %4, 0\n\ts_cbranch_scc1 1f\n\ts_waitcnt vmcnt(0)\n1:" : "+v"(v[0]), "+v"(v[1]), "+v"(v[2]), "+v"(v[3]) : "s"(pass) : "memory", "scc");
      float s = 0.f, s2 = 0.f;
#pragma unroll
      for (int i = 0; i < 4; ++i)
#pragma unroll
        for (int e = 0; e < 4; e += 2) { s = dot2(v[i][e], 0x3f803f80u, s); s2 = dot2(v[i][e + 1], 0x3f803f80u, s2); }
      const float mean = half_sum(s + s2) * (1.0f / 1024.0f);
      f32x2 m2 = {0.f, 0.f};
#pragma unroll
      for (int i = 0; i < 4; ++i)
#pragma unroll
        for (int e = 0; e < 4; ++e) { const f32x2 d = f32x2{lo16(v[i][e]), hi16(v[i][e])} - mean; m2 += d * d; }
      float mb, den;
      ln_scalars(mean, half_sum(m2[0] + m2[1]) * (1.0f / 1024.0f), mb, den);
      const float rd = 1.0f / den;
#pragma unroll
      for (int i = 0; i < 4; ++i) {
        char* p = sm + ((16 * i + (r32 >> 1)) * 2) * 144 + row * 16 + (r32 & 1) * 8;
        u32x2 lo = {ln_pair(v[i][0], mb, rd), ln_pair(v[i][1], mb, rd)}, hi = {ln_pair(v[i][2], mb, rd), ln_pair(v[i][3], mb, rd)};
        *reinterpret_cast<u32x2*>(p) = lo;
        *reinterpret_cast<u32x2*>(p + 144) = hi;
      }
      STAMPV(37);
      lds_set(lbase + L_W + 64 + 4 * wid, pass);
      lds_wait_ge<0>(lbase + L_W + 64 + 4 * (lane & 3), pass);
      u32x4 t[4];
#pragma unroll
      for (int i = 0; i < 4; ++i) t[i] = *reinterpret_cast<const u32x4*>(sm + (i * 32 + (tid >> 3)) * 144 + (tid & 7) * 16);
      SBAR();
      unsigned dst = OFF_HF + (tm * 4 + (tn >> 2)) * TS + (tn & 3) * 128 + (tid >> 3) * 512 + (tid & 7) * 16;
      PIN(dst);
      if (pass) {
#pragma unroll
        for (int i = 0; i < 4; ++i) stg(rs, dst + i * 16384, t[i]);
        break;
      }
#pragma unroll
      for (int i = 0; i < 4; ++i) PIN(t[i]);
      pass = fresh_s(1);
    }
    DRAIN();
    lds_set(lbase + L_W + 64 + 4 * wid, 2);
    STAMPV(38);
  }
  const u32x4 srd = {(unsigned)(uintptr_t)ws, (unsigned)((uintptr_t)ws >> 32) & 0xffffu, WS_BYTES, 0x00020000u};
  u32x4 x1p[4];
  asm volatile("" : "=v"(x1p[0]), "=v"(x1p[1]), "=v"(x1p[2]), "=v"(x1p[3]));
#pragma unroll 1
  for (int L = 0; L < 2; ++L) {
    const unsigned sx = OFF_SX + L * SX_LAYER;
    u32x8 q[4];
    u32x16 kva, kvb;
    f32x16 oz[5];
    {
      f32x16 acc[6];
      u32x16 stA, stB, xr0, xr1, xr2, xr3;
      u32x4 stC;
      asm volatile("" : "={v[112:127]}"(stB), "={v[128:131]}"(stC));
      { PHASE_VARS;
      stB[8] = lane * 16 + (L ? OFF_X1 : OFF_HF) + tile * TS;
      stB[9] = lbase + wrow * 384 + (((4 * hh + wch) ^ wsw) << 4) + 8 * (tp & 1);
      stB[10] = lbase + wrow * 384 + (((4 * (hh ^ 1) + wch) ^ wsw) << 4) + 8 * (tp & 1);
      stB[13] = lbase + L_FULL + (lane & 1) * 256;
      stB[15] = lbase + L_FREE + wid * 256 + lane * 4;
      stC[0] = 32 * L; }
      unsigned soff, slot, need = 32 * L + 1;
      if (L == 0) {
        lds_wait_ge<0>(lbase + L_W + 24, 1);
        STAMPV(39);
        soff = tn * 4096;
        asm volatile(
          "buffer_load_dwordx4 v[132:135], v120, s[40:43], s44 offen offset:0 sc1\n\t"
          "buffer_load_dwordx4 v[136:139], v120, s[40:43], s44 offen offset:1024 sc1\n\t"
          "buffer_load_dwordx4 v[140:143], v120, s[40:43], s44 offen offset:2048 sc1\n\t"
          "buffer_load_dwordx4 v[144:147], v120, s[40:43], s44 offen offset:3072 sc1\n\t"
          "s_add_u32 s44, s44, 0x1000\n\t"
          "s_and_b32 s44, s44, 0xffff\n\t"
          "buffer_load_dwordx4 v[148:151], v120, s[40:43], s44 offen offset:0 sc1\n\t"
          "buffer_load_dwordx4 v[152:155], v120, s[40:43], s44 offen offset:1024 sc1\n\t"
          "buffer_load_dwordx4 v[156:159], v120, s[40:43], s44 offen offset:2048 sc1\n\t"
          "buffer_load_dwordx4 v[160:163], v120, s[40:43], s44 offen offset:3072 sc1\n\t"
          "s_add_u32 s44, s44, 0x1000\n\t"
          "s_and_b32 s44, s44, 0xffff\n\t"
          "buffer_load_dwordx4 v[164:167], v120, s[40:43], s44 offen offset:0 sc1\n\t"
          "buffer_load_dwordx4 v[168:171], v120, s[40:43], s44 offen offset:1024 sc1\n\t"
          "buffer_load_dwordx4 v[172:175], v120, s[40:43], s44 offen offset:2048 sc1\n\t"
          "buffer_load_dwordx4 v[176:179], v120, s[40:43], s44 offen offset:3072 sc1\n\t"
          "s_add_u32 s44, s44, 0x1000\n\t"
          "s_and_b32 s44, s44, 0xffff\n\t"
          "buffer_load_dwordx4 v[180:183], v120, s[40:43], s44 offen offset:0 sc1\n\t"
          "buffer_load_dwordx4 v[184:187], v120, s[40:43], s44 offen offset:1024 sc1\n\t"
          "buffer_load_dwordx4 v[188:191], v120, s[40:43], s44 offen offset:2048 sc1\n\t"
          "buffer_load_dwordx4 v[192:195], v120, s[40:43], s44 offen offset:3072 sc1\n\t"
          : "={v[132:147]}"(xr0), "={v[148:163]}"(xr1), "={v[164:179]}"(xr2), "={v[180:195]}"(xr3), "+{s44}"(soff) : "{v[112:127]}"(stB), "{s[40:43]}"(srd) : "scc", "memory");
      } else {
        xr0 = cat4(x1p[0], x1p[1], x1p[2], x1p[3]);
        asm volatile("" : "={v[148:163]}"(xr1), "={v[164:179]}"(xr2), "={v[180:195]}"(xr3));
      }
      STAMPV(1 + 16 * L);
      asm volatile(
        "v_mov_b32 v123, v121\n\t"
        "v_mov_b32 v124, v122\n\t"
        "s_mov_b64 vcc, -1\n\t"
        "s_cbranch_vccnz 10f\n\t"
        "11:\n\t"
        "s_mov_b32 s46, 12288\n\t"
        "s_add_u32 s47, s47, 1\n\t"
        "ds_read_b64_tr_b16 v[96:97], v123 offset:0\n\t"
        "ds_read_b64_tr_b16 v[98:99], v123 offset:3072\n\t"
        "ds_read_b64_tr_b16 v[100:101], v124 offset:0\n\t"
        "ds_read_b64_tr_b16 v[102:103], v124 offset:3072\n\t"
        "ds_read_b64_tr_b16 v[104:105], v123 offset:128\n\t"
        "ds_read_b64_tr_b16 v[106:107], v123 offset:3200\n\t"
        "ds_read_b64_tr_b16 v[108:109], v124 offset:128\n\t"
        "ds_read_b64_tr_b16 v[110:111], v124 offset:3200\n\t"
        "ds_read_b64_tr_b16 v[112:113], v123 offset:256\n\t"
        "ds_read_b64_tr_b16 v[114:115], v123 offset:3328\n\t"
        "ds_write_b32 v127, v128\n\t"
        "ds_read_b64_tr_b16 v[116:117], v124 offset:256\n\t"
        "ds_read_b64_tr_b16 v[118:119], v124 offset:3328\n\t"
        "s_waitcnt vmcnt(15) lgkmcnt(11)\n\t"
        "v_mfma_f32_32x32x16_bf16 v[0:15], v[96:99], v[132:135], 0\n\t"
        "ds_read_b32 v126, v125\n\t"
        "ds_read_b64_tr_b16 v[96:97], v123 offset:6144\n\t"
        "ds_read_b64_tr_b16 v[98:99], v123 offset:9216\n\t"
        "s_waitcnt lgkmcnt(12)\n\t"
        "v_mfma_f32_32x32x16_bf16 v[16:31], v[100:103], v[132:135], 0\n\t"
        "v_add_u32 v129, s46, v121\n\t"
        "ds_read_b64_tr_b16 v[100:101], v124 offset:6144\n\t"
        "ds_read_b64_tr_b16 v[102:103], v124 offset:9216\n\t"
        "s_waitcnt lgkmcnt(12)\n\t"
        "v_mfma_f32_32x32x16_bf16 v[32:47], v[104:107], v[132:135], 0\n\t"
        "ds_read_b64_tr_b16 v[104:105], v123 offset:6272\n\t"
        "ds_read_b64_tr_b16 v[106:107], v123 offset:9344\n\t"
        "s_waitcnt lgkmcnt(12)\n\t"
        "v_mfma_f32_32x32x16_bf16 v[48:63], v[108:111], v[132:135], 0\n\t"
        "v_add_u32 v130, s46, v122\n\t"
        "ds_read_b64_tr_b16 v[108:109], v124 offset:6272\n\t"
        "ds_read_b64_tr_b16 v[110:111], v124 offset:9344\n\t"
        "s_waitcnt lgkmcnt(12)\n\t"
        "v_mfma_f32_32x32x16_bf16 v[64:79], v[132:135], v[112:115], 0\n\t"
        "ds_read_b64_tr_b16 v[112:113], v123 offset:6400\n\t"
        "ds_read_b64_tr_b16 v[114:115], v123 offset:9472\n\t"
        "s_waitcnt lgkmcnt(10)\n\t"
        "v_mfma_f32_32x32x16_bf16 v[80:95], v[132:135], v[116:119], 0\n\t"
        "v_cmp_gt_u32 vcc, s47, v126\n\t"
        "ds_read_b64_tr_b16 v[116:117], v124 offset:6400\n\t"
        "ds_read_b64_tr_b16 v[118:119], v124 offset:9472\n\t"
        "s_waitcnt vmcnt(14) lgkmcnt(10)\n\t"
        "v_mfma_f32_32x32x16_bf16 v[0:15], v[96:99], v[136:139], v[0:15]\n\t"
        "s_cbranch_vccnz 12f\n\t"
        "13:\n\t"
        "ds_read_b64_tr_b16 v[96:97], v129 offset:0\n\t"
        "ds_read_b64_tr_b16 v[98:99], v129 offset:3072\n\t"
        "s_waitcnt lgkmcnt(10)\n\t"
        "v_mfma_f32_32x32x16_bf16 v[16:31], v[100:103], v[136:139], v[16:31]\n\t"
        "v_add_u32 v128, 1, v128\n\t"
        "ds_read_b64_tr_b16 v[100:101], v130 offset:0\n\t"
        "ds_read_b64_tr_b16 v[102:103], v130 offset:3072\n\t"
        "s_waitcnt lgkmcnt(10)\n\t"
        "v_mfma_f32_32x32x16_bf16 v[32:47], v[104:107], v[136:139], v[32:47]\n\t"
        "ds_read_b64_tr_b16 v[104:105], v129 offset:128\n\t"
        "ds_read_b64_tr_b16 v[106:107], v129 offset:3200\n\t"
        "s_waitcnt lgkmcnt(10)\n\t"
        "v_mfma_f32_32x32x16_bf16 v[48:63], v[108:111], v[136:139], v[48:63]\n\t"
        "s_add_u32 s46, s46, 12288\n\t"
        "s_add_u32 s47, s47, 1\n\t"
        "ds_read_b64_tr_b16 v[108:109], v130 offset:128\n\t"
        "ds_read_b64_tr_b16 v[110:111], v130 offset:3200\n\t"
        "s_waitcnt lgkmcnt(10)\n\t"
        "v_mfma_f32_32x32x16_bf16 v[64:79], v[136:139], v[112:115], v[64:79]\n\t"
        "s_cmp_eq_u32 s46, 110592\n\t"
        "s_cselect_b32 s46, 0, s46\n\t"
        "ds_read_b64_tr_b16 v[112:113], v129 offset:256\n\t"
        "ds_read_b64_tr_b16 v[114:115], v129 offset:3328\n\t"
        "s_waitcnt lgkmcnt(10)\n\t"
        "v_mfma_f32_32x32x16_bf16 v[80:95], v[136:139], v[116:119], v[80:95]\n\t"
        "s_cmp_lt_u32 s47, 32\n\t"
        "s_cbranch_scc1 4f\n\t"
        "s_waitcnt vmcnt(0)\n\t"
        "4:\n\t"
        "ds_write_b32 v127, v128\n\t"
        "ds_read_b64_tr_b16 v[116:117], v130 offset:256\n\t"
        "ds_read_b64_tr_b16 v[118:119], v130 offset:3328\n\t"
        "s_waitcnt vmcnt(13) lgkmcnt(11)\n\t"
        "v_mfma_f32_32x32x16_bf16 v[0:15], v[96:99], v[140:143], v[0:15]\n\t"
        "ds_read_b32 v126, v125\n\t"
        "ds_read_b64_tr_b16 v[96:97], v129 offset:6144\n\t"
        "ds_read_b64_tr_b16 v[98:99], v129 offset:9216\n\t"
        "s_waitcnt lgkmcnt(12)\n\t"
        "v_mfma_f32_32x32x16_bf16 v[16:31], v[100:103], v[140:143], v[16:31]\n\t"
        "v_add_u32 v123, s46, v121\n\t"
        "ds_read_b64_tr_b16 v[100:101], v130 offset:6144\n\t"
        "ds_read_b64_tr_b16 v[102:103], v130 offset:9216\n\t"
        "s_waitcnt lgkmcnt(12)\n\t"
        "v_mfma_f32_32x32x16_bf16 v[32:47], v[104:107], v[140:143], v[32:47]\n\t"
        "ds_read_b64_tr_b16 v[104:105], v129 offset:6272\n\t"
        "ds_read_b64_tr_b16 v[106:107], v129 offset:9344\n\t"
        "s_waitcnt lgkmcnt(12)\n\t"
        "v_mfma_f32_32x32x16_bf16 v[48:63], v[108:111], v[140:143], v[48:63]\n\t"
        "v_add_u32 v124, s46, v122\n\t"
        "ds_read_b64_tr_b16 v[108:109], v130 offset:6272\n\t"
        "ds_read_b64_tr_b16 v[110:111], v130 offset:9344\n\t"
        "s_waitcnt lgkmcnt(12)\n\t"
        "v_mfma_f32_32x32x16_bf16 v[64:79], v[140:143], v[112:115], v[64:79]\n\t"
        "ds_read_b64_tr_b16 v[112:113], v129 offset:6400\n\t"
        "ds_read_b64_tr_b16 v[114:115], v129 offset:9472\n\t"
        "s_waitcnt lgkmcnt(10)\n\t"
        "v_mfma_f32_32x32x16_bf16 v[80:95], v[140:143], v[116:119], v[80:95]\n\t"
        "v_cmp_gt_u32 vcc, s47, v126\n\t"
        "ds_read_b64_tr_b16 v[116:117], v130 offset:6400\n\t"
        "ds_read_b64_tr_b16 v[118:119], v130 offset:9472\n\t"
        "s_waitcnt vmcnt(12) lgkmcnt(10)\n\t"
        "v_mfma_f32_32x32x16_bf16 v[0:15], v[96:99], v[144:147], v[0:15]\n\t"
        "s_cbranch_vccnz 14f\n\t"
        "15:\n\t"
        "ds_read_b64_tr_b16 v[96:97], v123 offset:0\n\t"
        "ds_read_b64_tr_b16 v[98:99], v123 offset:3072\n\t"
        "s_waitcnt lgkmcnt(10)\n\t"
        "v_mfma_f32_32x32x16_bf16 v[16:31], v[100:103], v[144:147], v[16:31]\n\t"
        "v_add_u32 v128, 1, v128\n\t"
        "ds_read_b64_tr_b16 v[100:101], v124 offset:0\n\t"
        "ds_read_b64_tr_b16 v[102:103], v124 offset:3072\n\t"
        "s_waitcnt lgkmcnt(10)\n\t"
        "v_mfma_f32_32x32x16_bf16 v[32:47], v[104:107], v[144:147], v[32:47]\n\t"
        "ds_read_b64_tr_b16 v[104:105], v123 offset:128\n\t"
        "ds_read_b64_tr_b16 v[106:107], v123 offset:3200\n\t"
        "s_waitcnt lgkmcnt(10)\n\t"
        "v_mfma_f32_32x32x16_bf16 v[48:63], v[108:111], v[144:147], v[48:63]\n\t"
        "s_add_u32 s46, s46, 12288\n\t"
        "s_add_u32 s47, s47, 1\n\t"
        "ds_read_b64_tr_b16 v[108:109], v124 offset:128\n\t"
        "ds_read_b64_tr_b16 v[110:111], v124 offset:3200\n\t"
        "s_waitcnt lgkmcnt(10)\n\t"
        "v_mfma_f32_32x32x16_bf16 v[64:79], v[144:147], v[112:115], v[64:79]\n\t"
        "s_cmp_eq_u32 s46, 110592\n\t"
        "s_cselect_b32 s46, 0, s46\n\t"
        "ds_read_b64_tr_b16 v[112:113], v123 offset:256\n\t"
        "ds_read_b64_tr_b16 v[114:115], v123 offset:3328\n\t"
        "s_waitcnt lgkmcnt(10)\n\t"
        "v_mfma_f32_32x32x16_bf16 v[80:95], v[144:147], v[116:119], v[80:95]\n\t"
        "ds_write_b32 v127, v128\n\t"
        "ds_read_b64_tr_b16 v[116:117], v124 offset:256\n\t"
        "ds_read_b64_tr_b16 v[118:119], v124 offset:3328\n\t"
        "s_waitcnt lgkmcnt(0)\n\t"
        "s_nop 7\n\t"
        "s_branch 9f\n\t"
        "10:\n\t"
        "s_mov_b32 s48, 0x10000\n\t"
        "8:\n\t"
        "ds_read_b32 v126, v125\n\t"
        "s_sub_u32 s48, s48, 1\n\t"
        "s_waitcnt lgkmcnt(0)\n\t"
        "s_cbranch_scc1 11b\n\t"
        "v_cmp_gt_u32 vcc, s47, v126\n\t"
        "s_cbranch_vccnz 8b\n\t"
        "s_branch 11b\n\t"
        "12:\n\t"
        "s_mov_b32 s48, 0x10000\n\t"
        "8:\n\t"
        "ds_read_b32 v126, v125\n\t"
        "s_sub_u32 s48, s48, 1\n\t"
        "s_waitcnt lgkmcnt(0)\n\t"
        "s_cbranch_scc1 13b\n\t"
        "v_cmp_gt_u32 vcc, s47, v126\n\t"
        "s_cbranch_vccnz 8b\n\t"
        "s_branch 13b\n\t"
        "14:\n\t"
        "s_mov_b32 s48, 0x10000\n\t"
        "8:\n\t"
        "ds_read_b32 v126, v125\n\t"
        "s_sub_u32 s48, s48, 1\n\t"
        "s_waitcnt lgkmcnt(0)\n\t"
        "s_cbranch_scc1 15b\n\t"
        "v_cmp_gt_u32 vcc, s47, v126\n\t"
        "s_cbranch_vccnz 8b\n\t"
        "s_branch 15b\n\t"
        "9:\n\t"
        : "={v[0:15]}"(acc[0]), "={v[16:31]}"(acc[1]), "={v[32:47]}"(acc[2]), "={v[48:63]}"(acc[3]), "={v[64:79]}"(acc[4]), "={v[80:95]}"(acc[5]),
          "={v[96:111]}"(stA), "+{v[112:127]}"(stB), "+{v[128:131]}"(stC), "={s46}"(slot), "+{s47}"(need)
        : "{v[132:147]}"(xr0) : "s48", "vcc", "scc", "memory");
      STAMPV(2 + 16 * L);
      float al = 1.f;
      if (L == 1) {
        PHASE_VARS;
        float S, Q, Dd;
        row_stats_sum(rs, OFF_SX, tile, lane, S, Q, Dd);
        float mean, var, mb1, rd1, mb2, den2;
        row_stats_fin(smem, 1, tile, lane, S, Q, Dd, U2F(words[0]), x1p, mean, var, mb1, rd1);
        ln_scalars(mean + (mean - mb1) * rd1, var * (1.0f + rd1) * (1.0f + rd1), mb2, den2);
        const float rd2 = RCP(den2);
        al = (1.0f + rd1) * rd2;
        const float be = -(mb1 * rd1 + mb2) * RCP(1.0f + rd1);
        reinterpret_cast<float*>(smem + L_AB)[wid * 64 + lane] = hi ? be : al;
        reinterpret_cast<float*>(smem + L_AB)[256 + wid * 64 + lane] = hi ? rd1 : mb1;
        STAMPV(36);
        lds_wait_ge<0>(lbase + L_W + 24, 2);
        soff = ((tn + 1) & 15) * 4096;
        asm volatile(
          "buffer_load_dwordx4 v[148:151], v120, s[40:43], s44 offen offset:0 sc1\n\t"
          "buffer_load_dwordx4 v[152:155], v120, s[40:43], s44 offen offset:1024 sc1\n\t"
          "buffer_load_dwordx4 v[156:159], v120, s[40:43], s44 offen offset:2048 sc1\n\t"
          "buffer_load_dwordx4 v[160:163], v120, s[40:43], s44 offen offset:3072 sc1\n\t"
          "s_add_u32 s44, s44, 0x1000\n\t"
          "s_and_b32 s44, s44, 0xffff\n\t"
          "buffer_load_dwordx4 v[164:167], v120, s[40:43], s44 offen offset:0 sc1\n\t"
          "buffer_load_dwordx4 v[168:171], v120, s[40:43], s44 offen offset:1024 sc1\n\t"
          "buffer_load_dwordx4 v[172:175], v120, s[40:43], s44 offen offset:2048 sc1\n\t"
          "buffer_load_dwordx4 v[176:179], v120, s[40:43], s44 offen offset:3072 sc1\n\t"
          "s_add_u32 s44, s44, 0x1000\n\t"
          "s_and_b32 s44, s44, 0xffff\n\t"
          "buffer_load_dwordx4 v[180:183], v120, s[40:43], s44 offen offset:0 sc1\n\t"
          "buffer_load_dwordx4 v[184:187], v120, s[40:43], s44 offen offset:1024 sc1\n\t"
          "buffer_load_dwordx4 v[188:191], v120, s[40:43], s44 offen offset:2048 sc1\n\t"
          "buffer_load_dwordx4 v[192:195], v120, s[40:43], s44 offen offset:3072 sc1\n\t"
          : "={v[148:163]}"(xr1), "={v[164:179]}"(xr2), "={v[180:195]}"(xr3), "+{s44}"(soff) : "{v[112:127]}"(stB), "{s[40:43]}"(srd) : "scc", "memory");
#pragma unroll
        for (int jj = 0; jj < 3; ++jj) {
          f32x4 cs[2][4];
          float cv[2];
#pragma unroll
          for (int j = 0; j < 2; ++j) {
            const int cb = jj * 64 + (j ^ hh) * 32;
            if (jj == 2) cv[j] = *reinterpret_cast<const float*>(smem + L_CS + (cb + r32) * 4);
#pragma unroll
            for (int g = 0; g < 4; ++g)
              if (jj < 2 || j == 0) cs[j][g] = *reinterpret_cast<const f32x4*>(smem + (jj < 2 ? L_CS + cb * 4 : L_AB + wid * 256 + 128) + (8 * g + 4 * hi) * 4);
          }
          SBAR();
#pragma unroll
          for (int j = 0; j < 2; ++j)
#pragma unroll
            for (int e = 0; e < 16; ++e) acc[2 * jj + j][e] += jj < 2 ? be * cs[j][e >> 2][e & 3] : cs[0][e >> 2][e & 3] * cv[j];
          SBAR();
        }
      }
      STAMPV(3 + 16 * L);
      soff = ((tn + 1 + 2) & 15) * 4096;
      asm volatile(
        "s_nop 0\n\t"
        "s_add_u32 s44, s44, 0x1000\n\t"
        "s_and_b32 s44, s44, 0xffff\n\t"
        "buffer_load_dwordx4 v[132:135], v120, s[40:43], s44 offen offset:0 sc1\n\t"
        "buffer_load_dwordx4 v[136:139], v120, s[40:43], s44 offen offset:1024 sc1\n\t"
        "buffer_load_dwordx4 v[140:143], v120, s[40:43], s44 offen offset:2048 sc1\n\t"
        "s_mov_b32 s45, 3\n\t"
        "1:\n\t"
        "s_waitcnt vmcnt(14) lgkmcnt(11)\n\t"
        "v_mfma_f32_32x32x16_bf16 v[0:15], v[96:99], v[148:151], v[0:15]\n\t"
        "ds_read_b32 v126, v125\n\t"
        "ds_read_b64_tr_b16 v[96:97], v123 offset:6144\n\t"
        "ds_read_b64_tr_b16 v[98:99], v123 offset:9216\n\t"
        "s_waitcnt lgkmcnt(12)\n\t"
        "v_mfma_f32_32x32x16_bf16 v[16:31], v[100:103], v[148:151], v[16:31]\n\t"
        "v_add_u32 v129, s46, v121\n\t"
        "ds_read_b64_tr_b16 v[100:101], v124 offset:6144\n\t"
        "ds_read_b64_tr_b16 v[102:103], v124 offset:9216\n\t"
        "s_waitcnt lgkmcnt(12)\n\t"
        "v_mfma_f32_32x32x16_bf16 v[32:47], v[104:107], v[148:151], v[32:47]\n\t"
        "ds_read_b64_tr_b16 v[104:105], v123 offset:6272\n\t"
        "ds_read_b64_tr_b16 v[106:107], v123 offset:9344\n\t"
        "buffer_load_dwordx4 v[144:147], v120, s[40:43], s44 offen offset:3072 sc1\n\t"
        "s_waitcnt lgkmcnt(12)\n\t"
        "v_mfma_f32_32x32x16_bf16 v[48:63], v[108:111], v[148:151], v[48:63]\n\t"
        "v_add_u32 v130, s46, v122\n\t"
        "ds_read_b64_tr_b16 v[108:109], v124 offset:6272\n\t"
        "ds_read_b64_tr_b16 v[110:111], v124 offset:9344\n\t"
        "s_waitcnt lgkmcnt(12)\n\t"
        "v_mfma_f32_32x32x16_bf16 v[64:79], v[148:151], v[112:115], v[64:79]\n\t"
        "ds_read_b64_tr_b16 v[112:113], v123 offset:6400\n\t"
        "ds_read_b64_tr_b16 v[114:115], v123 offset:9472\n\t"
        "s_waitcnt lgkmcnt(10)\n\t"
        "v_mfma_f32_32x32x16_bf16 v[80:95], v[148:151], v[116:119], v[80:95]\n\t"
        "v_cmp_gt_u32 vcc, s47, v126\n\t"
        "ds_read_b64_tr_b16 v[116:117], v124 offset:6400\n\t"
        "ds_read_b64_tr_b16 v[118:119], v124 offset:9472\n\t"
        "s_waitcnt vmcnt(14) lgkmcnt(10)\n\t"
        "v_mfma_f32_32x32x16_bf16 v[0:15], v[96:99], v[152:155], v[0:15]\n\t"
        "s_cbranch_vccnz 16f\n\t"
        "17:\n\t"
        "ds_read_b64_tr_b16 v[96:97], v129 offset:0\n\t"
        "ds_read_b64_tr_b16 v[98:99], v129 offset:3072\n\t"
        "s_waitcnt lgkmcnt(10)\n\t"
        "v_mfma_f32_32x32x16_bf16 v[16:31], v[100:103], v[152:155], v[16:31]\n\t"
        "v_add_u32 v128, 1, v128\n\t"
        "ds_read_b64_tr_b16 v[100:101], v130 offset:0\n\t"
        "ds_read_b64_tr_b16 v[102:103], v130 offset:3072\n\t"
        "s_waitcnt lgkmcnt(10)\n\t"
        "v_mfma_f32_32x32x16_bf16 v[32:47], v[104:107], v[152:155], v[32:47]\n\t"
        "ds_read_b64_tr_b16 v[104:105], v129 offset:128\n\t"
        "ds_read_b64_tr_b16 v[106:107], v129 offset:3200\n\t"
        "s_add_u32 s44, s44, 0x1000\n\t"
        "s_and_b32 s44, s44, 0xffff\n\t"
        "buffer_load_dwordx4 v[148:151], v120, s[40:43], s44 offen offset:0 sc1\n\t"
        "s_waitcnt lgkmcnt(10)\n\t"
        "v_mfma_f32_32x32x16_bf16 v[48:63], v[108:111], v[152:155], v[48:63]\n\t"
        "s_add_u32 s46, s46, 12288\n\t"
        "s_add_u32 s47, s47, 1\n\t"
        "ds_read_b64_tr_b16 v[108:109], v130 offset:128\n\t"
        "ds_read_b64_tr_b16 v[110:111], v130 offset:3200\n\t"
        "s_waitcnt lgkmcnt(10)\n\t"
        "v_mfma_f32_32x32x16_bf16 v[64:79], v[152:155], v[112:115], v[64:79]\n\t"
        "s_cmp_eq_u32 s46, 110592\n\t"
        "s_cselect_b32 s46, 0, s46\n\t"
        "ds_read_b64_tr_b16 v[112:113], v129 offset:256\n\t"
        "ds_read_b64_tr_b16 v[114:115], v129 offset:3328\n\t"
        "s_waitcnt lgkmcnt(10)\n\t"
        "v_mfma_f32_32x32x16_bf16 v[80:95], v[152:155], v[116:119], v[80:95]\n\t"
        "ds_write_b32 v127, v128\n\t"
        "ds_read_b64_tr_b16 v[116:117], v130 offset:256\n\t"
        "ds_read_b64_tr_b16 v[118:119], v130 offset:3328\n\t"
        "s_waitcnt vmcnt(14) lgkmcnt(11)\n\t"
        "v_mfma_f32_32x32x16_bf16 v[0:15], v[96:99], v[156:159], v[0:15]\n\t"
        "ds_read_b32 v126, v125\n\t"
        "ds_read_b64_tr_b16 v[96:97], v129 offset:6144\n\t"
        "ds_read_b64_tr_b16 v[98:99], v129 offset:9216\n\t"
        "s_waitcnt lgkmcnt(12)\n\t"
        "v_mfma_f32_32x32x16_bf16 v[16:31], v[100:103], v[156:159], v[16:31]\n\t"
        "v_add_u32 v123, s46, v121\n\t"
        "ds_read_b64_tr_b16 v[100:101], v130 offset:6144\n\t"
        "ds_read_b64_tr_b16 v[102:103], v130 offset:9216\n\t"
        "s_waitcnt lgkmcnt(12)\n\t"
        "v_mfma_f32_32x32x16_bf16 v[32:47], v[104:107], v[156:159], v[32:47]\n\t"
        "ds_read_b64_tr_b16 v[104:105], v129 offset:6272\n\t"
        "ds_read_b64_tr_b16 v[106:107], v129 offset:9344\n\t"
        "buffer_load_dwordx4 v[152:155], v120, s[40:43], s44 offen offset:1024 sc1\n\t"
        "s_waitcnt lgkmcnt(12)\n\t"
        "v_mfma_f32_32x32x16_bf16 v[48:63], v[108:111], v[156:159], v[48:63]\n\t"
        "v_add_u32 v124, s46, v122\n\t"
        "ds_read_b64_tr_b16 v[108:109], v130 offset:6272\n\t"
        "ds_read_b64_tr_b16 v[110:111], v130 offset:9344\n\t"
        "s_waitcnt lgkmcnt(12)\n\t"
        "v_mfma_f32_32x32x16_bf16 v[64:79], v[156:159], v[112:115], v[64:79]\n\t"
        "ds_read_b64_tr_b16 v[112:113], v129 offset:6400\n\t"
        "ds_read_b64_tr_b16 v[114:115], v129 offset:9472\n\t"
        "s_waitcnt lgkmcnt(10)\n\t"
        "v_mfma_f32_32x32x16_bf16 v[80:95], v[156:159], v[116:119], v[80:95]\n\t"
        "v_cmp_gt_u32 vcc, s47, v126\n\t"
        "ds_read_b64_tr_b16 v[116:117], v130 offset:6400\n\t"
        "ds_read_b64_tr_b16 v[118:119], v130 offset:9472\n\t"
        "s_waitcnt vmcnt(14) lgkmcnt(10)\n\t"
        "v_mfma_f32_32x32x16_bf16 v[0:15], v[96:99], v[160:163], v[0:15]\n\t"
        "s_cbranch_vccnz 18f\n\t"
        "19:\n\t"
        "ds_read_b64_tr_b16 v[96:97], v123 offset:0\n\t"
        "ds_read_b64_tr_b16 v[98:99], v123 offset:3072\n\t"
        "s_waitcnt lgkmcnt(10)\n\t"
        "v_mfma_f32_32x32x16_bf16 v[16:31], v[100:103], v[160:163], v[16:31]\n\t"
        "v_add_u32 v128, 1, v128\n\t"
        "ds_read_b64_tr_b16 v[100:101], v124 offset:0\n\t"
        "ds_read_b64_tr_b16 v[102:103], v124 offset:3072\n\t"
        "s_waitcnt lgkmcnt(10)\n\t"
        "v_mfma_f32_32x32x16_bf16 v[32:47], v[104:107], v[160:163], v[32:47]\n\t"
        "ds_read_b64_tr_b16 v[104:105], v123 offset:128\n\t"
        "ds_read_b64_tr_b16 v[106:107], v123 offset:3200\n\t"
        "buffer_load_dwordx4 v[156:159], v120, s[40:43], s44 offen offset:2048 sc1\n\t"
        "s_waitcnt lgkmcnt(10)\n\t"
        "v_mfma_f32_32x32x16_bf16 v[48:63], v[108:111], v[160:163], v[48:63]\n\t"
        "s_add_u32 s46, s46, 12288\n\t"
        "s_add_u32 s47, s47, 1\n\t"
        "ds_read_b64_tr_b16 v[108:109], v124 offset:128\n\t"
        "ds_read_b64_tr_b16 v[110:111], v124 offset:3200\n\t"
        "s_waitcnt lgkmcnt(10)\n\t"
        "v_mfma_f32_32x32x16_bf16 v[64:79], v[160:163], v[112:115], v[64:79]\n\t"
        "s_cmp_eq_u32 s46, 110592\n\t"
        "s_cselect_b32 s46, 0, s46\n\t"
        "ds_read_b64_tr_b16 v[112:113], v123 offset:256\n\t"
        "ds_read_b64_tr_b16 v[114:115], v123 offset:3328\n\t"
        "s_waitcnt lgkmcnt(10)\n\t"
        "v_mfma_f32_32x32x16_bf16 v[80:95], v[160:163], v[116:119], v[80:95]\n\t"
        "ds_write_b32 v127, v128\n\t"
        "ds_read_b64_tr_b16 v[116:117], v124 offset:256\n\t"
        "ds_read_b64_tr_b16 v[118:119], v124 offset:3328\n\t"
        "s_waitcnt vmcnt(14) lgkmcnt(11)\n\t"
        "v_mfma_f32_32x32x16_bf16 v[0:15], v[96:99], v[164:167], v[0:15]\n\t"
        "ds_read_b32 v126, v125\n\t"
        "ds_read_b64_tr_b16 v[96:97], v123 offset:6144\n\t"
        "ds_read_b64_tr_b16 v[98:99], v123 offset:9216\n\t"
        "s_waitcnt lgkmcnt(12)\n\t"
        "v_mfma_f32_32x32x16_bf16 v[16:31], v[100:103], v[164:167], v[16:31]\n\t"
        "v_add_u32 v129, s46, v121\n\t"
        "ds_read_b64_tr_b16 v[100:101], v124 offset:6144\n\t"
        "ds_read_b64_tr_b16 v[102:103], v124 offset:9216\n\t"
        "s_waitcnt lgkmcnt(12)\n\t"
        "v_mfma_f32_32x32x16_bf16 v[32:47], v[104:107], v[164:167], v[32:47]\n\t"
        "ds_read_b64_tr_b16 v[104:105], v123 offset:6272\n\t"
        "ds_read_b64_tr_b16 v[106:107], v123 offset:9344\n\t"
        "buffer_load_dwordx4 v[160:163], v120, s[40:43], s44 offen offset:3072 sc1\n\t"
        "s_waitcnt lgkmcnt(12)\n\t"
        "v_mfma_f32_32x32x16_bf16 v[48:63], v[108:111], v[164:167], v[48:63]\n\t"
        "v_add_u32 v130, s46, v122\n\t"
        "ds_read_b64_tr_b16 v[108:109], v124 offset:6272\n\t"
        "ds_read_b64_tr_b16 v[110:111], v124 offset:9344\n\t"
        "s_waitcnt lgkmcnt(12)\n\t"
        "v_mfma_f32_32x32x16_bf16 v[64:79], v[164:167], v[112:115], v[64:79]\n\t"
        "ds_read_b64_tr_b16 v[112:113], v123 offset:6400\n\t"
        "ds_read_b64_tr_b16 v[114:115], v123 offset:9472\n\t"
        "s_waitcnt lgkmcnt(10)\n\t"
        "v_mfma_f32_32x32x16_bf16 v[80:95], v[164:167], v[116:119], v[80:95]\n\t"
        "v_cmp_gt_u32 vcc, s47, v126\n\t"
        "ds_read_b64_tr_b16 v[116:117], v124 offset:6400\n\t"
        "ds_read_b64_tr_b16 v[118:119], v124 offset:9472\n\t"
        "s_waitcnt vmcnt(14) lgkmcnt(10)\n\t"
        "v_mfma_f32_32x32x16_bf16 v[0:15], v[96:99], v[168:171], v[0:15]\n\t"
        "s_cbranch_vccnz 20f\n\t"
        "21:\n\t"
        "ds_read_b64_tr_b16 v[96:97], v129 offset:0\n\t"
        "ds_read_b64_tr_b16 v[98:99], v129 offset:3072\n\t"
        "s_waitcnt lgkmcnt(10)\n\t"
        "v_mfma_f32_32x32x16_bf16 v[16:31], v[100:103], v[168:171], v[16:31]\n\t"
        "v_add_u32 v128, 1, v128\n\t"
        "ds_read_b64_tr_b16 v[100:101], v130 offset:0\n\t"
        "ds_read_b64_tr_b16 v[102:103], v130 offset:3072\n\t"
        "s_waitcnt lgkmcnt(10)\n\t"
        "v_mfma_f32_32x32x16_bf16 v[32:47], v[104:107], v[168:171], v[32:47]\n\t"
        "ds_read_b64_tr_b16 v[104:105], v129 offset:128\n\t"
        "ds_read_b64_tr_b16 v[106:107], v129 offset:3200\n\t"
        "s_add_u32 s44, s44, 0x1000\n\t"
        "s_and_b32 s44, s44, 0xffff\n\t"
        "buffer_load_dwordx4 v[164:167], v120, s[40:43], s44 offen offset:0 sc1\n\t"
        "s_waitcnt lgkmcnt(10)\n\t"
        "v_mfma_f32_32x32x16_bf16 v[48:63], v[108:111], v[168:171], v[48:63]\n\t"
        "s_add_u32 s46, s46, 12288\n\t"
        "s_add_u32 s47, s47, 1\n\t"
        "ds_read_b64_tr_b16 v[108:109], v130 offset:128\n\t"
        "ds_read_b64_tr_b16 v[110:111], v130 offset:3200\n\t"
        "s_waitcnt lgkmcnt(10)\n\t"
        "v_mfma_f32_32x32x16_bf16 v[64:79], v[168:171], v[112:115], v[64:79]\n\t"
        "s_cmp_eq_u32 s46, 110592\n\t"
        "s_cselect_b32 s46, 0, s46\n\t"
        "ds_read_b64_tr_b16 v[112:113], v129 offset:256\n\t"
        "ds_read_b64_tr_b16 v[114:115], v129 offset:3328\n\t"
        "s_waitcnt lgkmcnt(10)\n\t"
        "v_mfma_f32_32x32x16_bf16 v[80:95], v[168:171], v[116:119], v[80:95]\n\t"
        "ds_write_b32 v127, v128\n\t"
        "ds_read_b64_tr_b16 v[116:117], v130 offset:256\n\t"
        "ds_read_b64_tr_b16 v[118:119], v130 offset:3328\n\t"
        "s_waitcnt vmcnt(14) lgkmcnt(11)\n\t"
        "v_mfma_f32_32x32x16_bf16 v[0:15], v[96:99], v[172:175], v[0:15]\n\t"
        "ds_read_b32 v126, v125\n\t"
        "ds_read_b64_tr_b16 v[96:97], v129 offset:6144\n\t"
        "ds_read_b64_tr_b16 v[98:99], v129 offset:9216\n\t"
        "s_waitcnt lgkmcnt(12)\n\t"
        "v_mfma_f32_32x32x16_bf16 v[16:31], v[100:103], v[172:175], v[16:31]\n\t"
        "v_add_u32 v123, s46, v121\n\t"
        "ds_read_b64_tr_b16 v[100:101], v130 offset:6144\n\t"
        "ds_read_b64_tr_b16 v[102:103], v130 offset:9216\n\t"
        "s_waitcnt lgkmcnt(12)\n\t"
        "v_mfma_f32_32x32x16_bf16 v[32:47], v[104:107], v[172:175], v[32:47]\n\t"
        "ds_read_b64_tr_b16 v[104:105], v129 offset:6272\n\t"
        "ds_read_b64_tr_b16 v[106:107], v129 offset:9344\n\t"
        "buffer_load_dwordx4 v[168:171], v120, s[40:43], s44 offen offset:1024 sc1\n\t"
        "s_waitcnt lgkmcnt(12)\n\t"
        "v_mfma_f32_32x32x16_bf16 v[48:63], v[108:111], v[172:175], v[48:63]\n\t"
        "v_add_u32 v124, s46, v122\n\t"
        "ds_read_b64_tr_b16 v[108:109], v130 offset:6272\n\t"
        "ds_read_b64_tr_b16 v[110:111], v130 offset:9344\n\t"
        "s_waitcnt lgkmcnt(12)\n\t"
        "v_mfma_f32_32x32x16_bf16 v[64:79], v[172:175], v[112:115], v[64:79]\n\t"
        "ds_read_b64_tr_b16 v[112:113], v129 offset:6400\n\t"
        "ds_read_b64_tr_b16 v[114:115], v129 offset:9472\n\t"
        "s_waitcnt lgkmcnt(10)\n\t"
        "v_mfma_f32_32x32x16_bf16 v[80:95], v[172:175], v[116:119], v[80:95]\n\t"
        "v_cmp_gt_u32 vcc, s47, v126\n\t"
        "ds_read_b64_tr_b16 v[116:117], v130 offset:6400\n\t"
        "ds_read_b64_tr_b16 v[118:119], v130 offset:9472\n\t"
        "s_waitcnt vmcnt(14) lgkmcnt(10)\n\t"
        "v_mfma_f32_32x32x16_bf16 v[0:15], v[96:99], v[176:179], v[0:15]\n\t"
        "s_cbranch_vccnz 22f\n\t"
        "23:\n\t"
        "ds_read_b64_tr_b16 v[96:97], v123 offset:0\n\t"
        "ds_read_b64_tr_b16 v[98:99], v123 offset:3072\n\t"
        "s_waitcnt lgkmcnt(10)\n\t"
        "v_mfma_f32_32x32x16_bf16 v[16:31], v[100:103], v[176:179], v[16:31]\n\t"
        "v_add_u32 v128, 1, v128\n\t"
        "ds_read_b64_tr_b16 v[100:101], v124 offset:0\n\t"
        "ds_read_b64_tr_b16 v[102:103], v124 offset:3072\n\t"
        "s_waitcnt lgkmcnt(10)\n\t"
        "v_mfma_f32_32x32x16_bf16 v[32:47], v[104:107], v[176:179], v[32:47]\n\t"
        "ds_read_b64_tr_b16 v[104:105], v123 offset:128\n\t"
        "ds_read_b64_tr_b16 v[106:107], v123 offset:3200\n\t"
        "buffer_load_dwordx4 v[172:175], v120, s[40:43], s44 offen offset:2048 sc1\n\t"
        "s_waitcnt lgkmcnt(10)\n\t"
        "v_mfma_f32_32x32x16_bf16 v[48:63], v[108:111], v[176:179], v[48:63]\n\t"
        "s_add_u32 s46, s46, 12288\n\t"
        "s_add_u32 s47, s47, 1\n\t"
        "ds_read_b64_tr_b16 v[108:109], v124 offset:128\n\t"
        "ds_read_b64_tr_b16 v[110:111], v124 offset:3200\n\t"
        "s_waitcnt lgkmcnt(10)\n\t"
        "v_mfma_f32_32x32x16_bf16 v[64:79], v[176:179], v[112:115], v[64:79]\n\t"
        "s_cmp_eq_u32 s46, 110592\n\t"
        "s_cselect_b32 s46, 0, s46\n\t"
        "ds_read_b64_tr_b16 v[112:113], v123 offset:256\n\t"
        "ds_read_b64_tr_b16 v[114:115], v123 offset:3328\n\t"
        "s_waitcnt lgkmcnt(10)\n\t"
        "v_mfma_f32_32x32x16_bf16 v[80:95], v[176:179], v[116:119], v[80:95]\n\t"
        "ds_write_b32 v127, v128\n\t"
        "ds_read_b64_tr_b16 v[116:117], v124 offset:256\n\t"
        "ds_read_b64_tr_b16 v[118:119], v124 offset:3328\n\t"
        "s_waitcnt vmcnt(14) lgkmcnt(11)\n\t"
        "v_mfma_f32_32x32x16_bf16 v[0:15], v[96:99], v[180:183], v[0:15]\n\t"
        "ds_read_b32 v126, v125\n\t"
        "ds_read_b64_tr_b16 v[96:97], v123 offset:6144\n\t"
        "ds_read_b64_tr_b16 v[98:99], v123 offset:9216\n\t"
        "s_waitcnt lgkmcnt(12)\n\t"
        "v_mfma_f32_32x32x16_bf16 v[16:31], v[100:103], v[180:183], v[16:31]\n\t"
        "v_add_u32 v129, s46, v121\n\t"
        "ds_read_b64_tr_b16 v[100:101], v124 offset:6144\n\t"
        "ds_read_b64_tr_b16 v[102:103], v124 offset:9216\n\t"
        "s_waitcnt lgkmcnt(12)\n\t"
        "v_mfma_f32_32x32x16_bf16 v[32:47], v[104:107], v[180:183], v[32:47]\n\t"
        "ds_read_b64_tr_b16 v[104:105], v123 offset:6272\n\t"
        "ds_read_b64_tr_b16 v[106:107], v123 offset:9344\n\t"
        "buffer_load_dwordx4 v[176:179], v120, s[40:43], s44 offen offset:3072 sc1\n\t"
        "s_waitcnt lgkmcnt(12)\n\t"
        "v_mfma_f32_32x32x16_bf16 v[48:63], v[108:111], v[180:183], v[48:63]\n\t"
        "v_add_u32 v130, s46, v122\n\t"
        "ds_read_b64_tr_b16 v[108:109], v124 offset:6272\n\t"
        "ds_read_b64_tr_b16 v[110:111], v124 offset:9344\n\t"
        "s_waitcnt lgkmcnt(12)\n\t"
        "v_mfma_f32_32x32x16_bf16 v[64:79], v[180:183], v[112:115], v[64:79]\n\t"
        "ds_read_b64_tr_b16 v[112:113], v123 offset:6400\n\t"
        "ds_read_b64_tr_b16 v[114:115], v123 offset:9472\n\t"
        "s_waitcnt lgkmcnt(10)\n\t"
        "v_mfma_f32_32x32x16_bf16 v[80:95], v[180:183], v[116:119], v[80:95]\n\t"
        "v_cmp_gt_u32 vcc, s47, v126\n\t"
        "ds_read_b64_tr_b16 v[116:117], v124 offset:6400\n\t"
        "ds_read_b64_tr_b16 v[118:119], v124 offset:9472\n\t"
        "s_waitcnt vmcnt(14) lgkmcnt(10)\n\t"
        "v_mfma_f32_32x32x16_bf16 v[0:15], v[96:99], v[184:187], v[0:15]\n\t"
        "s_cbranch_vccnz 24f\n\t"
        "25:\n\t"
        "ds_read_b64_tr_b16 v[96:97], v129 offset:0\n\t"
        "ds_read_b64_tr_b16 v[98:99], v129 offset:3072\n\t"
        "s_waitcnt lgkmcnt(10)\n\t"
        "v_mfma_f32_32x32x16_bf16 v[16:31], v[100:103], v[184:187], v[16:31]\n\t"
        "v_add_u32 v128, 1, v128\n\t"
        "ds_read_b64_tr_b16 v[100:101], v130 offset:0\n\t"
        "ds_read_b64_tr_b16 v[102:103], v130 offset:3072\n\t"
        "s_waitcnt lgkmcnt(10)\n\t"
        "v_mfma_f32_32x32x16_bf16 v[32:47], v[104:107], v[184:187], v[32:47]\n\t"
        "ds_read_b64_tr_b16 v[104:105], v129 offset:128\n\t"
        "ds_read_b64_tr_b16 v[106:107], v129 offset:3200\n\t"
        "s_add_u32 s44, s44, 0x1000\n\t"
        "s_and_b32 s44, s44, 0xffff\n\t"
        "buffer_load_dwordx4 v[180:183], v120, s[40:43], s44 offen offset:0 sc1\n\t"
        "s_waitcnt lgkmcnt(10)\n\t"
        "v_mfma_f32_32x32x16_bf16 v[48:63], v[108:111], v[184:187], v[48:63]\n\t"
        "s_add_u32 s46, s46, 12288\n\t"
        "s_add_u32 s47, s47, 1\n\t"
        "ds_read_b64_tr_b16 v[108:109], v130 offset:128\n\t"
        "ds_read_b64_tr_b16 v[110:111], v130 offset:3200\n\t"
        "s_waitcnt lgkmcnt(10)\n\t"
        "v_mfma_f32_32x32x16_bf16 v[64:79], v[184:187], v[112:115], v[64:79]\n\t"
        "s_cmp_eq_u32 s46, 110592\n\t"
        "s_cselect_b32 s46, 0, s46\n\t"
        "ds_read_b64_tr_b16 v[112:113], v129 offset:256\n\t"
        "ds_read_b64_tr_b16 v[114:115], v129 offset:3328\n\t"
        "s_waitcnt lgkmcnt(10)\n\t"
        "v_mfma_f32_32x32x16_bf16 v[80:95], v[184:187], v[116:119], v[80:95]\n\t"
        "ds_write_b32 v127, v128\n\t"
        "ds_read_b64_tr_b16 v[116:117], v130 offset:256\n\t"
        "ds_read_b64_tr_b16 v[118:119], v130 offset:3328\n\t"
        "s_waitcnt vmcnt(14) lgkmcnt(11)\n\t"
        "v_mfma_f32_32x32x16_bf16 v[0:15], v[96:99], v[188:191], v[0:15]\n\t"
        "ds_read_b32 v126, v125\n\t"
        "ds_read_b64_tr_b16 v[96:97], v129 offset:6144\n\t"
        "ds_read_b64_tr_b16 v[98:99], v129 offset:9216\n\t"
        "s_waitcnt lgkmcnt(12)\n\t"
        "v_mfma_f32_32x32x16_bf16 v[16:31], v[100:103], v[188:191], v[16:31]\n\t"
        "v_add_u32 v123, s46, v121\n\t"
        "ds_read_b64_tr_b16 v[100:101], v130 offset:6144\n\t"
        "ds_read_b64_tr_b16 v[102:103], v130 offset:9216\n\t"
        "s_waitcnt lgkmcnt(12)\n\t"
        "v_mfma_f32_32x32x16_bf16 v[32:47], v[104:107], v[188:191], v[32:47]\n\t"
        "ds_read_b64_tr_b16 v[104:105], v129 offset:6272\n\t"
        "ds_read_b64_tr_b16 v[106:107], v129 offset:9344\n\t"
        "buffer_load_dwordx4 v[184:187], v120, s[40:43], s44 offen offset:1024 sc1\n\t"
        "s_waitcnt lgkmcnt(12)\n\t"
        "v_mfma_f32_32x32x16_bf16 v[48:63], v[108:111], v[188:191], v[48:63]\n\t"
        "v_add_u32 v124, s46, v122\n\t"
        "ds_read_b64_tr_b16 v[108:109], v130 offset:6272\n\t"
        "ds_read_b64_tr_b16 v[110:111], v130 offset:9344\n\t"
        "s_waitcnt lgkmcnt(12)\n\t"
        "v_mfma_f32_32x32x16_bf16 v[64:79], v[188:191], v[112:115], v[64:79]\n\t"
        "ds_read_b64_tr_b16 v[112:113], v129 offset:6400\n\t"
        "ds_read_b64_tr_b16 v[114:115], v129 offset:9472\n\t"
        "s_waitcnt lgkmcnt(10)\n\t"
        "v_mfma_f32_32x32x16_bf16 v[80:95], v[188:191], v[116:119], v[80:95]\n\t"
        "v_cmp_gt_u32 vcc, s47, v126\n\t"
        "ds_read_b64_tr_b16 v[116:117], v130 offset:6400\n\t"
        "ds_read_b64_tr_b16 v[118:119], v130 offset:9472\n\t"
        "s_waitcnt vmcnt(14) lgkmcnt(10)\n\t"
        "v_mfma_f32_32x32x16_bf16 v[0:15], v[96:99], v[192:195], v[0:15]\n\t"
        "s_cbranch_vccnz 26f\n\t"
        "27:\n\t"
        "ds_read_b64_tr_b16 v[96:97], v123 offset:0\n\t"
        "ds_read_b64_tr_b16 v[98:99], v123 offset:3072\n\t"
        "s_waitcnt lgkmcnt(10)\n\t"
        "v_mfma_f32_32x32x16_bf16 v[16:31], v[100:103], v[192:195], v[16:31]\n\t"
        "v_add_u32 v128, 1, v128\n\t"
        "ds_read_b64_tr_b16 v[100:101], v124 offset:0\n\t"
        "ds_read_b64_tr_b16 v[102:103], v124 offset:3072\n\t"
        "s_waitcnt lgkmcnt(10)\n\t"
        "v_mfma_f32_32x32x16_bf16 v[32:47], v[104:107], v[192:195], v[32:47]\n\t"
        "ds_read_b64_tr_b16 v[104:105], v123 offset:128\n\t"
        "ds_read_b64_tr_b16 v[106:107], v123 offset:3200\n\t"
        "buffer_load_dwordx4 v[188:191], v120, s[40:43], s44 offen offset:2048 sc1\n\t"
        "s_waitcnt lgkmcnt(10)\n\t"
        "v_mfma_f32_32x32x16_bf16 v[48:63], v[108:111], v[192:195], v[48:63]\n\t"
        "s_add_u32 s46, s46, 12288\n\t"
        "s_add_u32 s47, s47, 1\n\t"
        "ds_read_b64_tr_b16 v[108:109], v124 offset:128\n\t"
        "ds_read_b64_tr_b16 v[110:111], v124 offset:3200\n\t"
        "s_waitcnt lgkmcnt(10)\n\t"
        "v_mfma_f32_32x32x16_bf16 v[64:79], v[192:195], v[112:115], v[64:79]\n\t"
        "s_cmp_eq_u32 s46, 110592\n\t"
        "s_cselect_b32 s46, 0, s46\n\t"
        "ds_read_b64_tr_b16 v[112:113], v123 offset:256\n\t"
        "ds_read_b64_tr_b16 v[114:115], v123 offset:3328\n\t"
        "s_waitcnt lgkmcnt(10)\n\t"
        "v_mfma_f32_32x32x16_bf16 v[80:95], v[192:195], v[116:119], v[80:95]\n\t"
        "ds_write_b32 v127, v128\n\t"
        "ds_read_b64_tr_b16 v[116:117], v124 offset:256\n\t"
        "ds_read_b64_tr_b16 v[118:119], v124 offset:3328\n\t"
        "s_waitcnt vmcnt(14) lgkmcnt(11)\n\t"
        "v_mfma_f32_32x32x16_bf16 v[0:15], v[96:99], v[132:135], v[0:15]\n\t"
        "ds_read_b32 v126, v125\n\t"
        "ds_read_b64_tr_b16 v[96:97], v123 offset:6144\n\t"
        "ds_read_b64_tr_b16 v[98:99], v123 offset:9216\n\t"
        "s_waitcnt lgkmcnt(12)\n\t"
        "v_mfma_f32_32x32x16_bf16 v[16:31], v[100:103], v[132:135], v[16:31]\n\t"
        "v_add_u32 v129, s46, v121\n\t"
        "ds_read_b64_tr_b16 v[100:101], v124 offset:6144\n\t"
        "ds_read_b64_tr_b16 v[102:103], v124 offset:9216\n\t"
        "s_waitcnt lgkmcnt(12)\n\t"
        "v_mfma_f32_32x32x16_bf16 v[32:47], v[104:107], v[132:135], v[32:47]\n\t"
        "ds_read_b64_tr_b16 v[104:105], v123 offset:6272\n\t"
        "ds_read_b64_tr_b16 v[106:107], v123 offset:9344\n\t"
        "buffer_load_dwordx4 v[192:195], v120, s[40:43], s44 offen offset:3072 sc1\n\t"
        "s_waitcnt lgkmcnt(12)\n\t"
        "v_mfma_f32_32x32x16_bf16 v[48:63], v[108:111], v[132:135], v[48:63]\n\t"
        "v_add_u32 v130, s46, v122\n\t"
        "ds_read_b64_tr_b16 v[108:109], v124 offset:6272\n\t"
        "ds_read_b64_tr_b16 v[110:111], v124 offset:9344\n\t"
        "s_waitcnt lgkmcnt(12)\n\t"
        "v_mfma_f32_32x32x16_bf16 v[64:79], v[132:135], v[112:115], v[64:79]\n\t"
        "ds_read_b64_tr_b16 v[112:113], v123 offset:6400\n\t"
        "ds_read_b64_tr_b16 v[114:115], v123 offset:9472\n\t"
        "s_waitcnt lgkmcnt(10)\n\t"
        "v_mfma_f32_32x32x16_bf16 v[80:95], v[132:135], v[116:119], v[80:95]\n\t"
        "v_cmp_gt_u32 vcc, s47, v126\n\t"
        "ds_read_b64_tr_b16 v[116:117], v124 offset:6400\n\t"
        "ds_read_b64_tr_b16 v[118:119], v124 offset:9472\n\t"
        "s_waitcnt vmcnt(14) lgkmcnt(10)\n\t"
        "v_mfma_f32_32x32x16_bf16 v[0:15], v[96:99], v[136:139], v[0:15]\n\t"
        "s_cbranch_vccnz 28f\n\t"
        "29:\n\t"
        "ds_read_b64_tr_b16 v[96:97], v129 offset:0\n\t"
        "ds_read_b64_tr_b16 v[98:99], v129 offset:3072\n\t"
        "s_waitcnt lgkmcnt(10)\n\t"
        "v_mfma_f32_32x32x16_bf16 v[16:31], v[100:103], v[136:139], v[16:31]\n\t"
        "v_add_u32 v128, 1, v128\n\t"
        "ds_read_b64_tr_b16 v[100:101], v130 offset:0\n\t"
        "ds_read_b64_tr_b16 v[102:103], v130 offset:3072\n\t"
        "s_waitcnt lgkmcnt(10)\n\t"
        "v_mfma_f32_32x32x16_bf16 v[32:47], v[104:107], v[136:139], v[32:47]\n\t"
        "ds_read_b64_tr_b16 v[104:105], v129 offset:128\n\t"
        "ds_read_b64_tr_b16 v[106:107], v129 offset:3200\n\t"
        "s_add_u32 s44, s44, 0x1000\n\t"
        "s_and_b32 s44, s44, 0xffff\n\t"
        "buffer_load_dwordx4 v[132:135], v120, s[40:43], s44 offen offset:0 sc1\n\t"
        "s_waitcnt lgkmcnt(10)\n\t"
        "v_mfma_f32_32x32x16_bf16 v[48:63], v[108:111], v[136:139], v[48:63]\n\t"
        "s_add_u32 s46, s46, 12288\n\t"
        "s_add_u32 s47, s47, 1\n\t"
        "ds_read_b64_tr_b16 v[108:109], v130 offset:128\n\t"
        "ds_read_b64_tr_b16 v[110:111], v130 offset:3200\n\t"
        "s_waitcnt lgkmcnt(10)\n\t"
        "v_mfma_f32_32x32x16_bf16 v[64:79], v[136:139], v[112:115], v[64:79]\n\t"
        "s_cmp_eq_u32 s46, 110592\n\t"
        "s_cselect_b32 s46, 0, s46\n\t"
        "ds_read_b64_tr_b16 v[112:113], v129 offset:256\n\t"
        "ds_read_b64_tr_b16 v[114:115], v129 offset:3328\n\t"
        "s_waitcnt lgkmcnt(10)\n\t"
        "v_mfma_f32_32x32x16_bf16 v[80:95], v[136:139], v[116:119], v[80:95]\n\t"
        "ds_write_b32 v127, v128\n\t"
        "ds_read_b64_tr_b16 v[116:117], v130 offset:256\n\t"
        "ds_read_b64_tr_b16 v[118:119], v130 offset:3328\n\t"
        "s_waitcnt vmcnt(14) lgkmcnt(11)\n\t"
        "v_mfma_f32_32x32x16_bf16 v[0:15], v[96:99], v[140:143], v[0:15]\n\t"
        "ds_read_b32 v126, v125\n\t"
        "ds_read_b64_tr_b16 v[96:97], v129 offset:6144\n\t"
        "ds_read_b64_tr_b16 v[98:99], v129 offset:9216\n\t"
        "s_waitcnt lgkmcnt(12)\n\t"
        "v_mfma_f32_32x32x16_bf16 v[16:31], v[100:103], v[140:143], v[16:31]\n\t"
        "v_add_u32 v123, s46, v121\n\t"
        "ds_read_b64_tr_b16 v[100:101], v130 offset:6144\n\t"
        "ds_read_b64_tr_b16 v[102:103], v130 offset:9216\n\t"
        "s_waitcnt lgkmcnt(12)\n\t"
        "v_mfma_f32_32x32x16_bf16 v[32:47], v[104:107], v[140:143], v[32:47]\n\t"
        "ds_read_b64_tr_b16 v[104:105], v129 offset:6272\n\t"
        "ds_read_b64_tr_b16 v[106:107], v129 offset:9344\n\t"
        "buffer_load_dwordx4 v[136:139], v120, s[40:43], s44 offen offset:1024 sc1\n\t"
        "s_waitcnt lgkmcnt(12)\n\t"
        "v_mfma_f32_32x32x16_bf16 v[48:63], v[108:111], v[140:143], v[48:63]\n\t"
        "v_add_u32 v124, s46, v122\n\t"
        "ds_read_b64_tr_b16 v[108:109], v130 offset:6272\n\t"
        "ds_read_b64_tr_b16 v[110:111], v130 offset:9344\n\t"
        "s_waitcnt lgkmcnt(12)\n\t"
        "v_mfma_f32_32x32x16_bf16 v[64:79], v[140:143], v[112:115], v[64:79]\n\t"
        "ds_read_b64_tr_b16 v[112:113], v129 offset:6400\n\t"
        "ds_read_b64_tr_b16 v[114:115], v129 offset:9472\n\t"
        "s_waitcnt lgkmcnt(10)\n\t"
        "v_mfma_f32_32x32x16_bf16 v[80:95], v[140:143], v[116:119], v[80:95]\n\t"
        "v_cmp_gt_u32 vcc, s47, v126\n\t"
        "ds_read_b64_tr_b16 v[116:117], v130 offset:6400\n\t"
        "ds_read_b64_tr_b16 v[118:119], v130 offset:9472\n\t"
        "s_waitcnt vmcnt(14) lgkmcnt(10)\n\t"
        "v_mfma_f32_32x32x16_bf16 v[0:15], v[96:99], v[144:147], v[0:15]\n\t"
        "s_cbranch_vccnz 30f\n\t"
        "31:\n\t"
        "ds_read_b64_tr_b16 v[96:97], v123 offset:0\n\t"
        "ds_read_b64_tr_b16 v[98:99], v123 offset:3072\n\t"
        "s_waitcnt lgkmcnt(10)\n\t"
        "v_mfma_f32_32x32x16_bf16 v[16:31], v[100:103], v[144:147], v[16:31]\n\t"
        "v_add_u32 v128, 1, v128\n\t"
        "ds_read_b64_tr_b16 v[100:101], v124 offset:0\n\t"
        "ds_read_b64_tr_b16 v[102:103], v124 offset:3072\n\t"
        "s_waitcnt lgkmcnt(10)\n\t"
        "v_mfma_f32_32x32x16_bf16 v[32:47], v[104:107], v[144:147], v[32:47]\n\t"
        "ds_read_b64_tr_b16 v[104:105], v123 offset:128\n\t"
        "ds_read_b64_tr_b16 v[106:107], v123 offset:3200\n\t"
        "buffer_load_dwordx4 v[140:143], v120, s[40:43], s44 offen offset:2048 sc1\n\t"
        "s_waitcnt lgkmcnt(10)\n\t"
        "v_mfma_f32_32x32x16_bf16 v[48:63], v[108:111], v[144:147], v[48:63]\n\t"
        "s_add_u32 s46, s46, 12288\n\t"
        "s_add_u32 s47, s47, 1\n\t"
        "ds_read_b64_tr_b16 v[108:109], v124 offset:128\n\t"
        "ds_read_b64_tr_b16 v[110:111], v124 offset:3200\n\t"
        "s_waitcnt lgkmcnt(10)\n\t"
        "v_mfma_f32_32x32x16_bf16 v[64:79], v[144:147], v[112:115], v[64:79]\n\t"
        "s_cmp_eq_u32 s46, 110592\n\t"
        "s_cselect_b32 s46, 0, s46\n\t"
        "ds_read_b64_tr_b16 v[112:113], v123 offset:256\n\t"
        "ds_read_b64_tr_b16 v[114:115], v123 offset:3328\n\t"
        "s_waitcnt lgkmcnt(10)\n\t"
        "v_mfma_f32_32x32x16_bf16 v[80:95], v[144:147], v[116:119], v[80:95]\n\t"
        "ds_write_b32 v127, v128\n\t"
        "ds_read_b64_tr_b16 v[116:117], v124 offset:256\n\t"
        "ds_read_b64_tr_b16 v[118:119], v124 offset:3328\n\t"
        "s_sub_u32 s45, s45, 1\n\t"
        "s_cmp_lg_u32 s45, 0\n\t"
        "s_cbranch_scc1 1b\n\t"
        "s_waitcnt vmcnt(14) lgkmcnt(11)\n\t"
        "v_mfma_f32_32x32x16_bf16 v[0:15], v[96:99], v[148:151], v[0:15]\n\t"
        "ds_read_b32 v126, v125\n\t"
        "ds_read_b64_tr_b16 v[96:97], v123 offset:6144\n\t"
        "ds_read_b64_tr_b16 v[98:99], v123 offset:9216\n\t"
        "s_waitcnt lgkmcnt(12)\n\t"
        "v_mfma_f32_32x32x16_bf16 v[16:31], v[100:103], v[148:151], v[16:31]\n\t"
        "v_add_u32 v129, s46, v121\n\t"
        "ds_read_b64_tr_b16 v[100:101], v124 offset:6144\n\t"
        "ds_read_b64_tr_b16 v[102:103], v124 offset:9216\n\t"
        "s_waitcnt lgkmcnt(12)\n\t"
        "v_mfma_f32_32x32x16_bf16 v[32:47], v[104:107], v[148:151], v[32:47]\n\t"
        "ds_read_b64_tr_b16 v[104:105], v123 offset:6272\n\t"
        "ds_read_b64_tr_b16 v[106:107], v123 offset:9344\n\t"
        "s_waitcnt lgkmcnt(12)\n\t"
        "v_mfma_f32_32x32x16_bf16 v[48:63], v[108:111], v[148:151], v[48:63]\n\t"
        "v_add_u32 v130, s46, v122\n\t"
        "ds_read_b64_tr_b16 v[108:109], v124 offset:6272\n\t"
        "ds_read_b64_tr_b16 v[110:111], v124 offset:9344\n\t"
        "s_waitcnt lgkmcnt(12)\n\t"
        "v_mfma_f32_32x32x16_bf16 v[64:79], v[148:151], v[112:115], v[64:79]\n\t"
        "ds_read_b64_tr_b16 v[112:113], v123 offset:6400\n\t"
        "ds_read_b64_tr_b16 v[114:115], v123 offset:9472\n\t"
        "s_waitcnt lgkmcnt(10)\n\t"
        "v_mfma_f32_32x32x16_bf16 v[80:95], v[148:151], v[116:119], v[80:95]\n\t"
        "v_cmp_gt_u32 vcc, s47, v126\n\t"
        "ds_read_b64_tr_b16 v[116:117], v124 offset:6400\n\t"
        "ds_read_b64_tr_b16 v[118:119], v124 offset:9472\n\t"
        "s_waitcnt vmcnt(13) lgkmcnt(10)\n\t"
        "v_mfma_f32_32x32x16_bf16 v[0:15], v[96:99], v[152:155], v[0:15]\n\t"
        "s_cbranch_vccnz 64f\n\t"
        "65:\n\t"
        "ds_read_b64_tr_b16 v[96:97], v129 offset:0\n\t"
        "ds_read_b64_tr_b16 v[98:99], v129 offset:3072\n\t"
        "s_waitcnt lgkmcnt(10)\n\t"
        "v_mfma_f32_32x32x16_bf16 v[16:31], v[100:103], v[152:155], v[16:31]\n\t"
        "v_add_u32 v128, 1, v128\n\t"
        "ds_read_b64_tr_b16 v[100:101], v130 offset:0\n\t"
        "ds_read_b64_tr_b16 v[102:103], v130 offset:3072\n\t"
        "s_waitcnt lgkmcnt(10)\n\t"
        "v_mfma_f32_32x32x16_bf16 v[32:47], v[104:107], v[152:155], v[32:47]\n\t"
        "ds_read_b64_tr_b16 v[104:105], v129 offset:128\n\t"
        "ds_read_b64_tr_b16 v[106:107], v129 offset:3200\n\t"
        "s_waitcnt lgkmcnt(10)\n\t"
        "v_mfma_f32_32x32x16_bf16 v[48:63], v[108:111], v[152:155], v[48:63]\n\t"
        "s_add_u32 s46, s46, 12288\n\t"
        "s_add_u32 s47, s47, 1\n\t"
        "ds_read_b64_tr_b16 v[108:109], v130 offset:128\n\t"
        "ds_read_b64_tr_b16 v[110:111], v130 offset:3200\n\t"
        "s_waitcnt lgkmcnt(10)\n\t"
        "v_mfma_f32_32x32x16_bf16 v[64:79], v[152:155], v[112:115], v[64:79]\n\t"
        "s_cmp_eq_u32 s46, 110592\n\t"
        "s_cselect_b32 s46, 0, s46\n\t"
        "ds_read_b64_tr_b16 v[112:113], v129 offset:256\n\t"
        "ds_read_b64_tr_b16 v[114:115], v129 offset:3328\n\t"
        "s_waitcnt lgkmcnt(10)\n\t"
        "v_mfma_f32_32x32x16_bf16 v[80:95], v[152:155], v[116:119], v[80:95]\n\t"
        "ds_write_b32 v127, v128\n\t"
        "ds_read_b64_tr_b16 v[116:117], v130 offset:256\n\t"
        "ds_read_b64_tr_b16 v[118:119], v130 offset:3328\n\t"
        "s_waitcnt vmcnt(12) lgkmcnt(11)\n\t"
        "v_mfma_f32_32x32x16_bf16 v[0:15], v[96:99], v[156:159], v[0:15]\n\t"
        "ds_read_b32 v126, v125\n\t"
        "ds_read_b64_tr_b16 v[96:97], v129 offset:6144\n\t"
        "ds_read_b64_tr_b16 v[98:99], v129 offset:9216\n\t"
        "s_waitcnt lgkmcnt(12)\n\t"
        "v_mfma_f32_32x32x16_bf16 v[16:31], v[100:103], v[156:159], v[16:31]\n\t"
        "v_add_u32 v123, s46, v121\n\t"
        "ds_read_b64_tr_b16 v[100:101], v130 offset:6144\n\t"
        "ds_read_b64_tr_b16 v[102:103], v130 offset:9216\n\t"
        "s_waitcnt lgkmcnt(12)\n\t"
        "v_mfma_f32_32x32x16_bf16 v[32:47], v[104:107], v[156:159], v[32:47]\n\t"
        "ds_read_b64_tr_b16 v[104:105], v129 offset:6272\n\t"
        "ds_read_b64_tr_b16 v[106:107], v129 offset:9344\n\t"
        "s_waitcnt lgkmcnt(12)\n\t"
        "v_mfma_f32_32x32x16_bf16 v[48:63], v[108:111], v[156:159], v[48:63]\n\t"
        "v_add_u32 v124, s46, v122\n\t"
        "ds_read_b64_tr_b16 v[108:109], v130 offset:6272\n\t"
        "ds_read_b64_tr_b16 v[110:111], v130 offset:9344\n\t"
        "s_waitcnt lgkmcnt(12)\n\t"
        "v_mfma_f32_32x32x16_bf16 v[64:79], v[156:159], v[112:115], v[64:79]\n\t"
        "ds_read_b64_tr_b16 v[112:113], v129 offset:6400\n\t"
        "ds_read_b64_tr_b16 v[114:115], v129 offset:9472\n\t"
        "s_waitcnt lgkmcnt(10)\n\t"
        "v_mfma_f32_32x32x16_bf16 v[80:95], v[156:159], v[116:119], v[80:95]\n\t"
        "v_cmp_gt_u32 vcc, s47, v126\n\t"
        "ds_read_b64_tr_b16 v[116:117], v130 offset:6400\n\t"
        "ds_read_b64_tr_b16 v[118:119], v130 offset:9472\n\t"
        "s_waitcnt vmcnt(11) lgkmcnt(10)\n\t"
        "v_mfma_f32_32x32x16_bf16 v[0:15], v[96:99], v[160:163], v[0:15]\n\t"
        "s_cbranch_vccnz 66f\n\t"
        "67:\n\t"
        "ds_read_b64_tr_b16 v[96:97], v123 offset:0\n\t"
        "ds_read_b64_tr_b16 v[98:99], v123 offset:3072\n\t"
        "s_waitcnt lgkmcnt(10)\n\t"
        "v_mfma_f32_32x32x16_bf16 v[16:31], v[100:103], v[160:163], v[16:31]\n\t"
        "v_add_u32 v128, 1, v128\n\t"
        "ds_read_b64_tr_b16 v[100:101], v124 offset:0\n\t"
        "ds_read_b64_tr_b16 v[102:103], v124 offset:3072\n\t"
        "s_waitcnt lgkmcnt(10)\n\t"
        "v_mfma_f32_32x32x16_bf16 v[32:47], v[104:107], v[160:163], v[32:47]\n\t"
        "ds_read_b64_tr_b16 v[104:105], v123 offset:128\n\t"
        "ds_read_b64_tr_b16 v[106:107], v123 offset:3200\n\t"
        "s_waitcnt lgkmcnt(10)\n\t"
        "v_mfma_f32_32x32x16_bf16 v[48:63], v[108:111], v[160:163], v[48:63]\n\t"
        "s_add_u32 s46, s46, 12288\n\t"
        "s_add_u32 s47, s47, 1\n\t"
        "ds_read_b64_tr_b16 v[108:109], v124 offset:128\n\t"
        "ds_read_b64_tr_b16 v[110:111], v124 offset:3200\n\t"
        "s_waitcnt lgkmcnt(10)\n\t"
        "v_mfma_f32_32x32x16_bf16 v[64:79], v[160:163], v[112:115], v[64:79]\n\t"
        "s_cmp_eq_u32 s46, 110592\n\t"
        "s_cselect_b32 s46, 0, s46\n\t"
        "ds_read_b64_tr_b16 v[112:113], v123 offset:256\n\t"
        "ds_read_b64_tr_b16 v[114:115], v123 offset:3328\n\t"
        "s_waitcnt lgkmcnt(10)\n\t"
        "v_mfma_f32_32x32x16_bf16 v[80:95], v[160:163], v[116:119], v[80:95]\n\t"
        "ds_write_b32 v127, v128\n\t"
        "ds_read_b64_tr_b16 v[116:117], v124 offset:256\n\t"
        "ds_read_b64_tr_b16 v[118:119], v124 offset:3328\n\t"
        "s_waitcnt vmcnt(10) lgkmcnt(11)\n\t"
        "v_mfma_f32_32x32x16_bf16 v[0:15], v[96:99], v[164:167], v[0:15]\n\t"
        "ds_read_b32 v126, v125\n\t"
        "ds_read_b64_tr_b16 v[96:97], v123 offset:6144\n\t"
        "ds_read_b64_tr_b16 v[98:99], v123 offset:9216\n\t"
        "s_waitcnt lgkmcnt(12)\n\t"
        "v_mfma_f32_32x32x16_bf16 v[16:31], v[100:103], v[164:167], v[16:31]\n\t"
        "v_add_u32 v129, s46, v121\n\t"
        "ds_read_b64_tr_b16 v[100:101], v124 offset:6144\n\t"
        "ds_read_b64_tr_b16 v[102:103], v124 offset:9216\n\t"
        "s_waitcnt lgkmcnt(12)\n\t"
        "v_mfma_f32_32x32x16_bf16 v[32:47], v[104:107], v[164:167], v[32:47]\n\t"
        "ds_read_b64_tr_b16 v[104:105], v123 offset:6272\n\t"
        "ds_read_b64_tr_b16 v[106:107], v123 offset:9344\n\t"
        "s_waitcnt lgkmcnt(12)\n\t"
        "v_mfma_f32_32x32x16_bf16 v[48:63], v[108:111], v[164:167], v[48:63]\n\t"
        "v_add_u32 v130, s46, v122\n\t"
        "ds_read_b64_tr_b16 v[108:109], v124 offset:6272\n\t"
        "ds_read_b64_tr_b16 v[110:111], v124 offset:9344\n\t"
        "s_waitcnt lgkmcnt(12)\n\t"
        "v_mfma_f32_32x32x16_bf16 v[64:79], v[164:167], v[112:115], v[64:79]\n\t"
        "ds_read_b64_tr_b16 v[112:113], v123 offset:6400\n\t"
        "ds_read_b64_tr_b16 v[114:115], v123 offset:9472\n\t"
        "s_waitcnt lgkmcnt(10)\n\t"
        "v_mfma_f32_32x32x16_bf16 v[80:95], v[164:167], v[116:119], v[80:95]\n\t"
        "v_cmp_gt_u32 vcc, s47, v126\n\t"
        "ds_read_b64_tr_b16 v[116:117], v124 offset:6400\n\t"
        "ds_read_b64_tr_b16 v[118:119], v124 offset:9472\n\t"
        "s_waitcnt vmcnt(9) lgkmcnt(10)\n\t"
        "v_mfma_f32_32x32x16_bf16 v[0:15], v[96:99], v[168:171], v[0:15]\n\t"
        "s_cbranch_vccnz 68f\n\t"
        "69:\n\t"
        "ds_read_b64_tr_b16 v[96:97], v129 offset:0\n\t"
        "ds_read_b64_tr_b16 v[98:99], v129 offset:3072\n\t"
        "s_waitcnt lgkmcnt(10)\n\t"
        "v_mfma_f32_32x32x16_bf16 v[16:31], v[100:103], v[168:171], v[16:31]\n\t"
        "v_add_u32 v128, 1, v128\n\t"
        "ds_read_b64_tr_b16 v[100:101], v130 offset:0\n\t"
        "ds_read_b64_tr_b16 v[102:103], v130 offset:3072\n\t"
        "s_waitcnt lgkmcnt(10)\n\t"
        "v_mfma_f32_32x32x16_bf16 v[32:47], v[104:107], v[168:171], v[32:47]\n\t"
        "ds_read_b64_tr_b16 v[104:105], v129 offset:128\n\t"
        "ds_read_b64_tr_b16 v[106:107], v129 offset:3200\n\t"
        "s_waitcnt lgkmcnt(10)\n\t"
        "v_mfma_f32_32x32x16_bf16 v[48:63], v[108:111], v[168:171], v[48:63]\n\t"
        "s_add_u32 s46, s46, 12288\n\t"
        "s_add_u32 s47, s47, 1\n\t"
        "ds_read_b64_tr_b16 v[108:109], v130 offset:128\n\t"
        "ds_read_b64_tr_b16 v[110:111], v130 offset:3200\n\t"
        "s_waitcnt lgkmcnt(10)\n\t"
        "v_mfma_f32_32x32x16_bf16 v[64:79], v[168:171], v[112:115], v[64:79]\n\t"
        "s_cmp_eq_u32 s46, 110592\n\t"
        "s_cselect_b32 s46, 0, s46\n\t"
        "ds_read_b64_tr_b16 v[112:113], v129 offset:256\n\t"
        "ds_read_b64_tr_b16 v[114:115], v129 offset:3328\n\t"
        "s_waitcnt lgkmcnt(10)\n\t"
        "v_mfma_f32_32x32x16_bf16 v[80:95], v[168:171], v[116:119], v[80:95]\n\t"
        "ds_write_b32 v127, v128\n\t"
        "ds_read_b64_tr_b16 v[116:117], v130 offset:256\n\t"
        "ds_read_b64_tr_b16 v[118:119], v130 offset:3328\n\t"
        "s_waitcnt vmcnt(8) lgkmcnt(11)\n\t"
        "v_mfma_f32_32x32x16_bf16 v[0:15], v[96:99], v[172:175], v[0:15]\n\t"
        "ds_read_b32 v126, v125\n\t"
        "ds_read_b64_tr_b16 v[96:97], v129 offset:6144\n\t"
        "ds_read_b64_tr_b16 v[98:99], v129 offset:9216\n\t"
        "s_waitcnt lgkmcnt(12)\n\t"
        "v_mfma_f32_32x32x16_bf16 v[16:31], v[100:103], v[172:175], v[16:31]\n\t"
        "v_add_u32 v123, s46, v121\n\t"
        "ds_read_b64_tr_b16 v[100:101], v130 offset:6144\n\t"
        "ds_read_b64_tr_b16 v[102:103], v130 offset:9216\n\t"
        "s_waitcnt lgkmcnt(12)\n\t"
        "v_mfma_f32_32x32x16_bf16 v[32:47], v[104:107], v[172:175], v[32:47]\n\t"
        "ds_read_b64_tr_b16 v[104:105], v129 offset:6272\n\t"
        "ds_read_b64_tr_b16 v[106:107], v129 offset:9344\n\t"
        "s_waitcnt lgkmcnt(12)\n\t"
        "v_mfma_f32_32x32x16_bf16 v[48:63], v[108:111], v[172:175], v[48:63]\n\t"
        "v_add_u32 v124, s46, v122\n\t"
        "ds_read_b64_tr_b16 v[108:109], v130 offset:6272\n\t"
        "ds_read_b64_tr_b16 v[110:111], v130 offset:9344\n\t"
        "s_waitcnt lgkmcnt(12)\n\t"
        "v_mfma_f32_32x32x16_bf16 v[64:79], v[172:175], v[112:115], v[64:79]\n\t"
        "ds_read_b64_tr_b16 v[112:113], v129 offset:6400\n\t"
        "ds_read_b64_tr_b16 v[114:115], v129 offset:9472\n\t"
        "s_waitcnt lgkmcnt(10)\n\t"
        "v_mfma_f32_32x32x16_bf16 v[80:95], v[172:175], v[116:119], v[80:95]\n\t"
        "v_cmp_gt_u32 vcc, s47, v126\n\t"
        "ds_read_b64_tr_b16 v[116:117], v130 offset:6400\n\t"
        "ds_read_b64_tr_b16 v[118:119], v130 offset:9472\n\t"
        "s_waitcnt vmcnt(7) lgkmcnt(10)\n\t"
        "v_mfma_f32_32x32x16_bf16 v[0:15], v[96:99], v[176:179], v[0:15]\n\t"
        "s_cbranch_vccnz 70f\n\t"
        "71:\n\t"
        "ds_read_b64_tr_b16 v[96:97], v123 offset:0\n\t"
        "ds_read_b64_tr_b16 v[98:99], v123 offset:3072\n\t"
        "s_waitcnt lgkmcnt(10)\n\t"
        "v_mfma_f32_32x32x16_bf16 v[16:31], v[100:103], v[176:179], v[16:31]\n\t"
        "v_add_u32 v128, 1, v128\n\t"
        "ds_read_b64_tr_b16 v[100:101], v124 offset:0\n\t"
        "ds_read_b64_tr_b16 v[102:103], v124 offset:3072\n\t"
        "s_waitcnt lgkmcnt(10)\n\t"
        "v_mfma_f32_32x32x16_bf16 v[32:47], v[104:107], v[176:179], v[32:47]\n\t"
        "ds_read_b64_tr_b16 v[104:105], v123 offset:128\n\t"
        "ds_read_b64_tr_b16 v[106:107], v123 offset:3200\n\t"
        "s_waitcnt lgkmcnt(10)\n\t"
        "v_mfma_f32_32x32x16_bf16 v[48:63], v[108:111], v[176:179], v[48:63]\n\t"
        "s_add_u32 s46, s46, 12288\n\t"
        "s_add_u32 s47, s47, 1\n\t"
        "ds_read_b64_tr_b16 v[108:109], v124 offset:128\n\t"
        "ds_read_b64_tr_b16 v[110:111], v124 offset:3200\n\t"
        "s_waitcnt lgkmcnt(10)\n\t"
        "v_mfma_f32_32x32x16_bf16 v[64:79], v[176:179], v[112:115], v[64:79]\n\t"
        "s_cmp_eq_u32 s46, 110592\n\t"
        "s_cselect_b32 s46, 0, s46\n\t"
        "ds_read_b64_tr_b16 v[112:113], v123 offset:256\n\t"
        "ds_read_b64_tr_b16 v[114:115], v123 offset:3328\n\t"
        "s_waitcnt lgkmcnt(10)\n\t"
        "v_mfma_f32_32x32x16_bf16 v[80:95], v[176:179], v[116:119], v[80:95]\n\t"
        "ds_write_b32 v127, v128\n\t"
        "ds_read_b64_tr_b16 v[116:117], v124 offset:256\n\t"
        "ds_read_b64_tr_b16 v[118:119], v124 offset:3328\n\t"
        "s_waitcnt vmcnt(6) lgkmcnt(11)\n\t"
        "v_mfma_f32_32x32x16_bf16 v[0:15], v[96:99], v[180:183], v[0:15]\n\t"
        "ds_read_b32 v126, v125\n\t"
        "ds_read_b64_tr_b16 v[96:97], v123 offset:6144\n\t"
        "ds_read_b64_tr_b16 v[98:99], v123 offset:9216\n\t"
        "s_waitcnt lgkmcnt(12)\n\t"
        "v_mfma_f32_32x32x16_bf16 v[16:31], v[100:103], v[180:183], v[16:31]\n\t"
        "v_add_u32 v129, s46, v121\n\t"
        "ds_read_b64_tr_b16 v[100:101], v124 offset:6144\n\t"
        "ds_read_b64_tr_b16 v[102:103], v124 offset:9216\n\t"
        "s_waitcnt lgkmcnt(12)\n\t"
        "v_mfma_f32_32x32x16_bf16 v[32:47], v[104:107], v[180:183], v[32:47]\n\t"
        "ds_read_b64_tr_b16 v[104:105], v123 offset:6272\n\t"
        "ds_read_b64_tr_b16 v[106:107], v123 offset:9344\n\t"
        "s_waitcnt lgkmcnt(12)\n\t"
        "v_mfma_f32_32x32x16_bf16 v[48:63], v[108:111], v[180:183], v[48:63]\n\t"
        "v_add_u32 v130, s46, v122\n\t"
        "ds_read_b64_tr_b16 v[108:109], v124 offset:6272\n\t"
        "ds_read_b64_tr_b16 v[110:111], v124 offset:9344\n\t"
        "s_waitcnt lgkmcnt(12)\n\t"
        "v_mfma_f32_32x32x16_bf16 v[64:79], v[180:183], v[112:115], v[64:79]\n\t"
        "ds_read_b64_tr_b16 v[112:113], v123 offset:6400\n\t"
        "ds_read_b64_tr_b16 v[114:115], v123 offset:9472\n\t"
        "s_waitcnt lgkmcnt(10)\n\t"
        "v_mfma_f32_32x32x16_bf16 v[80:95], v[180:183], v[116:119], v[80:95]\n\t"
        "v_cmp_gt_u32 vcc, s47, v126\n\t"
        "ds_read_b64_tr_b16 v[116:117], v124 offset:6400\n\t"
        "ds_read_b64_tr_b16 v[118:119], v124 offset:9472\n\t"
        "s_waitcnt vmcnt(5) lgkmcnt(10)\n\t"
        "v_mfma_f32_32x32x16_bf16 v[0:15], v[96:99], v[184:187], v[0:15]\n\t"
        "s_cbranch_vccnz 72f\n\t"
        "73:\n\t"
        "ds_read_b64_tr_b16 v[96:97], v129 offset:0\n\t"
        "ds_read_b64_tr_b16 v[98:99], v129 offset:3072\n\t"
        "s_waitcnt lgkmcnt(10)\n\t"
        "v_mfma_f32_32x32x16_bf16 v[16:31], v[100:103], v[184:187], v[16:31]\n\t"
        "v_add_u32 v128, 1, v128\n\t"
        "ds_read_b64_tr_b16 v[100:101], v130 offset:0\n\t"
        "ds_read_b64_tr_b16 v[102:103], v130 offset:3072\n\t"
        "s_waitcnt lgkmcnt(10)\n\t"
        "v_mfma_f32_32x32x16_bf16 v[32:47], v[104:107], v[184:187], v[32:47]\n\t"
        "ds_read_b64_tr_b16 v[104:105], v129 offset:128\n\t"
        "ds_read_b64_tr_b16 v[106:107], v129 offset:3200\n\t"
        "s_waitcnt lgkmcnt(10)\n\t"
        "v_mfma_f32_32x32x16_bf16 v[48:63], v[108:111], v[184:187], v[48:63]\n\t"
        "s_add_u32 s46, s46, 12288\n\t"
        "s_add_u32 s47, s47, 1\n\t"
        "ds_read_b64_tr_b16 v[108:109], v130 offset:128\n\t"
        "ds_read_b64_tr_b16 v[110:111], v130 offset:3200\n\t"
        "s_waitcnt lgkmcnt(10)\n\t"
        "v_mfma_f32_32x32x16_bf16 v[64:79], v[184:187], v[112:115], v[64:79]\n\t"
        "s_cmp_eq_u32 s46, 110592\n\t"
        "s_cselect_b32 s46, 0, s46\n\t"
        "ds_read_b64_tr_b16 v[112:113], v129 offset:256\n\t"
        "ds_read_b64_tr_b16 v[114:115], v129 offset:3328\n\t"
        "s_waitcnt lgkmcnt(10)\n\t"
        "v_mfma_f32_32x32x16_bf16 v[80:95], v[184:187], v[116:119], v[80:95]\n\t"
        "ds_write_b32 v127, v128\n\t"
        "ds_read_b64_tr_b16 v[116:117], v130 offset:256\n\t"
        "ds_read_b64_tr_b16 v[118:119], v130 offset:3328\n\t"
        "s_waitcnt vmcnt(4) lgkmcnt(11)\n\t"
        "v_mfma_f32_32x32x16_bf16 v[0:15], v[96:99], v[188:191], v[0:15]\n\t"
        "ds_read_b64_tr_b16 v[96:97], v129 offset:6144\n\t"
        "ds_read_b64_tr_b16 v[98:99], v129 offset:9216\n\t"
        "s_waitcnt lgkmcnt(11)\n\t"
        "v_mfma_f32_32x32x16_bf16 v[16:31], v[100:103], v[188:191], v[16:31]\n\t"
        "ds_read_b64_tr_b16 v[100:101], v130 offset:6144\n\t"
        "ds_read_b64_tr_b16 v[102:103], v130 offset:9216\n\t"
        "s_waitcnt lgkmcnt(11)\n\t"
        "v_mfma_f32_32x32x16_bf16 v[32:47], v[104:107], v[188:191], v[32:47]\n\t"
        "ds_read_b64_tr_b16 v[104:105], v129 offset:6272\n\t"
        "ds_read_b64_tr_b16 v[106:107], v129 offset:9344\n\t"
        "s_waitcnt lgkmcnt(11)\n\t"
        "v_mfma_f32_32x32x16_bf16 v[48:63], v[108:111], v[188:191], v[48:63]\n\t"
        "ds_read_b64_tr_b16 v[108:109], v130 offset:6272\n\t"
        "ds_read_b64_tr_b16 v[110:111], v130 offset:9344\n\t"
        "s_waitcnt lgkmcnt(11)\n\t"
        "v_mfma_f32_32x32x16_bf16 v[64:79], v[188:191], v[112:115], v[64:79]\n\t"
        "ds_read_b64_tr_b16 v[112:113], v129 offset:6400\n\t"
        "ds_read_b64_tr_b16 v[114:115], v129 offset:9472\n\t"
        "s_waitcnt lgkmcnt(10)\n\t"
        "v_mfma_f32_32x32x16_bf16 v[80:95], v[188:191], v[116:119], v[80:95]\n\t"
        "ds_read_b64_tr_b16 v[116:117], v130 offset:6400\n\t"
        "ds_read_b64_tr_b16 v[118:119], v130 offset:9472\n\t"
        "s_waitcnt vmcnt(3) lgkmcnt(10)\n\t"
        "v_mfma_f32_32x32x16_bf16 v[0:15], v[96:99], v[192:195], v[0:15]\n\t"
        "s_waitcnt lgkmcnt(8)\n\t"
        "v_mfma_f32_32x32x16_bf16 v[16:31], v[100:103], v[192:195], v[16:31]\n\t"
        "s_waitcnt lgkmcnt(6)\n\t"
        "v_mfma_f32_32x32x16_bf16 v[32:47], v[104:107], v[192:195], v[32:47]\n\t"
        "s_waitcnt lgkmcnt(4)\n\t"
        "v_mfma_f32_32x32x16_bf16 v[48:63], v[108:111], v[192:195], v[48:63]\n\t"
        "s_waitcnt lgkmcnt(2)\n\t"
        "v_mfma_f32_32x32x16_bf16 v[64:79], v[192:195], v[112:115], v[64:79]\n\t"
        "s_waitcnt lgkmcnt(0)\n\t"
        "v_mfma_f32_32x32x16_bf16 v[80:95], v[192:195], v[116:119], v[80:95]\n\t"
        "s_waitcnt vmcnt(0)\n\t"
        "s_nop 10\n\t"
        "s_branch 9f\n\t"
        "16:\n\t"
        "s_mov_b32 s48, 0x10000\n\t"
        "8:\n\t"
        "ds_read_b32 v126, v125\n\t"
        "s_sub_u32 s48, s48, 1\n\t"
        "s_waitcnt lgkmcnt(0)\n\t"
        "s_cbranch_scc1 17b\n\t"
        "v_cmp_gt_u32 vcc, s47, v126\n\t"
        "s_cbranch_vccnz 8b\n\t"
        "s_branch 17b\n\t"
        "18:\n\t"
        "s_mov_b32 s48, 0x10000\n\t"
        "8:\n\t"
        "ds_read_b32 v126, v125\n\t"
        "s_sub_u32 s48, s48, 1\n\t"
        "s_waitcnt lgkmcnt(0)\n\t"
        "s_cbranch_scc1 19b\n\t"
        "v_cmp_gt_u32 vcc, s47, v126\n\t"
        "s_cbranch_vccnz 8b\n\t"
        "s_branch 19b\n\t"
        "20:\n\t"
        "s_mov_b32 s48, 0x10000\n\t"
        "8:\n\t"
        "ds_read_b32 v126, v125\n\t"
        "s_sub_u32 s48, s48, 1\n\t"
        "s_waitcnt lgkmcnt(0)\n\t"
        "s_cbranch_scc1 21b\n\t"
        "v_cmp_gt_u32 vcc, s47, v126\n\t"
        "s_cbranch_vccnz 8b\n\t"
        "s_branch 21b\n\t"
        "22:\n\t"
        "s_mov_b32 s48, 0x10000\n\t"
        "8:\n\t"
        "ds_read_b32 v126, v125\n\t"
        "s_sub_u32 s48, s48, 1\n\t"
        "s_waitcnt lgkmcnt(0)\n\t"
        "s_cbranch_scc1 23b\n\t"
        "v_cmp_gt_u32 vcc, s47, v126\n\t"
        "s_cbranch_vccnz 8b\n\t"
        "s_branch 23b\n\t"
        "24:\n\t"
        "s_mov_b32 s48, 0x10000\n\t"
        "8:\n\t"
        "ds_read_b32 v126, v125\n\t"
        "s_sub_u32 s48, s48, 1\n\t"
        "s_waitcnt lgkmcnt(0)\n\t"
        "s_cbranch_scc1 25b\n\t"
        "v_cmp_gt_u32 vcc, s47, v126\n\t"
        "s_cbranch_vccnz 8b\n\t"
        "s_branch 25b\n\t"
        "26:\n\t"
        "s_mov_b32 s48, 0x10000\n\t"
        "8:\n\t"
        "ds_read_b32 v126, v125\n\t"
        "s_sub_u32 s48, s48, 1\n\t"
        "s_waitcnt lgkmcnt(0)\n\t"
        "s_cbranch_scc1 27b\n\t"
        "v_cmp_gt_u32 vcc, s47, v126\n\t"
        "s_cbranch_vccnz 8b\n\t"
        "s_branch 27b\n\t"
        "28:\n\t"
        "s_mov_b32 s48, 0x10000\n\t"
        "8:\n\t"
        "ds_read_b32 v126, v125\n\t"
        "s_sub_u32 s48, s48, 1\n\t"
        "s_waitcnt lgkmcnt(0)\n\t"
        "s_cbranch_scc1 29b\n\t"
        "v_cmp_gt_u32 vcc, s47, v126\n\t"
        "s_cbranch_vccnz 8b\n\t"
        "s_branch 29b\n\t"
        "30:\n\t"
        "s_mov_b32 s48, 0x10000\n\t"
        "8:\n\t"
        "ds_read_b32 v126, v125\n\t"
        "s_sub_u32 s48, s48, 1\n\t"
        "s_waitcnt lgkmcnt(0)\n\t"
        "s_cbranch_scc1 31b\n\t"
        "v_cmp_gt_u32 vcc, s47, v126\n\t"
        "s_cbranch_vccnz 8b\n\t"
        "s_branch 31b\n\t"
        "64:\n\t"
        "s_mov_b32 s48, 0x10000\n\t"
        "8:\n\t"
        "ds_read_b32 v126, v125\n\t"
        "s_sub_u32 s48, s48, 1\n\t"
        "s_waitcnt lgkmcnt(0)\n\t"
        "s_cbranch_scc1 65b\n\t"
        "v_cmp_gt_u32 vcc, s47, v126\n\t"
        "s_cbranch_vccnz 8b\n\t"
        "s_branch 65b\n\t"
        "66:\n\t"
        "s_mov_b32 s48, 0x10000\n\t"
        "8:\n\t"
        "ds_read_b32 v126, v125\n\t"
        "s_sub_u32 s48, s48, 1\n\t"
        "s_waitcnt lgkmcnt(0)\n\t"
        "s_cbranch_scc1 67b\n\t"
        "v_cmp_gt_u32 vcc, s47, v126\n\t"
        "s_cbranch_vccnz 8b\n\t"
        "s_branch 67b\n\t"
        "68:\n\t"
        "s_mov_b32 s48, 0x10000\n\t"
        "8:\n\t"
        "ds_read_b32 v126, v125\n\t"
        "s_sub_u32 s48, s48, 1\n\t"
        "s_waitcnt lgkmcnt(0)\n\t"
        "s_cbranch_scc1 69b\n\t"
        "v_cmp_gt_u32 vcc, s47, v126\n\t"
        "s_cbranch_vccnz 8b\n\t"
        "s_branch 69b\n\t"
        "70:\n\t"
        "s_mov_b32 s48, 0x10000\n\t"
        "8:\n\t"
        "ds_read_b32 v126, v125\n\t"
        "s_sub_u32 s48, s48, 1\n\t"
        "s_waitcnt lgkmcnt(0)\n\t"
        "s_cbranch_scc1 71b\n\t"
        "v_cmp_gt_u32 vcc, s47, v126\n\t"
        "s_cbranch_vccnz 8b\n\t"
        "s_branch 71b\n\t"
        "72:\n\t"
        "s_mov_b32 s48, 0x10000\n\t"
        "8:\n\t"
        "ds_read_b32 v126, v125\n\t"
        "s_sub_u32 s48, s48, 1\n\t"
        "s_waitcnt lgkmcnt(0)\n\t"
        "s_cbranch_scc1 73b\n\t"
        "v_cmp_gt_u32 vcc, s47, v126\n\t"
        "s_cbranch_vccnz 8b\n\t"
        "s_branch 73b\n\t"
        "9:\n\t"
        : "+{v[0:15]}"(acc[0]), "+{v[16:31]}"(acc[1]), "+{v[32:47]}"(acc[2]), "+{v[48:63]}"(acc[3]), "+{v[64:79]}"(acc[4]), "+{v[80:95]}"(acc[5]),
          "+{v[96:111]}"(stA), "+{v[112:127]}"(stB), "+{v[128:131]}"(stC), "+{v[148:163]}"(xr1), "+{v[164:179]}"(xr2), "+{v[180:195]}"(xr3),
          "+{s44}"(soff), "+{s46}"(slot), "+{s47}"(need)
        : "{s[40:43]}"(srd) : "v132", "v133", "v134", "v135", "v136", "v137", "v138", "v139", "v140", "v141", "v142", "v143", "v144", "v145", "v146", "v147", "s45", "s48", "vcc", "scc", "memory");
      STAMPV(4 + 16 * L);
      PHASE_VARS;
      if (L == 1) {
        f32x4 av[4], cs[4][4];
        float cv[2];
#pragma unroll
        for (int g = 0; g < 4; ++g) av[g] = *reinterpret_cast<const f32x4*>(smem + L_AB + wid * 256 + (8 * g + 4 * hi) * 4);
        SBAR();
#pragma unroll
        for (int j = 0; j < 6; ++j)
#pragma unroll
          for (int e = 0; e < 16; ++e) acc[j][e] *= j < 4 ? al : av[e >> 2][e & 3];
        SBAR();
        lds_wait_ge(lbase + L_W + 4 + 4 * (lane & 1), 1);
        const int idx = (int)__builtin_amdgcn_readfirstlane(words[3]);
        const bool mine = (idx >> 5) == tile;
        if (mine) {
#pragma unroll
          for (int j = 0; j < 6; ++j) {
            const int cb = (j >> 1) * 64 + ((j & 1) ^ hh) * 32;
            if (j >= 4) cv[j - 4] = *reinterpret_cast<const float*>(smem + L_PATCH + (cb + r32) * 4);
            else {
#pragma unroll
              for (int g = 0; g < 4; ++g) cs[j][g] = *reinterpret_cast<const f32x4*>(smem + L_PATCH + (cb + 8 * g + 4 * hi) * 4);
            }
          }
          SBAR();
          const int rel = (idx & 31) - 4 * hi;
#pragma unroll
          for (int j = 0; j < 6; ++j)
#pragma unroll
            for (int e = 0; e < 16; ++e)
              acc[j][e] = j < 4 ? (r32 == (idx & 31) ? cs[j & 3][e >> 2][e & 3] : acc[j][e]) : (rel == 8 * (e >> 2) + (e & 3) ? cv[j & 1] : acc[j][e]);
        }
      }
      constexpr float QS = 0.17677669529663687f * 1.4426950408889634f;
      u32x4 own[4];
#pragma unroll
      for (int j = 5; j >= 0; --j) {
        u32x4 o[2];
#pragma unroll
        for (int e = 0; e < 8; ++e) {
          unsigned w = pack2(acc[j][2 * e], acc[j][2 * e + 1]);
          if (j < 2) w = pack2(lo16(w) * QS, hi16(w) * QS);
          o[e >> 2][e & 3] = w;
        }
        if (j >= 4) { o[0] = fo_swap(o[0]); o[1] = fo_swap(o[1]); }
        const int hs = (j & 1) ^ hh;
        if (j >= 2) {
          const unsigned dst = OFF_KV + L * (10 * MBY) + (tn * 2 + hs) * HS + tile * 4096 + ((j >> 1) - 1) * 2048 + lane * 16;
          stg(rs, dst, o[0]); stg(rs, dst + 1024, o[1]);
        }
        if (j >= 2 && !(j & 1)) { own[j - 2] = o[0]; own[j - 1] = o[1]; }
        else {
          char* p = smem + L_XCH + (j < 2 ? (hs * 4 + wid) * 2048 : 16384 + wid * 4096 + ((j >> 1) - 1) * 2048) + lane * 16;
          *reinterpret_cast<u32x4*>(p) = o[0]; *reinterpret_cast<u32x4*>(p + 1024) = o[1];
        }
        SBAR();
      }
      BAR_ALL();
      kva = cat4(own[0], own[1], own[2], own[3]);
      const u32x4* pk = reinterpret_cast<const u32x4*>(smem + L_XCH + 16384 + (wid ^ 1) * 4096 + lane * 16);
#pragma unroll
      for (int u = 0; u < 4; ++u) {
        const u32x4* p = reinterpret_cast<const u32x4*>(smem + L_XCH + (hh * 4 + ((2 * kh + 2 + u) & 3)) * 2048 + lane * 16);
        q[u] = __builtin_shufflevector(p[0], p[64], 0, 1, 2, 3, 4, 5, 6, 7);
      }
      kvb = cat4(pk[0], pk[64], pk[128], pk[192]);
      SBAR();
      asm volatile(
        "v_mov_b64 v[0:1], 0\n\t"
        "v_mov_b64 v[2:3], 0\n\t"
        "v_mov_b64 v[4:5], 0\n\t"
        "v_mov_b64 v[6:7], 0\n\t"
        "v_mov_b64 v[8:9], 0\n\t"
        "v_mov_b64 v[10:11], 0\n\t"
        "v_mov_b64 v[12:13], 0\n\t"
        "v_mov_b64 v[14:15], 0\n\t"
        "v_mov_b64 v[16:17], 0\n\t"
        "v_mov_b64 v[18:19], 0\n\t"
        "v_mov_b64 v[20:21], 0\n\t"
        "v_mov_b64 v[22:23], 0\n\t"
        "v_mov_b64 v[24:25], 0\n\t"
        "v_mov_b64 v[26:27], 0\n\t"
        "v_mov_b64 v[28:29], 0\n\t"
        "v_mov_b64 v[30:31], 0\n\t"
        "v_mov_b64 v[32:33], 0\n\t"
        "v_mov_b64 v[34:35], 0\n\t"
        "v_mov_b64 v[36:37], 0\n\t"
        "v_mov_b64 v[38:39], 0\n\t"
        "v_mov_b64 v[40:41], 0\n\t"
        "v_mov_b64 v[42:43], 0\n\t"
        "v_mov_b64 v[44:45], 0\n\t"
        "v_mov_b64 v[46:47], 0\n\t"
        "v_mov_b64 v[48:49], 0\n\t"
        "v_mov_b64 v[50:51], 0\n\t"
        "v_mov_b64 v[52:53], 0\n\t"
        "v_mov_b64 v[54:55], 0\n\t"
        "v_mov_b64 v[56:57], 0\n\t"
        "v_mov_b64 v[58:59], 0\n\t"
        "v_mov_b64 v[60:61], 0\n\t"
        "v_mov_b64 v[62:63], 0\n\t"
        "v_mov_b64 v[128:129], 0\n\t"
        "v_mov_b64 v[130:131], 0\n\t"
        "v_mov_b64 v[132:133], 0\n\t"
        "v_mov_b64 v[134:135], 0\n\t"
        "v_mov_b64 v[136:137], 0\n\t"
        "v_mov_b64 v[138:139], 0\n\t"
        "v_mov_b64 v[140:141], 0\n\t"
        "v_mov_b64 v[142:143], 0\n\t"
        : "={v[0:15]}"(oz[0]), "={v[16:31]}"(oz[1]), "={v[32:47]}"(oz[2]), "={v[48:63]}"(oz[3]), "={v[128:143]}"(oz[4]));
    }
    STAMPV(5 + 16 * L);
    {
      u32x4 ones;
      unsigned kvoff;
      u32x8 fv, cv;
      asm volatile("" : "={v[184:191]}"(fv), "={v[216:223]}"(cv));
      { PHASE_VARS;
        const unsigned v = (lane & 3) == 0 ? 0x3F803F80u : 0u; const bool up = (lane & 4) != 0; ones[0] = ones[1] = up ? 0u : v; ones[2] = ones[3] = up ? v : 0u;
        kvoff = OFF_KV + L * (10 * MBY) + (tn * 2 + hh) * HS + kh * 8192 + lane * 16;
        fv[1] = lbase + L_FREE + wid * 256 + lane * 4; fv[2] = 32 * L + 32;
        cv[0] = lbase + L_XCH + wid * 8192 + lane * 16; cv[1] = lbase + L_XCH + (wid ^ 2) * 8192 + lane * 16;
        cv[2] = lbase + L_XCH + 32768 + wid * 512 + lane * 4; cv[3] = lbase + L_XCH + 32768 + (wid ^ 2) * 512 + lane * 4;
        cv[4] = lbase + L_W + 48 + wid * 4; cv[5] = lbase + L_W + 48 + (wid ^ 2) * 4; cv[6] = L + 1;
        cv[7] = ((tile * 32 + r32) * D_ + tn * 64 + hi * 8) * 2; }
      f32x16 o0 = oz[0], o1 = oz[1], o2 = oz[2], o3 = oz[3], lv = oz[4];
      unsigned soff = tm * 16384;
      STAMP_DECL;
      asm volatile(
        "s_nop 1\n\t"
        "v_mfma_f32_32x32x16_bf16 v[96:111], v[152:155], v[64:67], 0\n\t"
        "v_mfma_f32_32x32x16_bf16 v[96:111], v[156:159], v[68:71], v[96:111]\n\t"
        "s_mov_b32 s46, 0\n\t"
        "ds_write_b32 v185, v186\n\t"
        "s_nop 9\n\t"
        "v_exp_f32 v96, v96\n\t"
        "v_exp_f32 v97, v97\n\t"
        "v_exp_f32 v98, v98\n\t"
        "v_exp_f32 v99, v99\n\t"
        "v_exp_f32 v100, v100\n\t"
        "v_exp_f32 v101, v101\n\t"
        "v_mfma_f32_32x32x16_bf16 v[112:127], v[152:155], v[72:75], 0\n\t"
        "v_exp_f32 v102, v102\n\t"
        "v_exp_f32 v103, v103\n\t"
        "v_exp_f32 v104, v104\n\t"
        "v_mfma_f32_32x32x16_bf16 v[112:127], v[156:159], v[76:79], v[112:127]\n\t"
        "v_exp_f32 v105, v105\n\t"
        "v_exp_f32 v106, v106\n\t"
        "v_exp_f32 v107, v107\n\t"
        "v_smfmac_f32_32x32x32_bf16 v[0:15], v[160:163], v[96:103], v148\n\t"
        "v_exp_f32 v108, v108\n\t"
        "v_exp_f32 v109, v109\n\t"
        "v_exp_f32 v110, v110\n\t"
        "v_smfmac_f32_16x16x64_bf16 v[128:131], v[144:147], v[96:103], v148\n\t"
        "v_exp_f32 v111, v111\n\t"
        "v_exp_f32 v112, v112\n\t"
        "v_exp_f32 v113, v113\n\t"
        "v_smfmac_f32_32x32x32_bf16 v[0:15], v[164:167], v[104:111], v148\n\t"
        "v_exp_f32 v114, v114\n\t"
        "v_exp_f32 v115, v115\n\t"
        "v_exp_f32 v116, v116\n\t"
        "v_smfmac_f32_16x16x64_bf16 v[128:131], v[144:147], v[104:111], v148\n\t"
        "v_exp_f32 v117, v117\n\t"
        "v_mfma_f32_32x32x16_bf16 v[96:111], v[152:155], v[80:83], 0\n\t"
        "v_exp_f32 v118, v118\n\t"
        "v_exp_f32 v119, v119\n\t"
        "v_exp_f32 v120, v120\n\t"
        "v_mfma_f32_32x32x16_bf16 v[96:111], v[156:159], v[84:87], v[96:111]\n\t"
        "v_exp_f32 v121, v121\n\t"
        "v_exp_f32 v122, v122\n\t"
        "v_exp_f32 v123, v123\n\t"
        "v_smfmac_f32_32x32x32_bf16 v[16:31], v[160:163], v[112:119], v148\n\t"
        "v_exp_f32 v124, v124\n\t"
        "v_exp_f32 v125, v125\n\t"
        "v_exp_f32 v126, v126\n\t"
        "v_smfmac_f32_16x16x64_bf16 v[132:135], v[144:147], v[112:119], v148\n\t"
        "v_exp_f32 v127, v127\n\t"
        "v_exp_f32 v96, v96\n\t"
        "v_exp_f32 v97, v97\n\t"
        "v_smfmac_f32_32x32x32_bf16 v[16:31], v[164:167], v[120:127], v148\n\t"
        "v_exp_f32 v98, v98\n\t"
        "v_exp_f32 v99, v99\n\t"
        "v_exp_f32 v100, v100\n\t"
        "v_smfmac_f32_16x16x64_bf16 v[132:135], v[144:147], v[120:127], v148\n\t"
        "v_exp_f32 v101, v101\n\t"
        "v_mfma_f32_32x32x16_bf16 v[112:127], v[152:155], v[88:91], 0\n\t"
        "v_exp_f32 v102, v102\n\t"
        "v_exp_f32 v103, v103\n\t"
        "v_exp_f32 v104, v104\n\t"
        "v_mfma_f32_32x32x16_bf16 v[112:127], v[156:159], v[92:95], v[112:127]\n\t"
        "v_exp_f32 v105, v105\n\t"
        "v_exp_f32 v106, v106\n\t"
        "v_exp_f32 v107, v107\n\t"
        "v_smfmac_f32_32x32x32_bf16 v[32:47], v[160:163], v[96:103], v148\n\t"
        "v_exp_f32 v108, v108\n\t"
        "v_exp_f32 v109, v109\n\t"
        "v_exp_f32 v110, v110\n\t"
        "v_smfmac_f32_16x16x64_bf16 v[136:139], v[144:147], v[96:103], v148\n\t"
        "v_exp_f32 v111, v111\n\t"
        "v_exp_f32 v112, v112\n\t"
        "v_exp_f32 v113, v113\n\t"
        "v_smfmac_f32_32x32x32_bf16 v[32:47], v[164:167], v[104:111], v148\n\t"
        "v_exp_f32 v114, v114\n\t"
        "v_exp_f32 v115, v115\n\t"
        "v_exp_f32 v116, v116\n\t"
        "v_smfmac_f32_16x16x64_bf16 v[136:139], v[144:147], v[104:111], v148\n\t"
        "v_exp_f32 v117, v117\n\t"
        "v_mfma_f32_32x32x16_bf16 v[96:111], v[168:171], v[64:67], 0\n\t"
        "v_exp_f32 v118, v118\n\t"
        "v_exp_f32 v119, v119\n\t"
        "v_exp_f32 v120, v120\n\t"
        "v_mfma_f32_32x32x16_bf16 v[96:111], v[172:175], v[68:71], v[96:111]\n\t"
        "v_exp_f32 v121, v121\n\t"
        "v_exp_f32 v122, v122\n\t"
        "v_exp_f32 v123, v123\n\t"
        "v_smfmac_f32_32x32x32_bf16 v[48:63], v[160:163], v[112:119], v148\n\t"
        "v_exp_f32 v124, v124\n\t"
        "v_exp_f32 v125, v125\n\t"
        "v_exp_f32 v126, v126\n\t"
        "v_smfmac_f32_16x16x64_bf16 v[140:143], v[144:147], v[112:119], v148\n\t"
        "v_exp_f32 v127, v127\n\t"
        "v_exp_f32 v96, v96\n\t"
        "v_exp_f32 v97, v97\n\t"
        "v_smfmac_f32_32x32x32_bf16 v[48:63], v[164:167], v[120:127], v148\n\t"
        "v_exp_f32 v98, v98\n\t"
        "v_exp_f32 v99, v99\n\t"
        "v_exp_f32 v100, v100\n\t"
        "v_smfmac_f32_16x16x64_bf16 v[140:143], v[144:147], v[120:127], v148\n\t"
        "v_exp_f32 v101, v101\n\t"
        "v_mfma_f32_32x32x16_bf16 v[112:127], v[168:171], v[72:75], 0\n\t"
        "v_exp_f32 v102, v102\n\t"
        "v_exp_f32 v103, v103\n\t"
        "v_exp_f32 v104, v104\n\t"
        "ds_read_b32 v151, v224\n\t"
        "v_mfma_f32_32x32x16_bf16 v[112:127], v[172:175], v[76:79], v[112:127]\n\t"
        "v_exp_f32 v105, v105\n\t"
        "v_exp_f32 v106, v106\n\t"
        "v_exp_f32 v107, v107\n\t"
        "v_smfmac_f32_32x32x32_bf16 v[0:15], v[176:179], v[96:103], v148\n\t"
        "v_exp_f32 v108, v108\n\t"
        "v_exp_f32 v109, v109\n\t"
        "v_exp_f32 v110, v110\n\t"
        "v_smfmac_f32_16x16x64_bf16 v[128:131], v[144:147], v[96:103], v148\n\t"
        "v_exp_f32 v111, v111\n\t"
        "s_cmp_lg_u32 s46, 0\n\t"
        "s_cbranch_scc1 31f\n\t"
        "s_waitcnt lgkmcnt(0)\n\t"
        "v_cmp_ne_u32 vcc, v222, v151\n\t"
        "s_cbranch_vccz 30f\n\t"
        "31:\n\t"
        "v_exp_f32 v112, v112\n\t"
        "v_exp_f32 v113, v113\n\t"
        "v_smfmac_f32_32x32x32_bf16 v[0:15], v[180:183], v[104:111], v148\n\t"
        "v_exp_f32 v114, v114\n\t"
        "v_exp_f32 v115, v115\n\t"
        "v_exp_f32 v116, v116\n\t"
        "v_smfmac_f32_16x16x64_bf16 v[128:131], v[144:147], v[104:111], v148\n\t"
        "v_exp_f32 v117, v117\n\t"
        "v_mfma_f32_32x32x16_bf16 v[96:111], v[168:171], v[80:83], 0\n\t"
        "v_exp_f32 v118, v118\n\t"
        "v_exp_f32 v119, v119\n\t"
        "v_exp_f32 v120, v120\n\t"
        "ds_read_b32 v151, v224\n\t"
        "v_mfma_f32_32x32x16_bf16 v[96:111], v[172:175], v[84:87], v[96:111]\n\t"
        "v_exp_f32 v121, v121\n\t"
        "v_exp_f32 v122, v122\n\t"
        "v_exp_f32 v123, v123\n\t"
        "v_smfmac_f32_32x32x32_bf16 v[16:31], v[176:179], v[112:119], v148\n\t"
        "v_exp_f32 v124, v124\n\t"
        "v_exp_f32 v125, v125\n\t"
        "v_exp_f32 v126, v126\n\t"
        "v_smfmac_f32_16x16x64_bf16 v[132:135], v[144:147], v[112:119], v148\n\t"
        "v_exp_f32 v127, v127\n\t"
        "s_cmp_lg_u32 s46, 0\n\t"
        "s_cbranch_scc1 33f\n\t"
        "s_waitcnt lgkmcnt(0)\n\t"
        "v_cmp_ne_u32 vcc, v222, v151\n\t"
        "s_cbranch_vccz 32f\n\t"
        "33:\n\t"
        "v_exp_f32 v96, v96\n\t"
        "v_exp_f32 v97, v97\n\t"
        "v_smfmac_f32_32x32x32_bf16 v[16:31], v[180:183], v[120:127], v148\n\t"
        "v_exp_f32 v98, v98\n\t"
        "v_exp_f32 v99, v99\n\t"
        "v_exp_f32 v100, v100\n\t"
        "v_smfmac_f32_16x16x64_bf16 v[132:135], v[144:147], v[120:127], v148\n\t"
        "v_exp_f32 v101, v101\n\t"
        "v_mfma_f32_32x32x16_bf16 v[112:127], v[168:171], v[88:91], 0\n\t"
        "v_exp_f32 v102, v102\n\t"
        "v_exp_f32 v103, v103\n\t"
        "v_exp_f32 v104, v104\n\t"
        "v_mfma_f32_32x32x16_bf16 v[112:127], v[172:175], v[92:95], v[112:127]\n\t"
        "v_exp_f32 v105, v105\n\t"
        "v_exp_f32 v106, v106\n\t"
        "v_exp_f32 v107, v107\n\t"
        "v_smfmac_f32_32x32x32_bf16 v[32:47], v[176:179], v[96:103], v148\n\t"
        "v_exp_f32 v108, v108\n\t"
        "v_exp_f32 v109, v109\n\t"
        "v_exp_f32 v110, v110\n\t"
        "v_smfmac_f32_16x16x64_bf16 v[136:139], v[144:147], v[96:103], v148\n\t"
        "v_exp_f32 v111, v111\n\t"
        "s_cmp_lg_u32 s46, 0\n\t"
        "s_cbranch_scc1 5f\n\t"
        "s_mov_b32 s45, 0x200000\n\t"
        "2:\n\t"
        "ds_read_b32 v151, v224\n\t"
        "s_sub_u32 s45, s45, 1\n\t"
        "s_waitcnt lgkmcnt(0)\n\t"
        "s_cbranch_scc1 3f\n\t"
        "v_cmp_ne_u32 vcc, v222, v151\n\t"
        "s_cbranch_vccnz 2b\n\t"
        "3:\n\t"
        "s_add_u32 s44, s44, 0x4000\n\t"
        "s_and_b32 s44, s44, 0x3ffff\n\t"
        "buffer_load_dwordx4 v[184:187], v149, s[40:43], s44 offen offset:0 sc1\n\t"
        "buffer_load_dwordx4 v[188:191], v149, s[40:43], s44 offen offset:1024 sc1\n\t"
        "buffer_load_dwordx4 v[192:195], v149, s[40:43], s44 offen offset:2048 sc1\n\t"
        "buffer_load_dwordx4 v[196:199], v149, s[40:43], s44 offen offset:3072 sc1\n\t"
        "buffer_load_dwordx4 v[200:203], v150, s[40:43], s44 offen offset:0 sc1\n\t"
        "buffer_load_dwordx4 v[204:207], v150, s[40:43], s44 offen offset:1024 sc1\n\t"
        "buffer_load_dwordx4 v[208:211], v150, s[40:43], s44 offen offset:2048 sc1\n\t"
        "buffer_load_dwordx4 v[212:215], v150, s[40:43], s44 offen offset:3072 sc1\n\t"
        "s_add_u32 s44, s44, 0x4000\n\t"
        "s_and_b32 s44, s44, 0x3ffff\n\t"
        "buffer_load_dwordx4 v[152:155], v149, s[40:43], s44 offen offset:0 sc1\n\t"
        "buffer_load_dwordx4 v[156:159], v149, s[40:43], s44 offen offset:1024 sc1\n\t"
        "buffer_load_dwordx4 v[160:163], v149, s[40:43], s44 offen offset:2048 sc1\n\t"
        "buffer_load_dwordx4 v[164:167], v149, s[40:43], s44 offen offset:3072 sc1\n\t"
        "5:\n\t"
        "s_mov_b32 s45, 7\n\t"
        "1:\n\t"
        "v_exp_f32 v112, v112\n\t"
        "v_exp_f32 v113, v113\n\t"
        "v_smfmac_f32_32x32x32_bf16 v[32:47], v[180:183], v[104:111], v148\n\t"
        "v_exp_f32 v114, v114\n\t"
        "v_exp_f32 v115, v115\n\t"
        "v_exp_f32 v116, v116\n\t"
        "v_smfmac_f32_16x16x64_bf16 v[136:139], v[144:147], v[104:111], v148\n\t"
        "v_exp_f32 v117, v117\n\t"
        "s_waitcnt vmcnt(8)\n\t"
        "v_mfma_f32_32x32x16_bf16 v[96:111], v[184:187], v[64:67], 0\n\t"
        "v_exp_f32 v118, v118\n\t"
        "v_exp_f32 v119, v119\n\t"
        "v_exp_f32 v120, v120\n\t"
        "v_mfma_f32_32x32x16_bf16 v[96:111], v[188:191], v[68:71], v[96:111]\n\t"
        "v_exp_f32 v121, v121\n\t"
        "v_exp_f32 v122, v122\n\t"
        "v_exp_f32 v123, v123\n\t"
        "v_smfmac_f32_32x32x32_bf16 v[48:63], v[176:179], v[112:119], v148\n\t"
        "v_exp_f32 v124, v124\n\t"
        "v_exp_f32 v125, v125\n\t"
        "v_exp_f32 v126, v126\n\t"
        "v_smfmac_f32_16x16x64_bf16 v[140:143], v[144:147], v[112:119], v148\n\t"
        "buffer_load_dwordx4 v[168:171], v150, s[40:43], s44 offen offset:0 sc1\n\t"
        "v_exp_f32 v127, v127\n\t"
        "v_exp_f32 v96, v96\n\t"
        "v_exp_f32 v97, v97\n\t"
        "v_smfmac_f32_32x32x32_bf16 v[48:63], v[180:183], v[120:127], v148\n\t"
        "v_exp_f32 v98, v98\n\t"
        "v_exp_f32 v99, v99\n\t"
        "v_exp_f32 v100, v100\n\t"
        "v_smfmac_f32_16x16x64_bf16 v[140:143], v[144:147], v[120:127], v148\n\t"
        "v_exp_f32 v101, v101\n\t"
        "v_mfma_f32_32x32x16_bf16 v[112:127], v[184:187], v[72:75], 0\n\t"
        "v_exp_f32 v102, v102\n\t"
        "v_exp_f32 v103, v103\n\t"
        "v_exp_f32 v104, v104\n\t"
        "v_mfma_f32_32x32x16_bf16 v[112:127], v[188:191], v[76:79], v[112:127]\n\t"
        "v_exp_f32 v105, v105\n\t"
        "v_exp_f32 v106, v106\n\t"
        "v_exp_f32 v107, v107\n\t"
        "v_smfmac_f32_32x32x32_bf16 v[0:15], v[192:195], v[96:103], v148\n\t"
        "v_exp_f32 v108, v108\n\t"
        "v_exp_f32 v109, v109\n\t"
        "v_exp_f32 v110, v110\n\t"
        "v_smfmac_f32_16x16x64_bf16 v[128:131], v[144:147], v[96:103], v148\n\t"
        "buffer_load_dwordx4 v[172:175], v150, s[40:43], s44 offen offset:1024 sc1\n\t"
        "v_exp_f32 v111, v111\n\t"
        "v_exp_f32 v112, v112\n\t"
        "v_exp_f32 v113, v113\n\t"
        "v_smfmac_f32_32x32x32_bf16 v[0:15], v[196:199], v[104:111], v148\n\t"
        "v_exp_f32 v114, v114\n\t"
        "v_exp_f32 v115, v115\n\t"
        "v_exp_f32 v116, v116\n\t"
        "v_smfmac_f32_16x16x64_bf16 v[128:131], v[144:147], v[104:111], v148\n\t"
        "v_exp_f32 v117, v117\n\t"
        "v_mfma_f32_32x32x16_bf16 v[96:111], v[184:187], v[80:83], 0\n\t"
        "v_exp_f32 v118, v118\n\t"
        "v_exp_f32 v119, v119\n\t"
        "v_exp_f32 v120, v120\n\t"
        "v_mfma_f32_32x32x16_bf16 v[96:111], v[188:191], v[84:87], v[96:111]\n\t"
        "v_exp_f32 v121, v121\n\t"
        "v_exp_f32 v122, v122\n\t"
        "v_exp_f32 v123, v123\n\t"
        "v_smfmac_f32_32x32x32_bf16 v[16:31], v[192:195], v[112:119], v148\n\t"
        "v_exp_f32 v124, v124\n\t"
        "v_exp_f32 v125, v125\n\t"
        "v_exp_f32 v126, v126\n\t"
        "v_smfmac_f32_16x16x64_bf16 v[132:135], v[144:147], v[112:119], v148\n\t"
        "buffer_load_dwordx4 v[176:179], v150, s[40:43], s44 offen offset:2048 sc1\n\t"
        "v_exp_f32 v127, v127\n\t"
        "v_exp_f32 v96, v96\n\t"
        "v_exp_f32 v97, v97\n\t"
        "v_smfmac_f32_32x32x32_bf16 v[16:31], v[196:199], v[120:127], v148\n\t"
        "v_exp_f32 v98, v98\n\t"
        "v_exp_f32 v99, v99\n\t"
        "v_exp_f32 v100, v100\n\t"
        "v_smfmac_f32_16x16x64_bf16 v[132:135], v[144:147], v[120:127], v148\n\t"
        "v_exp_f32 v101, v101\n\t"
        "v_mfma_f32_32x32x16_bf16 v[112:127], v[184:187], v[88:91], 0\n\t"
        "v_exp_f32 v102, v102\n\t"
        "v_exp_f32 v103, v103\n\t"
        "v_exp_f32 v104, v104\n\t"
        "v_mfma_f32_32x32x16_bf16 v[112:127], v[188:191], v[92:95], v[112:127]\n\t"
        "v_exp_f32 v105, v105\n\t"
        "v_exp_f32 v106, v106\n\t"
        "v_exp_f32 v107, v107\n\t"
        "v_smfmac_f32_32x32x32_bf16 v[32:47], v[192:195], v[96:103], v148\n\t"
        "v_exp_f32 v108, v108\n\t"
        "v_exp_f32 v109, v109\n\t"
        "v_exp_f32 v110, v110\n\t"
        "v_smfmac_f32_16x16x64_bf16 v[136:139], v[144:147], v[96:103], v148\n\t"
        "buffer_load_dwordx4 v[180:183], v150, s[40:43], s44 offen offset:3072 sc1\n\t"
        "v_exp_f32 v111, v111\n\t"
        "v_exp_f32 v112, v112\n\t"
        "v_exp_f32 v113, v113\n\t"
        "v_smfmac_f32_32x32x32_bf16 v[32:47], v[196:199], v[104:111], v148\n\t"
        "s_add_u32 s44, s44, 0x4000\n\t"
        "s_and_b32 s44, s44, 0x3ffff\n\t"
        "v_exp_f32 v114, v114\n\t"
        "v_exp_f32 v115, v115\n\t"
        "v_exp_f32 v116, v116\n\t"
        "v_smfmac_f32_16x16x64_bf16 v[136:139], v[144:147], v[104:111], v148\n\t"
        "v_exp_f32 v117, v117\n\t"
        "s_waitcnt vmcnt(8)\n\t"
        "v_mfma_f32_32x32x16_bf16 v[96:111], v[200:203], v[64:67], 0\n\t"
        "v_exp_f32 v118, v118\n\t"
        "v_exp_f32 v119, v119\n\t"
        "v_exp_f32 v120, v120\n\t"
        "v_mfma_f32_32x32x16_bf16 v[96:111], v[204:207], v[68:71], v[96:111]\n\t"
        "v_exp_f32 v121, v121\n\t"
        "v_exp_f32 v122, v122\n\t"
        "v_exp_f32 v123, v123\n\t"
        "v_smfmac_f32_32x32x32_bf16 v[48:63], v[192:195], v[112:119], v148\n\t"
        "v_exp_f32 v124, v124\n\t"
        "v_exp_f32 v125, v125\n\t"
        "v_exp_f32 v126, v126\n\t"
        "v_smfmac_f32_16x16x64_bf16 v[140:143], v[144:147], v[112:119], v148\n\t"
        "buffer_load_dwordx4 v[184:187], v149, s[40:43], s44 offen offset:0 sc1\n\t"
        "v_exp_f32 v127, v127\n\t"
        "v_exp_f32 v96, v96\n\t"
        "v_exp_f32 v97, v97\n\t"
        "v_smfmac_f32_32x32x32_bf16 v[48:63], v[196:199], v[120:127], v148\n\t"
        "v_exp_f32 v98, v98\n\t"
        "v_exp_f32 v99, v99\n\t"
        "v_exp_f32 v100, v100\n\t"
        "v_smfmac_f32_16x16x64_bf16 v[140:143], v[144:147], v[120:127], v148\n\t"
        "v_exp_f32 v101, v101\n\t"
        "v_mfma_f32_32x32x16_bf16 v[112:127], v[200:203], v[72:75], 0\n\t"
        "v_exp_f32 v102, v102\n\t"
        "v_exp_f32 v103, v103\n\t"
        "v_exp_f32 v104, v104\n\t"
        "v_mfma_f32_32x32x16_bf16 v[112:127], v[204:207], v[76:79], v[112:127]\n\t"
        "v_exp_f32 v105, v105\n\t"
        "v_exp_f32 v106, v106\n\t"
        "v_exp_f32 v107, v107\n\t"
        "v_smfmac_f32_32x32x32_bf16 v[0:15], v[208:211], v[96:103], v148\n\t"
        "v_exp_f32 v108, v108\n\t"
        "v_exp_f32 v109, v109\n\t"
        "v_exp_f32 v110, v110\n\t"
        "v_smfmac_f32_16x16x64_bf16 v[128:131], v[144:147], v[96:103], v148\n\t"
        "buffer_load_dwordx4 v[188:191], v149, s[40:43], s44 offen offset:1024 sc1\n\t"
        "v_exp_f32 v111, v111\n\t"
        "v_exp_f32 v112, v112\n\t"
        "v_exp_f32 v113, v113\n\t"
        "v_smfmac_f32_32x32x32_bf16 v[0:15], v[212:215], v[104:111], v148\n\t"
        "v_exp_f32 v114, v114\n\t"
        "v_exp_f32 v115, v115\n\t"
        "v_exp_f32 v116, v116\n\t"
        "v_smfmac_f32_16x16x64_bf16 v[128:131], v[144:147], v[104:111], v148\n\t"
        "v_exp_f32 v117, v117\n\t"
        "v_mfma_f32_32x32x16_bf16 v[96:111], v[200:203], v[80:83], 0\n\t"
        "v_exp_f32 v118, v118\n\t"
        "v_exp_f32 v119, v119\n\t"
        "v_exp_f32 v120, v120\n\t"
        "v_mfma_f32_32x32x16_bf16 v[96:111], v[204:207], v[84:87], v[96:111]\n\t"
        "v_exp_f32 v121, v121\n\t"
        "v_exp_f32 v122, v122\n\t"
        "v_exp_f32 v123, v123\n\t"
        "v_smfmac_f32_32x32x32_bf16 v[16:31], v[208:211], v[112:119], v148\n\t"
        "v_exp_f32 v124, v124\n\t"
        "v_exp_f32 v125, v125\n\t"
        "v_exp_f32 v126, v126\n\t"
        "v_smfmac_f32_16x16x64_bf16 v[132:135], v[144:147], v[112:119], v148\n\t"
        "buffer_load_dwordx4 v[192:195], v149, s[40:43], s44 offen offset:2048 sc1\n\t"
        "v_exp_f32 v127, v127\n\t"
        "v_exp_f32 v96, v96\n\t"
        "v_exp_f32 v97, v97\n\t"
        "v_smfmac_f32_32x32x32_bf16 v[16:31], v[212:215], v[120:127], v148\n\t"
        "v_exp_f32 v98, v98\n\t"
        "v_exp_f32 v99, v99\n\t"
        "v_exp_f32 v100, v100\n\t"
        "v_smfmac_f32_16x16x64_bf16 v[132:135], v[144:147], v[120:127], v148\n\t"
        "v_exp_f32 v101, v101\n\t"
        "v_mfma_f32_32x32x16_bf16 v[112:127], v[200:203], v[88:91], 0\n\t"
        "v_exp_f32 v102, v102\n\t"
        "v_exp_f32 v103, v103\n\t"
        "v_exp_f32 v104, v104\n\t"
        "v_mfma_f32_32x32x16_bf16 v[112:127], v[204:207], v[92:95], v[112:127]\n\t"
        "v_exp_f32 v105, v105\n\t"
        "v_exp_f32 v106, v106\n\t"
        "v_exp_f32 v107, v107\n\t"
        "v_smfmac_f32_32x32x32_bf16 v[32:47], v[208:211], v[96:103], v148\n\t"
        "v_exp_f32 v108, v108\n\t"
        "v_exp_f32 v109, v109\n\t"
        "v_exp_f32 v110, v110\n\t"
        "v_smfmac_f32_16x16x64_bf16 v[136:139], v[144:147], v[96:103], v148\n\t"
        "buffer_load_dwordx4 v[196:199], v149, s[40:43], s44 offen offset:3072 sc1\n\t"
        "v_exp_f32 v111, v111\n\t"
        "v_exp_f32 v112, v112\n\t"
        "v_exp_f32 v113, v113\n\t"
        "v_smfmac_f32_32x32x32_bf16 v[32:47], v[212:215], v[104:111], v148\n\t"
        "v_exp_f32 v114, v114\n\t"
        "v_exp_f32 v115, v115\n\t"
        "v_exp_f32 v116, v116\n\t"
        "v_smfmac_f32_16x16x64_bf16 v[136:139], v[144:147], v[104:111], v148\n\t"
        "v_exp_f32 v117, v117\n\t"
        "s_waitcnt vmcnt(8)\n\t"
        "v_mfma_f32_32x32x16_bf16 v[96:111], v[152:155], v[64:67], 0\n\t"
        "v_exp_f32 v118, v118\n\t"
        "v_exp_f32 v119, v119\n\t"
        "v_exp_f32 v120, v120\n\t"
        "v_mfma_f32_32x32x16_bf16 v[96:111], v[156:159], v[68:71], v[96:111]\n\t"
        "v_exp_f32 v121, v121\n\t"
        "v_exp_f32 v122, v122\n\t"
        "v_exp_f32 v123, v123\n\t"
        "v_smfmac_f32_32x32x32_bf16 v[48:63], v[208:211], v[112:119], v148\n\t"
        "v_exp_f32 v124, v124\n\t"
        "v_exp_f32 v125, v125\n\t"
        "v_exp_f32 v126, v126\n\t"
        "v_smfmac_f32_16x16x64_bf16 v[140:143], v[144:147], v[112:119], v148\n\t"
        "buffer_load_dwordx4 v[200:203], v150, s[40:43], s44 offen offset:0 sc1\n\t"
        "v_exp_f32 v127, v127\n\t"
        "v_exp_f32 v96, v96\n\t"
        "v_exp_f32 v97, v97\n\t"
        "v_smfmac_f32_32x32x32_bf16 v[48:63], v[212:215], v[120:127], v148\n\t"
        "v_exp_f32 v98, v98\n\t"
        "v_exp_f32 v99, v99\n\t"
        "v_exp_f32 v100, v100\n\t"
        "v_smfmac_f32_16x16x64_bf16 v[140:143], v[144:147], v[120:127], v148\n\t"
        "v_exp_f32 v101, v101\n\t"
        "v_mfma_f32_32x32x16_bf16 v[112:127], v[152:155], v[72:75], 0\n\t"
        "v_exp_f32 v102, v102\n\t"
        "v_exp_f32 v103, v103\n\t"
        "v_exp_f32 v104, v104\n\t"
        "v_mfma_f32_32x32x16_bf16 v[112:127], v[156:159], v[76:79], v[112:127]\n\t"
        "v_exp_f32 v105, v105\n\t"
        "v_exp_f32 v106, v106\n\t"
        "v_exp_f32 v107, v107\n\t"
        "v_smfmac_f32_32x32x32_bf16 v[0:15], v[160:163], v[96:103], v148\n\t"
        "v_exp_f32 v108, v108\n\t"
        "v_exp_f32 v109, v109\n\t"
        "v_exp_f32 v110, v110\n\t"
        "v_smfmac_f32_16x16x64_bf16 v[128:131], v[144:147], v[96:103], v148\n\t"
        "buffer_load_dwordx4 v[204:207], v150, s[40:43], s44 offen offset:1024 sc1\n\t"
        "v_exp_f32 v111, v111\n\t"
        "v_exp_f32 v112, v112\n\t"
        "v_exp_f32 v113, v113\n\t"
        "v_smfmac_f32_32x32x32_bf16 v[0:15], v[164:167], v[104:111], v148\n\t"
        "v_exp_f32 v114, v114\n\t"
        "v_exp_f32 v115, v115\n\t"
        "v_exp_f32 v116, v116\n\t"
        "v_smfmac_f32_16x16x64_bf16 v[128:131], v[144:147], v[104:111], v148\n\t"
        "v_exp_f32 v117, v117\n\t"
        "v_mfma_f32_32x32x16_bf16 v[96:111], v[152:155], v[80:83], 0\n\t"
        "v_exp_f32 v118, v118\n\t"
        "v_exp_f32 v119, v119\n\t"
        "v_exp_f32 v120, v120\n\t"
        "v_mfma_f32_32x32x16_bf16 v[96:111], v[156:159], v[84:87], v[96:111]\n\t"
        "v_exp_f32 v121, v121\n\t"
        "v_exp_f32 v122, v122\n\t"
        "v_exp_f32 v123, v123\n\t"
        "v_smfmac_f32_32x32x32_bf16 v[16:31], v[160:163], v[112:119], v148\n\t"
        "v_exp_f32 v124, v124\n\t"
        "v_exp_f32 v125, v125\n\t"
        "v_exp_f32 v126, v126\n\t"
        "v_smfmac_f32_16x16x64_bf16 v[132:135], v[144:147], v[112:119], v148\n\t"
        "buffer_load_dwordx4 v[208:211], v150, s[40:43], s44 offen offset:2048 sc1\n\t"
        "v_exp_f32 v127, v127\n\t"
        "v_exp_f32 v96, v96\n\t"
        "v_exp_f32 v97, v97\n\t"
        "v_smfmac_f32_32x32x32_bf16 v[16:31], v[164:167], v[120:127], v148\n\t"
        "v_exp_f32 v98, v98\n\t"
        "v_exp_f32 v99, v99\n\t"
        "v_exp_f32 v100, v100\n\t"
        "v_smfmac_f32_16x16x64_bf16 v[132:135], v[144:147], v[120:127], v148\n\t"
        "v_exp_f32 v101, v101\n\t"
        "v_mfma_f32_32x32x16_bf16 v[112:127], v[152:155], v[88:91], 0\n\t"
        "v_exp_f32 v102, v102\n\t"
        "v_exp_f32 v103, v103\n\t"
        "v_exp_f32 v104, v104\n\t"
        "v_mfma_f32_32x32x16_bf16 v[112:127], v[156:159], v[92:95], v[112:127]\n\t"
        "v_exp_f32 v105, v105\n\t"
        "v_exp_f32 v106, v106\n\t"
        "v_exp_f32 v107, v107\n\t"
        "v_smfmac_f32_32x32x32_bf16 v[32:47], v[160:163], v[96:103], v148\n\t"
        "v_exp_f32 v108, v108\n\t"
        "v_exp_f32 v109, v109\n\t"
        "v_exp_f32 v110, v110\n\t"
        "v_smfmac_f32_16x16x64_bf16 v[136:139], v[144:147], v[96:103], v148\n\t"
        "buffer_load_dwordx4 v[212:215], v150, s[40:43], s44 offen offset:3072 sc1\n\t"
        "v_exp_f32 v111, v111\n\t"
        "v_exp_f32 v112, v112\n\t"
        "v_exp_f32 v113, v113\n\t"
        "v_smfmac_f32_32x32x32_bf16 v[32:47], v[164:167], v[104:111], v148\n\t"
        "s_add_u32 s44, s44, 0x4000\n\t"
        "s_and_b32 s44, s44, 0x3ffff\n\t"
        "v_exp_f32 v114, v114\n\t"
        "v_exp_f32 v115, v115\n\t"
        "v_exp_f32 v116, v116\n\t"
        "v_smfmac_f32_16x16x64_bf16 v[136:139], v[144:147], v[104:111], v148\n\t"
        "v_exp_f32 v117, v117\n\t"
        "s_waitcnt vmcnt(8)\n\t"
        "v_mfma_f32_32x32x16_bf16 v[96:111], v[168:171], v[64:67], 0\n\t"
        "v_exp_f32 v118, v118\n\t"
        "v_exp_f32 v119, v119\n\t"
        "v_exp_f32 v120, v120\n\t"
        "v_mfma_f32_32x32x16_bf16 v[96:111], v[172:175], v[68:71], v[96:111]\n\t"
        "v_exp_f32 v121, v121\n\t"
        "v_exp_f32 v122, v122\n\t"
        "v_exp_f32 v123, v123\n\t"
        "v_smfmac_f32_32x32x32_bf16 v[48:63], v[160:163], v[112:119], v148\n\t"
        "v_exp_f32 v124, v124\n\t"
        "v_exp_f32 v125, v125\n\t"
        "v_exp_f32 v126, v126\n\t"
        "v_smfmac_f32_16x16x64_bf16 v[140:143], v[144:147], v[112:119], v148\n\t"
        "buffer_load_dwordx4 v[152:155], v149, s[40:43], s44 offen offset:0 sc1\n\t"
        "v_exp_f32 v127, v127\n\t"
        "v_exp_f32 v96, v96\n\t"
        "v_exp_f32 v97, v97\n\t"
        "v_smfmac_f32_32x32x32_bf16 v[48:63], v[164:167], v[120:127], v148\n\t"
        "v_exp_f32 v98, v98\n\t"
        "v_exp_f32 v99, v99\n\t"
        "v_exp_f32 v100, v100\n\t"
        "v_smfmac_f32_16x16x64_bf16 v[140:143], v[144:147], v[120:127], v148\n\t"
        "v_exp_f32 v101, v101\n\t"
        "v_mfma_f32_32x32x16_bf16 v[112:127], v[168:171], v[72:75], 0\n\t"
        "v_exp_f32 v102, v102\n\t"
        "v_exp_f32 v103, v103\n\t"
        "v_exp_f32 v104, v104\n\t"
        "v_mfma_f32_32x32x16_bf16 v[112:127], v[172:175], v[76:79], v[112:127]\n\t"
        "v_exp_f32 v105, v105\n\t"
        "v_exp_f32 v106, v106\n\t"
        "v_exp_f32 v107, v107\n\t"
        "v_smfmac_f32_32x32x32_bf16 v[0:15], v[176:179], v[96:103], v148\n\t"
        "v_exp_f32 v108, v108\n\t"
        "v_exp_f32 v109, v109\n\t"
        "v_exp_f32 v110, v110\n\t"
        "v_smfmac_f32_16x16x64_bf16 v[128:131], v[144:147], v[96:103], v148\n\t"
        "buffer_load_dwordx4 v[156:159], v149, s[40:43], s44 offen offset:1024 sc1\n\t"
        "v_exp_f32 v111, v111\n\t"
        "v_exp_f32 v112, v112\n\t"
        "v_exp_f32 v113, v113\n\t"
        "v_smfmac_f32_32x32x32_bf16 v[0:15], v[180:183], v[104:111], v148\n\t"
        "v_exp_f32 v114, v114\n\t"
        "v_exp_f32 v115, v115\n\t"
        "v_exp_f32 v116, v116\n\t"
        "v_smfmac_f32_16x16x64_bf16 v[128:131], v[144:147], v[104:111], v148\n\t"
        "v_exp_f32 v117, v117\n\t"
        "v_mfma_f32_32x32x16_bf16 v[96:111], v[168:171], v[80:83], 0\n\t"
        "v_exp_f32 v118, v118\n\t"
        "v_exp_f32 v119, v119\n\t"
        "v_exp_f32 v120, v120\n\t"
        "v_mfma_f32_32x32x16_bf16 v[96:111], v[172:175], v[84:87], v[96:111]\n\t"
        "v_exp_f32 v121, v121\n\t"
        "v_exp_f32 v122, v122\n\t"
        "v_exp_f32 v123, v123\n\t"
        "v_smfmac_f32_32x32x32_bf16 v[16:31], v[176:179], v[112:119], v148\n\t"
        "v_exp_f32 v124, v124\n\t"
        "v_exp_f32 v125, v125\n\t"
        "v_exp_f32 v126, v126\n\t"
        "v_smfmac_f32_16x16x64_bf16 v[132:135], v[144:147], v[112:119], v148\n\t"
        "buffer_load_dwordx4 v[160:163], v149, s[40:43], s44 offen offset:2048 sc1\n\t"
        "v_exp_f32 v127, v127\n\t"
        "v_exp_f32 v96, v96\n\t"
        "v_exp_f32 v97, v97\n\t"
        "v_smfmac_f32_32x32x32_bf16 v[16:31], v[180:183], v[120:127], v148\n\t"
        "v_exp_f32 v98, v98\n\t"
        "v_exp_f32 v99, v99\n\t"
        "v_exp_f32 v100, v100\n\t"
        "v_smfmac_f32_16x16x64_bf16 v[132:135], v[144:147], v[120:127], v148\n\t"
        "v_exp_f32 v101, v101\n\t"
        "v_mfma_f32_32x32x16_bf16 v[112:127], v[168:171], v[88:91], 0\n\t"
        "v_exp_f32 v102, v102\n\t"
        "v_exp_f32 v103, v103\n\t"
        "v_exp_f32 v104, v104\n\t"
        "v_mfma_f32_32x32x16_bf16 v[112:127], v[172:175], v[92:95], v[112:127]\n\t"
        "v_exp_f32 v105, v105\n\t"
        "v_exp_f32 v106, v106\n\t"
        "v_exp_f32 v107, v107\n\t"
        "v_smfmac_f32_32x32x32_bf16 v[32:47], v[176:179], v[96:103], v148\n\t"
        "v_exp_f32 v108, v108\n\t"
        "v_exp_f32 v109, v109\n\t"
        "v_exp_f32 v110, v110\n\t"
        "v_smfmac_f32_16x16x64_bf16 v[136:139], v[144:147], v[96:103], v148\n\t"
        "buffer_load_dwordx4 v[164:167], v149, s[40:43], s44 offen offset:3072 sc1\n\t"
        "v_exp_f32 v111, v111\n\t"
        "s_sub_u32 s45, s45, 1\n\t"
        "s_cmp_lg_u32 s45, 0\n\t"
        "s_cbranch_scc1 1b\n\t"
        "v_exp_f32 v112, v112\n\t"
        "v_exp_f32 v113, v113\n\t"
        "v_smfmac_f32_32x32x32_bf16 v[32:47], v[180:183], v[104:111], v148\n\t"
        "v_exp_f32 v114, v114\n\t"
        "v_exp_f32 v115, v115\n\t"
        "v_exp_f32 v116, v116\n\t"
        "v_smfmac_f32_16x16x64_bf16 v[136:139], v[144:147], v[104:111], v148\n\t"
        "v_exp_f32 v117, v117\n\t"
        "s_waitcnt vmcnt(8)\n\t"
        "v_mfma_f32_32x32x16_bf16 v[96:111], v[184:187], v[64:67], 0\n\t"
        "v_exp_f32 v118, v118\n\t"
        "v_exp_f32 v119, v119\n\t"
        "v_exp_f32 v120, v120\n\t"
        "v_mfma_f32_32x32x16_bf16 v[96:111], v[188:191], v[68:71], v[96:111]\n\t"
        "v_exp_f32 v121, v121\n\t"
        "v_exp_f32 v122, v122\n\t"
        "v_exp_f32 v123, v123\n\t"
        "v_smfmac_f32_32x32x32_bf16 v[48:63], v[176:179], v[112:119], v148\n\t"
        "v_exp_f32 v124, v124\n\t"
        "v_exp_f32 v125, v125\n\t"
        "v_exp_f32 v126, v126\n\t"
        "v_smfmac_f32_16x16x64_bf16 v[140:143], v[144:147], v[112:119], v148\n\t"
        "v_exp_f32 v127, v127\n\t"
        "v_exp_f32 v96, v96\n\t"
        "v_exp_f32 v97, v97\n\t"
        "v_smfmac_f32_32x32x32_bf16 v[48:63], v[180:183], v[120:127], v148\n\t"
        "v_exp_f32 v98, v98\n\t"
        "v_exp_f32 v99, v99\n\t"
        "v_exp_f32 v100, v100\n\t"
        "v_smfmac_f32_16x16x64_bf16 v[140:143], v[144:147], v[120:127], v148\n\t"
        "v_exp_f32 v101, v101\n\t"
        "v_mfma_f32_32x32x16_bf16 v[112:127], v[184:187], v[72:75], 0\n\t"
        "v_exp_f32 v102, v102\n\t"
        "v_exp_f32 v103, v103\n\t"
        "v_exp_f32 v104, v104\n\t"
        "v_mfma_f32_32x32x16_bf16 v[112:127], v[188:191], v[76:79], v[112:127]\n\t"
        "v_exp_f32 v105, v105\n\t"
        "v_exp_f32 v106, v106\n\t"
        "v_exp_f32 v107, v107\n\t"
        "v_smfmac_f32_32x32x32_bf16 v[0:15], v[192:195], v[96:103], v148\n\t"
        "v_exp_f32 v108, v108\n\t"
        "v_exp_f32 v109, v109\n\t"
        "v_exp_f32 v110, v110\n\t"
        "v_smfmac_f32_16x16x64_bf16 v[128:131], v[144:147], v[96:103], v148\n\t"
        "v_exp_f32 v111, v111\n\t"
        "v_exp_f32 v112, v112\n\t"
        "v_exp_f32 v113, v113\n\t"
        "v_smfmac_f32_32x32x32_bf16 v[0:15], v[196:199], v[104:111], v148\n\t"
        "v_exp_f32 v114, v114\n\t"
        "v_exp_f32 v115, v115\n\t"
        "v_exp_f32 v116, v116\n\t"
        "v_smfmac_f32_16x16x64_bf16 v[128:131], v[144:147], v[104:111], v148\n\t"
        "v_exp_f32 v117, v117\n\t"
        "s_waitcnt vmcnt(4)\n\t"
        "v_mfma_f32_32x32x16_bf16 v[96:111], v[200:203], v[64:67], 0\n\t"
        "v_exp_f32 v118, v118\n\t"
        "v_exp_f32 v119, v119\n\t"
        "v_exp_f32 v120, v120\n\t"
        "v_mfma_f32_32x32x16_bf16 v[96:111], v[204:207], v[68:71], v[96:111]\n\t"
        "v_exp_f32 v121, v121\n\t"
        "v_exp_f32 v122, v122\n\t"
        "v_exp_f32 v123, v123\n\t"
        "v_smfmac_f32_32x32x32_bf16 v[16:31], v[192:195], v[112:119], v148\n\t"
        "v_exp_f32 v124, v124\n\t"
        "v_exp_f32 v125, v125\n\t"
        "v_exp_f32 v126, v126\n\t"
        "v_smfmac_f32_16x16x64_bf16 v[132:135], v[144:147], v[112:119], v148\n\t"
        "v_exp_f32 v127, v127\n\t"
        "s_cmp_lg_u32 s47, 0\n\t"
        "s_cbranch_scc1 4f\n\t"
        "global_load_dwordx4 %[r0], v223, %[xb] offset:0\n\t"
        "global_load_dwordx4 %[r1], v223, %[xb] offset:32\n\t"
        "global_load_dwordx4 %[r2], v223, %[xb] offset:64\n\t"
        "global_load_dwordx4 %[r3], v223, %[xb] offset:96\n\t"
        "4:\n\t"
        "v_exp_f32 v96, v96\n\t"
        "v_exp_f32 v97, v97\n\t"
        "v_smfmac_f32_32x32x32_bf16 v[16:31], v[196:199], v[120:127], v148\n\t"
        "v_exp_f32 v98, v98\n\t"
        "v_exp_f32 v99, v99\n\t"
        "v_exp_f32 v100, v100\n\t"
        "v_smfmac_f32_16x16x64_bf16 v[132:135], v[144:147], v[120:127], v148\n\t"
        "v_exp_f32 v101, v101\n\t"
        "v_mfma_f32_32x32x16_bf16 v[112:127], v[200:203], v[72:75], 0\n\t"
        "v_exp_f32 v102, v102\n\t"
        "v_exp_f32 v103, v103\n\t"
        "v_exp_f32 v104, v104\n\t"
        "v_mfma_f32_32x32x16_bf16 v[112:127], v[204:207], v[76:79], v[112:127]\n\t"
        "v_exp_f32 v105, v105\n\t"
        "v_exp_f32 v106, v106\n\t"
        "v_exp_f32 v107, v107\n\t"
        "v_smfmac_f32_32x32x32_bf16 v[0:15], v[208:211], v[96:103], v148\n\t"
        "v_exp_f32 v108, v108\n\t"
        "v_exp_f32 v109, v109\n\t"
        "v_exp_f32 v110, v110\n\t"
        "v_smfmac_f32_16x16x64_bf16 v[128:131], v[144:147], v[96:103], v148\n\t"
        "v_exp_f32 v111, v111\n\t"
        "v_exp_f32 v112, v112\n\t"
        "v_exp_f32 v113, v113\n\t"
        "v_smfmac_f32_32x32x32_bf16 v[0:15], v[212:215], v[104:111], v148\n\t"
        "v_exp_f32 v114, v114\n\t"
        "v_exp_f32 v115, v115\n\t"
        "v_exp_f32 v116, v116\n\t"
        "v_smfmac_f32_16x16x64_bf16 v[128:131], v[144:147], v[104:111], v148\n\t"
        "v_exp_f32 v117, v117\n\t"
        "v_mfma_f32_32x32x16_bf16 v[96:111], v[184:187], v[80:83], 0\n\t"
        "v_exp_f32 v118, v118\n\t"
        "v_exp_f32 v119, v119\n\t"
        "v_exp_f32 v120, v120\n\t"
        "ds_write_b128 v216, v[0:3] offset:0\n\t"
        "v_mfma_f32_32x32x16_bf16 v[96:111], v[188:191], v[84:87], v[96:111]\n\t"
        "v_exp_f32 v121, v121\n\t"
        "ds_write_b128 v216, v[4:7] offset:1024\n\t"
        "v_exp_f32 v122, v122\n\t"
        "ds_write_b128 v216, v[8:11] offset:2048\n\t"
        "v_exp_f32 v123, v123\n\t"
        "ds_write_b128 v216, v[12:15] offset:3072\n\t"
        "v_smfmac_f32_32x32x32_bf16 v[16:31], v[208:211], v[112:119], v148\n\t"
        "v_exp_f32 v124, v124\n\t"
        "ds_write_b32 v218, v128 offset:0\n\t"
        "v_exp_f32 v125, v125\n\t"
        "v_exp_f32 v126, v126\n\t"
        "v_smfmac_f32_16x16x64_bf16 v[132:135], v[144:147], v[112:119], v148\n\t"
        "v_exp_f32 v127, v127\n\t"
        "v_exp_f32 v96, v96\n\t"
        "v_exp_f32 v97, v97\n\t"
        "v_smfmac_f32_32x32x32_bf16 v[16:31], v[212:215], v[120:127], v148\n\t"
        "v_exp_f32 v98, v98\n\t"
        "v_exp_f32 v99, v99\n\t"
        "v_exp_f32 v100, v100\n\t"
        "v_smfmac_f32_16x16x64_bf16 v[132:135], v[144:147], v[120:127], v148\n\t"
        "v_exp_f32 v101, v101\n\t"
        "v_mfma_f32_32x32x16_bf16 v[112:127], v[184:187], v[88:91], 0\n\t"
        "v_exp_f32 v102, v102\n\t"
        "v_exp_f32 v103, v103\n\t"
        "v_exp_f32 v104, v104\n\t"
        "ds_write_b128 v216, v[16:19] offset:4096\n\t"
        "v_mfma_f32_32x32x16_bf16 v[112:127], v[188:191], v[92:95], v[112:127]\n\t"
        "v_exp_f32 v105, v105\n\t"
        "ds_write_b128 v216, v[20:23] offset:5120\n\t"
        "v_exp_f32 v106, v106\n\t"
        "ds_write_b128 v216, v[24:27] offset:6144\n\t"
        "v_exp_f32 v107, v107\n\t"
        "ds_write_b128 v216, v[28:31] offset:7168\n\t"
        "v_smfmac_f32_32x32x32_bf16 v[32:47], v[192:195], v[96:103], v148\n\t"
        "v_exp_f32 v108, v108\n\t"
        "ds_write_b32 v218, v132 offset:256\n\t"
        "v_exp_f32 v109, v109\n\t"
        "ds_write_b32 v220, v222\n\t"
        "v_exp_f32 v110, v110\n\t"
        "v_smfmac_f32_16x16x64_bf16 v[136:139], v[144:147], v[96:103], v148\n\t"
        "v_exp_f32 v111, v111\n\t"
        "v_exp_f32 v112, v112\n\t"
        "v_exp_f32 v113, v113\n\t"
        "v_smfmac_f32_32x32x32_bf16 v[32:47], v[196:199], v[104:111], v148\n\t"
        "v_exp_f32 v114, v114\n\t"
        "v_exp_f32 v115, v115\n\t"
        "v_exp_f32 v116, v116\n\t"
        "v_smfmac_f32_16x16x64_bf16 v[136:139], v[144:147], v[104:111], v148\n\t"
        "v_exp_f32 v117, v117\n\t"
        "v_mfma_f32_32x32x16_bf16 v[96:111], v[200:203], v[80:83], 0\n\t"
        "v_exp_f32 v118, v118\n\t"
        "v_exp_f32 v119, v119\n\t"
        "v_exp_f32 v120, v120\n\t"
        "v_mfma_f32_32x32x16_bf16 v[96:111], v[204:207], v[84:87], v[96:111]\n\t"
        "v_exp_f32 v121, v121\n\t"
        "v_exp_f32 v122, v122\n\t"
        "v_exp_f32 v123, v123\n\t"
        "v_smfmac_f32_32x32x32_bf16 v[48:63], v[192:195], v[112:119], v148\n\t"
        "v_exp_f32 v124, v124\n\t"
        "v_exp_f32 v125, v125\n\t"
        "v_exp_f32 v126, v126\n\t"
        "v_smfmac_f32_16x16x64_bf16 v[140:143], v[144:147], v[112:119], v148\n\t"
        "v_exp_f32 v127, v127\n\t"
        "v_exp_f32 v96, v96\n\t"
        "v_exp_f32 v97, v97\n\t"
        "v_smfmac_f32_32x32x32_bf16 v[48:63], v[196:199], v[120:127], v148\n\t"
        "v_exp_f32 v98, v98\n\t"
        "v_exp_f32 v99, v99\n\t"
        "v_exp_f32 v100, v100\n\t"
        "v_smfmac_f32_16x16x64_bf16 v[140:143], v[144:147], v[120:127], v148\n\t"
        "v_exp_f32 v101, v101\n\t"
        "v_mfma_f32_32x32x16_bf16 v[112:127], v[200:203], v[88:91], 0\n\t"
        "v_exp_f32 v102, v102\n\t"
        "v_exp_f32 v103, v103\n\t"
        "v_exp_f32 v104, v104\n\t"
        "ds_read_b32 v151, v221\n\t"
        "v_mfma_f32_32x32x16_bf16 v[112:127], v[204:207], v[92:95], v[112:127]\n\t"
        "v_exp_f32 v105, v105\n\t"
        "v_exp_f32 v106, v106\n\t"
        "v_exp_f32 v107, v107\n\t"
        "v_smfmac_f32_32x32x32_bf16 v[32:47], v[208:211], v[96:103], v148\n\t"
        "v_exp_f32 v108, v108\n\t"
        "v_exp_f32 v109, v109\n\t"
        "v_exp_f32 v110, v110\n\t"
        "v_smfmac_f32_16x16x64_bf16 v[136:139], v[144:147], v[96:103], v148\n\t"
        "v_exp_f32 v111, v111\n\t"
        "s_waitcnt lgkmcnt(0)\n\t"
        "v_cmp_ne_u32 vcc, v222, v151\n\t"
        "s_cbranch_vccnz 40f\n\t"
        "41:\n\t"
        "v_exp_f32 v112, v112\n\t"
        "ds_read_b128 v[0:3], v217 offset:0\n\t"
        "v_exp_f32 v113, v113\n\t"
        "ds_read_b128 v[4:7], v217 offset:1024\n\t"
        "v_smfmac_f32_32x32x32_bf16 v[32:47], v[212:215], v[104:111], v148\n\t"
        "v_exp_f32 v114, v114\n\t"
        "ds_read_b128 v[8:11], v217 offset:2048\n\t"
        "v_exp_f32 v115, v115\n\t"
        "ds_read_b128 v[12:15], v217 offset:3072\n\t"
        "v_exp_f32 v116, v116\n\t"
        "ds_read_b32 v128, v219 offset:0\n\t"
        "v_smfmac_f32_16x16x64_bf16 v[136:139], v[144:147], v[104:111], v148\n\t"
        "v_exp_f32 v117, v117\n\t"
        "ds_read_b128 v[16:19], v217 offset:4096\n\t"
        "v_exp_f32 v118, v118\n\t"
        "ds_read_b128 v[20:23], v217 offset:5120\n\t"
        "v_exp_f32 v119, v119\n\t"
        "ds_read_b128 v[24:27], v217 offset:6144\n\t"
        "v_exp_f32 v120, v120\n\t"
        "ds_read_b128 v[28:31], v217 offset:7168\n\t"
        "v_exp_f32 v121, v121\n\t"
        "ds_read_b32 v132, v219 offset:256\n\t"
        "v_exp_f32 v122, v122\n\t"
        "v_exp_f32 v123, v123\n\t"
        "v_smfmac_f32_32x32x32_bf16 v[48:63], v[208:211], v[112:119], v148\n\t"
        "v_exp_f32 v124, v124\n\t"
        "v_exp_f32 v125, v125\n\t"
        "v_exp_f32 v126, v126\n\t"
        "v_smfmac_f32_16x16x64_bf16 v[140:143], v[144:147], v[112:119], v148\n\t"
        "v_exp_f32 v127, v127\n\t"
        "s_nop 1\n\t"
        "v_smfmac_f32_32x32x32_bf16 v[48:63], v[212:215], v[120:127], v148\n\t"
        "v_smfmac_f32_16x16x64_bf16 v[140:143], v[144:147], v[120:127], v148\n\t"
        "s_waitcnt vmcnt(0) lgkmcnt(0)\n\t"
        "s_nop 6\n\t"
        "s_branch 9f\n\t"
        "30:\n\t"
        "s_add_u32 s44, s44, 0x4000\n\t"
        "s_and_b32 s44, s44, 0x3ffff\n\t"
        "buffer_load_dwordx4 v[184:187], v149, s[40:43], s44 offen offset:0 sc1\n\t"
        "buffer_load_dwordx4 v[188:191], v149, s[40:43], s44 offen offset:1024 sc1\n\t"
        "buffer_load_dwordx4 v[192:195], v149, s[40:43], s44 offen offset:2048 sc1\n\t"
        "buffer_load_dwordx4 v[196:199], v149, s[40:43], s44 offen offset:3072 sc1\n\t"
        "buffer_load_dwordx4 v[200:203], v150, s[40:43], s44 offen offset:0 sc1\n\t"
        "buffer_load_dwordx4 v[204:207], v150, s[40:43], s44 offen offset:1024 sc1\n\t"
        "buffer_load_dwordx4 v[208:211], v150, s[40:43], s44 offen offset:2048 sc1\n\t"
        "buffer_load_dwordx4 v[212:215], v150, s[40:43], s44 offen offset:3072 sc1\n\t"
        "s_add_u32 s44, s44, 0x4000\n\t"
        "s_and_b32 s44, s44, 0x3ffff\n\t"
        "buffer_load_dwordx4 v[152:155], v149, s[40:43], s44 offen offset:0 sc1\n\t"
        "buffer_load_dwordx4 v[156:159], v149, s[40:43], s44 offen offset:1024 sc1\n\t"
        "buffer_load_dwordx4 v[160:163], v149, s[40:43], s44 offen offset:2048 sc1\n\t"
        "buffer_load_dwordx4 v[164:167], v149, s[40:43], s44 offen offset:3072 sc1\n\t"
        "s_mov_b32 s46, 1\n\t"
        "s_branch 31b\n\t"
        "32:\n\t"
        "s_add_u32 s44, s44, 0x4000\n\t"
        "s_and_b32 s44, s44, 0x3ffff\n\t"
        "buffer_load_dwordx4 v[184:187], v149, s[40:43], s44 offen offset:0 sc1\n\t"
        "buffer_load_dwordx4 v[188:191], v149, s[40:43], s44 offen offset:1024 sc1\n\t"
        "buffer_load_dwordx4 v[192:195], v149, s[40:43], s44 offen offset:2048 sc1\n\t"
        "buffer_load_dwordx4 v[196:199], v149, s[40:43], s44 offen offset:3072 sc1\n\t"
        "buffer_load_dwordx4 v[200:203], v150, s[40:43], s44 offen offset:0 sc1\n\t"
        "buffer_load_dwordx4 v[204:207], v150, s[40:43], s44 offen offset:1024 sc1\n\t"
        "buffer_load_dwordx4 v[208:211], v150, s[40:43], s44 offen offset:2048 sc1\n\t"
        "buffer_load_dwordx4 v[212:215], v150, s[40:43], s44 offen offset:3072 sc1\n\t"
        "s_add_u32 s44, s44, 0x4000\n\t"
        "s_and_b32 s44, s44, 0x3ffff\n\t"
        "buffer_load_dwordx4 v[152:155], v149, s[40:43], s44 offen offset:0 sc1\n\t"
        "buffer_load_dwordx4 v[156:159], v149, s[40:43], s44 offen offset:1024 sc1\n\t"
        "buffer_load_dwordx4 v[160:163], v149, s[40:43], s44 offen offset:2048 sc1\n\t"
        "buffer_load_dwordx4 v[164:167], v149, s[40:43], s44 offen offset:3072 sc1\n\t"
        "s_mov_b32 s46, 1\n\t"
        "s_branch 33b\n\t"
        "40:\n\t"
        "s_mov_b32 s45, 0x100000\n\t"
        "42:\n\t"
        "ds_read_b32 v151, v221\n\t"
        "s_sub_u32 s45, s45, 1\n\t"
        "s_waitcnt lgkmcnt(0)\n\t"
        "s_cbranch_scc1 41b\n\t"
        "v_cmp_ne_u32 vcc, v222, v151\n\t"
        "s_cbranch_vccnz 42b\n\t"
        "s_branch 41b\n\t"
        "9:\n\t"
        : "+{v[0:15]}"(o0), "+{v[16:31]}"(o1), "+{v[32:47]}"(o2), "+{v[48:63]}"(o3), "+{v[128:143]}"(lv), "+{s44}"(soff), "+{v[152:167]}"(kva), "+{v[168:183]}"(kvb), "+{v[184:191]}"(fv),
          [r0] "+v"(x1p[0]), [r1] "+v"(x1p[1]), [r2] "+v"(x1p[2]), [r3] "+v"(x1p[3]) STAMP_OUTS
        : "{v[64:71]}"(q[0]), "{v[72:79]}"(q[1]), "{v[80:87]}"(q[2]), "{v[88:95]}"(q[3]), "{v[144:147]}"(ones), "{v148}"(0xddddu), "{v149}"(kvoff), "{v150}"(kvoff + 4096),
          "{v[216:223]}"(cv), "{v224}"(lbase + L_W + 16), "{s[40:43]}"(srd), "{s47}"(L), [xb] "s"(x0)
        : "v96", "v97", "v98", "v99", "v100", "v101", "v102", "v103", "v104", "v105", "v106", "v107", "v108", "v109", "v110", "v111", "v112", "v113", "v114", "v115", "v116", "v117", "v118", "v119", "v120", "v121", "v122", "v123", "v124", "v125", "v126", "v127", "v151", "v192", "v193", "v194", "v195", "v196", "v197", "v198", "v199", "v200", "v201", "v202", "v203", "v204", "v205", "v206", "v207", "v208", "v209", "v210", "v211", "v212", "v213", "v214", "v215", "s45", "s46", "vcc", "scc", "memory");
      STAMP_PUT(6 + 16 * L);
      STAMPV(9 + 16 * L);
      PHASE_VARS;
      const unsigned dst = OFF_OF + L * (5 * MBY) + (tm * 4 + 2 * kh) * TS + (tn * 2 + hh) * 2048 + lane * 16;
      auto put = [&](int u, const f32x16& o) {
#pragma unroll
        for (int c = 0; c < 2; ++c) {
          u32x4 w;
#pragma unroll
          for (int e = 0; e < 4; ++e) w[e] = pack2(o[8 * c + 2 * e], o[8 * c + 2 * e + 1]);
          stg(rs, dst + u * TS + c * 1024, w);
        }
      };
      const float la = lv[8] + lv[0], lb = lv[12] + lv[4];
      o2 = (o2 + o0) * RCP(la); put(0, o2);
      SBAR();
      o3 = (o3 + o1) * RCP(lb); put(1, o3);
      SBAR();
      {
        const f32x16 t = o2 + o3;
        float sum = 0.f;
#pragma unroll
        for (int e = 0; e < 16; e += 4) sum += (t[e] + t[e + 1]) + (t[e + 2] + t[e + 3]);
        if (__any(!(fabsf(sum) < 1e37f) | !(fmaxf(la, lb) < 1e27f) | !(fminf(la, lb) > 1e-9f) | !(la + lb == la + lb))) {
          DRAIN();
          attn_safe(rs, kvoff - kh * 8192, q[2], ones, o2); put(0, o2);
          attn_safe(rs, kvoff - kh * 8192, q[3], ones, o3); put(1, o3);
        }
      }
    }
    STAMPV(10 + 16 * L);
    {
      f32x16 acc[2];
      u32x4 res[4];
      u32x2 wrc[8];
      STAMP_DECL;
      { PHASE_VARS;
      BAR_ALL();
      STAMPV(11 + 16 * L);
#pragma unroll
      for (int g = 0; g < 8; ++g) wrc[g] = *reinterpret_cast<const u32x2*>(smem + L_WR + g * 16 + hi * 8);
      if (L == 0) {
#pragma unroll
        for (int i = 0; i < 4; ++i) res[i] = fo_swap(x1p[i]);
      } else {
        const bool routed = (int)words[3] == tile * 32 + r32;
        const float mb1 = reinterpret_cast<const float*>(smem + L_AB)[256 + wid * 64 + r32], rd1 = reinterpret_cast<const float*>(smem + L_AB)[256 + wid * 64 + 32 + r32];
#pragma unroll
        for (int i = 0; i < 4; ++i) {
          const u32x4 pr = *reinterpret_cast<const u32x4*>(smem + L_X2P + (i * 2 + hi) * 16);
#pragma unroll
          for (int e = 0; e < 4; ++e) {
            const unsigned h2 = ln_pair(x1p[i][e], mb1, rd1);
            unsigned t = pack2(lo16(x1p[i][e]) + lo16(h2), hi16(x1p[i][e]) + hi16(h2));
            PIN(t);
            res[i][e] = routed ? pr[e] : t;
          }
        }
      }
#pragma unroll
      for (int g = 0; g < 8; ++g) PIN(wrc[g]);
#pragma unroll
      for (int i = 0; i < 4; ++i) PIN(res[i]);
      unsigned soff = tn * 4096;
      const unsigned a0 = lbase + wrow * 128 + ((wch ^ wsw) << 4) + 8 * (tp & 1), a1 = lbase + wrow * 128 + (((4 + wch) ^ wsw) << 4) + 8 * (tp & 1);
      asm volatile(
        "ds_read_b64_tr_b16 v[32:33], v193 offset:0\n\t"
        "ds_read_b64_tr_b16 v[34:35], v193 offset:1024\n\t"
        "ds_read_b64_tr_b16 v[36:37], v194 offset:0\n\t"
        "ds_read_b64_tr_b16 v[38:39], v194 offset:1024\n\t"
        "ds_read_b64_tr_b16 v[40:41], v193 offset:2048\n\t"
        "ds_read_b64_tr_b16 v[42:43], v193 offset:3072\n\t"
        "ds_read_b64_tr_b16 v[44:45], v194 offset:2048\n\t"
        "ds_read_b64_tr_b16 v[46:47], v194 offset:3072\n\t"
        "ds_read_b64_tr_b16 v[48:49], v193 offset:4096\n\t"
        "ds_read_b64_tr_b16 v[50:51], v193 offset:5120\n\t"
        "ds_read_b64_tr_b16 v[52:53], v194 offset:4096\n\t"
        "ds_read_b64_tr_b16 v[54:55], v194 offset:5120\n\t"
        "ds_read_b64_tr_b16 v[56:57], v193 offset:6144\n\t"
        "ds_read_b64_tr_b16 v[58:59], v193 offset:7168\n\t"
        "ds_read_b64_tr_b16 v[60:61], v194 offset:6144\n\t"
        "ds_read_b64_tr_b16 v[62:63], v194 offset:7168\n\t"
        "s_mov_b32 s45, 0x200000\n\t"
        "2:\n\t"
        "ds_read_b32 v200, v198\n\t"
        "s_sub_u32 s45, s45, 1\n\t"
        "s_waitcnt lgkmcnt(0)\n\t"
        "s_cbranch_scc1 3f\n\t"
        "v_cmp_ne_u32 vcc, v199, v200\n\t"
        "s_cbranch_vccnz 2b\n\t"
        "3:\n\t"
        "buffer_load_dwordx4 v[64:67], v192, s[40:43], s44 offen offset:0 sc1\n\t"
        "buffer_load_dwordx4 v[68:71], v192, s[40:43], s44 offen offset:1024 sc1\n\t"
        "buffer_load_dwordx4 v[72:75], v192, s[40:43], s44 offen offset:2048 sc1\n\t"
        "buffer_load_dwordx4 v[76:79], v192, s[40:43], s44 offen offset:3072 sc1\n\t"
        "s_add_u32 s44, s44, 0x1000\n\t"
        "s_and_b32 s44, s44, 0xffff\n\t"
        "buffer_load_dwordx4 v[80:83], v192, s[40:43], s44 offen offset:0 sc1\n\t"
        "buffer_load_dwordx4 v[84:87], v192, s[40:43], s44 offen offset:1024 sc1\n\t"
        "buffer_load_dwordx4 v[88:91], v192, s[40:43], s44 offen offset:2048 sc1\n\t"
        "buffer_load_dwordx4 v[92:95], v192, s[40:43], s44 offen offset:3072 sc1\n\t"
        "s_add_u32 s44, s44, 0x1000\n\t"
        "s_and_b32 s44, s44, 0xffff\n\t"
        "buffer_load_dwordx4 v[96:99], v192, s[40:43], s44 offen offset:0 sc1\n\t"
        "buffer_load_dwordx4 v[100:103], v192, s[40:43], s44 offen offset:1024 sc1\n\t"
        "buffer_load_dwordx4 v[104:107], v192, s[40:43], s44 offen offset:2048 sc1\n\t"
        "buffer_load_dwordx4 v[108:111], v192, s[40:43], s44 offen offset:3072 sc1\n\t"
        "s_waitcnt vmcnt(11) lgkmcnt(14)\n\t"
        "v_mfma_f32_32x32x16_bf16 v[0:15], v[32:35], v[64:67], 0\n\t"
        "ds_read_b64_tr_b16 v[32:33], v193 offset:8192\n\t"
        "ds_read_b64_tr_b16 v[34:35], v193 offset:9216\n\t"
        "s_waitcnt lgkmcnt(14)\n\t"
        "v_mfma_f32_32x32x16_bf16 v[16:31], v[36:39], v[64:67], 0\n\t"
        "ds_read_b64_tr_b16 v[36:37], v194 offset:8192\n\t"
        "ds_read_b64_tr_b16 v[38:39], v194 offset:9216\n\t"
        "s_add_u32 s44, s44, 0x1000\n\t"
        "s_and_b32 s44, s44, 0xffff\n\t"
        "buffer_load_dwordx4 v[112:115], v192, s[40:43], s44 offen offset:0 sc1\n\t"
        "s_waitcnt vmcnt(11) lgkmcnt(14)\n\t"
        "v_mfma_f32_32x32x16_bf16 v[0:15], v[40:43], v[68:71], v[0:15]\n\t"
        "ds_read_b64_tr_b16 v[40:41], v193 offset:10240\n\t"
        "ds_read_b64_tr_b16 v[42:43], v193 offset:11264\n\t"
        "s_waitcnt lgkmcnt(14)\n\t"
        "v_mfma_f32_32x32x16_bf16 v[16:31], v[44:47], v[68:71], v[16:31]\n\t"
        "ds_read_b64_tr_b16 v[44:45], v194 offset:10240\n\t"
        "ds_read_b64_tr_b16 v[46:47], v194 offset:11264\n\t"
        "buffer_load_dwordx4 v[116:119], v192, s[40:43], s44 offen offset:1024 sc1\n\t"
        "s_waitcnt vmcnt(11) lgkmcnt(14)\n\t"
        "v_mfma_f32_32x32x16_bf16 v[0:15], v[48:51], v[72:75], v[0:15]\n\t"
        "ds_read_b64_tr_b16 v[48:49], v193 offset:12288\n\t"
        "ds_read_b64_tr_b16 v[50:51], v193 offset:13312\n\t"
        "s_waitcnt lgkmcnt(14)\n\t"
        "v_mfma_f32_32x32x16_bf16 v[16:31], v[52:55], v[72:75], v[16:31]\n\t"
        "ds_read_b64_tr_b16 v[52:53], v194 offset:12288\n\t"
        "ds_read_b64_tr_b16 v[54:55], v194 offset:13312\n\t"
        "buffer_load_dwordx4 v[120:123], v192, s[40:43], s44 offen offset:2048 sc1\n\t"
        "s_waitcnt vmcnt(11) lgkmcnt(14)\n\t"
        "v_mfma_f32_32x32x16_bf16 v[0:15], v[56:59], v[76:79], v[0:15]\n\t"
        "ds_read_b64_tr_b16 v[56:57], v193 offset:14336\n\t"
        "ds_read_b64_tr_b16 v[58:59], v193 offset:15360\n\t"
        "s_waitcnt lgkmcnt(14)\n\t"
        "v_mfma_f32_32x32x16_bf16 v[16:31], v[60:63], v[76:79], v[16:31]\n\t"
        "ds_read_b64_tr_b16 v[60:61], v194 offset:14336\n\t"
        "ds_read_b64_tr_b16 v[62:63], v194 offset:15360\n\t"
        "buffer_load_dwordx4 v[124:127], v192, s[40:43], s44 offen offset:3072 sc1\n\t"
        "s_waitcnt vmcnt(11) lgkmcnt(14)\n\t"
        "v_mfma_f32_32x32x16_bf16 v[0:15], v[32:35], v[80:83], v[0:15]\n\t"
        "ds_read_b64_tr_b16 v[32:33], v193 offset:16384\n\t"
        "ds_read_b64_tr_b16 v[34:35], v193 offset:17408\n\t"
        "s_waitcnt lgkmcnt(14)\n\t"
        "v_mfma_f32_32x32x16_bf16 v[16:31], v[36:39], v[80:83], v[16:31]\n\t"
        "ds_read_b64_tr_b16 v[36:37], v194 offset:16384\n\t"
        "ds_read_b64_tr_b16 v[38:39], v194 offset:17408\n\t"
        "s_add_u32 s44, s44, 0x1000\n\t"
        "s_and_b32 s44, s44, 0xffff\n\t"
        "buffer_load_dwordx4 v[128:131], v192, s[40:43], s44 offen offset:0 sc1\n\t"
        "s_waitcnt vmcnt(11) lgkmcnt(14)\n\t"
        "v_mfma_f32_32x32x16_bf16 v[0:15], v[40:43], v[84:87], v[0:15]\n\t"
        "ds_read_b64_tr_b16 v[40:41], v193 offset:18432\n\t"
        "ds_read_b64_tr_b16 v[42:43], v193 offset:19456\n\t"
        "s_waitcnt lgkmcnt(14)\n\t"
        "v_mfma_f32_32x32x16_bf16 v[16:31], v[44:47], v[84:87], v[16:31]\n\t"
        "ds_read_b64_tr_b16 v[44:45], v194 offset:18432\n\t"
        "ds_read_b64_tr_b16 v[46:47], v194 offset:19456\n\t"
        "buffer_load_dwordx4 v[132:135], v192, s[40:43], s44 offen offset:1024 sc1\n\t"
        "s_waitcnt vmcnt(11) lgkmcnt(14)\n\t"
        "v_mfma_f32_32x32x16_bf16 v[0:15], v[48:51], v[88:91], v[0:15]\n\t"
        "ds_read_b64_tr_b16 v[48:49], v193 offset:20480\n\t"
        "ds_read_b64_tr_b16 v[50:51], v193 offset:21504\n\t"
        "s_waitcnt lgkmcnt(14)\n\t"
        "v_mfma_f32_32x32x16_bf16 v[16:31], v[52:55], v[88:91], v[16:31]\n\t"
        "ds_read_b64_tr_b16 v[52:53], v194 offset:20480\n\t"
        "ds_read_b64_tr_b16 v[54:55], v194 offset:21504\n\t"
        "buffer_load_dwordx4 v[136:139], v192, s[40:43], s44 offen offset:2048 sc1\n\t"
        "s_waitcnt vmcnt(11) lgkmcnt(14)\n\t"
        "v_mfma_f32_32x32x16_bf16 v[0:15], v[56:59], v[92:95], v[0:15]\n\t"
        "ds_read_b64_tr_b16 v[56:57], v193 offset:22528\n\t"
        "ds_read_b64_tr_b16 v[58:59], v193 offset:23552\n\t"
        "s_waitcnt lgkmcnt(14)\n\t"
        "v_mfma_f32_32x32x16_bf16 v[16:31], v[60:63], v[92:95], v[16:31]\n\t"
        "ds_read_b64_tr_b16 v[60:61], v194 offset:22528\n\t"
        "ds_read_b64_tr_b16 v[62:63], v194 offset:23552\n\t"
        "buffer_load_dwordx4 v[140:143], v192, s[40:43], s44 offen offset:3072 sc1\n\t"
        "s_waitcnt vmcnt(11) lgkmcnt(14)\n\t"
        "v_mfma_f32_32x32x16_bf16 v[0:15], v[32:35], v[96:99], v[0:15]\n\t"
        "ds_read_b64_tr_b16 v[32:33], v193 offset:24576\n\t"
        "ds_read_b64_tr_b16 v[34:35], v193 offset:25600\n\t"
        "s_waitcnt lgkmcnt(14)\n\t"
        "v_mfma_f32_32x32x16_bf16 v[16:31], v[36:39], v[96:99], v[16:31]\n\t"
        "ds_read_b64_tr_b16 v[36:37], v194 offset:24576\n\t"
        "ds_read_b64_tr_b16 v[38:39], v194 offset:25600\n\t"
        "s_add_u32 s44, s44, 0x1000\n\t"
        "s_and_b32 s44, s44, 0xffff\n\t"
        "buffer_load_dwordx4 v[144:147], v192, s[40:43], s44 offen offset:0 sc1\n\t"
        "s_waitcnt vmcnt(11) lgkmcnt(14)\n\t"
        "v_mfma_f32_32x32x16_bf16 v[0:15], v[40:43], v[100:103], v[0:15]\n\t"
        "ds_read_b64_tr_b16 v[40:41], v193 offset:26624\n\t"
        "ds_read_b64_tr_b16 v[42:43], v193 offset:27648\n\t"
        "s_waitcnt lgkmcnt(14)\n\t"
        "v_mfma_f32_32x32x16_bf16 v[16:31], v[44:47], v[100:103], v[16:31]\n\t"
        "ds_read_b64_tr_b16 v[44:45], v194 offset:26624\n\t"
        "ds_read_b64_tr_b16 v[46:47], v194 offset:27648\n\t"
        "buffer_load_dwordx4 v[148:151], v192, s[40:43], s44 offen offset:1024 sc1\n\t"
        "s_waitcnt vmcnt(11) lgkmcnt(14)\n\t"
        "v_mfma_f32_32x32x16_bf16 v[0:15], v[48:51], v[104:107], v[0:15]\n\t"
        "ds_read_b64_tr_b16 v[48:49], v193 offset:28672\n\t"
        "ds_read_b64_tr_b16 v[50:51], v193 offset:29696\n\t"
        "s_waitcnt lgkmcnt(14)\n\t"
        "v_mfma_f32_32x32x16_bf16 v[16:31], v[52:55], v[104:107], v[16:31]\n\t"
        "ds_read_b64_tr_b16 v[52:53], v194 offset:28672\n\t"
        "ds_read_b64_tr_b16 v[54:55], v194 offset:29696\n\t"
        "buffer_load_dwordx4 v[152:155], v192, s[40:43], s44 offen offset:2048 sc1\n\t"
        "s_waitcnt vmcnt(11) lgkmcnt(14)\n\t"
        "v_mfma_f32_32x32x16_bf16 v[0:15], v[56:59], v[108:111], v[0:15]\n\t"
        "ds_read_b64_tr_b16 v[56:57], v193 offset:30720\n\t"
        "ds_read_b64_tr_b16 v[58:59], v193 offset:31744\n\t"
        "s_waitcnt lgkmcnt(14)\n\t"
        "v_mfma_f32_32x32x16_bf16 v[16:31], v[60:63], v[108:111], v[16:31]\n\t"
        "ds_read_b64_tr_b16 v[60:61], v194 offset:30720\n\t"
        "ds_read_b64_tr_b16 v[62:63], v194 offset:31744\n\t"
        "buffer_load_dwordx4 v[156:159], v192, s[40:43], s44 offen offset:3072 sc1\n\t"
        "s_waitcnt vmcnt(11) lgkmcnt(14)\n\t"
        "v_mfma_f32_32x32x16_bf16 v[0:15], v[32:35], v[112:115], v[0:15]\n\t"
        "ds_read_b64_tr_b16 v[32:33], v193 offset:32768\n\t"
        "ds_read_b64_tr_b16 v[34:35], v193 offset:33792\n\t"
        "s_waitcnt lgkmcnt(14)\n\t"
        "v_mfma_f32_32x32x16_bf16 v[16:31], v[36:39], v[112:115], v[16:31]\n\t"
        "ds_read_b64_tr_b16 v[36:37], v194 offset:32768\n\t"
        "ds_read_b64_tr_b16 v[38:39], v194 offset:33792\n\t"
        "s_add_u32 s44, s44, 0x1000\n\t"
        "s_and_b32 s44, s44, 0xffff\n\t"
        "buffer_load_dwordx4 v[160:163], v192, s[40:43], s44 offen offset:0 sc1\n\t"
        "s_waitcnt vmcnt(11) lgkmcnt(14)\n\t"
        "v_mfma_f32_32x32x16_bf16 v[0:15], v[40:43], v[116:119], v[0:15]\n\t"
        "ds_read_b64_tr_b16 v[40:41], v193 offset:34816\n\t"
        "ds_read_b64_tr_b16 v[42:43], v193 offset:35840\n\t"
        "s_waitcnt lgkmcnt(14)\n\t"
        "v_mfma_f32_32x32x16_bf16 v[16:31], v[44:47], v[116:119], v[16:31]\n\t"
        "ds_read_b64_tr_b16 v[44:45], v194 offset:34816\n\t"
        "ds_read_b64_tr_b16 v[46:47], v194 offset:35840\n\t"
        "buffer_load_dwordx4 v[164:167], v192, s[40:43], s44 offen offset:1024 sc1\n\t"
        "s_waitcnt vmcnt(11) lgkmcnt(14)\n\t"
        "v_mfma_f32_32x32x16_bf16 v[0:15], v[48:51], v[120:123], v[0:15]\n\t"
        "ds_read_b64_tr_b16 v[48:49], v193 offset:36864\n\t"
        "ds_read_b64_tr_b16 v[50:51], v193 offset:37888\n\t"
        "s_waitcnt lgkmcnt(14)\n\t"
        "v_mfma_f32_32x32x16_bf16 v[16:31], v[52:55], v[120:123], v[16:31]\n\t"
        "ds_read_b64_tr_b16 v[52:53], v194 offset:36864\n\t"
        "ds_read_b64_tr_b16 v[54:55], v194 offset:37888\n\t"
        "buffer_load_dwordx4 v[168:171], v192, s[40:43], s44 offen offset:2048 sc1\n\t"
        "s_waitcnt vmcnt(11) lgkmcnt(14)\n\t"
        "v_mfma_f32_32x32x16_bf16 v[0:15], v[56:59], v[124:127], v[0:15]\n\t"
        "ds_read_b64_tr_b16 v[56:57], v193 offset:38912\n\t"
        "ds_read_b64_tr_b16 v[58:59], v193 offset:39936\n\t"
        "s_waitcnt lgkmcnt(14)\n\t"
        "v_mfma_f32_32x32x16_bf16 v[16:31], v[60:63], v[124:127], v[16:31]\n\t"
        "ds_read_b64_tr_b16 v[60:61], v194 offset:38912\n\t"
        "ds_read_b64_tr_b16 v[62:63], v194 offset:39936\n\t"
        "buffer_load_dwordx4 v[172:175], v192, s[40:43], s44 offen offset:3072 sc1\n\t"
        "s_waitcnt vmcnt(11) lgkmcnt(14)\n\t"
        "v_mfma_f32_32x32x16_bf16 v[0:15], v[32:35], v[128:131], v[0:15]\n\t"
        "ds_read_b64_tr_b16 v[32:33], v193 offset:40960\n\t"
        "ds_read_b64_tr_b16 v[34:35], v193 offset:41984\n\t"
        "s_waitcnt lgkmcnt(14)\n\t"
        "v_mfma_f32_32x32x16_bf16 v[16:31], v[36:39], v[128:131], v[16:31]\n\t"
        "ds_read_b64_tr_b16 v[36:37], v194 offset:40960\n\t"
        "ds_read_b64_tr_b16 v[38:39], v194 offset:41984\n\t"
        "s_add_u32 s44, s44, 0x1000\n\t"
        "s_and_b32 s44, s44, 0xffff\n\t"
        "buffer_load_dwordx4 v[176:179], v192, s[40:43], s44 offen offset:0 sc1\n\t"
        "s_waitcnt vmcnt(11) lgkmcnt(14)\n\t"
        "v_mfma_f32_32x32x16_bf16 v[0:15], v[40:43], v[132:135], v[0:15]\n\t"
        "ds_read_b64_tr_b16 v[40:41], v193 offset:43008\n\t"
        "ds_read_b64_tr_b16 v[42:43], v193 offset:44032\n\t"
        "s_waitcnt lgkmcnt(14)\n\t"
        "v_mfma_f32_32x32x16_bf16 v[16:31], v[44:47], v[132:135], v[16:31]\n\t"
        "ds_read_b64_tr_b16 v[44:45], v194 offset:43008\n\t"
        "ds_read_b64_tr_b16 v[46:47], v194 offset:44032\n\t"
        "buffer_load_dwordx4 v[180:183], v192, s[40:43], s44 offen offset:1024 sc1\n\t"
        "s_waitcnt vmcnt(11) lgkmcnt(14)\n\t"
        "v_mfma_f32_32x32x16_bf16 v[0:15], v[48:51], v[136:139], v[0:15]\n\t"
        "ds_read_b64_tr_b16 v[48:49], v193 offset:45056\n\t"
        "ds_read_b64_tr_b16 v[50:51], v193 offset:46080\n\t"
        "s_waitcnt lgkmcnt(14)\n\t"
        "v_mfma_f32_32x32x16_bf16 v[16:31], v[52:55], v[136:139], v[16:31]\n\t"
        "ds_read_b64_tr_b16 v[52:53], v194 offset:45056\n\t"
        "ds_read_b64_tr_b16 v[54:55], v194 offset:46080\n\t"
        "buffer_load_dwordx4 v[184:187], v192, s[40:43], s44 offen offset:2048 sc1\n\t"
        "s_waitcnt vmcnt(11) lgkmcnt(14)\n\t"
        "v_mfma_f32_32x32x16_bf16 v[0:15], v[56:59], v[140:143], v[0:15]\n\t"
        "ds_read_b64_tr_b16 v[56:57], v193 offset:47104\n\t"
        "ds_read_b64_tr_b16 v[58:59], v193 offset:48128\n\t"
        "s_waitcnt lgkmcnt(14)\n\t"
        "v_mfma_f32_32x32x16_bf16 v[16:31], v[60:63], v[140:143], v[16:31]\n\t"
        "ds_read_b64_tr_b16 v[60:61], v194 offset:47104\n\t"
        "ds_read_b64_tr_b16 v[62:63], v194 offset:48128\n\t"
        "buffer_load_dwordx4 v[188:191], v192, s[40:43], s44 offen offset:3072 sc1\n\t"
        "s_waitcnt vmcnt(11) lgkmcnt(14)\n\t"
        "v_mfma_f32_32x32x16_bf16 v[0:15], v[32:35], v[144:147], v[0:15]\n\t"
        "ds_read_b64_tr_b16 v[32:33], v193 offset:49152\n\t"
        "ds_read_b64_tr_b16 v[34:35], v193 offset:50176\n\t"
        "s_waitcnt lgkmcnt(14)\n\t"
        "v_mfma_f32_32x32x16_bf16 v[16:31], v[36:39], v[144:147], v[16:31]\n\t"
        "ds_read_b64_tr_b16 v[36:37], v194 offset:49152\n\t"
        "ds_read_b64_tr_b16 v[38:39], v194 offset:50176\n\t"
        "s_add_u32 s44, s44, 0x1000\n\t"
        "s_and_b32 s44, s44, 0xffff\n\t"
        "buffer_load_dwordx4 v[64:67], v192, s[40:43], s44 offen offset:0 sc1\n\t"
        "s_waitcnt vmcnt(11) lgkmcnt(14)\n\t"
        "v_mfma_f32_32x32x16_bf16 v[0:15], v[40:43], v[148:151], v[0:15]\n\t"
        "ds_read_b64_tr_b16 v[40:41], v193 offset:51200\n\t"
        "ds_read_b64_tr_b16 v[42:43], v193 offset:52224\n\t"
        "s_waitcnt lgkmcnt(14)\n\t"
        "v_mfma_f32_32x32x16_bf16 v[16:31], v[44:47], v[148:151], v[16:31]\n\t"
        "ds_read_b64_tr_b16 v[44:45], v194 offset:51200\n\t"
        "ds_read_b64_tr_b16 v[46:47], v194 offset:52224\n\t"
        "buffer_load_dwordx4 v[68:71], v192, s[40:43], s44 offen offset:1024 sc1\n\t"
        "s_waitcnt vmcnt(11) lgkmcnt(14)\n\t"
        "v_mfma_f32_32x32x16_bf16 v[0:15], v[48:51], v[152:155], v[0:15]\n\t"
        "ds_read_b64_tr_b16 v[48:49], v193 offset:53248\n\t"
        "ds_read_b64_tr_b16 v[50:51], v193 offset:54272\n\t"
        "s_waitcnt lgkmcnt(14)\n\t"
        "v_mfma_f32_32x32x16_bf16 v[16:31], v[52:55], v[152:155], v[16:31]\n\t"
        "ds_read_b64_tr_b16 v[52:53], v194 offset:53248\n\t"
        "ds_read_b64_tr_b16 v[54:55], v194 offset:54272\n\t"
        "buffer_load_dwordx4 v[72:75], v192, s[40:43], s44 offen offset:2048 sc1\n\t"
        "s_waitcnt vmcnt(11) lgkmcnt(14)\n\t"
        "v_mfma_f32_32x32x16_bf16 v[0:15], v[56:59], v[156:159], v[0:15]\n\t"
        "ds_read_b64_tr_b16 v[56:57], v193 offset:55296\n\t"
        "ds_read_b64_tr_b16 v[58:59], v193 offset:56320\n\t"
        "s_waitcnt lgkmcnt(14)\n\t"
        "v_mfma_f32_32x32x16_bf16 v[16:31], v[60:63], v[156:159], v[16:31]\n\t"
        "ds_read_b64_tr_b16 v[60:61], v194 offset:55296\n\t"
        "ds_read_b64_tr_b16 v[62:63], v194 offset:56320\n\t"
        "buffer_load_dwordx4 v[76:79], v192, s[40:43], s44 offen offset:3072 sc1\n\t"
        "s_waitcnt vmcnt(11) lgkmcnt(14)\n\t"
        "v_mfma_f32_32x32x16_bf16 v[0:15], v[32:35], v[160:163], v[0:15]\n\t"
        "ds_read_b64_tr_b16 v[32:33], v193 offset:57344\n\t"
        "ds_read_b64_tr_b16 v[34:35], v193 offset:58368\n\t"
        "s_waitcnt lgkmcnt(14)\n\t"
        "v_mfma_f32_32x32x16_bf16 v[16:31], v[36:39], v[160:163], v[16:31]\n\t"
        "ds_read_b64_tr_b16 v[36:37], v194 offset:57344\n\t"
        "ds_read_b64_tr_b16 v[38:39], v194 offset:58368\n\t"
        "s_add_u32 s44, s44, 0x1000\n\t"
        "s_and_b32 s44, s44, 0xffff\n\t"
        "buffer_load_dwordx4 v[80:83], v192, s[40:43], s44 offen offset:0 sc1\n\t"
        "s_waitcnt vmcnt(11) lgkmcnt(14)\n\t"
        "v_mfma_f32_32x32x16_bf16 v[0:15], v[40:43], v[164:167], v[0:15]\n\t"
        "ds_read_b64_tr_b16 v[40:41], v193 offset:59392\n\t"
        "ds_read_b64_tr_b16 v[42:43], v193 offset:60416\n\t"
        "s_waitcnt lgkmcnt(14)\n\t"
        "v_mfma_f32_32x32x16_bf16 v[16:31], v[44:47], v[164:167], v[16:31]\n\t"
        "ds_read_b64_tr_b16 v[44:45], v194 offset:59392\n\t"
        "ds_read_b64_tr_b16 v[46:47], v194 offset:60416\n\t"
        "buffer_load_dwordx4 v[84:87], v192, s[40:43], s44 offen offset:1024 sc1\n\t"
        "s_waitcnt vmcnt(11) lgkmcnt(14)\n\t"
        "v_mfma_f32_32x32x16_bf16 v[0:15], v[48:51], v[168:171], v[0:15]\n\t"
        "ds_read_b64_tr_b16 v[48:49], v193 offset:61440\n\t"
        "ds_read_b64_tr_b16 v[50:51], v193 offset:62464\n\t"
        "s_waitcnt lgkmcnt(14)\n\t"
        "v_mfma_f32_32x32x16_bf16 v[16:31], v[52:55], v[168:171], v[16:31]\n\t"
        "ds_read_b64_tr_b16 v[52:53], v194 offset:61440\n\t"
        "ds_read_b64_tr_b16 v[54:55], v194 offset:62464\n\t"
        "buffer_load_dwordx4 v[88:91], v192, s[40:43], s44 offen offset:2048 sc1\n\t"
        "s_waitcnt vmcnt(11) lgkmcnt(14)\n\t"
        "v_mfma_f32_32x32x16_bf16 v[0:15], v[56:59], v[172:175], v[0:15]\n\t"
        "ds_read_b64_tr_b16 v[56:57], v193 offset:63488\n\t"
        "ds_read_b64_tr_b16 v[58:59], v193 offset:64512\n\t"
        "s_waitcnt lgkmcnt(14)\n\t"
        "v_mfma_f32_32x32x16_bf16 v[16:31], v[60:63], v[172:175], v[16:31]\n\t"
        "ds_read_b64_tr_b16 v[60:61], v194 offset:63488\n\t"
        "ds_read_b64_tr_b16 v[62:63], v194 offset:64512\n\t"
        "buffer_load_dwordx4 v[92:95], v192, s[40:43], s44 offen offset:3072 sc1\n\t"
        "s_waitcnt vmcnt(11) lgkmcnt(14)\n\t"
        "v_mfma_f32_32x32x16_bf16 v[0:15], v[32:35], v[176:179], v[0:15]\n\t"
        "ds_read_b64_tr_b16 v[32:33], v195 offset:0\n\t"
        "ds_read_b64_tr_b16 v[34:35], v195 offset:1024\n\t"
        "s_waitcnt lgkmcnt(14)\n\t"
        "v_mfma_f32_32x32x16_bf16 v[16:31], v[36:39], v[176:179], v[16:31]\n\t"
        "ds_read_b64_tr_b16 v[36:37], v196 offset:0\n\t"
        "ds_read_b64_tr_b16 v[38:39], v196 offset:1024\n\t"
        "s_add_u32 s44, s44, 0x1000\n\t"
        "s_and_b32 s44, s44, 0xffff\n\t"
        "buffer_load_dwordx4 v[96:99], v192, s[40:43], s44 offen offset:0 sc1\n\t"
        "s_waitcnt vmcnt(11) lgkmcnt(14)\n\t"
        "v_mfma_f32_32x32x16_bf16 v[0:15], v[40:43], v[180:183], v[0:15]\n\t"
        "ds_read_b64_tr_b16 v[40:41], v195 offset:2048\n\t"
        "ds_read_b64_tr_b16 v[42:43], v195 offset:3072\n\t"
        "s_waitcnt lgkmcnt(14)\n\t"
        "v_mfma_f32_32x32x16_bf16 v[16:31], v[44:47], v[180:183], v[16:31]\n\t"
        "ds_read_b64_tr_b16 v[44:45], v196 offset:2048\n\t"
        "ds_read_b64_tr_b16 v[46:47], v196 offset:3072\n\t"
        "buffer_load_dwordx4 v[100:103], v192, s[40:43], s44 offen offset:1024 sc1\n\t"
        "s_waitcnt vmcnt(11) lgkmcnt(14)\n\t"
        "v_mfma_f32_32x32x16_bf16 v[0:15], v[48:51], v[184:187], v[0:15]\n\t"
        "ds_read_b64_tr_b16 v[48:49], v195 offset:4096\n\t"
        "ds_read_b64_tr_b16 v[50:51], v195 offset:5120\n\t"
        "s_waitcnt lgkmcnt(14)\n\t"
        "v_mfma_f32_32x32x16_bf16 v[16:31], v[52:55], v[184:187], v[16:31]\n\t"
        "ds_read_b64_tr_b16 v[52:53], v196 offset:4096\n\t"
        "ds_read_b64_tr_b16 v[54:55], v196 offset:5120\n\t"
        "buffer_load_dwordx4 v[104:107], v192, s[40:43], s44 offen offset:2048 sc1\n\t"
        "s_waitcnt vmcnt(11) lgkmcnt(14)\n\t"
        "v_mfma_f32_32x32x16_bf16 v[0:15], v[56:59], v[188:191], v[0:15]\n\t"
        "ds_read_b64_tr_b16 v[56:57], v195 offset:6144\n\t"
        "ds_read_b64_tr_b16 v[58:59], v195 offset:7168\n\t"
        "s_waitcnt lgkmcnt(14)\n\t"
        "v_mfma_f32_32x32x16_bf16 v[16:31], v[60:63], v[188:191], v[16:31]\n\t"
        "ds_read_b64_tr_b16 v[60:61], v196 offset:6144\n\t"
        "ds_read_b64_tr_b16 v[62:63], v196 offset:7168\n\t"
        "buffer_load_dwordx4 v[108:111], v192, s[40:43], s44 offen offset:3072 sc1\n\t"
        "s_waitcnt vmcnt(11) lgkmcnt(14)\n\t"
        "v_mfma_f32_32x32x16_bf16 v[0:15], v[32:35], v[64:67], v[0:15]\n\t"
        "ds_read_b64_tr_b16 v[32:33], v195 offset:8192\n\t"
        "ds_read_b64_tr_b16 v[34:35], v195 offset:9216\n\t"
        "s_waitcnt lgkmcnt(14)\n\t"
        "v_mfma_f32_32x32x16_bf16 v[16:31], v[36:39], v[64:67], v[16:31]\n\t"
        "ds_read_b64_tr_b16 v[36:37], v196 offset:8192\n\t"
        "ds_read_b64_tr_b16 v[38:39], v196 offset:9216\n\t"
        "s_add_u32 s44, s44, 0x1000\n\t"
        "s_and_b32 s44, s44, 0xffff\n\t"
        "buffer_load_dwordx4 v[112:115], v192, s[40:43], s44 offen offset:0 sc1\n\t"
        "s_waitcnt vmcnt(11) lgkmcnt(14)\n\t"
        "v_mfma_f32_32x32x16_bf16 v[0:15], v[40:43], v[68:71], v[0:15]\n\t"
        "ds_read_b64_tr_b16 v[40:41], v195 offset:10240\n\t"
        "ds_read_b64_tr_b16 v[42:43], v195 offset:11264\n\t"
        "s_waitcnt lgkmcnt(14)\n\t"
        "v_mfma_f32_32x32x16_bf16 v[16:31], v[44:47], v[68:71], v[16:31]\n\t"
        "ds_read_b64_tr_b16 v[44:45], v196 offset:10240\n\t"
        "ds_read_b64_tr_b16 v[46:47], v196 offset:11264\n\t"
        "buffer_load_dwordx4 v[116:119], v192, s[40:43], s44 offen offset:1024 sc1\n\t"
        "s_waitcnt vmcnt(11) lgkmcnt(14)\n\t"
        "v_mfma_f32_32x32x16_bf16 v[0:15], v[48:51], v[72:75], v[0:15]\n\t"
        "ds_read_b64_tr_b16 v[48:49], v195 offset:12288\n\t"
        "ds_read_b64_tr_b16 v[50:51], v195 offset:13312\n\t"
        "s_waitcnt lgkmcnt(14)\n\t"
        "v_mfma_f32_32x32x16_bf16 v[16:31], v[52:55], v[72:75], v[16:31]\n\t"
        "ds_read_b64_tr_b16 v[52:53], v196 offset:12288\n\t"
        "ds_read_b64_tr_b16 v[54:55], v196 offset:13312\n\t"
        "buffer_load_dwordx4 v[120:123], v192, s[40:43], s44 offen offset:2048 sc1\n\t"
        "s_waitcnt vmcnt(11) lgkmcnt(14)\n\t"
        "v_mfma_f32_32x32x16_bf16 v[0:15], v[56:59], v[76:79], v[0:15]\n\t"
        "ds_read_b64_tr_b16 v[56:57], v195 offset:14336\n\t"
        "ds_read_b64_tr_b16 v[58:59], v195 offset:15360\n\t"
        "s_waitcnt lgkmcnt(14)\n\t"
        "v_mfma_f32_32x32x16_bf16 v[16:31], v[60:63], v[76:79], v[16:31]\n\t"
        "ds_read_b64_tr_b16 v[60:61], v196 offset:14336\n\t"
        "ds_read_b64_tr_b16 v[62:63], v196 offset:15360\n\t"
        "buffer_load_dwordx4 v[124:127], v192, s[40:43], s44 offen offset:3072 sc1\n\t"
        "s_waitcnt vmcnt(11) lgkmcnt(14)\n\t"
        "v_mfma_f32_32x32x16_bf16 v[0:15], v[32:35], v[80:83], v[0:15]\n\t"
        "ds_read_b64_tr_b16 v[32:33], v195 offset:16384\n\t"
        "ds_read_b64_tr_b16 v[34:35], v195 offset:17408\n\t"
        "s_waitcnt lgkmcnt(14)\n\t"
        "v_mfma_f32_32x32x16_bf16 v[16:31], v[36:39], v[80:83], v[16:31]\n\t"
        "ds_read_b64_tr_b16 v[36:37], v196 offset:16384\n\t"
        "ds_read_b64_tr_b16 v[38:39], v196 offset:17408\n\t"
        "s_add_u32 s44, s44, 0x1000\n\t"
        "s_and_b32 s44, s44, 0xffff\n\t"
        "buffer_load_dwordx4 v[128:131], v192, s[40:43], s44 offen offset:0 sc1\n\t"
        "s_waitcnt vmcnt(11) lgkmcnt(14)\n\t"
        "v_mfma_f32_32x32x16_bf16 v[0:15], v[40:43], v[84:87], v[0:15]\n\t"
        "ds_read_b64_tr_b16 v[40:41], v195 offset:18432\n\t"
        "ds_read_b64_tr_b16 v[42:43], v195 offset:19456\n\t"
        "s_waitcnt lgkmcnt(14)\n\t"
        "v_mfma_f32_32x32x16_bf16 v[16:31], v[44:47], v[84:87], v[16:31]\n\t"
        "ds_read_b64_tr_b16 v[44:45], v196 offset:18432\n\t"
        "ds_read_b64_tr_b16 v[46:47], v196 offset:19456\n\t"
        "buffer_load_dwordx4 v[132:135], v192, s[40:43], s44 offen offset:1024 sc1\n\t"
        "s_waitcnt vmcnt(11) lgkmcnt(14)\n\t"
        "v_mfma_f32_32x32x16_bf16 v[0:15], v[48:51], v[88:91], v[0:15]\n\t"
        "ds_read_b64_tr_b16 v[48:49], v195 offset:20480\n\t"
        "ds_read_b64_tr_b16 v[50:51], v195 offset:21504\n\t"
        "s_waitcnt lgkmcnt(14)\n\t"
        "v_mfma_f32_32x32x16_bf16 v[16:31], v[52:55], v[88:91], v[16:31]\n\t"
        "ds_read_b64_tr_b16 v[52:53], v196 offset:20480\n\t"
        "ds_read_b64_tr_b16 v[54:55], v196 offset:21504\n\t"
        "buffer_load_dwordx4 v[136:139], v192, s[40:43], s44 offen offset:2048 sc1\n\t"
        "s_waitcnt vmcnt(11) lgkmcnt(14)\n\t"
        "v_mfma_f32_32x32x16_bf16 v[0:15], v[56:59], v[92:95], v[0:15]\n\t"
        "ds_read_b64_tr_b16 v[56:57], v195 offset:22528\n\t"
        "ds_read_b64_tr_b16 v[58:59], v195 offset:23552\n\t"
        "s_waitcnt lgkmcnt(14)\n\t"
        "v_mfma_f32_32x32x16_bf16 v[16:31], v[60:63], v[92:95], v[16:31]\n\t"
        "ds_read_b64_tr_b16 v[60:61], v196 offset:22528\n\t"
        "ds_read_b64_tr_b16 v[62:63], v196 offset:23552\n\t"
        "buffer_load_dwordx4 v[140:143], v192, s[40:43], s44 offen offset:3072 sc1\n\t"
        "s_waitcnt vmcnt(11) lgkmcnt(14)\n\t"
        "v_mfma_f32_32x32x16_bf16 v[0:15], v[32:35], v[96:99], v[0:15]\n\t"
        "ds_read_b64_tr_b16 v[32:33], v195 offset:24576\n\t"
        "ds_read_b64_tr_b16 v[34:35], v195 offset:25600\n\t"
        "s_waitcnt lgkmcnt(14)\n\t"
        "v_mfma_f32_32x32x16_bf16 v[16:31], v[36:39], v[96:99], v[16:31]\n\t"
        "ds_read_b64_tr_b16 v[36:37], v196 offset:24576\n\t"
        "ds_read_b64_tr_b16 v[38:39], v196 offset:25600\n\t"
        "s_add_u32 s44, s44, 0x1000\n\t"
        "s_and_b32 s44, s44, 0xffff\n\t"
        "buffer_load_dwordx4 v[144:147], v192, s[40:43], s44 offen offset:0 sc1\n\t"
        "s_waitcnt vmcnt(11) lgkmcnt(14)\n\t"
        "v_mfma_f32_32x32x16_bf16 v[0:15], v[40:43], v[100:103], v[0:15]\n\t"
        "ds_read_b64_tr_b16 v[40:41], v195 offset:26624\n\t"
        "ds_read_b64_tr_b16 v[42:43], v195 offset:27648\n\t"
        "s_waitcnt lgkmcnt(14)\n\t"
        "v_mfma_f32_32x32x16_bf16 v[16:31], v[44:47], v[100:103], v[16:31]\n\t"
        "ds_read_b64_tr_b16 v[44:45], v196 offset:26624\n\t"
        "ds_read_b64_tr_b16 v[46:47], v196 offset:27648\n\t"
        "buffer_load_dwordx4 v[148:151], v192, s[40:43], s44 offen offset:1024 sc1\n\t"
        "s_waitcnt vmcnt(11) lgkmcnt(14)\n\t"
        "v_mfma_f32_32x32x16_bf16 v[0:15], v[48:51], v[104:107], v[0:15]\n\t"
        "ds_read_b64_tr_b16 v[48:49], v195 offset:28672\n\t"
        "ds_read_b64_tr_b16 v[50:51], v195 offset:29696\n\t"
        "s_waitcnt lgkmcnt(14)\n\t"
        "v_mfma_f32_32x32x16_bf16 v[16:31], v[52:55], v[104:107], v[16:31]\n\t"
        "ds_read_b64_tr_b16 v[52:53], v196 offset:28672\n\t"
        "ds_read_b64_tr_b16 v[54:55], v196 offset:29696\n\t"
        "buffer_load_dwordx4 v[152:155], v192, s[40:43], s44 offen offset:2048 sc1\n\t"
        "s_waitcnt vmcnt(11) lgkmcnt(14)\n\t"
        "v_mfma_f32_32x32x16_bf16 v[0:15], v[56:59], v[108:111], v[0:15]\n\t"
        "ds_read_b64_tr_b16 v[56:57], v195 offset:30720\n\t"
        "ds_read_b64_tr_b16 v[58:59], v195 offset:31744\n\t"
        "s_waitcnt lgkmcnt(14)\n\t"
        "v_mfma_f32_32x32x16_bf16 v[16:31], v[60:63], v[108:111], v[16:31]\n\t"
        "ds_read_b64_tr_b16 v[60:61], v196 offset:30720\n\t"
        "ds_read_b64_tr_b16 v[62:63], v196 offset:31744\n\t"
        "buffer_load_dwordx4 v[156:159], v192, s[40:43], s44 offen offset:3072 sc1\n\t"
        "s_waitcnt vmcnt(11) lgkmcnt(14)\n\t"
        "v_mfma_f32_32x32x16_bf16 v[0:15], v[32:35], v[112:115], v[0:15]\n\t"
        "ds_read_b64_tr_b16 v[32:33], v195 offset:32768\n\t"
        "ds_read_b64_tr_b16 v[34:35], v195 offset:33792\n\t"
        "s_waitcnt lgkmcnt(14)\n\t"
        "v_mfma_f32_32x32x16_bf16 v[16:31], v[36:39], v[112:115], v[16:31]\n\t"
        "ds_read_b64_tr_b16 v[36:37], v196 offset:32768\n\t"
        "ds_read_b64_tr_b16 v[38:39], v196 offset:33792\n\t"
        "s_add_u32 s44, s44, 0x1000\n\t"
        "s_and_b32 s44, s44, 0xffff\n\t"
        "buffer_load_dwordx4 v[160:163], v192, s[40:43], s44 offen offset:0 sc1\n\t"
        "s_waitcnt vmcnt(11) lgkmcnt(14)\n\t"
        "v_mfma_f32_32x32x16_bf16 v[0:15], v[40:43], v[116:119], v[0:15]\n\t"
        "ds_read_b64_tr_b16 v[40:41], v195 offset:34816\n\t"
        "ds_read_b64_tr_b16 v[42:43], v195 offset:35840\n\t"
        "s_waitcnt lgkmcnt(14)\n\t"
        "v_mfma_f32_32x32x16_bf16 v[16:31], v[44:47], v[116:119], v[16:31]\n\t"
        "ds_read_b64_tr_b16 v[44:45], v196 offset:34816\n\t"
        "ds_read_b64_tr_b16 v[46:47], v196 offset:35840\n\t"
        "buffer_load_dwordx4 v[164:167], v192, s[40:43], s44 offen offset:1024 sc1\n\t"
        "s_waitcnt vmcnt(11) lgkmcnt(14)\n\t"
        "v_mfma_f32_32x32x16_bf16 v[0:15], v[48:51], v[120:123], v[0:15]\n\t"
        "ds_read_b64_tr_b16 v[48:49], v195 offset:36864\n\t"
        "ds_read_b64_tr_b16 v[50:51], v195 offset:37888\n\t"
        "s_waitcnt lgkmcnt(14)\n\t"
        "v_mfma_f32_32x32x16_bf16 v[16:31], v[52:55], v[120:123], v[16:31]\n\t"
        "ds_read_b64_tr_b16 v[52:53], v196 offset:36864\n\t"
        "ds_read_b64_tr_b16 v[54:55], v196 offset:37888\n\t"
        "buffer_load_dwordx4 v[168:171], v192, s[40:43], s44 offen offset:2048 sc1\n\t"
        "s_waitcnt vmcnt(11) lgkmcnt(14)\n\t"
        "v_mfma_f32_32x32x16_bf16 v[0:15], v[56:59], v[124:127], v[0:15]\n\t"
        "ds_read_b64_tr_b16 v[56:57], v195 offset:38912\n\t"
        "ds_read_b64_tr_b16 v[58:59], v195 offset:39936\n\t"
        "s_waitcnt lgkmcnt(14)\n\t"
        "v_mfma_f32_32x32x16_bf16 v[16:31], v[60:63], v[124:127], v[16:31]\n\t"
        "ds_read_b64_tr_b16 v[60:61], v196 offset:38912\n\t"
        "ds_read_b64_tr_b16 v[62:63], v196 offset:39936\n\t"
        "buffer_load_dwordx4 v[172:175], v192, s[40:43], s44 offen offset:3072 sc1\n\t"
        "s_waitcnt vmcnt(11) lgkmcnt(14)\n\t"
        "v_mfma_f32_32x32x16_bf16 v[0:15], v[32:35], v[128:131], v[0:15]\n\t"
        "ds_read_b64_tr_b16 v[32:33], v195 offset:40960\n\t"
        "ds_read_b64_tr_b16 v[34:35], v195 offset:41984\n\t"
        "s_waitcnt lgkmcnt(14)\n\t"
        "v_mfma_f32_32x32x16_bf16 v[16:31], v[36:39], v[128:131], v[16:31]\n\t"
        "ds_read_b64_tr_b16 v[36:37], v196 offset:40960\n\t"
        "ds_read_b64_tr_b16 v[38:39], v196 offset:41984\n\t"
        "s_add_u32 s44, s44, 0x1000\n\t"
        "s_and_b32 s44, s44, 0xffff\n\t"
        "buffer_load_dwordx4 v[176:179], v192, s[40:43], s44 offen offset:0 sc1\n\t"
        "s_waitcnt vmcnt(11) lgkmcnt(14)\n\t"
        "v_mfma_f32_32x32x16_bf16 v[0:15], v[40:43], v[132:135], v[0:15]\n\t"
        "ds_read_b64_tr_b16 v[40:41], v195 offset:43008\n\t"
        "ds_read_b64_tr_b16 v[42:43], v195 offset:44032\n\t"
        "s_waitcnt lgkmcnt(14)\n\t"
        "v_mfma_f32_32x32x16_bf16 v[16:31], v[44:47], v[132:135], v[16:31]\n\t"
        "ds_read_b64_tr_b16 v[44:45], v196 offset:43008\n\t"
        "ds_read_b64_tr_b16 v[46:47], v196 offset:44032\n\t"
        "buffer_load_dwordx4 v[180:183], v192, s[40:43], s44 offen offset:1024 sc1\n\t"
        "s_waitcnt vmcnt(11) lgkmcnt(14)\n\t"
        "v_mfma_f32_32x32x16_bf16 v[0:15], v[48:51], v[136:139], v[0:15]\n\t"
        "ds_read_b64_tr_b16 v[48:49], v195 offset:45056\n\t"
        "ds_read_b64_tr_b16 v[50:51], v195 offset:46080\n\t"
        "s_waitcnt lgkmcnt(14)\n\t"
        "v_mfma_f32_32x32x16_bf16 v[16:31], v[52:55], v[136:139], v[16:31]\n\t"
        "ds_read_b64_tr_b16 v[52:53], v196 offset:45056\n\t"
        "ds_read_b64_tr_b16 v[54:55], v196 offset:46080\n\t"
        "buffer_load_dwordx4 v[184:187], v192, s[40:43], s44 offen offset:2048 sc1\n\t"
        "s_waitcnt vmcnt(11) lgkmcnt(14)\n\t"
        "v_mfma_f32_32x32x16_bf16 v[0:15], v[56:59], v[140:143], v[0:15]\n\t"
        "ds_read_b64_tr_b16 v[56:57], v195 offset:47104\n\t"
        "ds_read_b64_tr_b16 v[58:59], v195 offset:48128\n\t"
        "s_waitcnt lgkmcnt(14)\n\t"
        "v_mfma_f32_32x32x16_bf16 v[16:31], v[60:63], v[140:143], v[16:31]\n\t"
        "ds_read_b64_tr_b16 v[60:61], v196 offset:47104\n\t"
        "ds_read_b64_tr_b16 v[62:63], v196 offset:48128\n\t"
        "buffer_load_dwordx4 v[188:191], v192, s[40:43], s44 offen offset:3072 sc1\n\t"
        "s_waitcnt vmcnt(11) lgkmcnt(14)\n\t"
        "v_mfma_f32_32x32x16_bf16 v[0:15], v[32:35], v[144:147], v[0:15]\n\t"
        "ds_read_b64_tr_b16 v[32:33], v195 offset:49152\n\t"
        "ds_read_b64_tr_b16 v[34:35], v195 offset:50176\n\t"
        "s_waitcnt lgkmcnt(14)\n\t"
        "v_mfma_f32_32x32x16_bf16 v[16:31], v[36:39], v[144:147], v[16:31]\n\t"
        "ds_read_b64_tr_b16 v[36:37], v196 offset:49152\n\t"
        "ds_read_b64_tr_b16 v[38:39], v196 offset:50176\n\t"
        "s_waitcnt vmcnt(10) lgkmcnt(14)\n\t"
        "v_mfma_f32_32x32x16_bf16 v[0:15], v[40:43], v[148:151], v[0:15]\n\t"
        "ds_read_b64_tr_b16 v[40:41], v195 offset:51200\n\t"
        "ds_read_b64_tr_b16 v[42:43], v195 offset:52224\n\t"
        "s_waitcnt lgkmcnt(14)\n\t"
        "v_mfma_f32_32x32x16_bf16 v[16:31], v[44:47], v[148:151], v[16:31]\n\t"
        "ds_read_b64_tr_b16 v[44:45], v196 offset:51200\n\t"
        "ds_read_b64_tr_b16 v[46:47], v196 offset:52224\n\t"
        "s_waitcnt vmcnt(9) lgkmcnt(14)\n\t"
        "v_mfma_f32_32x32x16_bf16 v[0:15], v[48:51], v[152:155], v[0:15]\n\t"
        "ds_read_b64_tr_b16 v[48:49], v195 offset:53248\n\t"
        "ds_read_b64_tr_b16 v[50:51], v195 offset:54272\n\t"
        "s_waitcnt lgkmcnt(14)\n\t"
        "v_mfma_f32_32x32x16_bf16 v[16:31], v[52:55], v[152:155], v[16:31]\n\t"
        "ds_read_b64_tr_b16 v[52:53], v196 offset:53248\n\t"
        "ds_read_b64_tr_b16 v[54:55], v196 offset:54272\n\t"
        "s_waitcnt vmcnt(8) lgkmcnt(14)\n\t"
        "v_mfma_f32_32x32x16_bf16 v[0:15], v[56:59], v[156:159], v[0:15]\n\t"
        "ds_read_b64_tr_b16 v[56:57], v195 offset:55296\n\t"
        "ds_read_b64_tr_b16 v[58:59], v195 offset:56320\n\t"
        "s_waitcnt lgkmcnt(14)\n\t"
        "v_mfma_f32_32x32x16_bf16 v[16:31], v[60:63], v[156:159], v[16:31]\n\t"
        "ds_read_b64_tr_b16 v[60:61], v196 offset:55296\n\t"
        "ds_read_b64_tr_b16 v[62:63], v196 offset:56320\n\t"
        "s_waitcnt vmcnt(7) lgkmcnt(14)\n\t"
        "v_mfma_f32_32x32x16_bf16 v[0:15], v[32:35], v[160:163], v[0:15]\n\t"
        "s_barrier\n\t"
        "ds_read_b64_tr_b16 v[32:33], v195 offset:57344\n\t"
        "ds_read_b64_tr_b16 v[34:35], v195 offset:58368\n\t"
        "s_waitcnt lgkmcnt(14)\n\t"
        "v_mfma_f32_32x32x16_bf16 v[16:31], v[36:39], v[160:163], v[16:31]\n\t"
        "ds_read_b64_tr_b16 v[36:37], v196 offset:57344\n\t"
        "ds_read_b64_tr_b16 v[38:39], v196 offset:58368\n\t"
        "s_waitcnt vmcnt(6) lgkmcnt(14)\n\t"
        "v_mfma_f32_32x32x16_bf16 v[0:15], v[40:43], v[164:167], v[0:15]\n\t"
        "ds_read_b64_tr_b16 v[40:41], v195 offset:59392\n\t"
        "ds_read_b64_tr_b16 v[42:43], v195 offset:60416\n\t"
        "s_waitcnt lgkmcnt(14)\n\t"
        "v_mfma_f32_32x32x16_bf16 v[16:31], v[44:47], v[164:167], v[16:31]\n\t"
        "ds_read_b64_tr_b16 v[44:45], v196 offset:59392\n\t"
        "ds_read_b64_tr_b16 v[46:47], v196 offset:60416\n\t"
        "s_waitcnt vmcnt(5) lgkmcnt(14)\n\t"
        "v_mfma_f32_32x32x16_bf16 v[0:15], v[48:51], v[168:171], v[0:15]\n\t"
        "ds_read_b64_tr_b16 v[48:49], v195 offset:61440\n\t"
        "ds_read_b64_tr_b16 v[50:51], v195 offset:62464\n\t"
        "s_waitcnt lgkmcnt(14)\n\t"
        "v_mfma_f32_32x32x16_bf16 v[16:31], v[52:55], v[168:171], v[16:31]\n\t"
        "ds_read_b64_tr_b16 v[52:53], v196 offset:61440\n\t"
        "ds_read_b64_tr_b16 v[54:55], v196 offset:62464\n\t"
        "s_waitcnt vmcnt(4) lgkmcnt(14)\n\t"
        "v_mfma_f32_32x32x16_bf16 v[0:15], v[56:59], v[172:175], v[0:15]\n\t"
        "ds_read_b64_tr_b16 v[56:57], v195 offset:63488\n\t"
        "ds_read_b64_tr_b16 v[58:59], v195 offset:64512\n\t"
        "s_waitcnt lgkmcnt(14)\n\t"
        "v_mfma_f32_32x32x16_bf16 v[16:31], v[60:63], v[172:175], v[16:31]\n\t"
        "ds_read_b64_tr_b16 v[60:61], v196 offset:63488\n\t"
        "ds_read_b64_tr_b16 v[62:63], v196 offset:64512\n\t"
        "s_waitcnt vmcnt(3) lgkmcnt(14)\n\t"
        "v_mfma_f32_32x32x16_bf16 v[0:15], v[32:35], v[176:179], v[0:15]\n\t"
        "s_waitcnt lgkmcnt(12)\n\t"
        "v_mfma_f32_32x32x16_bf16 v[16:31], v[36:39], v[176:179], v[16:31]\n\t"
        "s_waitcnt vmcnt(2) lgkmcnt(10)\n\t"
        "v_mfma_f32_32x32x16_bf16 v[0:15], v[40:43], v[180:183], v[0:15]\n\t"
        "s_waitcnt lgkmcnt(8)\n\t"
        "v_mfma_f32_32x32x16_bf16 v[16:31], v[44:47], v[180:183], v[16:31]\n\t"
        "s_waitcnt vmcnt(1) lgkmcnt(6)\n\t"
        "v_mfma_f32_32x32x16_bf16 v[0:15], v[48:51], v[184:187], v[0:15]\n\t"
        "s_waitcnt lgkmcnt(4)\n\t"
        "v_mfma_f32_32x32x16_bf16 v[16:31], v[52:55], v[184:187], v[16:31]\n\t"
        "s_waitcnt vmcnt(0) lgkmcnt(2)\n\t"
        "v_mfma_f32_32x32x16_bf16 v[0:15], v[56:59], v[188:191], v[0:15]\n\t"
        "s_waitcnt lgkmcnt(0)\n\t"
        "v_mfma_f32_32x32x16_bf16 v[16:31], v[60:63], v[188:191], v[16:31]\n\t"
        "s_nop 11\n\t"
        : "={v[0:15]}"(acc[0]), "={v[16:31]}"(acc[1]), "+{s44}"(soff) STAMP_OUTS
        : "{v192}"(OFF_OF + L * (5 * MBY) + tile * TS + lane * 16), "{v193}"(a0), "{v194}"(a1), "{v195}"(a0 + 65536), "{v196}"(a1 + 65536),
          "{v198}"(lbase + L_W + 20), "{v199}"(L + 1), "{s[40:43]}"(srd)
        : "v32", "v33", "v34", "v35", "v36", "v37", "v38", "v39", "v40", "v41", "v42", "v43", "v44", "v45", "v46", "v47", "v48", "v49", "v50", "v51", "v52", "v53", "v54", "v55", "v56", "v57", "v58", "v59", "v60", "v61", "v62", "v63", "v64", "v65", "v66", "v67", "v68", "v69", "v70", "v71", "v72", "v73", "v74", "v75", "v76", "v77", "v78", "v79", "v80", "v81", "v82", "v83", "v84", "v85", "v86", "v87", "v88", "v89", "v90", "v91", "v92", "v93", "v94", "v95", "v96", "v97", "v98", "v99", "v100", "v101", "v102", "v103", "v104", "v105", "v106", "v107", "v108", "v109", "v110", "v111", "v112", "v113", "v114", "v115", "v116", "v117", "v118", "v119", "v120", "v121", "v122", "v123", "v124", "v125", "v126", "v127", "v128", "v129", "v130", "v131", "v132", "v133", "v134", "v135", "v136", "v137", "v138", "v139", "v140", "v141", "v142", "v143", "v144", "v145", "v146", "v147", "v148", "v149", "v150", "v151", "v152", "v153", "v154", "v155", "v156", "v157", "v158", "v159", "v160", "v161", "v162", "v163", "v164", "v165", "v166", "v167", "v168", "v169", "v170", "v171", "v172", "v173", "v174", "v175", "v176", "v177", "v178", "v179", "v180", "v181", "v182", "v183", "v184", "v185", "v186", "v187", "v188", "v189", "v190", "v191", "v200", "s45", "vcc", "scc", "memory");
      }
      PHASE_VARS;
      STAMP_PUT(12 + 16 * L);
      STAMPV(15 + 16 * L);
      float s = 0.f, sq = 0.f, d = 0.f;
      const unsigned xdst = (L ? OOB : OFF_X1 + tile * TS + tn * 4096) + lane * 16;
#pragma unroll
      for (int g = 0; g < 8; ++g) {
#pragma unroll
        for (int e = 0; e < 2; ++e) {
          const unsigned rr = res[g >> 1][2 * (g & 1) + e];
          const unsigned t = pack2(acc[g >> 2][4 * (g & 3) + 2 * e], acc[g >> 2][4 * (g & 3) + 2 * e + 1]);
          x1p[g >> 1][2 * (g & 1) + e] = pack2(lo16(rr) + lo16(t), hi16(rr) + hi16(t));
        }
        if (g & 1) { stg(rs, xdst + (g >> 1) * 1024, x1p[g >> 1]); SBAR(); }
      }
#pragma unroll
      for (int g = 0; g < 8; ++g)
#pragma unroll
        for (int e = 0; e < 2; ++e) {
          const unsigned o = x1p[g >> 1][2 * (g & 1) + e];
          s = dot2(o, 0x3f803f80u, s); sq = dot2(o, o, sq); d = dot2(o, wrc[g][e], d);
        }
      stg(rs, stats_dst(sx, tile, tn, lane), u32x4{nzf(half_swap_sum(s)), nzf(half_swap_sum(sq)), nzf(half_swap_sum(d)), 0u});
    }
    STAMPV(16 + 16 * L);
  }
  {
    PHASE_VARS;
    float S, Q, Dd, mean, var, mb1, rd1;
    __builtin_amdgcn_s_sleep(13);
    row_stats_sum(rs, OFF_SX + SX_LAYER, tile, lane, S, Q, Dd);
    STAMPV(33);
    row_stats_fin(smem, 2, tile, lane, S, Q, Dd, U2F(words[0]), x1p, mean, var, mb1, rd1);
    STAMPV(34);
    u32x4 xo[4];
#pragma unroll
    for (int i = 0; i < 4; ++i) {
#pragma unroll
      for (int e = 0; e < 4; ++e) {
        const unsigned h2 = ln_pair(x1p[i][e], mb1, rd1);
        xo[i][e] = pack2(lo16(x1p[i][e]) + lo16(h2), hi16(x1p[i][e]) + hi16(h2));
      }
      *reinterpret_cast<u32x4*>(smem + wid * 4096 + r32 * 128 + ((2 * i + hi) ^ (r32 & 7)) * 16) = fo_swap(xo[i]);
    }
    asm volatile("s_waitcnt lgkmcnt(0)" ::: "memory");
#pragma unroll
    for (int i = 0; i < 4; ++i) { xo[i] = *reinterpret_cast<const u32x4*>(smem + wid * 4096 + i * 1024 + (lane >> 3) * 128 + ((lane ^ (lane >> 3)) & 7) * 16); PIN(xo[i]); }
    u16* dst = out + (size_t)(tile * 32 + (lane >> 3)) * D_ + tn * 64 + (lane & 7) * 8;
    lds_wait_ge(lbase + L_W + 4 + 4 * (lane & 1), 2);
    const int rel = (int)words[3] - tile * 32 - (lane >> 3);
#pragma unroll
    for (int i = 0; i < 4; ++i)
      if (rel != 8 * i) st16(dst + (size_t)i * 8 * D_, xo[i]);
    STAMPV(35);
    STAMP_END;
  }
}

extern "C" __global__ void end_marker() {}

extern "C" void kernel_launch(void* const* d_in, const int* in_sizes, int n_in, void* d_out, int out_size,
                              void* d_ws, size_t ws_size, hipStream_t stream) {
  char* ws = (char*)d_ws;
  mega_kernel<<<256, 512, 0, stream>>>((const u16*)d_in[0], (const u16*)d_in[1], (const u16*)d_in[2], (const u16*)d_in[4], (const u16*)d_in[5],
                                       (const u16*)d_in[6], (const u16*)d_in[7], ws, (u16*)d_out);
}
```

```cpp
#include <hip/hip_runtime.h>
#include <hip/hip_bf16.h>
#include <stdint.h>

typedef unsigned short u16;
typedef short s16x4 __attribute__((ext_vector_type(4)));
typedef short s16x8 __attribute__((ext_vector_type(8)));
typedef float f32x16 __attribute__((ext_vector_type(16)));
typedef float f32x8 __attribute__((ext_vector_type(8)));
typedef float f32x4 __attribute__((ext_vector_type(4)));
typedef float f32x2 __attribute__((ext_vector_type(2)));
typedef unsigned int u32x16 __attribute__((ext_vector_type(16)));
typedef unsigned int u32x8 __attribute__((ext_vector_type(8)));
typedef unsigned int u32x4 __attribute__((ext_vector_type(4)));
typedef unsigned int u32x2 __attribute__((ext_vector_type(2)));
typedef __bf16 bf16x2 __attribute__((ext_vector_type(2)));
typedef __bf16 bf16x8 __attribute__((ext_vector_type(8)));
typedef __bf16 bf16x16 __attribute__((ext_vector_type(16)));

#define DI __device__ __forceinline__
#define LDS_PTR(T, p) ((__attribute__((address_space(3))) T*)(p))
#define BARRIER() asm volatile("s_barrier" ::: "memory")
#define BAR_LGKM() asm volatile("s_waitcnt lgkmcnt(0)\n\ts_barrier" ::: "memory")
#define BAR_ALL() asm volatile("s_waitcnt vmcnt(0) lgkmcnt(0)\n\ts_barrier" ::: "memory")
#define DRAIN() asm volatile("s_waitcnt vmcnt(0)" ::: "memory")
#define PACE() ({ asm volatile("s_sleep 1\n\ts_waitcnt vmcnt(3)" ::: "memory"); })
#define MFMA32(a, b, c) __builtin_amdgcn_mfma_f32_32x32x16_bf16(a, b, c, 0, 0, 0)
#define MFMA16(a, b, c) __builtin_amdgcn_mfma_f32_16x16x32_bf16(a, b, c, 0, 0, 0)
#define PIN(x) asm volatile("" : "+v"(x))
#define EX2(x) __builtin_amdgcn_exp2f(x)
#define RCP(x) __builtin_amdgcn_rcpf(x)
#define SBAR() __builtin_amdgcn_sched_barrier(0)

#define STAMP_INIT
#define STAMPV(i)
#define STAMP_END
#define STAMP_DECL
#define STAMP_OUTS
#define STAMP_PUT(i)
constexpr int D_ = 1024, F_ = 3584;

constexpr unsigned MBY = 1u << 20;
constexpr unsigned TS = 65536 + 4096, HS = 262144 + 4096;
constexpr unsigned OFF_HF = 0, OFF_X1 = 5 * MBY;
constexpr unsigned OFF_OF = 10 * MBY;
constexpr unsigned OFF_KV = 20 * MBY;
constexpr unsigned OFF_FL = 40 * MBY;
constexpr unsigned FL_EPOCH = OFF_FL;
constexpr unsigned FL_KV = OFF_FL + 4096;
constexpr unsigned FL_O = FL_KV + 65536;
constexpr unsigned FL_X = FL_O + 65536;
constexpr unsigned OFF_CS = 41 * MBY;
constexpr unsigned OFF_SX = 42 * MBY;
constexpr unsigned SX_ST = 0;
constexpr unsigned SX_ROW = 512 * 1024;
constexpr unsigned SX_CAND = SX_ROW + 64 * 1024;
constexpr unsigned SX_GP = SX_CAND + 32 * 1024;
constexpr unsigned SX_YP = SX_GP + 224 * 1024;
constexpr unsigned SX_LAYER = SX_YP + 224 * 1024;
constexpr unsigned SX_QP = 2 * SX_LAYER;
constexpr unsigned SX_BYTES = SX_QP + 512 * 1024;
static_assert(SX_BYTES <= 6 * 256 * 128 * 16, "sentinel fill");
constexpr unsigned WS_BYTES = 48 * MBY, OOB = 0x80000000u;
constexpr unsigned SENT = 0xFFFFFFFFu;

constexpr int NS = 9, DD = 3, SLOTB = 12288;
constexpr int L_XCH = 122880;
constexpr int L_SL = L_XCH + 8192;
constexpr int L_CAND = L_XCH + 32768;
constexpr int L_AB = L_XCH + 34816;
constexpr int L_FULL = 159744, L_FREE = L_FULL + 512;
constexpr int L_CS = L_FREE + 1024, L_PATCH = L_CS + 768;
constexpr int L_GU = L_PATCH + 768, L_TAG = L_GU + 256, L_WR = L_TAG + 256;
constexpr int L_X2F = L_WR + 128, L_X2P = L_X2F + 128;
constexpr int L_W = L_X2P + 128;
constexpr int LDS_BYTES = 163840;
static_assert(NS * SLOTB <= 54 * 2048 && L_W + 128 <= LDS_BYTES && (NS + 1) * SLOTB <= L_XCH && L_AB + 2048 <= L_FULL, "LDS map");

DI int fresh_lane() { int l; asm volatile("v_mbcnt_lo_u32_b32 %0, -1, 0\n\tv_mbcnt_hi_u32_b32 %0, -1, %0" : "=v"(l)); return l; }
#define LANE_VARS const int lane = fresh_lane(), r32 = lane & 31, hi = lane >> 5, tq = (lane & 15) >> 2, tp = lane & 3, tblk = (lane >> 4) & 1, \
  wrow = 4 * hi + tq, wsw = 4 * ((wrow >> 1) & 1), wch = 2 * tblk + (tp >> 1), c24 = r32 < 24 ? r32 : 23; \
  (void)r32; (void)hi; (void)wsw; (void)wch; (void)c24
DI int fresh_s(int v) { asm volatile("" : "+s"(v)); return v; }
#define PHASE_VARS const int tm = fresh_s(tm_), tn = fresh_s(tn_), wid = fresh_s(wid_), tile = tm * 4 + wid, hh = wid & 1, kh = wid >> 1; (void)tile; (void)hh; (void)kh; LANE_VARS
DI float bf2f(u16 v) { return __uint_as_float(((unsigned)v) << 16); }
DI unsigned pack2(float lo, float hi) { f32x2 v = {lo, hi}; return __builtin_bit_cast(unsigned, __builtin_convertvector(v, bf16x2)); }
DI float rbf(float f) { return __uint_as_float(pack2(f, 0.f) << 16); }
DI float lo16(unsigned u) { return __uint_as_float(u << 16); }
DI float hi16(unsigned u) { return __uint_as_float(u & 0xffff0000u); }
DI float opq(float r) { asm("" : "+v"(r)); return r; }
DI float fadd(float a, float b) { return opq(a + b); }
DI float fmul(float a, float b) { return opq(a * b); }
DI float ffma(float a, float b, float c) { return opq(__builtin_fmaf(a, b, c)); }
DI float U2F(unsigned u) { return __uint_as_float(u); }
DI float half_swap_sum(float v) {
  auto r = __builtin_amdgcn_permlane32_swap(__float_as_uint(v), __float_as_uint(v), false, false);
  return fadd(U2F(r[0]), U2F(r[1]));
}
DI float half_swap_max(float v) {
  auto r = __builtin_amdgcn_permlane32_swap(__float_as_uint(v), __float_as_uint(v), false, false);
  return fmaxf(__uint_as_float(r[0]), __uint_as_float(r[1]));
}
template <int CTRL> DI unsigned dpp(unsigned v) { return (unsigned)__builtin_amdgcn_update_dpp(0, (int)v, CTRL, 0xf, 0xf, false); }
DI float row_swap_sum(float v) {
  auto r = __builtin_amdgcn_permlane16_swap(__float_as_uint(v), __float_as_uint(v), false, false);
  return fadd(U2F(r[0]), U2F(r[1]));
}
DI float half_sum(float v) {
  v = fadd(v, U2F(dpp<0xB1>(__float_as_uint(v)))); v = fadd(v, U2F(dpp<0x4E>(__float_as_uint(v))));
  v = fadd(v, U2F(dpp<0x141>(__float_as_uint(v)))); v = fadd(v, U2F(dpp<0x140>(__float_as_uint(v))));
  return row_swap_sum(v);
}
DI float wave_sum(float v) { return half_swap_sum(half_sum(v)); }
DI unsigned half_max_u32(unsigned v) {
  v = max(v, dpp<0xB1>(v)); v = max(v, dpp<0x4E>(v)); v = max(v, dpp<0x141>(v)); v = max(v, dpp<0x140>(v));
  auto r = __builtin_amdgcn_permlane16_swap(v, v, false, false);
  return max(r[0], r[1]);
}
DI unsigned wave_max_u32(unsigned v) {
  v = half_max_u32(v);
  auto r = __builtin_amdgcn_permlane32_swap(v, v, false, false);
  return max(r[0], r[1]);
}
DI u32x4 fo_swap(u32x4 v) {
  auto a = __builtin_amdgcn_permlane32_swap(v[0], v[2], false, false);
  auto b = __builtin_amdgcn_permlane32_swap(v[1], v[3], false, false);
  u32x4 o; o[0] = a[0]; o[1] = b[0]; o[2] = a[1]; o[3] = b[1];
  return o;
}
DI void glds16(const void* gsrc, unsigned lds_dst) {
  unsigned keep;
  asm volatile("s_mov_b32 %0, m0\n\ts_mov_b32 m0, %2\n\ts_nop 0\n\tglobal_load_lds_dwordx4 %1, off\n\ts_mov_b32 m0, %0"
               : "=&s"(keep) : "v"(gsrc), "s"(lds_dst) : "memory");
}
#define GL_ "global_load_lds_dwordx4 "
DI void glds_v4(const void* sb, const unsigned (&v)[4], unsigned m0v) {
  unsigned keep;
  asm volatile("s_mov_b32 %0, m0\n\ts_mov_b32 m0, %6\n\ts_nop 0\n\t" GL_ "%1, %5\n\t" GL_ "%2, %5 offset:1024\n\t" GL_ "%3, %5 offset:2048\n\t"
               GL_ "%4, %5 offset:3072\n\ts_mov_b32 m0, %0" : "=&s"(keep) : "v"(v[0]), "v"(v[1]), "v"(v[2]), "v"(v[3]), "s"(sb), "s"(m0v) : "memory");
}
#define GP_ "\n\ts_sleep %8\n\ts_waitcnt vmcnt(%7)\n\t"
template <int TH, int SL> DI void glds_v4t(const void* sb, const unsigned (&v)[4], unsigned m0v) {
  unsigned keep;
  asm volatile("s_mov_b32 %0, m0\n\ts_mov_b32 m0, %6\n\ts_nop 0\n\t" GL_ "%1, %5" GP_ GL_ "%2, %5 offset:1024" GP_ GL_ "%3, %5 offset:2048" GP_ GL_ "%4, %5 offset:3072" GP_ "s_mov_b32 m0, %0"
               : "=&s"(keep) : "v"(v[0]), "v"(v[1]), "v"(v[2]), "v"(v[3]), "s"(sb), "s"(m0v), "n"(TH), "n"(SL) : "memory");
}
DI void glds_v3(const void* sb, const unsigned (&v)[3], unsigned m0v) {
  unsigned keep;
  asm volatile("s_mov_b32 %0, m0\n\ts_mov_b32 m0, %5\n\ts_nop 0\n\t" GL_ "%1, %4\n\t" GL_ "%2, %4 offset:1024\n\t" GL_ "%3, %4 offset:2048\n\t"
               "s_mov_b32 m0, %0" : "=&s"(keep) : "v"(v[0]), "v"(v[1]), "v"(v[2]), "s"(sb), "s"(m0v) : "memory");
}
DI float dot2(unsigned a, unsigned b, float c) {
  return __builtin_amdgcn_fdot2_f32_bf16(__builtin_bit_cast(bf16x2, a), __builtin_bit_cast(bf16x2, b), c, false);
}
#define CODE_ADDR(sym, out) do { unsigned long long pc_; unsigned off_; \
  asm volatile("s_getpc_b64 %0\n1:\n\ts_mov_b32 %1, " #sym "-1b" : "=s"(pc_), "=s"(off_)); out = (const char*)(pc_ + (long long)(int)off_); } while (0)
DI void st16(void* p, u32x4 v) { asm volatile("global_store_dwordx4 %0, %1, off sc0 sc1\n\ts_nop 1" :: "v"(p), "v"(v) : "memory"); }
DI void ln_scalars(float mean, float var, float& mb, float& den) { mb = rbf(mean); den = rbf(__builtin_amdgcn_sqrtf(rbf(rbf(var) + 1e-5f))); }
DI unsigned ln_pair(unsigned v, float mb, float rd) {
  const unsigned c = pack2(lo16(v) - mb, hi16(v) - mb);
  return pack2(lo16(c) * rd, hi16(c) * rd);
}
DI unsigned ln_pair_s(unsigned v, float nmb, float rd) {
  const unsigned c = pack2(fadd(lo16(v), nmb), fadd(hi16(v), nmb));
  return pack2(fmul(lo16(c), rd), fmul(hi16(c), rd));
}
template <int N, int M> DI f32x4 sum4(const u32x4 (&v)[M]) {
  f32x4 t;
#pragma unroll
  for (int e = 0; e < 4; ++e) {
    t[e] = U2F(v[0][e]);
#pragma unroll
    for (int i = 1; i < N; ++i) t[e] = fadd(t[e], U2F(v[i][e]));
  }
  return t;
}
#define PERM_LO(x, y) __builtin_amdgcn_perm(y, x, 0x05040100u)
#define PERM_HI(x, y) __builtin_amdgcn_perm(y, x, 0x07060302u)
constexpr unsigned SPIN_CAP = 1u << 15;
DI unsigned nz(unsigned v) { return min(v, SENT - 1u); }
DI unsigned nzf(float v) { return nz(__float_as_uint(v)); }
DI unsigned mx4(unsigned m, const u32x4& v) { return max(max(max(m, v[0]), v[1]), max(v[2], v[3])); }
template <int N> DI bool none_sent(const u32x4 (&v)[N]) {
  unsigned m = 0u;
#pragma unroll
  for (int i = 0; i < N; ++i) m = mx4(m, v[i]);
  return m != SENT;
}
typedef __amdgpu_buffer_rsrc_t rsrc_t;
DI u32x4 ldg(rsrc_t rs, unsigned off) { return __builtin_amdgcn_raw_buffer_load_b128(rs, (int)off, 0, 16); }
DI void stg(rsrc_t rs, unsigned off, u32x4 v) { __builtin_amdgcn_raw_buffer_store_b128(v, rs, (int)off, 0, 16); }
DI void poll_pause(bool slow) { if (slow) __builtin_amdgcn_s_sleep(4); asm volatile("s_sleep 1" ::: "memory"); }
template <int N, class LD, class OKF> DI void poll_all(u32x4 (&out)[N], bool slow, LD ld, OKF okf) {
  for (unsigned spins = 0;; ++spins) {
    ld(out);
    SBAR();
    if (__all(okf(out)) || spins > SPIN_CAP) break;
    poll_pause(slow);
  }
}
DI void lds_set(unsigned addr, unsigned v) { asm volatile("s_waitcnt lgkmcnt(0)\n\tds_write_b32 %0, %1" :: "v"(addr), "v"(v) : "memory"); }
template <int SLEEP = 1> DI void lds_wait_ge(unsigned addr, unsigned need) {
  unsigned spin = 0x100000, v;
  asm volatile("1:\n\tds_read_b32 %0, %2\n\ts_sub_u32 %1, %1, 1\n\ts_waitcnt lgkmcnt(0)\n\ts_cbranch_scc1 2f\n\tv_cmp_gt_u32 vcc, %3, %0\n\ts_cbranch_vccz 2f\n\ts_sleep %4\n\ts_branch 1b\n2:"
               : "=&v"(v), "+s"(spin) : "v"(addr), "s"(need), "n"(SLEEP) : "vcc", "scc", "memory");
}
DI s16x8 tr2(const char* p, int second) {
  s16x4 lo = __builtin_amdgcn_ds_read_tr16_b64_v4i16(LDS_PTR(s16x4, p)), hi = __builtin_amdgcn_ds_read_tr16_b64_v4i16(LDS_PTR(s16x4, p + second));
  return __builtin_shufflevector(lo, hi, 0, 1, 2, 3, 4, 5, 6, 7);
}
DI u32x16 cat4(const u32x4& a, const u32x4& b, const u32x4& c, const u32x4& d) {
  const u32x8 x = __builtin_shufflevector(a, b, 0, 1, 2, 3, 4, 5, 6, 7), y = __builtin_shufflevector(c, d, 0, 1, 2, 3, 4, 5, 6, 7);
  return __builtin_shufflevector(x, y, 0, 1, 2, 3, 4, 5, 6, 7, 8, 9, 10, 11, 12, 13, 14, 15);
}

DI unsigned stats_dst(unsigned sx, int tile, int tn, int lane) { return lane < 32 ? sx + SX_ST + tile * 8192 + tn * 512 + lane * 16 : OOB; }
DI void row_stats_sum(rsrc_t rs, unsigned sx, int tile, int lane, float& S, float& Q, float& Dd) {
  const unsigned exb = sx + SX_ST + tile * 8192 + (lane & 31) * 16;
  u32x4 pv[8];
  poll_all<8>(pv, false, [&](u32x4 (&v)[8]) {
#pragma unroll
    for (int i = 0; i < 8; ++i) v[i] = ldg(rs, exb + ((lane >> 5) * 8 + i) * 512);
  }, [&](const u32x4 (&v)[8]) { return none_sent(v); });
  S = 0.f; Q = 0.f; Dd = 0.f;
#pragma unroll
  for (int i = 0; i < 8; ++i) { S += __uint_as_float(pv[i][0]); Q += __uint_as_float(pv[i][1]); Dd += __uint_as_float(pv[i][2]); }
}
DI void row_stats_fin(char* smem, int epoch, int tile, int lane, float S, float Q, float Dd, float wsum, const u32x4 (&x1p)[4],
                      float& mean, float& var, float& mb, float& rd) {
  S = half_swap_sum(S); Q = half_swap_sum(Q); Dd = half_swap_sum(Dd);
  mean = S * (1.0f / 1024.0f); var = fmaxf(Q * (1.0f / 1024.0f) - mean * mean, 0.f);
  float den;
  ln_scalars(mean, var, mb, den);
  rd = RCP(den);
  const unsigned rb = pack2((Dd - mb * wsum) * rd, 0.f) & 0xffffu;
  const unsigned mine = (((rb & 0x8000u) ? (~rb & 0xffffu) : (rb | 0x8000u)) << 16) | (unsigned)(2047 - (tile * 32 + (lane & 31)));
  const bool best = half_max_u32(mine) == mine;
  if (best) {
    char* c = smem + L_CAND + (tile & 3) * 144;
#pragma unroll
    for (int i = 0; i < 4; ++i) *reinterpret_cast<u32x4*>(c + (2 * i + (lane >> 5)) * 16) = x1p[i];
    *reinterpret_cast<u32x4*>(c + 128) = u32x4{mine, __float_as_uint(mb), __float_as_uint(rd), 0u};
  }
  lds_set((unsigned)(uintptr_t)smem + L_W + 32 + (tile & 3) * 4, epoch);
}

DI void attn_safe(rsrc_t rs, unsigned kvoff, const u32x8& q, const u32x4& ones, f32x16& o) {
  const s16x8 q0 = __builtin_bit_cast(s16x8, __builtin_shufflevector(q, q, 0, 1, 2, 3)), q1 = __builtin_bit_cast(s16x8, __builtin_shufflevector(q, q, 4, 5, 6, 7));
  f32x4 l = {0.f, 0.f, 0.f, 0.f};
#pragma unroll
  for (int e = 0; e < 16; ++e) o[e] = 0.f;
  float m = -INFINITY;
#pragma unroll 1
  for (int kt = 0; kt < 64; ++kt) {
    const unsigned p = kvoff + kt * 4096;
    const u32x4 k0 = ldg(rs, p), k1 = ldg(rs, p + 1024), v0 = ldg(rs, p + 2048), v1 = ldg(rs, p + 3072);
    f32x16 s = {};
    s = MFMA32(__builtin_bit_cast(s16x8, k0), q0, s);
    s = MFMA32(__builtin_bit_cast(s16x8, k1), q1, s);
    float mt = fmaxf(s[0], s[1]);
#pragma unroll
    for (int e = 2; e < 16; ++e) mt = fmaxf(mt, s[e]);
    const float mn = fmaxf(m, half_swap_max(mt));
    const float alpha = EX2(m - mn);
    m = mn;
#pragma unroll
    for (int e = 0; e < 16; ++e) { s[e] = EX2(s[e] - mn); o[e] *= alpha; }
    l[0] *= alpha;
    const bf16x16 pa = __builtin_bit_cast(bf16x16, __builtin_shufflevector(s, s, 0, 1, 2, 3, 4, 5, 6, 7));
    const bf16x16 pb = __builtin_bit_cast(bf16x16, __builtin_shufflevector(s, s, 8, 9, 10, 11, 12, 13, 14, 15));
    o = __builtin_amdgcn_smfmac_f32_32x32x32_bf16(__builtin_bit_cast(bf16x8, v0), pa, o, 0xdddd, 0, 0);
    l = __builtin_amdgcn_smfmac_f32_16x16x64_bf16(__builtin_bit_cast(bf16x8, ones), pa, l, 0xdddd, 0, 0);
    o = __builtin_amdgcn_smfmac_f32_32x32x32_bf16(__builtin_bit_cast(bf16x8, v1), pb, o, 0xdddd, 0, 0);
    l = __builtin_amdgcn_smfmac_f32_16x16x64_bf16(__builtin_bit_cast(bf16x8, ones), pb, l, 0xdddd, 0, 0);
  }
  o *= 1.0f / l[0];
}

extern "C" __global__ __launch_bounds__(512) void mega_kernel(const u16* __restrict__ x0, const u16* __restrict__ wqkv, const u16* __restrict__ wo,
                                                              const u16* __restrict__ w_r, const u16* __restrict__ wg, const u16* __restrict__ wu,
                                                              const u16* __restrict__ wd, char* ws, u16* __restrict__ out) {
  __shared__ __attribute__((aligned(16))) char smem[LDS_BYTES];
  const int wid = __builtin_amdgcn_readfirstlane(threadIdx.x >> 6);
  STAMP_INIT;
  STAMPV(0);
  const int bid = blockIdx.x, xcd = bid & 7, loc = bid >> 3;
  const int tm = 8 * (xcd >> 2) + (loc >> 2), tn = 4 * (xcd & 3) + (loc & 3);
  const unsigned lbase = (unsigned)(uintptr_t)smem;
  const rsrc_t rs = __builtin_amdgcn_make_buffer_rsrc(ws, 0, (int)WS_BYTES, 0x00020000);
  volatile __attribute__((address_space(3))) unsigned* words = LDS_PTR(volatile unsigned, smem + L_W);
  *reinterpret_cast<unsigned*>(smem + L_FULL + threadIdx.x * 4) = 0u;
  words[threadIdx.x & 31] = 0u;
  BAR_LGKM();
  const int lw = wid & 1;
  const char* wb = reinterpret_cast<const char*>(wqkv + (size_t)(16 * lw) * (3 * D_) + tn * 64);
  auto stage_lanes = [](unsigned (&wv)[3]) {
    const int lane = fresh_lane();
#pragma unroll
    for (int i = 0; i < 3; ++i) {
      const int li = lane + 64 * i, k = li / 24, cp = li % 24, c = cp ^ (4 * ((k >> 1) & 1));
      wv[i] = (unsigned)(k * (3 * D_ * 2) + (c >> 3) * (D_ * 2) + (c & 7) * 16 - i * 1024);
    }
  };
  auto stage = [&](const unsigned (&wv)[3], int s, int slot) {
    const char* p = wb + (size_t)((s + 2 * tn) & 31) * (32 * 3 * D_ * 2);
    glds_v3(p, wv, lbase + slot * SLOTB + lw * 6144);
    glds_v3(p + 8 * (3 * D_ * 2), wv, lbase + slot * SLOTB + lw * 6144 + 3072);
  };
  auto load_tag = [&]() {
    u32x2 t = __builtin_amdgcn_raw_buffer_load_b64(rs, FL_EPOCH, 0, 16);
    t[0] += 1u; t[1] += 0x9E3779B9u;
    PIN(t);
    return t;
  };
  auto exchange = [&](const u32x2& tag, unsigned line0, int me) {
    const int lane = fresh_lane();
    __builtin_amdgcn_raw_buffer_store_b64(tag, rs, lane == 0 ? line0 + me * 128 : OOB, 0, 16);
    if (1) {
      unsigned po = lane < 16 ? line0 + lane * 128 : OOB;
      const u32x4 srd = {(unsigned)(uintptr_t)ws, (unsigned)((uintptr_t)ws >> 32) & 0xffffu, WS_BYTES, 0x00020000u};
      for (unsigned spins = 0;; ++spins) {
        u32x2 v = {0u, 0u};
        asm volatile("buffer_atomic_or_x2 %0, %1, %2, 0 offen sc0 sc1\n\ts_waitcnt vmcnt(0)" : "+v"(v) : "v"(po), "s"(srd) : "memory");
        po = ((v[0] == tag[0]) & (v[1] == tag[1])) ? OOB : po;
        if (__all(po == OOB) || spins > SPIN_CAP) break;
      }
      return;
    }
    __builtin_amdgcn_s_sleep(8);
    for (unsigned spins = 0;; ++spins) {
      const u32x2 v = __builtin_amdgcn_raw_buffer_load_b64(rs, line0 + (lane & 15) * 128, 0, 16);
      if (__all((v[0] == tag[0]) & (v[1] == tag[1])) || spins > SPIN_CAP) break;
      asm volatile("s_sleep 1" ::: "memory");
    }
  };
  if (wid >= 4) {
    auto wo_rows = [&](int g0, int n, int gs) {
      const int lane = fresh_lane();
      unsigned wov[4];
#pragma unroll
      for (int i = 0; i < 4; ++i) wov[i] = (unsigned)((8 * i + (lane >> 3)) * (D_ * 2) + ((lane & 7) ^ (4 * ((lane >> 4) & 1))) * 16 - i * 1024);
#pragma unroll 1
      for (int i = 0, k = (2 * (tm & 7)) % n, g; i < n && !0; ++i, k = k + 1 == n ? 0 : k + 1) g = g0 + gs * k, glds_v4t<3, 1>(reinterpret_cast<const char*>(wo) + (size_t)((64 * tn + 32 * g) & 1023) * (D_ * 2) + tn * 128, wov, lbase + g * 4096);
    };
    if (wid < 6) {
      const int lane = fresh_lane();
      unsigned wv[3];
      stage_lanes(wv);
      __builtin_amdgcn_s_setprio(3);
      const unsigned fra = lbase + L_FREE + (lane & 3) * 256, fla = lbase + L_FULL + lw * 256 + lane * 4;
      const rsrc_t rw = __builtin_amdgcn_make_buffer_rsrc(const_cast<char*>(wb), 0, 0 ? 0 : 6 * MBY - (16 * lw * 3 * D_ + tn * 64) * 2, 0x00020000);
#pragma unroll 1
      for (int L = 0; L < 2; ++L) {
        const int s0 = NS, lead = L ? 8 : NS - 1;
        int slot = 0;
        if (L == 0) {
#pragma unroll 1
          for (int i = 0, s = tm & 7; i < 2 * NS; ++i) {
            glds_v3(wb + (size_t)((s + 2 * tn) & 31) * (32 * 3 * D_ * 2) + (i & 1) * (8 * 3 * D_ * 2), wv, lbase + s * SLOTB + lw * 6144 + (i & 1) * 3072);
            asm volatile("s_waitcnt vmcnt(6)" ::: "memory");
            if (i & 1) s = s + 1 == NS ? 0 : s + 1;
          }
          asm volatile("s_waitcnt vmcnt(0)\n\tds_write_b32 %0, %1" :: "v"(fla), "v"(NS) : "memory");
          STAMPV(1);
        }
#pragma unroll 1
        for (int s = NS; s < 32; ++s) {
          lds_wait_ge(fra, 32 * L + s - lead);
          stage(wv, s, slot);
          slot = (slot == NS - 1) ? 0 : slot + 1;
          if (s >= s0 + DD) asm volatile("s_waitcnt vmcnt(%0)\n\tds_write_b32 %1, %2" :: "n"(DD * 6), "v"(fla), "v"(32 * L + s - DD + 1) : "memory");
        }
#pragma unroll
        for (int d = DD - 1; d >= 0; --d) asm volatile("s_waitcnt vmcnt(%0)\n\tds_write_b32 %1, %2" :: "n"(d * 6), "v"(fla), "v"(32 * L + 32 - d) : "memory");
        u32x2 tag = *reinterpret_cast<const u32x2*>(smem + L_TAG);
        PIN(tag);
        BAR_LGKM();
        if (lw == 0) {
          exchange(tag, FL_KV + (L * 16 + tn) * 2048, tm);
          lds_wait_ge(fra, 32 * L + 32);
          lds_set(lbase + L_W + 16, L + 1);
        } else lds_wait_ge<8>(lbase + L_W + 16, L + 1);
        __builtin_amdgcn_s_sleep(24);
        wo_rows(lw, 15, 2);
        u32x4 pre[NS * 6];
        if (L == 0) {
#pragma unroll
          for (int s = 0; s < NS; ++s) {
#pragma unroll
            for (int i = 0; i < 6; ++i)
              pre[s * 6 + i] = __builtin_amdgcn_raw_buffer_load_b128(rw, wv[i % 3], ((s + 2 * tn) & 31) * (32 * 3 * D_ * 2) + (i / 3) * (8 * 3 * D_ * 2) + (i % 3) * 1024, 0), PACE();
          }
        } else {
#pragma unroll
          for (int i = 0; i < NS * 6; ++i) asm volatile("" : "=v"(pre[i]));
        }
        BAR_ALL();
        if (lw == 0) {
          exchange(tag, FL_O + (L * 16 + tm) * 2048, tn); lds_set(lbase + L_W + 20, L + 1);
          __builtin_amdgcn_raw_buffer_store_b64(tag, rs, (bid == 0 && lane == 0) ? FL_EPOCH : OOB, 0, 16);
        }
        else wo_rows(30, 2, 1);
        BAR_ALL();
        if (L == 0) {
          char* d = smem + lw * 6144 + fresh_lane() * 16;
#pragma unroll
          for (int s = 0; s < NS; ++s) {
#pragma unroll
            for (int i = 0; i < 6; ++i) *reinterpret_cast<u32x4*>(d + s * SLOTB + i * 1024) = pre[s * 6 + i];
            lds_set(fla, 32 + s + 1);
          }
          STAMPV(2);
          lds_wait_ge<2>(lbase + L_W + 24, 2);
        }
      }
      STAMP_END;
      return;
    }
    const int a = wid - 6;
    const bool ffn = loc < 28;
    const int grp = xcd * 7 + (ffn ? loc >> 2 : 0), qa = (loc & 3) * 2 + a;
    const int kr = loc, cg = xcd;
    const unsigned wa = lbase + L_W;
    if (a) {
      const int lane = fresh_lane();
      unsigned t = __builtin_amdgcn_raw_buffer_load_b32(rs, 1 ? lane * MBY + MBY - 128 : OOB, 0, 16);
      if (1) {
        const char *c0, *c1;
        CODE_ADDR(mega_kernel, c0); CODE_ADDR(end_marker, c1);
        const long long o = (loc * 64 + lane) * 32;
        t |= *reinterpret_cast<const __attribute__((address_space(1))) unsigned*>((uintptr_t)(c0 + (o + 4 <= c1 - c0 ? o : 0)));
      }
      const u32x2 tag = load_tag();
      *reinterpret_cast<u32x2*>(smem + L_TAG) = tag;
      PIN(t);
      lds_wait_ge<0>(wa + 64 + 4 * (lane & 3), 2);
      exchange(tag, FL_X + tm * 2048, tn);
      lds_set(wa + 24, 1);
    } else {
      unsigned wv[3];
      stage_lanes(wv);
#pragma unroll 1
      for (int i = 0; i < 4 * 0; ++i) {
        glds_v3(reinterpret_cast<const char*>(wqkv + tn * 64) + (size_t)((4 * (NS + 2 * tn + 3 * (tm & 7)) + i) & 127) * (8 * 3 * D_ * 2), wv, lbase + NS * SLOTB);
        asm volatile("s_waitcnt vmcnt(6)" ::: "memory");
      }
      STAMPV(11);
      lds_wait_ge<2>(wa + 24, 1);
    }
    __builtin_amdgcn_s_sleep(16);
    auto transpose8 = [](u32x4* v) {
      u32x4 t[8];
#pragma unroll
      for (int j = 0; j < 8; ++j)
#pragma unroll
        for (int e = 0; e < 4; ++e) t[j][e] = (j & 1) ? PERM_HI(v[2 * e][j >> 1], v[2 * e + 1][j >> 1]) : PERM_LO(v[2 * e][j >> 1], v[2 * e + 1][j >> 1]);
#pragma unroll
      for (int j = 0; j < 8; ++j) { v[j] = t[j]; PIN(v[j]); }
    };
    u32x4 gf[32];
    u32x4 wdf[16];
    auto wait_go = [&](int L) {
      lds_wait_ge<8>(lbase + L_W + 16, L + 1);
      __builtin_amdgcn_s_sleep(24);
    };
    auto load_wd = [&](unsigned dv) {
      const rsrc_t rd = __builtin_amdgcn_make_buffer_rsrc(const_cast<u16*>(wd) + (size_t)grp * 64 * D_ + 128 * qa, 0, 0 ? 0 : 64 * D_ * 2 - 256 * qa, 0x00020000);
#pragma unroll
      for (int i = 0; i < 16; ++i) {
        wdf[i] = __builtin_amdgcn_raw_buffer_load_b128(rd, dv, (32 * (i >> 3) + (i & 7)) * (D_ * 2), 0);
        asm volatile("s_sleep 2\n\ts_waitcnt vmcnt(3)" ::: "memory");
      }
    };
    {
      LANE_VARS;
      const u32x4 sv = {SENT, SENT, SENT, SENT};
#pragma unroll
      for (int i = 0; i < 6; ++i) {
        const unsigned o = ((i * 256 + bid) * 128 + a * 64 + lane) * 16;
        stg(rs, o < SX_BYTES ? OFF_SX + o : OOB, sv);
      }
      if (a == 0) {
        const u32x4 w0 = *reinterpret_cast<const u32x4*>(w_r + lane * 8), w1 = *reinterpret_cast<const u32x4*>(w_r + 512 + lane * 8);
        float t = 0.f;
#pragma unroll
        for (int e = 0; e < 4; ++e) t = fadd(fadd(t, fadd(lo16(w0[e]), hi16(w0[e]))), fadd(lo16(w1[e]), hi16(w1[e])));
        words[0] = __float_as_uint(wave_sum(t));
        if ((lane >> 3) == (tn & 7)) *reinterpret_cast<u32x4*>(smem + L_WR + (lane & 7) * 16) = tn < 8 ? w0 : w1;
      }
      const u16* p = wqkv + (size_t)(64 * tm + 32 * a + hi) * (3 * D_) + (c24 >> 3) * D_ + tn * 64 + (c24 & 7) * 8;
      float cs[8] = {0.f, 0.f, 0.f, 0.f, 0.f, 0.f, 0.f, 0.f};
      u32x4 v[16];
#pragma unroll
      for (int i = 0; i < 16; ++i) v[i] = *reinterpret_cast<const u32x4*>(p + (size_t)i * (2 * 3 * D_));
      SBAR();
#pragma unroll
      for (int i = 0; i < 16; ++i)
#pragma unroll
        for (int e = 0; e < 4; ++e) { cs[2 * e] = fadd(cs[2 * e], lo16(v[i][e])); cs[2 * e + 1] = fadd(cs[2 * e + 1], hi16(v[i][e])); }
      u32x4 o;
#pragma unroll
      for (int e = 0; e < 4; ++e) { const float x = half_swap_sum(cs[e]), y = half_swap_sum(cs[4 + e]); o[e] = __float_as_uint(hi ? y : x); }
      stg(rs, r32 < 24 ? OFF_CS + (tn * 32 + tm * 2 + a) * 768 + r32 * 32 + hi * 16 : OOB, o);
    }
    {
      BAR_ALL();
      LANE_VARS;
      const char* gp = reinterpret_cast<const char*>(((lane & 8) ? wu : wg) + (size_t)(128 * qa + 16 * hi + 4 * tblk) * F_ + 64 * grp + (lane & 7) * 8);
      unsigned dv = (lane >> 4) * (8 * D_ * 2) + (lane & 15) * 16;
      PIN(gp); PIN(dv);
      unsigned sv[3];
#pragma unroll
      for (int i = 0; i < 3; ++i) { const int li = lane + 64 * i, k = li / 24, c = li % 24; sv[i] = (unsigned)(k * (3 * D_ * 2) + (c >> 3) * (D_ * 2) + (c & 7) * 16 - i * 1024); PIN(sv[i]); }
      wait_go(0);
#pragma unroll
      for (int i = 0; i < 32; ++i) {
        gf[i] = *reinterpret_cast<const u32x4*>(gp + (size_t)(0 ? 0 : 32 * (i >> 3) + 8 * ((i >> 2) & 1) + (i & 3)) * (F_ * 2));
        asm volatile("s_sleep 2\n\ts_waitcnt vmcnt(3)" ::: "memory");
      }
      load_wd(dv);
      STAMPV(7);
      BARRIER();
      __builtin_amdgcn_s_sleep(2);
#pragma unroll
      for (int g = 0; g < 4; ++g) glds_v3(wqkv + (size_t)(32 * kr + 8 * g) * (3 * D_) + (2 * cg + a) * 64, sv, lbase + L_SL + a * 12288 + g * 3072);
      transpose8(gf); transpose8(gf + 8); transpose8(gf + 16); transpose8(gf + 24);
      transpose8(wdf); transpose8(wdf + 8);
      {
        LANE_VARS;
        f32x4 t[2];
#pragma unroll
        for (int c = 0; c < 2; ++c) {
          u32x4 v[8];
#pragma unroll
          for (int i = 0; i < 8; ++i) v[i] = ldg(rs, OFF_CS + (tn * 32 + 16 * c + 2 * i + hi) * 768 + a * 384 + c24 * 16);
          SBAR();
          t[c] = sum4<8>(v);
          SBAR();
        }
#pragma unroll
        for (int e = 0; e < 4; ++e) t[0][e] = half_swap_sum(fadd(t[0][e], t[1][e]));
        if (lane < 24) *reinterpret_cast<f32x4*>(smem + L_CS + a * 384 + lane * 16) = t[0];
      }
      u32x2 tag = *reinterpret_cast<const u32x2*>(smem + L_TAG);
      PIN(tag);
      BAR_ALL();
      if (a) {
        lds_wait_ge<0>(wa + 64 + 4 * (lane & 3), 3);
        exchange(tag, FL_X + (16 + tm) * 2048, tn);
        lds_set(wa + 24, 2);
        STAMPV(12);
      }
    }
#pragma unroll 1
    for (int L = fresh_s(0);; ++L) {
      const unsigned sx = OFF_SX + L * SX_LAYER;
      STAMPV(1 + 16 * L);
      if (0 && L == 0) { words[3] = 5000u; lds_set(wa + 4 + 4 * a, 1); } else {
      u32x4 hf[5];
      unsigned key;
      { LANE_VARS;
      {
        unsigned cdst = lane < 16 ? sx + SX_ROW + (lane >> 3) * 32768 + tm * 2048 + tn * 128 + (lane & 7) * 16 : sx + SX_CAND + (tn * 16 + tm) * 128;
        cdst = (a == 0 && lane < 17) ? cdst : OOB;
        PIN(cdst);
        lds_wait_ge<0>(wa + 32 + 4 * tp, L + 1);
        key = *reinterpret_cast<const unsigned*>(smem + L_CAND + tp * 144 + 128);
        key = max(key, dpp<0xB1>(key)); key = max(key, dpp<0x4E>(key));
        const char* cp = smem + L_CAND + (((2047 - (int)(key & 2047u)) >> 5) & 3) * 144;
        const u32x4 c = *reinterpret_cast<const u32x4*>(cp + (lane < 16 ? lane & 7 : 8) * 16), rec = *reinterpret_cast<const u32x4*>(cp + 128);
        u32x4 o;
#pragma unroll
        for (int e = 0; e < 4; ++e) { unsigned h = ln_pair_s(c[e], -U2F(rec[1]), U2F(rec[2])); PIN(h); o[e] = nz(lane < 8 ? h : c[e]); }
        stg(rs, cdst, o);
      }
      STAMPV(8 + 16 * L);
      __builtin_amdgcn_s_sleep(13);
      poll_all<5>(hf, L == 0, [&](u32x4 (&t)[5]) {
        t[4][0] = __builtin_amdgcn_raw_buffer_load_b32(rs, sx + SX_CAND + (tn * 16 + (lane & 15)) * 128, 0, 16);
#pragma unroll
        for (int i = 0; i < 4; ++i) t[i] = ldg(rs, sx + SX_ROW + ((2047 - (int)(key & 2047u)) >> 7) * 2048 + (16 * qa + 4 * i + (lane >> 4)) * 16);
        t[4][1] = t[4][2] = t[4][3] = key;
      }, [&](const u32x4 (&t)[5]) {
        unsigned k = t[4][0] == SENT ? 0u : t[4][0];
        k = max(k, dpp<0xB1>(k)); k = max(k, dpp<0x4E>(k)); k = max(k, dpp<0x141>(k)); k = max(k, dpp<0x140>(k));
        key = max(key, k);
        return none_sent(t) & (key == t[4][1]);
      });
      key = __builtin_amdgcn_readfirstlane(key);
      words[3] = 2047u - (key & 2047u);
      if (L == 1) lds_set(wa + 4 + 4 * a, 2);
      }
      STAMPV(2 + 16 * L);
      f32x4 d[8];
      const f32x4 zero = {0.f, 0.f, 0.f, 0.f};
      SBAR();
      __builtin_amdgcn_s_setprio(3);
#pragma unroll
      for (int t = 0; t < 32; ++t) d[t & 7] = MFMA16(__builtin_bit_cast(s16x8, hf[t >> 3]), __builtin_bit_cast(s16x8, gf[t]), t < 8 ? zero : d[t & 7]);
      SBAR();
      __builtin_amdgcn_s_setprio(0);
      LANE_VARS;
      const int idx = 2047 - (int)(key & 2047u), win = idx >> 7;
      const bool owner = win == tm;
      auto pick2 = [&](const f32x4 (&d)[8]) {
        u32x2 o;
#pragma unroll
        for (int e = 0; e < 2; ++e) {
          const float x = tblk ? d[2 + e][0] : d[e][0], y = tblk ? d[6 + e][0] : d[4 + e][0];
          o[e] = nzf(hi ? y : x);
        }
        return o;
      };
      if (ffn) {
        const unsigned ppos = (lane & 15) * 32 + (lane >> 4) * 8;
        __builtin_amdgcn_raw_buffer_store_b64(pick2(d), rs, sx + SX_GP + (grp * 8 + qa) * 512 + ppos, 0, 16);
        STAMPV(9 + 16 * L);
        __builtin_amdgcn_s_sleep(13);
        if (L == 0) __builtin_amdgcn_s_sleep(0);
        u32x2 gu[8];
        for (unsigned spins = 0;; ++spins) {
#pragma unroll
          for (int i = 0; i < 8; ++i) gu[i] = __builtin_amdgcn_raw_buffer_load_b64(rs, sx + SX_GP + (grp * 8 + i) * 512 + lane * 8, 0, 16);
          SBAR();
          unsigned m = 0u;
#pragma unroll
          for (int i = 0; i < 8; ++i) m = max(max(m, gu[i][0]), gu[i][1]);
          if (__all(m != SENT) || spins > SPIN_CAP) break;
          poll_pause(L == 0);
        }
        STAMPV(10 + 16 * L);
        float t0 = opq(U2F(gu[0][0])), t1 = opq(U2F(gu[0][1]));
#pragma unroll
        for (int i = 1; i < 8; ++i) { t0 = fadd(t0, U2F(gu[i][0])); t1 = fadd(t1, U2F(gu[i][1])); }
        auto sw = __builtin_amdgcn_permlane32_swap(__float_as_uint(t0), __float_as_uint(t1), false, false);
        const float gv = rbf(U2F(sw[0])), uv = rbf(U2F(sw[1]));
        reinterpret_cast<u16*>(smem + L_GU + a * 128)[2 * r32 + hi] = (u16)pack2(fmul(rbf(fmul(gv, RCP(fadd(1.0f, __expf(-gv))))), uv), 0.f);
        asm volatile("s_waitcnt lgkmcnt(0)" ::: "memory");
        const s16x8 a0 = *reinterpret_cast<const s16x8*>(smem + L_GU + a * 128 + (lane >> 4) * 16), a1 = *reinterpret_cast<const s16x8*>(smem + L_GU + a * 128 + 64 + (lane >> 4) * 16);
        SBAR();
        __builtin_amdgcn_s_setprio(3);
#pragma unroll
        for (int t = 0; t < 16; ++t) d[t & 7] = MFMA16(t < 8 ? a0 : a1, __builtin_bit_cast(s16x8, wdf[t]), t < 8 ? zero : d[t & 7]);
        SBAR();
        __builtin_amdgcn_s_setprio(0);
        __builtin_amdgcn_raw_buffer_store_b64(pick2(d), rs, sx + SX_YP + grp * 4096 + qa * 512 + ppos, 0, 16);
      }
      STAMPV(3 + 16 * L);
      if (L == 1 && (cg != (kr & 7) || a)) { STAMP_END; __builtin_amdgcn_endpgm(); }
      s16x8 wpf[12];
#pragma unroll
      for (int t = 0; t < 12; ++t) wpf[t] = tr2(smem + L_SL + a * 12288 + (16 * hi + 4 * tblk + tq) * 384 + t * 32 + tp * 8, 8 * 384);
#pragma unroll
      for (int t = 0; t < 12; ++t) PIN(wpf[t]);
      if (L == 0 && !owner) lds_set(wa + 4 + 4 * a, 1);
      __builtin_amdgcn_s_sleep(13 - 4);
      if (L == 0) __builtin_amdgcn_s_sleep(0);
      if (L == 0 && !ffn) __builtin_amdgcn_s_sleep(64);
      const unsigned rk = key >> 16;
      float rsig = rbf(RCP(fadd(1.0f, __expf(-lo16((rk & 0x8000u) ? (rk & 0x7fffu) : (~rk & 0xffffu))))));
      PIN(rsig);
      const int n = 4 * (lane & 7) + ((lane >> 3) & 3);
      const int fo = ((n >> 4) * 2 + ((n >> 2) & 1)) * 8 + ((n >> 3) & 1) * 4 + (n & 3);
      u32x4 pv[8];
      poll_all<8>(pv, L == 0, [&](u32x4 (&t)[8]) {
#pragma unroll
        for (int i = 0; i < 7; ++i) t[i] = ldg(rs, sx + SX_YP + (8 * i + (lane >> 3)) * 4096 + kr * 128 + (lane & 7) * 16);
        t[7][0] = t[7][2] = __builtin_amdgcn_raw_buffer_load_b32(rs, sx + SX_ROW + win * 2048 + kr * 64 + (fo >> 1) * 4, 0, 16);
        t[7][1] = t[7][3] = __builtin_amdgcn_raw_buffer_load_b32(rs, sx + SX_ROW + 32768 + win * 2048 + kr * 64 + (fo >> 1) * 4, 0, 16);
      }, [&](const u32x4 (&t)[8]) { return none_sent(t); });
      const float hv = (fo & 1) ? hi16(pv[7][0]) : lo16(pv[7][0]), xv = (fo & 1) ? hi16(pv[7][1]) : lo16(pv[7][1]);
      STAMPV(4 + 16 * L);
      float ys;
      {
        f32x4 t = sum4<7>(pv);
#pragma unroll
        for (int e = 0; e < 4; ++e) {
          t[e] = fadd(t[e], U2F(dpp<0x128>(__float_as_uint(t[e]))));
          t[e] = half_swap_sum(row_swap_sum(t[e]));
        }
        const float x = (lane & 8) ? t[1] : t[0], y = (lane & 8) ? t[3] : t[2];
        ys = tblk ? y : x;
      }
      const unsigned ob = pack2(fadd(xv, rbf(fadd(hv, rbf(fmul(rbf(ys), rsig))))), 0.f) & 0xffffu;
      if (L == 1) {
        if (lane < 32) out[(size_t)idx * D_ + kr * 32 + n] = (u16)ob;
        STAMPV(5 + 16 * L);
        STAMP_END;
        __builtin_amdgcn_endpgm();
      }
      reinterpret_cast<u16*>(smem + L_X2F + a * 64)[fo] = (u16)ob;
      asm volatile("s_waitcnt lgkmcnt(0)" ::: "memory");
      {
        const s16x8 xa = *reinterpret_cast<const s16x8*>(smem + L_X2F + a * 64 + (lane >> 4) * 16);
        const u32x4 xf = *reinterpret_cast<const u32x4*>(smem + L_X2F + a * 64 + tp * 16);
        f32x4 z[12];
        SBAR();
        __builtin_amdgcn_s_setprio(3);
#pragma unroll
        for (int t = 0; t < 12; ++t) z[t] = MFMA16(xa, wpf[t], zero);
        SBAR();
        __builtin_amdgcn_s_setprio(0);
        const float x2 = lo16(ob), s1 = half_sum(x2), s2 = half_sum(fmul(x2, x2));
        u32x4 o;
#pragma unroll
        for (int e = 0; e < 4; ++e) {
          const unsigned sp = lane < 52 ? (e == 0 ? __float_as_uint(s1) : __float_as_uint(s2)) : xf[e];
          const unsigned x = __float_as_uint(tblk ? z[4 + e][0] : z[e][0]), y = tblk ? sp : __float_as_uint(z[8 + e][0]);
          o[e] = nz(hi ? y : x);
        }
        const unsigned pos = lane < 24 ? lane * 16 : lane < 48 ? lane * 16 + 128 : lane < 52 ? (lane & 1) * 512 + 384 : 400 + tp * 16;
        stg(rs, lane < 56 ? OFF_SX + SX_QP + ((2 * cg + a) * 32 + kr) * 1024 + pos : OOB, o);
      }
      STAMPV(5 + 16 * L);
      if (owner) {
        __builtin_amdgcn_s_sleep(13);
        const int c25 = r32 < 25 ? r32 : 24, pl = 24 * a + c24, col = 64 * (pl >> 4) + (pl & 15);
        float cs[4];
#pragma unroll
        for (int e = 0; e < 4; ++e) { cs[e] = reinterpret_cast<const float*>(smem + L_CS)[col + 16 * e]; PIN(cs[e]); }
        u32x4 qv[17];
        poll_all<17>(qv, true, [&](u32x4 (&t)[17]) {
#pragma unroll
          for (int i = 0; i < 16; ++i) t[i] = ldg(rs, OFF_SX + SX_QP + (tn * 32 + 2 * i + hi) * 1024 + a * 512 + c25 * 16);
          t[16] = ldg(rs, OFF_SX + SX_QP + (tn * 34 + hi) * 1024 + 400 + tp * 16);
        }, [&](const u32x4 (&t)[17]) { return none_sent(t); });
        *reinterpret_cast<u32x4*>(smem + L_X2P + hi * 64 + tp * 16) = qv[16];
        f32x4 t = sum4<16>(qv);
#pragma unroll
        for (int e = 0; e < 4; ++e) t[e] = half_swap_sum(t[e]);
        const float mean = fmul(U2F(__builtin_amdgcn_readlane(__float_as_uint(t[0]), 24)), 1.0f / 1024.0f);
        const float var = fmaxf(ffma(-mean, mean, fmul(U2F(__builtin_amdgcn_readlane(__float_as_uint(t[1]), 24)), 1.0f / 1024.0f)), 0.f);
        float mb, den;
        ln_scalars(mean, var, mb, den);
        const float rd = RCP(den);
        if (lane < 24) {
#pragma unroll
          for (int e = 0; e < 4; ++e) reinterpret_cast<float*>(smem + L_PATCH)[col + 16 * e] = fmul(ffma(-mb, cs[e], t[e]), rd);
        }
        lds_set(wa + 4 + 4 * a, L + 1);
      }
      STAMPV(6 + 16 * L);
      }
      BAR_ALL();
      unsigned dv = fresh_lane();
      dv = (dv >> 4) * (8 * D_ * 2) + (dv & 15) * 16;
      PIN(dv);
      wait_go(1);
      load_wd(dv);
      STAMPV(23);
      BARRIER();
      transpose8(wdf); transpose8(wdf + 8);
      BARRIER();
    }
  }
  const int tm_ = tm, tn_ = tn, wid_ = wid;
  {
    const int tid = threadIdx.x, lane = tid & 63, r32 = lane & 31, row = 2 * wid + (lane >> 5);
    char* sm = smem + L_XCH;
    u32x4 v[4];
    asm volatile("global_load_dwordx4 %0, %4, off\n\tglobal_load_dwordx4 %1, %4, off offset:512\n\tglobal_load_dwordx4 %2, %4, off offset:1024\n\tglobal_load_dwordx4 %3, %4, off offset:1536"
                 : "=&v"(v[0]), "=&v"(v[1]), "=&v"(v[2]), "=&v"(v[3]) : "v"(x0 + (size_t)(tm * 128 + tn * 8 + row) * D_ + r32 * 8) : "memory");
    int pass = fresh_s(0);
#pragma unroll 1
    for (;;) {
      asm volatile("s_cmp_eq_u32 %4, 0\n\ts_cbranch_scc1 1f\n\ts_waitcnt vmcnt(0)\n1:" : "+v"(v[0]), "+v"(v[1]), "+v"(v[2]), "+v"(v[3]) : "s"(pass) : "memory", "scc");
      float s = 0.f, s2 = 0.f;
#pragma unroll
      for (int i = 0; i < 4; ++i)
#pragma unroll
        for (int e = 0; e < 4; e += 2) { s = dot2(v[i][e], 0x3f803f80u, s); s2 = dot2(v[i][e + 1], 0x3f803f80u, s2); }
      const float mean = half_sum(s + s2) * (1.0f / 1024.0f);
      f32x2 m2 = {0.f, 0.f};
#pragma unroll
      for (int i = 0; i < 4; ++i)
#pragma unroll
        for (int e = 0; e < 4; ++e) { const f32x2 d = f32x2{lo16(v[i][e]), hi16(v[i][e])} - mean; m2 += d * d; }
      float mb, den;
      ln_scalars(mean, half_sum(m2[0] + m2[1]) * (1.0f / 1024.0f), mb, den);
      const float rd = 1.0f / den;
#pragma unroll
      for (int i = 0; i < 4; ++i) {
        char* p = sm + ((16 * i + (r32 >> 1)) * 2) * 144 + row * 16 + (r32 & 1) * 8;
        u32x2 lo = {ln_pair(v[i][0], mb, rd), ln_pair(v[i][1], mb, rd)}, hi = {ln_pair(v[i][2], mb, rd), ln_pair(v[i][3], mb, rd)};
        *reinterpret_cast<u32x2*>(p) = lo;
        *reinterpret_cast<u32x2*>(p + 144) = hi;
      }
      STAMPV(37);
      lds_set(lbase + L_W + 64 + 4 * wid, pass);
      lds_wait_ge<0>(lbase + L_W + 64 + 4 * (lane & 3), pass);
      u32x4 t[4];
#pragma unroll
      for (int i = 0; i < 4; ++i) t[i] = *reinterpret_cast<const u32x4*>(sm + (i * 32 + (tid >> 3)) * 144 + (tid & 7) * 16);
      SBAR();
      unsigned dst = OFF_HF + (tm * 4 + (tn >> 2)) * TS + (tn & 3) * 128 + (tid >> 3) * 512 + (tid & 7) * 16;
      PIN(dst);
      if (pass) {
#pragma unroll
        for (int i = 0; i < 4; ++i) stg(rs, dst + i * 16384, t[i]);
        break;
      }
#pragma unroll
      for (int i = 0; i < 4; ++i) PIN(t[i]);
      pass = fresh_s(1);
    }
    DRAIN();
    lds_set(lbase + L_W + 64 + 4 * wid, 2);
    STAMPV(38);
  }
  const u32x4 srd = {(unsigned)(uintptr_t)ws, (unsigned)((uintptr_t)ws >> 32) & 0xffffu, WS_BYTES, 0x00020000u};
  u32x4 x1p[4];
  asm volatile("" : "=v"(x1p[0]), "=v"(x1p[1]), "=v"(x1p[2]), "=v"(x1p[3]));
#pragma unroll 1
  for (int L = 0; L < 2; ++L) {
    const unsigned sx = OFF_SX + L * SX_LAYER;
    u32x8 q[4];
    u32x16 kva, kvb;
    f32x16 oz[5];
    {
      f32x16 acc[6];
      u32x16 stA, stB, xr0, xr1, xr2, xr3;
      u32x4 stC;
      asm volatile("" : "={v[112:127]}"(stB), "={v[128:131]}"(stC));
      { PHASE_VARS;
      stB[8] = lane * 16 + (L ? OFF_X1 : OFF_HF) + tile * TS;
      stB[9] = lbase + wrow * 384 + (((4 * hh + wch) ^ wsw) << 4) + 8 * (tp & 1);
      stB[10] = lbase + wrow * 384 + (((4 * (hh ^ 1) + wch) ^ wsw) << 4) + 8 * (tp & 1);
      stB[13] = lbase + L_FULL + (lane & 1) * 256;
      stB[15] = lbase + L_FREE + wid * 256 + lane * 4;
      stC[0] = 32 * L; }
      unsigned soff, slot, need = 32 * L + 1;
      if (L == 0) {
        lds_wait_ge<0>(lbase + L_W + 24, 1);
        STAMPV(39);
        soff = tn * 4096;
        asm volatile(
          "buffer_load_dwordx4 v[132:135], v120, s[40:43], s44 offen offset:0 sc1\n\t"
          "buffer_load_dwordx4 v[136:139], v120, s[40:43], s44 offen offset:1024 sc1\n\t"
          "buffer_load_dwordx4 v[140:143], v120, s[40:43], s44 offen offset:2048 sc1\n\t"
          "buffer_load_dwordx4 v[144:147], v120, s[40:43], s44 offen offset:3072 sc1\n\t"
          "s_add_u32 s44, s44, 0x1000\n\t"
          "s_and_b32 s44, s44, 0xffff\n\t"
          "buffer_load_dwordx4 v[148:151], v120, s[40:43], s44 offen offset:0 sc1\n\t"
          "buffer_load_dwordx4 v[152:155], v120, s[40:43], s44 offen offset:1024 sc1\n\t"
          "buffer_load_dwordx4 v[156:159], v120, s[40:43], s44 offen offset:2048 sc1\n\t"
          "buffer_load_dwordx4 v[160:163], v120, s[40:43], s44 offen offset:3072 sc1\n\t"
          "s_add_u32 s44, s44, 0x1000\n\t"
          "s_and_b32 s44, s44, 0xffff\n\t"
          "buffer_load_dwordx4 v[164:167], v120, s[40:43], s44 offen offset:0 sc1\n\t"
          "buffer_load_dwordx4 v[168:171], v120, s[40:43], s44 offen offset:1024 sc1\n\t"
          "buffer_load_dwordx4 v[172:175], v120, s[40:43], s44 offen offset:2048 sc1\n\t"
          "buffer_load_dwordx4 v[176:179], v120, s[40:43], s44 offen offset:3072 sc1\n\t"
          "s_add_u32 s44, s44, 0x1000\n\t"
          "s_and_b32 s44, s44, 0xffff\n\t"
          "buffer_load_dwordx4 v[180:183], v120, s[40:43], s44 offen offset:0 sc1\n\t"
          "buffer_load_dwordx4 v[184:187], v120, s[40:43], s44 offen offset:1024 sc1\n\t"
          "buffer_load_dwordx4 v[188:191], v120, s[40:43], s44 offen offset:2048 sc1\n\t"
          "buffer_load_dwordx4 v[192:195], v120, s[40:43], s44 offen offset:3072 sc1\n\t"
          : "={v[132:147]}"(xr0), "={v[148:163]}"(xr1), "={v[164:179]}"(xr2), "={v[180:195]}"(xr3), "+{s44}"(soff) : "{v[112:127]}"(stB), "{s[40:43]}"(srd) : "scc", "memory");
      } else {
        xr0 = cat4(x1p[0], x1p[1], x1p[2], x1p[3]);
        asm volatile("" : "={v[148:163]}"(xr1), "={v[164:179]}"(xr2), "={v[180:195]}"(xr3));
      }
      STAMPV(1 + 16 * L);
      asm volatile(
        "v_mov_b32 v123, v121\n\t"
        "v_mov_b32 v124, v122\n\t"
        "s_mov_b64 vcc, -1\n\t"
        "s_cbranch_vccnz 10f\n\t"
        "11:\n\t"
        "s_mov_b32 s46, 12288\n\t"
        "s_add_u32 s47, s47, 1\n\t"
        "ds_read_b64_tr_b16 v[96:97], v123 offset:0\n\t"
        "ds_read_b64_tr_b16 v[98:99], v123 offset:3072\n\t"
        "ds_read_b64_tr_b16 v[100:101], v124 offset:0\n\t"
        "ds_read_b64_tr_b16 v[102:103], v124 offset:3072\n\t"
        "ds_read_b64_tr_b16 v[104:105], v123 offset:128\n\t"
        "ds_read_b64_tr_b16 v[106:107], v123 offset:3200\n\t"
        "ds_read_b64_tr_b16 v[108:109], v124 offset:128\n\t"
        "ds_read_b64_tr_b16 v[110:111], v124 offset:3200\n\t"
        "ds_read_b64_tr_b16 v[112:113], v123 offset:256\n\t"
        "ds_read_b64_tr_b16 v[114:115], v123 offset:3328\n\t"
        "ds_write_b32 v127, v128\n\t"
        "ds_read_b64_tr_b16 v[116:117], v124 offset:256\n\t"
        "ds_read_b64_tr_b16 v[118:119], v124 offset:3328\n\t"
        "s_waitcnt vmcnt(15) lgkmcnt(11)\n\t"
        "v_mfma_f32_32x32x16_bf16 v[0:15], v[96:99], v[132:135], 0\n\t"
        "ds_read_b32 v126, v125\n\t"
        "ds_read_b64_tr_b16 v[96:97], v123 offset:6144\n\t"
        "ds_read_b64_tr_b16 v[98:99], v123 offset:9216\n\t"
        "s_waitcnt lgkmcnt(12)\n\t"
        "v_mfma_f32_32x32x16_bf16 v[16:31], v[100:103], v[132:135], 0\n\t"
        "v_add_u32 v129, s46, v121\n\t"
        "ds_read_b64_tr_b16 v[100:101], v124 offset:6144\n\t"
        "ds_read_b64_tr_b16 v[102:103], v124 offset:9216\n\t"
        "s_waitcnt lgkmcnt(12)\n\t"
        "v_mfma_f32_32x32x16_bf16 v[32:47], v[104:107], v[132:135], 0\n\t"
        "ds_read_b64_tr_b16 v[104:105], v123 offset:6272\n\t"
        "ds_read_b64_tr_b16 v[106:107], v123 offset:9344\n\t"
        "s_waitcnt lgkmcnt(12)\n\t"
        "v_mfma_f32_32x32x16_bf16 v[48:63], v[108:111], v[132:135], 0\n\t"
        "v_add_u32 v130, s46, v122\n\t"
        "ds_read_b64_tr_b16 v[108:109], v124 offset:6272\n\t"
        "ds_read_b64_tr_b16 v[110:111], v124 offset:9344\n\t"
        "s_waitcnt lgkmcnt(12)\n\t"
        "v_mfma_f32_32x32x16_bf16 v[64:79], v[132:135], v[112:115], 0\n\t"
        "ds_read_b64_tr_b16 v[112:113], v123 offset:6400\n\t"
        "ds_read_b64_tr_b16 v[114:115], v123 offset:9472\n\t"
        "s_waitcnt lgkmcnt(10)\n\t"
        "v_mfma_f32_32x32x16_bf16 v[80:95], v[132:135], v[116:119], 0\n\t"
        "v_cmp_gt_u32 vcc, s47, v126\n\t"
        "ds_read_b64_tr_b16 v[116:117], v124 offset:6400\n\t"
        "ds_read_b64_tr_b16 v[118:119], v124 offset:9472\n\t"
        "s_waitcnt vmcnt(14) lgkmcnt(10)\n\t"
        "v_mfma_f32_32x32x16_bf16 v[0:15], v[96:99], v[136:139], v[0:15]\n\t"
        "s_cbranch_vccnz 12f\n\t"
        "13:\n\t"
        "ds_read_b64_tr_b16 v[96:97], v129 offset:0\n\t"
        "ds_read_b64_tr_b16 v[98:99], v129 offset:3072\n\t"
        "s_waitcnt lgkmcnt(10)\n\t"
        "v_mfma_f32_32x32x16_bf16 v[16:31], v[100:103], v[136:139], v[16:31]\n\t"
        "v_add_u32 v128, 1, v128\n\t"
        "ds_read_b64_tr_b16 v[100:101], v130 offset:0\n\t"
        "ds_read_b64_tr_b16 v[102:103], v130 offset:3072\n\t"
        "s_waitcnt lgkmcnt(10)\n\t"
        "v_mfma_f32_32x32x16_bf16 v[32:47], v[104:107], v[136:139], v[32:47]\n\t"
        "ds_read_b64_tr_b16 v[104:105], v129 offset:128\n\t"
        "ds_read_b64_tr_b16 v[106:107], v129 offset:3200\n\t"
        "s_waitcnt lgkmcnt(10)\n\t"
        "v_mfma_f32_32x32x16_bf16 v[48:63], v[108:111], v[136:139], v[48:63]\n\t"
        "s_add_u32 s46, s46, 12288\n\t"
        "s_add_u32 s47, s47, 1\n\t"
        "ds_read_b64_tr_b16 v[108:109], v130 offset:128\n\t"
        "ds_read_b64_tr_b16 v[110:111], v130 offset:3200\n\t"
        "s_waitcnt lgkmcnt(10)\n\t"
        "v_mfma_f32_32x32x16_bf16 v[64:79], v[136:139], v[112:115], v[64:79]\n\t"
        "s_cmp_eq_u32 s46, 110592\n\t"
        "s_cselect_b32 s46, 0, s46\n\t"
        "ds_read_b64_tr_b16 v[112:113], v129 offset:256\n\t"
        "ds_read_b64_tr_b16 v[114:115], v129 offset:3328\n\t"
        "s_waitcnt lgkmcnt(10)\n\t"
        "v_mfma_f32_32x32x16_bf16 v[80:95], v[136:139], v[116:119], v[80:95]\n\t"
        "ds_write_b32 v127, v128\n\t"
        "ds_read_b64_tr_b16 v[116:117], v130 offset:256\n\t"
        "ds_read_b64_tr_b16 v[118:119], v130 offset:3328\n\t"
        "s_waitcnt vmcnt(13) lgkmcnt(11)\n\t"
        "v_mfma_f32_32x32x16_bf16 v[0:15], v[96:99], v[140:143], v[0:15]\n\t"
        "ds_read_b32 v126, v125\n\t"
        "ds_read_b64_tr_b16 v[96:97], v129 offset:6144\n\t"
        "ds_read_b64_tr_b16 v[98:99], v129 offset:9216\n\t"
        "s_waitcnt lgkmcnt(12)\n\t"
        "v_mfma_f32_32x32x16_bf16 v[16:31], v[100:103], v[140:143], v[16:31]\n\t"
        "v_add_u32 v123, s46, v121\n\t"
        "ds_read_b64_tr_b16 v[100:101], v130 offset:6144\n\t"
        "ds_read_b64_tr_b16 v[102:103], v130 offset:9216\n\t"
        "s_waitcnt lgkmcnt(12)\n\t"
        "v_mfma_f32_32x32x16_bf16 v[32:47], v[104:107], v[140:143], v[32:47]\n\t"
        "ds_read_b64_tr_b16 v[104:105], v129 offset:6272\n\t"
        "ds_read_b64_tr_b16 v[106:107], v129 offset:9344\n\t"
        "s_waitcnt lgkmcnt(12)\n\t"
        "v_mfma_f32_32x32x16_bf16 v[48:63], v[108:111], v[140:143], v[48:63]\n\t"
        "v_add_u32 v124, s46, v122\n\t"
        "ds_read_b64_tr_b16 v[108:109], v130 offset:6272\n\t"
        "ds_read_b64_tr_b16 v[110:111], v130 offset:9344\n\t"
        "s_waitcnt lgkmcnt(12)\n\t"
        "v_mfma_f32_32x32x16_bf16 v[64:79], v[140:143], v[112:115], v[64:79]\n\t"
        "ds_read_b64_tr_b16 v[112:113], v129 offset:6400\n\t"
        "ds_read_b64_tr_b16 v[114:115], v129 offset:9472\n\t"
        "s_waitcnt lgkmcnt(10)\n\t"
        "v_mfma_f32_32x32x16_bf16 v[80:95], v[140:143], v[116:119], v[80:95]\n\t"
        "v_cmp_gt_u32 vcc, s47, v126\n\t"
        "ds_read_b64_tr_b16 v[116:117], v130 offset:6400\n\t"
        "ds_read_b64_tr_b16 v[118:119], v130 offset:9472\n\t"
        "s_waitcnt vmcnt(12) lgkmcnt(10)\n\t"
        "v_mfma_f32_32x32x16_bf16 v[0:15], v[96:99], v[144:147], v[0:15]\n\t"
        "s_cbranch_vccnz 14f\n\t"
        "15:\n\t"
        "ds_read_b64_tr_b16 v[96:97], v123 offset:0\n\t"
        "ds_read_b64_tr_b16 v[98:99], v123 offset:3072\n\t"
        "s_waitcnt lgkmcnt(10)\n\t"
        "v_mfma_f32_32x32x16_bf16 v[16:31], v[100:103], v[144:147], v[16:31]\n\t"
        "v_add_u32 v128, 1, v128\n\t"
        "ds_read_b64_tr_b16 v[100:101], v124 offset:0\n\t"
        "ds_read_b64_tr_b16 v[102:103], v124 offset:3072\n\t"
        "s_waitcnt lgkmcnt(10)\n\t"
        "v_mfma_f32_32x32x16_bf16 v[32:47], v[104:107], v[144:147], v[32:47]\n\t"
        "ds_read_b64_tr_b16 v[104:105], v123 offset:128\n\t"
        "ds_read_b64_tr_b16 v[106:107], v123 offset:3200\n\t"
        "s_waitcnt lgkmcnt(10)\n\t"
        "v_mfma_f32_32x32x16_bf16 v[48:63], v[108:111], v[144:147], v[48:63]\n\t"
        "s_add_u32 s46, s46, 12288\n\t"
        "s_add_u32 s47, s47, 1\n\t"
        "ds_read_b64_tr_b16 v[108:109], v124 offset:128\n\t"
        "ds_read_b64_tr_b16 v[110:111], v124 offset:3200\n\t"
        "s_waitcnt lgkmcnt(10)\n\t"
        "v_mfma_f32_32x32x16_bf16 v[64:79], v[144:147], v[112:115], v[64:79]\n\t"
        "s_cmp_eq_u32 s46, 110592\n\t"
        "s_cselect_b32 s46, 0, s46\n\t"
        "ds_read_b64_tr_b16 v[112:113], v123 offset:256\n\t"
        "ds_read_b64_tr_b16 v[114:115], v123 offset:3328\n\t"
        "s_waitcnt lgkmcnt(10)\n\t"
        "v_mfma_f32_32x32x16_bf16 v[80:95], v[144:147], v[116:119], v[80:95]\n\t"
        "ds_write_b32 v127, v128\n\t"
        "ds_read_b64_tr_b16 v[116:117], v124 offset:256\n\t"
        "ds_read_b64_tr_b16 v[118:119], v124 offset:3328\n\t"
        "s_waitcnt lgkmcnt(0)\n\t"
        "s_nop 7\n\t"
        "s_branch 9f\n\t"
        "10:\n\t"
        "s_mov_b32 s48, 0x10000\n\t"
        "8:\n\t"
        "ds_read_b32 v126, v125\n\t"
        "s_sub_u32 s48, s48, 1\n\t"
        "s_waitcnt lgkmcnt(0)\n\t"
        "s_cbranch_scc1 11b\n\t"
        "v_cmp_gt_u32 vcc, s47, v126\n\t"
        "s_cbranch_vccnz 8b\n\t"
        "s_branch 11b\n\t"
        "12:\n\t"
        "s_mov_b32 s48, 0x10000\n\t"
        "8:\n\t"
        "ds_read_b32 v126, v125\n\t"
        "s_sub_u32 s48, s48, 1\n\t"
        "s_waitcnt lgkmcnt(0)\n\t"
        "s_cbranch_scc1 13b\n\t"
        "v_cmp_gt_u32 vcc, s47, v126\n\t"
        "s_cbranch_vccnz 8b\n\t"
        "s_branch 13b\n\t"
        "14:\n\t"
        "s_mov_b32 s48, 0x10000\n\t"
        "8:\n\t"
        "ds_read_b32 v126, v125\n\t"
        "s_sub_u32 s48, s48, 1\n\t"
        "s_waitcnt lgkmcnt(0)\n\t"
        "s_cbranch_scc1 15b\n\t"
        "v_cmp_gt_u32 vcc, s47, v126\n\t"
        "s_cbranch_vccnz 8b\n\t"
        "s_branch 15b\n\t"
        "9:\n\t"
        : "={v[0:15]}"(acc[0]), "={v[16:31]}"(acc[1]), "={v[32:47]}"(acc[2]), "={v[48:63]}"(acc[3]), "={v[64:79]}"(acc[4]), "={v[80:95]}"(acc[5]),
          "={v[96:111]}"(stA), "+{v[112:127]}"(stB), "+{v[128:131]}"(stC), "={s46}"(slot), "+{s47}"(need)
        : "{v[132:147]}"(xr0) : "s48", "vcc", "scc", "memory");
      STAMPV(2 + 16 * L);
      float al = 1.f;
      if (L == 1) {
        PHASE_VARS;
        float S, Q, Dd;
        row_stats_sum(rs, OFF_SX, tile, lane, S, Q, Dd);
        STAMPV(36);
        lds_wait_ge<0>(lbase + L_W + 24, 2);
        soff = ((tn + 1) & 15) * 4096;
        asm volatile(
          "buffer_load_dwordx4 v[148:151], v120, s[40:43], s44 offen offset:0 sc1\n\t"
          "buffer_load_dwordx4 v[152:155], v120, s[40:43], s44 offen offset:1024 sc1\n\t"
          "buffer_load_dwordx4 v[156:159], v120, s[40:43], s44 offen offset:2048 sc1\n\t"
          "buffer_load_dwordx4 v[160:163], v120, s[40:43], s44 offen offset:3072 sc1\n\t"
          : "={v[148:163]}"(xr1), "={v[164:179]}"(xr2), "={v[180:195]}"(xr3), "+{s44}"(soff), "+v"(S), "+v"(Q), "+v"(Dd) : "{v[112:127]}"(stB), "{s[40:43]}"(srd) : "scc", "memory");
        float mean, var, mb1, rd1, mb2, den2;
        row_stats_fin(smem, 1, tile, lane, S, Q, Dd, U2F(words[0]), x1p, mean, var, mb1, rd1);
        ln_scalars(mean + (mean - mb1) * rd1, var * (1.0f + rd1) * (1.0f + rd1), mb2, den2);
        const float rd2 = RCP(den2);
        al = (1.0f + rd1) * rd2;
        const float be = -(mb1 * rd1 + mb2) * RCP(1.0f + rd1);
        reinterpret_cast<float*>(smem + L_AB)[wid * 64 + lane] = hi ? be : al;
        reinterpret_cast<float*>(smem + L_AB)[256 + wid * 64 + lane] = hi ? rd1 : mb1;
        soff = ((tn + 1 + 0) & 15) * 4096;
        asm volatile(
          "s_nop 0\n\t"
          "s_add_u32 s44, s44, 0x1000\n\t"
          "s_and_b32 s44, s44, 0xffff\n\t"
          "buffer_load_dwordx4 v[164:167], v120, s[40:43], s44 offen offset:0 sc1\n\t"
          "buffer_load_dwordx4 v[168:171], v120, s[40:43], s44 offen offset:1024 sc1\n\t"
          "buffer_load_dwordx4 v[172:175], v120, s[40:43], s44 offen offset:2048 sc1\n\t"
          "buffer_load_dwordx4 v[176:179], v120, s[40:43], s44 offen offset:3072 sc1\n\t"
          "s_add_u32 s44, s44, 0x1000\n\t"
          "s_and_b32 s44, s44, 0xffff\n\t"
          "buffer_load_dwordx4 v[180:183], v120, s[40:43], s44 offen offset:0 sc1\n\t"
          "buffer_load_dwordx4 v[184:187], v120, s[40:43], s44 offen offset:1024 sc1\n\t"
          "buffer_load_dwordx4 v[188:191], v120, s[40:43], s44 offen offset:2048 sc1\n\t"
          "buffer_load_dwordx4 v[192:195], v120, s[40:43], s44 offen offset:3072 sc1\n\t"
          : "+{v[148:163]}"(xr1), "+{v[164:179]}"(xr2), "+{v[180:195]}"(xr3), "+{s44}"(soff) : "{v[112:127]}"(stB), "{s[40:43]}"(srd) : "scc", "memory");
#pragma unroll
        for (int jj = 0; jj < 3; ++jj) {
          f32x4 cs[2][4];
          float cv[2];
#pragma unroll
          for (int j = 0; j < 2; ++j) {
            const int cb = jj * 64 + (j ^ hh) * 32;
            if (jj == 2) cv[j] = *reinterpret_cast<const float*>(smem + L_CS + (cb + r32) * 4);
#pragma unroll
            for (int g = 0; g < 4; ++g)
              if (jj < 2 || j == 0) cs[j][g] = *reinterpret_cast<const f32x4*>(smem + (jj < 2 ? L_CS + cb * 4 : L_AB + wid * 256 + 128) + (8 * g + 4 * hi) * 4);
          }
          SBAR();
#pragma unroll
          for (int j = 0; j < 2; ++j)
#pragma unroll
            for (int e = 0; e < 16; ++e) acc[2 * jj + j][e] += jj < 2 ? be * cs[j][e >> 2][e & 3] : cs[0][e >> 2][e & 3] * cv[j];
          SBAR();
        }
      }
      STAMPV(3 + 16 * L);
      soff = ((tn + 1 + 2) & 15) * 4096;
      asm volatile(
        "s_nop 0\n\t"
        "s_add_u32 s44, s44, 0x1000\n\t"
        "s_and_b32 s44, s44, 0xffff\n\t"
        "buffer_load_dwordx4 v[132:135], v120, s[40:43], s44 offen offset:0 sc1\n\t"
        "buffer_load_dwordx4 v[136:139], v120, s[40:43], s44 offen offset:1024 sc1\n\t"
        "buffer_load_dwordx4 v[140:143], v120, s[40:43], s44 offen offset:2048 sc1\n\t"
        "s_mov_b32 s45, 3\n\t"
        "1:\n\t"
        "s_waitcnt vmcnt(14) lgkmcnt(11)\n\t"
        "v_mfma_f32_32x32x16_bf16 v[0:15], v[96:99], v[148:151], v[0:15]\n\t"
        "ds_read_b32 v126, v125\n\t"
        "ds_read_b64_tr_b16 v[96:97], v123 offset:6144\n\t"
        "ds_read_b64_tr_b16 v[98:99], v123 offset:9216\n\t"
        "s_waitcnt lgkmcnt(12)\n\t"
        "v_mfma_f32_32x32x16_bf16 v[16:31], v[100:103], v[148:151], v[16:31]\n\t"
        "v_add_u32 v129, s46, v121\n\t"
        "ds_read_b64_tr_b16 v[100:101], v124 offset:6144\n\t"
        "ds_read_b64_tr_b16 v[102:103], v124 offset:9216\n\t"
        "s_waitcnt lgkmcnt(12)\n\t"
        "v_mfma_f32_32x32x16_bf16 v[32:47], v[104:107], v[148:151], v[32:47]\n\t"
        "ds_read_b64_tr_b16 v[104:105], v123 offset:6272\n\t"
        "ds_read_b64_tr_b16 v[106:107], v123 offset:9344\n\t"
        "buffer_load_dwordx4 v[144:147], v120, s[40:43], s44 offen offset:3072 sc1\n\t"
        "s_waitcnt lgkmcnt(12)\n\t"
        "v_mfma_f32_32x32x16_bf16 v[48:63], v[108:111], v[148:151], v[48:63]\n\t"
        "v_add_u32 v130, s46, v122\n\t"
        "ds_read_b64_tr_b16 v[108:109], v124 offset:6272\n\t"
        "ds_read_b64_tr_b16 v[110:111], v124 offset:9344\n\t"
        "s_waitcnt lgkmcnt(12)\n\t"
        "v_mfma_f32_32x32x16_bf16 v[64:79], v[148:151], v[112:115], v[64:79]\n\t"
        "ds_read_b64_tr_b16 v[112:113], v123 offset:6400\n\t"
        "ds_read_b64_tr_b16 v[114:115], v123 offset:9472\n\t"
        "s_waitcnt lgkmcnt(10)\n\t"
        "v_mfma_f32_32x32x16_bf16 v[80:95], v[148:151], v[116:119], v[80:95]\n\t"
        "v_cmp_gt_u32 vcc, s47, v126\n\t"
        "ds_read_b64_tr_b16 v[116:117], v124 offset:6400\n\t"
        "ds_read_b64_tr_b16 v[118:119], v124 offset:9472\n\t"
        "s_waitcnt vmcnt(14) lgkmcnt(10)\n\t"
        "v_mfma_f32_32x32x16_bf16 v[0:15], v[96:99], v[152:155], v[0:15]\n\t"
        "s_cbranch_vccnz 16f\n\t"
        "17:\n\t"
        "ds_read_b64_tr_b16 v[96:97], v129 offset:0\n\t"
        "ds_read_b64_tr_b16 v[98:99], v129 offset:3072\n\t"
        "s_waitcnt lgkmcnt(10)\n\t"
        "v_mfma_f32_32x32x16_bf16 v[16:31], v[100:103], v[152:155], v[16:31]\n\t"
        "v_add_u32 v128, 1, v128\n\t"
        "ds_read_b64_tr_b16 v[100:101], v130 offset:0\n\t"
        "ds_read_b64_tr_b16 v[102:103], v130 offset:3072\n\t"
        "s_waitcnt lgkmcnt(10)\n\t"
        "v_mfma_f32_32x32x16_bf16 v[32:47], v[104:107], v[152:155], v[32:47]\n\t"
        "ds_read_b64_tr_b16 v[104:105], v129 offset:128\n\t"
        "ds_read_b64_tr_b16 v[106:107], v129 offset:3200\n\t"
        "s_add_u32 s44, s44, 0x1000\n\t"
        "s_and_b32 s44, s44, 0xffff\n\t"
        "buffer_load_dwordx4 v[148:151], v120, s[40:43], s44 offen offset:0 sc1\n\t"
        "s_waitcnt lgkmcnt(10)\n\t"
        "v_mfma_f32_32x32x16_bf16 v[48:63], v[108:111], v[152:155], v[48:63]\n\t"
        "s_add_u32 s46, s46, 12288\n\t"
        "s_add_u32 s47, s47, 1\n\t"
        "ds_read_b64_tr_b16 v[108:109], v130 offset:128\n\t"
        "ds_read_b64_tr_b16 v[110:111], v130 offset:3200\n\t"
        "s_waitcnt lgkmcnt(10)\n\t"
        "v_mfma_f32_32x32x16_bf16 v[64:79], v[152:155], v[112:115], v[64:79]\n\t"
        "s_cmp_eq_u32 s46, 110592\n\t"
        "s_cselect_b32 s46, 0, s46\n\t"
        "ds_read_b64_tr_b16 v[112:113], v129 offset:256\n\t"
        "ds_read_b64_tr_b16 v[114:115], v129 offset:3328\n\t"
        "s_waitcnt lgkmcnt(10)\n\t"
        "v_mfma_f32_32x32x16_bf16 v[80:95], v[152:155], v[116:119], v[80:95]\n\t"
        "ds_write_b32 v127, v128\n\t"
        "ds_read_b64_tr_b16 v[116:117], v130 offset:256\n\t"
        "ds_read_b64_tr_b16 v[118:119], v130 offset:3328\n\t"
        "s_waitcnt vmcnt(14) lgkmcnt(11)\n\t"
        "v_mfma_f32_32x32x16_bf16 v[0:15], v[96:99], v[156:159], v[0:15]\n\t"
        "ds_read_b32 v126, v125\n\t"
        "ds_read_b64_tr_b16 v[96:97], v129 offset:6144\n\t"
        "ds_read_b64_tr_b16 v[98:99], v129 offset:9216\n\t"
        "s_waitcnt lgkmcnt(12)\n\t"
        "v_mfma_f32_32x32x16_bf16 v[16:31], v[100:103], v[156:159], v[16:31]\n\t"
        "v_add_u32 v123, s46, v121\n\t"
        "ds_read_b64_tr_b16 v[100:101], v130 offset:6144\n\t"
        "ds_read_b64_tr_b16 v[102:103], v130 offset:9216\n\t"
        "s_waitcnt lgkmcnt(12)\n\t"
        "v_mfma_f32_32x32x16_bf16 v[32:47], v[104:107], v[156:159], v[32:47]\n\t"
        "ds_read_b64_tr_b16 v[104:105], v129 offset:6272\n\t"
        "ds_read_b64_tr_b16 v[106:107], v129 offset:9344\n\t"
        "buffer_load_dwordx4 v[152:155], v120, s[40:43], s44 offen offset:1024 sc1\n\t"
        "s_waitcnt lgkmcnt(12)\n\t"
        "v_mfma_f32_32x32x16_bf16 v[48:63], v[108:111], v[156:159], v[48:63]\n\t"
        "v_add_u32 v124, s46, v122\n\t"
        "ds_read_b64_tr_b16 v[108:109], v130 offset:6272\n\t"
        "ds_read_b64_tr_b16 v[110:111], v130 offset:9344\n\t"
        "s_waitcnt lgkmcnt(12)\n\t"
        "v_mfma_f32_32x32x16_bf16 v[64:79], v[156:159], v[112:115], v[64:79]\n\t"
        "ds_read_b64_tr_b16 v[112:113], v129 offset:6400\n\t"
        "ds_read_b64_tr_b16 v[114:115], v129 offset:9472\n\t"
        "s_waitcnt lgkmcnt(10)\n\t"
        "v_mfma_f32_32x32x16_bf16 v[80:95], v[156:159], v[116:119], v[80:95]\n\t"
        "v_cmp_gt_u32 vcc, s47, v126\n\t"
        "ds_read_b64_tr_b16 v[116:117], v130 offset:6400\n\t"
        "ds_read_b64_tr_b16 v[118:119], v130 offset:9472\n\t"
        "s_waitcnt vmcnt(14) lgkmcnt(10)\n\t"
        "v_mfma_f32_32x32x16_bf16 v[0:15], v[96:99], v[160:163], v[0:15]\n\t"
        "s_cbranch_vccnz 18f\n\t"
        "19:\n\t"
        "ds_read_b64_tr_b16 v[96:97], v123 offset:0\n\t"
        "ds_read_b64_tr_b16 v[98:99], v123 offset:3072\n\t"
        "s_waitcnt lgkmcnt(10)\n\t"
        "v_mfma_f32_32x32x16_bf16 v[16:31], v[100:103], v[160:163], v[16:31]\n\t"
        "v_add_u32 v128, 1, v128\n\t"
        "ds_read_b64_tr_b16 v[100:101], v124 offset:0\n\t"
        "ds_read_b64_tr_b16 v[102:103], v124 offset:3072\n\t"
        "s_waitcnt lgkmcnt(10)\n\t"
        "v_mfma_f32_32x32x16_bf16 v[32:47], v[104:107], v[160:163], v[32:47]\n\t"
        "ds_read_b64_tr_b16 v[104:105], v123 offset:128\n\t"
        "ds_read_b64_tr_b16 v[106:107], v123 offset:3200\n\t"
        "buffer_load_dwordx4 v[156:159], v120, s[40:43], s44 offen offset:2048 sc1\n\t"
        "s_waitcnt lgkmcnt(10)\n\t"
        "v_mfma_f32_32x32x16_bf16 v[48:63], v[108:111], v[160:163], v[48:63]\n\t"
        "s_add_u32 s46, s46, 12288\n\t"
        "s_add_u32 s47, s47, 1\n\t"
        "ds_read_b64_tr_b16 v[108:109], v124 offset:128\n\t"
        "ds_read_b64_tr_b16 v[110:111], v124 offset:3200\n\t"
        "s_waitcnt lgkmcnt(10)\n\t"
        "v_mfma_f32_32x32x16_bf16 v[64:79], v[160:163], v[112:115], v[64:79]\n\t"
        "s_cmp_eq_u32 s46, 110592\n\t"
        "s_cselect_b32 s46, 0, s46\n\t"
        "ds_read_b64_tr_b16 v[112:113], v123 offset:256\n\t"
        "ds_read_b64_tr_b16 v[114:115], v123 offset:3328\n\t"
        "s_waitcnt lgkmcnt(10)\n\t"
        "v_mfma_f32_32x32x16_bf16 v[80:95], v[160:163], v[116:119], v[80:95]\n\t"
        "ds_write_b32 v127, v128\n\t"
        "ds_read_b64_tr_b16 v[116:117], v124 offset:256\n\t"
        "ds_read_b64_tr_b16 v[118:119], v124 offset:3328\n\t"
        "s_waitcnt vmcnt(14) lgkmcnt(11)\n\t"
        "v_mfma_f32_32x32x16_bf16 v[0:15], v[96:99], v[164:167], v[0:15]\n\t"
        "ds_read_b32 v126, v125\n\t"
        "ds_read_b64_tr_b16 v[96:97], v123 offset:6144\n\t"
        "ds_read_b64_tr_b16 v[98:99], v123 offset:9216\n\t"
        "s_waitcnt lgkmcnt(12)\n\t"
        "v_mfma_f32_32x32x16_bf16 v[16:31], v[100:103], v[164:167], v[16:31]\n\t"
        "v_add_u32 v129, s46, v121\n\t"
        "ds_read_b64_tr_b16 v[100:101], v124 offset:6144\n\t"
        "ds_read_b64_tr_b16 v[102:103], v124 offset:9216\n\t"
        "s_waitcnt lgkmcnt(12)\n\t"
        "v_mfma_f32_32x32x16_bf16 v[32:47], v[104:107], v[164:167], v[32:47]\n\t"
        "ds_read_b64_tr_b16 v[104:105], v123 offset:6272\n\t"
        "ds_read_b64_tr_b16 v[106:107], v123 offset:9344\n\t"
        "buffer_load_dwordx4 v[160:163], v120, s[40:43], s44 offen offset:3072 sc1\n\t"
        "s_waitcnt lgkmcnt(12)\n\t"
        "v_mfma_f32_32x32x16_bf16 v[48:63], v[108:111], v[164:167], v[48:63]\n\t"
        "v_add_u32 v130, s46, v122\n\t"
        "ds_read_b64_tr_b16 v[108:109], v124 offset:6272\n\t"
        "ds_read_b64_tr_b16 v[110:111], v124 offset:9344\n\t"
        "s_waitcnt lgkmcnt(12)\n\t"
        "v_mfma_f32_32x32x16_bf16 v[64:79], v[164:167], v[112:115], v[64:79]\n\t"
        "ds_read_b64_tr_b16 v[112:113], v123 offset:6400\n\t"
        "ds_read_b64_tr_b16 v[114:115], v123 offset:9472\n\t"
        "s_waitcnt lgkmcnt(10)\n\t"
        "v_mfma_f32_32x32x16_bf16 v[80:95], v[164:167], v[116:119], v[80:95]\n\t"
        "v_cmp_gt_u32 vcc, s47, v126\n\t"
        "ds_read_b64_tr_b16 v[116:117], v124 offset:6400\n\t"
        "ds_read_b64_tr_b16 v[118:119], v124 offset:9472\n\t"
        "s_waitcnt vmcnt(14) lgkmcnt(10)\n\t"
        "v_mfma_f32_32x32x16_bf16 v[0:15], v[96:99], v[168:171], v[0:15]\n\t"
        "s_cbranch_vccnz 20f\n\t"
        "21:\n\t"
        "ds_read_b64_tr_b16 v[96:97], v129 offset:0\n\t"
        "ds_read_b64_tr_b16 v[98:99], v129 offset:3072\n\t"
        "s_waitcnt lgkmcnt(10)\n\t"
        "v_mfma_f32_32x32x16_bf16 v[16:31], v[100:103], v[168:171], v[16:31]\n\t"
        "v_add_u32 v128, 1, v128\n\t"
        "ds_read_b64_tr_b16 v[100:101], v130 offset:0\n\t"
        "ds_read_b64_tr_b16 v[102:103], v130 offset:3072\n\t"
        "s_waitcnt lgkmcnt(10)\n\t"
        "v_mfma_f32_32x32x16_bf16 v[32:47], v[104:107], v[168:171], v[32:47]\n\t"
        "ds_read_b64_tr_b16 v[104:105], v129 offset:128\n\t"
        "ds_read_b64_tr_b16 v[106:107], v129 offset:3200\n\t"
        "s_add_u32 s44, s44, 0x1000\n\t"
        "s_and_b32 s44, s44, 0xffff\n\t"
        "buffer_load_dwordx4 v[164:167], v120, s[40:43], s44 offen offset:0 sc1\n\t"
        "s_waitcnt lgkmcnt(10)\n\t"
        "v_mfma_f32_32x32x16_bf16 v[48:63], v[108:111], v[168:171], v[48:63]\n\t"
        "s_add_u32 s46, s46, 12288\n\t"
        "s_add_u32 s47, s47, 1\n\t"
        "ds_read_b64_tr_b16 v[108:109], v130 offset:128\n\t"
        "ds_read_b64_tr_b16 v[110:111], v130 offset:3200\n\t"
        "s_waitcnt lgkmcnt(10)\n\t"
        "v_mfma_f32_32x32x16_bf16 v[64:79], v[168:171], v[112:115], v[64:79]\n\t"
        "s_cmp_eq_u32 s46, 110592\n\t"
        "s_cselect_b32 s46, 0, s46\n\t"
        "ds_read_b64_tr_b16 v[112:113], v129 offset:256\n\t"
        "ds_read_b64_tr_b16 v[114:115], v129 offset:3328\n\t"
        "s_waitcnt lgkmcnt(10)\n\t"
        "v_mfma_f32_32x32x16_bf16 v[80:95], v[168:171], v[116:119], v[80:95]\n\t"
        "ds_write_b32 v127, v128\n\t"
        "ds_read_b64_tr_b16 v[116:117], v130 offset:256\n\t"
        "ds_read_b64_tr_b16 v[118:119], v130 offset:3328\n\t"
        "s_waitcnt vmcnt(14) lgkmcnt(11)\n\t"
        "v_mfma_f32_32x32x16_bf16 v[0:15], v[96:99], v[172:175], v[0:15]\n\t"
        "ds_read_b32 v126, v125\n\t"
        "ds_read_b64_tr_b16 v[96:97], v129 offset:6144\n\t"
        "ds_read_b64_tr_b16 v[98:99], v129 offset:9216\n\t"
        "s_waitcnt lgkmcnt(12)\n\t"
        "v_mfma_f32_32x32x16_bf16 v[16:31], v[100:103], v[172:175], v[16:31]\n\t"
        "v_add_u32 v123, s46, v121\n\t"
        "ds_read_b64_tr_b16 v[100:101], v130 offset:6144\n\t"
        "ds_read_b64_tr_b16 v[102:103], v130 offset:9216\n\t"
        "s_waitcnt lgkmcnt(12)\n\t"
        "v_mfma_f32_32x32x16_bf16 v[32:47], v[104:107], v[172:175], v[32:47]\n\t"
        "ds_read_b64_tr_b16 v[104:105], v129 offset:6272\n\t"
        "ds_read_b64_tr_b16 v[106:107], v129 offset:9344\n\t"
        "buffer_load_dwordx4 v[168:171], v120, s[40:43], s44 offen offset:1024 sc1\n\t"
        "s_waitcnt lgkmcnt(12)\n\t"
        "v_mfma_f32_32x32x16_bf16 v[48:63], v[108:111], v[172:175], v[48:63]\n\t"
        "v_add_u32 v124, s46, v122\n\t"
        "ds_read_b64_tr_b16 v[108:109], v130 offset:6272\n\t"
        "ds_read_b64_tr_b16 v[110:111], v130 offset:9344\n\t"
        "s_waitcnt lgkmcnt(12)\n\t"
        "v_mfma_f32_32x32x16_bf16 v[64:79], v[172:175], v[112:115], v[64:79]\n\t"
        "ds_read_b64_tr_b16 v[112:113], v129 offset:6400\n\t"
        "ds_read_b64_tr_b16 v[114:115], v129 offset:9472\n\t"
        "s_waitcnt lgkmcnt(10)\n\t"
        "v_mfma_f32_32x32x16_bf16 v[80:95], v[172:175], v[116:119], v[80:95]\n\t"
        "v_cmp_gt_u32 vcc, s47, v126\n\t"
        "ds_read_b64_tr_b16 v[116:117], v130 offset:6400\n\t"
        "ds_read_b64_tr_b16 v[118:119], v130 offset:9472\n\t"
        "s_waitcnt vmcnt(14) lgkmcnt(10)\n\t"
        "v_mfma_f32_32x32x16_bf16 v[0:15], v[96:99], v[176:179], v[0:15]\n\t"
        "s_cbranch_vccnz 22f\n\t"
        "23:\n\t"
        "ds_read_b64_tr_b16 v[96:97], v123 offset:0\n\t"
        "ds_read_b64_tr_b16 v[98:99], v123 offset:3072\n\t"
        "s_waitcnt lgkmcnt(10)\n\t"
        "v_mfma_f32_32x32x16_bf16 v[16:31], v[100:103], v[176:179], v[16:31]\n\t"
        "v_add_u32 v128, 1, v128\n\t"
        "ds_read_b64_tr_b16 v[100:101], v124 offset:0\n\t"
        "ds_read_b64_tr_b16 v[102:103], v124 offset:3072\n\t"
        "s_waitcnt lgkmcnt(10)\n\t"
        "v_mfma_f32_32x32x16_bf16 v[32:47], v[104:107], v[176:179], v[32:47]\n\t"
        "ds_read_b64_tr_b16 v[104:105], v123 offset:128\n\t"
        "ds_read_b64_tr_b16 v[106:107], v123 offset:3200\n\t"
        "buffer_load_dwordx4 v[172:175], v120, s[40:43], s44 offen offset:2048 sc1\n\t"
        "s_waitcnt lgkmcnt(10)\n\t"
        "v_mfma_f32_32x32x16_bf16 v[48:63], v[108:111], v[176:179], v[48:63]\n\t"
        "s_add_u32 s46, s46, 12288\n\t"
        "s_add_u32 s47, s47, 1\n\t"
        "ds_read_b64_tr_b16 v[108:109], v124 offset:128\n\t"
        "ds_read_b64_tr_b16 v[110:111], v124 offset:3200\n\t"
        "s_waitcnt lgkmcnt(10)\n\t"
        "v_mfma_f32_32x32x16_bf16 v[64:79], v[176:179], v[112:115], v[64:79]\n\t"
        "s_cmp_eq_u32 s46, 110592\n\t"
        "s_cselect_b32 s46, 0, s46\n\t"
        "ds_read_b64_tr_b16 v[112:113], v123 offset:256\n\t"
        "ds_read_b64_tr_b16 v[114:115], v123 offset:3328\n\t"
        "s_waitcnt lgkmcnt(10)\n\t"
        "v_mfma_f32_32x32x16_bf16 v[80:95], v[176:179], v[116:119], v[80:95]\n\t"
        "ds_write_b32 v127, v128\n\t"
        "ds_read_b64_tr_b16 v[116:117], v124 offset:256\n\t"
        "ds_read_b64_tr_b16 v[118:119], v124 offset:3328\n\t"
        "s_waitcnt vmcnt(14) lgkmcnt(11)\n\t"
        "v_mfma_f32_32x32x16_bf16 v[0:15], v[96:99], v[180:183], v[0:15]\n\t"
        "ds_read_b32 v126, v125\n\t"
        "ds_read_b64_tr_b16 v[96:97], v123 offset:6144\n\t"
        "ds_read_b64_tr_b16 v[98:99], v123 offset:9216\n\t"
        "s_waitcnt lgkmcnt(12)\n\t"
        "v_mfma_f32_32x32x16_bf16 v[16:31], v[100:103], v[180:183], v[16:31]\n\t"
        "v_add_u32 v129, s46, v121\n\t"
        "ds_read_b64_tr_b16 v[100:101], v124 offset:6144\n\t"
        "ds_read_b64_tr_b16 v[102:103], v124 offset:9216\n\t"
        "s_waitcnt lgkmcnt(12)\n\t"
        "v_mfma_f32_32x32x16_bf16 v[32:47], v[104:107], v[180:183], v[32:47]\n\t"
        "ds_read_b64_tr_b16 v[104:105], v123 offset:6272\n\t"
        "ds_read_b64_tr_b16 v[106:107], v123 offset:9344\n\t"
        "buffer_load_dwordx4 v[176:179], v120, s[40:43], s44 offen offset:3072 sc1\n\t"
        "s_waitcnt lgkmcnt(12)\n\t"
        "v_mfma_f32_32x32x16_bf16 v[48:63], v[108:111], v[180:183], v[48:63]\n\t"
        "v_add_u32 v130, s46, v122\n\t"
        "ds_read_b64_tr_b16 v[108:109], v124 offset:6272\n\t"
        "ds_read_b64_tr_b16 v[110:111], v124 offset:9344\n\t"
        "s_waitcnt lgkmcnt(12)\n\t"
        "v_mfma_f32_32x32x16_bf16 v[64:79], v[180:183], v[112:115], v[64:79]\n\t"
        "ds_read_b64_tr_b16 v[112:113], v123 offset:6400\n\t"
        "ds_read_b64_tr_b16 v[114:115], v123 offset:9472\n\t"
        "s_waitcnt lgkmcnt(10)\n\t"
        "v_mfma_f32_32x32x16_bf16 v[80:95], v[180:183], v[116:119], v[80:95]\n\t"
        "v_cmp_gt_u32 vcc, s47, v126\n\t"
        "ds_read_b64_tr_b16 v[116:117], v124 offset:6400\n\t"
        "ds_read_b64_tr_b16 v[118:119], v124 offset:9472\n\t"
        "s_waitcnt vmcnt(14) lgkmcnt(10)\n\t"
        "v_mfma_f32_32x32x16_bf16 v[0:15], v[96:99], v[184:187], v[0:15]\n\t"
        "s_cbranch_vccnz 24f\n\t"
        "25:\n\t"
        "ds_read_b64_tr_b16 v[96:97], v129 offset:0\n\t"
        "ds_read_b64_tr_b16 v[98:99], v129 offset:3072\n\t"
        "s_waitcnt lgkmcnt(10)\n\t"
        "v_mfma_f32_32x32x16_bf16 v[16:31], v[100:103], v[184:187], v[16:31]\n\t"
        "v_add_u32 v128, 1, v128\n\t"
        "ds_read_b64_tr_b16 v[100:101], v130 offset:0\n\t"
        "ds_read_b64_tr_b16 v[102:103], v130 offset:3072\n\t"
        "s_waitcnt lgkmcnt(10)\n\t"
        "v_mfma_f32_32x32x16_bf16 v[32:47], v[104:107], v[184:187], v[32:47]\n\t"
        "ds_read_b64_tr_b16 v[104:105], v129 offset:128\n\t"
        "ds_read_b64_tr_b16 v[106:107], v129 offset:3200\n\t"
        "s_add_u32 s44, s44, 0x1000\n\t"
        "s_and_b32 s44, s44, 0xffff\n\t"
        "buffer_load_dwordx4 v[180:183], v120, s[40:43], s44 offen offset:0 sc1\n\t"
        "s_waitcnt lgkmcnt(10)\n\t"
        "v_mfma_f32_32x32x16_bf16 v[48:63], v[108:111], v[184:187], v[48:63]\n\t"
        "s_add_u32 s46, s46, 12288\n\t"
        "s_add_u32 s47, s47, 1\n\t"
        "ds_read_b64_tr_b16 v[108:109], v130 offset:128\n\t"
        "ds_read_b64_tr_b16 v[110:111], v130 offset:3200\n\t"
        "s_waitcnt lgkmcnt(10)\n\t"
        "v_mfma_f32_32x32x16_bf16 v[64:79], v[184:187], v[112:115], v[64:79]\n\t"
        "s_cmp_eq_u32 s46, 110592\n\t"
        "s_cselect_b32 s46, 0, s46\n\t"
        "ds_read_b64_tr_b16 v[112:113], v129 offset:256\n\t"
        "ds_read_b64_tr_b16 v[114:115], v129 offset:3328\n\t"
        "s_waitcnt lgkmcnt(10)\n\t"
        "v_mfma_f32_32x32x16_bf16 v[80:95], v[184:187], v[116:119], v[80:95]\n\t"
        "ds_write_b32 v127, v128\n\t"
        "ds_read_b64_tr_b16 v[116:117], v130 offset:256\n\t"
        "ds_read_b64_tr_b16 v[118:119], v130 offset:3328\n\t"
        "s_waitcnt vmcnt(14) lgkmcnt(11)\n\t"
        "v_mfma_f32_32x32x16_bf16 v[0:15], v[96:99], v[188:191], v[0:15]\n\t"
        "ds_read_b32 v126, v125\n\t"
        "ds_read_b64_tr_b16 v[96:97], v129 offset:6144\n\t"
        "ds_read_b64_tr_b16 v[98:99], v129 offset:9216\n\t"
        "s_waitcnt lgkmcnt(12)\n\t"
        "v_mfma_f32_32x32x16_bf16 v[16:31], v[100:103], v[188:191], v[16:31]\n\t"
        "v_add_u32 v123, s46, v121\n\t"
        "ds_read_b64_tr_b16 v[100:101], v130 offset:6144\n\t"
        "ds_read_b64_tr_b16 v[102:103], v130 offset:9216\n\t"
        "s_waitcnt lgkmcnt(12)\n\t"
        "v_mfma_f32_32x32x16_bf16 v[32:47], v[104:107], v[188:191], v[32:47]\n\t"
        "ds_read_b64_tr_b16 v[104:105], v129 offset:6272\n\t"
        "ds_read_b64_tr_b16 v[106:107], v129 offset:9344\n\t"
        "buffer_load_dwordx4 v[184:187], v120, s[40:43], s44 offen offset:1024 sc1\n\t"
        "s_waitcnt lgkmcnt(12)\n\t"
        "v_mfma_f32_32x32x16_bf16 v[48:63], v[108:111], v[188:191], v[48:63]\n\t"
        "v_add_u32 v124, s46, v122\n\t"
        "ds_read_b64_tr_b16 v[108:109], v130 offset:6272\n\t"
        "ds_read_b64_tr_b16 v[110:111], v130 offset:9344\n\t"
        "s_waitcnt lgkmcnt(12)\n\t"
        "v_mfma_f32_32x32x16_bf16 v[64:79], v[188:191], v[112:115], v[64:79]\n\t"
        "ds_read_b64_tr_b16 v[112:113], v129 offset:6400\n\t"
        "ds_read_b64_tr_b16 v[114:115], v129 offset:9472\n\t"
        "s_waitcnt lgkmcnt(10)\n\t"
        "v_mfma_f32_32x32x16_bf16 v[80:95], v[188:191], v[116:119], v[80:95]\n\t"
        "v_cmp_gt_u32 vcc, s47, v126\n\t"
        "ds_read_b64_tr_b16 v[116:117], v130 offset:6400\n\t"
        "ds_read_b64_tr_b16 v[118:119], v130 offset:9472\n\t"
        "s_waitcnt vmcnt(14) lgkmcnt(10)\n\t"
        "v_mfma_f32_32x32x16_bf16 v[0:15], v[96:99], v[192:195], v[0:15]\n\t"
        "s_cbranch_vccnz 26f\n\t"
        "27:\n\t"
        "ds_read_b64_tr_b16 v[96:97], v123 offset:0\n\t"
        "ds_read_b64_tr_b16 v[98:99], v123 offset:3072\n\t"
        "s_waitcnt lgkmcnt(10)\n\t"
        "v_mfma_f32_32x32x16_bf16 v[16:31], v[100:103], v[192:195], v[16:31]\n\t"
        "v_add_u32 v128, 1, v128\n\t"
        "ds_read_b64_tr_b16 v[100:101], v124 offset:0\n\t"
        "ds_read_b64_tr_b16 v[102:103], v124 offset:3072\n\t"
        "s_waitcnt lgkmcnt(10)\n\t"
        "v_mfma_f32_32x32x16_bf16 v[32:47], v[104:107], v[192:195], v[32:47]\n\t"
        "ds_read_b64_tr_b16 v[104:105], v123 offset:128\n\t"
        "ds_read_b64_tr_b16 v[106:107], v123 offset:3200\n\t"
        "buffer_load_dwordx4 v[188:191], v120, s[40:43], s44 offen offset:2048 sc1\n\t"
        "s_waitcnt lgkmcnt(10)\n\t"
        "v_mfma_f32_32x32x16_bf16 v[48:63], v[108:111], v[192:195], v[48:63]\n\t"
        "s_add_u32 s46, s46, 12288\n\t"
        "s_add_u32 s47, s47, 1\n\t"
        "ds_read_b64_tr_b16 v[108:109], v124 offset:128\n\t"
        "ds_read_b64_tr_b16 v[110:111], v124 offset:3200\n\t"
        "s_waitcnt lgkmcnt(10)\n\t"
        "v_mfma_f32_32x32x16_bf16 v[64:79], v[192:195], v[112:115], v[64:79]\n\t"
        "s_cmp_eq_u32 s46, 110592\n\t"
        "s_cselect_b32 s46, 0, s46\n\t"
        "ds_read_b64_tr_b16 v[112:113], v123 offset:256\n\t"
        "ds_read_b64_tr_b16 v[114:115], v123 offset:3328\n\t"
        "s_waitcnt lgkmcnt(10)\n\t"
        "v_mfma_f32_32x32x16_bf16 v[80:95], v[192:195], v[116:119], v[80:95]\n\t"
        "ds_write_b32 v127, v128\n\t"
        "ds_read_b64_tr_b16 v[116:117], v124 offset:256\n\t"
        "ds_read_b64_tr_b16 v[118:119], v124 offset:3328\n\t"
        "s_waitcnt vmcnt(14) lgkmcnt(11)\n\t"
        "v_mfma_f32_32x32x16_bf16 v[0:15], v[96:99], v[132:135], v[0:15]\n\t"
        "ds_read_b32 v126, v125\n\t"
        "ds_read_b64_tr_b16 v[96:97], v123 offset:6144\n\t"
        "ds_read_b64_tr_b16 v[98:99], v123 offset:9216\n\t"
        "s_waitcnt lgkmcnt(12)\n\t"
        "v_mfma_f32_32x32x16_bf16 v[16:31], v[100:103], v[132:135], v[16:31]\n\t"
        "v_add_u32 v129, s46, v121\n\t"
        "ds_read_b64_tr_b16 v[100:101], v124 offset:6144\n\t"
        "ds_read_b64_tr_b16 v[102:103], v124 offset:9216\n\t"
        "s_waitcnt lgkmcnt(12)\n\t"
        "v_mfma_f32_32x32x16_bf16 v[32:47], v[104:107], v[132:135], v[32:47]\n\t"
        "ds_read_b64_tr_b16 v[104:105], v123 offset:6272\n\t"
        "ds_read_b64_tr_b16 v[106:107], v123 offset:9344\n\t"
        "buffer_load_dwordx4 v[192:195], v120, s[40:43], s44 offen offset:3072 sc1\n\t"
        "s_waitcnt lgkmcnt(12)\n\t"
        "v_mfma_f32_32x32x16_bf16 v[48:63], v[108:111], v[132:135], v[48:63]\n\t"
        "v_add_u32 v130, s46, v122\n\t"
        "ds_read_b64_tr_b16 v[108:109], v124 offset:6272\n\t"
        "ds_read_b64_tr_b16 v[110:111], v124 offset:9344\n\t"
        "s_waitcnt lgkmcnt(12)\n\t"
        "v_mfma_f32_32x32x16_bf16 v[64:79], v[132:135], v[112:115], v[64:79]\n\t"
        "ds_read_b64_tr_b16 v[112:113], v123 offset:6400\n\t"
        "ds_read_b64_tr_b16 v[114:115], v123 offset:9472\n\t"
        "s_waitcnt lgkmcnt(10)\n\t"
        "v_mfma_f32_32x32x16_bf16 v[80:95], v[132:135], v[116:119], v[80:95]\n\t"
        "v_cmp_gt_u32 vcc, s47, v126\n\t"
        "ds_read_b64_tr_b16 v[116:117], v124 offset:6400\n\t"
        "ds_read_b64_tr_b16 v[118:119], v124 offset:9472\n\t"
        "s_waitcnt vmcnt(14) lgkmcnt(10)\n\t"
        "v_mfma_f32_32x32x16_bf16 v[0:15], v[96:99], v[136:139], v[0:15]\n\t"
        "s_cbranch_vccnz 28f\n\t"
        "29:\n\t"
        "ds_read_b64_tr_b16 v[96:97], v129 offset:0\n\t"
        "ds_read_b64_tr_b16 v[98:99], v129 offset:3072\n\t"
        "s_waitcnt lgkmcnt(10)\n\t"
        "v_mfma_f32_32x32x16_bf16 v[16:31], v[100:103], v[136:139], v[16:31]\n\t"
        "v_add_u32 v128, 1, v128\n\t"
        "ds_read_b64_tr_b16 v[100:101], v130 offset:0\n\t"
        "ds_read_b64_tr_b16 v[102:103], v130 offset:3072\n\t"
        "s_waitcnt lgkmcnt(10)\n\t"
        "v_mfma_f32_32x32x16_bf16 v[32:47], v[104:107], v[136:139], v[32:47]\n\t"
        "ds_read_b64_tr_b16 v[104:105], v129 offset:128\n\t"
        "ds_read_b64_tr_b16 v[106:107], v129 offset:3200\n\t"
        "s_add_u32 s44, s44, 0x1000\n\t"
        "s_and_b32 s44, s44, 0xffff\n\t"
        "buffer_load_dwordx4 v[132:135], v120, s[40:43], s44 offen offset:0 sc1\n\t"
        "s_waitcnt lgkmcnt(10)\n\t"
        "v_mfma_f32_32x32x16_bf16 v[48:63], v[108:111], v[136:139], v[48:63]\n\t"
        "s_add_u32 s46, s46, 12288\n\t"
        "s_add_u32 s47, s47, 1\n\t"
        "ds_read_b64_tr_b16 v[108:109], v130 offset:128\n\t"
        "ds_read_b64_tr_b16 v[110:111], v130 offset:3200\n\t"
        "s_waitcnt lgkmcnt(10)\n\t"
        "v_mfma_f32_32x32x16_bf16 v[64:79], v[136:139], v[112:115], v[64:79]\n\t"
        "s_cmp_eq_u32 s46, 110592\n\t"
        "s_cselect_b32 s46, 0, s46\n\t"
        "ds_read_b64_tr_b16 v[112:113], v129 offset:256\n\t"
        "ds_read_b64_tr_b16 v[114:115], v129 offset:3328\n\t"
        "s_waitcnt lgkmcnt(10)\n\t"
        "v_mfma_f32_32x32x16_bf16 v[80:95], v[136:139], v[116:119], v[80:95]\n\t"
        "ds_write_b32 v127, v128\n\t"
        "ds_read_b64_tr_b16 v[116:117], v130 offset:256\n\t"
        "ds_read_b64_tr_b16 v[118:119], v130 offset:3328\n\t"
        "s_waitcnt vmcnt(14) lgkmcnt(11)\n\t"
        "v_mfma_f32_32x32x16_bf16 v[0:15], v[96:99], v[140:143], v[0:15]\n\t"
        "ds_read_b32 v126, v125\n\t"
        "ds_read_b64_tr_b16 v[96:97], v129 offset:6144\n\t"
        "ds_read_b64_tr_b16 v[98:99], v129 offset:9216\n\t"
        "s_waitcnt lgkmcnt(12)\n\t"
        "v_mfma_f32_32x32x16_bf16 v[16:31], v[100:103], v[140:143], v[16:31]\n\t"
        "v_add_u32 v123, s46, v121\n\t"
        "ds_read_b64_tr_b16 v[100:101], v130 offset:6144\n\t"
        "ds_read_b64_tr_b16 v[102:103], v130 offset:9216\n\t"
        "s_waitcnt lgkmcnt(12)\n\t"
        "v_mfma_f32_32x32x16_bf16 v[32:47], v[104:107], v[140:143], v[32:47]\n\t"
        "ds_read_b64_tr_b16 v[104:105], v129 offset:6272\n\t"
        "ds_read_b64_tr_b16 v[106:107], v129 offset:9344\n\t"
        "buffer_load_dwordx4 v[136:139], v120, s[40:43], s44 offen offset:1024 sc1\n\t"
        "s_waitcnt lgkmcnt(12)\n\t"
        "v_mfma_f32_32x32x16_bf16 v[48:63], v[108:111], v[140:143], v[48:63]\n\t"
        "v_add_u32 v124, s46, v122\n\t"
        "ds_read_b64_tr_b16 v[108:109], v130 offset:6272\n\t"
        "ds_read_b64_tr_b16 v[110:111], v130 offset:9344\n\t"
        "s_waitcnt lgkmcnt(12)\n\t"
        "v_mfma_f32_32x32x16_bf16 v[64:79], v[140:143], v[112:115], v[64:79]\n\t"
        "ds_read_b64_tr_b16 v[112:113], v129 offset:6400\n\t"
        "ds_read_b64_tr_b16 v[114:115], v129 offset:9472\n\t"
        "s_waitcnt lgkmcnt(10)\n\t"
        "v_mfma_f32_32x32x16_bf16 v[80:95], v[140:143], v[116:119], v[80:95]\n\t"
        "v_cmp_gt_u32 vcc, s47, v126\n\t"
        "ds_read_b64_tr_b16 v[116:117], v130 offset:6400\n\t"
        "ds_read_b64_tr_b16 v[118:119], v130 offset:9472\n\t"
        "s_waitcnt vmcnt(14) lgkmcnt(10)\n\t"
        "v_mfma_f32_32x32x16_bf16 v[0:15], v[96:99], v[144:147], v[0:15]\n\t"
        "s_cbranch_vccnz 30f\n\t"
        "31:\n\t"
        "ds_read_b64_tr_b16 v[96:97], v123 offset:0\n\t"
        "ds_read_b64_tr_b16 v[98:99], v123 offset:3072\n\t"
        "s_waitcnt lgkmcnt(10)\n\t"
        "v_mfma_f32_32x32x16_bf16 v[16:31], v[100:103], v[144:147], v[16:31]\n\t"
        "v_add_u32 v128, 1, v128\n\t"
        "ds_read_b64_tr_b16 v[100:101], v124 offset:0\n\t"
        "ds_read_b64_tr_b16 v[102:103], v124 offset:3072\n\t"
        "s_waitcnt lgkmcnt(10)\n\t"
        "v_mfma_f32_32x32x16_bf16 v[32:47], v[104:107], v[144:147], v[32:47]\n\t"
        "ds_read_b64_tr_b16 v[104:105], v123 offset:128\n\t"
        "ds_read_b64_tr_b16 v[106:107], v123 offset:3200\n\t"
        "buffer_load_dwordx4 v[140:143], v120, s[40:43], s44 offen offset:2048 sc1\n\t"
        "s_waitcnt lgkmcnt(10)\n\t"
        "v_mfma_f32_32x32x16_bf16 v[48:63], v[108:111], v[144:147], v[48:63]\n\t"
        "s_add_u32 s46, s46, 12288\n\t"
        "s_add_u32 s47, s47, 1\n\t"
        "ds_read_b64_tr_b16 v[108:109], v124 offset:128\n\t"
        "ds_read_b64_tr_b16 v[110:111], v124 offset:3200\n\t"
        "s_waitcnt lgkmcnt(10)\n\t"
        "v_mfma_f32_32x32x16_bf16 v[64:79], v[144:147], v[112:115], v[64:79]\n\t"
        "s_cmp_eq_u32 s46, 110592\n\t"
        "s_cselect_b32 s46, 0, s46\n\t"
        "ds_read_b64_tr_b16 v[112:113], v123 offset:256\n\t"
        "ds_read_b64_tr_b16 v[114:115], v123 offset:3328\n\t"
        "s_waitcnt lgkmcnt(10)\n\t"
        "v_mfma_f32_32x32x16_bf16 v[80:95], v[144:147], v[116:119], v[80:95]\n\t"
        "ds_write_b32 v127, v128\n\t"
        "ds_read_b64_tr_b16 v[116:117], v124 offset:256\n\t"
        "ds_read_b64_tr_b16 v[118:119], v124 offset:3328\n\t"
        "s_sub_u32 s45, s45, 1\n\t"
        "s_cmp_lg_u32 s45, 0\n\t"
        "s_cbranch_scc1 1b\n\t"
        "s_waitcnt vmcnt(14) lgkmcnt(11)\n\t"
        "v_mfma_f32_32x32x16_bf16 v[0:15], v[96:99], v[148:151], v[0:15]\n\t"
        "ds_read_b32 v126, v125\n\t"
        "ds_read_b64_tr_b16 v[96:97], v123 offset:6144\n\t"
        "ds_read_b64_tr_b16 v[98:99], v123 offset:9216\n\t"
        "s_waitcnt lgkmcnt(12)\n\t"
        "v_mfma_f32_32x32x16_bf16 v[16:31], v[100:103], v[148:151], v[16:31]\n\t"
        "v_add_u32 v129, s46, v121\n\t"
        "ds_read_b64_tr_b16 v[100:101], v124 offset:6144\n\t"
        "ds_read_b64_tr_b16 v[102:103], v124 offset:9216\n\t"
        "s_waitcnt lgkmcnt(12)\n\t"
        "v_mfma_f32_32x32x16_bf16 v[32:47], v[104:107], v[148:151], v[32:47]\n\t"
        "ds_read_b64_tr_b16 v[104:105], v123 offset:6272\n\t"
        "ds_read_b64_tr_b16 v[106:107], v123 offset:9344\n\t"
        "s_waitcnt lgkmcnt(12)\n\t"
        "v_mfma_f32_32x32x16_bf16 v[48:63], v[108:111], v[148:151], v[48:63]\n\t"
        "v_add_u32 v130, s46, v122\n\t"
        "ds_read_b64_tr_b16 v[108:109], v124 offset:6272\n\t"
        "ds_read_b64_tr_b16 v[110:111], v124 offset:9344\n\t"
        "s_waitcnt lgkmcnt(12)\n\t"
        "v_mfma_f32_32x32x16_bf16 v[64:79], v[148:151], v[112:115], v[64:79]\n\t"
        "ds_read_b64_tr_b16 v[112:113], v123 offset:6400\n\t"
        "ds_read_b64_tr_b16 v[114:115], v123 offset:9472\n\t"
        "s_waitcnt lgkmcnt(10)\n\t"
        "v_mfma_f32_32x32x16_bf16 v[80:95], v[148:151], v[116:119], v[80:95]\n\t"
        "v_cmp_gt_u32 vcc, s47, v126\n\t"
        "ds_read_b64_tr_b16 v[116:117], v124 offset:6400\n\t"
        "ds_read_b64_tr_b16 v[118:119], v124 offset:9472\n\t"
        "s_waitcnt vmcnt(13) lgkmcnt(10)\n\t"
        "v_mfma_f32_32x32x16_bf16 v[0:15], v[96:99], v[152:155], v[0:15]\n\t"
        "s_cbranch_vccnz 64f\n\t"
        "65:\n\t"
        "ds_read_b64_tr_b16 v[96:97], v129 offset:0\n\t"
        "ds_read_b64_tr_b16 v[98:99], v129 offset:3072\n\t"
        "s_waitcnt lgkmcnt(10)\n\t"
        "v_mfma_f32_32x32x16_bf16 v[16:31], v[100:103], v[152:155], v[16:31]\n\t"
        "v_add_u32 v128, 1, v128\n\t"
        "ds_read_b64_tr_b16 v[100:101], v130 offset:0\n\t"
        "ds_read_b64_tr_b16 v[102:103], v130 offset:3072\n\t"
        "s_waitcnt lgkmcnt(10)\n\t"
        "v_mfma_f32_32x32x16_bf16 v[32:47], v[104:107], v[152:155], v[32:47]\n\t"
        "ds_read_b64_tr_b16 v[104:105], v129 offset:128\n\t"
        "ds_read_b64_tr_b16 v[106:107], v129 offset:3200\n\t"
        "s_waitcnt lgkmcnt(10)\n\t"
        "v_mfma_f32_32x32x16_bf16 v[48:63], v[108:111], v[152:155], v[48:63]\n\t"
        "s_add_u32 s46, s46, 12288\n\t"
        "s_add_u32 s47, s47, 1\n\t"
        "ds_read_b64_tr_b16 v[108:109], v130 offset:128\n\t"
        "ds_read_b64_tr_b16 v[110:111], v130 offset:3200\n\t"
        "s_waitcnt lgkmcnt(10)\n\t"
        "v_mfma_f32_32x32x16_bf16 v[64:79], v[152:155], v[112:115], v[64:79]\n\t"
        "s_cmp_eq_u32 s46, 110592\n\t"
        "s_cselect_b32 s46, 0, s46\n\t"
        "ds_read_b64_tr_b16 v[112:113], v129 offset:256\n\t"
        "ds_read_b64_tr_b16 v[114:115], v129 offset:3328\n\t"
        "s_waitcnt lgkmcnt(10)\n\t"
        "v_mfma_f32_32x32x16_bf16 v[80:95], v[152:155], v[116:119], v[80:95]\n\t"
        "ds_write_b32 v127, v128\n\t"
        "ds_read_b64_tr_b16 v[116:117], v130 offset:256\n\t"
        "ds_read_b64_tr_b16 v[118:119], v130 offset:3328\n\t"
        "s_waitcnt vmcnt(12) lgkmcnt(11)\n\t"
        "v_mfma_f32_32x32x16_bf16 v[0:15], v[96:99], v[156:159], v[0:15]\n\t"
        "ds_read_b32 v126, v125\n\t"
        "ds_read_b64_tr_b16 v[96:97], v129 offset:6144\n\t"
        "ds_read_b64_tr_b16 v[98:99], v129 offset:9216\n\t"
        "s_waitcnt lgkmcnt(12)\n\t"
        "v_mfma_f32_32x32x16_bf16 v[16:31], v[100:103], v[156:159], v[16:31]\n\t"
        "v_add_u32 v123, s46, v121\n\t"
        "ds_read_b64_tr_b16 v[100:101], v130 offset:6144\n\t"
        "ds_read_b64_tr_b16 v[102:103], v130 offset:9216\n\t"
        "s_waitcnt lgkmcnt(12)\n\t"
        "v_mfma_f32_32x32x16_bf16 v[32:47], v[104:107], v[156:159], v[32:47]\n\t"
        "ds_read_b64_tr_b16 v[104:105], v129 offset:6272\n\t"
        "ds_read_b64_tr_b16 v[106:107], v129 offset:9344\n\t"
        "s_waitcnt lgkmcnt(12)\n\t"
        "v_mfma_f32_32x32x16_bf16 v[48:63], v[108:111], v[156:159], v[48:63]\n\t"
        "v_add_u32 v124, s46, v122\n\t"
        "ds_read_b64_tr_b16 v[108:109], v130 offset:6272\n\t"
        "ds_read_b64_tr_b16 v[110:111], v130 offset:9344\n\t"
        "s_waitcnt lgkmcnt(12)\n\t"
        "v_mfma_f32_32x32x16_bf16 v[64:79], v[156:159], v[112:115], v[64:79]\n\t"
        "ds_read_b64_tr_b16 v[112:113], v129 offset:6400\n\t"
        "ds_read_b64_tr_b16 v[114:115], v129 offset:9472\n\t"
        "s_waitcnt lgkmcnt(10)\n\t"
        "v_mfma_f32_32x32x16_bf16 v[80:95], v[156:159], v[116:119], v[80:95]\n\t"
        "v_cmp_gt_u32 vcc, s47, v126\n\t"
        "ds_read_b64_tr_b16 v[116:117], v130 offset:6400\n\t"
        "ds_read_b64_tr_b16 v[118:119], v130 offset:9472\n\t"
        "s_waitcnt vmcnt(11) lgkmcnt(10)\n\t"
        "v_mfma_f32_32x32x16_bf16 v[0:15], v[96:99], v[160:163], v[0:15]\n\t"
        "s_cbranch_vccnz 66f\n\t"
        "67:\n\t"
        "ds_read_b64_tr_b16 v[96:97], v123 offset:0\n\t"
        "ds_read_b64_tr_b16 v[98:99], v123 offset:3072\n\t"
        "s_waitcnt lgkmcnt(10)\n\t"
        "v_mfma_f32_32x32x16_bf16 v[16:31], v[100:103], v[160:163], v[16:31]\n\t"
        "v_add_u32 v128, 1, v128\n\t"
        "ds_read_b64_tr_b16 v[100:101], v124 offset:0\n\t"
        "ds_read_b64_tr_b16 v[102:103], v124 offset:3072\n\t"
        "s_waitcnt lgkmcnt(10)\n\t"
        "v_mfma_f32_32x32x16_bf16 v[32:47], v[104:107], v[160:163], v[32:47]\n\t"
        "ds_read_b64_tr_b16 v[104:105], v123 offset:128\n\t"
        "ds_read_b64_tr_b16 v[106:107], v123 offset:3200\n\t"
        "s_waitcnt lgkmcnt(10)\n\t"
        "v_mfma_f32_32x32x16_bf16 v[48:63], v[108:111], v[160:163], v[48:63]\n\t"
        "s_add_u32 s46, s46, 12288\n\t"
        "s_add_u32 s47, s47, 1\n\t"
        "ds_read_b64_tr_b16 v[108:109], v124 offset:128\n\t"
        "ds_read_b64_tr_b16 v[110:111], v124 offset:3200\n\t"
        "s_waitcnt lgkmcnt(10)\n\t"
        "v_mfma_f32_32x32x16_bf16 v[64:79], v[160:163], v[112:115], v[64:79]\n\t"
        "s_cmp_eq_u32 s46, 110592\n\t"
        "s_cselect_b32 s46, 0, s46\n\t"
        "ds_read_b64_tr_b16 v[112:113], v123 offset:256\n\t"
        "ds_read_b64_tr_b16 v[114:115], v123 offset:3328\n\t"
        "s_waitcnt lgkmcnt(10)\n\t"
        "v_mfma_f32_32x32x16_bf16 v[80:95], v[160:163], v[116:119], v[80:95]\n\t"
        "ds_write_b32 v127, v128\n\t"
        "ds_read_b64_tr_b16 v[116:117], v124 offset:256\n\t"
        "ds_read_b64_tr_b16 v[118:119], v124 offset:3328\n\t"
        "s_waitcnt vmcnt(10) lgkmcnt(11)\n\t"
        "v_mfma_f32_32x32x16_bf16 v[0:15], v[96:99], v[164:167], v[0:15]\n\t"
        "ds_read_b32 v126, v125\n\t"
        "ds_read_b64_tr_b16 v[96:97], v123 offset:6144\n\t"
        "ds_read_b64_tr_b16 v[98:99], v123 offset:9216\n\t"
        "s_waitcnt lgkmcnt(12)\n\t"
        "v_mfma_f32_32x32x16_bf16 v[16:31], v[100:103], v[164:167], v[16:31]\n\t"
        "v_add_u32 v129, s46, v121\n\t"
        "ds_read_b64_tr_b16 v[100:101], v124 offset:6144\n\t"
        "ds_read_b64_tr_b16 v[102:103], v124 offset:9216\n\t"
        "s_waitcnt lgkmcnt(12)\n\t"
        "v_mfma_f32_32x32x16_bf16 v[32:47], v[104:107], v[164:167], v[32:47]\n\t"
        "ds_read_b64_tr_b16 v[104:105], v123 offset:6272\n\t"
        "ds_read_b64_tr_b16 v[106:107], v123 offset:9344\n\t"
        "s_waitcnt lgkmcnt(12)\n\t"
        "v_mfma_f32_32x32x16_bf16 v[48:63], v[108:111], v[164:167], v[48:63]\n\t"
        "v_add_u32 v130, s46, v122\n\t"
        "ds_read_b64_tr_b16 v[108:109], v124 offset:6272\n\t"
        "ds_read_b64_tr_b16 v[110:111], v124 offset:9344\n\t"
        "s_waitcnt lgkmcnt(12)\n\t"
        "v_mfma_f32_32x32x16_bf16 v[64:79], v[164:167], v[112:115], v[64:79]\n\t"
        "ds_read_b64_tr_b16 v[112:113], v123 offset:6400\n\t"
        "ds_read_b64_tr_b16 v[114:115], v123 offset:9472\n\t"
        "s_waitcnt lgkmcnt(10)\n\t"
        "v_mfma_f32_32x32x16_bf16 v[80:95], v[164:167], v[116:119], v[80:95]\n\t"
        "v_cmp_gt_u32 vcc, s47, v126\n\t"
        "ds_read_b64_tr_b16 v[116:117], v124 offset:6400\n\t"
        "ds_read_b64_tr_b16 v[118:119], v124 offset:9472\n\t"
        "s_waitcnt vmcnt(9) lgkmcnt(10)\n\t"
        "v_mfma_f32_32x32x16_bf16 v[0:15], v[96:99], v[168:171], v[0:15]\n\t"
        "s_cbranch_vccnz 68f\n\t"
        "69:\n\t"
        "ds_read_b64_tr_b16 v[96:97], v129 offset:0\n\t"
        "ds_read_b64_tr_b16 v[98:99], v129 offset:3072\n\t"
        "s_waitcnt lgkmcnt(10)\n\t"
        "v_mfma_f32_32x32x16_bf16 v[16:31], v[100:103], v[168:171], v[16:31]\n\t"
        "v_add_u32 v128, 1, v128\n\t"
        "ds_read_b64_tr_b16 v[100:101], v130 offset:0\n\t"
        "ds_read_b64_tr_b16 v[102:103], v130 offset:3072\n\t"
        "s_waitcnt lgkmcnt(10)\n\t"
        "v_mfma_f32_32x32x16_bf16 v[32:47], v[104:107], v[168:171], v[32:47]\n\t"
        "ds_read_b64_tr_b16 v[104:105], v129 offset:128\n\t"
        "ds_read_b64_tr_b16 v[106:107], v129 offset:3200\n\t"
        "s_waitcnt lgkmcnt(10)\n\t"
        "v_mfma_f32_32x32x16_bf16 v[48:63], v[108:111], v[168:171], v[48:63]\n\t"
        "s_add_u32 s46, s46, 12288\n\t"
        "s_add_u32 s47, s47, 1\n\t"
        "ds_read_b64_tr_b16 v[108:109], v130 offset:128\n\t"
        "ds_read_b64_tr_b16 v[110:111], v130 offset:3200\n\t"
        "s_waitcnt lgkmcnt(10)\n\t"
        "v_mfma_f32_32x32x16_bf16 v[64:79], v[168:171], v[112:115], v[64:79]\n\t"
        "s_cmp_eq_u32 s46, 110592\n\t"
        "s_cselect_b32 s46, 0, s46\n\t"
        "ds_read_b64_tr_b16 v[112:113], v129 offset:256\n\t"
        "ds_read_b64_tr_b16 v[114:115], v129 offset:3328\n\t"
        "s_waitcnt lgkmcnt(10)\n\t"
        "v_mfma_f32_32x32x16_bf16 v[80:95], v[168:171], v[116:119], v[80:95]\n\t"
        "ds_write_b32 v127, v128\n\t"
        "ds_read_b64_tr_b16 v[116:117], v130 offset:256\n\t"
        "ds_read_b64_tr_b16 v[118:119], v130 offset:3328\n\t"
        "s_waitcnt vmcnt(8) lgkmcnt(11)\n\t"
        "v_mfma_f32_32x32x16_bf16 v[0:15], v[96:99], v[172:175], v[0:15]\n\t"
        "ds_read_b32 v126, v125\n\t"
        "ds_read_b64_tr_b16 v[96:97], v129 offset:6144\n\t"
        "ds_read_b64_tr_b16 v[98:99], v129 offset:9216\n\t"
        "s_waitcnt lgkmcnt(12)\n\t"
        "v_mfma_f32_32x32x16_bf16 v[16:31], v[100:103], v[172:175], v[16:31]\n\t"
        "v_add_u32 v123, s46, v121\n\t"
        "ds_read_b64_tr_b16 v[100:101], v130 offset:6144\n\t"
        "ds_read_b64_tr_b16 v[102:103], v130 offset:9216\n\t"
        "s_waitcnt lgkmcnt(12)\n\t"
        "v_mfma_f32_32x32x16_bf16 v[32:47], v[104:107], v[172:175], v[32:47]\n\t"
        "ds_read_b64_tr_b16 v[104:105], v129 offset:6272\n\t"
        "ds_read_b64_tr_b16 v[106:107], v129 offset:9344\n\t"
        "s_waitcnt lgkmcnt(12)\n\t"
        "v_mfma_f32_32x32x16_bf16 v[48:63], v[108:111], v[172:175], v[48:63]\n\t"
        "v_add_u32 v124, s46, v122\n\t"
        "ds_read_b64_tr_b16 v[108:109], v130 offset:6272\n\t"
        "ds_read_b64_tr_b16 v[110:111], v130 offset:9344\n\t"
        "s_waitcnt lgkmcnt(12)\n\t"
        "v_mfma_f32_32x32x16_bf16 v[64:79], v[172:175], v[112:115], v[64:79]\n\t"
        "ds_read_b64_tr_b16 v[112:113], v129 offset:6400\n\t"
        "ds_read_b64_tr_b16 v[114:115], v129 offset:9472\n\t"
        "s_waitcnt lgkmcnt(10)\n\t"
        "v_mfma_f32_32x32x16_bf16 v[80:95], v[172:175], v[116:119], v[80:95]\n\t"
        "v_cmp_gt_u32 vcc, s47, v126\n\t"
        "ds_read_b64_tr_b16 v[116:117], v130 offset:6400\n\t"
        "ds_read_b64_tr_b16 v[118:119], v130 offset:9472\n\t"
        "s_waitcnt vmcnt(7) lgkmcnt(10)\n\t"
        "v_mfma_f32_32x32x16_bf16 v[0:15], v[96:99], v[176:179], v[0:15]\n\t"
        "s_cbranch_vccnz 70f\n\t"
        "71:\n\t"
        "ds_read_b64_tr_b16 v[96:97], v123 offset:0\n\t"
        "ds_read_b64_tr_b16 v[98:99], v123 offset:3072\n\t"
        "s_waitcnt lgkmcnt(10)\n\t"
        "v_mfma_f32_32x32x16_bf16 v[16:31], v[100:103], v[176:179], v[16:31]\n\t"
        "v_add_u32 v128, 1, v128\n\t"
        "ds_read_b64_tr_b16 v[100:101], v124 offset:0\n\t"
        "ds_read_b64_tr_b16 v[102:103], v124 offset:3072\n\t"
        "s_waitcnt lgkmcnt(10)\n\t"
        "v_mfma_f32_32x32x16_bf16 v[32:47], v[104:107], v[176:179], v[32:47]\n\t"
        "ds_read_b64_tr_b16 v[104:105], v123 offset:128\n\t"
        "ds_read_b64_tr_b16 v[106:107], v123 offset:3200\n\t"
        "s_waitcnt lgkmcnt(10)\n\t"
        "v_mfma_f32_32x32x16_bf16 v[48:63], v[108:111], v[176:179], v[48:63]\n\t"
        "s_add_u32 s46, s46, 12288\n\t"
        "s_add_u32 s47, s47, 1\n\t"
        "ds_read_b64_tr_b16 v[108:109], v124 offset:128\n\t"
        "ds_read_b64_tr_b16 v[110:111], v124 offset:3200\n\t"
        "s_waitcnt lgkmcnt(10)\n\t"
        "v_mfma_f32_32x32x16_bf16 v[64:79], v[176:179], v[112:115], v[64:79]\n\t"
        "s_cmp_eq_u32 s46, 110592\n\t"
        "s_cselect_b32 s46, 0, s46\n\t"
        "ds_read_b64_tr_b16 v[112:113], v123 offset:256\n\t"
        "ds_read_b64_tr_b16 v[114:115], v123 offset:3328\n\t"
        "s_waitcnt lgkmcnt(10)\n\t"
        "v_mfma_f32_32x32x16_bf16 v[80:95], v[176:179], v[116:119], v[80:95]\n\t"
        "ds_write_b32 v127, v128\n\t"
        "ds_read_b64_tr_b16 v[116:117], v124 offset:256\n\t"
        "ds_read_b64_tr_b16 v[118:119], v124 offset:3328\n\t"
        "s_waitcnt vmcnt(6) lgkmcnt(11)\n\t"
        "v_mfma_f32_32x32x16_bf16 v[0:15], v[96:99], v[180:183], v[0:15]\n\t"
        "ds_read_b32 v126, v125\n\t"
        "ds_read_b64_tr_b16 v[96:97], v123 offset:6144\n\t"
        "ds_read_b64_tr_b16 v[98:99], v123 offset:9216\n\t"
        "s_waitcnt lgkmcnt(12)\n\t"
        "v_mfma_f32_32x32x16_bf16 v[16:31], v[100:103], v[180:183], v[16:31]\n\t"
        "v_add_u32 v129, s46, v121\n\t"
        "ds_read_b64_tr_b16 v[100:101], v124 offset:6144\n\t"
        "ds_read_b64_tr_b16 v[102:103], v124 offset:9216\n\t"
        "s_waitcnt lgkmcnt(12)\n\t"
        "v_mfma_f32_32x32x16_bf16 v[32:47], v[104:107], v[180:183], v[32:47]\n\t"
        "ds_read_b64_tr_b16 v[104:105], v123 offset:6272\n\t"
        "ds_read_b64_tr_b16 v[106:107], v123 offset:9344\n\t"
        "s_waitcnt lgkmcnt(12)\n\t"
        "v_mfma_f32_32x32x16_bf16 v[48:63], v[108:111], v[180:183], v[48:63]\n\t"
        "v_add_u32 v130, s46, v122\n\t"
        "ds_read_b64_tr_b16 v[108:109], v124 offset:6272\n\t"
        "ds_read_b64_tr_b16 v[110:111], v124 offset:9344\n\t"
        "s_waitcnt lgkmcnt(12)\n\t"
        "v_mfma_f32_32x32x16_bf16 v[64:79], v[180:183], v[112:115], v[64:79]\n\t"
        "ds_read_b64_tr_b16 v[112:113], v123 offset:6400\n\t"
        "ds_read_b64_tr_b16 v[114:115], v123 offset:9472\n\t"
        "s_waitcnt lgkmcnt(10)\n\t"
        "v_mfma_f32_32x32x16_bf16 v[80:95], v[180:183], v[116:119], v[80:95]\n\t"
        "v_cmp_gt_u32 vcc, s47, v126\n\t"
        "ds_read_b64_tr_b16 v[116:117], v124 offset:6400\n\t"
        "ds_read_b64_tr_b16 v[118:119], v124 offset:9472\n\t"
        "s_waitcnt vmcnt(5) lgkmcnt(10)\n\t"
        "v_mfma_f32_32x32x16_bf16 v[0:15], v[96:99], v[184:187], v[0:15]\n\t"
        "s_cbranch_vccnz 72f\n\t"
        "73:\n\t"
        "ds_read_b64_tr_b16 v[96:97], v129 offset:0\n\t"
        "ds_read_b64_tr_b16 v[98:99], v129 offset:3072\n\t"
        "s_waitcnt lgkmcnt(10)\n\t"
        "v_mfma_f32_32x32x16_bf16 v[16:31], v[100:103], v[184:187], v[16:31]\n\t"
        "v_add_u32 v128, 1, v128\n\t"
        "ds_read_b64_tr_b16 v[100:101], v130 offset:0\n\t"
        "ds_read_b64_tr_b16 v[102:103], v130 offset:3072\n\t"
        "s_waitcnt lgkmcnt(10)\n\t"
        "v_mfma_f32_32x32x16_bf16 v[32:47], v[104:107], v[184:187], v[32:47]\n\t"
        "ds_read_b64_tr_b16 v[104:105], v129 offset:128\n\t"
        "ds_read_b64_tr_b16 v[106:107], v129 offset:3200\n\t"
        "s_waitcnt lgkmcnt(10)\n\t"
        "v_mfma_f32_32x32x16_bf16 v[48:63], v[108:111], v[184:187], v[48:63]\n\t"
        "s_add_u32 s46, s46, 12288\n\t"
        "s_add_u32 s47, s47, 1\n\t"
        "ds_read_b64_tr_b16 v[108:109], v130 offset:128\n\t"
        "ds_read_b64_tr_b16 v[110:111], v130 offset:3200\n\t"
        "s_waitcnt lgkmcnt(10)\n\t"
        "v_mfma_f32_32x32x16_bf16 v[64:79], v[184:187], v[112:115], v[64:79]\n\t"
        "s_cmp_eq_u32 s46, 110592\n\t"
        "s_cselect_b32 s46, 0, s46\n\t"
        "ds_read_b64_tr_b16 v[112:113], v129 offset:256\n\t"
        "ds_read_b64_tr_b16 v[114:115], v129 offset:3328\n\t"
        "s_waitcnt lgkmcnt(10)\n\t"
        "v_mfma_f32_32x32x16_bf16 v[80:95], v[184:187], v[116:119], v[80:95]\n\t"
        "ds_write_b32 v127, v128\n\t"
        "ds_read_b64_tr_b16 v[116:117], v130 offset:256\n\t"
        "ds_read_b64_tr_b16 v[118:119], v130 offset:3328\n\t"
        "s_waitcnt vmcnt(4) lgkmcnt(11)\n\t"
        "v_mfma_f32_32x32x16_bf16 v[0:15], v[96:99], v[188:191], v[0:15]\n\t"
        "ds_read_b64_tr_b16 v[96:97], v129 offset:6144\n\t"
        "ds_read_b64_tr_b16 v[98:99], v129 offset:9216\n\t"
        "s_waitcnt lgkmcnt(11)\n\t"
        "v_mfma_f32_32x32x16_bf16 v[16:31], v[100:103], v[188:191], v[16:31]\n\t"
        "ds_read_b64_tr_b16 v[100:101], v130 offset:6144\n\t"
        "ds_read_b64_tr_b16 v[102:103], v130 offset:9216\n\t"
        "s_waitcnt lgkmcnt(11)\n\t"
        "v_mfma_f32_32x32x16_bf16 v[32:47], v[104:107], v[188:191], v[32:47]\n\t"
        "ds_read_b64_tr_b16 v[104:105], v129 offset:6272\n\t"
        "ds_read_b64_tr_b16 v[106:107], v129 offset:9344\n\t"
        "s_waitcnt lgkmcnt(11)\n\t"
        "v_mfma_f32_32x32x16_bf16 v[48:63], v[108:111], v[188:191], v[48:63]\n\t"
        "ds_read_b64_tr_b16 v[108:109], v130 offset:6272\n\t"
        "ds_read_b64_tr_b16 v[110:111], v130 offset:9344\n\t"
        "s_waitcnt lgkmcnt(11)\n\t"
        "v_mfma_f32_32x32x16_bf16 v[64:79], v[188:191], v[112:115], v[64:79]\n\t"
        "ds_read_b64_tr_b16 v[112:113], v129 offset:6400\n\t"
        "ds_read_b64_tr_b16 v[114:115], v129 offset:9472\n\t"
        "s_waitcnt lgkmcnt(10)\n\t"
        "v_mfma_f32_32x32x16_bf16 v[80:95], v[188:191], v[116:119], v[80:95]\n\t"
        "ds_read_b64_tr_b16 v[116:117], v130 offset:6400\n\t"
        "ds_read_b64_tr_b16 v[118:119], v130 offset:9472\n\t"
        "s_waitcnt vmcnt(3) lgkmcnt(10)\n\t"
        "v_mfma_f32_32x32x16_bf16 v[0:15], v[96:99], v[192:195], v[0:15]\n\t"
        "s_waitcnt lgkmcnt(8)\n\t"
        "v_mfma_f32_32x32x16_bf16 v[16:31], v[100:103], v[192:195], v[16:31]\n\t"
        "s_waitcnt lgkmcnt(6)\n\t"
        "v_mfma_f32_32x32x16_bf16 v[32:47], v[104:107], v[192:195], v[32:47]\n\t"
        "s_waitcnt lgkmcnt(4)\n\t"
        "v_mfma_f32_32x32x16_bf16 v[48:63], v[108:111], v[192:195], v[48:63]\n\t"
        "s_waitcnt lgkmcnt(2)\n\t"
        "v_mfma_f32_32x32x16_bf16 v[64:79], v[192:195], v[112:115], v[64:79]\n\t"
        "s_waitcnt lgkmcnt(0)\n\t"
        "v_mfma_f32_32x32x16_bf16 v[80:95], v[192:195], v[116:119], v[80:95]\n\t"
        "s_waitcnt vmcnt(0)\n\t"
        "s_nop 10\n\t"
        "s_branch 9f\n\t"
        "16:\n\t"
        "s_mov_b32 s48, 0x10000\n\t"
        "8:\n\t"
        "ds_read_b32 v126, v125\n\t"
        "s_sub_u32 s48, s48, 1\n\t"
        "s_waitcnt lgkmcnt(0)\n\t"
        "s_cbranch_scc1 17b\n\t"
        "v_cmp_gt_u32 vcc, s47, v126\n\t"
        "s_cbranch_vccnz 8b\n\t"
        "s_branch 17b\n\t"
        "18:\n\t"
        "s_mov_b32 s48, 0x10000\n\t"
        "8:\n\t"
        "ds_read_b32 v126, v125\n\t"
        "s_sub_u32 s48, s48, 1\n\t"
        "s_waitcnt lgkmcnt(0)\n\t"
        "s_cbranch_scc1 19b\n\t"
        "v_cmp_gt_u32 vcc, s47, v126\n\t"
        "s_cbranch_vccnz 8b\n\t"
        "s_branch 19b\n\t"
        "20:\n\t"
        "s_mov_b32 s48, 0x10000\n\t"
        "8:\n\t"
        "ds_read_b32 v126, v125\n\t"
        "s_sub_u32 s48, s48, 1\n\t"
        "s_waitcnt lgkmcnt(0)\n\t"
        "s_cbranch_scc1 21b\n\t"
        "v_cmp_gt_u32 vcc, s47, v126\n\t"
        "s_cbranch_vccnz 8b\n\t"
        "s_branch 21b\n\t"
        "22:\n\t"
        "s_mov_b32 s48, 0x10000\n\t"
        "8:\n\t"
        "ds_read_b32 v126, v125\n\t"
        "s_sub_u32 s48, s48, 1\n\t"
        "s_waitcnt lgkmcnt(0)\n\t"
        "s_cbranch_scc1 23b\n\t"
        "v_cmp_gt_u32 vcc, s47, v126\n\t"
        "s_cbranch_vccnz 8b\n\t"
        "s_branch 23b\n\t"
        "24:\n\t"
        "s_mov_b32 s48, 0x10000\n\t"
        "8:\n\t"
        "ds_read_b32 v126, v125\n\t"
        "s_sub_u32 s48, s48, 1\n\t"
        "s_waitcnt lgkmcnt(0)\n\t"
        "s_cbranch_scc1 25b\n\t"
        "v_cmp_gt_u32 vcc, s47, v126\n\t"
        "s_cbranch_vccnz 8b\n\t"
        "s_branch 25b\n\t"
        "26:\n\t"
        "s_mov_b32 s48, 0x10000\n\t"
        "8:\n\t"
        "ds_read_b32 v126, v125\n\t"
        "s_sub_u32 s48, s48, 1\n\t"
        "s_waitcnt lgkmcnt(0)\n\t"
        "s_cbranch_scc1 27b\n\t"
        "v_cmp_gt_u32 vcc, s47, v126\n\t"
        "s_cbranch_vccnz 8b\n\t"
        "s_branch 27b\n\t"
        "28:\n\t"
        "s_mov_b32 s48, 0x10000\n\t"
        "8:\n\t"
        "ds_read_b32 v126, v125\n\t"
        "s_sub_u32 s48, s48, 1\n\t"
        "s_waitcnt lgkmcnt(0)\n\t"
        "s_cbranch_scc1 29b\n\t"
        "v_cmp_gt_u32 vcc, s47, v126\n\t"
        "s_cbranch_vccnz 8b\n\t"
        "s_branch 29b\n\t"
        "30:\n\t"
        "s_mov_b32 s48, 0x10000\n\t"
        "8:\n\t"
        "ds_read_b32 v126, v125\n\t"
        "s_sub_u32 s48, s48, 1\n\t"
        "s_waitcnt lgkmcnt(0)\n\t"
        "s_cbranch_scc1 31b\n\t"
        "v_cmp_gt_u32 vcc, s47, v126\n\t"
        "s_cbranch_vccnz 8b\n\t"
        "s_branch 31b\n\t"
        "64:\n\t"
        "s_mov_b32 s48, 0x10000\n\t"
        "8:\n\t"
        "ds_read_b32 v126, v125\n\t"
        "s_sub_u32 s48, s48, 1\n\t"
        "s_waitcnt lgkmcnt(0)\n\t"
        "s_cbranch_scc1 65b\n\t"
        "v_cmp_gt_u32 vcc, s47, v126\n\t"
        "s_cbranch_vccnz 8b\n\t"
        "s_branch 65b\n\t"
        "66:\n\t"
        "s_mov_b32 s48, 0x10000\n\t"
        "8:\n\t"
        "ds_read_b32 v126, v125\n\t"
        "s_sub_u32 s48, s48, 1\n\t"
        "s_waitcnt lgkmcnt(0)\n\t"
        "s_cbranch_scc1 67b\n\t"
        "v_cmp_gt_u32 vcc, s47, v126\n\t"
        "s_cbranch_vccnz 8b\n\t"
        "s_branch 67b\n\t"
        "68:\n\t"
        "s_mov_b32 s48, 0x10000\n\t"
        "8:\n\t"
        "ds_read_b32 v126, v125\n\t"
        "s_sub_u32 s48, s48, 1\n\t"
        "s_waitcnt lgkmcnt(0)\n\t"
        "s_cbranch_scc1 69b\n\t"
        "v_cmp_gt_u32 vcc, s47, v126\n\t"
        "s_cbranch_vccnz 8b\n\t"
        "s_branch 69b\n\t"
        "70:\n\t"
        "s_mov_b32 s48, 0x10000\n\t"
        "8:\n\t"
        "ds_read_b32 v126, v125\n\t"
        "s_sub_u32 s48, s48, 1\n\t"
        "s_waitcnt lgkmcnt(0)\n\t"
        "s_cbranch_scc1 71b\n\t"
        "v_cmp_gt_u32 vcc, s47, v126\n\t"
        "s_cbranch_vccnz 8b\n\t"
        "s_branch 71b\n\t"
        "72:\n\t"
        "s_mov_b32 s48, 0x10000\n\t"
        "8:\n\t"
        "ds_read_b32 v126, v125\n\t"
        "s_sub_u32 s48, s48, 1\n\t"
        "s_waitcnt lgkmcnt(0)\n\t"
        "s_cbranch_scc1 73b\n\t"
        "v_cmp_gt_u32 vcc, s47, v126\n\t"
        "s_cbranch_vccnz 8b\n\t"
        "s_branch 73b\n\t"
        "9:\n\t"
        : "+{v[0:15]}"(acc[0]), "+{v[16:31]}"(acc[1]), "+{v[32:47]}"(acc[2]), "+{v[48:63]}"(acc[3]), "+{v[64:79]}"(acc[4]), "+{v[80:95]}"(acc[5]),
          "+{v[96:111]}"(stA), "+{v[112:127]}"(stB), "+{v[128:131]}"(stC), "+{v[148:163]}"(xr1), "+{v[164:179]}"(xr2), "+{v[180:195]}"(xr3),
          "+{s44}"(soff), "+{s46}"(slot), "+{s47}"(need)
        : "{s[40:43]}"(srd) : "v132", "v133", "v134", "v135", "v136", "v137", "v138", "v139", "v140", "v141", "v142", "v143", "v144", "v145", "v146", "v147", "s45", "s48", "vcc", "scc", "memory");
      STAMPV(4 + 16 * L);
      PHASE_VARS;
      if (L == 1) {
        f32x4 av[4], cs[4][4];
        float cv[2];
#pragma unroll
        for (int g = 0; g < 4; ++g) av[g] = *reinterpret_cast<const f32x4*>(smem + L_AB + wid * 256 + (8 * g + 4 * hi) * 4);
        SBAR();
#pragma unroll
        for (int j = 0; j < 6; ++j)
#pragma unroll
          for (int e = 0; e < 16; ++e) acc[j][e] *= j < 4 ? al : av[e >> 2][e & 3];
        SBAR();
        lds_wait_ge(lbase + L_W + 4 + 4 * (lane & 1), 1);
        const int idx = (int)__builtin_amdgcn_readfirstlane(words[3]);
        const bool mine = (idx >> 5) == tile;
        if (mine) {
#pragma unroll
          for (int j = 0; j < 6; ++j) {
            const int cb = (j >> 1) * 64 + ((j & 1) ^ hh) * 32;
            if (j >= 4) cv[j - 4] = *reinterpret_cast<const float*>(smem + L_PATCH + (cb + r32) * 4);
            else {
#pragma unroll
              for (int g = 0; g < 4; ++g) cs[j][g] = *reinterpret_cast<const f32x4*>(smem + L_PATCH + (cb + 8 * g + 4 * hi) * 4);
            }
          }
          SBAR();
          const int rel = (idx & 31) - 4 * hi;
#pragma unroll
          for (int j = 0; j < 6; ++j)
#pragma unroll
            for (int e = 0; e < 16; ++e)
              acc[j][e] = j < 4 ? (r32 == (idx & 31) ? cs[j & 3][e >> 2][e & 3] : acc[j][e]) : (rel == 8 * (e >> 2) + (e & 3) ? cv[j & 1] : acc[j][e]);
        }
      }
      constexpr float QS = 0.17677669529663687f * 1.4426950408889634f;
      u32x4 own[4];
#pragma unroll
      for (int j = 5; j >= 0; --j) {
        u32x4 o[2];
#pragma unroll
        for (int e = 0; e < 8; ++e) {
          unsigned w = pack2(acc[j][2 * e], acc[j][2 * e + 1]);
          if (j < 2) w = pack2(lo16(w) * QS, hi16(w) * QS);
          o[e >> 2][e & 3] = w;
        }
        if (j >= 4) { o[0] = fo_swap(o[0]); o[1] = fo_swap(o[1]); }
        const int hs = (j & 1) ^ hh;
        if (j >= 2) {
          const unsigned dst = OFF_KV + L * (10 * MBY) + (tn * 2 + hs) * HS + tile * 4096 + ((j >> 1) - 1) * 2048 + lane * 16;
          stg(rs, dst, o[0]); stg(rs, dst + 1024, o[1]);
        }
        if (j >= 2 && !(j & 1)) { own[j - 2] = o[0]; own[j - 1] = o[1]; }
        else {
          char* p = smem + L_XCH + (j < 2 ? (hs * 4 + wid) * 2048 : 16384 + wid * 4096 + ((j >> 1) - 1) * 2048) + lane * 16;
          *reinterpret_cast<u32x4*>(p) = o[0]; *reinterpret_cast<u32x4*>(p + 1024) = o[1];
        }
        SBAR();
      }
      BAR_ALL();
      kva = cat4(own[0], own[1], own[2], own[3]);
      const u32x4* pk = reinterpret_cast<const u32x4*>(smem + L_XCH + 16384 + (wid ^ 1) * 4096 + lane * 16);
#pragma unroll
      for (int u = 0; u < 4; ++u) {
        const u32x4* p = reinterpret_cast<const u32x4*>(smem + L_XCH + (hh * 4 + ((2 * kh + 2 + u) & 3)) * 2048 + lane * 16);
        q[u] = __builtin_shufflevector(p[0], p[64], 0, 1, 2, 3, 4, 5, 6, 7);
      }
      kvb = cat4(pk[0], pk[64], pk[128], pk[192]);
      SBAR();
      asm volatile(
        "v_mov_b64 v[0:1], 0\n\t"
        "v_mov_b64 v[2:3], 0\n\t"
        "v_mov_b64 v[4:5], 0\n\t"
        "v_mov_b64 v[6:7], 0\n\t"
        "v_mov_b64 v[8:9], 0\n\t"
        "v_mov_b64 v[10:11], 0\n\t"
        "v_mov_b64 v[12:13], 0\n\t"
        "v_mov_b64 v[14:15], 0\n\t"
        "v_mov_b64 v[16:17], 0\n\t"
        "v_mov_b64 v[18:19], 0\n\t"
        "v_mov_b64 v[20:21], 0\n\t"
        "v_mov_b64 v[22:23], 0\n\t"
        "v_mov_b64 v[24:25], 0\n\t"
        "v_mov_b64 v[26:27], 0\n\t"
        "v_mov_b64 v[28:29], 0\n\t"
        "v_mov_b64 v[30:31], 0\n\t"
        "v_mov_b64 v[32:33], 0\n\t"
        "v_mov_b64 v[34:35], 0\n\t"
        "v_mov_b64 v[36:37], 0\n\t"
        "v_mov_b64 v[38:39], 0\n\t"
        "v_mov_b64 v[40:41], 0\n\t"
        "v_mov_b64 v[42:43], 0\n\t"
        "v_mov_b64 v[44:45], 0\n\t"
        "v_mov_b64 v[46:47], 0\n\t"
        "v_mov_b64 v[48:49], 0\n\t"
        "v_mov_b64 v[50:51], 0\n\t"
        "v_mov_b64 v[52:53], 0\n\t"
        "v_mov_b64 v[54:55], 0\n\t"
        "v_mov_b64 v[56:57], 0\n\t"
        "v_mov_b64 v[58:59], 0\n\t"
        "v_mov_b64 v[60:61], 0\n\t"
        "v_mov_b64 v[62:63], 0\n\t"
        "v_mov_b64 v[128:129], 0\n\t"
        "v_mov_b64 v[130:131], 0\n\t"
        "v_mov_b64 v[132:133], 0\n\t"
        "v_mov_b64 v[134:135], 0\n\t"
        "v_mov_b64 v[136:137], 0\n\t"
        "v_mov_b64 v[138:139], 0\n\t"
        "v_mov_b64 v[140:141], 0\n\t"
        "v_mov_b64 v[142:143], 0\n\t"
        : "={v[0:15]}"(oz[0]), "={v[16:31]}"(oz[1]), "={v[32:47]}"(oz[2]), "={v[48:63]}"(oz[3]), "={v[128:143]}"(oz[4]));
    }
    STAMPV(5 + 16 * L);
    {
      u32x4 ones;
      unsigned kvoff;
      u32x8 fv, cv;
      asm volatile("" : "={v[184:191]}"(fv), "={v[216:223]}"(cv));
      { PHASE_VARS;
        const unsigned v = (lane & 3) == 0 ? 0x3F803F80u : 0u; const bool up = (lane & 4) != 0; ones[0] = ones[1] = up ? 0u : v; ones[2] = ones[3] = up ? v : 0u;
        kvoff = OFF_KV + L * (10 * MBY) + (tn * 2 + hh) * HS + kh * 8192 + lane * 16;
        fv[1] = lbase + L_FREE + wid * 256 + lane * 4; fv[2] = 32 * L + 32;
        cv[0] = lbase + L_XCH + wid * 8192 + lane * 16; cv[1] = lbase + L_XCH + (wid ^ 2) * 8192 + lane * 16;
        cv[2] = lbase + L_XCH + 32768 + wid * 512 + lane * 4; cv[3] = lbase + L_XCH + 32768 + (wid ^ 2) * 512 + lane * 4;
        cv[4] = lbase + L_W + 48 + wid * 4; cv[5] = lbase + L_W + 48 + (wid ^ 2) * 4; cv[6] = L + 1;
        cv[7] = ((tile * 32 + r32) * D_ + tn * 64 + hi * 8) * 2; }
      f32x16 o0 = oz[0], o1 = oz[1], o2 = oz[2], o3 = oz[3], lv = oz[4];
      unsigned soff = tm * 16384;
      STAMP_DECL;
      asm volatile(
        "s_nop 1\n\t"
        "v_mfma_f32_32x32x16_bf16 v[96:111], v[152:155], v[64:67], 0\n\t"
        "v_mfma_f32_32x32x16_bf16 v[96:111], v[156:159], v[68:71], v[96:111]\n\t"
        "s_mov_b32 s46, 0\n\t"
        "ds_write_b32 v185, v186\n\t"
        "s_nop 9\n\t"
        "v_exp_f32 v96, v96\n\t"
        "v_exp_f32 v97, v97\n\t"
        "v_exp_f32 v98, v98\n\t"
        "v_exp_f32 v99, v99\n\t"
        "v_exp_f32 v100, v100\n\t"
        "v_exp_f32 v101, v101\n\t"
        "v_mfma_f32_32x32x16_bf16 v[112:127], v[152:155], v[72:75], 0\n\t"
        "v_exp_f32 v102, v102\n\t"
        "v_exp_f32 v103, v103\n\t"
        "v_exp_f32 v104, v104\n\t"
        "v_mfma_f32_32x32x16_bf16 v[112:127], v[156:159], v[76:79], v[112:127]\n\t"
        "v_exp_f32 v105, v105\n\t"
        "v_exp_f32 v106, v106\n\t"
        "v_exp_f32 v107, v107\n\t"
        "v_smfmac_f32_32x32x32_bf16 v[0:15], v[160:163], v[96:103], v148\n\t"
        "v_exp_f32 v108, v108\n\t"
        "v_exp_f32 v109, v109\n\t"
        "v_exp_f32 v110, v110\n\t"
        "v_smfmac_f32_16x16x64_bf16 v[128:131], v[144:147], v[96:103], v148\n\t"
        "v_exp_f32 v111, v111\n\t"
        "v_exp_f32 v112, v112\n\t"
        "v_exp_f32 v113, v113\n\t"
        "v_smfmac_f32_32x32x32_bf16 v[0:15], v[164:167], v[104:111], v148\n\t"
        "v_exp_f32 v114, v114\n\t"
        "v_exp_f32 v115, v115\n\t"
        "v_exp_f32 v116, v116\n\t"
        "v_smfmac_f32_16x16x64_bf16 v[128:131], v[144:147], v[104:111], v148\n\t"
        "v_exp_f32 v117, v117\n\t"
        "v_mfma_f32_32x32x16_bf16 v[96:111], v[152:155], v[80:83], 0\n\t"
        "v_exp_f32 v118, v118\n\t"
        "v_exp_f32 v119, v119\n\t"
        "v_exp_f32 v120, v120\n\t"
        "v_mfma_f32_32x32x16_bf16 v[96:111], v[156:159], v[84:87], v[96:111]\n\t"
        "v_exp_f32 v121, v121\n\t"
        "v_exp_f32 v122, v122\n\t"
        "v_exp_f32 v123, v123\n\t"
        "v_smfmac_f32_32x32x32_bf16 v[16:31], v[160:163], v[112:119], v148\n\t"
        "v_exp_f32 v124, v124\n\t"
        "v_exp_f32 v125, v125\n\t"
        "v_exp_f32 v126, v126\n\t"
        "v_smfmac_f32_16x16x64_bf16 v[132:135], v[144:147], v[112:119], v148\n\t"
        "v_exp_f32 v127, v127\n\t"
        "v_exp_f32 v96, v96\n\t"
        "v_exp_f32 v97, v97\n\t"
        "v_smfmac_f32_32x32x32_bf16 v[16:31], v[164:167], v[120:127], v148\n\t"
        "v_exp_f32 v98, v98\n\t"
        "v_exp_f32 v99, v99\n\t"
        "v_exp_f32 v100, v100\n\t"
        "v_smfmac_f32_16x16x64_bf16 v[132:135], v[144:147], v[120:127], v148\n\t"
        "v_exp_f32 v101, v101\n\t"
        "v_mfma_f32_32x32x16_bf16 v[112:127], v[152:155], v[88:91], 0\n\t"
        "v_exp_f32 v102, v102\n\t"
        "v_exp_f32 v103, v103\n\t"
        "v_exp_f32 v104, v104\n\t"
        "v_mfma_f32_32x32x16_bf16 v[112:127], v[156:159], v[92:95], v[112:127]\n\t"
        "v_exp_f32 v105, v105\n\t"
        "v_exp_f32 v106, v106\n\t"
        "v_exp_f32 v107, v107\n\t"
        "v_smfmac_f32_32x32x32_bf16 v[32:47], v[160:163], v[96:103], v148\n\t"
        "v_exp_f32 v108, v108\n\t"
        "v_exp_f32 v109, v109\n\t"
        "v_exp_f32 v110, v110\n\t"
        "v_smfmac_f32_16x16x64_bf16 v[136:139], v[144:147], v[96:103], v148\n\t"
        "v_exp_f32 v111, v111\n\t"
        "v_exp_f32 v112, v112\n\t"
        "v_exp_f32 v113, v113\n\t"
        "v_smfmac_f32_32x32x32_bf16 v[32:47], v[164:167], v[104:111], v148\n\t"
        "v_exp_f32 v114, v114\n\t"
        "v_exp_f32 v115, v115\n\t"
        "v_exp_f32 v116, v116\n\t"
        "v_smfmac_f32_16x16x64_bf16 v[136:139], v[144:147], v[104:111], v148\n\t"
        "v_exp_f32 v117, v117\n\t"
        "v_mfma_f32_32x32x16_bf16 v[96:111], v[168:171], v[64:67], 0\n\t"
        "v_exp_f32 v118, v118\n\t"
        "v_exp_f32 v119, v119\n\t"
        "v_exp_f32 v120, v120\n\t"
        "v_mfma_f32_32x32x16_bf16 v[96:111], v[172:175], v[68:71], v[96:111]\n\t"
        "v_exp_f32 v121, v121\n\t"
        "v_exp_f32 v122, v122\n\t"
        "v_exp_f32 v123, v123\n\t"
        "v_smfmac_f32_32x32x32_bf16 v[48:63], v[160:163], v[112:119], v148\n\t"
        "v_exp_f32 v124, v124\n\t"
        "v_exp_f32 v125, v125\n\t"
        "v_exp_f32 v126, v126\n\t"
        "v_smfmac_f32_16x16x64_bf16 v[140:143], v[144:147], v[112:119], v148\n\t"
        "v_exp_f32 v127, v127\n\t"
        "v_exp_f32 v96, v96\n\t"
        "v_exp_f32 v97, v97\n\t"
        "v_smfmac_f32_32x32x32_bf16 v[48:63], v[164:167], v[120:127], v148\n\t"
        "v_exp_f32 v98, v98\n\t"
        "v_exp_f32 v99, v99\n\t"
        "v_exp_f32 v100, v100\n\t"
        "v_smfmac_f32_16x16x64_bf16 v[140:143], v[144:147], v[120:127], v148\n\t"
        "v_exp_f32 v101, v101\n\t"
        "v_mfma_f32_32x32x16_bf16 v[112:127], v[168:171], v[72:75], 0\n\t"
        "v_exp_f32 v102, v102\n\t"
        "v_exp_f32 v103, v103\n\t"
        "v_exp_f32 v104, v104\n\t"
        "ds_read_b32 v151, v224\n\t"
        "v_mfma_f32_32x32x16_bf16 v[112:127], v[172:175], v[76:79], v[112:127]\n\t"
        "v_exp_f32 v105, v105\n\t"
        "v_exp_f32 v106, v106\n\t"
        "v_exp_f32 v107, v107\n\t"
        "v_smfmac_f32_32x32x32_bf16 v[0:15], v[176:179], v[96:103], v148\n\t"
        "v_exp_f32 v108, v108\n\t"
        "v_exp_f32 v109, v109\n\t"
        "v_exp_f32 v110, v110\n\t"
        "v_smfmac_f32_16x16x64_bf16 v[128:131], v[144:147], v[96:103], v148\n\t"
        "v_exp_f32 v111, v111\n\t"
        "s_cmp_lg_u32 s46, 0\n\t"
        "s_cbranch_scc1 31f\n\t"
        "s_waitcnt lgkmcnt(0)\n\t"
        "v_cmp_ne_u32 vcc, v222, v151\n\t"
        "s_cbranch_vccz 30f\n\t"
        "31:\n\t"
        "v_exp_f32 v112, v112\n\t"
        "v_exp_f32 v113, v113\n\t"
        "v_smfmac_f32_32x32x32_bf16 v[0:15], v[180:183], v[104:111], v148\n\t"
        "v_exp_f32 v114, v114\n\t"
        "v_exp_f32 v115, v115\n\t"
        "v_exp_f32 v116, v116\n\t"
        "v_smfmac_f32_16x16x64_bf16 v[128:131], v[144:147], v[104:111], v148\n\t"
        "v_exp_f32 v117, v117\n\t"
        "v_mfma_f32_32x32x16_bf16 v[96:111], v[168:171], v[80:83], 0\n\t"
        "v_exp_f32 v118, v118\n\t"
        "v_exp_f32 v119, v119\n\t"
        "v_exp_f32 v120, v120\n\t"
        "ds_read_b32 v151, v224\n\t"
        "v_mfma_f32_32x32x16_bf16 v[96:111], v[172:175], v[84:87], v[96:111]\n\t"
        "v_exp_f32 v121, v121\n\t"
        "v_exp_f32 v122, v122\n\t"
        "v_exp_f32 v123, v123\n\t"
        "v_smfmac_f32_32x32x32_bf16 v[16:31], v[176:179], v[112:119], v148\n\t"
        "v_exp_f32 v124, v124\n\t"
        "v_exp_f32 v125, v125\n\t"
        "v_exp_f32 v126, v126\n\t"
        "v_smfmac_f32_16x16x64_bf16 v[132:135], v[144:147], v[112:119], v148\n\t"
        "v_exp_f32 v127, v127\n\t"
        "s_cmp_lg_u32 s46, 0\n\t"
        "s_cbranch_scc1 33f\n\t"
        "s_waitcnt lgkmcnt(0)\n\t"
        "v_cmp_ne_u32 vcc, v222, v151\n\t"
        "s_cbranch_vccz 32f\n\t"
        "33:\n\t"
        "v_exp_f32 v96, v96\n\t"
        "v_exp_f32 v97, v97\n\t"
        "v_smfmac_f32_32x32x32_bf16 v[16:31], v[180:183], v[120:127], v148\n\t"
        "v_exp_f32 v98, v98\n\t"
        "v_exp_f32 v99, v99\n\t"
        "v_exp_f32 v100, v100\n\t"
        "v_smfmac_f32_16x16x64_bf16 v[132:135], v[144:147], v[120:127], v148\n\t"
        "v_exp_f32 v101, v101\n\t"
        "v_mfma_f32_32x32x16_bf16 v[112:127], v[168:171], v[88:91], 0\n\t"
        "v_exp_f32 v102, v102\n\t"
        "v_exp_f32 v103, v103\n\t"
        "v_exp_f32 v104, v104\n\t"
        "v_mfma_f32_32x32x16_bf16 v[112:127], v[172:175], v[92:95], v[112:127]\n\t"
        "v_exp_f32 v105, v105\n\t"
        "v_exp_f32 v106, v106\n\t"
        "v_exp_f32 v107, v107\n\t"
        "v_smfmac_f32_32x32x32_bf16 v[32:47], v[176:179], v[96:103], v148\n\t"
        "v_exp_f32 v108, v108\n\t"
        "v_exp_f32 v109, v109\n\t"
        "v_exp_f32 v110, v110\n\t"
        "v_smfmac_f32_16x16x64_bf16 v[136:139], v[144:147], v[96:103], v148\n\t"
        "v_exp_f32 v111, v111\n\t"
        "s_cmp_lg_u32 s46, 0\n\t"
        "s_cbranch_scc1 5f\n\t"
        "s_mov_b32 s45, 0x200000\n\t"
        "2:\n\t"
        "ds_read_b32 v151, v224\n\t"
        "s_sub_u32 s45, s45, 1\n\t"
        "s_waitcnt lgkmcnt(0)\n\t"
        "s_cbranch_scc1 3f\n\t"
        "v_cmp_ne_u32 vcc, v222, v151\n\t"
        "s_cbranch_vccnz 2b\n\t"
        "3:\n\t"
        "s_add_u32 s44, s44, 0x4000\n\t"
        "s_and_b32 s44, s44, 0x3ffff\n\t"
        "buffer_load_dwordx4 v[184:187], v149, s[40:43], s44 offen offset:0 sc1\n\t"
        "buffer_load_dwordx4 v[188:191], v149, s[40:43], s44 offen offset:1024 sc1\n\t"
        "buffer_load_dwordx4 v[192:195], v149, s[40:43], s44 offen offset:2048 sc1\n\t"
        "buffer_load_dwordx4 v[196:199], v149, s[40:43], s44 offen offset:3072 sc1\n\t"
        "buffer_load_dwordx4 v[200:203], v150, s[40:43], s44 offen offset:0 sc1\n\t"
        "buffer_load_dwordx4 v[204:207], v150, s[40:43], s44 offen offset:1024 sc1\n\t"
        "buffer_load_dwordx4 v[208:211], v150, s[40:43], s44 offen offset:2048 sc1\n\t"
        "buffer_load_dwordx4 v[212:215], v150, s[40:43], s44 offen offset:3072 sc1\n\t"
        "s_add_u32 s44, s44, 0x4000\n\t"
        "s_and_b32 s44, s44, 0x3ffff\n\t"
        "buffer_load_dwordx4 v[152:155], v149, s[40:43], s44 offen offset:0 sc1\n\t"
        "buffer_load_dwordx4 v[156:159], v149, s[40:43], s44 offen offset:1024 sc1\n\t"
        "buffer_load_dwordx4 v[160:163], v149, s[40:43], s44 offen offset:2048 sc1\n\t"
        "buffer_load_dwordx4 v[164:167], v149, s[40:43], s44 offen offset:3072 sc1\n\t"
        "5:\n\t"
        "s_mov_b32 s45, 7\n\t"
        "1:\n\t"
        "v_exp_f32 v112, v112\n\t"
        "v_exp_f32 v113, v113\n\t"
        "v_smfmac_f32_32x32x32_bf16 v[32:47], v[180:183], v[104:111], v148\n\t"
        "v_exp_f32 v114, v114\n\t"
        "v_exp_f32 v115, v115\n\t"
        "v_exp_f32 v116, v116\n\t"
        "v_smfmac_f32_16x16x64_bf16 v[136:139], v[144:147], v[104:111], v148\n\t"
        "v_exp_f32 v117, v117\n\t"
        "s_waitcnt vmcnt(8)\n\t"
        "v_mfma_f32_32x32x16_bf16 v[96:111], v[184:187], v[64:67], 0\n\t"
        "v_exp_f32 v118, v118\n\t"
        "v_exp_f32 v119, v119\n\t"
        "v_exp_f32 v120, v120\n\t"
        "v_mfma_f32_32x32x16_bf16 v[96:111], v[188:191], v[68:71], v[96:111]\n\t"
        "v_exp_f32 v121, v121\n\t"
        "v_exp_f32 v122, v122\n\t"
        "v_exp_f32 v123, v123\n\t"
        "v_smfmac_f32_32x32x32_bf16 v[48:63], v[176:179], v[112:119], v148\n\t"
        "v_exp_f32 v124, v124\n\t"
        "v_exp_f32 v125, v125\n\t"
        "v_exp_f32 v126, v126\n\t"
        "v_smfmac_f32_16x16x64_bf16 v[140:143], v[144:147], v[112:119], v148\n\t"
        "buffer_load_dwordx4 v[168:171], v150, s[40:43], s44 offen offset:0 sc1\n\t"
        "v_exp_f32 v127, v127\n\t"
        "v_exp_f32 v96, v96\n\t"
        "v_exp_f32 v97, v97\n\t"
        "v_smfmac_f32_32x32x32_bf16 v[48:63], v[180:183], v[120:127], v148\n\t"
        "v_exp_f32 v98, v98\n\t"
        "v_exp_f32 v99, v99\n\t"
        "v_exp_f32 v100, v100\n\t"
        "v_smfmac_f32_16x16x64_bf16 v[140:143], v[144:147], v[120:127], v148\n\t"
        "v_exp_f32 v101, v101\n\t"
        "v_mfma_f32_32x32x16_bf16 v[112:127], v[184:187], v[72:75], 0\n\t"
        "v_exp_f32 v102, v102\n\t"
        "v_exp_f32 v103, v103\n\t"
        "v_exp_f32 v104, v104\n\t"
        "v_mfma_f32_32x32x16_bf16 v[112:127], v[188:191], v[76:79], v[112:127]\n\t"
        "v_exp_f32 v105, v105\n\t"
        "v_exp_f32 v106, v106\n\t"
        "v_exp_f32 v107, v107\n\t"
        "v_smfmac_f32_32x32x32_bf16 v[0:15], v[192:195], v[96:103], v148\n\t"
        "v_exp_f32 v108, v108\n\t"
        "v_exp_f32 v109, v109\n\t"
        "v_exp_f32 v110, v110\n\t"
        "v_smfmac_f32_16x16x64_bf16 v[128:131], v[144:147], v[96:103], v148\n\t"
        "buffer_load_dwordx4 v[172:175], v150, s[40:43], s44 offen offset:1024 sc1\n\t"
        "v_exp_f32 v111, v111\n\t"
        "v_exp_f32 v112, v112\n\t"
        "v_exp_f32 v113, v113\n\t"
        "v_smfmac_f32_32x32x32_bf16 v[0:15], v[196:199], v[104:111], v148\n\t"
        "v_exp_f32 v114, v114\n\t"
        "v_exp_f32 v115, v115\n\t"
        "v_exp_f32 v116, v116\n\t"
        "v_smfmac_f32_16x16x64_bf16 v[128:131], v[144:147], v[104:111], v148\n\t"
        "v_exp_f32 v117, v117\n\t"
        "v_mfma_f32_32x32x16_bf16 v[96:111], v[184:187], v[80:83], 0\n\t"
        "v_exp_f32 v118, v118\n\t"
        "v_exp_f32 v119, v119\n\t"
        "v_exp_f32 v120, v120\n\t"
        "v_mfma_f32_32x32x16_bf16 v[96:111], v[188:191], v[84:87], v[96:111]\n\t"
        "v_exp_f32 v121, v121\n\t"
        "v_exp_f32 v122, v122\n\t"
        "v_exp_f32 v123, v123\n\t"
        "v_smfmac_f32_32x32x32_bf16 v[16:31], v[192:195], v[112:119], v148\n\t"
        "v_exp_f32 v124, v124\n\t"
        "v_exp_f32 v125, v125\n\t"
        "v_exp_f32 v126, v126\n\t"
        "v_smfmac_f32_16x16x64_bf16 v[132:135], v[144:147], v[112:119], v148\n\t"
        "buffer_load_dwordx4 v[176:179], v150, s[40:43], s44 offen offset:2048 sc1\n\t"
        "v_exp_f32 v127, v127\n\t"
        "v_exp_f32 v96, v96\n\t"
        "v_exp_f32 v97, v97\n\t"
        "v_smfmac_f32_32x32x32_bf16 v[16:31], v[196:199], v[120:127], v148\n\t"
        "v_exp_f32 v98, v98\n\t"
        "v_exp_f32 v99, v99\n\t"
        "v_exp_f32 v100, v100\n\t"
        "v_smfmac_f32_16x16x64_bf16 v[132:135], v[144:147], v[120:127], v148\n\t"
        "v_exp_f32 v101, v101\n\t"
        "v_mfma_f32_32x32x16_bf16 v[112:127], v[184:187], v[88:91], 0\n\t"
        "v_exp_f32 v102, v102\n\t"
        "v_exp_f32 v103, v103\n\t"
        "v_exp_f32 v104, v104\n\t"
        "v_mfma_f32_32x32x16_bf16 v[112:127], v[188:191], v[92:95], v[112:127]\n\t"
        "v_exp_f32 v105, v105\n\t"
        "v_exp_f32 v106, v106\n\t"
        "v_exp_f32 v107, v107\n\t"
        "v_smfmac_f32_32x32x32_bf16 v[32:47], v[192:195], v[96:103], v148\n\t"
        "v_exp_f32 v108, v108\n\t"
        "v_exp_f32 v109, v109\n\t"
        "v_exp_f32 v110, v110\n\t"
        "v_smfmac_f32_16x16x64_bf16 v[136:139], v[144:147], v[96:103], v148\n\t"
        "buffer_load_dwordx4 v[180:183], v150, s[40:43], s44 offen offset:3072 sc1\n\t"
        "v_exp_f32 v111, v111\n\t"
        "v_exp_f32 v112, v112\n\t"
        "v_exp_f32 v113, v113\n\t"
        "v_smfmac_f32_32x32x32_bf16 v[32:47], v[196:199], v[104:111], v148\n\t"
        "s_add_u32 s44, s44, 0x4000\n\t"
        "s_and_b32 s44, s44, 0x3ffff\n\t"
        "v_exp_f32 v114, v114\n\t"
        "v_exp_f32 v115, v115\n\t"
        "v_exp_f32 v116, v116\n\t"
        "v_smfmac_f32_16x16x64_bf16 v[136:139], v[144:147], v[104:111], v148\n\t"
        "v_exp_f32 v117, v117\n\t"
        "s_waitcnt vmcnt(8)\n\t"
        "v_mfma_f32_32x32x16_bf16 v[96:111], v[200:203], v[64:67], 0\n\t"
        "v_exp_f32 v118, v118\n\t"
        "v_exp_f32 v119, v119\n\t"
        "v_exp_f32 v120, v120\n\t"
        "v_mfma_f32_32x32x16_bf16 v[96:111], v[204:207], v[68:71], v[96:111]\n\t"
        "v_exp_f32 v121, v121\n\t"
        "v_exp_f32 v122, v122\n\t"
        "v_exp_f32 v123, v123\n\t"
        "v_smfmac_f32_32x32x32_bf16 v[48:63], v[192:195], v[112:119], v148\n\t"
        "v_exp_f32 v124, v124\n\t"
        "v_exp_f32 v125, v125\n\t"
        "v_exp_f32 v126, v126\n\t"
        "v_smfmac_f32_16x16x64_bf16 v[140:143], v[144:147], v[112:119], v148\n\t"
        "buffer_load_dwordx4 v[184:187], v149, s[40:43], s44 offen offset:0 sc1\n\t"
        "v_exp_f32 v127, v127\n\t"
        "v_exp_f32 v96, v96\n\t"
        "v_exp_f32 v97, v97\n\t"
        "v_smfmac_f32_32x32x32_bf16 v[48:63], v[196:199], v[120:127], v148\n\t"
        "v_exp_f32 v98, v98\n\t"
        "v_exp_f32 v99, v99\n\t"
        "v_exp_f32 v100, v100\n\t"
        "v_smfmac_f32_16x16x64_bf16 v[140:143], v[144:147], v[120:127], v148\n\t"
        "v_exp_f32 v101, v101\n\t"
        "v_mfma_f32_32x32x16_bf16 v[112:127], v[200:203], v[72:75], 0\n\t"
        "v_exp_f32 v102, v102\n\t"
        "v_exp_f32 v103, v103\n\t"
        "v_exp_f32 v104, v104\n\t"
        "v_mfma_f32_32x32x16_bf16 v[112:127], v[204:207], v[76:79], v[112:127]\n\t"
        "v_exp_f32 v105, v105\n\t"
        "v_exp_f32 v106, v106\n\t"
        "v_exp_f32 v107, v107\n\t"
        "v_smfmac_f32_32x32x32_bf16 v[0:15], v[208:211], v[96:103], v148\n\t"
        "v_exp_f32 v108, v108\n\t"
        "v_exp_f32 v109, v109\n\t"
        "v_exp_f32 v110, v110\n\t"
        "v_smfmac_f32_16x16x64_bf16 v[128:131], v[144:147], v[96:103], v148\n\t"
        "buffer_load_dwordx4 v[188:191], v149, s[40:43], s44 offen offset:1024 sc1\n\t"
        "v_exp_f32 v111, v111\n\t"
        "v_exp_f32 v112, v112\n\t"
        "v_exp_f32 v113, v113\n\t"
        "v_smfmac_f32_32x32x32_bf16 v[0:15], v[212:215], v[104:111], v148\n\t"
        "v_exp_f32 v114, v114\n\t"
        "v_exp_f32 v115, v115\n\t"
        "v_exp_f32 v116, v116\n\t"
        "v_smfmac_f32_16x16x64_bf16 v[128:131], v[144:147], v[104:111], v148\n\t"
        "v_exp_f32 v117, v117\n\t"
        "v_mfma_f32_32x32x16_bf16 v[96:111], v[200:203], v[80:83], 0\n\t"
        "v_exp_f32 v118, v118\n\t"
        "v_exp_f32 v119, v119\n\t"
        "v_exp_f32 v120, v120\n\t"
        "v_mfma_f32_32x32x16_bf16 v[96:111], v[204:207], v[84:87], v[96:111]\n\t"
        "v_exp_f32 v121, v121\n\t"
        "v_exp_f32 v122, v122\n\t"
        "v_exp_f32 v123, v123\n\t"
        "v_smfmac_f32_32x32x32_bf16 v[16:31], v[208:211], v[112:119], v148\n\t"
        "v_exp_f32 v124, v124\n\t"
        "v_exp_f32 v125, v125\n\t"
        "v_exp_f32 v126, v126\n\t"
        "v_smfmac_f32_16x16x64_bf16 v[132:135], v[144:147], v[112:119], v148\n\t"
        "buffer_load_dwordx4 v[192:195], v149, s[40:43], s44 offen offset:2048 sc1\n\t"
        "v_exp_f32 v127, v127\n\t"
        "v_exp_f32 v96, v96\n\t"
        "v_exp_f32 v97, v97\n\t"
        "v_smfmac_f32_32x32x32_bf16 v[16:31], v[212:215], v[120:127], v148\n\t"
        "v_exp_f32 v98, v98\n\t"
        "v_exp_f32 v99, v99\n\t"
        "v_exp_f32 v100, v100\n\t"
        "v_smfmac_f32_16x16x64_bf16 v[132:135], v[144:147], v[120:127], v148\n\t"
        "v_exp_f32 v101, v101\n\t"
        "v_mfma_f32_32x32x16_bf16 v[112:127], v[200:203], v[88:91], 0\n\t"
        "v_exp_f32 v102, v102\n\t"
        "v_exp_f32 v103, v103\n\t"
        "v_exp_f32 v104, v104\n\t"
        "v_mfma_f32_32x32x16_bf16 v[112:127], v[204:207], v[92:95], v[112:127]\n\t"
        "v_exp_f32 v105, v105\n\t"
        "v_exp_f32 v106, v106\n\t"
        "v_exp_f32 v107, v107\n\t"
        "v_smfmac_f32_32x32x32_bf16 v[32:47], v[208:211], v[96:103], v148\n\t"
        "v_exp_f32 v108, v108\n\t"
        "v_exp_f32 v109, v109\n\t"
        "v_exp_f32 v110, v110\n\t"
        "v_smfmac_f32_16x16x64_bf16 v[136:139], v[144:147], v[96:103], v148\n\t"
        "buffer_load_dwordx4 v[196:199], v149, s[40:43], s44 offen offset:3072 sc1\n\t"
        "v_exp_f32 v111, v111\n\t"
        "v_exp_f32 v112, v112\n\t"
        "v_exp_f32 v113, v113\n\t"
        "v_smfmac_f32_32x32x32_bf16 v[32:47], v[212:215], v[104:111], v148\n\t"
        "v_exp_f32 v114, v114\n\t"
        "v_exp_f32 v115, v115\n\t"
        "v_exp_f32 v116, v116\n\t"
        "v_smfmac_f32_16x16x64_bf16 v[136:139], v[144:147], v[104:111], v148\n\t"
        "v_exp_f32 v117, v117\n\t"
        "s_waitcnt vmcnt(8)\n\t"
        "v_mfma_f32_32x32x16_bf16 v[96:111], v[152:155], v[64:67], 0\n\t"
        "v_exp_f32 v118, v118\n\t"
        "v_exp_f32 v119, v119\n\t"
        "v_exp_f32 v120, v120\n\t"
        "v_mfma_f32_32x32x16_bf16 v[96:111], v[156:159], v[68:71], v[96:111]\n\t"
        "v_exp_f32 v121, v121\n\t"
        "v_exp_f32 v122, v122\n\t"
        "v_exp_f32 v123, v123\n\t"
        "v_smfmac_f32_32x32x32_bf16 v[48:63], v[208:211], v[112:119], v148\n\t"
        "v_exp_f32 v124, v124\n\t"
        "v_exp_f32 v125, v125\n\t"
        "v_exp_f32 v126, v126\n\t"
        "v_smfmac_f32_16x16x64_bf16 v[140:143], v[144:147], v[112:119], v148\n\t"
        "buffer_load_dwordx4 v[200:203], v150, s[40:43], s44 offen offset:0 sc1\n\t"
        "v_exp_f32 v127, v127\n\t"
        "v_exp_f32 v96, v96\n\t"
        "v_exp_f32 v97, v97\n\t"
        "v_smfmac_f32_32x32x32_bf16 v[48:63], v[212:215], v[120:127], v148\n\t"
        "v_exp_f32 v98, v98\n\t"
        "v_exp_f32 v99, v99\n\t"
        "v_exp_f32 v100, v100\n\t"
        "v_smfmac_f32_16x16x64_bf16 v[140:143], v[144:147], v[120:127], v148\n\t"
        "v_exp_f32 v101, v101\n\t"
        "v_mfma_f32_32x32x16_bf16 v[112:127], v[152:155], v[72:75], 0\n\t"
        "v_exp_f32 v102, v102\n\t"
        "v_exp_f32 v103, v103\n\t"
        "v_exp_f32 v104, v104\n\t"
        "v_mfma_f32_32x32x16_bf16 v[112:127], v[156:159], v[76:79], v[112:127]\n\t"
        "v_exp_f32 v105, v105\n\t"
        "v_exp_f32 v106, v106\n\t"
        "v_exp_f32 v107, v107\n\t"
        "v_smfmac_f32_32x32x32_bf16 v[0:15], v[160:163], v[96:103], v148\n\t"
        "v_exp_f32 v108, v108\n\t"
        "v_exp_f32 v109, v109\n\t"
        "v_exp_f32 v110, v110\n\t"
        "v_smfmac_f32_16x16x64_bf16 v[128:131], v[144:147], v[96:103], v148\n\t"
        "buffer_load_dwordx4 v[204:207], v150, s[40:43], s44 offen offset:1024 sc1\n\t"
        "v_exp_f32 v111, v111\n\t"
        "v_exp_f32 v112, v112\n\t"
        "v_exp_f32 v113, v113\n\t"
        "v_smfmac_f32_32x32x32_bf16 v[0:15], v[164:167], v[104:111], v148\n\t"
        "v_exp_f32 v114, v114\n\t"
        "v_exp_f32 v115, v115\n\t"
        "v_exp_f32 v116, v116\n\t"
        "v_smfmac_f32_16x16x64_bf16 v[128:131], v[144:147], v[104:111], v148\n\t"
        "v_exp_f32 v117, v117\n\t"
        "v_mfma_f32_32x32x16_bf16 v[96:111], v[152:155], v[80:83], 0\n\t"
        "v_exp_f32 v118, v118\n\t"
        "v_exp_f32 v119, v119\n\t"
        "v_exp_f32 v120, v120\n\t"
        "v_mfma_f32_32x32x16_bf16 v[96:111], v[156:159], v[84:87], v[96:111]\n\t"
        "v_exp_f32 v121, v121\n\t"
        "v_exp_f32 v122, v122\n\t"
        "v_exp_f32 v123, v123\n\t"
        "v_smfmac_f32_32x32x32_bf16 v[16:31], v[160:163], v[112:119], v148\n\t"
        "v_exp_f32 v124, v124\n\t"
        "v_exp_f32 v125, v125\n\t"
        "v_exp_f32 v126, v126\n\t"
        "v_smfmac_f32_16x16x64_bf16 v[132:135], v[144:147], v[112:119], v148\n\t"
        "buffer_load_dwordx4 v[208:211], v150, s[40:43], s44 offen offset:2048 sc1\n\t"
        "v_exp_f32 v127, v127\n\t"
        "v_exp_f32 v96, v96\n\t"
        "v_exp_f32 v97, v97\n\t"
        "v_smfmac_f32_32x32x32_bf16 v[16:31], v[164:167], v[120:127], v148\n\t"
        "v_exp_f32 v98, v98\n\t"
        "v_exp_f32 v99, v99\n\t"
        "v_exp_f32 v100, v100\n\t"
        "v_smfmac_f32_16x16x64_bf16 v[132:135], v[144:147], v[120:127], v148\n\t"
        "v_exp_f32 v101, v101\n\t"
        "v_mfma_f32_32x32x16_bf16 v[112:127], v[152:155], v[88:91], 0\n\t"
        "v_exp_f32 v102, v102\n\t"
        "v_exp_f32 v103, v103\n\t"
        "v_exp_f32 v104, v104\n\t"
        "v_mfma_f32_32x32x16_bf16 v[112:127], v[156:159], v[92:95], v[112:127]\n\t"
        "v_exp_f32 v105, v105\n\t"
        "v_exp_f32 v106, v106\n\t"
        "v_exp_f32 v107, v107\n\t"
        "v_smfmac_f32_32x32x32_bf16 v[32:47], v[160:163], v[96:103], v148\n\t"
        "v_exp_f32 v108, v108\n\t"
        "v_exp_f32 v109, v109\n\t"
        "v_exp_f32 v110, v110\n\t"
        "v_smfmac_f32_16x16x64_bf16 v[136:139], v[144:147], v[96:103], v148\n\t"
        "buffer_load_dwordx4 v[212:215], v150, s[40:43], s44 offen offset:3072 sc1\n\t"
        "v_exp_f32 v111, v111\n\t"
        "v_exp_f32 v112, v112\n\t"
        "v_exp_f32 v113, v113\n\t"
        "v_smfmac_f32_32x32x32_bf16 v[32:47], v[164:167], v[104:111], v148\n\t"
        "s_add_u32 s44, s44, 0x4000\n\t"
        "s_and_b32 s44, s44, 0x3ffff\n\t"
        "v_exp_f32 v114, v114\n\t"
        "v_exp_f32 v115, v115\n\t"
        "v_exp_f32 v116, v116\n\t"
        "v_smfmac_f32_16x16x64_bf16 v[136:139], v[144:147], v[104:111], v148\n\t"
        "v_exp_f32 v117, v117\n\t"
        "s_waitcnt vmcnt(8)\n\t"
        "v_mfma_f32_32x32x16_bf16 v[96:111], v[168:171], v[64:67], 0\n\t"
        "v_exp_f32 v118, v118\n\t"
        "v_exp_f32 v119, v119\n\t"
        "v_exp_f32 v120, v120\n\t"
        "v_mfma_f32_32x32x16_bf16 v[96:111], v[172:175], v[68:71], v[96:111]\n\t"
        "v_exp_f32 v121, v121\n\t"
        "v_exp_f32 v122, v122\n\t"
        "v_exp_f32 v123, v123\n\t"
        "v_smfmac_f32_32x32x32_bf16 v[48:63], v[160:163], v[112:119], v148\n\t"
        "v_exp_f32 v124, v124\n\t"
        "v_exp_f32 v125, v125\n\t"
        "v_exp_f32 v126, v126\n\t"
        "v_smfmac_f32_16x16x64_bf16 v[140:143], v[144:147], v[112:119], v148\n\t"
        "buffer_load_dwordx4 v[152:155], v149, s[40:43], s44 offen offset:0 sc1\n\t"
        "v_exp_f32 v127, v127\n\t"
        "v_exp_f32 v96, v96\n\t"
        "v_exp_f32 v97, v97\n\t"
        "v_smfmac_f32_32x32x32_bf16 v[48:63], v[164:167], v[120:127], v148\n\t"
        "v_exp_f32 v98, v98\n\t"
        "v_exp_f32 v99, v99\n\t"
        "v_exp_f32 v100, v100\n\t"
        "v_smfmac_f32_16x16x64_bf16 v[140:143], v[144:147], v[120:127], v148\n\t"
        "v_exp_f32 v101, v101\n\t"
        "v_mfma_f32_32x32x16_bf16 v[112:127], v[168:171], v[72:75], 0\n\t"
        "v_exp_f32 v102, v102\n\t"
        "v_exp_f32 v103, v103\n\t"
        "v_exp_f32 v104, v104\n\t"
        "v_mfma_f32_32x32x16_bf16 v[112:127], v[172:175], v[76:79], v[112:127]\n\t"
        "v_exp_f32 v105, v105\n\t"
        "v_exp_f32 v106, v106\n\t"
        "v_exp_f32 v107, v107\n\t"
        "v_smfmac_f32_32x32x32_bf16 v[0:15], v[176:179], v[96:103], v148\n\t"
        "v_exp_f32 v108, v108\n\t"
        "v_exp_f32 v109, v109\n\t"
        "v_exp_f32 v110, v110\n\t"
        "v_smfmac_f32_16x16x64_bf16 v[128:131], v[144:147], v[96:103], v148\n\t"
        "buffer_load_dwordx4 v[156:159], v149, s[40:43], s44 offen offset:1024 sc1\n\t"
        "v_exp_f32 v111, v111\n\t"
        "v_exp_f32 v112, v112\n\t"
        "v_exp_f32 v113, v113\n\t"
        "v_smfmac_f32_32x32x32_bf16 v[0:15], v[180:183], v[104:111], v148\n\t"
        "v_exp_f32 v114, v114\n\t"
        "v_exp_f32 v115, v115\n\t"
        "v_exp_f32 v116, v116\n\t"
        "v_smfmac_f32_16x16x64_bf16 v[128:131], v[144:147], v[104:111], v148\n\t"
        "v_exp_f32 v117, v117\n\t"
        "v_mfma_f32_32x32x16_bf16 v[96:111], v[168:171], v[80:83], 0\n\t"
        "v_exp_f32 v118, v118\n\t"
        "v_exp_f32 v119, v119\n\t"
        "v_exp_f32 v120, v120\n\t"
        "v_mfma_f32_32x32x16_bf16 v[96:111], v[172:175], v[84:87], v[96:111]\n\t"
        "v_exp_f32 v121, v121\n\t"
        "v_exp_f32 v122, v122\n\t"
        "v_exp_f32 v123, v123\n\t"
        "v_smfmac_f32_32x32x32_bf16 v[16:31], v[176:179], v[112:119], v148\n\t"
        "v_exp_f32 v124, v124\n\t"
        "v_exp_f32 v125, v125\n\t"
        "v_exp_f32 v126, v126\n\t"
        "v_smfmac_f32_16x16x64_bf16 v[132:135], v[144:147], v[112:119], v148\n\t"
        "buffer_load_dwordx4 v[160:163], v149, s[40:43], s44 offen offset:2048 sc1\n\t"
        "v_exp_f32 v127, v127\n\t"
        "v_exp_f32 v96, v96\n\t"
        "v_exp_f32 v97, v97\n\t"
        "v_smfmac_f32_32x32x32_bf16 v[16:31], v[180:183], v[120:127], v148\n\t"
        "v_exp_f32 v98, v98\n\t"
        "v_exp_f32 v99, v99\n\t"
        "v_exp_f32 v100, v100\n\t"
        "v_smfmac_f32_16x16x64_bf16 v[132:135], v[144:147], v[120:127], v148\n\t"
        "v_exp_f32 v101, v101\n\t"
        "v_mfma_f32_32x32x16_bf16 v[112:127], v[168:171], v[88:91], 0\n\t"
        "v_exp_f32 v102, v102\n\t"
        "v_exp_f32 v103, v103\n\t"
        "v_exp_f32 v104, v104\n\t"
        "v_mfma_f32_32x32x16_bf16 v[112:127], v[172:175], v[92:95], v[112:127]\n\t"
        "v_exp_f32 v105, v105\n\t"
        "v_exp_f32 v106, v106\n\t"
        "v_exp_f32 v107, v107\n\t"
        "v_smfmac_f32_32x32x32_bf16 v[32:47], v[176:179], v[96:103], v148\n\t"
        "v_exp_f32 v108, v108\n\t"
        "v_exp_f32 v109, v109\n\t"
        "v_exp_f32 v110, v110\n\t"
        "v_smfmac_f32_16x16x64_bf16 v[136:139], v[144:147], v[96:103], v148\n\t"
        "buffer_load_dwordx4 v[164:167], v149, s[40:43], s44 offen offset:3072 sc1\n\t"
        "v_exp_f32 v111, v111\n\t"
        "s_sub_u32 s45, s45, 1\n\t"
        "s_cmp_lg_u32 s45, 0\n\t"
        "s_cbranch_scc1 1b\n\t"
        "v_exp_f32 v112, v112\n\t"
        "v_exp_f32 v113, v113\n\t"
        "v_smfmac_f32_32x32x32_bf16 v[32:47], v[180:183], v[104:111], v148\n\t"
        "v_exp_f32 v114, v114\n\t"
        "v_exp_f32 v115, v115\n\t"
        "v_exp_f32 v116, v116\n\t"
        "v_smfmac_f32_16x16x64_bf16 v[136:139], v[144:147], v[104:111], v148\n\t"
        "v_exp_f32 v117, v117\n\t"
        "s_waitcnt vmcnt(8)\n\t"
        "v_mfma_f32_32x32x16_bf16 v[96:111], v[184:187], v[64:67], 0\n\t"
        "v_exp_f32 v118, v118\n\t"
        "v_exp_f32 v119, v119\n\t"
        "v_exp_f32 v120, v120\n\t"
        "v_mfma_f32_32x32x16_bf16 v[96:111], v[188:191], v[68:71], v[96:111]\n\t"
        "v_exp_f32 v121, v121\n\t"
        "v_exp_f32 v122, v122\n\t"
        "v_exp_f32 v123, v123\n\t"
        "v_smfmac_f32_32x32x32_bf16 v[48:63], v[176:179], v[112:119], v148\n\t"
        "v_exp_f32 v124, v124\n\t"
        "v_exp_f32 v125, v125\n\t"
        "v_exp_f32 v126, v126\n\t"
        "v_smfmac_f32_16x16x64_bf16 v[140:143], v[144:147], v[112:119], v148\n\t"
        "v_exp_f32 v127, v127\n\t"
        "v_exp_f32 v96, v96\n\t"
        "v_exp_f32 v97, v97\n\t"
        "v_smfmac_f32_32x32x32_bf16 v[48:63], v[180:183], v[120:127], v148\n\t"
        "v_exp_f32 v98, v98\n\t"
        "v_exp_f32 v99, v99\n\t"
        "v_exp_f32 v100, v100\n\t"
        "v_smfmac_f32_16x16x64_bf16 v[140:143], v[144:147], v[120:127], v148\n\t"
        "v_exp_f32 v101, v101\n\t"
        "v_mfma_f32_32x32x16_bf16 v[112:127], v[184:187], v[72:75], 0\n\t"
        "v_exp_f32 v102, v102\n\t"
        "v_exp_f32 v103, v103\n\t"
        "v_exp_f32 v104, v104\n\t"
        "v_mfma_f32_32x32x16_bf16 v[112:127], v[188:191], v[76:79], v[112:127]\n\t"
        "v_exp_f32 v105, v105\n\t"
        "v_exp_f32 v106, v106\n\t"
        "v_exp_f32 v107, v107\n\t"
        "v_smfmac_f32_32x32x32_bf16 v[0:15], v[192:195], v[96:103], v148\n\t"
        "v_exp_f32 v108, v108\n\t"
        "v_exp_f32 v109, v109\n\t"
        "v_exp_f32 v110, v110\n\t"
        "v_smfmac_f32_16x16x64_bf16 v[128:131], v[144:147], v[96:103], v148\n\t"
        "v_exp_f32 v111, v111\n\t"
        "v_exp_f32 v112, v112\n\t"
        "v_exp_f32 v113, v113\n\t"
        "v_smfmac_f32_32x32x32_bf16 v[0:15], v[196:199], v[104:111], v148\n\t"
        "v_exp_f32 v114, v114\n\t"
        "v_exp_f32 v115, v115\n\t"
        "v_exp_f32 v116, v116\n\t"
        "v_smfmac_f32_16x16x64_bf16 v[128:131], v[144:147], v[104:111], v148\n\t"
        "v_exp_f32 v117, v117\n\t"
        "s_waitcnt vmcnt(4)\n\t"
        "v_mfma_f32_32x32x16_bf16 v[96:111], v[200:203], v[64:67], 0\n\t"
        "v_exp_f32 v118, v118\n\t"
        "v_exp_f32 v119, v119\n\t"
        "v_exp_f32 v120, v120\n\t"
        "v_mfma_f32_32x32x16_bf16 v[96:111], v[204:207], v[68:71], v[96:111]\n\t"
        "v_exp_f32 v121, v121\n\t"
        "v_exp_f32 v122, v122\n\t"
        "v_exp_f32 v123, v123\n\t"
        "v_smfmac_f32_32x32x32_bf16 v[16:31], v[192:195], v[112:119], v148\n\t"
        "v_exp_f32 v124, v124\n\t"
        "v_exp_f32 v125, v125\n\t"
        "v_exp_f32 v126, v126\n\t"
        "v_smfmac_f32_16x16x64_bf16 v[132:135], v[144:147], v[112:119], v148\n\t"
        "v_exp_f32 v127, v127\n\t"
        "s_cmp_lg_u32 s47, 0\n\t"
        "s_cbranch_scc1 4f\n\t"
        "global_load_dwordx4 %[r0], v223, %[xb] offset:0\n\t"
        "global_load_dwordx4 %[r1], v223, %[xb] offset:32\n\t"
        "global_load_dwordx4 %[r2], v223, %[xb] offset:64\n\t"
        "global_load_dwordx4 %[r3], v223, %[xb] offset:96\n\t"
        "4:\n\t"
        "v_exp_f32 v96, v96\n\t"
        "v_exp_f32 v97, v97\n\t"
        "v_smfmac_f32_32x32x32_bf16 v[16:31], v[196:199], v[120:127], v148\n\t"
        "v_exp_f32 v98, v98\n\t"
        "v_exp_f32 v99, v99\n\t"
        "v_exp_f32 v100, v100\n\t"
        "v_smfmac_f32_16x16x64_bf16 v[132:135], v[144:147], v[120:127], v148\n\t"
        "v_exp_f32 v101, v101\n\t"
        "v_mfma_f32_32x32x16_bf16 v[112:127], v[200:203], v[72:75], 0\n\t"
        "v_exp_f32 v102, v102\n\t"
        "v_exp_f32 v103, v103\n\t"
        "v_exp_f32 v104, v104\n\t"
        "v_mfma_f32_32x32x16_bf16 v[112:127], v[204:207], v[76:79], v[112:127]\n\t"
        "v_exp_f32 v105, v105\n\t"
        "v_exp_f32 v106, v106\n\t"
        "v_exp_f32 v107, v107\n\t"
        "v_smfmac_f32_32x32x32_bf16 v[0:15], v[208:211], v[96:103], v148\n\t"
        "v_exp_f32 v108, v108\n\t"
        "v_exp_f32 v109, v109\n\t"
        "v_exp_f32 v110, v110\n\t"
        "v_smfmac_f32_16x16x64_bf16 v[128:131], v[144:147], v[96:103], v148\n\t"
        "v_exp_f32 v111, v111\n\t"
        "v_exp_f32 v112, v112\n\t"
        "v_exp_f32 v113, v113\n\t"
        "v_smfmac_f32_32x32x32_bf16 v[0:15], v[212:215], v[104:111], v148\n\t"
        "v_exp_f32 v114, v114\n\t"
        "v_exp_f32 v115, v115\n\t"
        "v_exp_f32 v116, v116\n\t"
        "v_smfmac_f32_16x16x64_bf16 v[128:131], v[144:147], v[104:111], v148\n\t"
        "v_exp_f32 v117, v117\n\t"
        "v_mfma_f32_32x32x16_bf16 v[96:111], v[184:187], v[80:83], 0\n\t"
        "v_exp_f32 v118, v118\n\t"
        "v_exp_f32 v119, v119\n\t"
        "v_exp_f32 v120, v120\n\t"
        "ds_write_b128 v216, v[0:3] offset:0\n\t"
        "v_mfma_f32_32x32x16_bf16 v[96:111], v[188:191], v[84:87], v[96:111]\n\t"
        "v_exp_f32 v121, v121\n\t"
        "ds_write_b128 v216, v[4:7] offset:1024\n\t"
        "v_exp_f32 v122, v122\n\t"
        "ds_write_b128 v216, v[8:11] offset:2048\n\t"
        "v_exp_f32 v123, v123\n\t"
        "ds_write_b128 v216, v[12:15] offset:3072\n\t"
        "v_smfmac_f32_32x32x32_bf16 v[16:31], v[208:211], v[112:119], v148\n\t"
        "v_exp_f32 v124, v124\n\t"
        "ds_write_b32 v218, v128 offset:0\n\t"
        "v_exp_f32 v125, v125\n\t"
        "v_exp_f32 v126, v126\n\t"
        "v_smfmac_f32_16x16x64_bf16 v[132:135], v[144:147], v[112:119], v148\n\t"
        "v_exp_f32 v127, v127\n\t"
        "v_exp_f32 v96, v96\n\t"
        "v_exp_f32 v97, v97\n\t"
        "v_smfmac_f32_32x32x32_bf16 v[16:31], v[212:215], v[120:127], v148\n\t"
        "v_exp_f32 v98, v98\n\t"
        "v_exp_f32 v99, v99\n\t"
        "v_exp_f32 v100, v100\n\t"
        "v_smfmac_f32_16x16x64_bf16 v[132:135], v[144:147], v[120:127], v148\n\t"
        "v_exp_f32 v101, v101\n\t"
        "v_mfma_f32_32x32x16_bf16 v[112:127], v[184:187], v[88:91], 0\n\t"
        "v_exp_f32 v102, v102\n\t"
        "v_exp_f32 v103, v103\n\t"
        "v_exp_f32 v104, v104\n\t"
        "ds_write_b128 v216, v[16:19] offset:4096\n\t"
        "v_mfma_f32_32x32x16_bf16 v[112:127], v[188:191], v[92:95], v[112:127]\n\t"
        "v_exp_f32 v105, v105\n\t"
        "ds_write_b128 v216, v[20:23] offset:5120\n\t"
        "v_exp_f32 v106, v106\n\t"
        "ds_write_b128 v216, v[24:27] offset:6144\n\t"
        "v_exp_f32 v107, v107\n\t"
        "ds_write_b128 v216, v[28:31] offset:7168\n\t"
        "v_smfmac_f32_32x32x32_bf16 v[32:47], v[192:195], v[96:103], v148\n\t"
        "v_exp_f32 v108, v108\n\t"
        "ds_write_b32 v218, v132 offset:256\n\t"
        "v_exp_f32 v109, v109\n\t"
        "ds_write_b32 v220, v222\n\t"
        "v_exp_f32 v110, v110\n\t"
        "v_smfmac_f32_16x16x64_bf16 v[136:139], v[144:147], v[96:103], v148\n\t"
        "v_exp_f32 v111, v111\n\t"
        "v_exp_f32 v112, v112\n\t"
        "v_exp_f32 v113, v113\n\t"
        "v_smfmac_f32_32x32x32_bf16 v[32:47], v[196:199], v[104:111], v148\n\t"
        "v_exp_f32 v114, v114\n\t"
        "v_exp_f32 v115, v115\n\t"
        "v_exp_f32 v116, v116\n\t"
        "v_smfmac_f32_16x16x64_bf16 v[136:139], v[144:147], v[104:111], v148\n\t"
        "v_exp_f32 v117, v117\n\t"
        "v_mfma_f32_32x32x16_bf16 v[96:111], v[200:203], v[80:83], 0\n\t"
        "v_exp_f32 v118, v118\n\t"
        "v_exp_f32 v119, v119\n\t"
        "v_exp_f32 v120, v120\n\t"
        "v_mfma_f32_32x32x16_bf16 v[96:111], v[204:207], v[84:87], v[96:111]\n\t"
        "v_exp_f32 v121, v121\n\t"
        "v_exp_f32 v122, v122\n\t"
        "v_exp_f32 v123, v123\n\t"
        "v_smfmac_f32_32x32x32_bf16 v[48:63], v[192:195], v[112:119], v148\n\t"
        "v_exp_f32 v124, v124\n\t"
        "v_exp_f32 v125, v125\n\t"
        "v_exp_f32 v126, v126\n\t"
        "v_smfmac_f32_16x16x64_bf16 v[140:143], v[144:147], v[112:119], v148\n\t"
        "v_exp_f32 v127, v127\n\t"
        "v_exp_f32 v96, v96\n\t"
        "v_exp_f32 v97, v97\n\t"
        "v_smfmac_f32_32x32x32_bf16 v[48:63], v[196:199], v[120:127], v148\n\t"
        "v_exp_f32 v98, v98\n\t"
        "v_exp_f32 v99, v99\n\t"
        "v_exp_f32 v100, v100\n\t"
        "v_smfmac_f32_16x16x64_bf16 v[140:143], v[144:147], v[120:127], v148\n\t"
        "v_exp_f32 v101, v101\n\t"
        "v_mfma_f32_32x32x16_bf16 v[112:127], v[200:203], v[88:91], 0\n\t"
        "v_exp_f32 v102, v102\n\t"
        "v_exp_f32 v103, v103\n\t"
        "v_exp_f32 v104, v104\n\t"
        "ds_read_b32 v151, v221\n\t"
        "v_mfma_f32_32x32x16_bf16 v[112:127], v[204:207], v[92:95], v[112:127]\n\t"
        "v_exp_f32 v105, v105\n\t"
        "v_exp_f32 v106, v106\n\t"
        "v_exp_f32 v107, v107\n\t"
        "v_smfmac_f32_32x32x32_bf16 v[32:47], v[208:211], v[96:103], v148\n\t"
        "v_exp_f32 v108, v108\n\t"
        "v_exp_f32 v109, v109\n\t"
        "v_exp_f32 v110, v110\n\t"
        "v_smfmac_f32_16x16x64_bf16 v[136:139], v[144:147], v[96:103], v148\n\t"
        "v_exp_f32 v111, v111\n\t"
        "s_waitcnt lgkmcnt(0)\n\t"
        "v_cmp_ne_u32 vcc, v222, v151\n\t"
        "s_cbranch_vccnz 40f\n\t"
        "41:\n\t"
        "v_exp_f32 v112, v112\n\t"
        "ds_read_b128 v[0:3], v217 offset:0\n\t"
        "v_exp_f32 v113, v113\n\t"
        "ds_read_b128 v[4:7], v217 offset:1024\n\t"
        "v_smfmac_f32_32x32x32_bf16 v[32:47], v[212:215], v[104:111], v148\n\t"
        "v_exp_f32 v114, v114\n\t"
        "ds_read_b128 v[8:11], v217 offset:2048\n\t"
        "v_exp_f32 v115, v115\n\t"
        "ds_read_b128 v[12:15], v217 offset:3072\n\t"
        "v_exp_f32 v116, v116\n\t"
        "ds_read_b32 v128, v219 offset:0\n\t"
        "v_smfmac_f32_16x16x64_bf16 v[136:139], v[144:147], v[104:111], v148\n\t"
        "v_exp_f32 v117, v117\n\t"
        "ds_read_b128 v[16:19], v217 offset:4096\n\t"
        "v_exp_f32 v118, v118\n\t"
        "ds_read_b128 v[20:23], v217 offset:5120\n\t"
        "v_exp_f32 v119, v119\n\t"
        "ds_read_b128 v[24:27], v217 offset:6144\n\t"
        "v_exp_f32 v120, v120\n\t"
        "ds_read_b128 v[28:31], v217 offset:7168\n\t"
        "v_exp_f32 v121, v121\n\t"
        "ds_read_b32 v132, v219 offset:256\n\t"
        "v_exp_f32 v122, v122\n\t"
        "v_exp_f32 v123, v123\n\t"
        "v_smfmac_f32_32x32x32_bf16 v[48:63], v[208:211], v[112:119], v148\n\t"
        "v_exp_f32 v124, v124\n\t"
        "v_exp_f32 v125, v125\n\t"
        "v_exp_f32 v126, v126\n\t"
        "v_smfmac_f32_16x16x64_bf16 v[140:143], v[144:147], v[112:119], v148\n\t"
        "v_exp_f32 v127, v127\n\t"
        "s_nop 1\n\t"
        "v_smfmac_f32_32x32x32_bf16 v[48:63], v[212:215], v[120:127], v148\n\t"
        "v_smfmac_f32_16x16x64_bf16 v[140:143], v[144:147], v[120:127], v148\n\t"
        "s_waitcnt vmcnt(0) lgkmcnt(0)\n\t"
        "s_nop 6\n\t"
        "s_branch 9f\n\t"
        "30:\n\t"
        "s_add_u32 s44, s44, 0x4000\n\t"
        "s_and_b32 s44, s44, 0x3ffff\n\t"
        "buffer_load_dwordx4 v[184:187], v149, s[40:43], s44 offen offset:0 sc1\n\t"
        "buffer_load_dwordx4 v[188:191], v149, s[40:43], s44 offen offset:1024 sc1\n\t"
        "buffer_load_dwordx4 v[192:195], v149, s[40:43], s44 offen offset:2048 sc1\n\t"
        "buffer_load_dwordx4 v[196:199], v149, s[40:43], s44 offen offset:3072 sc1\n\t"
        "buffer_load_dwordx4 v[200:203], v150, s[40:43], s44 offen offset:0 sc1\n\t"
        "buffer_load_dwordx4 v[204:207], v150, s[40:43], s44 offen offset:1024 sc1\n\t"
        "buffer_load_dwordx4 v[208:211], v150, s[40:43], s44 offen offset:2048 sc1\n\t"
        "buffer_load_dwordx4 v[212:215], v150, s[40:43], s44 offen offset:3072 sc1\n\t"
        "s_add_u32 s44, s44, 0x4000\n\t"
        "s_and_b32 s44, s44, 0x3ffff\n\t"
        "buffer_load_dwordx4 v[152:155], v149, s[40:43], s44 offen offset:0 sc1\n\t"
        "buffer_load_dwordx4 v[156:159], v149, s[40:43], s44 offen offset:1024 sc1\n\t"
        "buffer_load_dwordx4 v[160:163], v149, s[40:43], s44 offen offset:2048 sc1\n\t"
        "buffer_load_dwordx4 v[164:167], v149, s[40:43], s44 offen offset:3072 sc1\n\t"
        "s_mov_b32 s46, 1\n\t"
        "s_branch 31b\n\t"
        "32:\n\t"
        "s_add_u32 s44, s44, 0x4000\n\t"
        "s_and_b32 s44, s44, 0x3ffff\n\t"
        "buffer_load_dwordx4 v[184:187], v149, s[40:43], s44 offen offset:0 sc1\n\t"
        "buffer_load_dwordx4 v[188:191], v149, s[40:43], s44 offen offset:1024 sc1\n\t"
        "buffer_load_dwordx4 v[192:195], v149, s[40:43], s44 offen offset:2048 sc1\n\t"
        "buffer_load_dwordx4 v[196:199], v149, s[40:43], s44 offen offset:3072 sc1\n\t"
        "buffer_load_dwordx4 v[200:203], v150, s[40:43], s44 offen offset:0 sc1\n\t"
        "buffer_load_dwordx4 v[204:207], v150, s[40:43], s44 offen offset:1024 sc1\n\t"
        "buffer_load_dwordx4 v[208:211], v150, s[40:43], s44 offen offset:2048 sc1\n\t"
        "buffer_load_dwordx4 v[212:215], v150, s[40:43], s44 offen offset:3072 sc1\n\t"
        "s_add_u32 s44, s44, 0x4000\n\t"
        "s_and_b32 s44, s44, 0x3ffff\n\t"
        "buffer_load_dwordx4 v[152:155], v149, s[40:43], s44 offen offset:0 sc1\n\t"
        "buffer_load_dwordx4 v[156:159], v149, s[40:43], s44 offen offset:1024 sc1\n\t"
        "buffer_load_dwordx4 v[160:163], v149, s[40:43], s44 offen offset:2048 sc1\n\t"
        "buffer_load_dwordx4 v[164:167], v149, s[40:43], s44 offen offset:3072 sc1\n\t"
        "s_mov_b32 s46, 1\n\t"
        "s_branch 33b\n\t"
        "40:\n\t"
        "s_mov_b32 s45, 0x100000\n\t"
        "42:\n\t"
        "ds_read_b32 v151, v221\n\t"
        "s_sub_u32 s45, s45, 1\n\t"
        "s_waitcnt lgkmcnt(0)\n\t"
        "s_cbranch_scc1 41b\n\t"
        "v_cmp_ne_u32 vcc, v222, v151\n\t"
        "s_cbranch_vccnz 42b\n\t"
        "s_branch 41b\n\t"
        "9:\n\t"
        : "+{v[0:15]}"(o0), "+{v[16:31]}"(o1), "+{v[32:47]}"(o2), "+{v[48:63]}"(o3), "+{v[128:143]}"(lv), "+{s44}"(soff), "+{v[152:167]}"(kva), "+{v[168:183]}"(kvb), "+{v[184:191]}"(fv),
          [r0] "+v"(x1p[0]), [r1] "+v"(x1p[1]), [r2] "+v"(x1p[2]), [r3] "+v"(x1p[3]) STAMP_OUTS
        : "{v[64:71]}"(q[0]), "{v[72:79]}"(q[1]), "{v[80:87]}"(q[2]), "{v[88:95]}"(q[3]), "{v[144:147]}"(ones), "{v148}"(0xddddu), "{v149}"(kvoff), "{v150}"(kvoff + 4096),
          "{v[216:223]}"(cv), "{v224}"(lbase + L_W + 16), "{s[40:43]}"(srd), "{s47}"(L), [xb] "s"(x0)
        : "v96", "v97", "v98", "v99", "v100", "v101", "v102", "v103", "v104", "v105", "v106", "v107", "v108", "v109", "v110", "v111", "v112", "v113", "v114", "v115", "v116", "v117", "v118", "v119", "v120", "v121", "v122", "v123", "v124", "v125", "v126", "v127", "v151", "v192", "v193", "v194", "v195", "v196", "v197", "v198", "v199", "v200", "v201", "v202", "v203", "v204", "v205", "v206", "v207", "v208", "v209", "v210", "v211", "v212", "v213", "v214", "v215", "s45", "s46", "vcc", "scc", "memory");
      STAMP_PUT(6 + 16 * L);
      STAMPV(9 + 16 * L);
      PHASE_VARS;
      const unsigned dst = OFF_OF + L * (5 * MBY) + (tm * 4 + 2 * kh) * TS + (tn * 2 + hh) * 2048 + lane * 16;
      auto put = [&](int u, const f32x16& o) {
#pragma unroll
        for (int c = 0; c < 2; ++c) {
          u32x4 w;
#pragma unroll
          for (int e = 0; e < 4; ++e) w[e] = pack2(o[8 * c + 2 * e], o[8 * c + 2 * e + 1]);
          stg(rs, dst + u * TS + c * 1024, w);
        }
      };
      const float la = lv[8] + lv[0], lb = lv[12] + lv[4];
      o2 = (o2 + o0) * RCP(la); put(0, o2);
      SBAR();
      o3 = (o3 + o1) * RCP(lb); put(1, o3);
      SBAR();
      {
        const f32x16 t = o2 + o3;
        float sum = 0.f;
#pragma unroll
        for (int e = 0; e < 16; e += 4) sum += (t[e] + t[e + 1]) + (t[e + 2] + t[e + 3]);
        if (__any(!(fabsf(sum) < 1e37f) | !(fmaxf(la, lb) < 1e27f) | !(fminf(la, lb) > 1e-9f) | !(la + lb == la + lb))) {
          DRAIN();
          attn_safe(rs, kvoff - kh * 8192, q[2], ones, o2); put(0, o2);
          attn_safe(rs, kvoff - kh * 8192, q[3], ones, o3); put(1, o3);
        }
      }
    }
    STAMPV(10 + 16 * L);
    {
      f32x16 acc[2];
      u32x4 res[4];
      u32x2 wrc[8];
      STAMP_DECL;
      { PHASE_VARS;
      BAR_ALL();
      STAMPV(11 + 16 * L);
#pragma unroll
      for (int g = 0; g < 8; ++g) wrc[g] = *reinterpret_cast<const u32x2*>(smem + L_WR + g * 16 + hi * 8);
      if (L == 0) {
#pragma unroll
        for (int i = 0; i < 4; ++i) res[i] = fo_swap(x1p[i]);
      } else {
        const bool routed = (int)words[3] == tile * 32 + r32;
        const float mb1 = reinterpret_cast<const float*>(smem + L_AB)[256 + wid * 64 + r32], rd1 = reinterpret_cast<const float*>(smem + L_AB)[256 + wid * 64 + 32 + r32];
#pragma unroll
        for (int i = 0; i < 4; ++i) {
          const u32x4 pr = *reinterpret_cast<const u32x4*>(smem + L_X2P + (i * 2 + hi) * 16);
#pragma unroll
          for (int e = 0; e < 4; ++e) {
            const unsigned h2 = ln_pair(x1p[i][e], mb1, rd1);
            unsigned t = pack2(lo16(x1p[i][e]) + lo16(h2), hi16(x1p[i][e]) + hi16(h2));
            PIN(t);
            res[i][e] = routed ? pr[e] : t;
          }
        }
      }
#pragma unroll
      for (int g = 0; g < 8; ++g) PIN(wrc[g]);
#pragma unroll
      for (int i = 0; i < 4; ++i) PIN(res[i]);
      unsigned soff = tn * 4096;
      const unsigned a0 = lbase + wrow * 128 + ((wch ^ wsw) << 4) + 8 * (tp & 1), a1 = lbase + wrow * 128 + (((4 + wch) ^ wsw) << 4) + 8 * (tp & 1);
      asm volatile(
        "ds_read_b64_tr_b16 v[32:33], v193 offset:0\n\t"
        "ds_read_b64_tr_b16 v[34:35], v193 offset:1024\n\t"
        "ds_read_b64_tr_b16 v[36:37], v194 offset:0\n\t"
        "ds_read_b64_tr_b16 v[38:39], v194 offset:1024\n\t"
        "ds_read_b64_tr_b16 v[40:41], v193 offset:2048\n\t"
        "ds_read_b64_tr_b16 v[42:43], v193 offset:3072\n\t"
        "ds_read_b64_tr_b16 v[44:45], v194 offset:2048\n\t"
        "ds_read_b64_tr_b16 v[46:47], v194 offset:3072\n\t"
        "ds_read_b64_tr_b16 v[48:49], v193 offset:4096\n\t"
        "ds_read_b64_tr_b16 v[50:51], v193 offset:5120\n\t"
        "ds_read_b64_tr_b16 v[52:53], v194 offset:4096\n\t"
        "ds_read_b64_tr_b16 v[54:55], v194 offset:5120\n\t"
        "ds_read_b64_tr_b16 v[56:57], v193 offset:6144\n\t"
        "ds_read_b64_tr_b16 v[58:59], v193 offset:7168\n\t"
        "ds_read_b64_tr_b16 v[60:61], v194 offset:6144\n\t"
        "ds_read_b64_tr_b16 v[62:63], v194 offset:7168\n\t"
        "s_mov_b32 s45, 0x200000\n\t"
        "2:\n\t"
        "ds_read_b32 v200, v198\n\t"
        "s_sub_u32 s45, s45, 1\n\t"
        "s_waitcnt lgkmcnt(0)\n\t"
        "s_cbranch_scc1 3f\n\t"
        "v_cmp_ne_u32 vcc, v199, v200\n\t"
        "s_cbranch_vccnz 2b\n\t"
        "3:\n\t"
        "buffer_load_dwordx4 v[64:67], v192, s[40:43], s44 offen offset:0 sc1\n\t"
        "buffer_load_dwordx4 v[68:71], v192, s[40:43], s44 offen offset:1024 sc1\n\t"
        "buffer_load_dwordx4 v[72:75], v192, s[40:43], s44 offen offset:2048 sc1\n\t"
        "buffer_load_dwordx4 v[76:79], v192, s[40:43], s44 offen offset:3072 sc1\n\t"
        "s_add_u32 s44, s44, 0x1000\n\t"
        "s_and_b32 s44, s44, 0xffff\n\t"
        "buffer_load_dwordx4 v[80:83], v192, s[40:43], s44 offen offset:0 sc1\n\t"
        "buffer_load_dwordx4 v[84:87], v192, s[40:43], s44 offen offset:1024 sc1\n\t"
        "buffer_load_dwordx4 v[88:91], v192, s[40:43], s44 offen offset:2048 sc1\n\t"
        "buffer_load_dwordx4 v[92:95], v192, s[40:43], s44 offen offset:3072 sc1\n\t"
        "s_add_u32 s44, s44, 0x1000\n\t"
        "s_and_b32 s44, s44, 0xffff\n\t"
        "buffer_load_dwordx4 v[96:99], v192, s[40:43], s44 offen offset:0 sc1\n\t"
        "buffer_load_dwordx4 v[100:103], v192, s[40:43], s44 offen offset:1024 sc1\n\t"
        "buffer_load_dwordx4 v[104:107], v192, s[40:43], s44 offen offset:2048 sc1\n\t"
        "buffer_load_dwordx4 v[108:111], v192, s[40:43], s44 offen offset:3072 sc1\n\t"
        "s_waitcnt vmcnt(11) lgkmcnt(14)\n\t"
        "v_mfma_f32_32x32x16_bf16 v[0:15], v[32:35], v[64:67], 0\n\t"
        "ds_read_b64_tr_b16 v[32:33], v193 offset:8192\n\t"
        "ds_read_b64_tr_b16 v[34:35], v193 offset:9216\n\t"
        "s_waitcnt lgkmcnt(14)\n\t"
        "v_mfma_f32_32x32x16_bf16 v[16:31], v[36:39], v[64:67], 0\n\t"
        "ds_read_b64_tr_b16 v[36:37], v194 offset:8192\n\t"
        "ds_read_b64_tr_b16 v[38:39], v194 offset:9216\n\t"
        "s_add_u32 s44, s44, 0x1000\n\t"
        "s_and_b32 s44, s44, 0xffff\n\t"
        "buffer_load_dwordx4 v[112:115], v192, s[40:43], s44 offen offset:0 sc1\n\t"
        "s_waitcnt vmcnt(11) lgkmcnt(14)\n\t"
        "v_mfma_f32_32x32x16_bf16 v[0:15], v[40:43], v[68:71], v[0:15]\n\t"
        "ds_read_b64_tr_b16 v[40:41], v193 offset:10240\n\t"
        "ds_read_b64_tr_b16 v[42:43], v193 offset:11264\n\t"
        "s_waitcnt lgkmcnt(14)\n\t"
        "v_mfma_f32_32x32x16_bf16 v[16:31], v[44:47], v[68:71], v[16:31]\n\t"
        "ds_read_b64_tr_b16 v[44:45], v194 offset:10240\n\t"
        "ds_read_b64_tr_b16 v[46:47], v194 offset:11264\n\t"
        "buffer_load_dwordx4 v[116:119], v192, s[40:43], s44 offen offset:1024 sc1\n\t"
        "s_waitcnt vmcnt(11) lgkmcnt(14)\n\t"
        "v_mfma_f32_32x32x16_bf16 v[0:15], v[48:51], v[72:75], v[0:15]\n\t"
        "ds_read_b64_tr_b16 v[48:49], v193 offset:12288\n\t"
        "ds_read_b64_tr_b16 v[50:51], v193 offset:13312\n\t"
        "s_waitcnt lgkmcnt(14)\n\t"
        "v_mfma_f32_32x32x16_bf16 v[16:31], v[52:55], v[72:75], v[16:31]\n\t"
        "ds_read_b64_tr_b16 v[52:53], v194 offset:12288\n\t"
        "ds_read_b64_tr_b16 v[54:55], v194 offset:13312\n\t"
        "buffer_load_dwordx4 v[120:123], v192, s[40:43], s44 offen offset:2048 sc1\n\t"
        "s_waitcnt vmcnt(11) lgkmcnt(14)\n\t"
        "v_mfma_f32_32x32x16_bf16 v[0:15], v[56:59], v[76:79], v[0:15]\n\t"
        "ds_read_b64_tr_b16 v[56:57], v193 offset:14336\n\t"
        "ds_read_b64_tr_b16 v[58:59], v193 offset:15360\n\t"
        "s_waitcnt lgkmcnt(14)\n\t"
        "v_mfma_f32_32x32x16_bf16 v[16:31], v[60:63], v[76:79], v[16:31]\n\t"
        "ds_read_b64_tr_b16 v[60:61], v194 offset:14336\n\t"
        "ds_read_b64_tr_b16 v[62:63], v194 offset:15360\n\t"
        "buffer_load_dwordx4 v[124:127], v192, s[40:43], s44 offen offset:3072 sc1\n\t"
        "s_waitcnt vmcnt(11) lgkmcnt(14)\n\t"
        "v_mfma_f32_32x32x16_bf16 v[0:15], v[32:35], v[80:83], v[0:15]\n\t"
        "ds_read_b64_tr_b16 v[32:33], v193 offset:16384\n\t"
        "ds_read_b64_tr_b16 v[34:35], v193 offset:17408\n\t"
        "s_waitcnt lgkmcnt(14)\n\t"
        "v_mfma_f32_32x32x16_bf16 v[16:31], v[36:39], v[80:83], v[16:31]\n\t"
        "ds_read_b64_tr_b16 v[36:37], v194 offset:16384\n\t"
        "ds_read_b64_tr_b16 v[38:39], v194 offset:17408\n\t"
        "s_add_u32 s44, s44, 0x1000\n\t"
        "s_and_b32 s44, s44, 0xffff\n\t"
        "buffer_load_dwordx4 v[128:131], v192, s[40:43], s44 offen offset:0 sc1\n\t"
        "s_waitcnt vmcnt(11) lgkmcnt(14)\n\t"
        "v_mfma_f32_32x32x16_bf16 v[0:15], v[40:43], v[84:87], v[0:15]\n\t"
        "ds_read_b64_tr_b16 v[40:41], v193 offset:18432\n\t"
        "ds_read_b64_tr_b16 v[42:43], v193 offset:19456\n\t"
        "s_waitcnt lgkmcnt(14)\n\t"
        "v_mfma_f32_32x32x16_bf16 v[16:31], v[44:47], v[84:87], v[16:31]\n\t"
        "ds_read_b64_tr_b16 v[44:45], v194 offset:18432\n\t"
        "ds_read_b64_tr_b16 v[46:47], v194 offset:19456\n\t"
        "buffer_load_dwordx4 v[132:135], v192, s[40:43], s44 offen offset:1024 sc1\n\t"
        "s_waitcnt vmcnt(11) lgkmcnt(14)\n\t"
        "v_mfma_f32_32x32x16_bf16 v[0:15], v[48:51], v[88:91], v[0:15]\n\t"
        "ds_read_b64_tr_b16 v[48:49], v193 offset:20480\n\t"
        "ds_read_b64_tr_b16 v[50:51], v193 offset:21504\n\t"
        "s_waitcnt lgkmcnt(14)\n\t"
        "v_mfma_f32_32x32x16_bf16 v[16:31], v[52:55], v[88:91], v[16:31]\n\t"
        "ds_read_b64_tr_b16 v[52:53], v194 offset:20480\n\t"
        "ds_read_b64_tr_b16 v[54:55], v194 offset:21504\n\t"
        "buffer_load_dwordx4 v[136:139], v192, s[40:43], s44 offen offset:2048 sc1\n\t"
        "s_waitcnt vmcnt(11) lgkmcnt(14)\n\t"
        "v_mfma_f32_32x32x16_bf16 v[0:15], v[56:59], v[92:95], v[0:15]\n\t"
        "ds_read_b64_tr_b16 v[56:57], v193 offset:22528\n\t"
        "ds_read_b64_tr_b16 v[58:59], v193 offset:23552\n\t"
        "s_waitcnt lgkmcnt(14)\n\t"
        "v_mfma_f32_32x32x16_bf16 v[16:31], v[60:63], v[92:95], v[16:31]\n\t"
        "ds_read_b64_tr_b16 v[60:61], v194 offset:22528\n\t"
        "ds_read_b64_tr_b16 v[62:63], v194 offset:23552\n\t"
        "buffer_load_dwordx4 v[140:143], v192, s[40:43], s44 offen offset:3072 sc1\n\t"
        "s_waitcnt vmcnt(11) lgkmcnt(14)\n\t"
        "v_mfma_f32_32x32x16_bf16 v[0:15], v[32:35], v[96:99], v[0:15]\n\t"
        "ds_read_b64_tr_b16 v[32:33], v193 offset:24576\n\t"
        "ds_read_b64_tr_b16 v[34:35], v193 offset:25600\n\t"
        "s_waitcnt lgkmcnt(14)\n\t"
        "v_mfma_f32_32x32x16_bf16 v[16:31], v[36:39], v[96:99], v[16:31]\n\t"
        "ds_read_b64_tr_b16 v[36:37], v194 offset:24576\n\t"
        "ds_read_b64_tr_b16 v[38:39], v194 offset:25600\n\t"
        "s_add_u32 s44, s44, 0x1000\n\t"
        "s_and_b32 s44, s44, 0xffff\n\t"
        "buffer_load_dwordx4 v[144:147], v192, s[40:43], s44 offen offset:0 sc1\n\t"
        "s_waitcnt vmcnt(11) lgkmcnt(14)\n\t"
        "v_mfma_f32_32x32x16_bf16 v[0:15], v[40:43], v[100:103], v[0:15]\n\t"
        "ds_read_b64_tr_b16 v[40:41], v193 offset:26624\n\t"
        "ds_read_b64_tr_b16 v[42:43], v193 offset:27648\n\t"
        "s_waitcnt lgkmcnt(14)\n\t"
        "v_mfma_f32_32x32x16_bf16 v[16:31], v[44:47], v[100:103], v[16:31]\n\t"
        "ds_read_b64_tr_b16 v[44:45], v194 offset:26624\n\t"
        "ds_read_b64_tr_b16 v[46:47], v194 offset:27648\n\t"
        "buffer_load_dwordx4 v[148:151], v192, s[40:43], s44 offen offset:1024 sc1\n\t"
        "s_waitcnt vmcnt(11) lgkmcnt(14)\n\t"
        "v_mfma_f32_32x32x16_bf16 v[0:15], v[48:51], v[104:107], v[0:15]\n\t"
        "ds_read_b64_tr_b16 v[48:49], v193 offset:28672\n\t"
        "ds_read_b64_tr_b16 v[50:51], v193 offset:29696\n\t"
        "s_waitcnt lgkmcnt(14)\n\t"
        "v_mfma_f32_32x32x16_bf16 v[16:31], v[52:55], v[104:107], v[16:31]\n\t"
        "ds_read_b64_tr_b16 v[52:53], v194 offset:28672\n\t"
        "ds_read_b64_tr_b16 v[54:55], v194 offset:29696\n\t"
        "buffer_load_dwordx4 v[152:155], v192, s[40:43], s44 offen offset:2048 sc1\n\t"
        "s_waitcnt vmcnt(11) lgkmcnt(14)\n\t"
        "v_mfma_f32_32x32x16_bf16 v[0:15], v[56:59], v[108:111], v[0:15]\n\t"
        "ds_read_b64_tr_b16 v[56:57], v193 offset:30720\n\t"
        "ds_read_b64_tr_b16 v[58:59], v193 offset:31744\n\t"
        "s_waitcnt lgkmcnt(14)\n\t"
        "v_mfma_f32_32x32x16_bf16 v[16:31], v[60:63], v[108:111], v[16:31]\n\t"
        "ds_read_b64_tr_b16 v[60:61], v194 offset:30720\n\t"
        "ds_read_b64_tr_b16 v[62:63], v194 offset:31744\n\t"
        "buffer_load_dwordx4 v[156:159], v192, s[40:43], s44 offen offset:3072 sc1\n\t"
        "s_waitcnt vmcnt(11) lgkmcnt(14)\n\t"
        "v_mfma_f32_32x32x16_bf16 v[0:15], v[32:35], v[112:115], v[0:15]\n\t"
        "ds_read_b64_tr_b16 v[32:33], v193 offset:32768\n\t"
        "ds_read_b64_tr_b16 v[34:35], v193 offset:33792\n\t"
        "s_waitcnt lgkmcnt(14)\n\t"
        "v_mfma_f32_32x32x16_bf16 v[16:31], v[36:39], v[112:115], v[16:31]\n\t"
        "ds_read_b64_tr_b16 v[36:37], v194 offset:32768\n\t"
        "ds_read_b64_tr_b16 v[38:39], v194 offset:33792\n\t"
        "s_add_u32 s44, s44, 0x1000\n\t"
        "s_and_b32 s44, s44, 0xffff\n\t"
        "buffer_load_dwordx4 v[160:163], v192, s[40:43], s44 offen offset:0 sc1\n\t"
        "s_waitcnt vmcnt(11) lgkmcnt(14)\n\t"
        "v_mfma_f32_32x32x16_bf16 v[0:15], v[40:43], v[116:119], v[0:15]\n\t"
        "ds_read_b64_tr_b16 v[40:41], v193 offset:34816\n\t"
        "ds_read_b64_tr_b16 v[42:43], v193 offset:35840\n\t"
        "s_waitcnt lgkmcnt(14)\n\t"
        "v_mfma_f32_32x32x16_bf16 v[16:31], v[44:47], v[116:119], v[16:31]\n\t"
        "ds_read_b64_tr_b16 v[44:45], v194 offset:34816\n\t"
        "ds_read_b64_tr_b16 v[46:47], v194 offset:35840\n\t"
        "buffer_load_dwordx4 v[164:167], v192, s[40:43], s44 offen offset:1024 sc1\n\t"
        "s_waitcnt vmcnt(11) lgkmcnt(14)\n\t"
        "v_mfma_f32_32x32x16_bf16 v[0:15], v[48:51], v[120:123], v[0:15]\n\t"
        "ds_read_b64_tr_b16 v[48:49], v193 offset:36864\n\t"
        "ds_read_b64_tr_b16 v[50:51], v193 offset:37888\n\t"
        "s_waitcnt lgkmcnt(14)\n\t"
        "v_mfma_f32_32x32x16_bf16 v[16:31], v[52:55], v[120:123], v[16:31]\n\t"
        "ds_read_b64_tr_b16 v[52:53], v194 offset:36864\n\t"
        "ds_read_b64_tr_b16 v[54:55], v194 offset:37888\n\t"
        "buffer_load_dwordx4 v[168:171], v192, s[40:43], s44 offen offset:2048 sc1\n\t"
        "s_waitcnt vmcnt(11) lgkmcnt(14)\n\t"
        "v_mfma_f32_32x32x16_bf16 v[0:15], v[56:59], v[124:127], v[0:15]\n\t"
        "ds_read_b64_tr_b16 v[56:57], v193 offset:38912\n\t"
        "ds_read_b64_tr_b16 v[58:59], v193 offset:39936\n\t"
        "s_waitcnt lgkmcnt(14)\n\t"
        "v_mfma_f32_32x32x16_bf16 v[16:31], v[60:63], v[124:127], v[16:31]\n\t"
        "ds_read_b64_tr_b16 v[60:61], v194 offset:38912\n\t"
        "ds_read_b64_tr_b16 v[62:63], v194 offset:39936\n\t"
        "buffer_load_dwordx4 v[172:175], v192, s[40:43], s44 offen offset:3072 sc1\n\t"
        "s_waitcnt vmcnt(11) lgkmcnt(14)\n\t"
        "v_mfma_f32_32x32x16_bf16 v[0:15], v[32:35], v[128:131], v[0:15]\n\t"
        "ds_read_b64_tr_b16 v[32:33], v193 offset:40960\n\t"
        "ds_read_b64_tr_b16 v[34:35], v193 offset:41984\n\t"
        "s_waitcnt lgkmcnt(14)\n\t"
        "v_mfma_f32_32x32x16_bf16 v[16:31], v[36:39], v[128:131], v[16:31]\n\t"
        "ds_read_b64_tr_b16 v[36:37], v194 offset:40960\n\t"
        "ds_read_b64_tr_b16 v[38:39], v194 offset:41984\n\t"
        "s_add_u32 s44, s44, 0x1000\n\t"
        "s_and_b32 s44, s44, 0xffff\n\t"
        "buffer_load_dwordx4 v[176:179], v192, s[40:43], s44 offen offset:0 sc1\n\t"
        "s_waitcnt vmcnt(11) lgkmcnt(14)\n\t"
        "v_mfma_f32_32x32x16_bf16 v[0:15], v[40:43], v[132:135], v[0:15]\n\t"
        "ds_read_b64_tr_b16 v[40:41], v193 offset:43008\n\t"
        "ds_read_b64_tr_b16 v[42:43], v193 offset:44032\n\t"
        "s_waitcnt lgkmcnt(14)\n\t"
        "v_mfma_f32_32x32x16_bf16 v[16:31], v[44:47], v[132:135], v[16:31]\n\t"
        "ds_read_b64_tr_b16 v[44:45], v194 offset:43008\n\t"
        "ds_read_b64_tr_b16 v[46:47], v194 offset:44032\n\t"
        "buffer_load_dwordx4 v[180:183], v192, s[40:43], s44 offen offset:1024 sc1\n\t"
        "s_waitcnt vmcnt(11) lgkmcnt(14)\n\t"
        "v_mfma_f32_32x32x16_bf16 v[0:15], v[48:51], v[136:139], v[0:15]\n\t"
        "ds_read_b64_tr_b16 v[48:49], v193 offset:45056\n\t"
        "ds_read_b64_tr_b16 v[50:51], v193 offset:46080\n\t"
        "s_waitcnt lgkmcnt(14)\n\t"
        "v_mfma_f32_32x32x16_bf16 v[16:31], v[52:55], v[136:139], v[16:31]\n\t"
        "ds_read_b64_tr_b16 v[52:53], v194 offset:45056\n\t"
        "ds_read_b64_tr_b16 v[54:55], v194 offset:46080\n\t"
        "buffer_load_dwordx4 v[184:187], v192, s[40:43], s44 offen offset:2048 sc1\n\t"
        "s_waitcnt vmcnt(11) lgkmcnt(14)\n\t"
        "v_mfma_f32_32x32x16_bf16 v[0:15], v[56:59], v[140:143], v[0:15]\n\t"
        "ds_read_b64_tr_b16 v[56:57], v193 offset:47104\n\t"
        "ds_read_b64_tr_b16 v[58:59], v193 offset:48128\n\t"
        "s_waitcnt lgkmcnt(14)\n\t"
        "v_mfma_f32_32x32x16_bf16 v[16:31], v[60:63], v[140:143], v[16:31]\n\t"
        "ds_read_b64_tr_b16 v[60:61], v194 offset:47104\n\t"
        "ds_read_b64_tr_b16 v[62:63], v194 offset:48128\n\t"
        "buffer_load_dwordx4 v[188:191], v192, s[40:43], s44 offen offset:3072 sc1\n\t"
        "s_waitcnt vmcnt(11) lgkmcnt(14)\n\t"
        "v_mfma_f32_32x32x16_bf16 v[0:15], v[32:35], v[144:147], v[0:15]\n\t"
        "ds_read_b64_tr_b16 v[32:33], v193 offset:49152\n\t"
        "ds_read_b64_tr_b16 v[34:35], v193 offset:50176\n\t"
        "s_waitcnt lgkmcnt(14)\n\t"
        "v_mfma_f32_32x32x16_bf16 v[16:31], v[36:39], v[144:147], v[16:31]\n\t"
        "ds_read_b64_tr_b16 v[36:37], v194 offset:49152\n\t"
        "ds_read_b64_tr_b16 v[38:39], v194 offset:50176\n\t"
        "s_add_u32 s44, s44, 0x1000\n\t"
        "s_and_b32 s44, s44, 0xffff\n\t"
        "buffer_load_dwordx4 v[64:67], v192, s[40:43], s44 offen offset:0 sc1\n\t"
        "s_waitcnt vmcnt(11) lgkmcnt(14)\n\t"
        "v_mfma_f32_32x32x16_bf16 v[0:15], v[40:43], v[148:151], v[0:15]\n\t"
        "ds_read_b64_tr_b16 v[40:41], v193 offset:51200\n\t"
        "ds_read_b64_tr_b16 v[42:43], v193 offset:52224\n\t"
        "s_waitcnt lgkmcnt(14)\n\t"
        "v_mfma_f32_32x32x16_bf16 v[16:31], v[44:47], v[148:151], v[16:31]\n\t"
        "ds_read_b64_tr_b16 v[44:45], v194 offset:51200\n\t"
        "ds_read_b64_tr_b16 v[46:47], v194 offset:52224\n\t"
        "buffer_load_dwordx4 v[68:71], v192, s[40:43], s44 offen offset:1024 sc1\n\t"
        "s_waitcnt vmcnt(11) lgkmcnt(14)\n\t"
        "v_mfma_f32_32x32x16_bf16 v[0:15], v[48:51], v[152:155], v[0:15]\n\t"
        "ds_read_b64_tr_b16 v[48:49], v193 offset:53248\n\t"
        "ds_read_b64_tr_b16 v[50:51], v193 offset:54272\n\t"
        "s_waitcnt lgkmcnt(14)\n\t"
        "v_mfma_f32_32x32x16_bf16 v[16:31], v[52:55], v[152:155], v[16:31]\n\t"
        "ds_read_b64_tr_b16 v[52:53], v194 offset:53248\n\t"
        "ds_read_b64_tr_b16 v[54:55], v194 offset:54272\n\t"
        "buffer_load_dwordx4 v[72:75], v192, s[40:43], s44 offen offset:2048 sc1\n\t"
        "s_waitcnt vmcnt(11) lgkmcnt(14)\n\t"
        "v_mfma_f32_32x32x16_bf16 v[0:15], v[56:59], v[156:159], v[0:15]\n\t"
        "ds_read_b64_tr_b16 v[56:57], v193 offset:55296\n\t"
        "ds_read_b64_tr_b16 v[58:59], v193 offset:56320\n\t"
        "s_waitcnt lgkmcnt(14)\n\t"
        "v_mfma_f32_32x32x16_bf16 v[16:31], v[60:63], v[156:159], v[16:31]\n\t"
        "ds_read_b64_tr_b16 v[60:61], v194 offset:55296\n\t"
        "ds_read_b64_tr_b16 v[62:63], v194 offset:56320\n\t"
        "buffer_load_dwordx4 v[76:79], v192, s[40:43], s44 offen offset:3072 sc1\n\t"
        "s_waitcnt vmcnt(11) lgkmcnt(14)\n\t"
        "v_mfma_f32_32x32x16_bf16 v[0:15], v[32:35], v[160:163], v[0:15]\n\t"
        "ds_read_b64_tr_b16 v[32:33], v193 offset:57344\n\t"
        "ds_read_b64_tr_b16 v[34:35], v193 offset:58368\n\t"
        "s_waitcnt lgkmcnt(14)\n\t"
        "v_mfma_f32_32x32x16_bf16 v[16:31], v[36:39], v[160:163], v[16:31]\n\t"
        "ds_read_b64_tr_b16 v[36:37], v194 offset:57344\n\t"
        "ds_read_b64_tr_b16 v[38:39], v194 offset:58368\n\t"
        "s_add_u32 s44, s44, 0x1000\n\t"
        "s_and_b32 s44, s44, 0xffff\n\t"
        "buffer_load_dwordx4 v[80:83], v192, s[40:43], s44 offen offset:0 sc1\n\t"
        "s_waitcnt vmcnt(11) lgkmcnt(14)\n\t"
        "v_mfma_f32_32x32x16_bf16 v[0:15], v[40:43], v[164:167], v[0:15]\n\t"
        "ds_read_b64_tr_b16 v[40:41], v193 offset:59392\n\t"
        "ds_read_b64_tr_b16 v[42:43], v193 offset:60416\n\t"
        "s_waitcnt lgkmcnt(14)\n\t"
        "v_mfma_f32_32x32x16_bf16 v[16:31], v[44:47], v[164:167], v[16:31]\n\t"
        "ds_read_b64_tr_b16 v[44:45], v194 offset:59392\n\t"
        "ds_read_b64_tr_b16 v[46:47], v194 offset:60416\n\t"
        "buffer_load_dwordx4 v[84:87], v192, s[40:43], s44 offen offset:1024 sc1\n\t"
        "s_waitcnt vmcnt(11) lgkmcnt(14)\n\t"
        "v_mfma_f32_32x32x16_bf16 v[0:15], v[48:51], v[168:171], v[0:15]\n\t"
        "ds_read_b64_tr_b16 v[48:49], v193 offset:61440\n\t"
        "ds_read_b64_tr_b16 v[50:51], v193 offset:62464\n\t"
        "s_waitcnt lgkmcnt(14)\n\t"
        "v_mfma_f32_32x32x16_bf16 v[16:31], v[52:55], v[168:171], v[16:31]\n\t"
        "ds_read_b64_tr_b16 v[52:53], v194 offset:61440\n\t"
        "ds_read_b64_tr_b16 v[54:55], v194 offset:62464\n\t"
        "buffer_load_dwordx4 v[88:91], v192, s[40:43], s44 offen offset:2048 sc1\n\t"
        "s_waitcnt vmcnt(11) lgkmcnt(14)\n\t"
        "v_mfma_f32_32x32x16_bf16 v[0:15], v[56:59], v[172:175], v[0:15]\n\t"
        "ds_read_b64_tr_b16 v[56:57], v193 offset:63488\n\t"
        "ds_read_b64_tr_b16 v[58:59], v193 offset:64512\n\t"
        "s_waitcnt lgkmcnt(14)\n\t"
        "v_mfma_f32_32x32x16_bf16 v[16:31], v[60:63], v[172:175], v[16:31]\n\t"
        "ds_read_b64_tr_b16 v[60:61], v194 offset:63488\n\t"
        "ds_read_b64_tr_b16 v[62:63], v194 offset:64512\n\t"
        "buffer_load_dwordx4 v[92:95], v192, s[40:43], s44 offen offset:3072 sc1\n\t"
        "s_waitcnt vmcnt(11) lgkmcnt(14)\n\t"
        "v_mfma_f32_32x32x16_bf16 v[0:15], v[32:35], v[176:179], v[0:15]\n\t"
        "ds_read_b64_tr_b16 v[32:33], v195 offset:0\n\t"
        "ds_read_b64_tr_b16 v[34:35], v195 offset:1024\n\t"
        "s_waitcnt lgkmcnt(14)\n\t"
        "v_mfma_f32_32x32x16_bf16 v[16:31], v[36:39], v[176:179], v[16:31]\n\t"
        "ds_read_b64_tr_b16 v[36:37], v196 offset:0\n\t"
        "ds_read_b64_tr_b16 v[38:39], v196 offset:1024\n\t"
        "s_add_u32 s44, s44, 0x1000\n\t"
        "s_and_b32 s44, s44, 0xffff\n\t"
        "buffer_load_dwordx4 v[96:99], v192, s[40:43], s44 offen offset:0 sc1\n\t"
        "s_waitcnt vmcnt(11) lgkmcnt(14)\n\t"
        "v_mfma_f32_32x32x16_bf16 v[0:15], v[40:43], v[180:183], v[0:15]\n\t"
        "ds_read_b64_tr_b16 v[40:41], v195 offset:2048\n\t"
        "ds_read_b64_tr_b16 v[42:43], v195 offset:3072\n\t"
        "s_waitcnt lgkmcnt(14)\n\t"
        "v_mfma_f32_32x32x16_bf16 v[16:31], v[44:47], v[180:183], v[16:31]\n\t"
        "ds_read_b64_tr_b16 v[44:45], v196 offset:2048\n\t"
        "ds_read_b64_tr_b16 v[46:47], v196 offset:3072\n\t"
        "buffer_load_dwordx4 v[100:103], v192, s[40:43], s44 offen offset:1024 sc1\n\t"
        "s_waitcnt vmcnt(11) lgkmcnt(14)\n\t"
        "v_mfma_f32_32x32x16_bf16 v[0:15], v[48:51], v[184:187], v[0:15]\n\t"
        "ds_read_b64_tr_b16 v[48:49], v195 offset:4096\n\t"
        "ds_read_b64_tr_b16 v[50:51], v195 offset:5120\n\t"
        "s_waitcnt lgkmcnt(14)\n\t"
        "v_mfma_f32_32x32x16_bf16 v[16:31], v[52:55], v[184:187], v[16:31]\n\t"
        "ds_read_b64_tr_b16 v[52:53], v196 offset:4096\n\t"
        "ds_read_b64_tr_b16 v[54:55], v196 offset:5120\n\t"
        "buffer_load_dwordx4 v[104:107], v192, s[40:43], s44 offen offset:2048 sc1\n\t"
        "s_waitcnt vmcnt(11) lgkmcnt(14)\n\t"
        "v_mfma_f32_32x32x16_bf16 v[0:15], v[56:59], v[188:191], v[0:15]\n\t"
        "ds_read_b64_tr_b16 v[56:57], v195 offset:6144\n\t"
        "ds_read_b64_tr_b16 v[58:59], v195 offset:7168\n\t"
        "s_waitcnt lgkmcnt(14)\n\t"
        "v_mfma_f32_32x32x16_bf16 v[16:31], v[60:63], v[188:191], v[16:31]\n\t"
        "ds_read_b64_tr_b16 v[60:61], v196 offset:6144\n\t"
        "ds_read_b64_tr_b16 v[62:63], v196 offset:7168\n\t"
        "buffer_load_dwordx4 v[108:111], v192, s[40:43], s44 offen offset:3072 sc1\n\t"
        "s_waitcnt vmcnt(11) lgkmcnt(14)\n\t"
        "v_mfma_f32_32x32x16_bf16 v[0:15], v[32:35], v[64:67], v[0:15]\n\t"
        "ds_read_b64_tr_b16 v[32:33], v195 offset:8192\n\t"
        "ds_read_b64_tr_b16 v[34:35], v195 offset:9216\n\t"
        "s_waitcnt lgkmcnt(14)\n\t"
        "v_mfma_f32_32x32x16_bf16 v[16:31], v[36:39], v[64:67], v[16:31]\n\t"
        "ds_read_b64_tr_b16 v[36:37], v196 offset:8192\n\t"
        "ds_read_b64_tr_b16 v[38:39], v196 offset:9216\n\t"
        "s_add_u32 s44, s44, 0x1000\n\t"
        "s_and_b32 s44, s44, 0xffff\n\t"
        "buffer_load_dwordx4 v[112:115], v192, s[40:43], s44 offen offset:0 sc1\n\t"
        "s_waitcnt vmcnt(11) lgkmcnt(14)\n\t"
        "v_mfma_f32_32x32x16_bf16 v[0:15], v[40:43], v[68:71], v[0:15]\n\t"
        "ds_read_b64_tr_b16 v[40:41], v195 offset:10240\n\t"
        "ds_read_b64_tr_b16 v[42:43], v195 offset:11264\n\t"
        "s_waitcnt lgkmcnt(14)\n\t"
        "v_mfma_f32_32x32x16_bf16 v[16:31], v[44:47], v[68:71], v[16:31]\n\t"
        "ds_read_b64_tr_b16 v[44:45], v196 offset:10240\n\t"
        "ds_read_b64_tr_b16 v[46:47], v196 offset:11264\n\t"
        "buffer_load_dwordx4 v[116:119], v192, s[40:43], s44 offen offset:1024 sc1\n\t"
        "s_waitcnt vmcnt(11) lgkmcnt(14)\n\t"
        "v_mfma_f32_32x32x16_bf16 v[0:15], v[48:51], v[72:75], v[0:15]\n\t"
        "ds_read_b64_tr_b16 v[48:49], v195 offset:12288\n\t"
        "ds_read_b64_tr_b16 v[50:51], v195 offset:13312\n\t"
        "s_waitcnt lgkmcnt(14)\n\t"
        "v_mfma_f32_32x32x16_bf16 v[16:31], v[52:55], v[72:75], v[16:31]\n\t"
        "ds_read_b64_tr_b16 v[52:53], v196 offset:12288\n\t"
        "ds_read_b64_tr_b16 v[54:55], v196 offset:13312\n\t"
        "buffer_load_dwordx4 v[120:123], v192, s[40:43], s44 offen offset:2048 sc1\n\t"
        "s_waitcnt vmcnt(11) lgkmcnt(14)\n\t"
        "v_mfma_f32_32x32x16_bf16 v[0:15], v[56:59], v[76:79], v[0:15]\n\t"
        "ds_read_b64_tr_b16 v[56:57], v195 offset:14336\n\t"
        "ds_read_b64_tr_b16 v[58:59], v195 offset:15360\n\t"
        "s_waitcnt lgkmcnt(14)\n\t"
        "v_mfma_f32_32x32x16_bf16 v[16:31], v[60:63], v[76:79], v[16:31]\n\t"
        "ds_read_b64_tr_b16 v[60:61], v196 offset:14336\n\t"
        "ds_read_b64_tr_b16 v[62:63], v196 offset:15360\n\t"
        "buffer_load_dwordx4 v[124:127], v192, s[40:43], s44 offen offset:3072 sc1\n\t"
        "s_waitcnt vmcnt(11) lgkmcnt(14)\n\t"
        "v_mfma_f32_32x32x16_bf16 v[0:15], v[32:35], v[80:83], v[0:15]\n\t"
        "ds_read_b64_tr_b16 v[32:33], v195 offset:16384\n\t"
        "ds_read_b64_tr_b16 v[34:35], v195 offset:17408\n\t"
        "s_waitcnt lgkmcnt(14)\n\t"
        "v_mfma_f32_32x32x16_bf16 v[16:31], v[36:39], v[80:83], v[16:31]\n\t"
        "ds_read_b64_tr_b16 v[36:37], v196 offset:16384\n\t"
        "ds_read_b64_tr_b16 v[38:39], v196 offset:17408\n\t"
        "s_add_u32 s44, s44, 0x1000\n\t"
        "s_and_b32 s44, s44, 0xffff\n\t"
        "buffer_load_dwordx4 v[128:131], v192, s[40:43], s44 offen offset:0 sc1\n\t"
        "s_waitcnt vmcnt(11) lgkmcnt(14)\n\t"
        "v_mfma_f32_32x32x16_bf16 v[0:15], v[40:43], v[84:87], v[0:15]\n\t"
        "ds_read_b64_tr_b16 v[40:41], v195 offset:18432\n\t"
        "ds_read_b64_tr_b16 v[42:43], v195 offset:19456\n\t"
        "s_waitcnt lgkmcnt(14)\n\t"
        "v_mfma_f32_32x32x16_bf16 v[16:31], v[44:47], v[84:87], v[16:31]\n\t"
        "ds_read_b64_tr_b16 v[44:45], v196 offset:18432\n\t"
        "ds_read_b64_tr_b16 v[46:47], v196 offset:19456\n\t"
        "buffer_load_dwordx4 v[132:135], v192, s[40:43], s44 offen offset:1024 sc1\n\t"
        "s_waitcnt vmcnt(11) lgkmcnt(14)\n\t"
        "v_mfma_f32_32x32x16_bf16 v[0:15], v[48:51], v[88:91], v[0:15]\n\t"
        "ds_read_b64_tr_b16 v[48:49], v195 offset:20480\n\t"
        "ds_read_b64_tr_b16 v[50:51], v195 offset:21504\n\t"
        "s_waitcnt lgkmcnt(14)\n\t"
        "v_mfma_f32_32x32x16_bf16 v[16:31], v[52:55], v[88:91], v[16:31]\n\t"
        "ds_read_b64_tr_b16 v[52:53], v196 offset:20480\n\t"
        "ds_read_b64_tr_b16 v[54:55], v196 offset:21504\n\t"
        "buffer_load_dwordx4 v[136:139], v192, s[40:43], s44 offen offset:2048 sc1\n\t"
        "s_waitcnt vmcnt(11) lgkmcnt(14)\n\t"
        "v_mfma_f32_32x32x16_bf16 v[0:15], v[56:59], v[92:95], v[0:15]\n\t"
        "ds_read_b64_tr_b16 v[56:57], v195 offset:22528\n\t"
        "ds_read_b64_tr_b16 v[58:59], v195 offset:23552\n\t"
        "s_waitcnt lgkmcnt(14)\n\t"
        "v_mfma_f32_32x32x16_bf16 v[16:31], v[60:63], v[92:95], v[16:31]\n\t"
        "ds_read_b64_tr_b16 v[60:61], v196 offset:22528\n\t"
        "ds_read_b64_tr_b16 v[62:63], v196 offset:23552\n\t"
        "buffer_load_dwordx4 v[140:143], v192, s[40:43], s44 offen offset:3072 sc1\n\t"
        "s_waitcnt vmcnt(11) lgkmcnt(14)\n\t"
        "v_mfma_f32_32x32x16_bf16 v[0:15], v[32:35], v[96:99], v[0:15]\n\t"
        "ds_read_b64_tr_b16 v[32:33], v195 offset:24576\n\t"
        "ds_read_b64_tr_b16 v[34:35], v195 offset:25600\n\t"
        "s_waitcnt lgkmcnt(14)\n\t"
        "v_mfma_f32_32x32x16_bf16 v[16:31], v[36:39], v[96:99], v[16:31]\n\t"
        "ds_read_b64_tr_b16 v[36:37], v196 offset:24576\n\t"
        "ds_read_b64_tr_b16 v[38:39], v196 offset:25600\n\t"
        "s_add_u32 s44, s44, 0x1000\n\t"
        "s_and_b32 s44, s44, 0xffff\n\t"
        "buffer_load_dwordx4 v[144:147], v192, s[40:43], s44 offen offset:0 sc1\n\t"
        "s_waitcnt vmcnt(11) lgkmcnt(14)\n\t"
        "v_mfma_f32_32x32x16_bf16 v[0:15], v[40:43], v[100:103], v[0:15]\n\t"
        "ds_read_b64_tr_b16 v[40:41], v195 offset:26624\n\t"
        "ds_read_b64_tr_b16 v[42:43], v195 offset:27648\n\t"
        "s_waitcnt lgkmcnt(14)\n\t"
        "v_mfma_f32_32x32x16_bf16 v[16:31], v[44:47], v[100:103], v[16:31]\n\t"
        "ds_read_b64_tr_b16 v[44:45], v196 offset:26624\n\t"
        "ds_read_b64_tr_b16 v[46:47], v196 offset:27648\n\t"
        "buffer_load_dwordx4 v[148:151], v192, s[40:43], s44 offen offset:1024 sc1\n\t"
        "s_waitcnt vmcnt(11) lgkmcnt(14)\n\t"
        "v_mfma_f32_32x32x16_bf16 v[0:15], v[48:51], v[104:107], v[0:15]\n\t"
        "ds_read_b64_tr_b16 v[48:49], v195 offset:28672\n\t"
        "ds_read_b64_tr_b16 v[50:51], v195 offset:29696\n\t"
        "s_waitcnt lgkmcnt(14)\n\t"
        "v_mfma_f32_32x32x16_bf16 v[16:31], v[52:55], v[104:107], v[16:31]\n\t"
        "ds_read_b64_tr_b16 v[52:53], v196 offset:28672\n\t"
        "ds_read_b64_tr_b16 v[54:55], v196 offset:29696\n\t"
        "buffer_load_dwordx4 v[152:155], v192, s[40:43], s44 offen offset:2048 sc1\n\t"
        "s_waitcnt vmcnt(11) lgkmcnt(14)\n\t"
        "v_mfma_f32_32x32x16_bf16 v[0:15], v[56:59], v[108:111], v[0:15]\n\t"
        "ds_read_b64_tr_b16 v[56:57], v195 offset:30720\n\t"
        "ds_read_b64_tr_b16 v[58:59], v195 offset:31744\n\t"
        "s_waitcnt lgkmcnt(14)\n\t"
        "v_mfma_f32_32x32x16_bf16 v[16:31], v[60:63], v[108:111], v[16:31]\n\t"
        "ds_read_b64_tr_b16 v[60:61], v196 offset:30720\n\t"
        "ds_read_b64_tr_b16 v[62:63], v196 offset:31744\n\t"
        "buffer_load_dwordx4 v[156:159], v192, s[40:43], s44 offen offset:3072 sc1\n\t"
        "s_waitcnt vmcnt(11) lgkmcnt(14)\n\t"
        "v_mfma_f32_32x32x16_bf16 v[0:15], v[32:35], v[112:115], v[0:15]\n\t"
        "ds_read_b64_tr_b16 v[32:33], v195 offset:32768\n\t"
        "ds_read_b64_tr_b16 v[34:35], v195 offset:33792\n\t"
        "s_waitcnt lgkmcnt(14)\n\t"
        "v_mfma_f32_32x32x16_bf16 v[16:31], v[36:39], v[112:115], v[16:31]\n\t"
        "ds_read_b64_tr_b16 v[36:37], v196 offset:32768\n\t"
        "ds_read_b64_tr_b16 v[38:39], v196 offset:33792\n\t"
        "s_add_u32 s44, s44, 0x1000\n\t"
        "s_and_b32 s44, s44, 0xffff\n\t"
        "buffer_load_dwordx4 v[160:163], v192, s[40:43], s44 offen offset:0 sc1\n\t"
        "s_waitcnt vmcnt(11) lgkmcnt(14)\n\t"
        "v_mfma_f32_32x32x16_bf16 v[0:15], v[40:43], v[116:119], v[0:15]\n\t"
        "ds_read_b64_tr_b16 v[40:41], v195 offset:34816\n\t"
        "ds_read_b64_tr_b16 v[42:43], v195 offset:35840\n\t"
        "s_waitcnt lgkmcnt(14)\n\t"
        "v_mfma_f32_32x32x16_bf16 v[16:31], v[44:47], v[116:119], v[16:31]\n\t"
        "ds_read_b64_tr_b16 v[44:45], v196 offset:34816\n\t"
        "ds_read_b64_tr_b16 v[46:47], v196 offset:35840\n\t"
        "buffer_load_dwordx4 v[164:167], v192, s[40:43], s44 offen offset:1024 sc1\n\t"
        "s_waitcnt vmcnt(11) lgkmcnt(14)\n\t"
        "v_mfma_f32_32x32x16_bf16 v[0:15], v[48:51], v[120:123], v[0:15]\n\t"
        "ds_read_b64_tr_b16 v[48:49], v195 offset:36864\n\t"
        "ds_read_b64_tr_b16 v[50:51], v195 offset:37888\n\t"
        "s_waitcnt lgkmcnt(14)\n\t"
        "v_mfma_f32_32x32x16_bf16 v[16:31], v[52:55], v[120:123], v[16:31]\n\t"
        "ds_read_b64_tr_b16 v[52:53], v196 offset:36864\n\t"
        "ds_read_b64_tr_b16 v[54:55], v196 offset:37888\n\t"
        "buffer_load_dwordx4 v[168:171], v192, s[40:43], s44 offen offset:2048 sc1\n\t"
        "s_waitcnt vmcnt(11) lgkmcnt(14)\n\t"
        "v_mfma_f32_32x32x16_bf16 v[0:15], v[56:59], v[124:127], v[0:15]\n\t"
        "ds_read_b64_tr_b16 v[56:57], v195 offset:38912\n\t"
        "ds_read_b64_tr_b16 v[58:59], v195 offset:39936\n\t"
        "s_waitcnt lgkmcnt(14)\n\t"
        "v_mfma_f32_32x32x16_bf16 v[16:31], v[60:63], v[124:127], v[16:31]\n\t"
        "ds_read_b64_tr_b16 v[60:61], v196 offset:38912\n\t"
        "ds_read_b64_tr_b16 v[62:63], v196 offset:39936\n\t"
        "buffer_load_dwordx4 v[172:175], v192, s[40:43], s44 offen offset:3072 sc1\n\t"
        "s_waitcnt vmcnt(11) lgkmcnt(14)\n\t"
        "v_mfma_f32_32x32x16_bf16 v[0:15], v[32:35], v[128:131], v[0:15]\n\t"
        "ds_read_b64_tr_b16 v[32:33], v195 offset:40960\n\t"
        "ds_read_b64_tr_b16 v[34:35], v195 offset:41984\n\t"
        "s_waitcnt lgkmcnt(14)\n\t"
        "v_mfma_f32_32x32x16_bf16 v[16:31], v[36:39], v[128:131], v[16:31]\n\t"
        "ds_read_b64_tr_b16 v[36:37], v196 offset:40960\n\t"
        "ds_read_b64_tr_b16 v[38:39], v196 offset:41984\n\t"
        "s_add_u32 s44, s44, 0x1000\n\t"
        "s_and_b32 s44, s44, 0xffff\n\t"
        "buffer_load_dwordx4 v[176:179], v192, s[40:43], s44 offen offset:0 sc1\n\t"
        "s_waitcnt vmcnt(11) lgkmcnt(14)\n\t"
        "v_mfma_f32_32x32x16_bf16 v[0:15], v[40:43], v[132:135], v[0:15]\n\t"
        "ds_read_b64_tr_b16 v[40:41], v195 offset:43008\n\t"
        "ds_read_b64_tr_b16 v[42:43], v195 offset:44032\n\t"
        "s_waitcnt lgkmcnt(14)\n\t"
        "v_mfma_f32_32x32x16_bf16 v[16:31], v[44:47], v[132:135], v[16:31]\n\t"
        "ds_read_b64_tr_b16 v[44:45], v196 offset:43008\n\t"
        "ds_read_b64_tr_b16 v[46:47], v196 offset:44032\n\t"
        "buffer_load_dwordx4 v[180:183], v192, s[40:43], s44 offen offset:1024 sc1\n\t"
        "s_waitcnt vmcnt(11) lgkmcnt(14)\n\t"
        "v_mfma_f32_32x32x16_bf16 v[0:15], v[48:51], v[136:139], v[0:15]\n\t"
        "ds_read_b64_tr_b16 v[48:49], v195 offset:45056\n\t"
        "ds_read_b64_tr_b16 v[50:51], v195 offset:46080\n\t"
        "s_waitcnt lgkmcnt(14)\n\t"
        "v_mfma_f32_32x32x16_bf16 v[16:31], v[52:55], v[136:139], v[16:31]\n\t"
        "ds_read_b64_tr_b16 v[52:53], v196 offset:45056\n\t"
        "ds_read_b64_tr_b16 v[54:55], v196 offset:46080\n\t"
        "buffer_load_dwordx4 v[184:187], v192, s[40:43], s44 offen offset:2048 sc1\n\t"
        "s_waitcnt vmcnt(11) lgkmcnt(14)\n\t"
        "v_mfma_f32_32x32x16_bf16 v[0:15], v[56:59], v[140:143], v[0:15]\n\t"
        "ds_read_b64_tr_b16 v[56:57], v195 offset:47104\n\t"
        "ds_read_b64_tr_b16 v[58:59], v195 offset:48128\n\t"
        "s_waitcnt lgkmcnt(14)\n\t"
        "v_mfma_f32_32x32x16_bf16 v[16:31], v[60:63], v[140:143], v[16:31]\n\t"
        "ds_read_b64_tr_b16 v[60:61], v196 offset:47104\n\t"
        "ds_read_b64_tr_b16 v[62:63], v196 offset:48128\n\t"
        "buffer_load_dwordx4 v[188:191], v192, s[40:43], s44 offen offset:3072 sc1\n\t"
        "s_waitcnt vmcnt(11) lgkmcnt(14)\n\t"
        "v_mfma_f32_32x32x16_bf16 v[0:15], v[32:35], v[144:147], v[0:15]\n\t"
        "ds_read_b64_tr_b16 v[32:33], v195 offset:49152\n\t"
        "ds_read_b64_tr_b16 v[34:35], v195 offset:50176\n\t"
        "s_waitcnt lgkmcnt(14)\n\t"
        "v_mfma_f32_32x32x16_bf16 v[16:31], v[36:39], v[144:147], v[16:31]\n\t"
        "ds_read_b64_tr_b16 v[36:37], v196 offset:49152\n\t"
        "ds_read_b64_tr_b16 v[38:39], v196 offset:50176\n\t"
        "s_waitcnt vmcnt(10) lgkmcnt(14)\n\t"
        "v_mfma_f32_32x32x16_bf16 v[0:15], v[40:43], v[148:151], v[0:15]\n\t"
        "ds_read_b64_tr_b16 v[40:41], v195 offset:51200\n\t"
        "ds_read_b64_tr_b16 v[42:43], v195 offset:52224\n\t"
        "s_waitcnt lgkmcnt(14)\n\t"
        "v_mfma_f32_32x32x16_bf16 v[16:31], v[44:47], v[148:151], v[16:31]\n\t"
        "ds_read_b64_tr_b16 v[44:45], v196 offset:51200\n\t"
        "ds_read_b64_tr_b16 v[46:47], v196 offset:52224\n\t"
        "s_waitcnt vmcnt(9) lgkmcnt(14)\n\t"
        "v_mfma_f32_32x32x16_bf16 v[0:15], v[48:51], v[152:155], v[0:15]\n\t"
        "ds_read_b64_tr_b16 v[48:49], v195 offset:53248\n\t"
        "ds_read_b64_tr_b16 v[50:51], v195 offset:54272\n\t"
        "s_waitcnt lgkmcnt(14)\n\t"
        "v_mfma_f32_32x32x16_bf16 v[16:31], v[52:55], v[152:155], v[16:31]\n\t"
        "ds_read_b64_tr_b16 v[52:53], v196 offset:53248\n\t"
        "ds_read_b64_tr_b16 v[54:55], v196 offset:54272\n\t"
        "s_waitcnt vmcnt(8) lgkmcnt(14)\n\t"
        "v_mfma_f32_32x32x16_bf16 v[0:15], v[56:59], v[156:159], v[0:15]\n\t"
        "ds_read_b64_tr_b16 v[56:57], v195 offset:55296\n\t"
        "ds_read_b64_tr_b16 v[58:59], v195 offset:56320\n\t"
        "s_waitcnt lgkmcnt(14)\n\t"
        "v_mfma_f32_32x32x16_bf16 v[16:31], v[60:63], v[156:159], v[16:31]\n\t"
        "ds_read_b64_tr_b16 v[60:61], v196 offset:55296\n\t"
        "ds_read_b64_tr_b16 v[62:63], v196 offset:56320\n\t"
        "s_waitcnt vmcnt(7) lgkmcnt(14)\n\t"
        "v_mfma_f32_32x32x16_bf16 v[0:15], v[32:35], v[160:163], v[0:15]\n\t"
        "s_barrier\n\t"
        "ds_read_b64_tr_b16 v[32:33], v195 offset:57344\n\t"
        "ds_read_b64_tr_b16 v[34:35], v195 offset:58368\n\t"
        "s_waitcnt lgkmcnt(14)\n\t"
        "v_mfma_f32_32x32x16_bf16 v[16:31], v[36:39], v[160:163], v[16:31]\n\t"
        "ds_read_b64_tr_b16 v[36:37], v196 offset:57344\n\t"
        "ds_read_b64_tr_b16 v[38:39], v196 offset:58368\n\t"
        "s_waitcnt vmcnt(6) lgkmcnt(14)\n\t"
        "v_mfma_f32_32x32x16_bf16 v[0:15], v[40:43], v[164:167], v[0:15]\n\t"
        "ds_read_b64_tr_b16 v[40:41], v195 offset:59392\n\t"
        "ds_read_b64_tr_b16 v[42:43], v195 offset:60416\n\t"
        "s_waitcnt lgkmcnt(14)\n\t"
        "v_mfma_f32_32x32x16_bf16 v[16:31], v[44:47], v[164:167], v[16:31]\n\t"
        "ds_read_b64_tr_b16 v[44:45], v196 offset:59392\n\t"
        "ds_read_b64_tr_b16 v[46:47], v196 offset:60416\n\t"
        "s_waitcnt vmcnt(5) lgkmcnt(14)\n\t"
        "v_mfma_f32_32x32x16_bf16 v[0:15], v[48:51], v[168:171], v[0:15]\n\t"
        "ds_read_b64_tr_b16 v[48:49], v195 offset:61440\n\t"
        "ds_read_b64_tr_b16 v[50:51], v195 offset:62464\n\t"
        "s_waitcnt lgkmcnt(14)\n\t"
        "v_mfma_f32_32x32x16_bf16 v[16:31], v[52:55], v[168:171], v[16:31]\n\t"
        "ds_read_b64_tr_b16 v[52:53], v196 offset:61440\n\t"
        "ds_read_b64_tr_b16 v[54:55], v196 offset:62464\n\t"
        "s_waitcnt vmcnt(4) lgkmcnt(14)\n\t"
        "v_mfma_f32_32x32x16_bf16 v[0:15], v[56:59], v[172:175], v[0:15]\n\t"
        "ds_read_b64_tr_b16 v[56:57], v195 offset:63488\n\t"
        "ds_read_b64_tr_b16 v[58:59], v195 offset:64512\n\t"
        "s_waitcnt lgkmcnt(14)\n\t"
        "v_mfma_f32_32x32x16_bf16 v[16:31], v[60:63], v[172:175], v[16:31]\n\t"
        "ds_read_b64_tr_b16 v[60:61], v196 offset:63488\n\t"
        "ds_read_b64_tr_b16 v[62:63], v196 offset:64512\n\t"
        "s_waitcnt vmcnt(3) lgkmcnt(14)\n\t"
        "v_mfma_f32_32x32x16_bf16 v[0:15], v[32:35], v[176:179], v[0:15]\n\t"
        "s_waitcnt lgkmcnt(12)\n\t"
        "v_mfma_f32_32x32x16_bf16 v[16:31], v[36:39], v[176:179], v[16:31]\n\t"
        "s_waitcnt vmcnt(2) lgkmcnt(10)\n\t"
        "v_mfma_f32_32x32x16_bf16 v[0:15], v[40:43], v[180:183], v[0:15]\n\t"
        "s_waitcnt lgkmcnt(8)\n\t"
        "v_mfma_f32_32x32x16_bf16 v[16:31], v[44:47], v[180:183], v[16:31]\n\t"
        "s_waitcnt vmcnt(1) lgkmcnt(6)\n\t"
        "v_mfma_f32_32x32x16_bf16 v[0:15], v[48:51], v[184:187], v[0:15]\n\t"
        "s_waitcnt lgkmcnt(4)\n\t"
        "v_mfma_f32_32x32x16_bf16 v[16:31], v[52:55], v[184:187], v[16:31]\n\t"
        "s_waitcnt vmcnt(0) lgkmcnt(2)\n\t"
        "v_mfma_f32_32x32x16_bf16 v[0:15], v[56:59], v[188:191], v[0:15]\n\t"
        "s_waitcnt lgkmcnt(0)\n\t"
        "v_mfma_f32_32x32x16_bf16 v[16:31], v[60:63], v[188:191], v[16:31]\n\t"
        "s_nop 11\n\t"
        : "={v[0:15]}"(acc[0]), "={v[16:31]}"(acc[1]), "+{s44}"(soff) STAMP_OUTS
        : "{v192}"(OFF_OF + L * (5 * MBY) + tile * TS + lane * 16), "{v193}"(a0), "{v194}"(a1), "{v195}"(a0 + 65536), "{v196}"(a1 + 65536),
          "{v198}"(lbase + L_W + 20), "{v199}"(L + 1), "{s[40:43]}"(srd)
        : "v32", "v33", "v34", "v35", "v36", "v37", "v38", "v39", "v40", "v41", "v42", "v43", "v44", "v45", "v46", "v47", "v48", "v49", "v50", "v51", "v52", "v53", "v54", "v55", "v56", "v57", "v58", "v59", "v60", "v61", "v62", "v63", "v64", "v65", "v66", "v67", "v68", "v69", "v70", "v71", "v72", "v73", "v74", "v75", "v76", "v77", "v78", "v79", "v80", "v81", "v82", "v83", "v84", "v85", "v86", "v87", "v88", "v89", "v90", "v91", "v92", "v93", "v94", "v95", "v96", "v97", "v98", "v99", "v100", "v101", "v102", "v103", "v104", "v105", "v106", "v107", "v108", "v109", "v110", "v111", "v112", "v113", "v114", "v115", "v116", "v117", "v118", "v119", "v120", "v121", "v122", "v123", "v124", "v125", "v126", "v127", "v128", "v129", "v130", "v131", "v132", "v133", "v134", "v135", "v136", "v137", "v138", "v139", "v140", "v141", "v142", "v143", "v144", "v145", "v146", "v147", "v148", "v149", "v150", "v151", "v152", "v153", "v154", "v155", "v156", "v157", "v158", "v159", "v160", "v161", "v162", "v163", "v164", "v165", "v166", "v167", "v168", "v169", "v170", "v171", "v172", "v173", "v174", "v175", "v176", "v177", "v178", "v179", "v180", "v181", "v182", "v183", "v184", "v185", "v186", "v187", "v188", "v189", "v190", "v191", "v200", "s45", "vcc", "scc", "memory");
      }
      PHASE_VARS;
      STAMP_PUT(12 + 16 * L);
      STAMPV(15 + 16 * L);
      float s = 0.f, sq = 0.f, d = 0.f;
      const unsigned xdst = (L ? OOB : OFF_X1 + tile * TS + tn * 4096) + lane * 16;
#pragma unroll
      for (int g = 0; g < 8; ++g) {
#pragma unroll
        for (int e = 0; e < 2; ++e) {
          const unsigned rr = res[g >> 1][2 * (g & 1) + e];
          const unsigned t = pack2(acc[g >> 2][4 * (g & 3) + 2 * e], acc[g >> 2][4 * (g & 3) + 2 * e + 1]);
          x1p[g >> 1][2 * (g & 1) + e] = pack2(lo16(rr) + lo16(t), hi16(rr) + hi16(t));
        }
        if (g & 1) { stg(rs, xdst + (g >> 1) * 1024, x1p[g >> 1]); SBAR(); }
      }
#pragma unroll
      for (int g = 0; g < 8; ++g)
#pragma unroll
        for (int e = 0; e < 2; ++e) {
          const unsigned o = x1p[g >> 1][2 * (g & 1) + e];
          s = dot2(o, 0x3f803f80u, s); sq = dot2(o, o, sq); d = dot2(o, wrc[g][e], d);
        }
      const u32x4 pst = {nzf(half_swap_sum(s)), nzf(half_swap_sum(sq)), nzf(half_swap_sum(d)), 0u};
      asm volatile("buffer_store_dwordx4 %0, %1, %2, 0 offen sc1\n\ts_waitcnt vmcnt(1)\n\tds_write_b32 %3, %4"
                   :: "v"(pst), "v"(stats_dst(sx, tile, tn, lane)), "s"(srd), "v"(lbase + L_W + 64 + 4 * wid), "v"(3) : "memory");
    }
    STAMPV(16 + 16 * L);
  }
  {
    PHASE_VARS;
    float S, Q, Dd, mean, var, mb1, rd1;
    __builtin_amdgcn_s_sleep(13);
    row_stats_sum(rs, OFF_SX + SX_LAYER, tile, lane, S, Q, Dd);
    STAMPV(33);
    row_stats_fin(smem, 2, tile, lane, S, Q, Dd, U2F(words[0]), x1p, mean, var, mb1, rd1);
    STAMPV(34);
    u32x4 xo[4];
#pragma unroll
    for (int i = 0; i < 4; ++i) {
#pragma unroll
      for (int e = 0; e < 4; ++e) {
        const unsigned h2 = ln_pair(x1p[i][e], mb1, rd1);
        xo[i][e] = pack2(lo16(x1p[i][e]) + lo16(h2), hi16(x1p[i][e]) + hi16(h2));
      }
      *reinterpret_cast<u32x4*>(smem + wid * 4096 + r32 * 128 + ((2 * i + hi) ^ (r32 & 7)) * 16) = fo_swap(xo[i]);
    }
    asm volatile("s_waitcnt lgkmcnt(0)" ::: "memory");
#pragma unroll
    for (int i = 0; i < 4; ++i) { xo[i] = *reinterpret_cast<const u32x4*>(smem + wid * 4096 + i * 1024 + (lane >> 3) * 128 + ((lane ^ (lane >> 3)) & 7) * 16); PIN(xo[i]); }
    u16* dst = out + (size_t)(tile * 32 + (lane >> 3)) * D_ + tn * 64 + (lane & 7) * 8;
    lds_wait_ge(lbase + L_W + 4 + 4 * (lane & 1), 2);
    const int rel = (int)words[3] - tile * 32 - (lane >> 3);
#pragma unroll
    for (int i = 0; i < 4; ++i)
      if (rel != 8 * i) st16(dst + (size_t)i * 8 * D_, xo[i]);
    STAMPV(35);
    STAMP_END;
  }
}

extern "C" __global__ void end_marker() {}

extern "C" void kernel_launch(void* const* d_in, const int* in_sizes, int n_in, void* d_out, int out_size,
                              void* d_ws, size_t ws_size, hipStream_t stream) {
  char* ws = (char*)d_ws;
  mega_kernel<<<256, 512, 0, stream>>>((const u16*)d_in[0], (const u16*)d_in[1], (const u16*)d_in[2], (const u16*)d_in[4], (const u16*)d_in[5],
                                       (const u16*)d_in[6], (const u16*)d_in[7], ws, (u16*)d_out);
}
```

```cpp
#include <hip/hip_runtime.h>
#include <hip/hip_bf16.h>
#include <stdint.h>

typedef unsigned short u16;
typedef short s16x4 __attribute__((ext_vector_type(4)));
typedef short s16x8 __attribute__((ext_vector_type(8)));
typedef float f32x16 __attribute__((ext_vector_type(16)));
typedef float f32x8 __attribute__((ext_vector_type(8)));
typedef float f32x4 __attribute__((ext_vector_type(4)));
typedef float f32x2 __attribute__((ext_vector_type(2)));
typedef unsigned int u32x16 __attribute__((ext_vector_type(16)));
typedef unsigned int u32x8 __attribute__((ext_vector_type(8)));
typedef unsigned int u32x4 __attribute__((ext_vector_type(4)));
typedef unsigned int u32x2 __attribute__((ext_vector_type(2)));
typedef __bf16 bf16x2 __attribute__((ext_vector_type(2)));
typedef __bf16 bf16x8 __attribute__((ext_vector_type(8)));
typedef __bf16 bf16x16 __attribute__((ext_vector_type(16)));

#define DI __device__ __forceinline__
#define LDS_PTR(T, p) ((__attribute__((address_space(3))) T*)(p))
#define BARRIER() asm volatile("s_barrier" ::: "memory")
#define BAR_LGKM() asm volatile("s_waitcnt lgkmcnt(0)\n\ts_barrier" ::: "memory")
#define BAR_ALL() asm volatile("s_waitcnt vmcnt(0) lgkmcnt(0)\n\ts_barrier" ::: "memory")
#define DRAIN() asm volatile("s_waitcnt vmcnt(0)" ::: "memory")
#define PACE() ({ asm volatile("s_sleep 1\n\ts_waitcnt vmcnt(3)" ::: "memory"); })
#define MFMA32(a, b, c) __builtin_amdgcn_mfma_f32_32x32x16_bf16(a, b, c, 0, 0, 0)
#define MFMA16(a, b, c) __builtin_amdgcn_mfma_f32_16x16x32_bf16(a, b, c, 0, 0, 0)
#define PIN(x) asm volatile("" : "+v"(x))
#define EX2(x) __builtin_amdgcn_exp2f(x)
#define RCP(x) __builtin_amdgcn_rcpf(x)
#define SBAR() __builtin_amdgcn_sched_barrier(0)

#define STAMP_INIT
#define STAMPV(i)
#define STAMP_END
#define STAMP_DECL
#define STAMP_OUTS
#define STAMP_PUT(i)
constexpr int D_ = 1024, F_ = 3584;

constexpr unsigned MBY = 1u << 20;
constexpr unsigned TS = 65536 + 4096, HS = 262144 + 4096;
constexpr unsigned OFF_HF = 0, OFF_X1 = 5 * MBY;
constexpr unsigned OFF_OF = 10 * MBY;
constexpr unsigned OFF_KV = 20 * MBY;
constexpr unsigned OFF_FL = 40 * MBY;
constexpr unsigned FL_EPOCH = OFF_FL;
constexpr unsigned FL_KV = OFF_FL + 4096;
constexpr unsigned FL_O = FL_KV + 65536;
constexpr unsigned FL_X = FL_O + 65536;
constexpr unsigned OFF_CS = 41 * MBY;
constexpr unsigned OFF_SX = 42 * MBY;
constexpr unsigned SX_ST = 0;
constexpr unsigned SX_ROW = 512 * 1024;
constexpr unsigned SX_CAND = SX_ROW + 64 * 1024;
constexpr unsigned SX_GP = SX_CAND + 32 * 1024;
constexpr unsigned SX_YP = SX_GP + 224 * 1024;
constexpr unsigned SX_LAYER = SX_YP + 224 * 1024;
constexpr unsigned SX_QP = 2 * SX_LAYER;
constexpr unsigned SX_BYTES = SX_QP + 512 * 1024;
static_assert(SX_BYTES <= 6 * 256 * 128 * 16, "sentinel fill");
constexpr unsigned WS_BYTES = 48 * MBY, OOB = 0x80000000u;
constexpr unsigned SENT = 0xFFFFFFFFu;

constexpr int NS = 9, DD = 3, SLOTB = 12288;
constexpr int L_XCH = 122880;
constexpr int L_SL = L_XCH + 8192;
constexpr int L_CAND = L_XCH + 32768;
constexpr int L_AB = L_XCH + 34816;
constexpr int L_FULL = 159744, L_FREE = L_FULL + 512;
constexpr int L_CS = L_FREE + 1024, L_PATCH = L_CS + 768;
constexpr int L_GU = L_PATCH + 768, L_TAG = L_GU + 256, L_WR = L_TAG + 256;
constexpr int L_X2F = L_WR + 128, L_X2P = L_X2F + 128;
constexpr int L_W = L_X2P + 128;
constexpr int LDS_BYTES = 163840;
static_assert(NS * SLOTB <= 54 * 2048 && L_W + 128 <= LDS_BYTES && (NS + 1) * SLOTB <= L_XCH && L_AB + 2048 <= L_FULL, "LDS map");

DI int fresh_lane() { int l; asm volatile("v_mbcnt_lo_u32_b32 %0, -1, 0\n\tv_mbcnt_hi_u32_b32 %0, -1, %0" : "=v"(l)); return l; }
#define LANE_VARS const int lane = fresh_lane(), r32 = lane & 31, hi = lane >> 5, tq = (lane & 15) >> 2, tp = lane & 3, tblk = (lane >> 4) & 1, \
  wrow = 4 * hi + tq, wsw = 4 * ((wrow >> 1) & 1), wch = 2 * tblk + (tp >> 1), c24 = r32 < 24 ? r32 : 23; \
  (void)r32; (void)hi; (void)wsw; (void)wch; (void)c24
DI int fresh_s(int v) { asm volatile("" : "+s"(v)); return v; }
#define PHASE_VARS const int tm = fresh_s(tm_), tn = fresh_s(tn_), wid = fresh_s(wid_), tile = tm * 4 + wid, hh = wid & 1, kh = wid >> 1; (void)tile; (void)hh; (void)kh; LANE_VARS
DI float bf2f(u16 v) { return __uint_as_float(((unsigned)v) << 16); }
DI unsigned pack2(float lo, float hi) { f32x2 v = {lo, hi}; return __builtin_bit_cast(unsigned, __builtin_convertvector(v, bf16x2)); }
DI float rbf(float f) { return __uint_as_float(pack2(f, 0.f) << 16); }
DI float lo16(unsigned u) { return __uint_as_float(u << 16); }
DI float hi16(unsigned u) { return __uint_as_float(u & 0xffff0000u); }
DI float opq(float r) { asm("" : "+v"(r)); return r; }
DI float fadd(float a, float b) { return opq(a + b); }
DI float fmul(float a, float b) { return opq(a * b); }
DI float ffma(float a, float b, float c) { return opq(__builtin_fmaf(a, b, c)); }
DI float U2F(unsigned u) { return __uint_as_float(u); }
DI float half_swap_sum(float v) {
  auto r = __builtin_amdgcn_permlane32_swap(__float_as_uint(v), __float_as_uint(v), false, false);
  return fadd(U2F(r[0]), U2F(r[1]));
}
DI float half_swap_max(float v) {
  auto r = __builtin_amdgcn_permlane32_swap(__float_as_uint(v), __float_as_uint(v), false, false);
  return fmaxf(__uint_as_float(r[0]), __uint_as_float(r[1]));
}
template <int CTRL> DI unsigned dpp(unsigned v) { return (unsigned)__builtin_amdgcn_update_dpp(0, (int)v, CTRL, 0xf, 0xf, false); }
DI float row_swap_sum(float v) {
  auto r = __builtin_amdgcn_permlane16_swap(__float_as_uint(v), __float_as_uint(v), false, false);
  return fadd(U2F(r[0]), U2F(r[1]));
}
DI float half_sum(float v) {
  v = fadd(v, U2F(dpp<0xB1>(__float_as_uint(v)))); v = fadd(v, U2F(dpp<0x4E>(__float_as_uint(v))));
  v = fadd(v, U2F(dpp<0x141>(__float_as_uint(v)))); v = fadd(v, U2F(dpp<0x140>(__float_as_uint(v))));
  return row_swap_sum(v);
}
DI float wave_sum(float v) { return half_swap_sum(half_sum(v)); }
DI unsigned half_max_u32(unsigned v) {
  v = max(v, dpp<0xB1>(v)); v = max(v, dpp<0x4E>(v)); v = max(v, dpp<0x141>(v)); v = max(v, dpp<0x140>(v));
  auto r = __builtin_amdgcn_permlane16_swap(v, v, false, false);
  return max(r[0], r[1]);
}
DI unsigned wave_max_u32(unsigned v) {
  v = half_max_u32(v);
  auto r = __builtin_amdgcn_permlane32_swap(v, v, false, false);
  return max(r[0], r[1]);
}
DI u32x4 fo_swap(u32x4 v) {
  auto a = __builtin_amdgcn_permlane32_swap(v[0], v[2], false, false);
  auto b = __builtin_amdgcn_permlane32_swap(v[1], v[3], false, false);
  u32x4 o; o[0] = a[0]; o[1] = b[0]; o[2] = a[1]; o[3] = b[1];
  return o;
}
DI void glds16(const void* gsrc, unsigned lds_dst) {
  unsigned keep;
  asm volatile("s_mov_b32 %0, m0\n\ts_mov_b32 m0, %2\n\ts_nop 0\n\tglobal_load_lds_dwordx4 %1, off\n\ts_mov_b32 m0, %0"
               : "=&s"(keep) : "v"(gsrc), "s"(lds_dst) : "memory");
}
#define GL_ "global_load_lds_dwordx4 "
DI void glds_v4(const void* sb, const unsigned (&v)[4], unsigned m0v) {
  unsigned keep;
  asm volatile("s_mov_b32 %0, m0\n\ts_mov_b32 m0, %6\n\ts_nop 0\n\t" GL_ "%1, %5\n\t" GL_ "%2, %5 offset:1024\n\t" GL_ "%3, %5 offset:2048\n\t"
               GL_ "%4, %5 offset:3072\n\ts_mov_b32 m0, %0" : "=&s"(keep) : "v"(v[0]), "v"(v[1]), "v"(v[2]), "v"(v[3]), "s"(sb), "s"(m0v) : "memory");
}
#define GP_ "\n\ts_sleep %8\n\ts_waitcnt vmcnt(%7)\n\t"
template <int TH, int SL> DI void glds_v4t(const void* sb, const unsigned (&v)[4], unsigned m0v) {
  unsigned keep;
  asm volatile("s_mov_b32 %0, m0\n\ts_mov_b32 m0, %6\n\ts_nop 0\n\t" GL_ "%1, %5" GP_ GL_ "%2, %5 offset:1024" GP_ GL_ "%3, %5 offset:2048" GP_ GL_ "%4, %5 offset:3072" GP_ "s_mov_b32 m0, %0"
               : "=&s"(keep) : "v"(v[0]), "v"(v[1]), "v"(v[2]), "v"(v[3]), "s"(sb), "s"(m0v), "n"(TH), "n"(SL) : "memory");
}
DI void glds_v3(const void* sb, const unsigned (&v)[3], unsigned m0v) {
  unsigned keep;
  asm volatile("s_mov_b32 %0, m0\n\ts_mov_b32 m0, %5\n\ts_nop 0\n\t" GL_ "%1, %4\n\t" GL_ "%2, %4 offset:1024\n\t" GL_ "%3, %4 offset:2048\n\t"
               "s_mov_b32 m0, %0" : "=&s"(keep) : "v"(v[0]), "v"(v[1]), "v"(v[2]), "s"(sb), "s"(m0v) : "memory");
}
DI float dot2(unsigned a, unsigned b, float c) {
  return __builtin_amdgcn_fdot2_f32_bf16(__builtin_bit_cast(bf16x2, a), __builtin_bit_cast(bf16x2, b), c, false);
}
#define CODE_ADDR(sym, out) do { unsigned long long pc_; unsigned off_; \
  asm volatile("s_getpc_b64 %0\n1:\n\ts_mov_b32 %1, " #sym "-1b" : "=s"(pc_), "=s"(off_)); out = (const char*)(pc_ + (long long)(int)off_); } while (0)
DI void st16(void* p, u32x4 v) { asm volatile("global_store_dwordx4 %0, %1, off sc0 sc1\n\ts_nop 1" :: "v"(p), "v"(v) : "memory"); }
DI void ln_scalars(float mean, float var, float& mb, float& den) { mb = rbf(mean); den = rbf(__builtin_amdgcn_sqrtf(rbf(rbf(var) + 1e-5f))); }
DI unsigned ln_pair(unsigned v, float mb, float rd) {
  const unsigned c = pack2(lo16(v) - mb, hi16(v) - mb);
  return pack2(lo16(c) * rd, hi16(c) * rd);
}
DI unsigned ln_pair_s(unsigned v, float nmb, float rd) {
  const unsigned c = pack2(fadd(lo16(v), nmb), fadd(hi16(v), nmb));
  return pack2(fmul(lo16(c), rd), fmul(hi16(c), rd));
}
template <int N, int M> DI f32x4 sum4(const u32x4 (&v)[M]) {
  f32x4 t;
#pragma unroll
  for (int e = 0; e < 4; ++e) {
    t[e] = U2F(v[0][e]);
#pragma unroll
    for (int i = 1; i < N; ++i) t[e] = fadd(t[e], U2F(v[i][e]));
  }
  return t;
}
#define PERM_LO(x, y) __builtin_amdgcn_perm(y, x, 0x05040100u)
#define PERM_HI(x, y) __builtin_amdgcn_perm(y, x, 0x07060302u)
constexpr unsigned SPIN_CAP = 1u << 15;
DI unsigned nz(unsigned v) { return min(v, SENT - 1u); }
DI unsigned nzf(float v) { return nz(__float_as_uint(v)); }
DI unsigned mx4(unsigned m, const u32x4& v) { return max(max(max(m, v[0]), v[1]), max(v[2], v[3])); }
template <int N> DI bool none_sent(const u32x4 (&v)[N]) {
  unsigned m = 0u;
#pragma unroll
  for (int i = 0; i < N; ++i) m = mx4(m, v[i]);
  return m != SENT;
}
typedef __amdgpu_buffer_rsrc_t rsrc_t;
DI u32x4 ldg(rsrc_t rs, unsigned off) { return __builtin_amdgcn_raw_buffer_load_b128(rs, (int)off, 0, 16); }
DI void stg(rsrc_t rs, unsigned off, u32x4 v) { __builtin_amdgcn_raw_buffer_store_b128(v, rs, (int)off, 0, 16); }
DI void poll_pause(bool slow) { if (slow) __builtin_amdgcn_s_sleep(4); asm volatile("s_sleep 1" ::: "memory"); }
template <int N, class LD, class OKF> DI void poll_all(u32x4 (&out)[N], bool slow, LD ld, OKF okf) {
  for (unsigned spins = 0;; ++spins) {
    ld(out);
    SBAR();
    if (__all(okf(out)) || spins > SPIN_CAP) break;
    poll_pause(slow);
  }
}
DI void lds_set(unsigned addr, unsigned v) { asm volatile("s_waitcnt lgkmcnt(0)\n\tds_write_b32 %0, %1" :: "v"(addr), "v"(v) : "memory"); }
template <int SLEEP = 1> DI void lds_wait_ge(unsigned addr, unsigned need) {
  unsigned spin = 0x100000, v;
  asm volatile("1:\n\tds_read_b32 %0, %2\n\ts_sub_u32 %1, %1, 1\n\ts_waitcnt lgkmcnt(0)\n\ts_cbranch_scc1 2f\n\tv_cmp_gt_u32 vcc, %3, %0\n\ts_cbranch_vccz 2f\n\ts_sleep %4\n\ts_branch 1b\n2:"
               : "=&v"(v), "+s"(spin) : "v"(addr), "s"(need), "n"(SLEEP) : "vcc", "scc", "memory");
}
DI s16x8 tr2(const char* p, int second) {
  s16x4 lo = __builtin_amdgcn_ds_read_tr16_b64_v4i16(LDS_PTR(s16x4, p)), hi = __builtin_amdgcn_ds_read_tr16_b64_v4i16(LDS_PTR(s16x4, p + second));
  return __builtin_shufflevector(lo, hi, 0, 1, 2, 3, 4, 5, 6, 7);
}
DI u32x16 cat4(const u32x4& a, const u32x4& b, const u32x4& c, const u32x4& d) {
  const u32x8 x = __builtin_shufflevector(a, b, 0, 1, 2, 3, 4, 5, 6, 7), y = __builtin_shufflevector(c, d, 0, 1, 2, 3, 4, 5, 6, 7);
  return __builtin_shufflevector(x, y, 0, 1, 2, 3, 4, 5, 6, 7, 8, 9, 10, 11, 12, 13, 14, 15);
}

DI unsigned stats_dst(unsigned sx, int tile, int tn, int lane) { return lane < 32 ? sx + SX_ST + tile * 8192 + tn * 512 + lane * 16 : OOB; }
DI void row_stats_sum(rsrc_t rs, unsigned sx, int tile, int lane, float& S, float& Q, float& Dd) {
  const unsigned exb = sx + SX_ST + tile * 8192 + (lane & 31) * 16;
  u32x4 pv[8];
  poll_all<8>(pv, false, [&](u32x4 (&v)[8]) {
#pragma unroll
    for (int i = 0; i < 8; ++i) v[i] = ldg(rs, exb + ((lane >> 5) * 8 + i) * 512);
  }, [&](const u32x4 (&v)[8]) { return none_sent(v); });
  S = 0.f; Q = 0.f; Dd = 0.f;
#pragma unroll
  for (int i = 0; i < 8; ++i) { S += __uint_as_float(pv[i][0]); Q += __uint_as_float(pv[i][1]); Dd += __uint_as_float(pv[i][2]); }
}
DI void row_stats_fin(char* smem, int epoch, int tile, int lane, float S, float Q, float Dd, float wsum, const u32x4 (&x1p)[4],
                      float& mean, float& var, float& mb, float& rd) {
  S = half_swap_sum(S); Q = half_swap_sum(Q); Dd = half_swap_sum(Dd);
  mean = S * (1.0f / 1024.0f); var = fmaxf(Q * (1.0f / 1024.0f) - mean * mean, 0.f);
  float den;
  ln_scalars(mean, var, mb, den);
  rd = RCP(den);
  const unsigned rb = pack2((Dd - mb * wsum) * rd, 0.f) & 0xffffu;
  const unsigned mine = (((rb & 0x8000u) ? (~rb & 0xffffu) : (rb | 0x8000u)) << 16) | (unsigned)(2047 - (tile * 32 + (lane & 31)));
  const bool best = half_max_u32(mine) == mine;
  if (best) {
    char* c = smem + L_CAND + (tile & 3) * 144;
#pragma unroll
    for (int i = 0; i < 4; ++i) *reinterpret_cast<u32x4*>(c + (2 * i + (lane >> 5)) * 16) = x1p[i];
    *reinterpret_cast<u32x4*>(c + 128) = u32x4{mine, __float_as_uint(mb), __float_as_uint(rd), 0u};
  }
  lds_set((unsigned)(uintptr_t)smem + L_W + 32 + (tile & 3) * 4, epoch);
}

DI void attn_safe(rsrc_t rs, unsigned kvoff, const u32x8& q, const u32x4& ones, f32x16& o) {
  const s16x8 q0 = __builtin_bit_cast(s16x8, __builtin_shufflevector(q, q, 0, 1, 2, 3)), q1 = __builtin_bit_cast(s16x8, __builtin_shufflevector(q, q, 4, 5, 6, 7));
  f32x4 l = {0.f, 0.f, 0.f, 0.f};
#pragma unroll
  for (int e = 0; e < 16; ++e) o[e] = 0.f;
  float m = -INFINITY;
#pragma unroll 1
  for (int kt = 0; kt < 64; ++kt) {
    const unsigned p = kvoff + kt * 4096;
    const u32x4 k0 = ldg(rs, p), k1 = ldg(rs, p + 1024), v0 = ldg(rs, p + 2048), v1 = ldg(rs, p + 3072);
    f32x16 s = {};
    s = MFMA32(__builtin_bit_cast(s16x8, k0), q0, s);
    s = MFMA32(__builtin_bit_cast(s16x8, k1), q1, s);
    float mt = fmaxf(s[0], s[1]);
#pragma unroll
    for (int e = 2; e < 16; ++e) mt = fmaxf(mt, s[e]);
    const float mn = fmaxf(m, half_swap_max(mt));
    const float alpha = EX2(m - mn);
    m = mn;
#pragma unroll
    for (int e = 0; e < 16; ++e) { s[e] = EX2(s[e] - mn); o[e] *= alpha; }
    l[0] *= alpha;
    const bf16x16 pa = __builtin_bit_cast(bf16x16, __builtin_shufflevector(s, s, 0, 1, 2, 3, 4, 5, 6, 7));
    const bf16x16 pb = __builtin_bit_cast(bf16x16, __builtin_shufflevector(s, s, 8, 9, 10, 11, 12, 13, 14, 15));
    o = __builtin_amdgcn_smfmac_f32_32x32x32_bf16(__builtin_bit_cast(bf16x8, v0), pa, o, 0xdddd, 0, 0);
    l = __builtin_amdgcn_smfmac_f32_16x16x64_bf16(__builtin_bit_cast(bf16x8, ones), pa, l, 0xdddd, 0, 0);
    o = __builtin_amdgcn_smfmac_f32_32x32x32_bf16(__builtin_bit_cast(bf16x8, v1), pb, o, 0xdddd, 0, 0);
    l = __builtin_amdgcn_smfmac_f32_16x16x64_bf16(__builtin_bit_cast(bf16x8, ones), pb, l, 0xdddd, 0, 0);
  }
  o *= 1.0f / l[0];
}

extern "C" __global__ __launch_bounds__(512) void mega_kernel(const u16* __restrict__ x0, const u16* __restrict__ wqkv, const u16* __restrict__ wo,
                                                              const u16* __restrict__ w_r, const u16* __restrict__ wg, const u16* __restrict__ wu,
                                                              const u16* __restrict__ wd, char* ws, u16* __restrict__ out) {
  __shared__ __attribute__((aligned(16))) char smem[LDS_BYTES];
  const int wid = __builtin_amdgcn_readfirstlane(threadIdx.x >> 6);
  STAMP_INIT;
  STAMPV(0);
  const int bid = blockIdx.x, xcd = bid & 7, loc = bid >> 3;
  const int tm = 8 * (xcd >> 2) + (loc >> 2), tn = 4 * (xcd & 3) + (loc & 3);
  const unsigned lbase = (unsigned)(uintptr_t)smem;
  const rsrc_t rs = __builtin_amdgcn_make_buffer_rsrc(ws, 0, (int)WS_BYTES, 0x00020000);
  volatile __attribute__((address_space(3))) unsigned* words = LDS_PTR(volatile unsigned, smem + L_W);
  *reinterpret_cast<unsigned*>(smem + L_FULL + threadIdx.x * 4) = 0u;
  words[threadIdx.x & 31] = 0u;
  BAR_LGKM();
  const int lw = wid & 1;
  const char* wb = reinterpret_cast<const char*>(wqkv + (size_t)(16 * lw) * (3 * D_) + tn * 64);
  auto stage_lanes = [](unsigned (&wv)[3]) {
    const int lane = fresh_lane();
#pragma unroll
    for (int i = 0; i < 3; ++i) {
      const int li = lane + 64 * i, k = li / 24, cp = li % 24, c = cp ^ (4 * ((k >> 1) & 1));
      wv[i] = (unsigned)(k * (3 * D_ * 2) + (c >> 3) * (D_ * 2) + (c & 7) * 16 - i * 1024);
    }
  };
  auto stage = [&](const unsigned (&wv)[3], int s, int slot) {
    const char* p = wb + (size_t)((s + 2 * tn) & 31) * (32 * 3 * D_ * 2);
    glds_v3(p, wv, lbase + slot * SLOTB + lw * 6144);
    glds_v3(p + 8 * (3 * D_ * 2), wv, lbase + slot * SLOTB + lw * 6144 + 3072);
  };
  auto load_tag = [&]() {
    u32x2 t = __builtin_amdgcn_raw_buffer_load_b64(rs, FL_EPOCH, 0, 16);
    t[0] += 1u; t[1] += 0x9E3779B9u;
    PIN(t);
    return t;
  };
  auto exchange = [&](const u32x2& tag, unsigned line0, int me, bool bar = false) {
    const int lane = fresh_lane();
    __builtin_amdgcn_raw_buffer_store_b64(tag, rs, lane == 0 ? line0 + me * 128 : OOB, 0, 16);
    if (bar) BARRIER();
    if (1) {
      unsigned po = lane < 16 ? line0 + lane * 128 : OOB;
      const u32x4 srd = {(unsigned)(uintptr_t)ws, (unsigned)((uintptr_t)ws >> 32) & 0xffffu, WS_BYTES, 0x00020000u};
      for (unsigned spins = 0;; ++spins) {
        u32x2 v = {0u, 0u};
        asm volatile("buffer_atomic_or_x2 %0, %1, %2, 0 offen sc0 sc1\n\ts_waitcnt vmcnt(0)" : "+v"(v) : "v"(po), "s"(srd) : "memory");
        po = ((v[0] == tag[0]) & (v[1] == tag[1])) ? OOB : po;
        if (__all(po == OOB) || spins > SPIN_CAP) break;
      }
      return;
    }
    __builtin_amdgcn_s_sleep(8);
    for (unsigned spins = 0;; ++spins) {
      const u32x2 v = __builtin_amdgcn_raw_buffer_load_b64(rs, line0 + (lane & 15) * 128, 0, 16);
      if (__all((v[0] == tag[0]) & (v[1] == tag[1])) || spins > SPIN_CAP) break;
      asm volatile("s_sleep 1" ::: "memory");
    }
  };
  if (wid >= 4) {
    auto wo_rows = [&](int g0, int n, int gs) {
      const int lane = fresh_lane();
      unsigned wov[4];
#pragma unroll
      for (int i = 0; i < 4; ++i) wov[i] = (unsigned)((8 * i + (lane >> 3)) * (D_ * 2) + ((lane & 7) ^ (4 * ((lane >> 4) & 1))) * 16 - i * 1024);
#pragma unroll 1
      for (int i = 0, k = (2 * (tm & 7)) % n, g; i < n && !0; ++i, k = k + 1 == n ? 0 : k + 1) g = g0 + gs * k, glds_v4t<3, 1>(reinterpret_cast<const char*>(wo) + (size_t)((64 * tn + 32 * g) & 1023) * (D_ * 2) + tn * 128, wov, lbase + g * 4096);
    };
    if (wid < 6) {
      const int lane = fresh_lane();
      unsigned wv[3];
      stage_lanes(wv);
      __builtin_amdgcn_s_setprio(3);
      const unsigned fra = lbase + L_FREE + (lane & 3) * 256, fla = lbase + L_FULL + lw * 256 + lane * 4;
      const rsrc_t rw = __builtin_amdgcn_make_buffer_rsrc(const_cast<char*>(wb), 0, 0 ? 0 : 6 * MBY - (16 * lw * 3 * D_ + tn * 64) * 2, 0x00020000);
#pragma unroll 1
      for (int L = 0; L < 2; ++L) {
        const int s0 = NS, lead = L ? 8 : NS - 1;
        int slot = 0;
        if (L == 0) {
#pragma unroll 1
          for (int i = 0, s = tm & 7; i < 2 * NS; ++i) {
            glds_v3(wb + (size_t)((s + 2 * tn) & 31) * (32 * 3 * D_ * 2) + (i & 1) * (8 * 3 * D_ * 2), wv, lbase + s * SLOTB + lw * 6144 + (i & 1) * 3072);
            asm volatile("s_waitcnt vmcnt(6)" ::: "memory");
            if (i & 1) s = s + 1 == NS ? 0 : s + 1;
          }
          asm volatile("s_waitcnt vmcnt(0)\n\tds_write_b32 %0, %1" :: "v"(fla), "v"(NS) : "memory");
          STAMPV(1);
        }
#pragma unroll 1
        for (int s = NS; s < 32; ++s) {
          lds_wait_ge(fra, 32 * L + s - lead);
          stage(wv, s, slot);
          slot = (slot == NS - 1) ? 0 : slot + 1;
          if (s >= s0 + DD) asm volatile("s_waitcnt vmcnt(%0)\n\tds_write_b32 %1, %2" :: "n"(DD * 6), "v"(fla), "v"(32 * L + s - DD + 1) : "memory");
        }
#pragma unroll
        for (int d = DD - 1; d >= 0; --d) asm volatile("s_waitcnt vmcnt(%0)\n\tds_write_b32 %1, %2" :: "n"(d * 6), "v"(fla), "v"(32 * L + 32 - d) : "memory");
        u32x2 tag = *reinterpret_cast<const u32x2*>(smem + L_TAG);
        PIN(tag);
        if (lw == 0) {
          lds_wait_ge(lbase + L_W + 96 + 4 * (lane & 1), 1);
          lds_wait_ge<0>(lbase + L_W + 80 + 4 * (lane & 3), L + 1);
          exchange(tag, FL_KV + (L * 16 + tn) * 2048, tm, true);
          lds_wait_ge(fra, 32 * L + 32);
          lds_set(lbase + L_W + 16, L + 1);
        } else { BAR_LGKM(); lds_wait_ge<8>(lbase + L_W + 16, L + 1); }
        __builtin_amdgcn_s_sleep(24);
        wo_rows(lw, 15, 2);
        u32x4 pre[NS * 6];
        if (L == 0) {
#pragma unroll
          for (int s = 0; s < NS; ++s) {
#pragma unroll
            for (int i = 0; i < 6; ++i)
              pre[s * 6 + i] = __builtin_amdgcn_raw_buffer_load_b128(rw, wv[i % 3], ((s + 2 * tn) & 31) * (32 * 3 * D_ * 2) + (i / 3) * (8 * 3 * D_ * 2) + (i % 3) * 1024, 0), PACE();
          }
        } else {
#pragma unroll
          for (int i = 0; i < NS * 6; ++i) asm volatile("" : "=v"(pre[i]));
        }
        BAR_ALL();
        if (lw == 0) {
          exchange(tag, FL_O + (L * 16 + tm) * 2048, tn); lds_set(lbase + L_W + 20, L + 1);
          __builtin_amdgcn_raw_buffer_store_b64(tag, rs, (bid == 0 && lane == 0) ? FL_EPOCH : OOB, 0, 16);
        }
        else wo_rows(30, 2, 1);
        BAR_ALL();
        if (L == 0) {
          char* d = smem + lw * 6144 + fresh_lane() * 16;
#pragma unroll
          for (int s = 0; s < NS; ++s) {
#pragma unroll
            for (int i = 0; i < 6; ++i) *reinterpret_cast<u32x4*>(d + s * SLOTB + i * 1024) = pre[s * 6 + i];
            lds_set(fla, 32 + s + 1);
          }
          STAMPV(2);
          lds_wait_ge<2>(lbase + L_W + 24, 2);
        }
      }
      STAMP_END;
      return;
    }
    const int a = wid - 6;
    const bool ffn = loc < 28;
    const int grp = xcd * 7 + (ffn ? loc >> 2 : 0), qa = (loc & 3) * 2 + a;
    const int kr = loc, cg = xcd;
    const unsigned wa = lbase + L_W;
    if (a) {
      const int lane = fresh_lane();
      unsigned t = __builtin_amdgcn_raw_buffer_load_b32(rs, 1 ? lane * MBY + MBY - 128 : OOB, 0, 16);
      if (1) {
        const char *c0, *c1;
        CODE_ADDR(mega_kernel, c0); CODE_ADDR(end_marker, c1);
        const long long o = (loc * 64 + lane) * 32;
        t |= *reinterpret_cast<const __attribute__((address_space(1))) unsigned*>((uintptr_t)(c0 + (o + 4 <= c1 - c0 ? o : 0)));
      }
      const u32x2 tag = load_tag();
      *reinterpret_cast<u32x2*>(smem + L_TAG) = tag;
      PIN(t);
      lds_wait_ge<0>(wa + 64 + 4 * (lane & 3), 2);
      exchange(tag, FL_X + tm * 2048, tn);
      lds_set(wa + 24, 1);
    } else {
      unsigned wv[3];
      stage_lanes(wv);
#pragma unroll 1
      for (int i = 0; i < 4 * 0; ++i) {
        glds_v3(reinterpret_cast<const char*>(wqkv + tn * 64) + (size_t)((4 * (NS + 2 * tn + 3 * (tm & 7)) + i) & 127) * (8 * 3 * D_ * 2), wv, lbase + NS * SLOTB);
        asm volatile("s_waitcnt vmcnt(6)" ::: "memory");
      }
      STAMPV(11);
      lds_wait_ge<2>(wa + 24, 1);
    }
    __builtin_amdgcn_s_sleep(16);
    auto transpose8 = [](u32x4* v) {
      u32x4 t[8];
#pragma unroll
      for (int j = 0; j < 8; ++j)
#pragma unroll
        for (int e = 0; e < 4; ++e) t[j][e] = (j & 1) ? PERM_HI(v[2 * e][j >> 1], v[2 * e + 1][j >> 1]) : PERM_LO(v[2 * e][j >> 1], v[2 * e + 1][j >> 1]);
#pragma unroll
      for (int j = 0; j < 8; ++j) { v[j] = t[j]; PIN(v[j]); }
    };
    u32x4 gf[32];
    u32x4 wdf[16];
    auto wait_go = [&](int L) {
      lds_wait_ge<8>(lbase + L_W + 16, L + 1);
      __builtin_amdgcn_s_sleep(24);
    };
    auto load_wd = [&](unsigned dv) {
      const rsrc_t rd = __builtin_amdgcn_make_buffer_rsrc(const_cast<u16*>(wd) + (size_t)grp * 64 * D_ + 128 * qa, 0, 0 ? 0 : 64 * D_ * 2 - 256 * qa, 0x00020000);
#pragma unroll
      for (int i = 0; i < 16; ++i) {
        wdf[i] = __builtin_amdgcn_raw_buffer_load_b128(rd, dv, (32 * (i >> 3) + (i & 7)) * (D_ * 2), 0);
        asm volatile("s_sleep 2\n\ts_waitcnt vmcnt(3)" ::: "memory");
      }
    };
    {
      LANE_VARS;
      const u32x4 sv = {SENT, SENT, SENT, SENT};
#pragma unroll
      for (int i = 0; i < 6; ++i) {
        const unsigned o = ((i * 256 + bid) * 128 + a * 64 + lane) * 16;
        stg(rs, o < SX_BYTES ? OFF_SX + o : OOB, sv);
      }
      if (a == 0) {
        const u32x4 w0 = *reinterpret_cast<const u32x4*>(w_r + lane * 8), w1 = *reinterpret_cast<const u32x4*>(w_r + 512 + lane * 8);
        float t = 0.f;
#pragma unroll
        for (int e = 0; e < 4; ++e) t = fadd(fadd(t, fadd(lo16(w0[e]), hi16(w0[e]))), fadd(lo16(w1[e]), hi16(w1[e])));
        words[0] = __float_as_uint(wave_sum(t));
        if ((lane >> 3) == (tn & 7)) *reinterpret_cast<u32x4*>(smem + L_WR + (lane & 7) * 16) = tn < 8 ? w0 : w1;
      }
      const u16* p = wqkv + (size_t)(64 * tm + 32 * a + hi) * (3 * D_) + (c24 >> 3) * D_ + tn * 64 + (c24 & 7) * 8;
      float cs[8] = {0.f, 0.f, 0.f, 0.f, 0.f, 0.f, 0.f, 0.f};
      u32x4 v[16];
#pragma unroll
      for (int i = 0; i < 16; ++i) v[i] = *reinterpret_cast<const u32x4*>(p + (size_t)i * (2 * 3 * D_));
      SBAR();
#pragma unroll
      for (int i = 0; i < 16; ++i)
#pragma unroll
        for (int e = 0; e < 4; ++e) { cs[2 * e] = fadd(cs[2 * e], lo16(v[i][e])); cs[2 * e + 1] = fadd(cs[2 * e + 1], hi16(v[i][e])); }
      u32x4 o;
#pragma unroll
      for (int e = 0; e < 4; ++e) { const float x = half_swap_sum(cs[e]), y = half_swap_sum(cs[4 + e]); o[e] = __float_as_uint(hi ? y : x); }
      stg(rs, r32 < 24 ? OFF_CS + (tn * 32 + tm * 2 + a) * 768 + r32 * 32 + hi * 16 : OOB, o);
    }
    {
      DRAIN();
      lds_set(wa + 96 + 4 * a, 1);
      BAR_LGKM();
      LANE_VARS;
      const char* gp = reinterpret_cast<const char*>(((lane & 8) ? wu : wg) + (size_t)(128 * qa + 16 * hi + 4 * tblk) * F_ + 64 * grp + (lane & 7) * 8);
      unsigned dv = (lane >> 4) * (8 * D_ * 2) + (lane & 15) * 16;
      PIN(gp); PIN(dv);
      unsigned sv[3];
#pragma unroll
      for (int i = 0; i < 3; ++i) { const int li = lane + 64 * i, k = li / 24, c = li % 24; sv[i] = (unsigned)(k * (3 * D_ * 2) + (c >> 3) * (D_ * 2) + (c & 7) * 16 - i * 1024); PIN(sv[i]); }
      wait_go(0);
#pragma unroll
      for (int i = 0; i < 32; ++i) {
        gf[i] = *reinterpret_cast<const u32x4*>(gp + (size_t)(0 ? 0 : 32 * (i >> 3) + 8 * ((i >> 2) & 1) + (i & 3)) * (F_ * 2));
        asm volatile("s_sleep 2\n\ts_waitcnt vmcnt(3)" ::: "memory");
      }
      load_wd(dv);
      STAMPV(7);
      BARRIER();
      __builtin_amdgcn_s_sleep(2);
#pragma unroll
      for (int g = 0; g < 4; ++g) glds_v3(wqkv + (size_t)(32 * kr + 8 * g) * (3 * D_) + (2 * cg + a) * 64, sv, lbase + L_SL + a * 12288 + g * 3072);
      transpose8(gf); transpose8(gf + 8); transpose8(gf + 16); transpose8(gf + 24);
      transpose8(wdf); transpose8(wdf + 8);
      {
        LANE_VARS;
        f32x4 t[2];
#pragma unroll
        for (int c = 0; c < 2; ++c) {
          u32x4 v[8];
#pragma unroll
          for (int i = 0; i < 8; ++i) v[i] = ldg(rs, OFF_CS + (tn * 32 + 16 * c + 2 * i + hi) * 768 + a * 384 + c24 * 16);
          SBAR();
          t[c] = sum4<8>(v);
          SBAR();
        }
#pragma unroll
        for (int e = 0; e < 4; ++e) t[0][e] = half_swap_sum(fadd(t[0][e], t[1][e]));
        if (lane < 24) *reinterpret_cast<f32x4*>(smem + L_CS + a * 384 + lane * 16) = t[0];
      }
      u32x2 tag = *reinterpret_cast<const u32x2*>(smem + L_TAG);
      PIN(tag);
      BAR_ALL();
      if (a) {
        lds_wait_ge<0>(wa + 64 + 4 * (lane & 3), 3);
        exchange(tag, FL_X + (16 + tm) * 2048, tn);
        lds_set(wa + 24, 2);
        STAMPV(12);
      }
    }
#pragma unroll 1
    for (int L = fresh_s(0);; ++L) {
      const unsigned sx = OFF_SX + L * SX_LAYER;
      STAMPV(1 + 16 * L);
      if (0 && L == 0) { words[3] = 5000u; lds_set(wa + 4 + 4 * a, 1); } else {
      u32x4 hf[5];
      unsigned key;
      { LANE_VARS;
      {
        unsigned cdst = lane < 16 ? sx + SX_ROW + (lane >> 3) * 32768 + tm * 2048 + tn * 128 + (lane & 7) * 16 : sx + SX_CAND + (tn * 16 + tm) * 128;
        cdst = (a == 0 && lane < 17) ? cdst : OOB;
        PIN(cdst);
        lds_wait_ge<0>(wa + 32 + 4 * tp, L + 1);
        key = *reinterpret_cast<const unsigned*>(smem + L_CAND + tp * 144 + 128);
        key = max(key, dpp<0xB1>(key)); key = max(key, dpp<0x4E>(key));
        const char* cp = smem + L_CAND + (((2047 - (int)(key & 2047u)) >> 5) & 3) * 144;
        const u32x4 c = *reinterpret_cast<const u32x4*>(cp + (lane < 16 ? lane & 7 : 8) * 16), rec = *reinterpret_cast<const u32x4*>(cp + 128);
        u32x4 o;
#pragma unroll
        for (int e = 0; e < 4; ++e) { unsigned h = ln_pair_s(c[e], -U2F(rec[1]), U2F(rec[2])); PIN(h); o[e] = nz(lane < 8 ? h : c[e]); }
        stg(rs, cdst, o);
      }
      STAMPV(8 + 16 * L);
      __builtin_amdgcn_s_sleep(13);
      poll_all<5>(hf, L == 0, [&](u32x4 (&t)[5]) {
        t[4][0] = __builtin_amdgcn_raw_buffer_load_b32(rs, sx + SX_CAND + (tn * 16 + (lane & 15)) * 128, 0, 16);
#pragma unroll
        for (int i = 0; i < 4; ++i) t[i] = ldg(rs, sx + SX_ROW + ((2047 - (int)(key & 2047u)) >> 7) * 2048 + (16 * qa + 4 * i + (lane >> 4)) * 16);
        t[4][1] = t[4][2] = t[4][3] = key;
      }, [&](const u32x4 (&t)[5]) {
        unsigned k = t[4][0] == SENT ? 0u : t[4][0];
        k = max(k, dpp<0xB1>(k)); k = max(k, dpp<0x4E>(k)); k = max(k, dpp<0x141>(k)); k = max(k, dpp<0x140>(k));
        key = max(key, k);
        return none_sent(t) & (key == t[4][1]);
      });
      key = __builtin_amdgcn_readfirstlane(key);
      words[3] = 2047u - (key & 2047u);
      if (L == 1) lds_set(wa + 4 + 4 * a, 2);
      }
      STAMPV(2 + 16 * L);
      f32x4 d[8];
      const f32x4 zero = {0.f, 0.f, 0.f, 0.f};
      SBAR();
      __builtin_amdgcn_s_setprio(3);
#pragma unroll
      for (int t = 0; t < 32; ++t) d[t & 7] = MFMA16(__builtin_bit_cast(s16x8, hf[t >> 3]), __builtin_bit_cast(s16x8, gf[t]), t < 8 ? zero : d[t & 7]);
      SBAR();
      __builtin_amdgcn_s_setprio(0);
      LANE_VARS;
      const int idx = 2047 - (int)(key & 2047u), win = idx >> 7;
      const bool owner = win == tm;
      auto pick2 = [&](const f32x4 (&d)[8]) {
        u32x2 o;
#pragma unroll
        for (int e = 0; e < 2; ++e) {
          const float x = tblk ? d[2 + e][0] : d[e][0], y = tblk ? d[6 + e][0] : d[4 + e][0];
          o[e] = nzf(hi ? y : x);
        }
        return o;
      };
      if (ffn) {
        const unsigned ppos = (lane & 15) * 32 + (lane >> 4) * 8;
        __builtin_amdgcn_raw_buffer_store_b64(pick2(d), rs, sx + SX_GP + (grp * 8 + qa) * 512 + ppos, 0, 16);
        STAMPV(9 + 16 * L);
        __builtin_amdgcn_s_sleep(13);
        if (L == 0) __builtin_amdgcn_s_sleep(0);
        u32x2 gu[8];
        for (unsigned spins = 0;; ++spins) {
#pragma unroll
          for (int i = 0; i < 8; ++i) gu[i] = __builtin_amdgcn_raw_buffer_load_b64(rs, sx + SX_GP + (grp * 8 + i) * 512 + lane * 8, 0, 16);
          SBAR();
          unsigned m = 0u;
#pragma unroll
          for (int i = 0; i < 8; ++i) m = max(max(m, gu[i][0]), gu[i][1]);
          if (__all(m != SENT) || spins > SPIN_CAP) break;
          poll_pause(L == 0);
        }
        STAMPV(10 + 16 * L);
        float t0 = opq(U2F(gu[0][0])), t1 = opq(U2F(gu[0][1]));
#pragma unroll
        for (int i = 1; i < 8; ++i) { t0 = fadd(t0, U2F(gu[i][0])); t1 = fadd(t1, U2F(gu[i][1])); }
        auto sw = __builtin_amdgcn_permlane32_swap(__float_as_uint(t0), __float_as_uint(t1), false, false);
        const float gv = rbf(U2F(sw[0])), uv = rbf(U2F(sw[1]));
        reinterpret_cast<u16*>(smem + L_GU + a * 128)[2 * r32 + hi] = (u16)pack2(fmul(rbf(fmul(gv, RCP(fadd(1.0f, __expf(-gv))))), uv), 0.f);
        asm volatile("s_waitcnt lgkmcnt(0)" ::: "memory");
        const s16x8 a0 = *reinterpret_cast<const s16x8*>(smem + L_GU + a * 128 + (lane >> 4) * 16), a1 = *reinterpret_cast<const s16x8*>(smem + L_GU + a * 128 + 64 + (lane >> 4) * 16);
        SBAR();
        __builtin_amdgcn_s_setprio(3);
#pragma unroll
        for (int t = 0; t < 16; ++t) d[t & 7] = MFMA16(t < 8 ? a0 : a1, __builtin_bit_cast(s16x8, wdf[t]), t < 8 ? zero : d[t & 7]);
        SBAR();
        __builtin_amdgcn_s_setprio(0);
        __builtin_amdgcn_raw_buffer_store_b64(pick2(d), rs, sx + SX_YP + grp * 4096 + qa * 512 + ppos, 0, 16);
      }
      STAMPV(3 + 16 * L);
      if (L == 1 && (cg != (kr & 7) || a)) { STAMP_END; __builtin_amdgcn_endpgm(); }
      s16x8 wpf[12];
#pragma unroll
      for (int t = 0; t < 12; ++t) wpf[t] = tr2(smem + L_SL + a * 12288 + (16 * hi + 4 * tblk + tq) * 384 + t * 32 + tp * 8, 8 * 384);
#pragma unroll
      for (int t = 0; t < 12; ++t) PIN(wpf[t]);
      if (L == 0 && !owner) lds_set(wa + 4 + 4 * a, 1);
      __builtin_amdgcn_s_sleep(13 - 4);
      if (L == 0) __builtin_amdgcn_s_sleep(0);
      if (L == 0 && !ffn) __builtin_amdgcn_s_sleep(64);
      const unsigned rk = key >> 16;
      float rsig = rbf(RCP(fadd(1.0f, __expf(-lo16((rk & 0x8000u) ? (rk & 0x7fffu) : (~rk & 0xffffu))))));
      PIN(rsig);
      const int n = 4 * (lane & 7) + ((lane >> 3) & 3);
      const int fo = ((n >> 4) * 2 + ((n >> 2) & 1)) * 8 + ((n >> 3) & 1) * 4 + (n & 3);
      u32x4 pv[8];
      poll_all<8>(pv, L == 0, [&](u32x4 (&t)[8]) {
#pragma unroll
        for (int i = 0; i < 7; ++i) t[i] = ldg(rs, sx + SX_YP + (8 * i + (lane >> 3)) * 4096 + kr * 128 + (lane & 7) * 16);
        t[7][0] = t[7][2] = __builtin_amdgcn_raw_buffer_load_b32(rs, sx + SX_ROW + win * 2048 + kr * 64 + (fo >> 1) * 4, 0, 16);
        t[7][1] = t[7][3] = __builtin_amdgcn_raw_buffer_load_b32(rs, sx + SX_ROW + 32768 + win * 2048 + kr * 64 + (fo >> 1) * 4, 0, 16);
      }, [&](const u32x4 (&t)[8]) { return none_sent(t); });
      const float hv = (fo & 1) ? hi16(pv[7][0]) : lo16(pv[7][0]), xv = (fo & 1) ? hi16(pv[7][1]) : lo16(pv[7][1]);
      STAMPV(4 + 16 * L);
      float ys;
      {
        f32x4 t = sum4<7>(pv);
#pragma unroll
        for (int e = 0; e < 4; ++e) {
          t[e] = fadd(t[e], U2F(dpp<0x128>(__float_as_uint(t[e]))));
          t[e] = half_swap_sum(row_swap_sum(t[e]));
        }
        const float x = (lane & 8) ? t[1] : t[0], y = (lane & 8) ? t[3] : t[2];
        ys = tblk ? y : x;
      }
      const unsigned ob = pack2(fadd(xv, rbf(fadd(hv, rbf(fmul(rbf(ys), rsig))))), 0.f) & 0xffffu;
      if (L == 1) {
        if (lane < 32) out[(size_t)idx * D_ + kr * 32 + n] = (u16)ob;
        STAMPV(5 + 16 * L);
        STAMP_END;
        __builtin_amdgcn_endpgm();
      }
      reinterpret_cast<u16*>(smem + L_X2F + a * 64)[fo] = (u16)ob;
      asm volatile("s_waitcnt lgkmcnt(0)" ::: "memory");
      {
        const s16x8 xa = *reinterpret_cast<const s16x8*>(smem + L_X2F + a * 64 + (lane >> 4) * 16);
        const u32x4 xf = *reinterpret_cast<const u32x4*>(smem + L_X2F + a * 64 + tp * 16);
        f32x4 z[12];
        SBAR();
        __builtin_amdgcn_s_setprio(3);
#pragma unroll
        for (int t = 0; t < 12; ++t) z[t] = MFMA16(xa, wpf[t], zero);
        SBAR();
        __builtin_amdgcn_s_setprio(0);
        const float x2 = lo16(ob), s1 = half_sum(x2), s2 = half_sum(fmul(x2, x2));
        u32x4 o;
#pragma unroll
        for (int e = 0; e < 4; ++e) {
          const unsigned sp = lane < 52 ? (e == 0 ? __float_as_uint(s1) : __float_as_uint(s2)) : xf[e];
          const unsigned x = __float_as_uint(tblk ? z[4 + e][0] : z[e][0]), y = tblk ? sp : __float_as_uint(z[8 + e][0]);
          o[e] = nz(hi ? y : x);
        }
        const unsigned pos = lane < 24 ? lane * 16 : lane < 48 ? lane * 16 + 128 : lane < 52 ? (lane & 1) * 512 + 384 : 400 + tp * 16;
        stg(rs, lane < 56 ? OFF_SX + SX_QP + ((2 * cg + a) * 32 + kr) * 1024 + pos : OOB, o);
      }
      STAMPV(5 + 16 * L);
      if (owner) {
        __builtin_amdgcn_s_sleep(13);
        const int c25 = r32 < 25 ? r32 : 24, pl = 24 * a + c24, col = 64 * (pl >> 4) + (pl & 15);
        float cs[4];
#pragma unroll
        for (int e = 0; e < 4; ++e) { cs[e] = reinterpret_cast<const float*>(smem + L_CS)[col + 16 * e]; PIN(cs[e]); }
        u32x4 qv[17];
        poll_all<17>(qv, true, [&](u32x4 (&t)[17]) {
#pragma unroll
          for (int i = 0; i < 16; ++i) t[i] = ldg(rs, OFF_SX + SX_QP + (tn * 32 + 2 * i + hi) * 1024 + a * 512 + c25 * 16);
          t[16] = ldg(rs, OFF_SX + SX_QP + (tn * 34 + hi) * 1024 + 400 + tp * 16);
        }, [&](const u32x4 (&t)[17]) { return none_sent(t); });
        *reinterpret_cast<u32x4*>(smem + L_X2P + hi * 64 + tp * 16) = qv[16];
        f32x4 t = sum4<16>(qv);
#pragma unroll
        for (int e = 0; e < 4; ++e) t[e] = half_swap_sum(t[e]);
        const float mean = fmul(U2F(__builtin_amdgcn_readlane(__float_as_uint(t[0]), 24)), 1.0f / 1024.0f);
        const float var = fmaxf(ffma(-mean, mean, fmul(U2F(__builtin_amdgcn_readlane(__float_as_uint(t[1]), 24)), 1.0f / 1024.0f)), 0.f);
        float mb, den;
        ln_scalars(mean, var, mb, den);
        const float rd = RCP(den);
        if (lane < 24) {
#pragma unroll
          for (int e = 0; e < 4; ++e) reinterpret_cast<float*>(smem + L_PATCH)[col + 16 * e] = fmul(ffma(-mb, cs[e], t[e]), rd);
        }
        lds_set(wa + 4 + 4 * a, L + 1);
      }
      STAMPV(6 + 16 * L);
      }
      BAR_ALL();
      unsigned dv = fresh_lane();
      dv = (dv >> 4) * (8 * D_ * 2) + (dv & 15) * 16;
      PIN(dv);
      wait_go(1);
      load_wd(dv);
      STAMPV(23);
      BARRIER();
      transpose8(wdf); transpose8(wdf + 8);
      BARRIER();
    }
  }
  const int tm_ = tm, tn_ = tn, wid_ = wid;
  {
    const int tid = threadIdx.x, lane = tid & 63, r32 = lane & 31, row = 2 * wid + (lane >> 5);
    char* sm = smem + L_XCH;
    u32x4 v[4];
    asm volatile("global_load_dwordx4 %0, %4, off\n\tglobal_load_dwordx4 %1, %4, off offset:512\n\tglobal_load_dwordx4 %2, %4, off offset:1024\n\tglobal_load_dwordx4 %3, %4, off offset:1536"
                 : "=&v"(v[0]), "=&v"(v[1]), "=&v"(v[2]), "=&v"(v[3]) : "v"(x0 + (size_t)(tm * 128 + tn * 8 + row) * D_ + r32 * 8) : "memory");
    int pass = fresh_s(0);
#pragma unroll 1
    for (;;) {
      asm volatile("s_cmp_eq_u32 %4, 0\n\ts_cbranch_scc1 1f\n\ts_waitcnt vmcnt(0)\n1:" : "+v"(v[0]), "+v"(v[1]), "+v"(v[2]), "+v"(v[3]) : "s"(pass) : "memory", "scc");
      float s = 0.f, s2 = 0.f;
#pragma unroll
      for (int i = 0; i < 4; ++i)
#pragma unroll
        for (int e = 0; e < 4; e += 2) { s = dot2(v[i][e], 0x3f803f80u, s); s2 = dot2(v[i][e + 1], 0x3f803f80u, s2); }
      const float mean = half_sum(s + s2) * (1.0f / 1024.0f);
      f32x2 m2 = {0.f, 0.f};
#pragma unroll
      for (int i = 0; i < 4; ++i)
#pragma unroll
        for (int e = 0; e < 4; ++e) { const f32x2 d = f32x2{lo16(v[i][e]), hi16(v[i][e])} - mean; m2 += d * d; }
      float mb, den;
      ln_scalars(mean, half_sum(m2[0] + m2[1]) * (1.0f / 1024.0f), mb, den);
      const float rd = 1.0f / den;
#pragma unroll
      for (int i = 0; i < 4; ++i) {
        char* p = sm + ((16 * i + (r32 >> 1)) * 2) * 144 + row * 16 + (r32 & 1) * 8;
        u32x2 lo = {ln_pair(v[i][0], mb, rd), ln_pair(v[i][1], mb, rd)}, hi = {ln_pair(v[i][2], mb, rd), ln_pair(v[i][3], mb, rd)};
        *reinterpret_cast<u32x2*>(p) = lo;
        *reinterpret_cast<u32x2*>(p + 144) = hi;
      }
      STAMPV(37);
      lds_set(lbase + L_W + 64 + 4 * wid, pass);
      lds_wait_ge<0>(lbase + L_W + 64 + 4 * (lane & 3), pass);
      u32x4 t[4];
#pragma unroll
      for (int i = 0; i < 4; ++i) t[i] = *reinterpret_cast<const u32x4*>(sm + (i * 32 + (tid >> 3)) * 144 + (tid & 7) * 16);
      SBAR();
      unsigned dst = OFF_HF + (tm * 4 + (tn >> 2)) * TS + (tn & 3) * 128 + (tid >> 3) * 512 + (tid & 7) * 16;
      PIN(dst);
      if (pass) {
#pragma unroll
        for (int i = 0; i < 4; ++i) stg(rs, dst + i * 16384, t[i]);
        break;
      }
#pragma unroll
      for (int i = 0; i < 4; ++i) PIN(t[i]);
      pass = fresh_s(1);
    }
    DRAIN();
    lds_set(lbase + L_W + 64 + 4 * wid, 2);
    STAMPV(38);
  }
  const u32x4 srd = {(unsigned)(uintptr_t)ws, (unsigned)((uintptr_t)ws >> 32) & 0xffffu, WS_BYTES, 0x00020000u};
  u32x4 x1p[4];
  asm volatile("" : "=v"(x1p[0]), "=v"(x1p[1]), "=v"(x1p[2]), "=v"(x1p[3]));
#pragma unroll 1
  for (int L = 0; L < 2; ++L) {
    const unsigned sx = OFF_SX + L * SX_LAYER;
    u32x8 q[4];
    u32x16 kva, kvb;
    f32x16 oz[5];
    {
      f32x16 acc[6];
      u32x16 stA, stB, xr0, xr1, xr2, xr3;
      u32x4 stC;
      asm volatile("" : "={v[112:127]}"(stB), "={v[128:131]}"(stC));
      { PHASE_VARS;
      stB[8] = lane * 16 + (L ? OFF_X1 : OFF_HF) + tile * TS;
      stB[9] = lbase + wrow * 384 + (((4 * hh + wch) ^ wsw) << 4) + 8 * (tp & 1);
      stB[10] = lbase + wrow * 384 + (((4 * (hh ^ 1) + wch) ^ wsw) << 4) + 8 * (tp & 1);
      stB[13] = lbase + L_FULL + (lane & 1) * 256;
      stB[15] = lbase + L_FREE + wid * 256 + lane * 4;
      stC[0] = 32 * L; }
      unsigned soff, slot, need = 32 * L + 1;
      if (L == 0) {
        lds_wait_ge<0>(lbase + L_W + 24, 1);
        STAMPV(39);
        soff = tn * 4096;
        asm volatile(
          "buffer_load_dwordx4 v[132:135], v120, s[40:43], s44 offen offset:0 sc1\n\t"
          "buffer_load_dwordx4 v[136:139], v120, s[40:43], s44 offen offset:1024 sc1\n\t"
          "buffer_load_dwordx4 v[140:143], v120, s[40:43], s44 offen offset:2048 sc1\n\t"
          "buffer_load_dwordx4 v[144:147], v120, s[40:43], s44 offen offset:3072 sc1\n\t"
          "s_add_u32 s44, s44, 0x1000\n\t"
          "s_and_b32 s44, s44, 0xffff\n\t"
          "buffer_load_dwordx4 v[148:151], v120, s[40:43], s44 offen offset:0 sc1\n\t"
          "buffer_load_dwordx4 v[152:155], v120, s[40:43], s44 offen offset:1024 sc1\n\t"
          "buffer_load_dwordx4 v[156:159], v120, s[40:43], s44 offen offset:2048 sc1\n\t"
          "buffer_load_dwordx4 v[160:163], v120, s[40:43], s44 offen offset:3072 sc1\n\t"
          "s_add_u32 s44, s44, 0x1000\n\t"
          "s_and_b32 s44, s44, 0xffff\n\t"
          "buffer_load_dwordx4 v[164:167], v120, s[40:43], s44 offen offset:0 sc1\n\t"
          "buffer_load_dwordx4 v[168:171], v120, s[40:43], s44 offen offset:1024 sc1\n\t"
          "buffer_load_dwordx4 v[172:175], v120, s[40:43], s44 offen offset:2048 sc1\n\t"
          "buffer_load_dwordx4 v[176:179], v120, s[40:43], s44 offen offset:3072 sc1\n\t"
          "s_add_u32 s44, s44, 0x1000\n\t"
          "s_and_b32 s44, s44, 0xffff\n\t"
          "buffer_load_dwordx4 v[180:183], v120, s[40:43], s44 offen offset:0 sc1\n\t"
          "buffer_load_dwordx4 v[184:187], v120, s[40:43], s44 offen offset:1024 sc1\n\t"
          "buffer_load_dwordx4 v[188:191], v120, s[40:43], s44 offen offset:2048 sc1\n\t"
          "buffer_load_dwordx4 v[192:195], v120, s[40:43], s44 offen offset:3072 sc1\n\t"
          : "={v[132:147]}"(xr0), "={v[148:163]}"(xr1), "={v[164:179]}"(xr2), "={v[180:195]}"(xr3), "+{s44}"(soff) : "{v[112:127]}"(stB), "{s[40:43]}"(srd) : "scc", "memory");
      } else {
        xr0 = cat4(x1p[0], x1p[1], x1p[2], x1p[3]);
        asm volatile("" : "={v[148:163]}"(xr1), "={v[164:179]}"(xr2), "={v[180:195]}"(xr3));
      }
      STAMPV(1 + 16 * L);
      asm volatile(
        "v_mov_b32 v123, v121\n\t"
        "v_mov_b32 v124, v122\n\t"
        "s_mov_b64 vcc, -1\n\t"
        "s_cbranch_vccnz 10f\n\t"
        "11:\n\t"
        "s_mov_b32 s46, 12288\n\t"
        "s_add_u32 s47, s47, 1\n\t"
        "ds_read_b64_tr_b16 v[96:97], v123 offset:0\n\t"
        "ds_read_b64_tr_b16 v[98:99], v123 offset:3072\n\t"
        "ds_read_b64_tr_b16 v[100:101], v124 offset:0\n\t"
        "ds_read_b64_tr_b16 v[102:103], v124 offset:3072\n\t"
        "ds_read_b64_tr_b16 v[104:105], v123 offset:128\n\t"
        "ds_read_b64_tr_b16 v[106:107], v123 offset:3200\n\t"
        "ds_read_b64_tr_b16 v[108:109], v124 offset:128\n\t"
        "ds_read_b64_tr_b16 v[110:111], v124 offset:3200\n\t"
        "ds_read_b64_tr_b16 v[112:113], v123 offset:256\n\t"
        "ds_read_b64_tr_b16 v[114:115], v123 offset:3328\n\t"
        "ds_write_b32 v127, v128\n\t"
        "ds_read_b64_tr_b16 v[116:117], v124 offset:256\n\t"
        "ds_read_b64_tr_b16 v[118:119], v124 offset:3328\n\t"
        "s_waitcnt vmcnt(15) lgkmcnt(11)\n\t"
        "v_mfma_f32_32x32x16_bf16 v[0:15], v[96:99], v[132:135], 0\n\t"
        "ds_read_b32 v126, v125\n\t"
        "ds_read_b64_tr_b16 v[96:97], v123 offset:6144\n\t"
        "ds_read_b64_tr_b16 v[98:99], v123 offset:9216\n\t"
        "s_waitcnt lgkmcnt(12)\n\t"
        "v_mfma_f32_32x32x16_bf16 v[16:31], v[100:103], v[132:135], 0\n\t"
        "v_add_u32 v129, s46, v121\n\t"
        "ds_read_b64_tr_b16 v[100:101], v124 offset:6144\n\t"
        "ds_read_b64_tr_b16 v[102:103], v124 offset:9216\n\t"
        "s_waitcnt lgkmcnt(12)\n\t"
        "v_mfma_f32_32x32x16_bf16 v[32:47], v[104:107], v[132:135], 0\n\t"
        "ds_read_b64_tr_b16 v[104:105], v123 offset:6272\n\t"
        "ds_read_b64_tr_b16 v[106:107], v123 offset:9344\n\t"
        "s_waitcnt lgkmcnt(12)\n\t"
        "v_mfma_f32_32x32x16_bf16 v[48:63], v[108:111], v[132:135], 0\n\t"
        "v_add_u32 v130, s46, v122\n\t"
        "ds_read_b64_tr_b16 v[108:109], v124 offset:6272\n\t"
        "ds_read_b64_tr_b16 v[110:111], v124 offset:9344\n\t"
        "s_waitcnt lgkmcnt(12)\n\t"
        "v_mfma_f32_32x32x16_bf16 v[64:79], v[132:135], v[112:115], 0\n\t"
        "ds_read_b64_tr_b16 v[112:113], v123 offset:6400\n\t"
        "ds_read_b64_tr_b16 v[114:115], v123 offset:9472\n\t"
        "s_waitcnt lgkmcnt(10)\n\t"
        "v_mfma_f32_32x32x16_bf16 v[80:95], v[132:135], v[116:119], 0\n\t"
        "v_cmp_gt_u32 vcc, s47, v126\n\t"
        "ds_read_b64_tr_b16 v[116:117], v124 offset:6400\n\t"
        "ds_read_b64_tr_b16 v[118:119], v124 offset:9472\n\t"
        "s_waitcnt vmcnt(14) lgkmcnt(10)\n\t"
        "v_mfma_f32_32x32x16_bf16 v[0:15], v[96:99], v[136:139], v[0:15]\n\t"
        "s_cbranch_vccnz 12f\n\t"
        "13:\n\t"
        "ds_read_b64_tr_b16 v[96:97], v129 offset:0\n\t"
        "ds_read_b64_tr_b16 v[98:99], v129 offset:3072\n\t"
        "s_waitcnt lgkmcnt(10)\n\t"
        "v_mfma_f32_32x32x16_bf16 v[16:31], v[100:103], v[136:139], v[16:31]\n\t"
        "v_add_u32 v128, 1, v128\n\t"
        "ds_read_b64_tr_b16 v[100:101], v130 offset:0\n\t"
        "ds_read_b64_tr_b16 v[102:103], v130 offset:3072\n\t"
        "s_waitcnt lgkmcnt(10)\n\t"
        "v_mfma_f32_32x32x16_bf16 v[32:47], v[104:107], v[136:139], v[32:47]\n\t"
        "ds_read_b64_tr_b16 v[104:105], v129 offset:128\n\t"
        "ds_read_b64_tr_b16 v[106:107], v129 offset:3200\n\t"
        "s_waitcnt lgkmcnt(10)\n\t"
        "v_mfma_f32_32x32x16_bf16 v[48:63], v[108:111], v[136:139], v[48:63]\n\t"
        "s_add_u32 s46, s46, 12288\n\t"
        "s_add_u32 s47, s47, 1\n\t"
        "ds_read_b64_tr_b16 v[108:109], v130 offset:128\n\t"
        "ds_read_b64_tr_b16 v[110:111], v130 offset:3200\n\t"
        "s_waitcnt lgkmcnt(10)\n\t"
        "v_mfma_f32_32x32x16_bf16 v[64:79], v[136:139], v[112:115], v[64:79]\n\t"
        "s_cmp_eq_u32 s46, 110592\n\t"
        "s_cselect_b32 s46, 0, s46\n\t"
        "ds_read_b64_tr_b16 v[112:113], v129 offset:256\n\t"
        "ds_read_b64_tr_b16 v[114:115], v129 offset:3328\n\t"
        "s_waitcnt lgkmcnt(10)\n\t"
        "v_mfma_f32_32x32x16_bf16 v[80:95], v[136:139], v[116:119], v[80:95]\n\t"
        "ds_write_b32 v127, v128\n\t"
        "ds_read_b64_tr_b16 v[116:117], v130 offset:256\n\t"
        "ds_read_b64_tr_b16 v[118:119], v130 offset:3328\n\t"
        "s_waitcnt vmcnt(13) lgkmcnt(11)\n\t"
        "v_mfma_f32_32x32x16_bf16 v[0:15], v[96:99], v[140:143], v[0:15]\n\t"
        "ds_read_b32 v126, v125\n\t"
        "ds_read_b64_tr_b16 v[96:97], v129 offset:6144\n\t"
        "ds_read_b64_tr_b16 v[98:99], v129 offset:9216\n\t"
        "s_waitcnt lgkmcnt(12)\n\t"
        "v_mfma_f32_32x32x16_bf16 v[16:31], v[100:103], v[140:143], v[16:31]\n\t"
        "v_add_u32 v123, s46, v121\n\t"
        "ds_read_b64_tr_b16 v[100:101], v130 offset:6144\n\t"
        "ds_read_b64_tr_b16 v[102:103], v130 offset:9216\n\t"
        "s_waitcnt lgkmcnt(12)\n\t"
        "v_mfma_f32_32x32x16_bf16 v[32:47], v[104:107], v[140:143], v[32:47]\n\t"
        "ds_read_b64_tr_b16 v[104:105], v129 offset:6272\n\t"
        "ds_read_b64_tr_b16 v[106:107], v129 offset:9344\n\t"
        "s_waitcnt lgkmcnt(12)\n\t"
        "v_mfma_f32_32x32x16_bf16 v[48:63], v[108:111], v[140:143], v[48:63]\n\t"
        "v_add_u32 v124, s46, v122\n\t"
        "ds_read_b64_tr_b16 v[108:109], v130 offset:6272\n\t"
        "ds_read_b64_tr_b16 v[110:111], v130 offset:9344\n\t"
        "s_waitcnt lgkmcnt(12)\n\t"
        "v_mfma_f32_32x32x16_bf16 v[64:79], v[140:143], v[112:115], v[64:79]\n\t"
        "ds_read_b64_tr_b16 v[112:113], v129 offset:6400\n\t"
        "ds_read_b64_tr_b16 v[114:115], v129 offset:9472\n\t"
        "s_waitcnt lgkmcnt(10)\n\t"
        "v_mfma_f32_32x32x16_bf16 v[80:95], v[140:143], v[116:119], v[80:95]\n\t"
        "v_cmp_gt_u32 vcc, s47, v126\n\t"
        "ds_read_b64_tr_b16 v[116:117], v130 offset:6400\n\t"
        "ds_read_b64_tr_b16 v[118:119], v130 offset:9472\n\t"
        "s_waitcnt vmcnt(12) lgkmcnt(10)\n\t"
        "v_mfma_f32_32x32x16_bf16 v[0:15], v[96:99], v[144:147], v[0:15]\n\t"
        "s_cbranch_vccnz 14f\n\t"
        "15:\n\t"
        "ds_read_b64_tr_b16 v[96:97], v123 offset:0\n\t"
        "ds_read_b64_tr_b16 v[98:99], v123 offset:3072\n\t"
        "s_waitcnt lgkmcnt(10)\n\t"
        "v_mfma_f32_32x32x16_bf16 v[16:31], v[100:103], v[144:147], v[16:31]\n\t"
        "v_add_u32 v128, 1, v128\n\t"
        "ds_read_b64_tr_b16 v[100:101], v124 offset:0\n\t"
        "ds_read_b64_tr_b16 v[102:103], v124 offset:3072\n\t"
        "s_waitcnt lgkmcnt(10)\n\t"
        "v_mfma_f32_32x32x16_bf16 v[32:47], v[104:107], v[144:147], v[32:47]\n\t"
        "ds_read_b64_tr_b16 v[104:105], v123 offset:128\n\t"
        "ds_read_b64_tr_b16 v[106:107], v123 offset:3200\n\t"
        "s_waitcnt lgkmcnt(10)\n\t"
        "v_mfma_f32_32x32x16_bf16 v[48:63], v[108:111], v[144:147], v[48:63]\n\t"
        "s_add_u32 s46, s46, 12288\n\t"
        "s_add_u32 s47, s47, 1\n\t"
        "ds_read_b64_tr_b16 v[108:109], v124 offset:128\n\t"
        "ds_read_b64_tr_b16 v[110:111], v124 offset:3200\n\t"
        "s_waitcnt lgkmcnt(10)\n\t"
        "v_mfma_f32_32x32x16_bf16 v[64:79], v[144:147], v[112:115], v[64:79]\n\t"
        "s_cmp_eq_u32 s46, 110592\n\t"
        "s_cselect_b32 s46, 0, s46\n\t"
        "ds_read_b64_tr_b16 v[112:113], v123 offset:256\n\t"
        "ds_read_b64_tr_b16 v[114:115], v123 offset:3328\n\t"
        "s_waitcnt lgkmcnt(10)\n\t"
        "v_mfma_f32_32x32x16_bf16 v[80:95], v[144:147], v[116:119], v[80:95]\n\t"
        "ds_write_b32 v127, v128\n\t"
        "ds_read_b64_tr_b16 v[116:117], v124 offset:256\n\t"
        "ds_read_b64_tr_b16 v[118:119], v124 offset:3328\n\t"
        "s_waitcnt lgkmcnt(0)\n\t"
        "s_nop 7\n\t"
        "s_branch 9f\n\t"
        "10:\n\t"
        "s_mov_b32 s48, 0x10000\n\t"
        "8:\n\t"
        "ds_read_b32 v126, v125\n\t"
        "s_sub_u32 s48, s48, 1\n\t"
        "s_waitcnt lgkmcnt(0)\n\t"
        "s_cbranch_scc1 11b\n\t"
        "v_cmp_gt_u32 vcc, s47, v126\n\t"
        "s_cbranch_vccnz 8b\n\t"
        "s_branch 11b\n\t"
        "12:\n\t"
        "s_mov_b32 s48, 0x10000\n\t"
        "8:\n\t"
        "ds_read_b32 v126, v125\n\t"
        "s_sub_u32 s48, s48, 1\n\t"
        "s_waitcnt lgkmcnt(0)\n\t"
        "s_cbranch_scc1 13b\n\t"
        "v_cmp_gt_u32 vcc, s47, v126\n\t"
        "s_cbranch_vccnz 8b\n\t"
        "s_branch 13b\n\t"
        "14:\n\t"
        "s_mov_b32 s48, 0x10000\n\t"
        "8:\n\t"
        "ds_read_b32 v126, v125\n\t"
        "s_sub_u32 s48, s48, 1\n\t"
        "s_waitcnt lgkmcnt(0)\n\t"
        "s_cbranch_scc1 15b\n\t"
        "v_cmp_gt_u32 vcc, s47, v126\n\t"
        "s_cbranch_vccnz 8b\n\t"
        "s_branch 15b\n\t"
        "9:\n\t"
        : "={v[0:15]}"(acc[0]), "={v[16:31]}"(acc[1]), "={v[32:47]}"(acc[2]), "={v[48:63]}"(acc[3]), "={v[64:79]}"(acc[4]), "={v[80:95]}"(acc[5]),
          "={v[96:111]}"(stA), "+{v[112:127]}"(stB), "+{v[128:131]}"(stC), "={s46}"(slot), "+{s47}"(need)
        : "{v[132:147]}"(xr0) : "s48", "vcc", "scc", "memory");
      STAMPV(2 + 16 * L);
      float al = 1.f;
      if (L == 1) {
        PHASE_VARS;
        float S, Q, Dd;
        row_stats_sum(rs, OFF_SX, tile, lane, S, Q, Dd);
        STAMPV(36);
        lds_wait_ge<0>(lbase + L_W + 24, 2);
        soff = ((tn + 1) & 15) * 4096;
        asm volatile(
          "buffer_load_dwordx4 v[148:151], v120, s[40:43], s44 offen offset:0 sc1\n\t"
          "buffer_load_dwordx4 v[152:155], v120, s[40:43], s44 offen offset:1024 sc1\n\t"
          "buffer_load_dwordx4 v[156:159], v120, s[40:43], s44 offen offset:2048 sc1\n\t"
          "buffer_load_dwordx4 v[160:163], v120, s[40:43], s44 offen offset:3072 sc1\n\t"
          : "={v[148:163]}"(xr1), "={v[164:179]}"(xr2), "={v[180:195]}"(xr3), "+{s44}"(soff), "+v"(S), "+v"(Q), "+v"(Dd) : "{v[112:127]}"(stB), "{s[40:43]}"(srd) : "scc", "memory");
        float mean, var, mb1, rd1, mb2, den2;
        row_stats_fin(smem, 1, tile, lane, S, Q, Dd, U2F(words[0]), x1p, mean, var, mb1, rd1);
        ln_scalars(mean + (mean - mb1) * rd1, var * (1.0f + rd1) * (1.0f + rd1), mb2, den2);
        const float rd2 = RCP(den2);
        al = (1.0f + rd1) * rd2;
        const float be = -(mb1 * rd1 + mb2) * RCP(1.0f + rd1);
        reinterpret_cast<float*>(smem + L_AB)[wid * 64 + lane] = hi ? be : al;
        reinterpret_cast<float*>(smem + L_AB)[256 + wid * 64 + lane] = hi ? rd1 : mb1;
        soff = ((tn + 1 + 0) & 15) * 4096;
        asm volatile(
          "s_nop 0\n\t"
          "s_add_u32 s44, s44, 0x1000\n\t"
          "s_and_b32 s44, s44, 0xffff\n\t"
          "buffer_load_dwordx4 v[164:167], v120, s[40:43], s44 offen offset:0 sc1\n\t"
          "buffer_load_dwordx4 v[168:171], v120, s[40:43], s44 offen offset:1024 sc1\n\t"
          "buffer_load_dwordx4 v[172:175], v120, s[40:43], s44 offen offset:2048 sc1\n\t"
          "buffer_load_dwordx4 v[176:179], v120, s[40:43], s44 offen offset:3072 sc1\n\t"
          "s_add_u32 s44, s44, 0x1000\n\t"
          "s_and_b32 s44, s44, 0xffff\n\t"
          "buffer_load_dwordx4 v[180:183], v120, s[40:43], s44 offen offset:0 sc1\n\t"
          "buffer_load_dwordx4 v[184:187], v120, s[40:43], s44 offen offset:1024 sc1\n\t"
          "buffer_load_dwordx4 v[188:191], v120, s[40:43], s44 offen offset:2048 sc1\n\t"
          "buffer_load_dwordx4 v[192:195], v120, s[40:43], s44 offen offset:3072 sc1\n\t"
          : "+{v[148:163]}"(xr1), "+{v[164:179]}"(xr2), "+{v[180:195]}"(xr3), "+{s44}"(soff) : "{v[112:127]}"(stB), "{s[40:43]}"(srd) : "scc", "memory");
#pragma unroll
        for (int jj = 0; jj < 3; ++jj) {
          f32x4 cs[2][4];
          float cv[2];
#pragma unroll
          for (int j = 0; j < 2; ++j) {
            const int cb = jj * 64 + (j ^ hh) * 32;
            if (jj == 2) cv[j] = *reinterpret_cast<const float*>(smem + L_CS + (cb + r32) * 4);
#pragma unroll
            for (int g = 0; g < 4; ++g)
              if (jj < 2 || j == 0) cs[j][g] = *reinterpret_cast<const f32x4*>(smem + (jj < 2 ? L_CS + cb * 4 : L_AB + wid * 256 + 128) + (8 * g + 4 * hi) * 4);
          }
          SBAR();
#pragma unroll
          for (int j = 0; j < 2; ++j)
#pragma unroll
            for (int e = 0; e < 16; ++e) acc[2 * jj + j][e] += jj < 2 ? be * cs[j][e >> 2][e & 3] : cs[0][e >> 2][e & 3] * cv[j];
          SBAR();
        }
      }
      STAMPV(3 + 16 * L);
      soff = ((tn + 1 + 2) & 15) * 4096;
      asm volatile(
        "s_nop 0\n\t"
        "s_add_u32 s44, s44, 0x1000\n\t"
        "s_and_b32 s44, s44, 0xffff\n\t"
        "buffer_load_dwordx4 v[132:135], v120, s[40:43], s44 offen offset:0 sc1\n\t"
        "buffer_load_dwordx4 v[136:139], v120, s[40:43], s44 offen offset:1024 sc1\n\t"
        "buffer_load_dwordx4 v[140:143], v120, s[40:43], s44 offen offset:2048 sc1\n\t"
        "s_mov_b32 s45, 3\n\t"
        "1:\n\t"
        "s_waitcnt vmcnt(14) lgkmcnt(11)\n\t"
        "v_mfma_f32_32x32x16_bf16 v[0:15], v[96:99], v[148:151], v[0:15]\n\t"
        "ds_read_b32 v126, v125\n\t"
        "ds_read_b64_tr_b16 v[96:97], v123 offset:6144\n\t"
        "ds_read_b64_tr_b16 v[98:99], v123 offset:9216\n\t"
        "s_waitcnt lgkmcnt(12)\n\t"
        "v_mfma_f32_32x32x16_bf16 v[16:31], v[100:103], v[148:151], v[16:31]\n\t"
        "v_add_u32 v129, s46, v121\n\t"
        "ds_read_b64_tr_b16 v[100:101], v124 offset:6144\n\t"
        "ds_read_b64_tr_b16 v[102:103], v124 offset:9216\n\t"
        "s_waitcnt lgkmcnt(12)\n\t"
        "v_mfma_f32_32x32x16_bf16 v[32:47], v[104:107], v[148:151], v[32:47]\n\t"
        "ds_read_b64_tr_b16 v[104:105], v123 offset:6272\n\t"
        "ds_read_b64_tr_b16 v[106:107], v123 offset:9344\n\t"
        "buffer_load_dwordx4 v[144:147], v120, s[40:43], s44 offen offset:3072 sc1\n\t"
        "s_waitcnt lgkmcnt(12)\n\t"
        "v_mfma_f32_32x32x16_bf16 v[48:63], v[108:111], v[148:151], v[48:63]\n\t"
        "v_add_u32 v130, s46, v122\n\t"
        "ds_read_b64_tr_b16 v[108:109], v124 offset:6272\n\t"
        "ds_read_b64_tr_b16 v[110:111], v124 offset:9344\n\t"
        "s_waitcnt lgkmcnt(12)\n\t"
        "v_mfma_f32_32x32x16_bf16 v[64:79], v[148:151], v[112:115], v[64:79]\n\t"
        "ds_read_b64_tr_b16 v[112:113], v123 offset:6400\n\t"
        "ds_read_b64_tr_b16 v[114:115], v123 offset:9472\n\t"
        "s_waitcnt lgkmcnt(10)\n\t"
        "v_mfma_f32_32x32x16_bf16 v[80:95], v[148:151], v[116:119], v[80:95]\n\t"
        "v_cmp_gt_u32 vcc, s47, v126\n\t"
        "ds_read_b64_tr_b16 v[116:117], v124 offset:6400\n\t"
        "ds_read_b64_tr_b16 v[118:119], v124 offset:9472\n\t"
        "s_waitcnt vmcnt(14) lgkmcnt(10)\n\t"
        "v_mfma_f32_32x32x16_bf16 v[0:15], v[96:99], v[152:155], v[0:15]\n\t"
        "s_cbranch_vccnz 16f\n\t"
        "17:\n\t"
        "ds_read_b64_tr_b16 v[96:97], v129 offset:0\n\t"
        "ds_read_b64_tr_b16 v[98:99], v129 offset:3072\n\t"
        "s_waitcnt lgkmcnt(10)\n\t"
        "v_mfma_f32_32x32x16_bf16 v[16:31], v[100:103], v[152:155], v[16:31]\n\t"
        "v_add_u32 v128, 1, v128\n\t"
        "ds_read_b64_tr_b16 v[100:101], v130 offset:0\n\t"
        "ds_read_b64_tr_b16 v[102:103], v130 offset:3072\n\t"
        "s_waitcnt lgkmcnt(10)\n\t"
        "v_mfma_f32_32x32x16_bf16 v[32:47], v[104:107], v[152:155], v[32:47]\n\t"
        "ds_read_b64_tr_b16 v[104:105], v129 offset:128\n\t"
        "ds_read_b64_tr_b16 v[106:107], v129 offset:3200\n\t"
        "s_add_u32 s44, s44, 0x1000\n\t"
        "s_and_b32 s44, s44, 0xffff\n\t"
        "buffer_load_dwordx4 v[148:151], v120, s[40:43], s44 offen offset:0 sc1\n\t"
        "s_waitcnt lgkmcnt(10)\n\t"
        "v_mfma_f32_32x32x16_bf16 v[48:63], v[108:111], v[152:155], v[48:63]\n\t"
        "s_add_u32 s46, s46, 12288\n\t"
        "s_add_u32 s47, s47, 1\n\t"
        "ds_read_b64_tr_b16 v[108:109], v130 offset:128\n\t"
        "ds_read_b64_tr_b16 v[110:111], v130 offset:3200\n\t"
        "s_waitcnt lgkmcnt(10)\n\t"
        "v_mfma_f32_32x32x16_bf16 v[64:79], v[152:155], v[112:115], v[64:79]\n\t"
        "s_cmp_eq_u32 s46, 110592\n\t"
        "s_cselect_b32 s46, 0, s46\n\t"
        "ds_read_b64_tr_b16 v[112:113], v129 offset:256\n\t"
        "ds_read_b64_tr_b16 v[114:115], v129 offset:3328\n\t"
        "s_waitcnt lgkmcnt(10)\n\t"
        "v_mfma_f32_32x32x16_bf16 v[80:95], v[152:155], v[116:119], v[80:95]\n\t"
        "ds_write_b32 v127, v128\n\t"
        "ds_read_b64_tr_b16 v[116:117], v130 offset:256\n\t"
        "ds_read_b64_tr_b16 v[118:119], v130 offset:3328\n\t"
        "s_waitcnt vmcnt(14) lgkmcnt(11)\n\t"
        "v_mfma_f32_32x32x16_bf16 v[0:15], v[96:99], v[156:159], v[0:15]\n\t"
        "ds_read_b32 v126, v125\n\t"
        "ds_read_b64_tr_b16 v[96:97], v129 offset:6144\n\t"
        "ds_read_b64_tr_b16 v[98:99], v129 offset:9216\n\t"
        "s_waitcnt lgkmcnt(12)\n\t"
        "v_mfma_f32_32x32x16_bf16 v[16:31], v[100:103], v[156:159], v[16:31]\n\t"
        "v_add_u32 v123, s46, v121\n\t"
        "ds_read_b64_tr_b16 v[100:101], v130 offset:6144\n\t"
        "ds_read_b64_tr_b16 v[102:103], v130 offset:9216\n\t"
        "s_waitcnt lgkmcnt(12)\n\t"
        "v_mfma_f32_32x32x16_bf16 v[32:47], v[104:107], v[156:159], v[32:47]\n\t"
        "ds_read_b64_tr_b16 v[104:105], v129 offset:6272\n\t"
        "ds_read_b64_tr_b16 v[106:107], v129 offset:9344\n\t"
        "buffer_load_dwordx4 v[152:155], v120, s[40:43], s44 offen offset:1024 sc1\n\t"
        "s_waitcnt lgkmcnt(12)\n\t"
        "v_mfma_f32_32x32x16_bf16 v[48:63], v[108:111], v[156:159], v[48:63]\n\t"
        "v_add_u32 v124, s46, v122\n\t"
        "ds_read_b64_tr_b16 v[108:109], v130 offset:6272\n\t"
        "ds_read_b64_tr_b16 v[110:111], v130 offset:9344\n\t"
        "s_waitcnt lgkmcnt(12)\n\t"
        "v_mfma_f32_32x32x16_bf16 v[64:79], v[156:159], v[112:115], v[64:79]\n\t"
        "ds_read_b64_tr_b16 v[112:113], v129 offset:6400\n\t"
        "ds_read_b64_tr_b16 v[114:115], v129 offset:9472\n\t"
        "s_waitcnt lgkmcnt(10)\n\t"
        "v_mfma_f32_32x32x16_bf16 v[80:95], v[156:159], v[116:119], v[80:95]\n\t"
        "v_cmp_gt_u32 vcc, s47, v126\n\t"
        "ds_read_b64_tr_b16 v[116:117], v130 offset:6400\n\t"
        "ds_read_b64_tr_b16 v[118:119], v130 offset:9472\n\t"
        "s_waitcnt vmcnt(14) lgkmcnt(10)\n\t"
        "v_mfma_f32_32x32x16_bf16 v[0:15], v[96:99], v[160:163], v[0:15]\n\t"
        "s_cbranch_vccnz 18f\n\t"
        "19:\n\t"
        "ds_read_b64_tr_b16 v[96:97], v123 offset:0\n\t"
        "ds_read_b64_tr_b16 v[98:99], v123 offset:3072\n\t"
        "s_waitcnt lgkmcnt(10)\n\t"
        "v_mfma_f32_32x32x16_bf16 v[16:31], v[100:103], v[160:163], v[16:31]\n\t"
        "v_add_u32 v128, 1, v128\n\t"
        "ds_read_b64_tr_b16 v[100:101], v124 offset:0\n\t"
        "ds_read_b64_tr_b16 v[102:103], v124 offset:3072\n\t"
        "s_waitcnt lgkmcnt(10)\n\t"
        "v_mfma_f32_32x32x16_bf16 v[32:47], v[104:107], v[160:163], v[32:47]\n\t"
        "ds_read_b64_tr_b16 v[104:105], v123 offset:128\n\t"
        "ds_read_b64_tr_b16 v[106:107], v123 offset:3200\n\t"
        "buffer_load_dwordx4 v[156:159], v120, s[40:43], s44 offen offset:2048 sc1\n\t"
        "s_waitcnt lgkmcnt(10)\n\t"
        "v_mfma_f32_32x32x16_bf16 v[48:63], v[108:111], v[160:163], v[48:63]\n\t"
        "s_add_u32 s46, s46, 12288\n\t"
        "s_add_u32 s47, s47, 1\n\t"
        "ds_read_b64_tr_b16 v[108:109], v124 offset:128\n\t"
        "ds_read_b64_tr_b16 v[110:111], v124 offset:3200\n\t"
        "s_waitcnt lgkmcnt(10)\n\t"
        "v_mfma_f32_32x32x16_bf16 v[64:79], v[160:163], v[112:115], v[64:79]\n\t"
        "s_cmp_eq_u32 s46, 110592\n\t"
        "s_cselect_b32 s46, 0, s46\n\t"
        "ds_read_b64_tr_b16 v[112:113], v123 offset:256\n\t"
        "ds_read_b64_tr_b16 v[114:115], v123 offset:3328\n\t"
        "s_waitcnt lgkmcnt(10)\n\t"
        "v_mfma_f32_32x32x16_bf16 v[80:95], v[160:163], v[116:119], v[80:95]\n\t"
        "ds_write_b32 v127, v128\n\t"
        "ds_read_b64_tr_b16 v[116:117], v124 offset:256\n\t"
        "ds_read_b64_tr_b16 v[118:119], v124 offset:3328\n\t"
        "s_waitcnt vmcnt(14) lgkmcnt(11)\n\t"
        "v_mfma_f32_32x32x16_bf16 v[0:15], v[96:99], v[164:167], v[0:15]\n\t"
        "ds_read_b32 v126, v125\n\t"
        "ds_read_b64_tr_b16 v[96:97], v123 offset:6144\n\t"
        "ds_read_b64_tr_b16 v[98:99], v123 offset:9216\n\t"
        "s_waitcnt lgkmcnt(12)\n\t"
        "v_mfma_f32_32x32x16_bf16 v[16:31], v[100:103], v[164:167], v[16:31]\n\t"
        "v_add_u32 v129, s46, v121\n\t"
        "ds_read_b64_tr_b16 v[100:101], v124 offset:6144\n\t"
        "ds_read_b64_tr_b16 v[102:103], v124 offset:9216\n\t"
        "s_waitcnt lgkmcnt(12)\n\t"
        "v_mfma_f32_32x32x16_bf16 v[32:47], v[104:107], v[164:167], v[32:47]\n\t"
        "ds_read_b64_tr_b16 v[104:105], v123 offset:6272\n\t"
        "ds_read_b64_tr_b16 v[106:107], v123 offset:9344\n\t"
        "buffer_load_dwordx4 v[160:163], v120, s[40:43], s44 offen offset:3072 sc1\n\t"
        "s_waitcnt lgkmcnt(12)\n\t"
        "v_mfma_f32_32x32x16_bf16 v[48:63], v[108:111], v[164:167], v[48:63]\n\t"
        "v_add_u32 v130, s46, v122\n\t"
        "ds_read_b64_tr_b16 v[108:109], v124 offset:6272\n\t"
        "ds_read_b64_tr_b16 v[110:111], v124 offset:9344\n\t"
        "s_waitcnt lgkmcnt(12)\n\t"
        "v_mfma_f32_32x32x16_bf16 v[64:79], v[164:167], v[112:115], v[64:79]\n\t"
        "ds_read_b64_tr_b16 v[112:113], v123 offset:6400\n\t"
        "ds_read_b64_tr_b16 v[114:115], v123 offset:9472\n\t"
        "s_waitcnt lgkmcnt(10)\n\t"
        "v_mfma_f32_32x32x16_bf16 v[80:95], v[164:167], v[116:119], v[80:95]\n\t"
        "v_cmp_gt_u32 vcc, s47, v126\n\t"
        "ds_read_b64_tr_b16 v[116:117], v124 offset:6400\n\t"
        "ds_read_b64_tr_b16 v[118:119], v124 offset:9472\n\t"
        "s_waitcnt vmcnt(14) lgkmcnt(10)\n\t"
        "v_mfma_f32_32x32x16_bf16 v[0:15], v[96:99], v[168:171], v[0:15]\n\t"
        "s_cbranch_vccnz 20f\n\t"
        "21:\n\t"
        "ds_read_b64_tr_b16 v[96:97], v129 offset:0\n\t"
        "ds_read_b64_tr_b16 v[98:99], v129 offset:3072\n\t"
        "s_waitcnt lgkmcnt(10)\n\t"
        "v_mfma_f32_32x32x16_bf16 v[16:31], v[100:103], v[168:171], v[16:31]\n\t"
        "v_add_u32 v128, 1, v128\n\t"
        "ds_read_b64_tr_b16 v[100:101], v130 offset:0\n\t"
        "ds_read_b64_tr_b16 v[102:103], v130 offset:3072\n\t"
        "s_waitcnt lgkmcnt(10)\n\t"
        "v_mfma_f32_32x32x16_bf16 v[32:47], v[104:107], v[168:171], v[32:47]\n\t"
        "ds_read_b64_tr_b16 v[104:105], v129 offset:128\n\t"
        "ds_read_b64_tr_b16 v[106:107], v129 offset:3200\n\t"
        "s_add_u32 s44, s44, 0x1000\n\t"
        "s_and_b32 s44, s44, 0xffff\n\t"
        "buffer_load_dwordx4 v[164:167], v120, s[40:43], s44 offen offset:0 sc1\n\t"
        "s_waitcnt lgkmcnt(10)\n\t"
        "v_mfma_f32_32x32x16_bf16 v[48:63], v[108:111], v[168:171], v[48:63]\n\t"
        "s_add_u32 s46, s46, 12288\n\t"
        "s_add_u32 s47, s47, 1\n\t"
        "ds_read_b64_tr_b16 v[108:109], v130 offset:128\n\t"
        "ds_read_b64_tr_b16 v[110:111], v130 offset:3200\n\t"
        "s_waitcnt lgkmcnt(10)\n\t"
        "v_mfma_f32_32x32x16_bf16 v[64:79], v[168:171], v[112:115], v[64:79]\n\t"
        "s_cmp_eq_u32 s46, 110592\n\t"
        "s_cselect_b32 s46, 0, s46\n\t"
        "ds_read_b64_tr_b16 v[112:113], v129 offset:256\n\t"
        "ds_read_b64_tr_b16 v[114:115], v129 offset:3328\n\t"
        "s_waitcnt lgkmcnt(10)\n\t"
        "v_mfma_f32_32x32x16_bf16 v[80:95], v[168:171], v[116:119], v[80:95]\n\t"
        "ds_write_b32 v127, v128\n\t"
        "ds_read_b64_tr_b16 v[116:117], v130 offset:256\n\t"
        "ds_read_b64_tr_b16 v[118:119], v130 offset:3328\n\t"
        "s_waitcnt vmcnt(14) lgkmcnt(11)\n\t"
        "v_mfma_f32_32x32x16_bf16 v[0:15], v[96:99], v[172:175], v[0:15]\n\t"
        "ds_read_b32 v126, v125\n\t"
        "ds_read_b64_tr_b16 v[96:97], v129 offset:6144\n\t"
        "ds_read_b64_tr_b16 v[98:99], v129 offset:9216\n\t"
        "s_waitcnt lgkmcnt(12)\n\t"
        "v_mfma_f32_32x32x16_bf16 v[16:31], v[100:103], v[172:175], v[16:31]\n\t"
        "v_add_u32 v123, s46, v121\n\t"
        "ds_read_b64_tr_b16 v[100:101], v130 offset:6144\n\t"
        "ds_read_b64_tr_b16 v[102:103], v130 offset:9216\n\t"
        "s_waitcnt lgkmcnt(12)\n\t"
        "v_mfma_f32_32x32x16_bf16 v[32:47], v[104:107], v[172:175], v[32:47]\n\t"
        "ds_read_b64_tr_b16 v[104:105], v129 offset:6272\n\t"
        "ds_read_b64_tr_b16 v[106:107], v129 offset:9344\n\t"
        "buffer_load_dwordx4 v[168:171], v120, s[40:43], s44 offen offset:1024 sc1\n\t"
        "s_waitcnt lgkmcnt(12)\n\t"
        "v_mfma_f32_32x32x16_bf16 v[48:63], v[108:111], v[172:175], v[48:63]\n\t"
        "v_add_u32 v124, s46, v122\n\t"
        "ds_read_b64_tr_b16 v[108:109], v130 offset:6272\n\t"
        "ds_read_b64_tr_b16 v[110:111], v130 offset:9344\n\t"
        "s_waitcnt lgkmcnt(12)\n\t"
        "v_mfma_f32_32x32x16_bf16 v[64:79], v[172:175], v[112:115], v[64:79]\n\t"
        "ds_read_b64_tr_b16 v[112:113], v129 offset:6400\n\t"
        "ds_read_b64_tr_b16 v[114:115], v129 offset:9472\n\t"
        "s_waitcnt lgkmcnt(10)\n\t"
        "v_mfma_f32_32x32x16_bf16 v[80:95], v[172:175], v[116:119], v[80:95]\n\t"
        "v_cmp_gt_u32 vcc, s47, v126\n\t"
        "ds_read_b64_tr_b16 v[116:117], v130 offset:6400\n\t"
        "ds_read_b64_tr_b16 v[118:119], v130 offset:9472\n\t"
        "s_waitcnt vmcnt(14) lgkmcnt(10)\n\t"
        "v_mfma_f32_32x32x16_bf16 v[0:15], v[96:99], v[176:179], v[0:15]\n\t"
        "s_cbranch_vccnz 22f\n\t"
        "23:\n\t"
        "ds_read_b64_tr_b16 v[96:97], v123 offset:0\n\t"
        "ds_read_b64_tr_b16 v[98:99], v123 offset:3072\n\t"
        "s_waitcnt lgkmcnt(10)\n\t"
        "v_mfma_f32_32x32x16_bf16 v[16:31], v[100:103], v[176:179], v[16:31]\n\t"
        "v_add_u32 v128, 1, v128\n\t"
        "ds_read_b64_tr_b16 v[100:101], v124 offset:0\n\t"
        "ds_read_b64_tr_b16 v[102:103], v124 offset:3072\n\t"
        "s_waitcnt lgkmcnt(10)\n\t"
        "v_mfma_f32_32x32x16_bf16 v[32:47], v[104:107], v[176:179], v[32:47]\n\t"
        "ds_read_b64_tr_b16 v[104:105], v123 offset:128\n\t"
        "ds_read_b64_tr_b16 v[106:107], v123 offset:3200\n\t"
        "buffer_load_dwordx4 v[172:175], v120, s[40:43], s44 offen offset:2048 sc1\n\t"
        "s_waitcnt lgkmcnt(10)\n\t"
        "v_mfma_f32_32x32x16_bf16 v[48:63], v[108:111], v[176:179], v[48:63]\n\t"
        "s_add_u32 s46, s46, 12288\n\t"
        "s_add_u32 s47, s47, 1\n\t"
        "ds_read_b64_tr_b16 v[108:109], v124 offset:128\n\t"
        "ds_read_b64_tr_b16 v[110:111], v124 offset:3200\n\t"
        "s_waitcnt lgkmcnt(10)\n\t"
        "v_mfma_f32_32x32x16_bf16 v[64:79], v[176:179], v[112:115], v[64:79]\n\t"
        "s_cmp_eq_u32 s46, 110592\n\t"
        "s_cselect_b32 s46, 0, s46\n\t"
        "ds_read_b64_tr_b16 v[112:113], v123 offset:256\n\t"
        "ds_read_b64_tr_b16 v[114:115], v123 offset:3328\n\t"
        "s_waitcnt lgkmcnt(10)\n\t"
        "v_mfma_f32_32x32x16_bf16 v[80:95], v[176:179], v[116:119], v[80:95]\n\t"
        "ds_write_b32 v127, v128\n\t"
        "ds_read_b64_tr_b16 v[116:117], v124 offset:256\n\t"
        "ds_read_b64_tr_b16 v[118:119], v124 offset:3328\n\t"
        "s_waitcnt vmcnt(14) lgkmcnt(11)\n\t"
        "v_mfma_f32_32x32x16_bf16 v[0:15], v[96:99], v[180:183], v[0:15]\n\t"
        "ds_read_b32 v126, v125\n\t"
        "ds_read_b64_tr_b16 v[96:97], v123 offset:6144\n\t"
        "ds_read_b64_tr_b16 v[98:99], v123 offset:9216\n\t"
        "s_waitcnt lgkmcnt(12)\n\t"
        "v_mfma_f32_32x32x16_bf16 v[16:31], v[100:103], v[180:183], v[16:31]\n\t"
        "v_add_u32 v129, s46, v121\n\t"
        "ds_read_b64_tr_b16 v[100:101], v124 offset:6144\n\t"
        "ds_read_b64_tr_b16 v[102:103], v124 offset:9216\n\t"
        "s_waitcnt lgkmcnt(12)\n\t"
        "v_mfma_f32_32x32x16_bf16 v[32:47], v[104:107], v[180:183], v[32:47]\n\t"
        "ds_read_b64_tr_b16 v[104:105], v123 offset:6272\n\t"
        "ds_read_b64_tr_b16 v[106:107], v123 offset:9344\n\t"
        "buffer_load_dwordx4 v[176:179], v120, s[40:43], s44 offen offset:3072 sc1\n\t"
        "s_waitcnt lgkmcnt(12)\n\t"
        "v_mfma_f32_32x32x16_bf16 v[48:63], v[108:111], v[180:183], v[48:63]\n\t"
        "v_add_u32 v130, s46, v122\n\t"
        "ds_read_b64_tr_b16 v[108:109], v124 offset:6272\n\t"
        "ds_read_b64_tr_b16 v[110:111], v124 offset:9344\n\t"
        "s_waitcnt lgkmcnt(12)\n\t"
        "v_mfma_f32_32x32x16_bf16 v[64:79], v[180:183], v[112:115], v[64:79]\n\t"
        "ds_read_b64_tr_b16 v[112:113], v123 offset:6400\n\t"
        "ds_read_b64_tr_b16 v[114:115], v123 offset:9472\n\t"
        "s_waitcnt lgkmcnt(10)\n\t"
        "v_mfma_f32_32x32x16_bf16 v[80:95], v[180:183], v[116:119], v[80:95]\n\t"
        "v_cmp_gt_u32 vcc, s47, v126\n\t"
        "ds_read_b64_tr_b16 v[116:117], v124 offset:6400\n\t"
        "ds_read_b64_tr_b16 v[118:119], v124 offset:9472\n\t"
        "s_waitcnt vmcnt(14) lgkmcnt(10)\n\t"
        "v_mfma_f32_32x32x16_bf16 v[0:15], v[96:99], v[184:187], v[0:15]\n\t"
        "s_cbranch_vccnz 24f\n\t"
        "25:\n\t"
        "ds_read_b64_tr_b16 v[96:97], v129 offset:0\n\t"
        "ds_read_b64_tr_b16 v[98:99], v129 offset:3072\n\t"
        "s_waitcnt lgkmcnt(10)\n\t"
        "v_mfma_f32_32x32x16_bf16 v[16:31], v[100:103], v[184:187], v[16:31]\n\t"
        "v_add_u32 v128, 1, v128\n\t"
        "ds_read_b64_tr_b16 v[100:101], v130 offset:0\n\t"
        "ds_read_b64_tr_b16 v[102:103], v130 offset:3072\n\t"
        "s_waitcnt lgkmcnt(10)\n\t"
        "v_mfma_f32_32x32x16_bf16 v[32:47], v[104:107], v[184:187], v[32:47]\n\t"
        "ds_read_b64_tr_b16 v[104:105], v129 offset:128\n\t"
        "ds_read_b64_tr_b16 v[106:107], v129 offset:3200\n\t"
        "s_add_u32 s44, s44, 0x1000\n\t"
        "s_and_b32 s44, s44, 0xffff\n\t"
        "buffer_load_dwordx4 v[180:183], v120, s[40:43], s44 offen offset:0 sc1\n\t"
        "s_waitcnt lgkmcnt(10)\n\t"
        "v_mfma_f32_32x32x16_bf16 v[48:63], v[108:111], v[184:187], v[48:63]\n\t"
        "s_add_u32 s46, s46, 12288\n\t"
        "s_add_u32 s47, s47, 1\n\t"
        "ds_read_b64_tr_b16 v[108:109], v130 offset:128\n\t"
        "ds_read_b64_tr_b16 v[110:111], v130 offset:3200\n\t"
        "s_waitcnt lgkmcnt(10)\n\t"
        "v_mfma_f32_32x32x16_bf16 v[64:79], v[184:187], v[112:115], v[64:79]\n\t"
        "s_cmp_eq_u32 s46, 110592\n\t"
        "s_cselect_b32 s46, 0, s46\n\t"
        "ds_read_b64_tr_b16 v[112:113], v129 offset:256\n\t"
        "ds_read_b64_tr_b16 v[114:115], v129 offset:3328\n\t"
        "s_waitcnt lgkmcnt(10)\n\t"
        "v_mfma_f32_32x32x16_bf16 v[80:95], v[184:187], v[116:119], v[80:95]\n\t"
        "ds_write_b32 v127, v128\n\t"
        "ds_read_b64_tr_b16 v[116:117], v130 offset:256\n\t"
        "ds_read_b64_tr_b16 v[118:119], v130 offset:3328\n\t"
        "s_waitcnt vmcnt(14) lgkmcnt(11)\n\t"
        "v_mfma_f32_32x32x16_bf16 v[0:15], v[96:99], v[188:191], v[0:15]\n\t"
        "ds_read_b32 v126, v125\n\t"
        "ds_read_b64_tr_b16 v[96:97], v129 offset:6144\n\t"
        "ds_read_b64_tr_b16 v[98:99], v129 offset:9216\n\t"
        "s_waitcnt lgkmcnt(12)\n\t"
        "v_mfma_f32_32x32x16_bf16 v[16:31], v[100:103], v[188:191], v[16:31]\n\t"
        "v_add_u32 v123, s46, v121\n\t"
        "ds_read_b64_tr_b16 v[100:101], v130 offset:6144\n\t"
        "ds_read_b64_tr_b16 v[102:103], v130 offset:9216\n\t"
        "s_waitcnt lgkmcnt(12)\n\t"
        "v_mfma_f32_32x32x16_bf16 v[32:47], v[104:107], v[188:191], v[32:47]\n\t"
        "ds_read_b64_tr_b16 v[104:105], v129 offset:6272\n\t"
        "ds_read_b64_tr_b16 v[106:107], v129 offset:9344\n\t"
        "buffer_load_dwordx4 v[184:187], v120, s[40:43], s44 offen offset:1024 sc1\n\t"
        "s_waitcnt lgkmcnt(12)\n\t"
        "v_mfma_f32_32x32x16_bf16 v[48:63], v[108:111], v[188:191], v[48:63]\n\t"
        "v_add_u32 v124, s46, v122\n\t"
        "ds_read_b64_tr_b16 v[108:109], v130 offset:6272\n\t"
        "ds_read_b64_tr_b16 v[110:111], v130 offset:9344\n\t"
        "s_waitcnt lgkmcnt(12)\n\t"
        "v_mfma_f32_32x32x16_bf16 v[64:79], v[188:191], v[112:115], v[64:79]\n\t"
        "ds_read_b64_tr_b16 v[112:113], v129 offset:6400\n\t"
        "ds_read_b64_tr_b16 v[114:115], v129 offset:9472\n\t"
        "s_waitcnt lgkmcnt(10)\n\t"
        "v_mfma_f32_32x32x16_bf16 v[80:95], v[188:191], v[116:119], v[80:95]\n\t"
        "v_cmp_gt_u32 vcc, s47, v126\n\t"
        "ds_read_b64_tr_b16 v[116:117], v130 offset:6400\n\t"
        "ds_read_b64_tr_b16 v[118:119], v130 offset:9472\n\t"
        "s_waitcnt vmcnt(14) lgkmcnt(10)\n\t"
        "v_mfma_f32_32x32x16_bf16 v[0:15], v[96:99], v[192:195], v[0:15]\n\t"
        "s_cbranch_vccnz 26f\n\t"
        "27:\n\t"
        "ds_read_b64_tr_b16 v[96:97], v123 offset:0\n\t"
        "ds_read_b64_tr_b16 v[98:99], v123 offset:3072\n\t"
        "s_waitcnt lgkmcnt(10)\n\t"
        "v_mfma_f32_32x32x16_bf16 v[16:31], v[100:103], v[192:195], v[16:31]\n\t"
        "v_add_u32 v128, 1, v128\n\t"
        "ds_read_b64_tr_b16 v[100:101], v124 offset:0\n\t"
        "ds_read_b64_tr_b16 v[102:103], v124 offset:3072\n\t"
        "s_waitcnt lgkmcnt(10)\n\t"
        "v_mfma_f32_32x32x16_bf16 v[32:47], v[104:107], v[192:195], v[32:47]\n\t"
        "ds_read_b64_tr_b16 v[104:105], v123 offset:128\n\t"
        "ds_read_b64_tr_b16 v[106:107], v123 offset:3200\n\t"
        "buffer_load_dwordx4 v[188:191], v120, s[40:43], s44 offen offset:2048 sc1\n\t"
        "s_waitcnt lgkmcnt(10)\n\t"
        "v_mfma_f32_32x32x16_bf16 v[48:63], v[108:111], v[192:195], v[48:63]\n\t"
        "s_add_u32 s46, s46, 12288\n\t"
        "s_add_u32 s47, s47, 1\n\t"
        "ds_read_b64_tr_b16 v[108:109], v124 offset:128\n\t"
        "ds_read_b64_tr_b16 v[110:111], v124 offset:3200\n\t"
        "s_waitcnt lgkmcnt(10)\n\t"
        "v_mfma_f32_32x32x16_bf16 v[64:79], v[192:195], v[112:115], v[64:79]\n\t"
        "s_cmp_eq_u32 s46, 110592\n\t"
        "s_cselect_b32 s46, 0, s46\n\t"
        "ds_read_b64_tr_b16 v[112:113], v123 offset:256\n\t"
        "ds_read_b64_tr_b16 v[114:115], v123 offset:3328\n\t"
        "s_waitcnt lgkmcnt(10)\n\t"
        "v_mfma_f32_32x32x16_bf16 v[80:95], v[192:195], v[116:119], v[80:95]\n\t"
        "ds_write_b32 v127, v128\n\t"
        "ds_read_b64_tr_b16 v[116:117], v124 offset:256\n\t"
        "ds_read_b64_tr_b16 v[118:119], v124 offset:3328\n\t"
        "s_waitcnt vmcnt(14) lgkmcnt(11)\n\t"
        "v_mfma_f32_32x32x16_bf16 v[0:15], v[96:99], v[132:135], v[0:15]\n\t"
        "ds_read_b32 v126, v125\n\t"
        "ds_read_b64_tr_b16 v[96:97], v123 offset:6144\n\t"
        "ds_read_b64_tr_b16 v[98:99], v123 offset:9216\n\t"
        "s_waitcnt lgkmcnt(12)\n\t"
        "v_mfma_f32_32x32x16_bf16 v[16:31], v[100:103], v[132:135], v[16:31]\n\t"
        "v_add_u32 v129, s46, v121\n\t"
        "ds_read_b64_tr_b16 v[100:101], v124 offset:6144\n\t"
        "ds_read_b64_tr_b16 v[102:103], v124 offset:9216\n\t"
        "s_waitcnt lgkmcnt(12)\n\t"
        "v_mfma_f32_32x32x16_bf16 v[32:47], v[104:107], v[132:135], v[32:47]\n\t"
        "ds_read_b64_tr_b16 v[104:105], v123 offset:6272\n\t"
        "ds_read_b64_tr_b16 v[106:107], v123 offset:9344\n\t"
        "buffer_load_dwordx4 v[192:195], v120, s[40:43], s44 offen offset:3072 sc1\n\t"
        "s_waitcnt lgkmcnt(12)\n\t"
        "v_mfma_f32_32x32x16_bf16 v[48:63], v[108:111], v[132:135], v[48:63]\n\t"
        "v_add_u32 v130, s46, v122\n\t"
        "ds_read_b64_tr_b16 v[108:109], v124 offset:6272\n\t"
        "ds_read_b64_tr_b16 v[110:111], v124 offset:9344\n\t"
        "s_waitcnt lgkmcnt(12)\n\t"
        "v_mfma_f32_32x32x16_bf16 v[64:79], v[132:135], v[112:115], v[64:79]\n\t"
        "ds_read_b64_tr_b16 v[112:113], v123 offset:6400\n\t"
        "ds_read_b64_tr_b16 v[114:115], v123 offset:9472\n\t"
        "s_waitcnt lgkmcnt(10)\n\t"
        "v_mfma_f32_32x32x16_bf16 v[80:95], v[132:135], v[116:119], v[80:95]\n\t"
        "v_cmp_gt_u32 vcc, s47, v126\n\t"
        "ds_read_b64_tr_b16 v[116:117], v124 offset:6400\n\t"
        "ds_read_b64_tr_b16 v[118:119], v124 offset:9472\n\t"
        "s_waitcnt vmcnt(14) lgkmcnt(10)\n\t"
        "v_mfma_f32_32x32x16_bf16 v[0:15], v[96:99], v[136:139], v[0:15]\n\t"
        "s_cbranch_vccnz 28f\n\t"
        "29:\n\t"
        "ds_read_b64_tr_b16 v[96:97], v129 offset:0\n\t"
        "ds_read_b64_tr_b16 v[98:99], v129 offset:3072\n\t"
        "s_waitcnt lgkmcnt(10)\n\t"
        "v_mfma_f32_32x32x16_bf16 v[16:31], v[100:103], v[136:139], v[16:31]\n\t"
        "v_add_u32 v128, 1, v128\n\t"
        "ds_read_b64_tr_b16 v[100:101], v130 offset:0\n\t"
        "ds_read_b64_tr_b16 v[102:103], v130 offset:3072\n\t"
        "s_waitcnt lgkmcnt(10)\n\t"
        "v_mfma_f32_32x32x16_bf16 v[32:47], v[104:107], v[136:139], v[32:47]\n\t"
        "ds_read_b64_tr_b16 v[104:105], v129 offset:128\n\t"
        "ds_read_b64_tr_b16 v[106:107], v129 offset:3200\n\t"
        "s_add_u32 s44, s44, 0x1000\n\t"
        "s_and_b32 s44, s44, 0xffff\n\t"
        "buffer_load_dwordx4 v[132:135], v120, s[40:43], s44 offen offset:0 sc1\n\t"
        "s_waitcnt lgkmcnt(10)\n\t"
        "v_mfma_f32_32x32x16_bf16 v[48:63], v[108:111], v[136:139], v[48:63]\n\t"
        "s_add_u32 s46, s46, 12288\n\t"
        "s_add_u32 s47, s47, 1\n\t"
        "ds_read_b64_tr_b16 v[108:109], v130 offset:128\n\t"
        "ds_read_b64_tr_b16 v[110:111], v130 offset:3200\n\t"
        "s_waitcnt lgkmcnt(10)\n\t"
        "v_mfma_f32_32x32x16_bf16 v[64:79], v[136:139], v[112:115], v[64:79]\n\t"
        "s_cmp_eq_u32 s46, 110592\n\t"
        "s_cselect_b32 s46, 0, s46\n\t"
        "ds_read_b64_tr_b16 v[112:113], v129 offset:256\n\t"
        "ds_read_b64_tr_b16 v[114:115], v129 offset:3328\n\t"
        "s_waitcnt lgkmcnt(10)\n\t"
        "v_mfma_f32_32x32x16_bf16 v[80:95], v[136:139], v[116:119], v[80:95]\n\t"
        "ds_write_b32 v127, v128\n\t"
        "ds_read_b64_tr_b16 v[116:117], v130 offset:256\n\t"
        "ds_read_b64_tr_b16 v[118:119], v130 offset:3328\n\t"
        "s_waitcnt vmcnt(14) lgkmcnt(11)\n\t"
        "v_mfma_f32_32x32x16_bf16 v[0:15], v[96:99], v[140:143], v[0:15]\n\t"
        "ds_read_b32 v126, v125\n\t"
        "ds_read_b64_tr_b16 v[96:97], v129 offset:6144\n\t"
        "ds_read_b64_tr_b16 v[98:99], v129 offset:9216\n\t"
        "s_waitcnt lgkmcnt(12)\n\t"
        "v_mfma_f32_32x32x16_bf16 v[16:31], v[100:103], v[140:143], v[16:31]\n\t"
        "v_add_u32 v123, s46, v121\n\t"
        "ds_read_b64_tr_b16 v[100:101], v130 offset:6144\n\t"
        "ds_read_b64_tr_b16 v[102:103], v130 offset:9216\n\t"
        "s_waitcnt lgkmcnt(12)\n\t"
        "v_mfma_f32_32x32x16_bf16 v[32:47], v[104:107], v[140:143], v[32:47]\n\t"
        "ds_read_b64_tr_b16 v[104:105], v129 offset:6272\n\t"
        "ds_read_b64_tr_b16 v[106:107], v129 offset:9344\n\t"
        "buffer_load_dwordx4 v[136:139], v120, s[40:43], s44 offen offset:1024 sc1\n\t"
        "s_waitcnt lgkmcnt(12)\n\t"
        "v_mfma_f32_32x32x16_bf16 v[48:63], v[108:111], v[140:143], v[48:63]\n\t"
        "v_add_u32 v124, s46, v122\n\t"
        "ds_read_b64_tr_b16 v[108:109], v130 offset:6272\n\t"
        "ds_read_b64_tr_b16 v[110:111], v130 offset:9344\n\t"
        "s_waitcnt lgkmcnt(12)\n\t"
        "v_mfma_f32_32x32x16_bf16 v[64:79], v[140:143], v[112:115], v[64:79]\n\t"
        "ds_read_b64_tr_b16 v[112:113], v129 offset:6400\n\t"
        "ds_read_b64_tr_b16 v[114:115], v129 offset:9472\n\t"
        "s_waitcnt lgkmcnt(10)\n\t"
        "v_mfma_f32_32x32x16_bf16 v[80:95], v[140:143], v[116:119], v[80:95]\n\t"
        "v_cmp_gt_u32 vcc, s47, v126\n\t"
        "ds_read_b64_tr_b16 v[116:117], v130 offset:6400\n\t"
        "ds_read_b64_tr_b16 v[118:119], v130 offset:9472\n\t"
        "s_waitcnt vmcnt(14) lgkmcnt(10)\n\t"
        "v_mfma_f32_32x32x16_bf16 v[0:15], v[96:99], v[144:147], v[0:15]\n\t"
        "s_cbranch_vccnz 30f\n\t"
        "31:\n\t"
        "ds_read_b64_tr_b16 v[96:97], v123 offset:0\n\t"
        "ds_read_b64_tr_b16 v[98:99], v123 offset:3072\n\t"
        "s_waitcnt lgkmcnt(10)\n\t"
        "v_mfma_f32_32x32x16_bf16 v[16:31], v[100:103], v[144:147], v[16:31]\n\t"
        "v_add_u32 v128, 1, v128\n\t"
        "ds_read_b64_tr_b16 v[100:101], v124 offset:0\n\t"
        "ds_read_b64_tr_b16 v[102:103], v124 offset:3072\n\t"
        "s_waitcnt lgkmcnt(10)\n\t"
        "v_mfma_f32_32x32x16_bf16 v[32:47], v[104:107], v[144:147], v[32:47]\n\t"
        "ds_read_b64_tr_b16 v[104:105], v123 offset:128\n\t"
        "ds_read_b64_tr_b16 v[106:107], v123 offset:3200\n\t"
        "buffer_load_dwordx4 v[140:143], v120, s[40:43], s44 offen offset:2048 sc1\n\t"
        "s_waitcnt lgkmcnt(10)\n\t"
        "v_mfma_f32_32x32x16_bf16 v[48:63], v[108:111], v[144:147], v[48:63]\n\t"
        "s_add_u32 s46, s46, 12288\n\t"
        "s_add_u32 s47, s47, 1\n\t"
        "ds_read_b64_tr_b16 v[108:109], v124 offset:128\n\t"
        "ds_read_b64_tr_b16 v[110:111], v124 offset:3200\n\t"
        "s_waitcnt lgkmcnt(10)\n\t"
        "v_mfma_f32_32x32x16_bf16 v[64:79], v[144:147], v[112:115], v[64:79]\n\t"
        "s_cmp_eq_u32 s46, 110592\n\t"
        "s_cselect_b32 s46, 0, s46\n\t"
        "ds_read_b64_tr_b16 v[112:113], v123 offset:256\n\t"
        "ds_read_b64_tr_b16 v[114:115], v123 offset:3328\n\t"
        "s_waitcnt lgkmcnt(10)\n\t"
        "v_mfma_f32_32x32x16_bf16 v[80:95], v[144:147], v[116:119], v[80:95]\n\t"
        "ds_write_b32 v127, v128\n\t"
        "ds_read_b64_tr_b16 v[116:117], v124 offset:256\n\t"
        "ds_read_b64_tr_b16 v[118:119], v124 offset:3328\n\t"
        "s_sub_u32 s45, s45, 1\n\t"
        "s_cmp_lg_u32 s45, 0\n\t"
        "s_cbranch_scc1 1b\n\t"
        "s_waitcnt vmcnt(14) lgkmcnt(11)\n\t"
        "v_mfma_f32_32x32x16_bf16 v[0:15], v[96:99], v[148:151], v[0:15]\n\t"
        "ds_read_b32 v126, v125\n\t"
        "ds_read_b64_tr_b16 v[96:97], v123 offset:6144\n\t"
        "ds_read_b64_tr_b16 v[98:99], v123 offset:9216\n\t"
        "s_waitcnt lgkmcnt(12)\n\t"
        "v_mfma_f32_32x32x16_bf16 v[16:31], v[100:103], v[148:151], v[16:31]\n\t"
        "v_add_u32 v129, s46, v121\n\t"
        "ds_read_b64_tr_b16 v[100:101], v124 offset:6144\n\t"
        "ds_read_b64_tr_b16 v[102:103], v124 offset:9216\n\t"
        "s_waitcnt lgkmcnt(12)\n\t"
        "v_mfma_f32_32x32x16_bf16 v[32:47], v[104:107], v[148:151], v[32:47]\n\t"
        "ds_read_b64_tr_b16 v[104:105], v123 offset:6272\n\t"
        "ds_read_b64_tr_b16 v[106:107], v123 offset:9344\n\t"
        "s_waitcnt lgkmcnt(12)\n\t"
        "v_mfma_f32_32x32x16_bf16 v[48:63], v[108:111], v[148:151], v[48:63]\n\t"
        "v_add_u32 v130, s46, v122\n\t"
        "ds_read_b64_tr_b16 v[108:109], v124 offset:6272\n\t"
        "ds_read_b64_tr_b16 v[110:111], v124 offset:9344\n\t"
        "s_waitcnt lgkmcnt(12)\n\t"
        "v_mfma_f32_32x32x16_bf16 v[64:79], v[148:151], v[112:115], v[64:79]\n\t"
        "ds_read_b64_tr_b16 v[112:113], v123 offset:6400\n\t"
        "ds_read_b64_tr_b16 v[114:115], v123 offset:9472\n\t"
        "s_waitcnt lgkmcnt(10)\n\t"
        "v_mfma_f32_32x32x16_bf16 v[80:95], v[148:151], v[116:119], v[80:95]\n\t"
        "v_cmp_gt_u32 vcc, s47, v126\n\t"
        "ds_read_b64_tr_b16 v[116:117], v124 offset:6400\n\t"
        "ds_read_b64_tr_b16 v[118:119], v124 offset:9472\n\t"
        "s_waitcnt vmcnt(13) lgkmcnt(10)\n\t"
        "v_mfma_f32_32x32x16_bf16 v[0:15], v[96:99], v[152:155], v[0:15]\n\t"
        "s_cbranch_vccnz 64f\n\t"
        "65:\n\t"
        "ds_read_b64_tr_b16 v[96:97], v129 offset:0\n\t"
        "ds_read_b64_tr_b16 v[98:99], v129 offset:3072\n\t"
        "s_waitcnt lgkmcnt(10)\n\t"
        "v_mfma_f32_32x32x16_bf16 v[16:31], v[100:103], v[152:155], v[16:31]\n\t"
        "v_add_u32 v128, 1, v128\n\t"
        "ds_read_b64_tr_b16 v[100:101], v130 offset:0\n\t"
        "ds_read_b64_tr_b16 v[102:103], v130 offset:3072\n\t"
        "s_waitcnt lgkmcnt(10)\n\t"
        "v_mfma_f32_32x32x16_bf16 v[32:47], v[104:107], v[152:155], v[32:47]\n\t"
        "ds_read_b64_tr_b16 v[104:105], v129 offset:128\n\t"
        "ds_read_b64_tr_b16 v[106:107], v129 offset:3200\n\t"
        "s_waitcnt lgkmcnt(10)\n\t"
        "v_mfma_f32_32x32x16_bf16 v[48:63], v[108:111], v[152:155], v[48:63]\n\t"
        "s_add_u32 s46, s46, 12288\n\t"
        "s_add_u32 s47, s47, 1\n\t"
        "ds_read_b64_tr_b16 v[108:109], v130 offset:128\n\t"
        "ds_read_b64_tr_b16 v[110:111], v130 offset:3200\n\t"
        "s_waitcnt lgkmcnt(10)\n\t"
        "v_mfma_f32_32x32x16_bf16 v[64:79], v[152:155], v[112:115], v[64:79]\n\t"
        "s_cmp_eq_u32 s46, 110592\n\t"
        "s_cselect_b32 s46, 0, s46\n\t"
        "ds_read_b64_tr_b16 v[112:113], v129 offset:256\n\t"
        "ds_read_b64_tr_b16 v[114:115], v129 offset:3328\n\t"
        "s_waitcnt lgkmcnt(10)\n\t"
        "v_mfma_f32_32x32x16_bf16 v[80:95], v[152:155], v[116:119], v[80:95]\n\t"
        "ds_write_b32 v127, v128\n\t"
        "ds_read_b64_tr_b16 v[116:117], v130 offset:256\n\t"
        "ds_read_b64_tr_b16 v[118:119], v130 offset:3328\n\t"
        "s_waitcnt vmcnt(12) lgkmcnt(11)\n\t"
        "v_mfma_f32_32x32x16_bf16 v[0:15], v[96:99], v[156:159], v[0:15]\n\t"
        "ds_read_b32 v126, v125\n\t"
        "ds_read_b64_tr_b16 v[96:97], v129 offset:6144\n\t"
        "ds_read_b64_tr_b16 v[98:99], v129 offset:9216\n\t"
        "s_waitcnt lgkmcnt(12)\n\t"
        "v_mfma_f32_32x32x16_bf16 v[16:31], v[100:103], v[156:159], v[16:31]\n\t"
        "v_add_u32 v123, s46, v121\n\t"
        "ds_read_b64_tr_b16 v[100:101], v130 offset:6144\n\t"
        "ds_read_b64_tr_b16 v[102:103], v130 offset:9216\n\t"
        "s_waitcnt lgkmcnt(12)\n\t"
        "v_mfma_f32_32x32x16_bf16 v[32:47], v[104:107], v[156:159], v[32:47]\n\t"
        "ds_read_b64_tr_b16 v[104:105], v129 offset:6272\n\t"
        "ds_read_b64_tr_b16 v[106:107], v129 offset:9344\n\t"
        "s_waitcnt lgkmcnt(12)\n\t"
        "v_mfma_f32_32x32x16_bf16 v[48:63], v[108:111], v[156:159], v[48:63]\n\t"
        "v_add_u32 v124, s46, v122\n\t"
        "ds_read_b64_tr_b16 v[108:109], v130 offset:6272\n\t"
        "ds_read_b64_tr_b16 v[110:111], v130 offset:9344\n\t"
        "s_waitcnt lgkmcnt(12)\n\t"
        "v_mfma_f32_32x32x16_bf16 v[64:79], v[156:159], v[112:115], v[64:79]\n\t"
        "ds_read_b64_tr_b16 v[112:113], v129 offset:6400\n\t"
        "ds_read_b64_tr_b16 v[114:115], v129 offset:9472\n\t"
        "s_waitcnt lgkmcnt(10)\n\t"
        "v_mfma_f32_32x32x16_bf16 v[80:95], v[156:159], v[116:119], v[80:95]\n\t"
        "v_cmp_gt_u32 vcc, s47, v126\n\t"
        "ds_read_b64_tr_b16 v[116:117], v130 offset:6400\n\t"
        "ds_read_b64_tr_b16 v[118:119], v130 offset:9472\n\t"
        "s_waitcnt vmcnt(11) lgkmcnt(10)\n\t"
        "v_mfma_f32_32x32x16_bf16 v[0:15], v[96:99], v[160:163], v[0:15]\n\t"
        "s_cbranch_vccnz 66f\n\t"
        "67:\n\t"
        "ds_read_b64_tr_b16 v[96:97], v123 offset:0\n\t"
        "ds_read_b64_tr_b16 v[98:99], v123 offset:3072\n\t"
        "s_waitcnt lgkmcnt(10)\n\t"
        "v_mfma_f32_32x32x16_bf16 v[16:31], v[100:103], v[160:163], v[16:31]\n\t"
        "v_add_u32 v128, 1, v128\n\t"
        "ds_read_b64_tr_b16 v[100:101], v124 offset:0\n\t"
        "ds_read_b64_tr_b16 v[102:103], v124 offset:3072\n\t"
        "s_waitcnt lgkmcnt(10)\n\t"
        "v_mfma_f32_32x32x16_bf16 v[32:47], v[104:107], v[160:163], v[32:47]\n\t"
        "ds_read_b64_tr_b16 v[104:105], v123 offset:128\n\t"
        "ds_read_b64_tr_b16 v[106:107], v123 offset:3200\n\t"
        "s_waitcnt lgkmcnt(10)\n\t"
        "v_mfma_f32_32x32x16_bf16 v[48:63], v[108:111], v[160:163], v[48:63]\n\t"
        "s_add_u32 s46, s46, 12288\n\t"
        "s_add_u32 s47, s47, 1\n\t"
        "ds_read_b64_tr_b16 v[108:109], v124 offset:128\n\t"
        "ds_read_b64_tr_b16 v[110:111], v124 offset:3200\n\t"
        "s_waitcnt lgkmcnt(10)\n\t"
        "v_mfma_f32_32x32x16_bf16 v[64:79], v[160:163], v[112:115], v[64:79]\n\t"
        "s_cmp_eq_u32 s46, 110592\n\t"
        "s_cselect_b32 s46, 0, s46\n\t"
        "ds_read_b64_tr_b16 v[112:113], v123 offset:256\n\t"
        "ds_read_b64_tr_b16 v[114:115], v123 offset:3328\n\t"
        "s_waitcnt lgkmcnt(10)\n\t"
        "v_mfma_f32_32x32x16_bf16 v[80:95], v[160:163], v[116:119], v[80:95]\n\t"
        "ds_write_b32 v127, v128\n\t"
        "ds_read_b64_tr_b16 v[116:117], v124 offset:256\n\t"
        "ds_read_b64_tr_b16 v[118:119], v124 offset:3328\n\t"
        "s_waitcnt vmcnt(10) lgkmcnt(11)\n\t"
        "v_mfma_f32_32x32x16_bf16 v[0:15], v[96:99], v[164:167], v[0:15]\n\t"
        "ds_read_b32 v126, v125\n\t"
        "ds_read_b64_tr_b16 v[96:97], v123 offset:6144\n\t"
        "ds_read_b64_tr_b16 v[98:99], v123 offset:9216\n\t"
        "s_waitcnt lgkmcnt(12)\n\t"
        "v_mfma_f32_32x32x16_bf16 v[16:31], v[100:103], v[164:167], v[16:31]\n\t"
        "v_add_u32 v129, s46, v121\n\t"
        "ds_read_b64_tr_b16 v[100:101], v124 offset:6144\n\t"
        "ds_read_b64_tr_b16 v[102:103], v124 offset:9216\n\t"
        "s_waitcnt lgkmcnt(12)\n\t"
        "v_mfma_f32_32x32x16_bf16 v[32:47], v[104:107], v[164:167], v[32:47]\n\t"
        "ds_read_b64_tr_b16 v[104:105], v123 offset:6272\n\t"
        "ds_read_b64_tr_b16 v[106:107], v123 offset:9344\n\t"
        "s_waitcnt lgkmcnt(12)\n\t"
        "v_mfma_f32_32x32x16_bf16 v[48:63], v[108:111], v[164:167], v[48:63]\n\t"
        "v_add_u32 v130, s46, v122\n\t"
        "ds_read_b64_tr_b16 v[108:109], v124 offset:6272\n\t"
        "ds_read_b64_tr_b16 v[110:111], v124 offset:9344\n\t"
        "s_waitcnt lgkmcnt(12)\n\t"
        "v_mfma_f32_32x32x16_bf16 v[64:79], v[164:167], v[112:115], v[64:79]\n\t"
        "ds_read_b64_tr_b16 v[112:113], v123 offset:6400\n\t"
        "ds_read_b64_tr_b16 v[114:115], v123 offset:9472\n\t"
        "s_waitcnt lgkmcnt(10)\n\t"
        "v_mfma_f32_32x32x16_bf16 v[80:95], v[164:167], v[116:119], v[80:95]\n\t"
        "v_cmp_gt_u32 vcc, s47, v126\n\t"
        "ds_read_b64_tr_b16 v[116:117], v124 offset:6400\n\t"
        "ds_read_b64_tr_b16 v[118:119], v124 offset:9472\n\t"
        "s_waitcnt vmcnt(9) lgkmcnt(10)\n\t"
        "v_mfma_f32_32x32x16_bf16 v[0:15], v[96:99], v[168:171], v[0:15]\n\t"
        "s_cbranch_vccnz 68f\n\t"
        "69:\n\t"
        "ds_read_b64_tr_b16 v[96:97], v129 offset:0\n\t"
        "ds_read_b64_tr_b16 v[98:99], v129 offset:3072\n\t"
        "s_waitcnt lgkmcnt(10)\n\t"
        "v_mfma_f32_32x32x16_bf16 v[16:31], v[100:103], v[168:171], v[16:31]\n\t"
        "v_add_u32 v128, 1, v128\n\t"
        "ds_read_b64_tr_b16 v[100:101], v130 offset:0\n\t"
        "ds_read_b64_tr_b16 v[102:103], v130 offset:3072\n\t"
        "s_waitcnt lgkmcnt(10)\n\t"
        "v_mfma_f32_32x32x16_bf16 v[32:47], v[104:107], v[168:171], v[32:47]\n\t"
        "ds_read_b64_tr_b16 v[104:105], v129 offset:128\n\t"
        "ds_read_b64_tr_b16 v[106:107], v129 offset:3200\n\t"
        "s_waitcnt lgkmcnt(10)\n\t"
        "v_mfma_f32_32x32x16_bf16 v[48:63], v[108:111], v[168:171], v[48:63]\n\t"
        "s_add_u32 s46, s46, 12288\n\t"
        "s_add_u32 s47, s47, 1\n\t"
        "ds_read_b64_tr_b16 v[108:109], v130 offset:128\n\t"
        "ds_read_b64_tr_b16 v[110:111], v130 offset:3200\n\t"
        "s_waitcnt lgkmcnt(10)\n\t"
        "v_mfma_f32_32x32x16_bf16 v[64:79], v[168:171], v[112:115], v[64:79]\n\t"
        "s_cmp_eq_u32 s46, 110592\n\t"
        "s_cselect_b32 s46, 0, s46\n\t"
        "ds_read_b64_tr_b16 v[112:113], v129 offset:256\n\t"
        "ds_read_b64_tr_b16 v[114:115], v129 offset:3328\n\t"
        "s_waitcnt lgkmcnt(10)\n\t"
        "v_mfma_f32_32x32x16_bf16 v[80:95], v[168:171], v[116:119], v[80:95]\n\t"
        "ds_write_b32 v127, v128\n\t"
        "ds_read_b64_tr_b16 v[116:117], v130 offset:256\n\t"
        "ds_read_b64_tr_b16 v[118:119], v130 offset:3328\n\t"
        "s_waitcnt vmcnt(8) lgkmcnt(11)\n\t"
        "v_mfma_f32_32x32x16_bf16 v[0:15], v[96:99], v[172:175], v[0:15]\n\t"
        "ds_read_b32 v126, v125\n\t"
        "ds_read_b64_tr_b16 v[96:97], v129 offset:6144\n\t"
        "ds_read_b64_tr_b16 v[98:99], v129 offset:9216\n\t"
        "s_waitcnt lgkmcnt(12)\n\t"
        "v_mfma_f32_32x32x16_bf16 v[16:31], v[100:103], v[172:175], v[16:31]\n\t"
        "v_add_u32 v123, s46, v121\n\t"
        "ds_read_b64_tr_b16 v[100:101], v130 offset:6144\n\t"
        "ds_read_b64_tr_b16 v[102:103], v130 offset:9216\n\t"
        "s_waitcnt lgkmcnt(12)\n\t"
        "v_mfma_f32_32x32x16_bf16 v[32:47], v[104:107], v[172:175], v[32:47]\n\t"
        "ds_read_b64_tr_b16 v[104:105], v129 offset:6272\n\t"
        "ds_read_b64_tr_b16 v[106:107], v129 offset:9344\n\t"
        "s_waitcnt lgkmcnt(12)\n\t"
        "v_mfma_f32_32x32x16_bf16 v[48:63], v[108:111], v[172:175], v[48:63]\n\t"
        "v_add_u32 v124, s46, v122\n\t"
        "ds_read_b64_tr_b16 v[108:109], v130 offset:6272\n\t"
        "ds_read_b64_tr_b16 v[110:111], v130 offset:9344\n\t"
        "s_waitcnt lgkmcnt(12)\n\t"
        "v_mfma_f32_32x32x16_bf16 v[64:79], v[172:175], v[112:115], v[64:79]\n\t"
        "ds_read_b64_tr_b16 v[112:113], v129 offset:6400\n\t"
        "ds_read_b64_tr_b16 v[114:115], v129 offset:9472\n\t"
        "s_waitcnt lgkmcnt(10)\n\t"
        "v_mfma_f32_32x32x16_bf16 v[80:95], v[172:175], v[116:119], v[80:95]\n\t"
        "v_cmp_gt_u32 vcc, s47, v126\n\t"
        "ds_read_b64_tr_b16 v[116:117], v130 offset:6400\n\t"
        "ds_read_b64_tr_b16 v[118:119], v130 offset:9472\n\t"
        "s_waitcnt vmcnt(7) lgkmcnt(10)\n\t"
        "v_mfma_f32_32x32x16_bf16 v[0:15], v[96:99], v[176:179], v[0:15]\n\t"
        "s_cbranch_vccnz 70f\n\t"
        "71:\n\t"
        "ds_read_b64_tr_b16 v[96:97], v123 offset:0\n\t"
        "ds_read_b64_tr_b16 v[98:99], v123 offset:3072\n\t"
        "s_waitcnt lgkmcnt(10)\n\t"
        "v_mfma_f32_32x32x16_bf16 v[16:31], v[100:103], v[176:179], v[16:31]\n\t"
        "v_add_u32 v128, 1, v128\n\t"
        "ds_read_b64_tr_b16 v[100:101], v124 offset:0\n\t"
        "ds_read_b64_tr_b16 v[102:103], v124 offset:3072\n\t"
        "s_waitcnt lgkmcnt(10)\n\t"
        "v_mfma_f32_32x32x16_bf16 v[32:47], v[104:107], v[176:179], v[32:47]\n\t"
        "ds_read_b64_tr_b16 v[104:105], v123 offset:128\n\t"
        "ds_read_b64_tr_b16 v[106:107], v123 offset:3200\n\t"
        "s_waitcnt lgkmcnt(10)\n\t"
        "v_mfma_f32_32x32x16_bf16 v[48:63], v[108:111], v[176:179], v[48:63]\n\t"
        "s_add_u32 s46, s46, 12288\n\t"
        "s_add_u32 s47, s47, 1\n\t"
        "ds_read_b64_tr_b16 v[108:109], v124 offset:128\n\t"
        "ds_read_b64_tr_b16 v[110:111], v124 offset:3200\n\t"
        "s_waitcnt lgkmcnt(10)\n\t"
        "v_mfma_f32_32x32x16_bf16 v[64:79], v[176:179], v[112:115], v[64:79]\n\t"
        "s_cmp_eq_u32 s46, 110592\n\t"
        "s_cselect_b32 s46, 0, s46\n\t"
        "ds_read_b64_tr_b16 v[112:113], v123 offset:256\n\t"
        "ds_read_b64_tr_b16 v[114:115], v123 offset:3328\n\t"
        "s_waitcnt lgkmcnt(10)\n\t"
        "v_mfma_f32_32x32x16_bf16 v[80:95], v[176:179], v[116:119], v[80:95]\n\t"
        "ds_write_b32 v127, v128\n\t"
        "ds_read_b64_tr_b16 v[116:117], v124 offset:256\n\t"
        "ds_read_b64_tr_b16 v[118:119], v124 offset:3328\n\t"
        "s_waitcnt vmcnt(6) lgkmcnt(11)\n\t"
        "v_mfma_f32_32x32x16_bf16 v[0:15], v[96:99], v[180:183], v[0:15]\n\t"
        "ds_read_b32 v126, v125\n\t"
        "ds_read_b64_tr_b16 v[96:97], v123 offset:6144\n\t"
        "ds_read_b64_tr_b16 v[98:99], v123 offset:9216\n\t"
        "s_waitcnt lgkmcnt(12)\n\t"
        "v_mfma_f32_32x32x16_bf16 v[16:31], v[100:103], v[180:183], v[16:31]\n\t"
        "v_add_u32 v129, s46, v121\n\t"
        "ds_read_b64_tr_b16 v[100:101], v124 offset:6144\n\t"
        "ds_read_b64_tr_b16 v[102:103], v124 offset:9216\n\t"
        "s_waitcnt lgkmcnt(12)\n\t"
        "v_mfma_f32_32x32x16_bf16 v[32:47], v[104:107], v[180:183], v[32:47]\n\t"
        "ds_read_b64_tr_b16 v[104:105], v123 offset:6272\n\t"
        "ds_read_b64_tr_b16 v[106:107], v123 offset:9344\n\t"
        "s_waitcnt lgkmcnt(12)\n\t"
        "v_mfma_f32_32x32x16_bf16 v[48:63], v[108:111], v[180:183], v[48:63]\n\t"
        "v_add_u32 v130, s46, v122\n\t"
        "ds_read_b64_tr_b16 v[108:109], v124 offset:6272\n\t"
        "ds_read_b64_tr_b16 v[110:111], v124 offset:9344\n\t"
        "s_waitcnt lgkmcnt(12)\n\t"
        "v_mfma_f32_32x32x16_bf16 v[64:79], v[180:183], v[112:115], v[64:79]\n\t"
        "ds_read_b64_tr_b16 v[112:113], v123 offset:6400\n\t"
        "ds_read_b64_tr_b16 v[114:115], v123 offset:9472\n\t"
        "s_waitcnt lgkmcnt(10)\n\t"
        "v_mfma_f32_32x32x16_bf16 v[80:95], v[180:183], v[116:119], v[80:95]\n\t"
        "v_cmp_gt_u32 vcc, s47, v126\n\t"
        "ds_read_b64_tr_b16 v[116:117], v124 offset:6400\n\t"
        "ds_read_b64_tr_b16 v[118:119], v124 offset:9472\n\t"
        "s_waitcnt vmcnt(5) lgkmcnt(10)\n\t"
        "v_mfma_f32_32x32x16_bf16 v[0:15], v[96:99], v[184:187], v[0:15]\n\t"
        "s_cbranch_vccnz 72f\n\t"
        "73:\n\t"
        "ds_read_b64_tr_b16 v[96:97], v129 offset:0\n\t"
        "ds_read_b64_tr_b16 v[98:99], v129 offset:3072\n\t"
        "s_waitcnt lgkmcnt(10)\n\t"
        "v_mfma_f32_32x32x16_bf16 v[16:31], v[100:103], v[184:187], v[16:31]\n\t"
        "v_add_u32 v128, 1, v128\n\t"
        "ds_read_b64_tr_b16 v[100:101], v130 offset:0\n\t"
        "ds_read_b64_tr_b16 v[102:103], v130 offset:3072\n\t"
        "s_waitcnt lgkmcnt(10)\n\t"
        "v_mfma_f32_32x32x16_bf16 v[32:47], v[104:107], v[184:187], v[32:47]\n\t"
        "ds_read_b64_tr_b16 v[104:105], v129 offset:128\n\t"
        "ds_read_b64_tr_b16 v[106:107], v129 offset:3200\n\t"
        "s_waitcnt lgkmcnt(10)\n\t"
        "v_mfma_f32_32x32x16_bf16 v[48:63], v[108:111], v[184:187], v[48:63]\n\t"
        "s_add_u32 s46, s46, 12288\n\t"
        "s_add_u32 s47, s47, 1\n\t"
        "ds_read_b64_tr_b16 v[108:109], v130 offset:128\n\t"
        "ds_read_b64_tr_b16 v[110:111], v130 offset:3200\n\t"
        "s_waitcnt lgkmcnt(10)\n\t"
        "v_mfma_f32_32x32x16_bf16 v[64:79], v[184:187], v[112:115], v[64:79]\n\t"
        "s_cmp_eq_u32 s46, 110592\n\t"
        "s_cselect_b32 s46, 0, s46\n\t"
        "ds_read_b64_tr_b16 v[112:113], v129 offset:256\n\t"
        "ds_read_b64_tr_b16 v[114:115], v129 offset:3328\n\t"
        "s_waitcnt lgkmcnt(10)\n\t"
        "v_mfma_f32_32x32x16_bf16 v[80:95], v[184:187], v[116:119], v[80:95]\n\t"
        "ds_write_b32 v127, v128\n\t"
        "ds_read_b64_tr_b16 v[116:117], v130 offset:256\n\t"
        "ds_read_b64_tr_b16 v[118:119], v130 offset:3328\n\t"
        "s_waitcnt vmcnt(4) lgkmcnt(11)\n\t"
        "v_mfma_f32_32x32x16_bf16 v[0:15], v[96:99], v[188:191], v[0:15]\n\t"
        "ds_read_b64_tr_b16 v[96:97], v129 offset:6144\n\t"
        "ds_read_b64_tr_b16 v[98:99], v129 offset:9216\n\t"
        "s_waitcnt lgkmcnt(11)\n\t"
        "v_mfma_f32_32x32x16_bf16 v[16:31], v[100:103], v[188:191], v[16:31]\n\t"
        "ds_read_b64_tr_b16 v[100:101], v130 offset:6144\n\t"
        "ds_read_b64_tr_b16 v[102:103], v130 offset:9216\n\t"
        "s_waitcnt lgkmcnt(11)\n\t"
        "v_mfma_f32_32x32x16_bf16 v[32:47], v[104:107], v[188:191], v[32:47]\n\t"
        "ds_read_b64_tr_b16 v[104:105], v129 offset:6272\n\t"
        "ds_read_b64_tr_b16 v[106:107], v129 offset:9344\n\t"
        "s_waitcnt lgkmcnt(11)\n\t"
        "v_mfma_f32_32x32x16_bf16 v[48:63], v[108:111], v[188:191], v[48:63]\n\t"
        "ds_read_b64_tr_b16 v[108:109], v130 offset:6272\n\t"
        "ds_read_b64_tr_b16 v[110:111], v130 offset:9344\n\t"
        "s_waitcnt lgkmcnt(11)\n\t"
        "v_mfma_f32_32x32x16_bf16 v[64:79], v[188:191], v[112:115], v[64:79]\n\t"
        "ds_read_b64_tr_b16 v[112:113], v129 offset:6400\n\t"
        "ds_read_b64_tr_b16 v[114:115], v129 offset:9472\n\t"
        "s_waitcnt lgkmcnt(10)\n\t"
        "v_mfma_f32_32x32x16_bf16 v[80:95], v[188:191], v[116:119], v[80:95]\n\t"
        "ds_read_b64_tr_b16 v[116:117], v130 offset:6400\n\t"
        "ds_read_b64_tr_b16 v[118:119], v130 offset:9472\n\t"
        "s_waitcnt vmcnt(3) lgkmcnt(10)\n\t"
        "v_mfma_f32_32x32x16_bf16 v[0:15], v[96:99], v[192:195], v[0:15]\n\t"
        "s_waitcnt lgkmcnt(8)\n\t"
        "v_mfma_f32_32x32x16_bf16 v[16:31], v[100:103], v[192:195], v[16:31]\n\t"
        "s_waitcnt lgkmcnt(6)\n\t"
        "v_mfma_f32_32x32x16_bf16 v[32:47], v[104:107], v[192:195], v[32:47]\n\t"
        "s_waitcnt lgkmcnt(4)\n\t"
        "v_mfma_f32_32x32x16_bf16 v[48:63], v[108:111], v[192:195], v[48:63]\n\t"
        "s_waitcnt lgkmcnt(2)\n\t"
        "v_mfma_f32_32x32x16_bf16 v[64:79], v[192:195], v[112:115], v[64:79]\n\t"
        "s_waitcnt lgkmcnt(0)\n\t"
        "v_mfma_f32_32x32x16_bf16 v[80:95], v[192:195], v[116:119], v[80:95]\n\t"
        "s_waitcnt vmcnt(0)\n\t"
        "s_nop 10\n\t"
        "s_branch 9f\n\t"
        "16:\n\t"
        "s_mov_b32 s48, 0x10000\n\t"
        "8:\n\t"
        "ds_read_b32 v126, v125\n\t"
        "s_sub_u32 s48, s48, 1\n\t"
        "s_waitcnt lgkmcnt(0)\n\t"
        "s_cbranch_scc1 17b\n\t"
        "v_cmp_gt_u32 vcc, s47, v126\n\t"
        "s_cbranch_vccnz 8b\n\t"
        "s_branch 17b\n\t"
        "18:\n\t"
        "s_mov_b32 s48, 0x10000\n\t"
        "8:\n\t"
        "ds_read_b32 v126, v125\n\t"
        "s_sub_u32 s48, s48, 1\n\t"
        "s_waitcnt lgkmcnt(0)\n\t"
        "s_cbranch_scc1 19b\n\t"
        "v_cmp_gt_u32 vcc, s47, v126\n\t"
        "s_cbranch_vccnz 8b\n\t"
        "s_branch 19b\n\t"
        "20:\n\t"
        "s_mov_b32 s48, 0x10000\n\t"
        "8:\n\t"
        "ds_read_b32 v126, v125\n\t"
        "s_sub_u32 s48, s48, 1\n\t"
        "s_waitcnt lgkmcnt(0)\n\t"
        "s_cbranch_scc1 21b\n\t"
        "v_cmp_gt_u32 vcc, s47, v126\n\t"
        "s_cbranch_vccnz 8b\n\t"
        "s_branch 21b\n\t"
        "22:\n\t"
        "s_mov_b32 s48, 0x10000\n\t"
        "8:\n\t"
        "ds_read_b32 v126, v125\n\t"
        "s_sub_u32 s48, s48, 1\n\t"
        "s_waitcnt lgkmcnt(0)\n\t"
        "s_cbranch_scc1 23b\n\t"
        "v_cmp_gt_u32 vcc, s47, v126\n\t"
        "s_cbranch_vccnz 8b\n\t"
        "s_branch 23b\n\t"
        "24:\n\t"
        "s_mov_b32 s48, 0x10000\n\t"
        "8:\n\t"
        "ds_read_b32 v126, v125\n\t"
        "s_sub_u32 s48, s48, 1\n\t"
        "s_waitcnt lgkmcnt(0)\n\t"
        "s_cbranch_scc1 25b\n\t"
        "v_cmp_gt_u32 vcc, s47, v126\n\t"
        "s_cbranch_vccnz 8b\n\t"
        "s_branch 25b\n\t"
        "26:\n\t"
        "s_mov_b32 s48, 0x10000\n\t"
        "8:\n\t"
        "ds_read_b32 v126, v125\n\t"
        "s_sub_u32 s48, s48, 1\n\t"
        "s_waitcnt lgkmcnt(0)\n\t"
        "s_cbranch_scc1 27b\n\t"
        "v_cmp_gt_u32 vcc, s47, v126\n\t"
        "s_cbranch_vccnz 8b\n\t"
        "s_branch 27b\n\t"
        "28:\n\t"
        "s_mov_b32 s48, 0x10000\n\t"
        "8:\n\t"
        "ds_read_b32 v126, v125\n\t"
        "s_sub_u32 s48, s48, 1\n\t"
        "s_waitcnt lgkmcnt(0)\n\t"
        "s_cbranch_scc1 29b\n\t"
        "v_cmp_gt_u32 vcc, s47, v126\n\t"
        "s_cbranch_vccnz 8b\n\t"
        "s_branch 29b\n\t"
        "30:\n\t"
        "s_mov_b32 s48, 0x10000\n\t"
        "8:\n\t"
        "ds_read_b32 v126, v125\n\t"
        "s_sub_u32 s48, s48, 1\n\t"
        "s_waitcnt lgkmcnt(0)\n\t"
        "s_cbranch_scc1 31b\n\t"
        "v_cmp_gt_u32 vcc, s47, v126\n\t"
        "s_cbranch_vccnz 8b\n\t"
        "s_branch 31b\n\t"
        "64:\n\t"
        "s_mov_b32 s48, 0x10000\n\t"
        "8:\n\t"
        "ds_read_b32 v126, v125\n\t"
        "s_sub_u32 s48, s48, 1\n\t"
        "s_waitcnt lgkmcnt(0)\n\t"
        "s_cbranch_scc1 65b\n\t"
        "v_cmp_gt_u32 vcc, s47, v126\n\t"
        "s_cbranch_vccnz 8b\n\t"
        "s_branch 65b\n\t"
        "66:\n\t"
        "s_mov_b32 s48, 0x10000\n\t"
        "8:\n\t"
        "ds_read_b32 v126, v125\n\t"
        "s_sub_u32 s48, s48, 1\n\t"
        "s_waitcnt lgkmcnt(0)\n\t"
        "s_cbranch_scc1 67b\n\t"
        "v_cmp_gt_u32 vcc, s47, v126\n\t"
        "s_cbranch_vccnz 8b\n\t"
        "s_branch 67b\n\t"
        "68:\n\t"
        "s_mov_b32 s48, 0x10000\n\t"
        "8:\n\t"
        "ds_read_b32 v126, v125\n\t"
        "s_sub_u32 s48, s48, 1\n\t"
        "s_waitcnt lgkmcnt(0)\n\t"
        "s_cbranch_scc1 69b\n\t"
        "v_cmp_gt_u32 vcc, s47, v126\n\t"
        "s_cbranch_vccnz 8b\n\t"
        "s_branch 69b\n\t"
        "70:\n\t"
        "s_mov_b32 s48, 0x10000\n\t"
        "8:\n\t"
        "ds_read_b32 v126, v125\n\t"
        "s_sub_u32 s48, s48, 1\n\t"
        "s_waitcnt lgkmcnt(0)\n\t"
        "s_cbranch_scc1 71b\n\t"
        "v_cmp_gt_u32 vcc, s47, v126\n\t"
        "s_cbranch_vccnz 8b\n\t"
        "s_branch 71b\n\t"
        "72:\n\t"
        "s_mov_b32 s48, 0x10000\n\t"
        "8:\n\t"
        "ds_read_b32 v126, v125\n\t"
        "s_sub_u32 s48, s48, 1\n\t"
        "s_waitcnt lgkmcnt(0)\n\t"
        "s_cbranch_scc1 73b\n\t"
        "v_cmp_gt_u32 vcc, s47, v126\n\t"
        "s_cbranch_vccnz 8b\n\t"
        "s_branch 73b\n\t"
        "9:\n\t"
        : "+{v[0:15]}"(acc[0]), "+{v[16:31]}"(acc[1]), "+{v[32:47]}"(acc[2]), "+{v[48:63]}"(acc[3]), "+{v[64:79]}"(acc[4]), "+{v[80:95]}"(acc[5]),
          "+{v[96:111]}"(stA), "+{v[112:127]}"(stB), "+{v[128:131]}"(stC), "+{v[148:163]}"(xr1), "+{v[164:179]}"(xr2), "+{v[180:195]}"(xr3),
          "+{s44}"(soff), "+{s46}"(slot), "+{s47}"(need)
        : "{s[40:43]}"(srd) : "v132", "v133", "v134", "v135", "v136", "v137", "v138", "v139", "v140", "v141", "v142", "v143", "v144", "v145", "v146", "v147", "s45", "s48", "vcc", "scc", "memory");
      STAMPV(4 + 16 * L);
      PHASE_VARS;
      if (L == 1) {
        f32x4 av[4], cs[4][4];
        float cv[2];
#pragma unroll
        for (int g = 0; g < 4; ++g) av[g] = *reinterpret_cast<const f32x4*>(smem + L_AB + wid * 256 + (8 * g + 4 * hi) * 4);
        SBAR();
#pragma unroll
        for (int j = 0; j < 6; ++j)
#pragma unroll
          for (int e = 0; e < 16; ++e) acc[j][e] *= j < 4 ? al : av[e >> 2][e & 3];
        SBAR();
        lds_wait_ge(lbase + L_W + 4 + 4 * (lane & 1), 1);
        const int idx = (int)__builtin_amdgcn_readfirstlane(words[3]);
        const bool mine = (idx >> 5) == tile;
        if (mine) {
#pragma unroll
          for (int j = 0; j < 6; ++j) {
            const int cb = (j >> 1) * 64 + ((j & 1) ^ hh) * 32;
            if (j >= 4) cv[j - 4] = *reinterpret_cast<const float*>(smem + L_PATCH + (cb + r32) * 4);
            else {
#pragma unroll
              for (int g = 0; g < 4; ++g) cs[j][g] = *reinterpret_cast<const f32x4*>(smem + L_PATCH + (cb + 8 * g + 4 * hi) * 4);
            }
          }
          SBAR();
          const int rel = (idx & 31) - 4 * hi;
#pragma unroll
          for (int j = 0; j < 6; ++j)
#pragma unroll
            for (int e = 0; e < 16; ++e)
              acc[j][e] = j < 4 ? (r32 == (idx & 31) ? cs[j & 3][e >> 2][e & 3] : acc[j][e]) : (rel == 8 * (e >> 2) + (e & 3) ? cv[j & 1] : acc[j][e]);
        }
      }
      constexpr float QS = 0.17677669529663687f * 1.4426950408889634f;
      u32x4 own[4];
#pragma unroll
      for (int j = 5; j >= 0; --j) {
        u32x4 o[2];
#pragma unroll
        for (int e = 0; e < 8; ++e) {
          unsigned w = pack2(acc[j][2 * e], acc[j][2 * e + 1]);
          if (j < 2) w = pack2(lo16(w) * QS, hi16(w) * QS);
          o[e >> 2][e & 3] = w;
        }
        if (j >= 4) { o[0] = fo_swap(o[0]); o[1] = fo_swap(o[1]); }
        const int hs = (j & 1) ^ hh;
        if (j >= 2) {
          const unsigned dst = OFF_KV + L * (10 * MBY) + (tn * 2 + hs) * HS + tile * 4096 + ((j >> 1) - 1) * 2048 + lane * 16;
          stg(rs, dst, o[0]); stg(rs, dst + 1024, o[1]);
        }
        if (j >= 2 && !(j & 1)) { own[j - 2] = o[0]; own[j - 1] = o[1]; }
        else {
          char* p = smem + L_XCH + (j < 2 ? (hs * 4 + wid) * 2048 : 16384 + wid * 4096 + ((j >> 1) - 1) * 2048) + lane * 16;
          *reinterpret_cast<u32x4*>(p) = o[0]; *reinterpret_cast<u32x4*>(p + 1024) = o[1];
        }
        if (j == 1) asm volatile("s_waitcnt vmcnt(0)\n\tds_write_b32 %0, %1" :: "v"(lbase + L_W + 80 + 4 * wid), "v"(L + 1) : "memory");
        SBAR();
      }
      BAR_LGKM();
      kva = cat4(own[0], own[1], own[2], own[3]);
      const u32x4* pk = reinterpret_cast<const u32x4*>(smem + L_XCH + 16384 + (wid ^ 1) * 4096 + lane * 16);
#pragma unroll
      for (int u = 0; u < 4; ++u) {
        const u32x4* p = reinterpret_cast<const u32x4*>(smem + L_XCH + (hh * 4 + ((2 * kh + 2 + u) & 3)) * 2048 + lane * 16);
        q[u] = __builtin_shufflevector(p[0], p[64], 0, 1, 2, 3, 4, 5, 6, 7);
      }
      kvb = cat4(pk[0], pk[64], pk[128], pk[192]);
      SBAR();
      asm volatile(
        "v_mov_b64 v[0:1], 0\n\t"
        "v_mov_b64 v[2:3], 0\n\t"
        "v_mov_b64 v[4:5], 0\n\t"
        "v_mov_b64 v[6:7], 0\n\t"
        "v_mov_b64 v[8:9], 0\n\t"
        "v_mov_b64 v[10:11], 0\n\t"
        "v_mov_b64 v[12:13], 0\n\t"
        "v_mov_b64 v[14:15], 0\n\t"
        "v_mov_b64 v[16:17], 0\n\t"
        "v_mov_b64 v[18:19], 0\n\t"
        "v_mov_b64 v[20:21], 0\n\t"
        "v_mov_b64 v[22:23], 0\n\t"
        "v_mov_b64 v[24:25], 0\n\t"
        "v_mov_b64 v[26:27], 0\n\t"
        "v_mov_b64 v[28:29], 0\n\t"
        "v_mov_b64 v[30:31], 0\n\t"
        "v_mov_b64 v[32:33], 0\n\t"
        "v_mov_b64 v[34:35], 0\n\t"
        "v_mov_b64 v[36:37], 0\n\t"
        "v_mov_b64 v[38:39], 0\n\t"
        "v_mov_b64 v[40:41], 0\n\t"
        "v_mov_b64 v[42:43], 0\n\t"
        "v_mov_b64 v[44:45], 0\n\t"
        "v_mov_b64 v[46:47], 0\n\t"
        "v_mov_b64 v[48:49], 0\n\t"
        "v_mov_b64 v[50:51], 0\n\t"
        "v_mov_b64 v[52:53], 0\n\t"
        "v_mov_b64 v[54:55], 0\n\t"
        "v_mov_b64 v[56:57], 0\n\t"
        "v_mov_b64 v[58:59], 0\n\t"
        "v_mov_b64 v[60:61], 0\n\t"
        "v_mov_b64 v[62:63], 0\n\t"
        "v_mov_b64 v[128:129], 0\n\t"
        "v_mov_b64 v[130:131], 0\n\t"
        "v_mov_b64 v[132:133], 0\n\t"
        "v_mov_b64 v[134:135], 0\n\t"
        "v_mov_b64 v[136:137], 0\n\t"
        "v_mov_b64 v[138:139], 0\n\t"
        "v_mov_b64 v[140:141], 0\n\t"
        "v_mov_b64 v[142:143], 0\n\t"
        : "={v[0:15]}"(oz[0]), "={v[16:31]}"(oz[1]), "={v[32:47]}"(oz[2]), "={v[48:63]}"(oz[3]), "={v[128:143]}"(oz[4]));
    }
    STAMPV(5 + 16 * L);
    {
      u32x4 ones;
      unsigned kvoff;
      u32x8 fv, cv;
      asm volatile("" : "={v[184:191]}"(fv), "={v[216:223]}"(cv));
      { PHASE_VARS;
        const unsigned v = (lane & 3) == 0 ? 0x3F803F80u : 0u; const bool up = (lane & 4) != 0; ones[0] = ones[1] = up ? 0u : v; ones[2] = ones[3] = up ? v : 0u;
        kvoff = OFF_KV + L * (10 * MBY) + (tn * 2 + hh) * HS + kh * 8192 + lane * 16;
        fv[1] = lbase + L_FREE + wid * 256 + lane * 4; fv[2] = 32 * L + 32;
        cv[0] = lbase + L_XCH + wid * 8192 + lane * 16; cv[1] = lbase + L_XCH + (wid ^ 2) * 8192 + lane * 16;
        cv[2] = lbase + L_XCH + 32768 + wid * 512 + lane * 4; cv[3] = lbase + L_XCH + 32768 + (wid ^ 2) * 512 + lane * 4;
        cv[4] = lbase + L_W + 48 + wid * 4; cv[5] = lbase + L_W + 48 + (wid ^ 2) * 4; cv[6] = L + 1;
        cv[7] = ((tile * 32 + r32) * D_ + tn * 64 + hi * 8) * 2; }
      f32x16 o0 = oz[0], o1 = oz[1], o2 = oz[2], o3 = oz[3], lv = oz[4];
      unsigned soff = tm * 16384;
      STAMP_DECL;
      asm volatile(
        "s_nop 1\n\t"
        "v_mfma_f32_32x32x16_bf16 v[96:111], v[152:155], v[64:67], 0\n\t"
        "v_mfma_f32_32x32x16_bf16 v[96:111], v[156:159], v[68:71], v[96:111]\n\t"
        "s_mov_b32 s46, 0\n\t"
        "ds_write_b32 v185, v186\n\t"
        "s_nop 9\n\t"
        "v_exp_f32 v96, v96\n\t"
        "v_exp_f32 v97, v97\n\t"
        "v_exp_f32 v98, v98\n\t"
        "v_exp_f32 v99, v99\n\t"
        "v_exp_f32 v100, v100\n\t"
        "v_exp_f32 v101, v101\n\t"
        "v_mfma_f32_32x32x16_bf16 v[112:127], v[152:155], v[72:75], 0\n\t"
        "v_exp_f32 v102, v102\n\t"
        "v_exp_f32 v103, v103\n\t"
        "v_exp_f32 v104, v104\n\t"
        "v_mfma_f32_32x32x16_bf16 v[112:127], v[156:159], v[76:79], v[112:127]\n\t"
        "v_exp_f32 v105, v105\n\t"
        "v_exp_f32 v106, v106\n\t"
        "v_exp_f32 v107, v107\n\t"
        "v_smfmac_f32_32x32x32_bf16 v[0:15], v[160:163], v[96:103], v148\n\t"
        "v_exp_f32 v108, v108\n\t"
        "v_exp_f32 v109, v109\n\t"
        "v_exp_f32 v110, v110\n\t"
        "v_smfmac_f32_16x16x64_bf16 v[128:131], v[144:147], v[96:103], v148\n\t"
        "v_exp_f32 v111, v111\n\t"
        "v_exp_f32 v112, v112\n\t"
        "v_exp_f32 v113, v113\n\t"
        "v_smfmac_f32_32x32x32_bf16 v[0:15], v[164:167], v[104:111], v148\n\t"
        "v_exp_f32 v114, v114\n\t"
        "v_exp_f32 v115, v115\n\t"
        "v_exp_f32 v116, v116\n\t"
        "v_smfmac_f32_16x16x64_bf16 v[128:131], v[144:147], v[104:111], v148\n\t"
        "v_exp_f32 v117, v117\n\t"
        "v_mfma_f32_32x32x16_bf16 v[96:111], v[152:155], v[80:83], 0\n\t"
        "v_exp_f32 v118, v118\n\t"
        "v_exp_f32 v119, v119\n\t"
        "v_exp_f32 v120, v120\n\t"
        "v_mfma_f32_32x32x16_bf16 v[96:111], v[156:159], v[84:87], v[96:111]\n\t"
        "v_exp_f32 v121, v121\n\t"
        "v_exp_f32 v122, v122\n\t"
        "v_exp_f32 v123, v123\n\t"
        "v_smfmac_f32_32x32x32_bf16 v[16:31], v[160:163], v[112:119], v148\n\t"
        "v_exp_f32 v124, v124\n\t"
        "v_exp_f32 v125, v125\n\t"
        "v_exp_f32 v126, v126\n\t"
        "v_smfmac_f32_16x16x64_bf16 v[132:135], v[144:147], v[112:119], v148\n\t"
        "v_exp_f32 v127, v127\n\t"
        "v_exp_f32 v96, v96\n\t"
        "v_exp_f32 v97, v97\n\t"
        "v_smfmac_f32_32x32x32_bf16 v[16:31], v[164:167], v[120:127], v148\n\t"
        "v_exp_f32 v98, v98\n\t"
        "v_exp_f32 v99, v99\n\t"
        "v_exp_f32 v100, v100\n\t"
        "v_smfmac_f32_16x16x64_bf16 v[132:135], v[144:147], v[120:127], v148\n\t"
        "v_exp_f32 v101, v101\n\t"
        "v_mfma_f32_32x32x16_bf16 v[112:127], v[152:155], v[88:91], 0\n\t"
        "v_exp_f32 v102, v102\n\t"
        "v_exp_f32 v103, v103\n\t"
        "v_exp_f32 v104, v104\n\t"
        "v_mfma_f32_32x32x16_bf16 v[112:127], v[156:159], v[92:95], v[112:127]\n\t"
        "v_exp_f32 v105, v105\n\t"
        "v_exp_f32 v106, v106\n\t"
        "v_exp_f32 v107, v107\n\t"
        "v_smfmac_f32_32x32x32_bf16 v[32:47], v[160:163], v[96:103], v148\n\t"
        "v_exp_f32 v108, v108\n\t"
        "v_exp_f32 v109, v109\n\t"
        "v_exp_f32 v110, v110\n\t"
        "v_smfmac_f32_16x16x64_bf16 v[136:139], v[144:147], v[96:103], v148\n\t"
        "v_exp_f32 v111, v111\n\t"
        "v_exp_f32 v112, v112\n\t"
        "v_exp_f32 v113, v113\n\t"
        "v_smfmac_f32_32x32x32_bf16 v[32:47], v[164:167], v[104:111], v148\n\t"
        "v_exp_f32 v114, v114\n\t"
        "v_exp_f32 v115, v115\n\t"
        "v_exp_f32 v116, v116\n\t"
        "v_smfmac_f32_16x16x64_bf16 v[136:139], v[144:147], v[104:111], v148\n\t"
        "v_exp_f32 v117, v117\n\t"
        "v_mfma_f32_32x32x16_bf16 v[96:111], v[168:171], v[64:67], 0\n\t"
        "v_exp_f32 v118, v118\n\t"
        "v_exp_f32 v119, v119\n\t"
        "v_exp_f32 v120, v120\n\t"
        "v_mfma_f32_32x32x16_bf16 v[96:111], v[172:175], v[68:71], v[96:111]\n\t"
        "v_exp_f32 v121, v121\n\t"
        "v_exp_f32 v122, v122\n\t"
        "v_exp_f32 v123, v123\n\t"
        "v_smfmac_f32_32x32x32_bf16 v[48:63], v[160:163], v[112:119], v148\n\t"
        "v_exp_f32 v124, v124\n\t"
        "v_exp_f32 v125, v125\n\t"
        "v_exp_f32 v126, v126\n\t"
        "v_smfmac_f32_16x16x64_bf16 v[140:143], v[144:147], v[112:119], v148\n\t"
        "v_exp_f32 v127, v127\n\t"
        "v_exp_f32 v96, v96\n\t"
        "v_exp_f32 v97, v97\n\t"
        "v_smfmac_f32_32x32x32_bf16 v[48:63], v[164:167], v[120:127], v148\n\t"
        "v_exp_f32 v98, v98\n\t"
        "v_exp_f32 v99, v99\n\t"
        "v_exp_f32 v100, v100\n\t"
        "v_smfmac_f32_16x16x64_bf16 v[140:143], v[144:147], v[120:127], v148\n\t"
        "v_exp_f32 v101, v101\n\t"
        "v_mfma_f32_32x32x16_bf16 v[112:127], v[168:171], v[72:75], 0\n\t"
        "v_exp_f32 v102, v102\n\t"
        "v_exp_f32 v103, v103\n\t"
        "v_exp_f32 v104, v104\n\t"
        "ds_read_b32 v151, v224\n\t"
        "v_mfma_f32_32x32x16_bf16 v[112:127], v[172:175], v[76:79], v[112:127]\n\t"
        "v_exp_f32 v105, v105\n\t"
        "v_exp_f32 v106, v106\n\t"
        "v_exp_f32 v107, v107\n\t"
        "v_smfmac_f32_32x32x32_bf16 v[0:15], v[176:179], v[96:103], v148\n\t"
        "v_exp_f32 v108, v108\n\t"
        "v_exp_f32 v109, v109\n\t"
        "v_exp_f32 v110, v110\n\t"
        "v_smfmac_f32_16x16x64_bf16 v[128:131], v[144:147], v[96:103], v148\n\t"
        "v_exp_f32 v111, v111\n\t"
        "s_cmp_lg_u32 s46, 0\n\t"
        "s_cbranch_scc1 31f\n\t"
        "s_waitcnt lgkmcnt(0)\n\t"
        "v_cmp_ne_u32 vcc, v222, v151\n\t"
        "s_cbranch_vccz 30f\n\t"
        "31:\n\t"
        "v_exp_f32 v112, v112\n\t"
        "v_exp_f32 v113, v113\n\t"
        "v_smfmac_f32_32x32x32_bf16 v[0:15], v[180:183], v[104:111], v148\n\t"
        "v_exp_f32 v114, v114\n\t"
        "v_exp_f32 v115, v115\n\t"
        "v_exp_f32 v116, v116\n\t"
        "v_smfmac_f32_16x16x64_bf16 v[128:131], v[144:147], v[104:111], v148\n\t"
        "v_exp_f32 v117, v117\n\t"
        "v_mfma_f32_32x32x16_bf16 v[96:111], v[168:171], v[80:83], 0\n\t"
        "v_exp_f32 v118, v118\n\t"
        "v_exp_f32 v119, v119\n\t"
        "v_exp_f32 v120, v120\n\t"
        "ds_read_b32 v151, v224\n\t"
        "v_mfma_f32_32x32x16_bf16 v[96:111], v[172:175], v[84:87], v[96:111]\n\t"
        "v_exp_f32 v121, v121\n\t"
        "v_exp_f32 v122, v122\n\t"
        "v_exp_f32 v123, v123\n\t"
        "v_smfmac_f32_32x32x32_bf16 v[16:31], v[176:179], v[112:119], v148\n\t"
        "v_exp_f32 v124, v124\n\t"
        "v_exp_f32 v125, v125\n\t"
        "v_exp_f32 v126, v126\n\t"
        "v_smfmac_f32_16x16x64_bf16 v[132:135], v[144:147], v[112:119], v148\n\t"
        "v_exp_f32 v127, v127\n\t"
        "s_cmp_lg_u32 s46, 0\n\t"
        "s_cbranch_scc1 33f\n\t"
        "s_waitcnt lgkmcnt(0)\n\t"
        "v_cmp_ne_u32 vcc, v222, v151\n\t"
        "s_cbranch_vccz 32f\n\t"
        "33:\n\t"
        "v_exp_f32 v96, v96\n\t"
        "v_exp_f32 v97, v97\n\t"
        "v_smfmac_f32_32x32x32_bf16 v[16:31], v[180:183], v[120:127], v148\n\t"
        "v_exp_f32 v98, v98\n\t"
        "v_exp_f32 v99, v99\n\t"
        "v_exp_f32 v100, v100\n\t"
        "v_smfmac_f32_16x16x64_bf16 v[132:135], v[144:147], v[120:127], v148\n\t"
        "v_exp_f32 v101, v101\n\t"
        "v_mfma_f32_32x32x16_bf16 v[112:127], v[168:171], v[88:91], 0\n\t"
        "v_exp_f32 v102, v102\n\t"
        "v_exp_f32 v103, v103\n\t"
        "v_exp_f32 v104, v104\n\t"
        "v_mfma_f32_32x32x16_bf16 v[112:127], v[172:175], v[92:95], v[112:127]\n\t"
        "v_exp_f32 v105, v105\n\t"
        "v_exp_f32 v106, v106\n\t"
        "v_exp_f32 v107, v107\n\t"
        "v_smfmac_f32_32x32x32_bf16 v[32:47], v[176:179], v[96:103], v148\n\t"
        "v_exp_f32 v108, v108\n\t"
        "v_exp_f32 v109, v109\n\t"
        "v_exp_f32 v110, v110\n\t"
        "v_smfmac_f32_16x16x64_bf16 v[136:139], v[144:147], v[96:103], v148\n\t"
        "v_exp_f32 v111, v111\n\t"
        "s_cmp_lg_u32 s46, 0\n\t"
        "s_cbranch_scc1 5f\n\t"
        "s_mov_b32 s45, 0x200000\n\t"
        "2:\n\t"
        "ds_read_b32 v151, v224\n\t"
        "s_sub_u32 s45, s45, 1\n\t"
        "s_waitcnt lgkmcnt(0)\n\t"
        "s_cbranch_scc1 3f\n\t"
        "v_cmp_ne_u32 vcc, v222, v151\n\t"
        "s_cbranch_vccnz 2b\n\t"
        "3:\n\t"
        "s_add_u32 s44, s44, 0x4000\n\t"
        "s_and_b32 s44, s44, 0x3ffff\n\t"
        "buffer_load_dwordx4 v[184:187], v149, s[40:43], s44 offen offset:0 sc1\n\t"
        "buffer_load_dwordx4 v[188:191], v149, s[40:43], s44 offen offset:1024 sc1\n\t"
        "buffer_load_dwordx4 v[192:195], v149, s[40:43], s44 offen offset:2048 sc1\n\t"
        "buffer_load_dwordx4 v[196:199], v149, s[40:43], s44 offen offset:3072 sc1\n\t"
        "buffer_load_dwordx4 v[200:203], v150, s[40:43], s44 offen offset:0 sc1\n\t"
        "buffer_load_dwordx4 v[204:207], v150, s[40:43], s44 offen offset:1024 sc1\n\t"
        "buffer_load_dwordx4 v[208:211], v150, s[40:43], s44 offen offset:2048 sc1\n\t"
        "buffer_load_dwordx4 v[212:215], v150, s[40:43], s44 offen offset:3072 sc1\n\t"
        "s_add_u32 s44, s44, 0x4000\n\t"
        "s_and_b32 s44, s44, 0x3ffff\n\t"
        "buffer_load_dwordx4 v[152:155], v149, s[40:43], s44 offen offset:0 sc1\n\t"
        "buffer_load_dwordx4 v[156:159], v149, s[40:43], s44 offen offset:1024 sc1\n\t"
        "buffer_load_dwordx4 v[160:163], v149, s[40:43], s44 offen offset:2048 sc1\n\t"
        "buffer_load_dwordx4 v[164:167], v149, s[40:43], s44 offen offset:3072 sc1\n\t"
        "5:\n\t"
        "s_mov_b32 s45, 7\n\t"
        "1:\n\t"
        "v_exp_f32 v112, v112\n\t"
        "v_exp_f32 v113, v113\n\t"
        "v_smfmac_f32_32x32x32_bf16 v[32:47], v[180:183], v[104:111], v148\n\t"
        "v_exp_f32 v114, v114\n\t"
        "v_exp_f32 v115, v115\n\t"
        "v_exp_f32 v116, v116\n\t"
        "v_smfmac_f32_16x16x64_bf16 v[136:139], v[144:147], v[104:111], v148\n\t"
        "v_exp_f32 v117, v117\n\t"
        "s_waitcnt vmcnt(8)\n\t"
        "v_mfma_f32_32x32x16_bf16 v[96:111], v[184:187], v[64:67], 0\n\t"
        "v_exp_f32 v118, v118\n\t"
        "v_exp_f32 v119, v119\n\t"
        "v_exp_f32 v120, v120\n\t"
        "v_mfma_f32_32x32x16_bf16 v[96:111], v[188:191], v[68:71], v[96:111]\n\t"
        "v_exp_f32 v121, v121\n\t"
        "v_exp_f32 v122, v122\n\t"
        "v_exp_f32 v123, v123\n\t"
        "v_smfmac_f32_32x32x32_bf16 v[48:63], v[176:179], v[112:119], v148\n\t"
        "v_exp_f32 v124, v124\n\t"
        "v_exp_f32 v125, v125\n\t"
        "v_exp_f32 v126, v126\n\t"
        "v_smfmac_f32_16x16x64_bf16 v[140:143], v[144:147], v[112:119], v148\n\t"
        "buffer_load_dwordx4 v[168:171], v150, s[40:43], s44 offen offset:0 sc1\n\t"
        "v_exp_f32 v127, v127\n\t"
        "v_exp_f32 v96, v96\n\t"
        "v_exp_f32 v97, v97\n\t"
        "v_smfmac_f32_32x32x32_bf16 v[48:63], v[180:183], v[120:127], v148\n\t"
        "v_exp_f32 v98, v98\n\t"
        "v_exp_f32 v99, v99\n\t"
        "v_exp_f32 v100, v100\n\t"
        "v_smfmac_f32_16x16x64_bf16 v[140:143], v[144:147], v[120:127], v148\n\t"
        "v_exp_f32 v101, v101\n\t"
        "v_mfma_f32_32x32x16_bf16 v[112:127], v[184:187], v[72:75], 0\n\t"
        "v_exp_f32 v102, v102\n\t"
        "v_exp_f32 v103, v103\n\t"
        "v_exp_f32 v104, v104\n\t"
        "v_mfma_f32_32x32x16_bf16 v[112:127], v[188:191], v[76:79], v[112:127]\n\t"
        "v_exp_f32 v105, v105\n\t"
        "v_exp_f32 v106, v106\n\t"
        "v_exp_f32 v107, v107\n\t"
        "v_smfmac_f32_32x32x32_bf16 v[0:15], v[192:195], v[96:103], v148\n\t"
        "v_exp_f32 v108, v108\n\t"
        "v_exp_f32 v109, v109\n\t"
        "v_exp_f32 v110, v110\n\t"
        "v_smfmac_f32_16x16x64_bf16 v[128:131], v[144:147], v[96:103], v148\n\t"
        "buffer_load_dwordx4 v[172:175], v150, s[40:43], s44 offen offset:1024 sc1\n\t"
        "v_exp_f32 v111, v111\n\t"
        "v_exp_f32 v112, v112\n\t"
        "v_exp_f32 v113, v113\n\t"
        "v_smfmac_f32_32x32x32_bf16 v[0:15], v[196:199], v[104:111], v148\n\t"
        "v_exp_f32 v114, v114\n\t"
        "v_exp_f32 v115, v115\n\t"
        "v_exp_f32 v116, v116\n\t"
        "v_smfmac_f32_16x16x64_bf16 v[128:131], v[144:147], v[104:111], v148\n\t"
        "v_exp_f32 v117, v117\n\t"
        "v_mfma_f32_32x32x16_bf16 v[96:111], v[184:187], v[80:83], 0\n\t"
        "v_exp_f32 v118, v118\n\t"
        "v_exp_f32 v119, v119\n\t"
        "v_exp_f32 v120, v120\n\t"
        "v_mfma_f32_32x32x16_bf16 v[96:111], v[188:191], v[84:87], v[96:111]\n\t"
        "v_exp_f32 v121, v121\n\t"
        "v_exp_f32 v122, v122\n\t"
        "v_exp_f32 v123, v123\n\t"
        "v_smfmac_f32_32x32x32_bf16 v[16:31], v[192:195], v[112:119], v148\n\t"
        "v_exp_f32 v124, v124\n\t"
        "v_exp_f32 v125, v125\n\t"
        "v_exp_f32 v126, v126\n\t"
        "v_smfmac_f32_16x16x64_bf16 v[132:135], v[144:147], v[112:119], v148\n\t"
        "buffer_load_dwordx4 v[176:179], v150, s[40:43], s44 offen offset:2048 sc1\n\t"
        "v_exp_f32 v127, v127\n\t"
        "v_exp_f32 v96, v96\n\t"
        "v_exp_f32 v97, v97\n\t"
        "v_smfmac_f32_32x32x32_bf16 v[16:31], v[196:199], v[120:127], v148\n\t"
        "v_exp_f32 v98, v98\n\t"
        "v_exp_f32 v99, v99\n\t"
        "v_exp_f32 v100, v100\n\t"
        "v_smfmac_f32_16x16x64_bf16 v[132:135], v[144:147], v[120:127], v148\n\t"
        "v_exp_f32 v101, v101\n\t"
        "v_mfma_f32_32x32x16_bf16 v[112:127], v[184:187], v[88:91], 0\n\t"
        "v_exp_f32 v102, v102\n\t"
        "v_exp_f32 v103, v103\n\t"
        "v_exp_f32 v104, v104\n\t"
        "v_mfma_f32_32x32x16_bf16 v[112:127], v[188:191], v[92:95], v[112:127]\n\t"
        "v_exp_f32 v105, v105\n\t"
        "v_exp_f32 v106, v106\n\t"
        "v_exp_f32 v107, v107\n\t"
        "v_smfmac_f32_32x32x32_bf16 v[32:47], v[192:195], v[96:103], v148\n\t"
        "v_exp_f32 v108, v108\n\t"
        "v_exp_f32 v109, v109\n\t"
        "v_exp_f32 v110, v110\n\t"
        "v_smfmac_f32_16x16x64_bf16 v[136:139], v[144:147], v[96:103], v148\n\t"
        "buffer_load_dwordx4 v[180:183], v150, s[40:43], s44 offen offset:3072 sc1\n\t"
        "v_exp_f32 v111, v111\n\t"
        "v_exp_f32 v112, v112\n\t"
        "v_exp_f32 v113, v113\n\t"
        "v_smfmac_f32_32x32x32_bf16 v[32:47], v[196:199], v[104:111], v148\n\t"
        "s_add_u32 s44, s44, 0x4000\n\t"
        "s_and_b32 s44, s44, 0x3ffff\n\t"
        "v_exp_f32 v114, v114\n\t"
        "v_exp_f32 v115, v115\n\t"
        "v_exp_f32 v116, v116\n\t"
        "v_smfmac_f32_16x16x64_bf16 v[136:139], v[144:147], v[104:111], v148\n\t"
        "v_exp_f32 v117, v117\n\t"
        "s_waitcnt vmcnt(8)\n\t"
        "v_mfma_f32_32x32x16_bf16 v[96:111], v[200:203], v[64:67], 0\n\t"
        "v_exp_f32 v118, v118\n\t"
        "v_exp_f32 v119, v119\n\t"
        "v_exp_f32 v120, v120\n\t"
        "v_mfma_f32_32x32x16_bf16 v[96:111], v[204:207], v[68:71], v[96:111]\n\t"
        "v_exp_f32 v121, v121\n\t"
        "v_exp_f32 v122, v122\n\t"
        "v_exp_f32 v123, v123\n\t"
        "v_smfmac_f32_32x32x32_bf16 v[48:63], v[192:195], v[112:119], v148\n\t"
        "v_exp_f32 v124, v124\n\t"
        "v_exp_f32 v125, v125\n\t"
        "v_exp_f32 v126, v126\n\t"
        "v_smfmac_f32_16x16x64_bf16 v[140:143], v[144:147], v[112:119], v148\n\t"
        "buffer_load_dwordx4 v[184:187], v149, s[40:43], s44 offen offset:0 sc1\n\t"
        "v_exp_f32 v127, v127\n\t"
        "v_exp_f32 v96, v96\n\t"
        "v_exp_f32 v97, v97\n\t"
        "v_smfmac_f32_32x32x32_bf16 v[48:63], v[196:199], v[120:127], v148\n\t"
        "v_exp_f32 v98, v98\n\t"
        "v_exp_f32 v99, v99\n\t"
        "v_exp_f32 v100, v100\n\t"
        "v_smfmac_f32_16x16x64_bf16 v[140:143], v[144:147], v[120:127], v148\n\t"
        "v_exp_f32 v101, v101\n\t"
        "v_mfma_f32_32x32x16_bf16 v[112:127], v[200:203], v[72:75], 0\n\t"
        "v_exp_f32 v102, v102\n\t"
        "v_exp_f32 v103, v103\n\t"
        "v_exp_f32 v104, v104\n\t"
        "v_mfma_f32_32x32x16_bf16 v[112:127], v[204:207], v[76:79], v[112:127]\n\t"
        "v_exp_f32 v105, v105\n\t"
        "v_exp_f32 v106, v106\n\t"
        "v_exp_f32 v107, v107\n\t"
        "v_smfmac_f32_32x32x32_bf16 v[0:15], v[208:211], v[96:103], v148\n\t"
        "v_exp_f32 v108, v108\n\t"
        "v_exp_f32 v109, v109\n\t"
        "v_exp_f32 v110, v110\n\t"
        "v_smfmac_f32_16x16x64_bf16 v[128:131], v[144:147], v[96:103], v148\n\t"
        "buffer_load_dwordx4 v[188:191], v149, s[40:43], s44 offen offset:1024 sc1\n\t"
        "v_exp_f32 v111, v111\n\t"
        "v_exp_f32 v112, v112\n\t"
        "v_exp_f32 v113, v113\n\t"
        "v_smfmac_f32_32x32x32_bf16 v[0:15], v[212:215], v[104:111], v148\n\t"
        "v_exp_f32 v114, v114\n\t"
        "v_exp_f32 v115, v115\n\t"
        "v_exp_f32 v116, v116\n\t"
        "v_smfmac_f32_16x16x64_bf16 v[128:131], v[144:147], v[104:111], v148\n\t"
        "v_exp_f32 v117, v117\n\t"
        "v_mfma_f32_32x32x16_bf16 v[96:111], v[200:203], v[80:83], 0\n\t"
        "v_exp_f32 v118, v118\n\t"
        "v_exp_f32 v119, v119\n\t"
        "v_exp_f32 v120, v120\n\t"
        "v_mfma_f32_32x32x16_bf16 v[96:111], v[204:207], v[84:87], v[96:111]\n\t"
        "v_exp_f32 v121, v121\n\t"
        "v_exp_f32 v122, v122\n\t"
        "v_exp_f32 v123, v123\n\t"
        "v_smfmac_f32_32x32x32_bf16 v[16:31], v[208:211], v[112:119], v148\n\t"
        "v_exp_f32 v124, v124\n\t"
        "v_exp_f32 v125, v125\n\t"
        "v_exp_f32 v126, v126\n\t"
        "v_smfmac_f32_16x16x64_bf16 v[132:135], v[144:147], v[112:119], v148\n\t"
        "buffer_load_dwordx4 v[192:195], v149, s[40:43], s44 offen offset:2048 sc1\n\t"
        "v_exp_f32 v127, v127\n\t"
        "v_exp_f32 v96, v96\n\t"
        "v_exp_f32 v97, v97\n\t"
        "v_smfmac_f32_32x32x32_bf16 v[16:31], v[212:215], v[120:127], v148\n\t"
        "v_exp_f32 v98, v98\n\t"
        "v_exp_f32 v99, v99\n\t"
        "v_exp_f32 v100, v100\n\t"
        "v_smfmac_f32_16x16x64_bf16 v[132:135], v[144:147], v[120:127], v148\n\t"
        "v_exp_f32 v101, v101\n\t"
        "v_mfma_f32_32x32x16_bf16 v[112:127], v[200:203], v[88:91], 0\n\t"
        "v_exp_f32 v102, v102\n\t"
        "v_exp_f32 v103, v103\n\t"
        "v_exp_f32 v104, v104\n\t"
        "v_mfma_f32_32x32x16_bf16 v[112:127], v[204:207], v[92:95], v[112:127]\n\t"
        "v_exp_f32 v105, v105\n\t"
        "v_exp_f32 v106, v106\n\t"
        "v_exp_f32 v107, v107\n\t"
        "v_smfmac_f32_32x32x32_bf16 v[32:47], v[208:211], v[96:103], v148\n\t"
        "v_exp_f32 v108, v108\n\t"
        "v_exp_f32 v109, v109\n\t"
        "v_exp_f32 v110, v110\n\t"
        "v_smfmac_f32_16x16x64_bf16 v[136:139], v[144:147], v[96:103], v148\n\t"
        "buffer_load_dwordx4 v[196:199], v149, s[40:43], s44 offen offset:3072 sc1\n\t"
        "v_exp_f32 v111, v111\n\t"
        "v_exp_f32 v112, v112\n\t"
        "v_exp_f32 v113, v113\n\t"
        "v_smfmac_f32_32x32x32_bf16 v[32:47], v[212:215], v[104:111], v148\n\t"
        "v_exp_f32 v114, v114\n\t"
        "v_exp_f32 v115, v115\n\t"
        "v_exp_f32 v116, v116\n\t"
        "v_smfmac_f32_16x16x64_bf16 v[136:139], v[144:147], v[104:111], v148\n\t"
        "v_exp_f32 v117, v117\n\t"
        "s_waitcnt vmcnt(8)\n\t"
        "v_mfma_f32_32x32x16_bf16 v[96:111], v[152:155], v[64:67], 0\n\t"
        "v_exp_f32 v118, v118\n\t"
        "v_exp_f32 v119, v119\n\t"
        "v_exp_f32 v120, v120\n\t"
        "v_mfma_f32_32x32x16_bf16 v[96:111], v[156:159], v[68:71], v[96:111]\n\t"
        "v_exp_f32 v121, v121\n\t"
        "v_exp_f32 v122, v122\n\t"
        "v_exp_f32 v123, v123\n\t"
        "v_smfmac_f32_32x32x32_bf16 v[48:63], v[208:211], v[112:119], v148\n\t"
        "v_exp_f32 v124, v124\n\t"
        "v_exp_f32 v125, v125\n\t"
        "v_exp_f32 v126, v126\n\t"
        "v_smfmac_f32_16x16x64_bf16 v[140:143], v[144:147], v[112:119], v148\n\t"
        "buffer_load_dwordx4 v[200:203], v150, s[40:43], s44 offen offset:0 sc1\n\t"
        "v_exp_f32 v127, v127\n\t"
        "v_exp_f32 v96, v96\n\t"
        "v_exp_f32 v97, v97\n\t"
        "v_smfmac_f32_32x32x32_bf16 v[48:63], v[212:215], v[120:127], v148\n\t"
        "v_exp_f32 v98, v98\n\t"
        "v_exp_f32 v99, v99\n\t"
        "v_exp_f32 v100, v100\n\t"
        "v_smfmac_f32_16x16x64_bf16 v[140:143], v[144:147], v[120:127], v148\n\t"
        "v_exp_f32 v101, v101\n\t"
        "v_mfma_f32_32x32x16_bf16 v[112:127], v[152:155], v[72:75], 0\n\t"
        "v_exp_f32 v102, v102\n\t"
        "v_exp_f32 v103, v103\n\t"
        "v_exp_f32 v104, v104\n\t"
        "v_mfma_f32_32x32x16_bf16 v[112:127], v[156:159], v[76:79], v[112:127]\n\t"
        "v_exp_f32 v105, v105\n\t"
        "v_exp_f32 v106, v106\n\t"
        "v_exp_f32 v107, v107\n\t"
        "v_smfmac_f32_32x32x32_bf16 v[0:15], v[160:163], v[96:103], v148\n\t"
        "v_exp_f32 v108, v108\n\t"
        "v_exp_f32 v109, v109\n\t"
        "v_exp_f32 v110, v110\n\t"
        "v_smfmac_f32_16x16x64_bf16 v[128:131], v[144:147], v[96:103], v148\n\t"
        "buffer_load_dwordx4 v[204:207], v150, s[40:43], s44 offen offset:1024 sc1\n\t"
        "v_exp_f32 v111, v111\n\t"
        "v_exp_f32 v112, v112\n\t"
        "v_exp_f32 v113, v113\n\t"
        "v_smfmac_f32_32x32x32_bf16 v[0:15], v[164:167], v[104:111], v148\n\t"
        "v_exp_f32 v114, v114\n\t"
        "v_exp_f32 v115, v115\n\t"
        "v_exp_f32 v116, v116\n\t"
        "v_smfmac_f32_16x16x64_bf16 v[128:131], v[144:147], v[104:111], v148\n\t"
        "v_exp_f32 v117, v117\n\t"
        "v_mfma_f32_32x32x16_bf16 v[96:111], v[152:155], v[80:83], 0\n\t"
        "v_exp_f32 v118, v118\n\t"
        "v_exp_f32 v119, v119\n\t"
        "v_exp_f32 v120, v120\n\t"
        "v_mfma_f32_32x32x16_bf16 v[96:111], v[156:159], v[84:87], v[96:111]\n\t"
        "v_exp_f32 v121, v121\n\t"
        "v_exp_f32 v122, v122\n\t"
        "v_exp_f32 v123, v123\n\t"
        "v_smfmac_f32_32x32x32_bf16 v[16:31], v[160:163], v[112:119], v148\n\t"
        "v_exp_f32 v124, v124\n\t"
        "v_exp_f32 v125, v125\n\t"
        "v_exp_f32 v126, v126\n\t"
        "v_smfmac_f32_16x16x64_bf16 v[132:135], v[144:147], v[112:119], v148\n\t"
        "buffer_load_dwordx4 v[208:211], v150, s[40:43], s44 offen offset:2048 sc1\n\t"
        "v_exp_f32 v127, v127\n\t"
        "v_exp_f32 v96, v96\n\t"
        "v_exp_f32 v97, v97\n\t"
        "v_smfmac_f32_32x32x32_bf16 v[16:31], v[164:167], v[120:127], v148\n\t"
        "v_exp_f32 v98, v98\n\t"
        "v_exp_f32 v99, v99\n\t"
        "v_exp_f32 v100, v100\n\t"
        "v_smfmac_f32_16x16x64_bf16 v[132:135], v[144:147], v[120:127], v148\n\t"
        "v_exp_f32 v101, v101\n\t"
        "v_mfma_f32_32x32x16_bf16 v[112:127], v[152:155], v[88:91], 0\n\t"
        "v_exp_f32 v102, v102\n\t"
        "v_exp_f32 v103, v103\n\t"
        "v_exp_f32 v104, v104\n\t"
        "v_mfma_f32_32x32x16_bf16 v[112:127], v[156:159], v[92:95], v[112:127]\n\t"
        "v_exp_f32 v105, v105\n\t"
        "v_exp_f32 v106, v106\n\t"
        "v_exp_f32 v107, v107\n\t"
        "v_smfmac_f32_32x32x32_bf16 v[32:47], v[160:163], v[96:103], v148\n\t"
        "v_exp_f32 v108, v108\n\t"
        "v_exp_f32 v109, v109\n\t"
        "v_exp_f32 v110, v110\n\t"
        "v_smfmac_f32_16x16x64_bf16 v[136:139], v[144:147], v[96:103], v148\n\t"
        "buffer_load_dwordx4 v[212:215], v150, s[40:43], s44 offen offset:3072 sc1\n\t"
        "v_exp_f32 v111, v111\n\t"
        "v_exp_f32 v112, v112\n\t"
        "v_exp_f32 v113, v113\n\t"
        "v_smfmac_f32_32x32x32_bf16 v[32:47], v[164:167], v[104:111], v148\n\t"
        "s_add_u32 s44, s44, 0x4000\n\t"
        "s_and_b32 s44, s44, 0x3ffff\n\t"
        "v_exp_f32 v114, v114\n\t"
        "v_exp_f32 v115, v115\n\t"
        "v_exp_f32 v116, v116\n\t"
        "v_smfmac_f32_16x16x64_bf16 v[136:139], v[144:147], v[104:111], v148\n\t"
        "v_exp_f32 v117, v117\n\t"
        "s_waitcnt vmcnt(8)\n\t"
        "v_mfma_f32_32x32x16_bf16 v[96:111], v[168:171], v[64:67], 0\n\t"
        "v_exp_f32 v118, v118\n\t"
        "v_exp_f32 v119, v119\n\t"
        "v_exp_f32 v120, v120\n\t"
        "v_mfma_f32_32x32x16_bf16 v[96:111], v[172:175], v[68:71], v[96:111]\n\t"
        "v_exp_f32 v121, v121\n\t"
        "v_exp_f32 v122, v122\n\t"
        "v_exp_f32 v123, v123\n\t"
        "v_smfmac_f32_32x32x32_bf16 v[48:63], v[160:163], v[112:119], v148\n\t"
        "v_exp_f32 v124, v124\n\t"
        "v_exp_f32 v125, v125\n\t"
        "v_exp_f32 v126, v126\n\t"
        "v_smfmac_f32_16x16x64_bf16 v[140:143], v[144:147], v[112:119], v148\n\t"
        "buffer_load_dwordx4 v[152:155], v149, s[40:43], s44 offen offset:0 sc1\n\t"
        "v_exp_f32 v127, v127\n\t"
        "v_exp_f32 v96, v96\n\t"
        "v_exp_f32 v97, v97\n\t"
        "v_smfmac_f32_32x32x32_bf16 v[48:63], v[164:167], v[120:127], v148\n\t"
        "v_exp_f32 v98, v98\n\t"
        "v_exp_f32 v99, v99\n\t"
        "v_exp_f32 v100, v100\n\t"
        "v_smfmac_f32_16x16x64_bf16 v[140:143], v[144:147], v[120:127], v148\n\t"
        "v_exp_f32 v101, v101\n\t"
        "v_mfma_f32_32x32x16_bf16 v[112:127], v[168:171], v[72:75], 0\n\t"
        "v_exp_f32 v102, v102\n\t"
        "v_exp_f32 v103, v103\n\t"
        "v_exp_f32 v104, v104\n\t"
        "v_mfma_f32_32x32x16_bf16 v[112:127], v[172:175], v[76:79], v[112:127]\n\t"
        "v_exp_f32 v105, v105\n\t"
        "v_exp_f32 v106, v106\n\t"
        "v_exp_f32 v107, v107\n\t"
        "v_smfmac_f32_32x32x32_bf16 v[0:15], v[176:179], v[96:103], v148\n\t"
        "v_exp_f32 v108, v108\n\t"
        "v_exp_f32 v109, v109\n\t"
        "v_exp_f32 v110, v110\n\t"
        "v_smfmac_f32_16x16x64_bf16 v[128:131], v[144:147], v[96:103], v148\n\t"
        "buffer_load_dwordx4 v[156:159], v149, s[40:43], s44 offen offset:1024 sc1\n\t"
        "v_exp_f32 v111, v111\n\t"
        "v_exp_f32 v112, v112\n\t"
        "v_exp_f32 v113, v113\n\t"
        "v_smfmac_f32_32x32x32_bf16 v[0:15], v[180:183], v[104:111], v148\n\t"
        "v_exp_f32 v114, v114\n\t"
        "v_exp_f32 v115, v115\n\t"
        "v_exp_f32 v116, v116\n\t"
        "v_smfmac_f32_16x16x64_bf16 v[128:131], v[144:147], v[104:111], v148\n\t"
        "v_exp_f32 v117, v117\n\t"
        "v_mfma_f32_32x32x16_bf16 v[96:111], v[168:171], v[80:83], 0\n\t"
        "v_exp_f32 v118, v118\n\t"
        "v_exp_f32 v119, v119\n\t"
        "v_exp_f32 v120, v120\n\t"
        "v_mfma_f32_32x32x16_bf16 v[96:111], v[172:175], v[84:87], v[96:111]\n\t"
        "v_exp_f32 v121, v121\n\t"
        "v_exp_f32 v122, v122\n\t"
        "v_exp_f32 v123, v123\n\t"
        "v_smfmac_f32_32x32x32_bf16 v[16:31], v[176:179], v[112:119], v148\n\t"
        "v_exp_f32 v124, v124\n\t"
        "v_exp_f32 v125, v125\n\t"
        "v_exp_f32 v126, v126\n\t"
        "v_smfmac_f32_16x16x64_bf16 v[132:135], v[144:147], v[112:119], v148\n\t"
        "buffer_load_dwordx4 v[160:163], v149, s[40:43], s44 offen offset:2048 sc1\n\t"
        "v_exp_f32 v127, v127\n\t"
        "v_exp_f32 v96, v96\n\t"
        "v_exp_f32 v97, v97\n\t"
        "v_smfmac_f32_32x32x32_bf16 v[16:31], v[180:183], v[120:127], v148\n\t"
        "v_exp_f32 v98, v98\n\t"
        "v_exp_f32 v99, v99\n\t"
        "v_exp_f32 v100, v100\n\t"
        "v_smfmac_f32_16x16x64_bf16 v[132:135], v[144:147], v[120:127], v148\n\t"
        "v_exp_f32 v101, v101\n\t"
        "v_mfma_f32_32x32x16_bf16 v[112:127], v[168:171], v[88:91], 0\n\t"
        "v_exp_f32 v102, v102\n\t"
        "v_exp_f32 v103, v103\n\t"
        "v_exp_f32 v104, v104\n\t"
        "v_mfma_f32_32x32x16_bf16 v[112:127], v[172:175], v[92:95], v[112:127]\n\t"
        "v_exp_f32 v105, v105\n\t"
        "v_exp_f32 v106, v106\n\t"
        "v_exp_f32 v107, v107\n\t"
        "v_smfmac_f32_32x32x32_bf16 v[32:47], v[176:179], v[96:103], v148\n\t"
        "v_exp_f32 v108, v108\n\t"
        "v_exp_f32 v109, v109\n\t"
        "v_exp_f32 v110, v110\n\t"
        "v_smfmac_f32_16x16x64_bf16 v[136:139], v[144:147], v[96:103], v148\n\t"
        "buffer_load_dwordx4 v[164:167], v149, s[40:43], s44 offen offset:3072 sc1\n\t"
        "v_exp_f32 v111, v111\n\t"
        "s_sub_u32 s45, s45, 1\n\t"
        "s_cmp_lg_u32 s45, 0\n\t"
        "s_cbranch_scc1 1b\n\t"
        "v_exp_f32 v112, v112\n\t"
        "v_exp_f32 v113, v113\n\t"
        "v_smfmac_f32_32x32x32_bf16 v[32:47], v[180:183], v[104:111], v148\n\t"
        "v_exp_f32 v114, v114\n\t"
        "v_exp_f32 v115, v115\n\t"
        "v_exp_f32 v116, v116\n\t"
        "v_smfmac_f32_16x16x64_bf16 v[136:139], v[144:147], v[104:111], v148\n\t"
        "v_exp_f32 v117, v117\n\t"
        "s_waitcnt vmcnt(8)\n\t"
        "v_mfma_f32_32x32x16_bf16 v[96:111], v[184:187], v[64:67], 0\n\t"
        "v_exp_f32 v118, v118\n\t"
        "v_exp_f32 v119, v119\n\t"
        "v_exp_f32 v120, v120\n\t"
        "v_mfma_f32_32x32x16_bf16 v[96:111], v[188:191], v[68:71], v[96:111]\n\t"
        "v_exp_f32 v121, v121\n\t"
        "v_exp_f32 v122, v122\n\t"
        "v_exp_f32 v123, v123\n\t"
        "v_smfmac_f32_32x32x32_bf16 v[48:63], v[176:179], v[112:119], v148\n\t"
        "v_exp_f32 v124, v124\n\t"
        "v_exp_f32 v125, v125\n\t"
        "v_exp_f32 v126, v126\n\t"
        "v_smfmac_f32_16x16x64_bf16 v[140:143], v[144:147], v[112:119], v148\n\t"
        "v_exp_f32 v127, v127\n\t"
        "v_exp_f32 v96, v96\n\t"
        "v_exp_f32 v97, v97\n\t"
        "v_smfmac_f32_32x32x32_bf16 v[48:63], v[180:183], v[120:127], v148\n\t"
        "v_exp_f32 v98, v98\n\t"
        "v_exp_f32 v99, v99\n\t"
        "v_exp_f32 v100, v100\n\t"
        "v_smfmac_f32_16x16x64_bf16 v[140:143], v[144:147], v[120:127], v148\n\t"
        "v_exp_f32 v101, v101\n\t"
        "v_mfma_f32_32x32x16_bf16 v[112:127], v[184:187], v[72:75], 0\n\t"
        "v_exp_f32 v102, v102\n\t"
        "v_exp_f32 v103, v103\n\t"
        "v_exp_f32 v104, v104\n\t"
        "v_mfma_f32_32x32x16_bf16 v[112:127], v[188:191], v[76:79], v[112:127]\n\t"
        "v_exp_f32 v105, v105\n\t"
        "v_exp_f32 v106, v106\n\t"
        "v_exp_f32 v107, v107\n\t"
        "v_smfmac_f32_32x32x32_bf16 v[0:15], v[192:195], v[96:103], v148\n\t"
        "v_exp_f32 v108, v108\n\t"
        "v_exp_f32 v109, v109\n\t"
        "v_exp_f32 v110, v110\n\t"
        "v_smfmac_f32_16x16x64_bf16 v[128:131], v[144:147], v[96:103], v148\n\t"
        "v_exp_f32 v111, v111\n\t"
        "v_exp_f32 v112, v112\n\t"
        "v_exp_f32 v113, v113\n\t"
        "v_smfmac_f32_32x32x32_bf16 v[0:15], v[196:199], v[104:111], v148\n\t"
        "v_exp_f32 v114, v114\n\t"
        "v_exp_f32 v115, v115\n\t"
        "v_exp_f32 v116, v116\n\t"
        "v_smfmac_f32_16x16x64_bf16 v[128:131], v[144:147], v[104:111], v148\n\t"
        "v_exp_f32 v117, v117\n\t"
        "s_waitcnt vmcnt(4)\n\t"
        "v_mfma_f32_32x32x16_bf16 v[96:111], v[200:203], v[64:67], 0\n\t"
        "v_exp_f32 v118, v118\n\t"
        "v_exp_f32 v119, v119\n\t"
        "v_exp_f32 v120, v120\n\t"
        "v_mfma_f32_32x32x16_bf16 v[96:111], v[204:207], v[68:71], v[96:111]\n\t"
        "v_exp_f32 v121, v121\n\t"
        "v_exp_f32 v122, v122\n\t"
        "v_exp_f32 v123, v123\n\t"
        "v_smfmac_f32_32x32x32_bf16 v[16:31], v[192:195], v[112:119], v148\n\t"
        "v_exp_f32 v124, v124\n\t"
        "v_exp_f32 v125, v125\n\t"
        "v_exp_f32 v126, v126\n\t"
        "v_smfmac_f32_16x16x64_bf16 v[132:135], v[144:147], v[112:119], v148\n\t"
        "v_exp_f32 v127, v127\n\t"
        "s_cmp_lg_u32 s47, 0\n\t"
        "s_cbranch_scc1 4f\n\t"
        "global_load_dwordx4 %[r0], v223, %[xb] offset:0\n\t"
        "global_load_dwordx4 %[r1], v223, %[xb] offset:32\n\t"
        "global_load_dwordx4 %[r2], v223, %[xb] offset:64\n\t"
        "global_load_dwordx4 %[r3], v223, %[xb] offset:96\n\t"
        "4:\n\t"
        "v_exp_f32 v96, v96\n\t"
        "v_exp_f32 v97, v97\n\t"
        "v_smfmac_f32_32x32x32_bf16 v[16:31], v[196:199], v[120:127], v148\n\t"
        "v_exp_f32 v98, v98\n\t"
        "v_exp_f32 v99, v99\n\t"
        "v_exp_f32 v100, v100\n\t"
        "v_smfmac_f32_16x16x64_bf16 v[132:135], v[144:147], v[120:127], v148\n\t"
        "v_exp_f32 v101, v101\n\t"
        "v_mfma_f32_32x32x16_bf16 v[112:127], v[200:203], v[72:75], 0\n\t"
        "v_exp_f32 v102, v102\n\t"
        "v_exp_f32 v103, v103\n\t"
        "v_exp_f32 v104, v104\n\t"
        "v_mfma_f32_32x32x16_bf16 v[112:127], v[204:207], v[76:79], v[112:127]\n\t"
        "v_exp_f32 v105, v105\n\t"
        "v_exp_f32 v106, v106\n\t"
        "v_exp_f32 v107, v107\n\t"
        "v_smfmac_f32_32x32x32_bf16 v[0:15], v[208:211], v[96:103], v148\n\t"
        "v_exp_f32 v108, v108\n\t"
        "v_exp_f32 v109, v109\n\t"
        "v_exp_f32 v110, v110\n\t"
        "v_smfmac_f32_16x16x64_bf16 v[128:131], v[144:147], v[96:103], v148\n\t"
        "v_exp_f32 v111, v111\n\t"
        "v_exp_f32 v112, v112\n\t"
        "v_exp_f32 v113, v113\n\t"
        "v_smfmac_f32_32x32x32_bf16 v[0:15], v[212:215], v[104:111], v148\n\t"
        "v_exp_f32 v114, v114\n\t"
        "v_exp_f32 v115, v115\n\t"
        "v_exp_f32 v116, v116\n\t"
        "v_smfmac_f32_16x16x64_bf16 v[128:131], v[144:147], v[104:111], v148\n\t"
        "v_exp_f32 v117, v117\n\t"
        "v_mfma_f32_32x32x16_bf16 v[96:111], v[184:187], v[80:83], 0\n\t"
        "v_exp_f32 v118, v118\n\t"
        "v_exp_f32 v119, v119\n\t"
        "v_exp_f32 v120, v120\n\t"
        "ds_write_b128 v216, v[0:3] offset:0\n\t"
        "v_mfma_f32_32x32x16_bf16 v[96:111], v[188:191], v[84:87], v[96:111]\n\t"
        "v_exp_f32 v121, v121\n\t"
        "ds_write_b128 v216, v[4:7] offset:1024\n\t"
        "v_exp_f32 v122, v122\n\t"
        "ds_write_b128 v216, v[8:11] offset:2048\n\t"
        "v_exp_f32 v123, v123\n\t"
        "ds_write_b128 v216, v[12:15] offset:3072\n\t"
        "v_smfmac_f32_32x32x32_bf16 v[16:31], v[208:211], v[112:119], v148\n\t"
        "v_exp_f32 v124, v124\n\t"
        "ds_write_b32 v218, v128 offset:0\n\t"
        "v_exp_f32 v125, v125\n\t"
        "v_exp_f32 v126, v126\n\t"
        "v_smfmac_f32_16x16x64_bf16 v[132:135], v[144:147], v[112:119], v148\n\t"
        "v_exp_f32 v127, v127\n\t"
        "v_exp_f32 v96, v96\n\t"
        "v_exp_f32 v97, v97\n\t"
        "v_smfmac_f32_32x32x32_bf16 v[16:31], v[212:215], v[120:127], v148\n\t"
        "v_exp_f32 v98, v98\n\t"
        "v_exp_f32 v99, v99\n\t"
        "v_exp_f32 v100, v100\n\t"
        "v_smfmac_f32_16x16x64_bf16 v[132:135], v[144:147], v[120:127], v148\n\t"
        "v_exp_f32 v101, v101\n\t"
        "v_mfma_f32_32x32x16_bf16 v[112:127], v[184:187], v[88:91], 0\n\t"
        "v_exp_f32 v102, v102\n\t"
        "v_exp_f32 v103, v103\n\t"
        "v_exp_f32 v104, v104\n\t"
        "ds_write_b128 v216, v[16:19] offset:4096\n\t"
        "v_mfma_f32_32x32x16_bf16 v[112:127], v[188:191], v[92:95], v[112:127]\n\t"
        "v_exp_f32 v105, v105\n\t"
        "ds_write_b128 v216, v[20:23] offset:5120\n\t"
        "v_exp_f32 v106, v106\n\t"
        "ds_write_b128 v216, v[24:27] offset:6144\n\t"
        "v_exp_f32 v107, v107\n\t"
        "ds_write_b128 v216, v[28:31] offset:7168\n\t"
        "v_smfmac_f32_32x32x32_bf16 v[32:47], v[192:195], v[96:103], v148\n\t"
        "v_exp_f32 v108, v108\n\t"
        "ds_write_b32 v218, v132 offset:256\n\t"
        "v_exp_f32 v109, v109\n\t"
        "ds_write_b32 v220, v222\n\t"
        "v_exp_f32 v110, v110\n\t"
        "v_smfmac_f32_16x16x64_bf16 v[136:139], v[144:147], v[96:103], v148\n\t"
        "v_exp_f32 v111, v111\n\t"
        "v_exp_f32 v112, v112\n\t"
        "v_exp_f32 v113, v113\n\t"
        "v_smfmac_f32_32x32x32_bf16 v[32:47], v[196:199], v[104:111], v148\n\t"
        "v_exp_f32 v114, v114\n\t"
        "v_exp_f32 v115, v115\n\t"
        "v_exp_f32 v116, v116\n\t"
        "v_smfmac_f32_16x16x64_bf16 v[136:139], v[144:147], v[104:111], v148\n\t"
        "v_exp_f32 v117, v117\n\t"
        "v_mfma_f32_32x32x16_bf16 v[96:111], v[200:203], v[80:83], 0\n\t"
        "v_exp_f32 v118, v118\n\t"
        "v_exp_f32 v119, v119\n\t"
        "v_exp_f32 v120, v120\n\t"
        "v_mfma_f32_32x32x16_bf16 v[96:111], v[204:207], v[84:87], v[96:111]\n\t"
        "v_exp_f32 v121, v121\n\t"
        "v_exp_f32 v122, v122\n\t"
        "v_exp_f32 v123, v123\n\t"
        "v_smfmac_f32_32x32x32_bf16 v[48:63], v[192:195], v[112:119], v148\n\t"
        "v_exp_f32 v124, v124\n\t"
        "v_exp_f32 v125, v125\n\t"
        "v_exp_f32 v126, v126\n\t"
        "v_smfmac_f32_16x16x64_bf16 v[140:143], v[144:147], v[112:119], v148\n\t"
        "v_exp_f32 v127, v127\n\t"
        "v_exp_f32 v96, v96\n\t"
        "v_exp_f32 v97, v97\n\t"
        "v_smfmac_f32_32x32x32_bf16 v[48:63], v[196:199], v[120:127], v148\n\t"
        "v_exp_f32 v98, v98\n\t"
        "v_exp_f32 v99, v99\n\t"
        "v_exp_f32 v100, v100\n\t"
        "v_smfmac_f32_16x16x64_bf16 v[140:143], v[144:147], v[120:127], v148\n\t"
        "v_exp_f32 v101, v101\n\t"
        "v_mfma_f32_32x32x16_bf16 v[112:127], v[200:203], v[88:91], 0\n\t"
        "v_exp_f32 v102, v102\n\t"
        "v_exp_f32 v103, v103\n\t"
        "v_exp_f32 v104, v104\n\t"
        "ds_read_b32 v151, v221\n\t"
        "v_mfma_f32_32x32x16_bf16 v[112:127], v[204:207], v[92:95], v[112:127]\n\t"
        "v_exp_f32 v105, v105\n\t"
        "v_exp_f32 v106, v106\n\t"
        "v_exp_f32 v107, v107\n\t"
        "v_smfmac_f32_32x32x32_bf16 v[32:47], v[208:211], v[96:103], v148\n\t"
        "v_exp_f32 v108, v108\n\t"
        "v_exp_f32 v109, v109\n\t"
        "v_exp_f32 v110, v110\n\t"
        "v_smfmac_f32_16x16x64_bf16 v[136:139], v[144:147], v[96:103], v148\n\t"
        "v_exp_f32 v111, v111\n\t"
        "s_waitcnt lgkmcnt(0)\n\t"
        "v_cmp_ne_u32 vcc, v222, v151\n\t"
        "s_cbranch_vccnz 40f\n\t"
        "41:\n\t"
        "v_exp_f32 v112, v112\n\t"
        "ds_read_b128 v[0:3], v217 offset:0\n\t"
        "v_exp_f32 v113, v113\n\t"
        "ds_read_b128 v[4:7], v217 offset:1024\n\t"
        "v_smfmac_f32_32x32x32_bf16 v[32:47], v[212:215], v[104:111], v148\n\t"
        "v_exp_f32 v114, v114\n\t"
        "ds_read_b128 v[8:11], v217 offset:2048\n\t"
        "v_exp_f32 v115, v115\n\t"
        "ds_read_b128 v[12:15], v217 offset:3072\n\t"
        "v_exp_f32 v116, v116\n\t"
        "ds_read_b32 v128, v219 offset:0\n\t"
        "v_smfmac_f32_16x16x64_bf16 v[136:139], v[144:147], v[104:111], v148\n\t"
        "v_exp_f32 v117, v117\n\t"
        "ds_read_b128 v[16:19], v217 offset:4096\n\t"
        "v_exp_f32 v118, v118\n\t"
        "ds_read_b128 v[20:23], v217 offset:5120\n\t"
        "v_exp_f32 v119, v119\n\t"
        "ds_read_b128 v[24:27], v217 offset:6144\n\t"
        "v_exp_f32 v120, v120\n\t"
        "ds_read_b128 v[28:31], v217 offset:7168\n\t"
        "v_exp_f32 v121, v121\n\t"
        "ds_read_b32 v132, v219 offset:256\n\t"
        "v_exp_f32 v122, v122\n\t"
        "v_exp_f32 v123, v123\n\t"
        "v_smfmac_f32_32x32x32_bf16 v[48:63], v[208:211], v[112:119], v148\n\t"
        "v_exp_f32 v124, v124\n\t"
        "v_exp_f32 v125, v125\n\t"
        "v_exp_f32 v126, v126\n\t"
        "v_smfmac_f32_16x16x64_bf16 v[140:143], v[144:147], v[112:119], v148\n\t"
        "v_exp_f32 v127, v127\n\t"
        "s_nop 1\n\t"
        "v_smfmac_f32_32x32x32_bf16 v[48:63], v[212:215], v[120:127], v148\n\t"
        "v_smfmac_f32_16x16x64_bf16 v[140:143], v[144:147], v[120:127], v148\n\t"
        "s_waitcnt vmcnt(0) lgkmcnt(0)\n\t"
        "s_nop 6\n\t"
        "s_branch 9f\n\t"
        "30:\n\t"
        "s_add_u32 s44, s44, 0x4000\n\t"
        "s_and_b32 s44, s44, 0x3ffff\n\t"
        "buffer_load_dwordx4 v[184:187], v149, s[40:43], s44 offen offset:0 sc1\n\t"
        "buffer_load_dwordx4 v[188:191], v149, s[40:43], s44 offen offset:1024 sc1\n\t"
        "buffer_load_dwordx4 v[192:195], v149, s[40:43], s44 offen offset:2048 sc1\n\t"
        "buffer_load_dwordx4 v[196:199], v149, s[40:43], s44 offen offset:3072 sc1\n\t"
        "buffer_load_dwordx4 v[200:203], v150, s[40:43], s44 offen offset:0 sc1\n\t"
        "buffer_load_dwordx4 v[204:207], v150, s[40:43], s44 offen offset:1024 sc1\n\t"
        "buffer_load_dwordx4 v[208:211], v150, s[40:43], s44 offen offset:2048 sc1\n\t"
        "buffer_load_dwordx4 v[212:215], v150, s[40:43], s44 offen offset:3072 sc1\n\t"
        "s_add_u32 s44, s44, 0x4000\n\t"
        "s_and_b32 s44, s44, 0x3ffff\n\t"
        "buffer_load_dwordx4 v[152:155], v149, s[40:43], s44 offen offset:0 sc1\n\t"
        "buffer_load_dwordx4 v[156:159], v149, s[40:43], s44 offen offset:1024 sc1\n\t"
        "buffer_load_dwordx4 v[160:163], v149, s[40:43], s44 offen offset:2048 sc1\n\t"
        "buffer_load_dwordx4 v[164:167], v149, s[40:43], s44 offen offset:3072 sc1\n\t"
        "s_mov_b32 s46, 1\n\t"
        "s_branch 31b\n\t"
        "32:\n\t"
        "s_add_u32 s44, s44, 0x4000\n\t"
        "s_and_b32 s44, s44, 0x3ffff\n\t"
        "buffer_load_dwordx4 v[184:187], v149, s[40:43], s44 offen offset:0 sc1\n\t"
        "buffer_load_dwordx4 v[188:191], v149, s[40:43], s44 offen offset:1024 sc1\n\t"
        "buffer_load_dwordx4 v[192:195], v149, s[40:43], s44 offen offset:2048 sc1\n\t"
        "buffer_load_dwordx4 v[196:199], v149, s[40:43], s44 offen offset:3072 sc1\n\t"
        "buffer_load_dwordx4 v[200:203], v150, s[40:43], s44 offen offset:0 sc1\n\t"
        "buffer_load_dwordx4 v[204:207], v150, s[40:43], s44 offen offset:1024 sc1\n\t"
        "buffer_load_dwordx4 v[208:211], v150, s[40:43], s44 offen offset:2048 sc1\n\t"
        "buffer_load_dwordx4 v[212:215], v150, s[40:43], s44 offen offset:3072 sc1\n\t"
        "s_add_u32 s44, s44, 0x4000\n\t"
        "s_and_b32 s44, s44, 0x3ffff\n\t"
        "buffer_load_dwordx4 v[152:155], v149, s[40:43], s44 offen offset:0 sc1\n\t"
        "buffer_load_dwordx4 v[156:159], v149, s[40:43], s44 offen offset:1024 sc1\n\t"
        "buffer_load_dwordx4 v[160:163], v149, s[40:43], s44 offen offset:2048 sc1\n\t"
        "buffer_load_dwordx4 v[164:167], v149, s[40:43], s44 offen offset:3072 sc1\n\t"
        "s_mov_b32 s46, 1\n\t"
        "s_branch 33b\n\t"
        "40:\n\t"
        "s_mov_b32 s45, 0x100000\n\t"
        "42:\n\t"
        "ds_read_b32 v151, v221\n\t"
        "s_sub_u32 s45, s45, 1\n\t"
        "s_waitcnt lgkmcnt(0)\n\t"
        "s_cbranch_scc1 41b\n\t"
        "v_cmp_ne_u32 vcc, v222, v151\n\t"
        "s_cbranch_vccnz 42b\n\t"
        "s_branch 41b\n\t"
        "9:\n\t"
        : "+{v[0:15]}"(o0), "+{v[16:31]}"(o1), "+{v[32:47]}"(o2), "+{v[48:63]}"(o3), "+{v[128:143]}"(lv), "+{s44}"(soff), "+{v[152:167]}"(kva), "+{v[168:183]}"(kvb), "+{v[184:191]}"(fv),
          [r0] "+v"(x1p[0]), [r1] "+v"(x1p[1]), [r2] "+v"(x1p[2]), [r3] "+v"(x1p[3]) STAMP_OUTS
        : "{v[64:71]}"(q[0]), "{v[72:79]}"(q[1]), "{v[80:87]}"(q[2]), "{v[88:95]}"(q[3]), "{v[144:147]}"(ones), "{v148}"(0xddddu), "{v149}"(kvoff), "{v150}"(kvoff + 4096),
          "{v[216:223]}"(cv), "{v224}"(lbase + L_W + 16), "{s[40:43]}"(srd), "{s47}"(L), [xb] "s"(x0)
        : "v96", "v97", "v98", "v99", "v100", "v101", "v102", "v103", "v104", "v105", "v106", "v107", "v108", "v109", "v110", "v111", "v112", "v113", "v114", "v115", "v116", "v117", "v118", "v119", "v120", "v121", "v122", "v123", "v124", "v125", "v126", "v127", "v151", "v192", "v193", "v194", "v195", "v196", "v197", "v198", "v199", "v200", "v201", "v202", "v203", "v204", "v205", "v206", "v207", "v208", "v209", "v210", "v211", "v212", "v213", "v214", "v215", "s45", "s46", "vcc", "scc", "memory");
      STAMP_PUT(6 + 16 * L);
      STAMPV(9 + 16 * L);
      PHASE_VARS;
      const unsigned dst = OFF_OF + L * (5 * MBY) + (tm * 4 + 2 * kh) * TS + (tn * 2 + hh) * 2048 + lane * 16;
      auto put = [&](int u, const f32x16& o) {
#pragma unroll
        for (int c = 0; c < 2; ++c) {
          u32x4 w;
#pragma unroll
          for (int e = 0; e < 4; ++e) w[e] = pack2(o[8 * c + 2 * e], o[8 * c + 2 * e + 1]);
          stg(rs, dst + u * TS + c * 1024, w);
        }
      };
      const float la = lv[8] + lv[0], lb = lv[12] + lv[4];
      o2 = (o2 + o0) * RCP(la); put(0, o2);
      SBAR();
      o3 = (o3 + o1) * RCP(lb); put(1, o3);
      SBAR();
      {
        const f32x16 t = o2 + o3;
        float sum = 0.f;
#pragma unroll
        for (int e = 0; e < 16; e += 4) sum += (t[e] + t[e + 1]) + (t[e + 2] + t[e + 3]);
        if (__any(!(fabsf(sum) < 1e37f) | !(fmaxf(la, lb) < 1e27f) | !(fminf(la, lb) > 1e-9f) | !(la + lb == la + lb))) {
          DRAIN();
          attn_safe(rs, kvoff - kh * 8192, q[2], ones, o2); put(0, o2);
          attn_safe(rs, kvoff - kh * 8192, q[3], ones, o3); put(1, o3);
        }
      }
    }
    STAMPV(10 + 16 * L);
    {
      f32x16 acc[2];
      u32x4 res[4];
      u32x2 wrc[8];
      STAMP_DECL;
      { PHASE_VARS;
      BAR_ALL();
      STAMPV(11 + 16 * L);
#pragma unroll
      for (int g = 0; g < 8; ++g) wrc[g] = *reinterpret_cast<const u32x2*>(smem + L_WR + g * 16 + hi * 8);
      if (L == 0) {
#pragma unroll
        for (int i = 0; i < 4; ++i) res[i] = fo_swap(x1p[i]);
      } else {
        const bool routed = (int)words[3] == tile * 32 + r32;
        const float mb1 = reinterpret_cast<const float*>(smem + L_AB)[256 + wid * 64 + r32], rd1 = reinterpret_cast<const float*>(smem + L_AB)[256 + wid * 64 + 32 + r32];
#pragma unroll
        for (int i = 0; i < 4; ++i) {
          const u32x4 pr = *reinterpret_cast<const u32x4*>(smem + L_X2P + (i * 2 + hi) * 16);
#pragma unroll
          for (int e = 0; e < 4; ++e) {
            const unsigned h2 = ln_pair(x1p[i][e], mb1, rd1);
            unsigned t = pack2(lo16(x1p[i][e]) + lo16(h2), hi16(x1p[i][e]) + hi16(h2));
            PIN(t);
            res[i][e] = routed ? pr[e] : t;
          }
        }
      }
#pragma unroll
      for (int g = 0; g < 8; ++g) PIN(wrc[g]);
#pragma unroll
      for (int e = 0; e < 16; ++e) { acc[e >> 3][2 * (e & 7)] = lo16(res[e >> 2][e & 3]); acc[e >> 3][2 * (e & 7) + 1] = hi16(res[e >> 2][e & 3]); }
      unsigned soff = tn * 4096;
      const unsigned a0 = lbase + wrow * 128 + ((wch ^ wsw) << 4) + 8 * (tp & 1), a1 = lbase + wrow * 128 + (((4 + wch) ^ wsw) << 4) + 8 * (tp & 1);
      asm volatile(
        "ds_read_b64_tr_b16 v[32:33], v193 offset:0\n\t"
        "ds_read_b64_tr_b16 v[34:35], v193 offset:1024\n\t"
        "ds_read_b64_tr_b16 v[36:37], v194 offset:0\n\t"
        "ds_read_b64_tr_b16 v[38:39], v194 offset:1024\n\t"
        "ds_read_b64_tr_b16 v[40:41], v193 offset:2048\n\t"
        "ds_read_b64_tr_b16 v[42:43], v193 offset:3072\n\t"
        "ds_read_b64_tr_b16 v[44:45], v194 offset:2048\n\t"
        "ds_read_b64_tr_b16 v[46:47], v194 offset:3072\n\t"
        "ds_read_b64_tr_b16 v[48:49], v193 offset:4096\n\t"
        "ds_read_b64_tr_b16 v[50:51], v193 offset:5120\n\t"
        "ds_read_b64_tr_b16 v[52:53], v194 offset:4096\n\t"
        "ds_read_b64_tr_b16 v[54:55], v194 offset:5120\n\t"
        "ds_read_b64_tr_b16 v[56:57], v193 offset:6144\n\t"
        "ds_read_b64_tr_b16 v[58:59], v193 offset:7168\n\t"
        "ds_read_b64_tr_b16 v[60:61], v194 offset:6144\n\t"
        "ds_read_b64_tr_b16 v[62:63], v194 offset:7168\n\t"
        "s_mov_b32 s45, 0x200000\n\t"
        "2:\n\t"
        "ds_read_b32 v200, v198\n\t"
        "s_sub_u32 s45, s45, 1\n\t"
        "s_waitcnt lgkmcnt(0)\n\t"
        "s_cbranch_scc1 3f\n\t"
        "v_cmp_ne_u32 vcc, v199, v200\n\t"
        "s_cbranch_vccnz 2b\n\t"
        "3:\n\t"
        "buffer_load_dwordx4 v[64:67], v192, s[40:43], s44 offen offset:0 sc1\n\t"
        "buffer_load_dwordx4 v[68:71], v192, s[40:43], s44 offen offset:1024 sc1\n\t"
        "buffer_load_dwordx4 v[72:75], v192, s[40:43], s44 offen offset:2048 sc1\n\t"
        "buffer_load_dwordx4 v[76:79], v192, s[40:43], s44 offen offset:3072 sc1\n\t"
        "s_add_u32 s44, s44, 0x1000\n\t"
        "s_and_b32 s44, s44, 0xffff\n\t"
        "buffer_load_dwordx4 v[80:83], v192, s[40:43], s44 offen offset:0 sc1\n\t"
        "buffer_load_dwordx4 v[84:87], v192, s[40:43], s44 offen offset:1024 sc1\n\t"
        "buffer_load_dwordx4 v[88:91], v192, s[40:43], s44 offen offset:2048 sc1\n\t"
        "buffer_load_dwordx4 v[92:95], v192, s[40:43], s44 offen offset:3072 sc1\n\t"
        "s_add_u32 s44, s44, 0x1000\n\t"
        "s_and_b32 s44, s44, 0xffff\n\t"
        "buffer_load_dwordx4 v[96:99], v192, s[40:43], s44 offen offset:0 sc1\n\t"
        "buffer_load_dwordx4 v[100:103], v192, s[40:43], s44 offen offset:1024 sc1\n\t"
        "buffer_load_dwordx4 v[104:107], v192, s[40:43], s44 offen offset:2048 sc1\n\t"
        "buffer_load_dwordx4 v[108:111], v192, s[40:43], s44 offen offset:3072 sc1\n\t"
        "s_waitcnt vmcnt(11) lgkmcnt(14)\n\t"
        "v_mfma_f32_32x32x16_bf16 v[0:15], v[32:35], v[64:67], v[0:15]\n\t"
        "ds_read_b64_tr_b16 v[32:33], v193 offset:8192\n\t"
        "ds_read_b64_tr_b16 v[34:35], v193 offset:9216\n\t"
        "s_waitcnt lgkmcnt(14)\n\t"
        "v_mfma_f32_32x32x16_bf16 v[16:31], v[36:39], v[64:67], v[16:31]\n\t"
        "ds_read_b64_tr_b16 v[36:37], v194 offset:8192\n\t"
        "ds_read_b64_tr_b16 v[38:39], v194 offset:9216\n\t"
        "s_add_u32 s44, s44, 0x1000\n\t"
        "s_and_b32 s44, s44, 0xffff\n\t"
        "buffer_load_dwordx4 v[112:115], v192, s[40:43], s44 offen offset:0 sc1\n\t"
        "s_waitcnt vmcnt(11) lgkmcnt(14)\n\t"
        "v_mfma_f32_32x32x16_bf16 v[0:15], v[40:43], v[68:71], v[0:15]\n\t"
        "ds_read_b64_tr_b16 v[40:41], v193 offset:10240\n\t"
        "ds_read_b64_tr_b16 v[42:43], v193 offset:11264\n\t"
        "s_waitcnt lgkmcnt(14)\n\t"
        "v_mfma_f32_32x32x16_bf16 v[16:31], v[44:47], v[68:71], v[16:31]\n\t"
        "ds_read_b64_tr_b16 v[44:45], v194 offset:10240\n\t"
        "ds_read_b64_tr_b16 v[46:47], v194 offset:11264\n\t"
        "buffer_load_dwordx4 v[116:119], v192, s[40:43], s44 offen offset:1024 sc1\n\t"
        "s_waitcnt vmcnt(11) lgkmcnt(14)\n\t"
        "v_mfma_f32_32x32x16_bf16 v[0:15], v[48:51], v[72:75], v[0:15]\n\t"
        "ds_read_b64_tr_b16 v[48:49], v193 offset:12288\n\t"
        "ds_read_b64_tr_b16 v[50:51], v193 offset:13312\n\t"
        "s_waitcnt lgkmcnt(14)\n\t"
        "v_mfma_f32_32x32x16_bf16 v[16:31], v[52:55], v[72:75], v[16:31]\n\t"
        "ds_read_b64_tr_b16 v[52:53], v194 offset:12288\n\t"
        "ds_read_b64_tr_b16 v[54:55], v194 offset:13312\n\t"
        "buffer_load_dwordx4 v[120:123], v192, s[40:43], s44 offen offset:2048 sc1\n\t"
        "s_waitcnt vmcnt(11) lgkmcnt(14)\n\t"
        "v_mfma_f32_32x32x16_bf16 v[0:15], v[56:59], v[76:79], v[0:15]\n\t"
        "ds_read_b64_tr_b16 v[56:57], v193 offset:14336\n\t"
        "ds_read_b64_tr_b16 v[58:59], v193 offset:15360\n\t"
        "s_waitcnt lgkmcnt(14)\n\t"
        "v_mfma_f32_32x32x16_bf16 v[16:31], v[60:63], v[76:79], v[16:31]\n\t"
        "ds_read_b64_tr_b16 v[60:61], v194 offset:14336\n\t"
        "ds_read_b64_tr_b16 v[62:63], v194 offset:15360\n\t"
        "buffer_load_dwordx4 v[124:127], v192, s[40:43], s44 offen offset:3072 sc1\n\t"
        "s_waitcnt vmcnt(11) lgkmcnt(14)\n\t"
        "v_mfma_f32_32x32x16_bf16 v[0:15], v[32:35], v[80:83], v[0:15]\n\t"
        "ds_read_b64_tr_b16 v[32:33], v193 offset:16384\n\t"
        "ds_read_b64_tr_b16 v[34:35], v193 offset:17408\n\t"
        "s_waitcnt lgkmcnt(14)\n\t"
        "v_mfma_f32_32x32x16_bf16 v[16:31], v[36:39], v[80:83], v[16:31]\n\t"
        "ds_read_b64_tr_b16 v[36:37], v194 offset:16384\n\t"
        "ds_read_b64_tr_b16 v[38:39], v194 offset:17408\n\t"
        "s_add_u32 s44, s44, 0x1000\n\t"
        "s_and_b32 s44, s44, 0xffff\n\t"
        "buffer_load_dwordx4 v[128:131], v192, s[40:43], s44 offen offset:0 sc1\n\t"
        "s_waitcnt vmcnt(11) lgkmcnt(14)\n\t"
        "v_mfma_f32_32x32x16_bf16 v[0:15], v[40:43], v[84:87], v[0:15]\n\t"
        "ds_read_b64_tr_b16 v[40:41], v193 offset:18432\n\t"
        "ds_read_b64_tr_b16 v[42:43], v193 offset:19456\n\t"
        "s_waitcnt lgkmcnt(14)\n\t"
        "v_mfma_f32_32x32x16_bf16 v[16:31], v[44:47], v[84:87], v[16:31]\n\t"
        "ds_read_b64_tr_b16 v[44:45], v194 offset:18432\n\t"
        "ds_read_b64_tr_b16 v[46:47], v194 offset:19456\n\t"
        "buffer_load_dwordx4 v[132:135], v192, s[40:43], s44 offen offset:1024 sc1\n\t"
        "s_waitcnt vmcnt(11) lgkmcnt(14)\n\t"
        "v_mfma_f32_32x32x16_bf16 v[0:15], v[48:51], v[88:91], v[0:15]\n\t"
        "ds_read_b64_tr_b16 v[48:49], v193 offset:20480\n\t"
        "ds_read_b64_tr_b16 v[50:51], v193 offset:21504\n\t"
        "s_waitcnt lgkmcnt(14)\n\t"
        "v_mfma_f32_32x32x16_bf16 v[16:31], v[52:55], v[88:91], v[16:31]\n\t"
        "ds_read_b64_tr_b16 v[52:53], v194 offset:20480\n\t"
        "ds_read_b64_tr_b16 v[54:55], v194 offset:21504\n\t"
        "buffer_load_dwordx4 v[136:139], v192, s[40:43], s44 offen offset:2048 sc1\n\t"
        "s_waitcnt vmcnt(11) lgkmcnt(14)\n\t"
        "v_mfma_f32_32x32x16_bf16 v[0:15], v[56:59], v[92:95], v[0:15]\n\t"
        "ds_read_b64_tr_b16 v[56:57], v193 offset:22528\n\t"
        "ds_read_b64_tr_b16 v[58:59], v193 offset:23552\n\t"
        "s_waitcnt lgkmcnt(14)\n\t"
        "v_mfma_f32_32x32x16_bf16 v[16:31], v[60:63], v[92:95], v[16:31]\n\t"
        "ds_read_b64_tr_b16 v[60:61], v194 offset:22528\n\t"
        "ds_read_b64_tr_b16 v[62:63], v194 offset:23552\n\t"
        "buffer_load_dwordx4 v[140:143], v192, s[40:43], s44 offen offset:3072 sc1\n\t"
        "s_waitcnt vmcnt(11) lgkmcnt(14)\n\t"
        "v_mfma_f32_32x32x16_bf16 v[0:15], v[32:35], v[96:99], v[0:15]\n\t"
        "ds_read_b64_tr_b16 v[32:33], v193 offset:24576\n\t"
        "ds_read_b64_tr_b16 v[34:35], v193 offset:25600\n\t"
        "s_waitcnt lgkmcnt(14)\n\t"
        "v_mfma_f32_32x32x16_bf16 v[16:31], v[36:39], v[96:99], v[16:31]\n\t"
        "ds_read_b64_tr_b16 v[36:37], v194 offset:24576\n\t"
        "ds_read_b64_tr_b16 v[38:39], v194 offset:25600\n\t"
        "s_add_u32 s44, s44, 0x1000\n\t"
        "s_and_b32 s44, s44, 0xffff\n\t"
        "buffer_load_dwordx4 v[144:147], v192, s[40:43], s44 offen offset:0 sc1\n\t"
        "s_waitcnt vmcnt(11) lgkmcnt(14)\n\t"
        "v_mfma_f32_32x32x16_bf16 v[0:15], v[40:43], v[100:103], v[0:15]\n\t"
        "ds_read_b64_tr_b16 v[40:41], v193 offset:26624\n\t"
        "ds_read_b64_tr_b16 v[42:43], v193 offset:27648\n\t"
        "s_waitcnt lgkmcnt(14)\n\t"
        "v_mfma_f32_32x32x16_bf16 v[16:31], v[44:47], v[100:103], v[16:31]\n\t"
        "ds_read_b64_tr_b16 v[44:45], v194 offset:26624\n\t"
        "ds_read_b64_tr_b16 v[46:47], v194 offset:27648\n\t"
        "buffer_load_dwordx4 v[148:151], v192, s[40:43], s44 offen offset:1024 sc1\n\t"
        "s_waitcnt vmcnt(11) lgkmcnt(14)\n\t"
        "v_mfma_f32_32x32x16_bf16 v[0:15], v[48:51], v[104:107], v[0:15]\n\t"
        "ds_read_b64_tr_b16 v[48:49], v193 offset:28672\n\t"
        "ds_read_b64_tr_b16 v[50:51], v193 offset:29696\n\t"
        "s_waitcnt lgkmcnt(14)\n\t"
        "v_mfma_f32_32x32x16_bf16 v[16:31], v[52:55], v[104:107], v[16:31]\n\t"
        "ds_read_b64_tr_b16 v[52:53], v194 offset:28672\n\t"
        "ds_read_b64_tr_b16 v[54:55], v194 offset:29696\n\t"
        "buffer_load_dwordx4 v[152:155], v192, s[40:43], s44 offen offset:2048 sc1\n\t"
        "s_waitcnt vmcnt(11) lgkmcnt(14)\n\t"
        "v_mfma_f32_32x32x16_bf16 v[0:15], v[56:59], v[108:111], v[0:15]\n\t"
        "ds_read_b64_tr_b16 v[56:57], v193 offset:30720\n\t"
        "ds_read_b64_tr_b16 v[58:59], v193 offset:31744\n\t"
        "s_waitcnt lgkmcnt(14)\n\t"
        "v_mfma_f32_32x32x16_bf16 v[16:31], v[60:63], v[108:111], v[16:31]\n\t"
        "ds_read_b64_tr_b16 v[60:61], v194 offset:30720\n\t"
        "ds_read_b64_tr_b16 v[62:63], v194 offset:31744\n\t"
        "buffer_load_dwordx4 v[156:159], v192, s[40:43], s44 offen offset:3072 sc1\n\t"
        "s_waitcnt vmcnt(11) lgkmcnt(14)\n\t"
        "v_mfma_f32_32x32x16_bf16 v[0:15], v[32:35], v[112:115], v[0:15]\n\t"
        "ds_read_b64_tr_b16 v[32:33], v193 offset:32768\n\t"
        "ds_read_b64_tr_b16 v[34:35], v193 offset:33792\n\t"
        "s_waitcnt lgkmcnt(14)\n\t"
        "v_mfma_f32_32x32x16_bf16 v[16:31], v[36:39], v[112:115], v[16:31]\n\t"
        "ds_read_b64_tr_b16 v[36:37], v194 offset:32768\n\t"
        "ds_read_b64_tr_b16 v[38:39], v194 offset:33792\n\t"
        "s_add_u32 s44, s44, 0x1000\n\t"
        "s_and_b32 s44, s44, 0xffff\n\t"
        "buffer_load_dwordx4 v[160:163], v192, s[40:43], s44 offen offset:0 sc1\n\t"
        "s_waitcnt vmcnt(11) lgkmcnt(14)\n\t"
        "v_mfma_f32_32x32x16_bf16 v[0:15], v[40:43], v[116:119], v[0:15]\n\t"
        "ds_read_b64_tr_b16 v[40:41], v193 offset:34816\n\t"
        "ds_read_b64_tr_b16 v[42:43], v193 offset:35840\n\t"
        "s_waitcnt lgkmcnt(14)\n\t"
        "v_mfma_f32_32x32x16_bf16 v[16:31], v[44:47], v[116:119], v[16:31]\n\t"
        "ds_read_b64_tr_b16 v[44:45], v194 offset:34816\n\t"
        "ds_read_b64_tr_b16 v[46:47], v194 offset:35840\n\t"
        "buffer_load_dwordx4 v[164:167], v192, s[40:43], s44 offen offset:1024 sc1\n\t"
        "s_waitcnt vmcnt(11) lgkmcnt(14)\n\t"
        "v_mfma_f32_32x32x16_bf16 v[0:15], v[48:51], v[120:123], v[0:15]\n\t"
        "ds_read_b64_tr_b16 v[48:49], v193 offset:36864\n\t"
        "ds_read_b64_tr_b16 v[50:51], v193 offset:37888\n\t"
        "s_waitcnt lgkmcnt(14)\n\t"
        "v_mfma_f32_32x32x16_bf16 v[16:31], v[52:55], v[120:123], v[16:31]\n\t"
        "ds_read_b64_tr_b16 v[52:53], v194 offset:36864\n\t"
        "ds_read_b64_tr_b16 v[54:55], v194 offset:37888\n\t"
        "buffer_load_dwordx4 v[168:171], v192, s[40:43], s44 offen offset:2048 sc1\n\t"
        "s_waitcnt vmcnt(11) lgkmcnt(14)\n\t"
        "v_mfma_f32_32x32x16_bf16 v[0:15], v[56:59], v[124:127], v[0:15]\n\t"
        "ds_read_b64_tr_b16 v[56:57], v193 offset:38912\n\t"
        "ds_read_b64_tr_b16 v[58:59], v193 offset:39936\n\t"
        "s_waitcnt lgkmcnt(14)\n\t"
        "v_mfma_f32_32x32x16_bf16 v[16:31], v[60:63], v[124:127], v[16:31]\n\t"
        "ds_read_b64_tr_b16 v[60:61], v194 offset:38912\n\t"
        "ds_read_b64_tr_b16 v[62:63], v194 offset:39936\n\t"
        "buffer_load_dwordx4 v[172:175], v192, s[40:43], s44 offen offset:3072 sc1\n\t"
        "s_waitcnt vmcnt(11) lgkmcnt(14)\n\t"
        "v_mfma_f32_32x32x16_bf16 v[0:15], v[32:35], v[128:131], v[0:15]\n\t"
        "ds_read_b64_tr_b16 v[32:33], v193 offset:40960\n\t"
        "ds_read_b64_tr_b16 v[34:35], v193 offset:41984\n\t"
        "s_waitcnt lgkmcnt(14)\n\t"
        "v_mfma_f32_32x32x16_bf16 v[16:31], v[36:39], v[128:131], v[16:31]\n\t"
        "ds_read_b64_tr_b16 v[36:37], v194 offset:40960\n\t"
        "ds_read_b64_tr_b16 v[38:39], v194 offset:41984\n\t"
        "s_add_u32 s44, s44, 0x1000\n\t"
        "s_and_b32 s44, s44, 0xffff\n\t"
        "buffer_load_dwordx4 v[176:179], v192, s[40:43], s44 offen offset:0 sc1\n\t"
        "s_waitcnt vmcnt(11) lgkmcnt(14)\n\t"
        "v_mfma_f32_32x32x16_bf16 v[0:15], v[40:43], v[132:135], v[0:15]\n\t"
        "ds_read_b64_tr_b16 v[40:41], v193 offset:43008\n\t"
        "ds_read_b64_tr_b16 v[42:43], v193 offset:44032\n\t"
        "s_waitcnt lgkmcnt(14)\n\t"
        "v_mfma_f32_32x32x16_bf16 v[16:31], v[44:47], v[132:135], v[16:31]\n\t"
        "ds_read_b64_tr_b16 v[44:45], v194 offset:43008\n\t"
        "ds_read_b64_tr_b16 v[46:47], v194 offset:44032\n\t"
        "buffer_load_dwordx4 v[180:183], v192, s[40:43], s44 offen offset:1024 sc1\n\t"
        "s_waitcnt vmcnt(11) lgkmcnt(14)\n\t"
        "v_mfma_f32_32x32x16_bf16 v[0:15], v[48:51], v[136:139], v[0:15]\n\t"
        "ds_read_b64_tr_b16 v[48:49], v193 offset:45056\n\t"
        "ds_read_b64_tr_b16 v[50:51], v193 offset:46080\n\t"
        "s_waitcnt lgkmcnt(14)\n\t"
        "v_mfma_f32_32x32x16_bf16 v[16:31], v[52:55], v[136:139], v[16:31]\n\t"
        "ds_read_b64_tr_b16 v[52:53], v194 offset:45056\n\t"
        "ds_read_b64_tr_b16 v[54:55], v194 offset:46080\n\t"
        "buffer_load_dwordx4 v[184:187], v192, s[40:43], s44 offen offset:2048 sc1\n\t"
        "s_waitcnt vmcnt(11) lgkmcnt(14)\n\t"
        "v_mfma_f32_32x32x16_bf16 v[0:15], v[56:59], v[140:143], v[0:15]\n\t"
        "ds_read_b64_tr_b16 v[56:57], v193 offset:47104\n\t"
        "ds_read_b64_tr_b16 v[58:59], v193 offset:48128\n\t"
        "s_waitcnt lgkmcnt(14)\n\t"
        "v_mfma_f32_32x32x16_bf16 v[16:31], v[60:63], v[140:143], v[16:31]\n\t"
        "ds_read_b64_tr_b16 v[60:61], v194 offset:47104\n\t"
        "ds_read_b64_tr_b16 v[62:63], v194 offset:48128\n\t"
        "buffer_load_dwordx4 v[188:191], v192, s[40:43], s44 offen offset:3072 sc1\n\t"
        "s_waitcnt vmcnt(11) lgkmcnt(14)\n\t"
        "v_mfma_f32_32x32x16_bf16 v[0:15], v[32:35], v[144:147], v[0:15]\n\t"
        "ds_read_b64_tr_b16 v[32:33], v193 offset:49152\n\t"
        "ds_read_b64_tr_b16 v[34:35], v193 offset:50176\n\t"
        "s_waitcnt lgkmcnt(14)\n\t"
        "v_mfma_f32_32x32x16_bf16 v[16:31], v[36:39], v[144:147], v[16:31]\n\t"
        "ds_read_b64_tr_b16 v[36:37], v194 offset:49152\n\t"
        "ds_read_b64_tr_b16 v[38:39], v194 offset:50176\n\t"
        "s_add_u32 s44, s44, 0x1000\n\t"
        "s_and_b32 s44, s44, 0xffff\n\t"
        "buffer_load_dwordx4 v[64:67], v192, s[40:43], s44 offen offset:0 sc1\n\t"
        "s_waitcnt vmcnt(11) lgkmcnt(14)\n\t"
        "v_mfma_f32_32x32x16_bf16 v[0:15], v[40:43], v[148:151], v[0:15]\n\t"
        "ds_read_b64_tr_b16 v[40:41], v193 offset:51200\n\t"
        "ds_read_b64_tr_b16 v[42:43], v193 offset:52224\n\t"
        "s_waitcnt lgkmcnt(14)\n\t"
        "v_mfma_f32_32x32x16_bf16 v[16:31], v[44:47], v[148:151], v[16:31]\n\t"
        "ds_read_b64_tr_b16 v[44:45], v194 offset:51200\n\t"
        "ds_read_b64_tr_b16 v[46:47], v194 offset:52224\n\t"
        "buffer_load_dwordx4 v[68:71], v192, s[40:43], s44 offen offset:1024 sc1\n\t"
        "s_waitcnt vmcnt(11) lgkmcnt(14)\n\t"
        "v_mfma_f32_32x32x16_bf16 v[0:15], v[48:51], v[152:155], v[0:15]\n\t"
        "ds_read_b64_tr_b16 v[48:49], v193 offset:53248\n\t"
        "ds_read_b64_tr_b16 v[50:51], v193 offset:54272\n\t"
        "s_waitcnt lgkmcnt(14)\n\t"
        "v_mfma_f32_32x32x16_bf16 v[16:31], v[52:55], v[152:155], v[16:31]\n\t"
        "ds_read_b64_tr_b16 v[52:53], v194 offset:53248\n\t"
        "ds_read_b64_tr_b16 v[54:55], v194 offset:54272\n\t"
        "buffer_load_dwordx4 v[72:75], v192, s[40:43], s44 offen offset:2048 sc1\n\t"
        "s_waitcnt vmcnt(11) lgkmcnt(14)\n\t"
        "v_mfma_f32_32x32x16_bf16 v[0:15], v[56:59], v[156:159], v[0:15]\n\t"
        "ds_read_b64_tr_b16 v[56:57], v193 offset:55296\n\t"
        "ds_read_b64_tr_b16 v[58:59], v193 offset:56320\n\t"
        "s_waitcnt lgkmcnt(14)\n\t"
        "v_mfma_f32_32x32x16_bf16 v[16:31], v[60:63], v[156:159], v[16:31]\n\t"
        "ds_read_b64_tr_b16 v[60:61], v194 offset:55296\n\t"
        "ds_read_b64_tr_b16 v[62:63], v194 offset:56320\n\t"
        "buffer_load_dwordx4 v[76:79], v192, s[40:43], s44 offen offset:3072 sc1\n\t"
        "s_waitcnt vmcnt(11) lgkmcnt(14)\n\t"
        "v_mfma_f32_32x32x16_bf16 v[0:15], v[32:35], v[160:163], v[0:15]\n\t"
        "ds_read_b64_tr_b16 v[32:33], v193 offset:57344\n\t"
        "ds_read_b64_tr_b16 v[34:35], v193 offset:58368\n\t"
        "s_waitcnt lgkmcnt(14)\n\t"
        "v_mfma_f32_32x32x16_bf16 v[16:31], v[36:39], v[160:163], v[16:31]\n\t"
        "ds_read_b64_tr_b16 v[36:37], v194 offset:57344\n\t"
        "ds_read_b64_tr_b16 v[38:39], v194 offset:58368\n\t"
        "s_add_u32 s44, s44, 0x1000\n\t"
        "s_and_b32 s44, s44, 0xffff\n\t"
        "buffer_load_dwordx4 v[80:83], v192, s[40:43], s44 offen offset:0 sc1\n\t"
        "s_waitcnt vmcnt(11) lgkmcnt(14)\n\t"
        "v_mfma_f32_32x32x16_bf16 v[0:15], v[40:43], v[164:167], v[0:15]\n\t"
        "ds_read_b64_tr_b16 v[40:41], v193 offset:59392\n\t"
        "ds_read_b64_tr_b16 v[42:43], v193 offset:60416\n\t"
        "s_waitcnt lgkmcnt(14)\n\t"
        "v_mfma_f32_32x32x16_bf16 v[16:31], v[44:47], v[164:167], v[16:31]\n\t"
        "ds_read_b64_tr_b16 v[44:45], v194 offset:59392\n\t"
        "ds_read_b64_tr_b16 v[46:47], v194 offset:60416\n\t"
        "buffer_load_dwordx4 v[84:87], v192, s[40:43], s44 offen offset:1024 sc1\n\t"
        "s_waitcnt vmcnt(11) lgkmcnt(14)\n\t"
        "v_mfma_f32_32x32x16_bf16 v[0:15], v[48:51], v[168:171], v[0:15]\n\t"
        "ds_read_b64_tr_b16 v[48:49], v193 offset:61440\n\t"
        "ds_read_b64_tr_b16 v[50:51], v193 offset:62464\n\t"
        "s_waitcnt lgkmcnt(14)\n\t"
        "v_mfma_f32_32x32x16_bf16 v[16:31], v[52:55], v[168:171], v[16:31]\n\t"
        "ds_read_b64_tr_b16 v[52:53], v194 offset:61440\n\t"
        "ds_read_b64_tr_b16 v[54:55], v194 offset:62464\n\t"
        "buffer_load_dwordx4 v[88:91], v192, s[40:43], s44 offen offset:2048 sc1\n\t"
        "s_waitcnt vmcnt(11) lgkmcnt(14)\n\t"
        "v_mfma_f32_32x32x16_bf16 v[0:15], v[56:59], v[172:175], v[0:15]\n\t"
        "ds_read_b64_tr_b16 v[56:57], v193 offset:63488\n\t"
        "ds_read_b64_tr_b16 v[58:59], v193 offset:64512\n\t"
        "s_waitcnt lgkmcnt(14)\n\t"
        "v_mfma_f32_32x32x16_bf16 v[16:31], v[60:63], v[172:175], v[16:31]\n\t"
        "ds_read_b64_tr_b16 v[60:61], v194 offset:63488\n\t"
        "ds_read_b64_tr_b16 v[62:63], v194 offset:64512\n\t"
        "buffer_load_dwordx4 v[92:95], v192, s[40:43], s44 offen offset:3072 sc1\n\t"
        "s_waitcnt vmcnt(11) lgkmcnt(14)\n\t"
        "v_mfma_f32_32x32x16_bf16 v[0:15], v[32:35], v[176:179], v[0:15]\n\t"
        "ds_read_b64_tr_b16 v[32:33], v195 offset:0\n\t"
        "ds_read_b64_tr_b16 v[34:35], v195 offset:1024\n\t"
        "s_waitcnt lgkmcnt(14)\n\t"
        "v_mfma_f32_32x32x16_bf16 v[16:31], v[36:39], v[176:179], v[16:31]\n\t"
        "ds_read_b64_tr_b16 v[36:37], v196 offset:0\n\t"
        "ds_read_b64_tr_b16 v[38:39], v196 offset:1024\n\t"
        "s_add_u32 s44, s44, 0x1000\n\t"
        "s_and_b32 s44, s44, 0xffff\n\t"
        "buffer_load_dwordx4 v[96:99], v192, s[40:43], s44 offen offset:0 sc1\n\t"
        "s_waitcnt vmcnt(11) lgkmcnt(14)\n\t"
        "v_mfma_f32_32x32x16_bf16 v[0:15], v[40:43], v[180:183], v[0:15]\n\t"
        "ds_read_b64_tr_b16 v[40:41], v195 offset:2048\n\t"
        "ds_read_b64_tr_b16 v[42:43], v195 offset:3072\n\t"
        "s_waitcnt lgkmcnt(14)\n\t"
        "v_mfma_f32_32x32x16_bf16 v[16:31], v[44:47], v[180:183], v[16:31]\n\t"
        "ds_read_b64_tr_b16 v[44:45], v196 offset:2048\n\t"
        "ds_read_b64_tr_b16 v[46:47], v196 offset:3072\n\t"
        "buffer_load_dwordx4 v[100:103], v192, s[40:43], s44 offen offset:1024 sc1\n\t"
        "s_waitcnt vmcnt(11) lgkmcnt(14)\n\t"
        "v_mfma_f32_32x32x16_bf16 v[0:15], v[48:51], v[184:187], v[0:15]\n\t"
        "ds_read_b64_tr_b16 v[48:49], v195 offset:4096\n\t"
        "ds_read_b64_tr_b16 v[50:51], v195 offset:5120\n\t"
        "s_waitcnt lgkmcnt(14)\n\t"
        "v_mfma_f32_32x32x16_bf16 v[16:31], v[52:55], v[184:187], v[16:31]\n\t"
        "ds_read_b64_tr_b16 v[52:53], v196 offset:4096\n\t"
        "ds_read_b64_tr_b16 v[54:55], v196 offset:5120\n\t"
        "buffer_load_dwordx4 v[104:107], v192, s[40:43], s44 offen offset:2048 sc1\n\t"
        "s_waitcnt vmcnt(11) lgkmcnt(14)\n\t"
        "v_mfma_f32_32x32x16_bf16 v[0:15], v[56:59], v[188:191], v[0:15]\n\t"
        "ds_read_b64_tr_b16 v[56:57], v195 offset:6144\n\t"
        "ds_read_b64_tr_b16 v[58:59], v195 offset:7168\n\t"
        "s_waitcnt lgkmcnt(14)\n\t"
        "v_mfma_f32_32x32x16_bf16 v[16:31], v[60:63], v[188:191], v[16:31]\n\t"
        "ds_read_b64_tr_b16 v[60:61], v196 offset:6144\n\t"
        "ds_read_b64_tr_b16 v[62:63], v196 offset:7168\n\t"
        "buffer_load_dwordx4 v[108:111], v192, s[40:43], s44 offen offset:3072 sc1\n\t"
        "s_waitcnt vmcnt(11) lgkmcnt(14)\n\t"
        "v_mfma_f32_32x32x16_bf16 v[0:15], v[32:35], v[64:67], v[0:15]\n\t"
        "ds_read_b64_tr_b16 v[32:33], v195 offset:8192\n\t"
        "ds_read_b64_tr_b16 v[34:35], v195 offset:9216\n\t"
        "s_waitcnt lgkmcnt(14)\n\t"
        "v_mfma_f32_32x32x16_bf16 v[16:31], v[36:39], v[64:67], v[16:31]\n\t"
        "ds_read_b64_tr_b16 v[36:37], v196 offset:8192\n\t"
        "ds_read_b64_tr_b16 v[38:39], v196 offset:9216\n\t"
        "s_add_u32 s44, s44, 0x1000\n\t"
        "s_and_b32 s44, s44, 0xffff\n\t"
        "buffer_load_dwordx4 v[112:115], v192, s[40:43], s44 offen offset:0 sc1\n\t"
        "s_waitcnt vmcnt(11) lgkmcnt(14)\n\t"
        "v_mfma_f32_32x32x16_bf16 v[0:15], v[40:43], v[68:71], v[0:15]\n\t"
        "ds_read_b64_tr_b16 v[40:41], v195 offset:10240\n\t"
        "ds_read_b64_tr_b16 v[42:43], v195 offset:11264\n\t"
        "s_waitcnt lgkmcnt(14)\n\t"
        "v_mfma_f32_32x32x16_bf16 v[16:31], v[44:47], v[68:71], v[16:31]\n\t"
        "ds_read_b64_tr_b16 v[44:45], v196 offset:10240\n\t"
        "ds_read_b64_tr_b16 v[46:47], v196 offset:11264\n\t"
        "buffer_load_dwordx4 v[116:119], v192, s[40:43], s44 offen offset:1024 sc1\n\t"
        "s_waitcnt vmcnt(11) lgkmcnt(14)\n\t"
        "v_mfma_f32_32x32x16_bf16 v[0:15], v[48:51], v[72:75], v[0:15]\n\t"
        "ds_read_b64_tr_b16 v[48:49], v195 offset:12288\n\t"
        "ds_read_b64_tr_b16 v[50:51], v195 offset:13312\n\t"
        "s_waitcnt lgkmcnt(14)\n\t"
        "v_mfma_f32_32x32x16_bf16 v[16:31], v[52:55], v[72:75], v[16:31]\n\t"
        "ds_read_b64_tr_b16 v[52:53], v196 offset:12288\n\t"
        "ds_read_b64_tr_b16 v[54:55], v196 offset:13312\n\t"
        "buffer_load_dwordx4 v[120:123], v192, s[40:43], s44 offen offset:2048 sc1\n\t"
        "s_waitcnt vmcnt(11) lgkmcnt(14)\n\t"
        "v_mfma_f32_32x32x16_bf16 v[0:15], v[56:59], v[76:79], v[0:15]\n\t"
        "ds_read_b64_tr_b16 v[56:57], v195 offset:14336\n\t"
        "ds_read_b64_tr_b16 v[58:59], v195 offset:15360\n\t"
        "s_waitcnt lgkmcnt(14)\n\t"
        "v_mfma_f32_32x32x16_bf16 v[16:31], v[60:63], v[76:79], v[16:31]\n\t"
        "ds_read_b64_tr_b16 v[60:61], v196 offset:14336\n\t"
        "ds_read_b64_tr_b16 v[62:63], v196 offset:15360\n\t"
        "buffer_load_dwordx4 v[124:127], v192, s[40:43], s44 offen offset:3072 sc1\n\t"
        "s_waitcnt vmcnt(11) lgkmcnt(14)\n\t"
        "v_mfma_f32_32x32x16_bf16 v[0:15], v[32:35], v[80:83], v[0:15]\n\t"
        "ds_read_b64_tr_b16 v[32:33], v195 offset:16384\n\t"
        "ds_read_b64_tr_b16 v[34:35], v195 offset:17408\n\t"
        "s_waitcnt lgkmcnt(14)\n\t"
        "v_mfma_f32_32x32x16_bf16 v[16:31], v[36:39], v[80:83], v[16:31]\n\t"
        "ds_read_b64_tr_b16 v[36:37], v196 offset:16384\n\t"
        "ds_read_b64_tr_b16 v[38:39], v196 offset:17408\n\t"
        "s_add_u32 s44, s44, 0x1000\n\t"
        "s_and_b32 s44, s44, 0xffff\n\t"
        "buffer_load_dwordx4 v[128:131], v192, s[40:43], s44 offen offset:0 sc1\n\t"
        "s_waitcnt vmcnt(11) lgkmcnt(14)\n\t"
        "v_mfma_f32_32x32x16_bf16 v[0:15], v[40:43], v[84:87], v[0:15]\n\t"
        "ds_read_b64_tr_b16 v[40:41], v195 offset:18432\n\t"
        "ds_read_b64_tr_b16 v[42:43], v195 offset:19456\n\t"
        "s_waitcnt lgkmcnt(14)\n\t"
        "v_mfma_f32_32x32x16_bf16 v[16:31], v[44:47], v[84:87], v[16:31]\n\t"
        "ds_read_b64_tr_b16 v[44:45], v196 offset:18432\n\t"
        "ds_read_b64_tr_b16 v[46:47], v196 offset:19456\n\t"
        "buffer_load_dwordx4 v[132:135], v192, s[40:43], s44 offen offset:1024 sc1\n\t"
        "s_waitcnt vmcnt(11) lgkmcnt(14)\n\t"
        "v_mfma_f32_32x32x16_bf16 v[0:15], v[48:51], v[88:91], v[0:15]\n\t"
        "ds_read_b64_tr_b16 v[48:49], v195 offset:20480\n\t"
        "ds_read_b64_tr_b16 v[50:51], v195 offset:21504\n\t"
        "s_waitcnt lgkmcnt(14)\n\t"
        "v_mfma_f32_32x32x16_bf16 v[16:31], v[52:55], v[88:91], v[16:31]\n\t"
        "ds_read_b64_tr_b16 v[52:53], v196 offset:20480\n\t"
        "ds_read_b64_tr_b16 v[54:55], v196 offset:21504\n\t"
        "buffer_load_dwordx4 v[136:139], v192, s[40:43], s44 offen offset:2048 sc1\n\t"
        "s_waitcnt vmcnt(11) lgkmcnt(14)\n\t"
        "v_mfma_f32_32x32x16_bf16 v[0:15], v[56:59], v[92:95], v[0:15]\n\t"
        "ds_read_b64_tr_b16 v[56:57], v195 offset:22528\n\t"
        "ds_read_b64_tr_b16 v[58:59], v195 offset:23552\n\t"
        "s_waitcnt lgkmcnt(14)\n\t"
        "v_mfma_f32_32x32x16_bf16 v[16:31], v[60:63], v[92:95], v[16:31]\n\t"
        "ds_read_b64_tr_b16 v[60:61], v196 offset:22528\n\t"
        "ds_read_b64_tr_b16 v[62:63], v196 offset:23552\n\t"
        "buffer_load_dwordx4 v[140:143], v192, s[40:43], s44 offen offset:3072 sc1\n\t"
        "s_waitcnt vmcnt(11) lgkmcnt(14)\n\t"
        "v_mfma_f32_32x32x16_bf16 v[0:15], v[32:35], v[96:99], v[0:15]\n\t"
        "ds_read_b64_tr_b16 v[32:33], v195 offset:24576\n\t"
        "ds_read_b64_tr_b16 v[34:35], v195 offset:25600\n\t"
        "s_waitcnt lgkmcnt(14)\n\t"
        "v_mfma_f32_32x32x16_bf16 v[16:31], v[36:39], v[96:99], v[16:31]\n\t"
        "ds_read_b64_tr_b16 v[36:37], v196 offset:24576\n\t"
        "ds_read_b64_tr_b16 v[38:39], v196 offset:25600\n\t"
        "s_add_u32 s44, s44, 0x1000\n\t"
        "s_and_b32 s44, s44, 0xffff\n\t"
        "buffer_load_dwordx4 v[144:147], v192, s[40:43], s44 offen offset:0 sc1\n\t"
        "s_waitcnt vmcnt(11) lgkmcnt(14)\n\t"
        "v_mfma_f32_32x32x16_bf16 v[0:15], v[40:43], v[100:103], v[0:15]\n\t"
        "ds_read_b64_tr_b16 v[40:41], v195 offset:26624\n\t"
        "ds_read_b64_tr_b16 v[42:43], v195 offset:27648\n\t"
        "s_waitcnt lgkmcnt(14)\n\t"
        "v_mfma_f32_32x32x16_bf16 v[16:31], v[44:47], v[100:103], v[16:31]\n\t"
        "ds_read_b64_tr_b16 v[44:45], v196 offset:26624\n\t"
        "ds_read_b64_tr_b16 v[46:47], v196 offset:27648\n\t"
        "buffer_load_dwordx4 v[148:151], v192, s[40:43], s44 offen offset:1024 sc1\n\t"
        "s_waitcnt vmcnt(11) lgkmcnt(14)\n\t"
        "v_mfma_f32_32x32x16_bf16 v[0:15], v[48:51], v[104:107], v[0:15]\n\t"
        "ds_read_b64_tr_b16 v[48:49], v195 offset:28672\n\t"
        "ds_read_b64_tr_b16 v[50:51], v195 offset:29696\n\t"
        "s_waitcnt lgkmcnt(14)\n\t"
        "v_mfma_f32_32x32x16_bf16 v[16:31], v[52:55], v[104:107], v[16:31]\n\t"
        "ds_read_b64_tr_b16 v[52:53], v196 offset:28672\n\t"
        "ds_read_b64_tr_b16 v[54:55], v196 offset:29696\n\t"
        "buffer_load_dwordx4 v[152:155], v192, s[40:43], s44 offen offset:2048 sc1\n\t"
        "s_waitcnt vmcnt(11) lgkmcnt(14)\n\t"
        "v_mfma_f32_32x32x16_bf16 v[0:15], v[56:59], v[108:111], v[0:15]\n\t"
        "ds_read_b64_tr_b16 v[56:57], v195 offset:30720\n\t"
        "ds_read_b64_tr_b16 v[58:59], v195 offset:31744\n\t"
        "s_waitcnt lgkmcnt(14)\n\t"
        "v_mfma_f32_32x32x16_bf16 v[16:31], v[60:63], v[108:111], v[16:31]\n\t"
        "ds_read_b64_tr_b16 v[60:61], v196 offset:30720\n\t"
        "ds_read_b64_tr_b16 v[62:63], v196 offset:31744\n\t"
        "buffer_load_dwordx4 v[156:159], v192, s[40:43], s44 offen offset:3072 sc1\n\t"
        "s_waitcnt vmcnt(11) lgkmcnt(14)\n\t"
        "v_mfma_f32_32x32x16_bf16 v[0:15], v[32:35], v[112:115], v[0:15]\n\t"
        "ds_read_b64_tr_b16 v[32:33], v195 offset:32768\n\t"
        "ds_read_b64_tr_b16 v[34:35], v195 offset:33792\n\t"
        "s_waitcnt lgkmcnt(14)\n\t"
        "v_mfma_f32_32x32x16_bf16 v[16:31], v[36:39], v[112:115], v[16:31]\n\t"
        "ds_read_b64_tr_b16 v[36:37], v196 offset:32768\n\t"
        "ds_read_b64_tr_b16 v[38:39], v196 offset:33792\n\t"
        "s_add_u32 s44, s44, 0x1000\n\t"
        "s_and_b32 s44, s44, 0xffff\n\t"
        "buffer_load_dwordx4 v[160:163], v192, s[40:43], s44 offen offset:0 sc1\n\t"
        "s_waitcnt vmcnt(11) lgkmcnt(14)\n\t"
        "v_mfma_f32_32x32x16_bf16 v[0:15], v[40:43], v[116:119], v[0:15]\n\t"
        "ds_read_b64_tr_b16 v[40:41], v195 offset:34816\n\t"
        "ds_read_b64_tr_b16 v[42:43], v195 offset:35840\n\t"
        "s_waitcnt lgkmcnt(14)\n\t"
        "v_mfma_f32_32x32x16_bf16 v[16:31], v[44:47], v[116:119], v[16:31]\n\t"
        "ds_read_b64_tr_b16 v[44:45], v196 offset:34816\n\t"
        "ds_read_b64_tr_b16 v[46:47], v196 offset:35840\n\t"
        "buffer_load_dwordx4 v[164:167], v192, s[40:43], s44 offen offset:1024 sc1\n\t"
        "s_waitcnt vmcnt(11) lgkmcnt(14)\n\t"
        "v_mfma_f32_32x32x16_bf16 v[0:15], v[48:51], v[120:123], v[0:15]\n\t"
        "ds_read_b64_tr_b16 v[48:49], v195 offset:36864\n\t"
        "ds_read_b64_tr_b16 v[50:51], v195 offset:37888\n\t"
        "s_waitcnt lgkmcnt(14)\n\t"
        "v_mfma_f32_32x32x16_bf16 v[16:31], v[52:55], v[120:123], v[16:31]\n\t"
        "ds_read_b64_tr_b16 v[52:53], v196 offset:36864\n\t"
        "ds_read_b64_tr_b16 v[54:55], v196 offset:37888\n\t"
        "buffer_load_dwordx4 v[168:171], v192, s[40:43], s44 offen offset:2048 sc1\n\t"
        "s_waitcnt vmcnt(11) lgkmcnt(14)\n\t"
        "v_mfma_f32_32x32x16_bf16 v[0:15], v[56:59], v[124:127], v[0:15]\n\t"
        "ds_read_b64_tr_b16 v[56:57], v195 offset:38912\n\t"
        "ds_read_b64_tr_b16 v[58:59], v195 offset:39936\n\t"
        "s_waitcnt lgkmcnt(14)\n\t"
        "v_mfma_f32_32x32x16_bf16 v[16:31], v[60:63], v[124:127], v[16:31]\n\t"
        "ds_read_b64_tr_b16 v[60:61], v196 offset:38912\n\t"
        "ds_read_b64_tr_b16 v[62:63], v196 offset:39936\n\t"
        "buffer_load_dwordx4 v[172:175], v192, s[40:43], s44 offen offset:3072 sc1\n\t"
        "s_waitcnt vmcnt(11) lgkmcnt(14)\n\t"
        "v_mfma_f32_32x32x16_bf16 v[0:15], v[32:35], v[128:131], v[0:15]\n\t"
        "ds_read_b64_tr_b16 v[32:33], v195 offset:40960\n\t"
        "ds_read_b64_tr_b16 v[34:35], v195 offset:41984\n\t"
        "s_waitcnt lgkmcnt(14)\n\t"
        "v_mfma_f32_32x32x16_bf16 v[16:31], v[36:39], v[128:131], v[16:31]\n\t"
        "ds_read_b64_tr_b16 v[36:37], v196 offset:40960\n\t"
        "ds_read_b64_tr_b16 v[38:39], v196 offset:41984\n\t"
        "s_add_u32 s44, s44, 0x1000\n\t"
        "s_and_b32 s44, s44, 0xffff\n\t"
        "buffer_load_dwordx4 v[176:179], v192, s[40:43], s44 offen offset:0 sc1\n\t"
        "s_waitcnt vmcnt(11) lgkmcnt(14)\n\t"
        "v_mfma_f32_32x32x16_bf16 v[0:15], v[40:43], v[132:135], v[0:15]\n\t"
        "ds_read_b64_tr_b16 v[40:41], v195 offset:43008\n\t"
        "ds_read_b64_tr_b16 v[42:43], v195 offset:44032\n\t"
        "s_waitcnt lgkmcnt(14)\n\t"
        "v_mfma_f32_32x32x16_bf16 v[16:31], v[44:47], v[132:135], v[16:31]\n\t"
        "ds_read_b64_tr_b16 v[44:45], v196 offset:43008\n\t"
        "ds_read_b64_tr_b16 v[46:47], v196 offset:44032\n\t"
        "buffer_load_dwordx4 v[180:183], v192, s[40:43], s44 offen offset:1024 sc1\n\t"
        "s_waitcnt vmcnt(11) lgkmcnt(14)\n\t"
        "v_mfma_f32_32x32x16_bf16 v[0:15], v[48:51], v[136:139], v[0:15]\n\t"
        "ds_read_b64_tr_b16 v[48:49], v195 offset:45056\n\t"
        "ds_read_b64_tr_b16 v[50:51], v195 offset:46080\n\t"
        "s_waitcnt lgkmcnt(14)\n\t"
        "v_mfma_f32_32x32x16_bf16 v[16:31], v[52:55], v[136:139], v[16:31]\n\t"
        "ds_read_b64_tr_b16 v[52:53], v196 offset:45056\n\t"
        "ds_read_b64_tr_b16 v[54:55], v196 offset:46080\n\t"
        "buffer_load_dwordx4 v[184:187], v192, s[40:43], s44 offen offset:2048 sc1\n\t"
        "s_waitcnt vmcnt(11) lgkmcnt(14)\n\t"
        "v_mfma_f32_32x32x16_bf16 v[0:15], v[56:59], v[140:143], v[0:15]\n\t"
        "ds_read_b64_tr_b16 v[56:57], v195 offset:47104\n\t"
        "ds_read_b64_tr_b16 v[58:59], v195 offset:48128\n\t"
        "s_waitcnt lgkmcnt(14)\n\t"
        "v_mfma_f32_32x32x16_bf16 v[16:31], v[60:63], v[140:143], v[16:31]\n\t"
        "ds_read_b64_tr_b16 v[60:61], v196 offset:47104\n\t"
        "ds_read_b64_tr_b16 v[62:63], v196 offset:48128\n\t"
        "buffer_load_dwordx4 v[188:191], v192, s[40:43], s44 offen offset:3072 sc1\n\t"
        "s_waitcnt vmcnt(11) lgkmcnt(14)\n\t"
        "v_mfma_f32_32x32x16_bf16 v[0:15], v[32:35], v[144:147], v[0:15]\n\t"
        "ds_read_b64_tr_b16 v[32:33], v195 offset:49152\n\t"
        "ds_read_b64_tr_b16 v[34:35], v195 offset:50176\n\t"
        "s_waitcnt lgkmcnt(14)\n\t"
        "v_mfma_f32_32x32x16_bf16 v[16:31], v[36:39], v[144:147], v[16:31]\n\t"
        "ds_read_b64_tr_b16 v[36:37], v196 offset:49152\n\t"
        "ds_read_b64_tr_b16 v[38:39], v196 offset:50176\n\t"
        "s_waitcnt vmcnt(10) lgkmcnt(14)\n\t"
        "v_mfma_f32_32x32x16_bf16 v[0:15], v[40:43], v[148:151], v[0:15]\n\t"
        "ds_read_b64_tr_b16 v[40:41], v195 offset:51200\n\t"
        "ds_read_b64_tr_b16 v[42:43], v195 offset:52224\n\t"
        "s_waitcnt lgkmcnt(14)\n\t"
        "v_mfma_f32_32x32x16_bf16 v[16:31], v[44:47], v[148:151], v[16:31]\n\t"
        "ds_read_b64_tr_b16 v[44:45], v196 offset:51200\n\t"
        "ds_read_b64_tr_b16 v[46:47], v196 offset:52224\n\t"
        "s_waitcnt vmcnt(9) lgkmcnt(14)\n\t"
        "v_mfma_f32_32x32x16_bf16 v[0:15], v[48:51], v[152:155], v[0:15]\n\t"
        "ds_read_b64_tr_b16 v[48:49], v195 offset:53248\n\t"
        "ds_read_b64_tr_b16 v[50:51], v195 offset:54272\n\t"
        "s_waitcnt lgkmcnt(14)\n\t"
        "v_mfma_f32_32x32x16_bf16 v[16:31], v[52:55], v[152:155], v[16:31]\n\t"
        "ds_read_b64_tr_b16 v[52:53], v196 offset:53248\n\t"
        "ds_read_b64_tr_b16 v[54:55], v196 offset:54272\n\t"
        "s_waitcnt vmcnt(8) lgkmcnt(14)\n\t"
        "v_mfma_f32_32x32x16_bf16 v[0:15], v[56:59], v[156:159], v[0:15]\n\t"
        "ds_read_b64_tr_b16 v[56:57], v195 offset:55296\n\t"
        "ds_read_b64_tr_b16 v[58:59], v195 offset:56320\n\t"
        "s_waitcnt lgkmcnt(14)\n\t"
        "v_mfma_f32_32x32x16_bf16 v[16:31], v[60:63], v[156:159], v[16:31]\n\t"
        "ds_read_b64_tr_b16 v[60:61], v196 offset:55296\n\t"
        "ds_read_b64_tr_b16 v[62:63], v196 offset:56320\n\t"
        "s_waitcnt vmcnt(7) lgkmcnt(14)\n\t"
        "v_mfma_f32_32x32x16_bf16 v[0:15], v[32:35], v[160:163], v[0:15]\n\t"
        "s_barrier\n\t"
        "ds_read_b64_tr_b16 v[32:33], v195 offset:57344\n\t"
        "ds_read_b64_tr_b16 v[34:35], v195 offset:58368\n\t"
        "s_waitcnt lgkmcnt(14)\n\t"
        "v_mfma_f32_32x32x16_bf16 v[16:31], v[36:39], v[160:163], v[16:31]\n\t"
        "ds_read_b64_tr_b16 v[36:37], v196 offset:57344\n\t"
        "ds_read_b64_tr_b16 v[38:39], v196 offset:58368\n\t"
        "s_waitcnt vmcnt(6) lgkmcnt(14)\n\t"
        "v_mfma_f32_32x32x16_bf16 v[0:15], v[40:43], v[164:167], v[0:15]\n\t"
        "ds_read_b64_tr_b16 v[40:41], v195 offset:59392\n\t"
        "ds_read_b64_tr_b16 v[42:43], v195 offset:60416\n\t"
        "s_waitcnt lgkmcnt(14)\n\t"
        "v_mfma_f32_32x32x16_bf16 v[16:31], v[44:47], v[164:167], v[16:31]\n\t"
        "ds_read_b64_tr_b16 v[44:45], v196 offset:59392\n\t"
        "ds_read_b64_tr_b16 v[46:47], v196 offset:60416\n\t"
        "s_waitcnt vmcnt(5) lgkmcnt(14)\n\t"
        "v_mfma_f32_32x32x16_bf16 v[0:15], v[48:51], v[168:171], v[0:15]\n\t"
        "ds_read_b64_tr_b16 v[48:49], v195 offset:61440\n\t"
        "ds_read_b64_tr_b16 v[50:51], v195 offset:62464\n\t"
        "s_waitcnt lgkmcnt(14)\n\t"
        "v_mfma_f32_32x32x16_bf16 v[16:31], v[52:55], v[168:171], v[16:31]\n\t"
        "ds_read_b64_tr_b16 v[52:53], v196 offset:61440\n\t"
        "ds_read_b64_tr_b16 v[54:55], v196 offset:62464\n\t"
        "s_waitcnt vmcnt(4) lgkmcnt(14)\n\t"
        "v_mfma_f32_32x32x16_bf16 v[0:15], v[56:59], v[172:175], v[0:15]\n\t"
        "ds_read_b64_tr_b16 v[56:57], v195 offset:63488\n\t"
        "ds_read_b64_tr_b16 v[58:59], v195 offset:64512\n\t"
        "s_waitcnt lgkmcnt(14)\n\t"
        "v_mfma_f32_32x32x16_bf16 v[16:31], v[60:63], v[172:175], v[16:31]\n\t"
        "ds_read_b64_tr_b16 v[60:61], v196 offset:63488\n\t"
        "ds_read_b64_tr_b16 v[62:63], v196 offset:64512\n\t"
        "s_waitcnt vmcnt(3) lgkmcnt(14)\n\t"
        "v_mfma_f32_32x32x16_bf16 v[0:15], v[32:35], v[176:179], v[0:15]\n\t"
        "s_waitcnt lgkmcnt(12)\n\t"
        "v_mfma_f32_32x32x16_bf16 v[16:31], v[36:39], v[176:179], v[16:31]\n\t"
        "s_waitcnt vmcnt(2) lgkmcnt(10)\n\t"
        "v_mfma_f32_32x32x16_bf16 v[0:15], v[40:43], v[180:183], v[0:15]\n\t"
        "s_waitcnt lgkmcnt(8)\n\t"
        "v_mfma_f32_32x32x16_bf16 v[16:31], v[44:47], v[180:183], v[16:31]\n\t"
        "s_waitcnt vmcnt(1) lgkmcnt(6)\n\t"
        "v_mfma_f32_32x32x16_bf16 v[0:15], v[48:51], v[184:187], v[0:15]\n\t"
        "s_waitcnt lgkmcnt(4)\n\t"
        "v_mfma_f32_32x32x16_bf16 v[16:31], v[52:55], v[184:187], v[16:31]\n\t"
        "s_waitcnt vmcnt(0) lgkmcnt(2)\n\t"
        "v_mfma_f32_32x32x16_bf16 v[0:15], v[56:59], v[188:191], v[0:15]\n\t"
        "s_waitcnt lgkmcnt(0)\n\t"
        "v_mfma_f32_32x32x16_bf16 v[16:31], v[60:63], v[188:191], v[16:31]\n\t"
        "s_nop 11\n\t"
        : "+{v[0:15]}"(acc[0]), "+{v[16:31]}"(acc[1]), "+{s44}"(soff) STAMP_OUTS
        : "{v192}"(OFF_OF + L * (5 * MBY) + tile * TS + lane * 16), "{v193}"(a0), "{v194}"(a1), "{v195}"(a0 + 65536), "{v196}"(a1 + 65536),
          "{v198}"(lbase + L_W + 20), "{v199}"(L + 1), "{s[40:43]}"(srd)
        : "v32", "v33", "v34", "v35", "v36", "v37", "v38", "v39", "v40", "v41", "v42", "v43", "v44", "v45", "v46", "v47", "v48", "v49", "v50", "v51", "v52", "v53", "v54", "v55", "v56", "v57", "v58", "v59", "v60", "v61", "v62", "v63", "v64", "v65", "v66", "v67", "v68", "v69", "v70", "v71", "v72", "v73", "v74", "v75", "v76", "v77", "v78", "v79", "v80", "v81", "v82", "v83", "v84", "v85", "v86", "v87", "v88", "v89", "v90", "v91", "v92", "v93", "v94", "v95", "v96", "v97", "v98", "v99", "v100", "v101", "v102", "v103", "v104", "v105", "v106", "v107", "v108", "v109", "v110", "v111", "v112", "v113", "v114", "v115", "v116", "v117", "v118", "v119", "v120", "v121", "v122", "v123", "v124", "v125", "v126", "v127", "v128", "v129", "v130", "v131", "v132", "v133", "v134", "v135", "v136", "v137", "v138", "v139", "v140", "v141", "v142", "v143", "v144", "v145", "v146", "v147", "v148", "v149", "v150", "v151", "v152", "v153", "v154", "v155", "v156", "v157", "v158", "v159", "v160", "v161", "v162", "v163", "v164", "v165", "v166", "v167", "v168", "v169", "v170", "v171", "v172", "v173", "v174", "v175", "v176", "v177", "v178", "v179", "v180", "v181", "v182", "v183", "v184", "v185", "v186", "v187", "v188", "v189", "v190", "v191", "v200", "s45", "vcc", "scc", "memory");
      }
      PHASE_VARS;
      STAMP_PUT(12 + 16 * L);
      STAMPV(15 + 16 * L);
      float s = 0.f, sq = 0.f, d = 0.f;
      const unsigned xdst = (L ? OOB : OFF_X1 + tile * TS + tn * 4096) + lane * 16;
#pragma unroll
      for (int i = 0; i < 4; ++i) {
#pragma unroll
        for (int e = 0; e < 4; ++e) x1p[i][e] = pack2(acc[i >> 1][8 * (i & 1) + 2 * e], acc[i >> 1][8 * (i & 1) + 2 * e + 1]);
        stg(rs, xdst + i * 1024, x1p[i]);
        SBAR();
      }
#pragma unroll
      for (int g = 0; g < 8; ++g)
#pragma unroll
        for (int e = 0; e < 2; ++e) {
          const unsigned o = x1p[g >> 1][2 * (g & 1) + e];
          s = dot2(o, 0x3f803f80u, s); sq = dot2(o, o, sq); d = dot2(o, wrc[g][e], d);
        }
      const u32x4 pst = {nzf(half_swap_sum(s)), nzf(half_swap_sum(sq)), nzf(half_swap_sum(d)), 0u};
      asm volatile("buffer_store_dwordx4 %0, %1, %2, 0 offen sc1\n\ts_waitcnt vmcnt(1)\n\tds_write_b32 %3, %4"
                   :: "v"(pst), "v"(stats_dst(sx, tile, tn, lane)), "s"(srd), "v"(lbase + L_W + 64 + 4 * wid), "v"(3) : "memory");
    }
    STAMPV(16 + 16 * L);
  }
  {
    PHASE_VARS;
    float S, Q, Dd, mean, var, mb1, rd1;
    __builtin_amdgcn_s_sleep(13);
    row_stats_sum(rs, OFF_SX + SX_LAYER, tile, lane, S, Q, Dd);
    STAMPV(33);
    row_stats_fin(smem, 2, tile, lane, S, Q, Dd, U2F(words[0]), x1p, mean, var, mb1, rd1);
    STAMPV(34);
    u32x4 xo[4];
#pragma unroll
    for (int i = 0; i < 4; ++i) {
#pragma unroll
      for (int e = 0; e < 4; ++e) {
        const unsigned h2 = ln_pair(x1p[i][e], mb1, rd1);
        xo[i][e] = pack2(lo16(x1p[i][e]) + lo16(h2), hi16(x1p[i][e]) + hi16(h2));
      }
      *reinterpret_cast<u32x4*>(smem + wid * 4096 + r32 * 128 + ((2 * i + hi) ^ (r32 & 7)) * 16) = fo_swap(xo[i]);
    }
    asm volatile("s_waitcnt lgkmcnt(0)" ::: "memory");
#pragma unroll
    for (int i = 0; i < 4; ++i) { xo[i] = *reinterpret_cast<const u32x4*>(smem + wid * 4096 + i * 1024 + (lane >> 3) * 128 + ((lane ^ (lane >> 3)) & 7) * 16); PIN(xo[i]); }
    u16* dst = out + (size_t)(tile * 32 + (lane >> 3)) * D_ + tn * 64 + (lane & 7) * 8;
    lds_wait_ge(lbase + L_W + 4 + 4 * (lane & 1), 2);
    const int rel = (int)words[3] - tile * 32 - (lane >> 3);
#pragma unroll
    for (int i = 0; i < 4; ++i)
      if (rel != 8 * i) st16(dst + (size_t)i * 8 * D_, xo[i]);
    STAMPV(35);
    STAMP_END;
  }
}

extern "C" __global__ void end_marker() {}

extern "C" void kernel_launch(void* const* d_in, const int* in_sizes, int n_in, void* d_out, int out_size,
                              void* d_ws, size_t ws_size, hipStream_t stream) {
  char* ws = (char*)d_ws;
  mega_kernel<<<256, 512, 0, stream>>>((const u16*)d_in[0], (const u16*)d_in[1], (const u16*)d_in[2], (const u16*)d_in[4], (const u16*)d_in[5],
                                       (const u16*)d_in[6], (const u16*)d_in[7], ws, (u16*)d_out);
}
```

```cpp
#include <hip/hip_runtime.h>
#include <hip/hip_bf16.h>
#include <stdint.h>

typedef unsigned short u16;
typedef short s16x4 __attribute__((ext_vector_type(4)));
typedef short s16x8 __attribute__((ext_vector_type(8)));
typedef float f32x16 __attribute__((ext_vector_type(16)));
typedef float f32x8 __attribute__((ext_vector_type(8)));
typedef float f32x4 __attribute__((ext_vector_type(4)));
typedef float f32x2 __attribute__((ext_vector_type(2)));
typedef unsigned int u32x16 __attribute__((ext_vector_type(16)));
typedef unsigned int u32x8 __attribute__((ext_vector_type(8)));
typedef unsigned int u32x4 __attribute__((ext_vector_type(4)));
typedef unsigned int u32x2 __attribute__((ext_vector_type(2)));
typedef __bf16 bf16x2 __attribute__((ext_vector_type(2)));
typedef __bf16 bf16x8 __attribute__((ext_vector_type(8)));
typedef __bf16 bf16x16 __attribute__((ext_vector_type(16)));

#define DI __device__ __forceinline__
#define LDS_PTR(T, p) ((__attribute__((address_space(3))) T*)(p))
#define BARRIER() asm volatile("s_barrier" ::: "memory")
#define BAR_LGKM() asm volatile("s_waitcnt lgkmcnt(0)\n\ts_barrier" ::: "memory")
#define BAR_ALL() asm volatile("s_waitcnt vmcnt(0) lgkmcnt(0)\n\ts_barrier" ::: "memory")
#define DRAIN() asm volatile("s_waitcnt vmcnt(0)" ::: "memory")
#define PACE() ({ asm volatile("s_sleep 1\n\ts_waitcnt vmcnt(3)" ::: "memory"); })
#define MFMA32(a, b, c) __builtin_amdgcn_mfma_f32_32x32x16_bf16(a, b, c, 0, 0, 0)
#define MFMA16(a, b, c) __builtin_amdgcn_mfma_f32_16x16x32_bf16(a, b, c, 0, 0, 0)
#define PIN(x) asm volatile("" : "+v"(x))
#define EX2(x) __builtin_amdgcn_exp2f(x)
#define RCP(x) __builtin_amdgcn_rcpf(x)
#define SBAR() __builtin_amdgcn_sched_barrier(0)

#define STAMP_INIT
#define STAMPV(i)
#define STAMP_END
#define STAMP_DECL
#define STAMP_OUTS
#define STAMP_PUT(i)
constexpr int D_ = 1024, F_ = 3584;

constexpr unsigned MBY = 1u << 20;
constexpr unsigned TS = 65536 + 4096, HS = 262144 + 4096;
constexpr unsigned OFF_HF = 0, OFF_X1 = 5 * MBY;
constexpr unsigned OFF_OF = 10 * MBY;
constexpr unsigned OFF_KV = 20 * MBY;
constexpr unsigned OFF_FL = 40 * MBY;
constexpr unsigned FL_EPOCH = OFF_FL;
constexpr unsigned FL_KV = OFF_FL + 4096;
constexpr unsigned FL_O = FL_KV + 65536;
constexpr unsigned FL_X = FL_O + 65536;
constexpr unsigned OFF_CS = 41 * MBY;
constexpr unsigned OFF_SX = 42 * MBY;
constexpr unsigned SX_ST = 0;
constexpr unsigned SX_ROW = 512 * 1024;
constexpr unsigned SX_CAND = SX_ROW + 64 * 1024;
constexpr unsigned SX_GP = SX_CAND + 32 * 1024;
constexpr unsigned SX_YP = SX_GP + 224 * 1024;
constexpr unsigned SX_LAYER = SX_YP + 224 * 1024;
constexpr unsigned SX_QP = 2 * SX_LAYER;
constexpr unsigned SX_BYTES = SX_QP + 512 * 1024;
static_assert(SX_BYTES <= 6 * 256 * 128 * 16, "sentinel fill");
constexpr unsigned WS_BYTES = 48 * MBY, OOB = 0x80000000u;
constexpr unsigned SENT = 0xFFFFFFFFu;

constexpr int NS = 9, DD = 3, SLOTB = 12288;
constexpr int L_XCH = 122880;
constexpr int L_SL = L_XCH + 8192;
constexpr int L_CAND = L_XCH + 32768;
constexpr int L_AB = L_XCH + 34816;
constexpr int L_FULL = 159744, L_FREE = L_FULL + 512;
constexpr int L_CS = L_FREE + 1024, L_PATCH = L_CS + 768;
constexpr int L_GU = L_PATCH + 768, L_TAG = L_GU + 256, L_WR = L_TAG + 256;
constexpr int L_X2F = L_WR + 128, L_X2P = L_X2F + 128;
constexpr int L_W = L_X2P + 128;
constexpr int LDS_BYTES = 163840;
static_assert(NS * SLOTB <= 54 * 2048 && L_W + 128 <= LDS_BYTES && (NS + 1) * SLOTB <= L_XCH && L_AB + 2048 <= L_FULL, "LDS map");

DI int fresh_lane() { int l; asm volatile("v_mbcnt_lo_u32_b32 %0, -1, 0\n\tv_mbcnt_hi_u32_b32 %0, -1, %0" : "=v"(l)); return l; }
#define LANE_VARS const int lane = fresh_lane(), r32 = lane & 31, hi = lane >> 5, tq = (lane & 15) >> 2, tp = lane & 3, tblk = (lane >> 4) & 1, \
  wrow = 4 * hi + tq, wsw = 4 * ((wrow >> 1) & 1), wch = 2 * tblk + (tp >> 1), c24 = r32 < 24 ? r32 : 23; \
  (void)r32; (void)hi; (void)wsw; (void)wch; (void)c24
DI int fresh_s(int v) { asm volatile("" : "+s"(v)); return v; }
#define PHASE_VARS const int tm = fresh_s(tm_), tn = fresh_s(tn_), wid = fresh_s(wid_), tile = tm * 4 + wid, hh = wid & 1, kh = wid >> 1; (void)tile; (void)hh; (void)kh; LANE_VARS
DI float bf2f(u16 v) { return __uint_as_float(((unsigned)v) << 16); }
DI unsigned pack2(float lo, float hi) { f32x2 v = {lo, hi}; return __builtin_bit_cast(unsigned, __builtin_convertvector(v, bf16x2)); }
DI float rbf(float f) { return __uint_as_float(pack2(f, 0.f) << 16); }
DI float lo16(unsigned u) { return __uint_as_float(u << 16); }
DI float hi16(unsigned u) { return __uint_as_float(u & 0xffff0000u); }
DI float opq(float r) { asm("" : "+v"(r)); return r; }
DI float fadd(float a, float b) { return opq(a + b); }
DI float fmul(float a, float b) { return opq(a * b); }
DI float ffma(float a, float b, float c) { return opq(__builtin_fmaf(a, b, c)); }
DI float U2F(unsigned u) { return __uint_as_float(u); }
DI float half_swap_sum(float v) {
  auto r = __builtin_amdgcn_permlane32_swap(__float_as_uint(v), __float_as_uint(v), false, false);
  return fadd(U2F(r[0]), U2F(r[1]));
}
DI float half_swap_max(float v) {
  auto r = __builtin_amdgcn_permlane32_swap(__float_as_uint(v), __float_as_uint(v), false, false);
  return fmaxf(__uint_as_float(r[0]), __uint_as_float(r[1]));
}
template <int CTRL> DI unsigned dpp(unsigned v) { return (unsigned)__builtin_amdgcn_update_dpp(0, (int)v, CTRL, 0xf, 0xf, false); }
DI float row_swap_sum(float v) {
  auto r = __builtin_amdgcn_permlane16_swap(__float_as_uint(v), __float_as_uint(v), false, false);
  return fadd(U2F(r[0]), U2F(r[1]));
}
DI float half_sum(float v) {
  v = fadd(v, U2F(dpp<0xB1>(__float_as_uint(v)))); v = fadd(v, U2F(dpp<0x4E>(__float_as_uint(v))));
  v = fadd(v, U2F(dpp<0x141>(__float_as_uint(v)))); v = fadd(v, U2F(dpp<0x140>(__float_as_uint(v))));
  return row_swap_sum(v);
}
DI float wave_sum(float v) { return half_swap_sum(half_sum(v)); }
DI unsigned half_max_u32(unsigned v) {
  v = max(v, dpp<0xB1>(v)); v = max(v, dpp<0x4E>(v)); v = max(v, dpp<0x141>(v)); v = max(v, dpp<0x140>(v));
  auto r = __builtin_amdgcn_permlane16_swap(v, v, false, false);
  return max(r[0], r[1]);
}
DI unsigned wave_max_u32(unsigned v) {
  v = half_max_u32(v);
  auto r = __builtin_amdgcn_permlane32_swap(v, v, false, false);
  return max(r[0], r[1]);
}
DI u32x4 fo_swap(u32x4 v) {
  auto a = __builtin_amdgcn_permlane32_swap(v[0], v[2], false, false);
  auto b = __builtin_amdgcn_permlane32_swap(v[1], v[3], false, false);
  u32x4 o; o[0] = a[0]; o[1] = b[0]; o[2] = a[1]; o[3] = b[1];
  return o;
}
DI void glds16(const void* gsrc, unsigned lds_dst) {
  unsigned keep;
  asm volatile("s_mov_b32 %0, m0\n\ts_mov_b32 m0, %2\n\ts_nop 0\n\tglobal_load_lds_dwordx4 %1, off\n\ts_mov_b32 m0, %0"
               : "=&s"(keep) : "v"(gsrc), "s"(lds_dst) : "memory");
}
#define GL_ "global_load_lds_dwordx4 "
DI void glds_v4(const void* sb, const unsigned (&v)[4], unsigned m0v) {
  unsigned keep;
  asm volatile("s_mov_b32 %0, m0\n\ts_mov_b32 m0, %6\n\ts_nop 0\n\t" GL_ "%1, %5\n\t" GL_ "%2, %5 offset:1024\n\t" GL_ "%3, %5 offset:2048\n\t"
               GL_ "%4, %5 offset:3072\n\ts_mov_b32 m0, %0" : "=&s"(keep) : "v"(v[0]), "v"(v[1]), "v"(v[2]), "v"(v[3]), "s"(sb), "s"(m0v) : "memory");
}
#define GP_ "\n\ts_sleep %8\n\ts_waitcnt vmcnt(%7)\n\t"
template <int TH, int SL> DI void glds_v4t(const void* sb, const unsigned (&v)[4], unsigned m0v) {
  unsigned keep;
  asm volatile("s_mov_b32 %0, m0\n\ts_mov_b32 m0, %6\n\ts_nop 0\n\t" GL_ "%1, %5" GP_ GL_ "%2, %5 offset:1024" GP_ GL_ "%3, %5 offset:2048" GP_ GL_ "%4, %5 offset:3072" GP_ "s_mov_b32 m0, %0"
               : "=&s"(keep) : "v"(v[0]), "v"(v[1]), "v"(v[2]), "v"(v[3]), "s"(sb), "s"(m0v), "n"(TH), "n"(SL) : "memory");
}
DI void glds_v3(const void* sb, const unsigned (&v)[3], unsigned m0v) {
  unsigned keep;
  asm volatile("s_mov_b32 %0, m0\n\ts_mov_b32 m0, %5\n\ts_nop 0\n\t" GL_ "%1, %4\n\t" GL_ "%2, %4 offset:1024\n\t" GL_ "%3, %4 offset:2048\n\t"
               "s_mov_b32 m0, %0" : "=&s"(keep) : "v"(v[0]), "v"(v[1]), "v"(v[2]), "s"(sb), "s"(m0v) : "memory");
}
DI float dot2(unsigned a, unsigned b, float c) {
  return __builtin_amdgcn_fdot2_f32_bf16(__builtin_bit_cast(bf16x2, a), __builtin_bit_cast(bf16x2, b), c, false);
}
#define CODE_ADDR(sym, out) do { unsigned long long pc_; unsigned off_; \
  asm volatile("s_getpc_b64 %0\n1:\n\ts_mov_b32 %1, " #sym "-1b" : "=s"(pc_), "=s"(off_)); out = (const char*)(pc_ + (long long)(int)off_); } while (0)
DI void st16(void* p, u32x4 v) { asm volatile("global_store_dwordx4 %0, %1, off sc0 sc1\n\ts_nop 1" :: "v"(p), "v"(v) : "memory"); }
DI void ln_scalars(float mean, float var, float& mb, float& den) { mb = rbf(mean); den = rbf(__builtin_amdgcn_sqrtf(rbf(rbf(var) + 1e-5f))); }
DI unsigned ln_pair(unsigned v, float mb, float rd) {
  const unsigned c = pack2(lo16(v) - mb, hi16(v) - mb);
  return pack2(lo16(c) * rd, hi16(c) * rd);
}
DI unsigned ln_pair_s(unsigned v, float nmb, float rd) {
  const unsigned c = pack2(fadd(lo16(v), nmb), fadd(hi16(v), nmb));
  return pack2(fmul(lo16(c), rd), fmul(hi16(c), rd));
}
template <int N, int M> DI f32x4 sum4(const u32x4 (&v)[M]) {
  f32x4 t;
#pragma unroll
  for (int e = 0; e < 4; ++e) {
    t[e] = U2F(v[0][e]);
#pragma unroll
    for (int i = 1; i < N; ++i) t[e] = fadd(t[e], U2F(v[i][e]));
  }
  return t;
}
#define PERM_LO(x, y) __builtin_amdgcn_perm(y, x, 0x05040100u)
#define PERM_HI(x, y) __builtin_amdgcn_perm(y, x, 0x07060302u)
constexpr unsigned SPIN_CAP = 1u << 15;
DI unsigned nz(unsigned v) { return min(v, SENT - 1u); }
DI unsigned nzf(float v) { return nz(__float_as_uint(v)); }
DI unsigned mx4(unsigned m, const u32x4& v) { return max(max(max(m, v[0]), v[1]), max(v[2], v[3])); }
template <int N> DI bool none_sent(const u32x4 (&v)[N]) {
  unsigned m = 0u;
#pragma unroll
  for (int i = 0; i < N; ++i) m = mx4(m, v[i]);
  return m != SENT;
}
typedef __amdgpu_buffer_rsrc_t rsrc_t;
DI u32x4 ldg(rsrc_t rs, unsigned off) { return __builtin_amdgcn_raw_buffer_load_b128(rs, (int)off, 0, 16); }
DI void stg(rsrc_t rs, unsigned off, u32x4 v) { __builtin_amdgcn_raw_buffer_store_b128(v, rs, (int)off, 0, 16); }
DI void poll_pause(bool slow) { if (slow) __builtin_amdgcn_s_sleep(4); asm volatile("s_sleep 1" ::: "memory"); }
template <int N, class LD, class OKF> DI void poll_all(u32x4 (&out)[N], bool slow, LD ld, OKF okf) {
  for (unsigned spins = 0;; ++spins) {
    ld(out);
    SBAR();
    if (__all(okf(out)) || spins > SPIN_CAP) break;
    poll_pause(slow);
  }
}
DI void lds_set(unsigned addr, unsigned v) { asm volatile("s_waitcnt lgkmcnt(0)\n\tds_write_b32 %0, %1" :: "v"(addr), "v"(v) : "memory"); }
template <int SLEEP = 1> DI void lds_wait_ge(unsigned addr, unsigned need) {
  unsigned spin = 0x100000, v;
  asm volatile("1:\n\tds_read_b32 %0, %2\n\ts_sub_u32 %1, %1, 1\n\ts_waitcnt lgkmcnt(0)\n\ts_cbranch_scc1 2f\n\tv_cmp_gt_u32 vcc, %3, %0\n\ts_cbranch_vccz 2f\n\ts_sleep %4\n\ts_branch 1b\n2:"
               : "=&v"(v), "+s"(spin) : "v"(addr), "s"(need), "n"(SLEEP) : "vcc", "scc", "memory");
}
DI s16x8 tr2(const char* p, int second) {
  s16x4 lo = __builtin_amdgcn_ds_read_tr16_b64_v4i16(LDS_PTR(s16x4, p)), hi = __builtin_amdgcn_ds_read_tr16_b64_v4i16(LDS_PTR(s16x4, p + second));
  return __builtin_shufflevector(lo, hi, 0, 1, 2, 3, 4, 5, 6, 7);
}
DI u32x16 cat4(const u32x4& a, const u32x4& b, const u32x4& c, const u32x4& d) {
  const u32x8 x = __builtin_shufflevector(a, b, 0, 1, 2, 3, 4, 5, 6, 7), y = __builtin_shufflevector(c, d, 0, 1, 2, 3, 4, 5, 6, 7);
  return __builtin_shufflevector(x, y, 0, 1, 2, 3, 4, 5, 6, 7, 8, 9, 10, 11, 12, 13, 14, 15);
}

DI unsigned stats_dst(unsigned sx, int tile, int tn, int lane) { return lane < 32 ? sx + SX_ST + tile * 8192 + tn * 512 + lane * 16 : OOB; }
DI void row_stats_sum(rsrc_t rs, unsigned sx, int tile, int lane, float& S, float& Q, float& Dd) {
  const unsigned exb = sx + SX_ST + tile * 8192 + (lane & 31) * 16;
  u32x4 pv[8];
  poll_all<8>(pv, false, [&](u32x4 (&v)[8]) {
#pragma unroll
    for (int i = 0; i < 8; ++i) v[i] = ldg(rs, exb + ((lane >> 5) * 8 + i) * 512);
  }, [&](const u32x4 (&v)[8]) { return none_sent(v); });
  S = 0.f; Q = 0.f; Dd = 0.f;
#pragma unroll
  for (int i = 0; i < 8; ++i) { S += __uint_as_float(pv[i][0]); Q += __uint_as_float(pv[i][1]); Dd += __uint_as_float(pv[i][2]); }
}
DI void row_stats_fin(char* smem, int epoch, int tile, int lane, float S, float Q, float Dd, float wsum, const u32x4 (&x1p)[4],
                      float& mean, float& var, float& mb, float& rd) {
  S = half_swap_sum(S); Q = half_swap_sum(Q); Dd = half_swap_sum(Dd);
  mean = S * (1.0f / 1024.0f); var = fmaxf(Q * (1.0f / 1024.0f) - mean * mean, 0.f);
  float den;
  ln_scalars(mean, var, mb, den);
  rd = RCP(den);
  const unsigned rb = pack2((Dd - mb * wsum) * rd, 0.f) & 0xffffu;
  const unsigned mine = (((rb & 0x8000u) ? (~rb & 0xffffu) : (rb | 0x8000u)) << 16) | (unsigned)(2047 - (tile * 32 + (lane & 31)));
  const bool best = half_max_u32(mine) == mine;
  if (best) {
    char* c = smem + L_CAND + (tile & 3) * 144;
#pragma unroll
    for (int i = 0; i < 4; ++i) *reinterpret_cast<u32x4*>(c + (2 * i + (lane >> 5)) * 16) = x1p[i];
    *reinterpret_cast<u32x4*>(c + 128) = u32x4{mine, __float_as_uint(mb), __float_as_uint(rd), 0u};
  }
  lds_set((unsigned)(uintptr_t)smem + L_W + 32 + (tile & 3) * 4, epoch);
}

DI void attn_safe(rsrc_t rs, unsigned kvoff, const u32x8& q, const u32x4& ones, f32x16& o) {
  const s16x8 q0 = __builtin_bit_cast(s16x8, __builtin_shufflevector(q, q, 0, 1, 2, 3)), q1 = __builtin_bit_cast(s16x8, __builtin_shufflevector(q, q, 4, 5, 6, 7));
  f32x4 l = {0.f, 0.f, 0.f, 0.f};
#pragma unroll
  for (int e = 0; e < 16; ++e) o[e] = 0.f;
  float m = -INFINITY;
#pragma unroll 1
  for (int kt = 0; kt < 64; ++kt) {
    const unsigned p = kvoff + kt * 4096;
    const u32x4 k0 = ldg(rs, p), k1 = ldg(rs, p + 1024), v0 = ldg(rs, p + 2048), v1 = ldg(rs, p + 3072);
    f32x16 s = {};
    s = MFMA32(__builtin_bit_cast(s16x8, k0), q0, s);
    s = MFMA32(__builtin_bit_cast(s16x8, k1), q1, s);
    float mt = fmaxf(s[0], s[1]);
#pragma unroll
    for (int e = 2; e < 16; ++e) mt = fmaxf(mt, s[e]);
    const float mn = fmaxf(m, half_swap_max(mt));
    const float alpha = EX2(m - mn);
    m = mn;
#pragma unroll
    for (int e = 0; e < 16; ++e) { s[e] = EX2(s[e] - mn); o[e] *= alpha; }
    l[0] *= alpha;
    const bf16x16 pa = __builtin_bit_cast(bf16x16, __builtin_shufflevector(s, s, 0, 1, 2, 3, 4, 5, 6, 7));
    const bf16x16 pb = __builtin_bit_cast(bf16x16, __builtin_shufflevector(s, s, 8, 9, 10, 11, 12, 13, 14, 15));
    o = __builtin_amdgcn_smfmac_f32_32x32x32_bf16(__builtin_bit_cast(bf16x8, v0), pa, o, 0xdddd, 0, 0);
    l = __builtin_amdgcn_smfmac_f32_16x16x64_bf16(__builtin_bit_cast(bf16x8, ones), pa, l, 0xdddd, 0, 0);
    o = __builtin_amdgcn_smfmac_f32_32x32x32_bf16(__builtin_bit_cast(bf16x8, v1), pb, o, 0xdddd, 0, 0);
    l = __builtin_amdgcn_smfmac_f32_16x16x64_bf16(__builtin_bit_cast(bf16x8, ones), pb, l, 0xdddd, 0, 0);
  }
  o *= 1.0f / l[0];
}

extern "C" __global__ __launch_bounds__(512) void mega_kernel(const u16* __restrict__ x0, const u16* __restrict__ wqkv, const u16* __restrict__ wo,
                                                              const u16* __restrict__ w_r, const u16* __restrict__ wg, const u16* __restrict__ wu,
                                                              const u16* __restrict__ wd, char* ws, u16* __restrict__ out) {
  __shared__ __attribute__((aligned(16))) char smem[LDS_BYTES];
  const int wid = __builtin_amdgcn_readfirstlane(threadIdx.x >> 6);
  STAMP_INIT;
  STAMPV(0);
  const int bid = blockIdx.x, xcd = bid & 7, loc = bid >> 3;
  const int tm = 8 * (xcd >> 2) + (loc >> 2), tn = 4 * (xcd & 3) + (loc & 3);
  const unsigned lbase = (unsigned)(uintptr_t)smem;
  const rsrc_t rs = __builtin_amdgcn_make_buffer_rsrc(ws, 0, (int)WS_BYTES, 0x00020000);
  volatile __attribute__((address_space(3))) unsigned* words = LDS_PTR(volatile unsigned, smem + L_W);
  *reinterpret_cast<unsigned*>(smem + L_FULL + threadIdx.x * 4) = 0u;
  words[threadIdx.x & 31] = 0u;
  BAR_LGKM();
  const int lw = wid & 1;
  const char* wb = reinterpret_cast<const char*>(wqkv + (size_t)(16 * lw) * (3 * D_) + tn * 64);
  auto stage_lanes = [](unsigned (&wv)[3]) {
    const int lane = fresh_lane();
#pragma unroll
    for (int i = 0; i < 3; ++i) {
      const int li = lane + 64 * i, k = li / 24, cp = li % 24, c = cp ^ (4 * ((k >> 1) & 1));
      wv[i] = (unsigned)(k * (3 * D_ * 2) + (c >> 3) * (D_ * 2) + (c & 7) * 16 - i * 1024);
    }
  };
  auto stage = [&](const unsigned (&wv)[3], int s, int slot) {
    const char* p = wb + (size_t)((s + 2 * tn) & 31) * (32 * 3 * D_ * 2);
    glds_v3(p, wv, lbase + slot * SLOTB + lw * 6144);
    glds_v3(p + 8 * (3 * D_ * 2), wv, lbase + slot * SLOTB + lw * 6144 + 3072);
  };
  auto load_tag = [&]() {
    u32x2 t = __builtin_amdgcn_raw_buffer_load_b64(rs, FL_EPOCH, 0, 16);
    t[0] += 1u; t[1] += 0x9E3779B9u;
    PIN(t);
    return t;
  };
  auto exchange = [&](const u32x2& tag, unsigned line0, int me, bool bar = false) {
    const int lane = fresh_lane();
    __builtin_amdgcn_raw_buffer_store_b64(tag, rs, lane == 0 ? line0 + me * 128 : OOB, 0, 16);
    if (bar) BARRIER();
    if (1) {
      unsigned po = lane < 16 ? line0 + lane * 128 : OOB;
      const u32x4 srd = {(unsigned)(uintptr_t)ws, (unsigned)((uintptr_t)ws >> 32) & 0xffffu, WS_BYTES, 0x00020000u};
      for (unsigned spins = 0;; ++spins) {
        u32x2 v = {0u, 0u};
        asm volatile("buffer_atomic_or_x2 %0, %1, %2, 0 offen sc0 sc1\n\ts_waitcnt vmcnt(0)" : "+v"(v) : "v"(po), "s"(srd) : "memory");
        po = ((v[0] == tag[0]) & (v[1] == tag[1])) ? OOB : po;
        if (__all(po == OOB) || spins > SPIN_CAP) break;
      }
      return;
    }
    __builtin_amdgcn_s_sleep(8);
    for (unsigned spins = 0;; ++spins) {
      const u32x2 v = __builtin_amdgcn_raw_buffer_load_b64(rs, line0 + (lane & 15) * 128, 0, 16);
      if (__all((v[0] == tag[0]) & (v[1] == tag[1])) || spins > SPIN_CAP) break;
      asm volatile("s_sleep 1" ::: "memory");
    }
  };
  if (wid >= 4) {
    auto wo_rows = [&](int g0, int n, int gs) {
      const int lane = fresh_lane();
      unsigned wov[4];
#pragma unroll
      for (int i = 0; i < 4; ++i) wov[i] = (unsigned)((8 * i + (lane >> 3)) * (D_ * 2) + ((lane & 7) ^ (4 * ((lane >> 4) & 1))) * 16 - i * 1024);
#pragma unroll 1
      for (int i = 0, k = (2 * (tm & 7)) % n, g; i < n && !0; ++i, k = k + 1 == n ? 0 : k + 1) g = g0 + gs * k, glds_v4t<3, 1>(reinterpret_cast<const char*>(wo) + (size_t)((64 * tn + 32 * g) & 1023) * (D_ * 2) + tn * 128, wov, lbase + g * 4096);
    };
    if (wid < 6) {
      const int lane = fresh_lane();
      unsigned wv[3];
      stage_lanes(wv);
      __builtin_amdgcn_s_setprio(3);
      const unsigned fra = lbase + L_FREE + (lane & 3) * 256, fla = lbase + L_FULL + lw * 256 + lane * 4;
      const rsrc_t rw = __builtin_amdgcn_make_buffer_rsrc(const_cast<char*>(wb), 0, 0 ? 0 : 6 * MBY - (16 * lw * 3 * D_ + tn * 64) * 2, 0x00020000);
#pragma unroll 1
      for (int L = 0; L < 2; ++L) {
        const int s0 = NS, lead = L ? 8 : NS - 1;
        int slot = 0;
        if (L == 0) {
#pragma unroll 1
          for (int i = 0, s = tm & 7; i < 2 * NS; ++i) {
            glds_v3(wb + (size_t)((s + 2 * tn) & 31) * (32 * 3 * D_ * 2) + (i & 1) * (8 * 3 * D_ * 2), wv, lbase + s * SLOTB + lw * 6144 + (i & 1) * 3072);
            asm volatile("s_waitcnt vmcnt(6)" ::: "memory");
            if (i & 1) s = s + 1 == NS ? 0 : s + 1;
          }
          asm volatile("s_waitcnt vmcnt(0)\n\tds_write_b32 %0, %1" :: "v"(fla), "v"(NS) : "memory");
          STAMPV(1);
        }
#pragma unroll 1
        for (int s = NS; s < 32; ++s) {
          lds_wait_ge(fra, 32 * L + s - lead);
          stage(wv, s, slot);
          slot = (slot == NS - 1) ? 0 : slot + 1;
          if (s >= s0 + DD) asm volatile("s_waitcnt vmcnt(%0)\n\tds_write_b32 %1, %2" :: "n"(DD * 6), "v"(fla), "v"(32 * L + s - DD + 1) : "memory");
        }
#pragma unroll
        for (int d = DD - 1; d >= 0; --d) asm volatile("s_waitcnt vmcnt(%0)\n\tds_write_b32 %1, %2" :: "n"(d * 6), "v"(fla), "v"(32 * L + 32 - d) : "memory");
        u32x2 tag = *reinterpret_cast<const u32x2*>(smem + L_TAG);
        PIN(tag);
        if (lw == 0) {
          lds_wait_ge(lbase + L_W + 96 + 4 * (lane & 1), 1);
          lds_wait_ge<0>(lbase + L_W + 80 + 4 * (lane & 3), L + 1);
          exchange(tag, FL_KV + (L * 16 + tn) * 2048, tm, true);
          lds_wait_ge(fra, 32 * L + 32);
          lds_set(lbase + L_W + 16, L + 1);
        } else { BAR_LGKM(); lds_wait_ge<8>(lbase + L_W + 16, L + 1); }
        __builtin_amdgcn_s_sleep(24);
        wo_rows(lw, 15, 2);
        u32x4 pre[NS * 6];
        if (L == 0) {
#pragma unroll
          for (int s = 0; s < NS; ++s) {
#pragma unroll
            for (int i = 0; i < 6; ++i)
              pre[s * 6 + i] = __builtin_amdgcn_raw_buffer_load_b128(rw, wv[i % 3], ((s + 2 * tn) & 31) * (32 * 3 * D_ * 2) + (i / 3) * (8 * 3 * D_ * 2) + (i % 3) * 1024, 0), PACE();
          }
        } else {
#pragma unroll
          for (int i = 0; i < NS * 6; ++i) asm volatile("" : "=v"(pre[i]));
        }
        BAR_ALL();
        if (lw == 0) {
          exchange(tag, FL_O + (L * 16 + tm) * 2048, tn); lds_set(lbase + L_W + 20, L + 1);
          __builtin_amdgcn_raw_buffer_store_b64(tag, rs, (bid == 0 && lane == 0) ? FL_EPOCH : OOB, 0, 16);
        }
        else wo_rows(30, 2, 1);
        BAR_ALL();
        if (L == 0) {
          char* d = smem + lw * 6144 + fresh_lane() * 16;
#pragma unroll
          for (int s = 0; s < NS; ++s) {
#pragma unroll
            for (int i = 0; i < 6; ++i) *reinterpret_cast<u32x4*>(d + s * SLOTB + i * 1024) = pre[s * 6 + i];
            lds_set(fla, 32 + s + 1);
          }
          STAMPV(2);
          lds_wait_ge<2>(lbase + L_W + 24, 2);
        }
      }
      STAMP_END;
      return;
    }
    const int a = wid - 6;
    const bool ffn = loc < 28;
    const int grp = xcd * 7 + (ffn ? loc >> 2 : 0), qa = (loc & 3) * 2 + a;
    const int kr = loc, cg = xcd;
    const unsigned wa = lbase + L_W;
    if (a) {
      const int lane = fresh_lane();
      unsigned t = __builtin_amdgcn_raw_buffer_load_b32(rs, 1 ? lane * MBY + MBY - 128 : OOB, 0, 16);
      if (1) {
        const char *c0, *c1;
        CODE_ADDR(mega_kernel, c0); CODE_ADDR(end_marker, c1);
        const long long o = (loc * 64 + lane) * 32;
        t |= *reinterpret_cast<const __attribute__((address_space(1))) unsigned*>((uintptr_t)(c0 + (o + 4 <= c1 - c0 ? o : 0)));
      }
      const u32x2 tag = load_tag();
      *reinterpret_cast<u32x2*>(smem + L_TAG) = tag;
      PIN(t);
      lds_wait_ge<0>(wa + 64 + 4 * (lane & 3), 2);
      exchange(tag, FL_X + tm * 2048, tn);
      lds_set(wa + 24, 1);
    } else {
      unsigned wv[3];
      stage_lanes(wv);
#pragma unroll 1
      for (int i = 0; i < 4 * 0; ++i) {
        glds_v3(reinterpret_cast<const char*>(wqkv + tn * 64) + (size_t)((4 * (NS + 2 * tn + 3 * (tm & 7)) + i) & 127) * (8 * 3 * D_ * 2), wv, lbase + NS * SLOTB);
        asm volatile("s_waitcnt vmcnt(6)" ::: "memory");
      }
      STAMPV(11);
      lds_wait_ge<2>(wa + 24, 1);
    }
    __builtin_amdgcn_s_sleep(16);
    auto transpose8 = [](u32x4* v) {
      u32x4 t[8];
#pragma unroll
      for (int j = 0; j < 8; ++j)
#pragma unroll
        for (int e = 0; e < 4; ++e) t[j][e] = (j & 1) ? PERM_HI(v[2 * e][j >> 1], v[2 * e + 1][j >> 1]) : PERM_LO(v[2 * e][j >> 1], v[2 * e + 1][j >> 1]);
#pragma unroll
      for (int j = 0; j < 8; ++j) { v[j] = t[j]; PIN(v[j]); }
    };
    u32x4 gf[32];
    u32x4 wdf[16];
    auto wait_go = [&](int L) {
      lds_wait_ge<8>(lbase + L_W + 16, L + 1);
      __builtin_amdgcn_s_sleep(24);
    };
    auto load_wd = [&](unsigned dv) {
      const rsrc_t rd = __builtin_amdgcn_make_buffer_rsrc(const_cast<u16*>(wd) + (size_t)grp * 64 * D_ + 128 * qa, 0, 0 ? 0 : 64 * D_ * 2 - 256 * qa, 0x00020000);
#pragma unroll
      for (int i = 0; i < 16; ++i) {
        wdf[i] = __builtin_amdgcn_raw_buffer_load_b128(rd, dv, (32 * (i >> 3) + (i & 7)) * (D_ * 2), 0);
        asm volatile("s_sleep 2\n\ts_waitcnt vmcnt(3)" ::: "memory");
      }
    };
    {
      LANE_VARS;
      const u32x4 sv = {SENT, SENT, SENT, SENT};
#pragma unroll
      for (int i = 0; i < 6; ++i) {
        const unsigned o = ((i * 256 + bid) * 128 + a * 64 + lane) * 16;
        stg(rs, o < SX_BYTES ? OFF_SX + o : OOB, sv);
      }
      if (a == 0) {
        const u32x4 w0 = *reinterpret_cast<const u32x4*>(w_r + lane * 8), w1 = *reinterpret_cast<const u32x4*>(w_r + 512 + lane * 8);
        float t = 0.f;
#pragma unroll
        for (int e = 0; e < 4; ++e) t = fadd(fadd(t, fadd(lo16(w0[e]), hi16(w0[e]))), fadd(lo16(w1[e]), hi16(w1[e])));
        words[0] = __float_as_uint(wave_sum(t));
        if ((lane >> 3) == (tn & 7)) *reinterpret_cast<u32x4*>(smem + L_WR + (lane & 7) * 16) = tn < 8 ? w0 : w1;
      }
      const u16* p = wqkv + (size_t)(64 * tm + 32 * a + hi) * (3 * D_) + (c24 >> 3) * D_ + tn * 64 + (c24 & 7) * 8;
      float cs[8] = {0.f, 0.f, 0.f, 0.f, 0.f, 0.f, 0.f, 0.f};
      u32x4 v[16];
#pragma unroll
      for (int i = 0; i < 16; ++i) v[i] = *reinterpret_cast<const u32x4*>(p + (size_t)i * (2 * 3 * D_));
      SBAR();
#pragma unroll
      for (int i = 0; i < 16; ++i)
#pragma unroll
        for (int e = 0; e < 4; ++e) { cs[2 * e] = fadd(cs[2 * e], lo16(v[i][e])); cs[2 * e + 1] = fadd(cs[2 * e + 1], hi16(v[i][e])); }
      u32x4 o;
#pragma unroll
      for (int e = 0; e < 4; ++e) { const float x = half_swap_sum(cs[e]), y = half_swap_sum(cs[4 + e]); o[e] = __float_as_uint(hi ? y : x); }
      stg(rs, r32 < 24 ? OFF_CS + (tn * 32 + tm * 2 + a) * 768 + r32 * 32 + hi * 16 : OOB, o);
    }
    {
      DRAIN();
      lds_set(wa + 96 + 4 * a, 1);
      BAR_LGKM();
      LANE_VARS;
      const char* gp = reinterpret_cast<const char*>(((lane & 8) ? wu : wg) + (size_t)(128 * qa + 16 * hi + 4 * tblk) * F_ + 64 * grp + (lane & 7) * 8);
      unsigned dv = (lane >> 4) * (8 * D_ * 2) + (lane & 15) * 16;
      PIN(gp); PIN(dv);
      unsigned sv[3];
#pragma unroll
      for (int i = 0; i < 3; ++i) { const int li = lane + 64 * i, k = li / 24, c = li % 24; sv[i] = (unsigned)(k * (3 * D_ * 2) + (c >> 3) * (D_ * 2) + (c & 7) * 16 - i * 1024); PIN(sv[i]); }
      wait_go(0);
#pragma unroll
      for (int i = 0; i < 32; ++i) {
        gf[i] = *reinterpret_cast<const u32x4*>(gp + (size_t)(0 ? 0 : 32 * (i >> 3) + 8 * ((i >> 2) & 1) + (i & 3)) * (F_ * 2));
        asm volatile("s_sleep 2\n\ts_waitcnt vmcnt(3)" ::: "memory");
      }
      load_wd(dv);
      STAMPV(7);
      BARRIER();
      __builtin_amdgcn_s_sleep(2);
#pragma unroll
      for (int g = 0; g < 4; ++g) glds_v3(wqkv + (size_t)(32 * kr + 8 * g) * (3 * D_) + (2 * cg + a) * 64, sv, lbase + L_SL + a * 12288 + g * 3072);
      transpose8(gf); transpose8(gf + 8); transpose8(gf + 16); transpose8(gf + 24);
      transpose8(wdf); transpose8(wdf + 8);
      {
        LANE_VARS;
        f32x4 t[2];
#pragma unroll
        for (int c = 0; c < 2; ++c) {
          u32x4 v[8];
#pragma unroll
          for (int i = 0; i < 8; ++i) v[i] = ldg(rs, OFF_CS + (tn * 32 + 16 * c + 2 * i + hi) * 768 + a * 384 + c24 * 16);
          SBAR();
          t[c] = sum4<8>(v);
          SBAR();
        }
#pragma unroll
        for (int e = 0; e < 4; ++e) t[0][e] = half_swap_sum(fadd(t[0][e], t[1][e]));
        if (lane < 24) *reinterpret_cast<f32x4*>(smem + L_CS + a * 384 + lane * 16) = t[0];
      }
      u32x2 tag = *reinterpret_cast<const u32x2*>(smem + L_TAG);
      PIN(tag);
      BAR_ALL();
      if (a) {
        lds_wait_ge<0>(wa + 64 + 4 * (lane & 3), 3);
        exchange(tag, FL_X + (16 + tm) * 2048, tn);
        lds_set(wa + 24, 2);
        STAMPV(12);
      }
    }
#pragma unroll 1
    for (int L = fresh_s(0);; ++L) {
      const unsigned sx = OFF_SX + L * SX_LAYER;
      STAMPV(1 + 16 * L);
      if (0 && L == 0) { words[3] = 5000u; lds_set(wa + 4 + 4 * a, 1); } else {
      u32x4 hf[5];
      unsigned key;
      { LANE_VARS;
      {
        unsigned cdst = lane < 16 ? sx + SX_ROW + (lane >> 3) * 32768 + tm * 2048 + tn * 128 + (lane & 7) * 16 : sx + SX_CAND + (tn * 16 + tm) * 128;
        cdst = (a == 0 && lane < 17) ? cdst : OOB;
        PIN(cdst);
        lds_wait_ge<0>(wa + 32 + 4 * tp, L + 1);
        key = *reinterpret_cast<const unsigned*>(smem + L_CAND + tp * 144 + 128);
        key = max(key, dpp<0xB1>(key)); key = max(key, dpp<0x4E>(key));
        const char* cp = smem + L_CAND + (((2047 - (int)(key & 2047u)) >> 5) & 3) * 144;
        const u32x4 c = *reinterpret_cast<const u32x4*>(cp + (lane < 16 ? lane & 7 : 8) * 16), rec = *reinterpret_cast<const u32x4*>(cp + 128);
        u32x4 o;
#pragma unroll
        for (int e = 0; e < 4; ++e) { unsigned h = ln_pair_s(c[e], -U2F(rec[1]), U2F(rec[2])); PIN(h); o[e] = nz(lane < 8 ? h : c[e]); }
        stg(rs, cdst, o);
      }
      STAMPV(8 + 16 * L);
      __builtin_amdgcn_s_sleep(13);
      poll_all<5>(hf, L == 0, [&](u32x4 (&t)[5]) {
        t[4][0] = __builtin_amdgcn_raw_buffer_load_b32(rs, sx + SX_CAND + (tn * 16 + (lane & 15)) * 128, 0, 16);
#pragma unroll
        for (int i = 0; i < 4; ++i) t[i] = ldg(rs, sx + SX_ROW + ((2047 - (int)(key & 2047u)) >> 7) * 2048 + (16 * qa + 4 * i + (lane >> 4)) * 16);
        t[4][1] = t[4][2] = t[4][3] = key;
      }, [&](const u32x4 (&t)[5]) {
        unsigned k = t[4][0] == SENT ? 0u : t[4][0];
        k = max(k, dpp<0xB1>(k)); k = max(k, dpp<0x4E>(k)); k = max(k, dpp<0x141>(k)); k = max(k, dpp<0x140>(k));
        key = max(key, k);
        return none_sent(t) & (key == t[4][1]);
      });
      key = __builtin_amdgcn_readfirstlane(key);
      words[3] = 2047u - (key & 2047u);
      if (L == 1) lds_set(wa + 4 + 4 * a, 2);
      }
      STAMPV(2 + 16 * L);
      f32x4 d[8];
      const f32x4 zero = {0.f, 0.f, 0.f, 0.f};
      SBAR();
      __builtin_amdgcn_s_setprio(3);
#pragma unroll
      for (int t = 0; t < 32; ++t) d[t & 7] = MFMA16(__builtin_bit_cast(s16x8, hf[t >> 3]), __builtin_bit_cast(s16x8, gf[t]), t < 8 ? zero : d[t & 7]);
      SBAR();
      __builtin_amdgcn_s_setprio(0);
      LANE_VARS;
      const int idx = 2047 - (int)(key & 2047u), win = idx >> 7;
      const bool owner = win == tm;
      auto pick2 = [&](const f32x4 (&d)[8]) {
        u32x2 o;
#pragma unroll
        for (int e = 0; e < 2; ++e) {
          const float x = tblk ? d[2 + e][0] : d[e][0], y = tblk ? d[6 + e][0] : d[4 + e][0];
          o[e] = nzf(hi ? y : x);
        }
        return o;
      };
      if (ffn) {
        const unsigned ppos = (lane & 15) * 32 + (lane >> 4) * 8;
        __builtin_amdgcn_raw_buffer_store_b64(pick2(d), rs, sx + SX_GP + (grp * 8 + qa) * 512 + ppos, 0, 16);
        STAMPV(9 + 16 * L);
        __builtin_amdgcn_s_sleep(13);
        if (L == 0) __builtin_amdgcn_s_sleep(0);
        u32x2 gu[8];
        for (unsigned spins = 0;; ++spins) {
#pragma unroll
          for (int i = 0; i < 8; ++i) gu[i] = __builtin_amdgcn_raw_buffer_load_b64(rs, sx + SX_GP + (grp * 8 + i) * 512 + lane * 8, 0, 16);
          SBAR();
          unsigned m = 0u;
#pragma unroll
          for (int i = 0; i < 8; ++i) m = max(max(m, gu[i][0]), gu[i][1]);
          if (__all(m != SENT) || spins > SPIN_CAP) break;
          poll_pause(L == 0);
        }
        STAMPV(10 + 16 * L);
        float t0 = opq(U2F(gu[0][0])), t1 = opq(U2F(gu[0][1]));
#pragma unroll
        for (int i = 1; i < 8; ++i) { t0 = fadd(t0, U2F(gu[i][0])); t1 = fadd(t1, U2F(gu[i][1])); }
        auto sw = __builtin_amdgcn_permlane32_swap(__float_as_uint(t0), __float_as_uint(t1), false, false);
        const float gv = rbf(U2F(sw[0])), uv = rbf(U2F(sw[1]));
        reinterpret_cast<u16*>(smem + L_GU + a * 128)[2 * r32 + hi] = (u16)pack2(fmul(rbf(fmul(gv, RCP(fadd(1.0f, __expf(-gv))))), uv), 0.f);
        asm volatile("s_waitcnt lgkmcnt(0)" ::: "memory");
        const s16x8 a0 = *reinterpret_cast<const s16x8*>(smem + L_GU + a * 128 + (lane >> 4) * 16), a1 = *reinterpret_cast<const s16x8*>(smem + L_GU + a * 128 + 64 + (lane >> 4) * 16);
        SBAR();
        __builtin_amdgcn_s_setprio(3);
#pragma unroll
        for (int t = 0; t < 16; ++t) d[t & 7] = MFMA16(t < 8 ? a0 : a1, __builtin_bit_cast(s16x8, wdf[t]), t < 8 ? zero : d[t & 7]);
        SBAR();
        __builtin_amdgcn_s_setprio(0);
        __builtin_amdgcn_raw_buffer_store_b64(pick2(d), rs, sx + SX_YP + grp * 4096 + qa * 512 + ppos, 0, 16);
      }
      STAMPV(3 + 16 * L);
      if (L == 1 && (cg != (kr & 7) || a)) { STAMP_END; __builtin_amdgcn_endpgm(); }
      s16x8 wpf[12];
#pragma unroll
      for (int t = 0; t < 12; ++t) wpf[t] = tr2(smem + L_SL + a * 12288 + (16 * hi + 4 * tblk + tq) * 384 + t * 32 + tp * 8, 8 * 384);
#pragma unroll
      for (int t = 0; t < 12; ++t) PIN(wpf[t]);
      if (L == 0 && !owner) lds_set(wa + 4 + 4 * a, 1);
      __builtin_amdgcn_s_sleep(13 - 4);
      if (L == 0) __builtin_amdgcn_s_sleep(0);
      if (L == 0 && !ffn) __builtin_amdgcn_s_sleep(64);
      const unsigned rk = key >> 16;
      float rsig = rbf(RCP(fadd(1.0f, __expf(-lo16((rk & 0x8000u) ? (rk & 0x7fffu) : (~rk & 0xffffu))))));
      PIN(rsig);
      const int n = 4 * (lane & 7) + ((lane >> 3) & 3);
      const int fo = ((n >> 4) * 2 + ((n >> 2) & 1)) * 8 + ((n >> 3) & 1) * 4 + (n & 3);
      u32x4 pv[8];
      poll_all<8>(pv, L == 0, [&](u32x4 (&t)[8]) {
#pragma unroll
        for (int i = 0; i < 7; ++i) t[i] = ldg(rs, sx + SX_YP + (8 * i + (lane >> 3)) * 4096 + kr * 128 + (lane & 7) * 16);
        t[7][0] = t[7][2] = __builtin_amdgcn_raw_buffer_load_b32(rs, sx + SX_ROW + win * 2048 + kr * 64 + (fo >> 1) * 4, 0, 16);
        t[7][1] = t[7][3] = __builtin_amdgcn_raw_buffer_load_b32(rs, sx + SX_ROW + 32768 + win * 2048 + kr * 64 + (fo >> 1) * 4, 0, 16);
      }, [&](const u32x4 (&t)[8]) { return none_sent(t); });
      const float hv = (fo & 1) ? hi16(pv[7][0]) : lo16(pv[7][0]), xv = (fo & 1) ? hi16(pv[7][1]) : lo16(pv[7][1]);
      STAMPV(4 + 16 * L);
      float ys;
      {
        f32x4 t = sum4<7>(pv);
#pragma unroll
        for (int e = 0; e < 4; ++e) {
          t[e] = fadd(t[e], U2F(dpp<0x128>(__float_as_uint(t[e]))));
          t[e] = half_swap_sum(row_swap_sum(t[e]));
        }
        const float x = (lane & 8) ? t[1] : t[0], y = (lane & 8) ? t[3] : t[2];
        ys = tblk ? y : x;
      }
      const unsigned ob = pack2(fadd(xv, rbf(fadd(hv, rbf(fmul(rbf(ys), rsig))))), 0.f) & 0xffffu;
      if (L == 1) {
        if (lane < 32) out[(size_t)idx * D_ + kr * 32 + n] = (u16)ob;
        STAMPV(5 + 16 * L);
        STAMP_END;
        __builtin_amdgcn_endpgm();
      }
      reinterpret_cast<u16*>(smem + L_X2F + a * 64)[fo] = (u16)ob;
      asm volatile("s_waitcnt lgkmcnt(0)" ::: "memory");
      {
        const s16x8 xa = *reinterpret_cast<const s16x8*>(smem + L_X2F + a * 64 + (lane >> 4) * 16);
        const u32x4 xf = *reinterpret_cast<const u32x4*>(smem + L_X2F + a * 64 + tp * 16);
        f32x4 z[12];
        SBAR();
        __builtin_amdgcn_s_setprio(3);
#pragma unroll
        for (int t = 0; t < 12; ++t) z[t] = MFMA16(xa, wpf[t], zero);
        SBAR();
        __builtin_amdgcn_s_setprio(0);
        const float x2 = lo16(ob), s1 = half_sum(x2), s2 = half_sum(fmul(x2, x2));
        u32x4 o;
#pragma unroll
        for (int e = 0; e < 4; ++e) {
          const unsigned sp = lane < 52 ? (e == 0 ? __float_as_uint(s1) : __float_as_uint(s2)) : xf[e];
          const unsigned x = __float_as_uint(tblk ? z[4 + e][0] : z[e][0]), y = tblk ? sp : __float_as_uint(z[8 + e][0]);
          o[e] = nz(hi ? y : x);
        }
        const unsigned pos = lane < 24 ? lane * 16 : lane < 48 ? lane * 16 + 128 : lane < 52 ? (lane & 1) * 512 + 384 : 400 + tp * 16;
        stg(rs, lane < 56 ? OFF_SX + SX_QP + ((2 * cg + a) * 32 + kr) * 1024 + pos : OOB, o);
      }
      STAMPV(5 + 16 * L);
      if (owner) {
        __builtin_amdgcn_s_sleep(13);
        const int c25 = r32 < 25 ? r32 : 24, pl = 24 * a + c24, col = 64 * (pl >> 4) + (pl & 15);
        float cs[4];
#pragma unroll
        for (int e = 0; e < 4; ++e) { cs[e] = reinterpret_cast<const float*>(smem + L_CS)[col + 16 * e]; PIN(cs[e]); }
        u32x4 qv[17];
        poll_all<17>(qv, true, [&](u32x4 (&t)[17]) {
#pragma unroll
          for (int i = 0; i < 16; ++i) t[i] = ldg(rs, OFF_SX + SX_QP + (tn * 32 + 2 * i + hi) * 1024 + a * 512 + c25 * 16);
          t[16] = ldg(rs, OFF_SX + SX_QP + (tn * 34 + hi) * 1024 + 400 + tp * 16);
        }, [&](const u32x4 (&t)[17]) { return none_sent(t); });
        *reinterpret_cast<u32x4*>(smem + L_X2P + hi * 64 + tp * 16) = qv[16];
        f32x4 t = sum4<16>(qv);
#pragma unroll
        for (int e = 0; e < 4; ++e) t[e] = half_swap_sum(t[e]);
        const float mean = fmul(U2F(__builtin_amdgcn_readlane(__float_as_uint(t[0]), 24)), 1.0f / 1024.0f);
        const float var = fmaxf(ffma(-mean, mean, fmul(U2F(__builtin_amdgcn_readlane(__float_as_uint(t[1]), 24)), 1.0f / 1024.0f)), 0.f);
        float mb, den;
        ln_scalars(mean, var, mb, den);
        const float rd = RCP(den);
        if (lane < 24) {
#pragma unroll
          for (int e = 0; e < 4; ++e) reinterpret_cast<float*>(smem + L_PATCH)[col + 16 * e] = fmul(ffma(-mb, cs[e], t[e]), rd);
        }
        lds_set(wa + 4 + 4 * a, L + 1);
      }
      STAMPV(6 + 16 * L);
      }
      BAR_ALL();
      unsigned dv = fresh_lane();
      dv = (dv >> 4) * (8 * D_ * 2) + (dv & 15) * 16;
      PIN(dv);
      wait_go(1);
      load_wd(dv);
      STAMPV(23);
      BARRIER();
      transpose8(wdf); transpose8(wdf + 8);
      BARRIER();
    }
  }
  const int tm_ = tm, tn_ = tn, wid_ = wid;
  {
    const int tid = threadIdx.x, lane = tid & 63, r32 = lane & 31, row = 2 * wid + (lane >> 5);
    char* sm = smem + L_XCH;
    u32x4 v[4];
    asm volatile("global_load_dwordx4 %0, %4, off\n\tglobal_load_dwordx4 %1, %4, off offset:512\n\tglobal_load_dwordx4 %2, %4, off offset:1024\n\tglobal_load_dwordx4 %3, %4, off offset:1536"
                 : "=&v"(v[0]), "=&v"(v[1]), "=&v"(v[2]), "=&v"(v[3]) : "v"(x0 + (size_t)(tm * 128 + tn * 8 + row) * D_ + r32 * 8) : "memory");
    int pass = fresh_s(0);
#pragma unroll 1
    for (;;) {
      asm volatile("s_cmp_eq_u32 %4, 0\n\ts_cbranch_scc1 1f\n\ts_waitcnt vmcnt(0)\n1:" : "+v"(v[0]), "+v"(v[1]), "+v"(v[2]), "+v"(v[3]) : "s"(pass) : "memory", "scc");
      float s = 0.f, s2 = 0.f;
#pragma unroll
      for (int i = 0; i < 4; ++i)
#pragma unroll
        for (int e = 0; e < 4; e += 2) { s = dot2(v[i][e], 0x3f803f80u, s); s2 = dot2(v[i][e + 1], 0x3f803f80u, s2); }
      const float mean = half_sum(s + s2) * (1.0f / 1024.0f);
      f32x2 m2 = {0.f, 0.f};
#pragma unroll
      for (int i = 0; i < 4; ++i)
#pragma unroll
        for (int e = 0; e < 4; ++e) { const f32x2 d = f32x2{lo16(v[i][e]), hi16(v[i][e])} - mean; m2 += d * d; }
      float mb, den;
      ln_scalars(mean, half_sum(m2[0] + m2[1]) * (1.0f / 1024.0f), mb, den);
      const float rd = 1.0f / den;
#pragma unroll
      for (int i = 0; i < 4; ++i) {
        char* p = sm + ((16 * i + (r32 >> 1)) * 2) * 144 + row * 16 + (r32 & 1) * 8;
        u32x2 lo = {ln_pair(v[i][0], mb, rd), ln_pair(v[i][1], mb, rd)}, hi = {ln_pair(v[i][2], mb, rd), ln_pair(v[i][3], mb, rd)};
        *reinterpret_cast<u32x2*>(p) = lo;
        *reinterpret_cast<u32x2*>(p + 144) = hi;
      }
      STAMPV(37);
      lds_set(lbase + L_W + 64 + 4 * wid, pass);
      lds_wait_ge<0>(lbase + L_W + 64 + 4 * (lane & 3), pass);
      u32x4 t[4];
#pragma unroll
      for (int i = 0; i < 4; ++i) t[i] = *reinterpret_cast<const u32x4*>(sm + (i * 32 + (tid >> 3)) * 144 + (tid & 7) * 16);
      SBAR();
      unsigned dst = OFF_HF + (tm * 4 + (tn >> 2)) * TS + (tn & 3) * 128 + (tid >> 3) * 512 + (tid & 7) * 16;
      PIN(dst);
      if (pass) {
#pragma unroll
        for (int i = 0; i < 4; ++i) stg(rs, dst + i * 16384, t[i]);
        break;
      }
#pragma unroll
      for (int i = 0; i < 4; ++i) PIN(t[i]);
      pass = fresh_s(1);
    }
    DRAIN();
    lds_set(lbase + L_W + 64 + 4 * wid, 2);
    STAMPV(38);
  }
  const u32x4 srd = {(unsigned)(uintptr_t)ws, (unsigned)((uintptr_t)ws >> 32) & 0xffffu, WS_BYTES, 0x00020000u};
  u32x4 x1p[4];
  asm volatile("" : "=v"(x1p[0]), "=v"(x1p[1]), "=v"(x1p[2]), "=v"(x1p[3]));
#pragma unroll 1
  for (int L = 0; L < 2; ++L) {
    const unsigned sx = OFF_SX + L * SX_LAYER;
    u32x8 q[4];
    u32x16 kva, kvb;
    f32x16 oz[5];
    {
      f32x16 acc[6];
      u32x16 stA, stB, xr0, xr1, xr2, xr3;
      u32x4 stC;
      asm volatile("" : "={v[112:127]}"(stB), "={v[128:131]}"(stC));
      { PHASE_VARS;
      stB[8] = lane * 16 + (L ? OFF_X1 : OFF_HF) + tile * TS;
      stB[9] = lbase + wrow * 384 + (((4 * hh + wch) ^ wsw) << 4) + 8 * (tp & 1);
      stB[10] = lbase + wrow * 384 + (((4 * (hh ^ 1) + wch) ^ wsw) << 4) + 8 * (tp & 1);
      stB[13] = lbase + L_FULL + (lane & 1) * 256;
      stB[15] = lbase + L_FREE + wid * 256 + lane * 4;
      stC[0] = 32 * L; }
      unsigned soff, slot, need = 32 * L + 1;
      if (L == 0) {
        lds_wait_ge<0>(lbase + L_W + 24, 1);
        STAMPV(39);
        soff = tn * 4096;
        asm volatile(
          "buffer_load_dwordx4 v[132:135], v120, s[40:43], s44 offen offset:0 sc1\n\t"
          "buffer_load_dwordx4 v[136:139], v120, s[40:43], s44 offen offset:1024 sc1\n\t"
          "buffer_load_dwordx4 v[140:143], v120, s[40:43], s44 offen offset:2048 sc1\n\t"
          "buffer_load_dwordx4 v[144:147], v120, s[40:43], s44 offen offset:3072 sc1\n\t"
          "s_add_u32 s44, s44, 0x1000\n\t"
          "s_and_b32 s44, s44, 0xffff\n\t"
          "buffer_load_dwordx4 v[148:151], v120, s[40:43], s44 offen offset:0 sc1\n\t"
          "buffer_load_dwordx4 v[152:155], v120, s[40:43], s44 offen offset:1024 sc1\n\t"
          "buffer_load_dwordx4 v[156:159], v120, s[40:43], s44 offen offset:2048 sc1\n\t"
          "buffer_load_dwordx4 v[160:163], v120, s[40:43], s44 offen offset:3072 sc1\n\t"
          "s_add_u32 s44, s44, 0x1000\n\t"
          "s_and_b32 s44, s44, 0xffff\n\t"
          "buffer_load_dwordx4 v[164:167], v120, s[40:43], s44 offen offset:0 sc1\n\t"
          "buffer_load_dwordx4 v[168:171], v120, s[40:43], s44 offen offset:1024 sc1\n\t"
          "buffer_load_dwordx4 v[172:175], v120, s[40:43], s44 offen offset:2048 sc1\n\t"
          "buffer_load_dwordx4 v[176:179], v120, s[40:43], s44 offen offset:3072 sc1\n\t"
          "s_add_u32 s44, s44, 0x1000\n\t"
          "s_and_b32 s44, s44, 0xffff\n\t"
          "buffer_load_dwordx4 v[180:183], v120, s[40:43], s44 offen offset:0 sc1\n\t"
          "buffer_load_dwordx4 v[184:187], v120, s[40:43], s44 offen offset:1024 sc1\n\t"
          "buffer_load_dwordx4 v[188:191], v120, s[40:43], s44 offen offset:2048 sc1\n\t"
          "buffer_load_dwordx4 v[192:195], v120, s[40:43], s44 offen offset:3072 sc1\n\t"
          : "={v[132:147]}"(xr0), "={v[148:163]}"(xr1), "={v[164:179]}"(xr2), "={v[180:195]}"(xr3), "+{s44}"(soff) : "{v[112:127]}"(stB), "{s[40:43]}"(srd) : "scc", "memory");
      } else {
        xr0 = cat4(x1p[0], x1p[1], x1p[2], x1p[3]);
        asm volatile("" : "={v[148:163]}"(xr1), "={v[164:179]}"(xr2), "={v[180:195]}"(xr3));
      }
      STAMPV(1 + 16 * L);
      asm volatile(
        "v_mov_b32 v123, v121\n\t"
        "v_mov_b32 v124, v122\n\t"
        "s_mov_b64 vcc, -1\n\t"
        "s_cbranch_vccnz 10f\n\t"
        "11:\n\t"
        "s_mov_b32 s46, 12288\n\t"
        "s_add_u32 s47, s47, 1\n\t"
        "ds_read_b64_tr_b16 v[96:97], v123 offset:0\n\t"
        "ds_read_b64_tr_b16 v[98:99], v123 offset:3072\n\t"
        "ds_read_b64_tr_b16 v[100:101], v124 offset:0\n\t"
        "ds_read_b64_tr_b16 v[102:103], v124 offset:3072\n\t"
        "ds_read_b64_tr_b16 v[104:105], v123 offset:128\n\t"
        "ds_read_b64_tr_b16 v[106:107], v123 offset:3200\n\t"
        "ds_read_b64_tr_b16 v[108:109], v124 offset:128\n\t"
        "ds_read_b64_tr_b16 v[110:111], v124 offset:3200\n\t"
        "ds_read_b64_tr_b16 v[112:113], v123 offset:256\n\t"
        "ds_read_b64_tr_b16 v[114:115], v123 offset:3328\n\t"
        "ds_write_b32 v127, v128\n\t"
        "ds_read_b64_tr_b16 v[116:117], v124 offset:256\n\t"
        "ds_read_b64_tr_b16 v[118:119], v124 offset:3328\n\t"
        "s_waitcnt vmcnt(15) lgkmcnt(11)\n\t"
        "v_mfma_f32_32x32x16_bf16 v[0:15], v[96:99], v[132:135], 0\n\t"
        "ds_read_b32 v126, v125\n\t"
        "ds_read_b64_tr_b16 v[96:97], v123 offset:6144\n\t"
        "ds_read_b64_tr_b16 v[98:99], v123 offset:9216\n\t"
        "s_waitcnt lgkmcnt(12)\n\t"
        "v_mfma_f32_32x32x16_bf16 v[16:31], v[100:103], v[132:135], 0\n\t"
        "v_add_u32 v129, s46, v121\n\t"
        "ds_read_b64_tr_b16 v[100:101], v124 offset:6144\n\t"
        "ds_read_b64_tr_b16 v[102:103], v124 offset:9216\n\t"
        "s_waitcnt lgkmcnt(12)\n\t"
        "v_mfma_f32_32x32x16_bf16 v[32:47], v[104:107], v[132:135], 0\n\t"
        "ds_read_b64_tr_b16 v[104:105], v123 offset:6272\n\t"
        "ds_read_b64_tr_b16 v[106:107], v123 offset:9344\n\t"
        "s_waitcnt lgkmcnt(12)\n\t"
        "v_mfma_f32_32x32x16_bf16 v[48:63], v[108:111], v[132:135], 0\n\t"
        "v_add_u32 v130, s46, v122\n\t"
        "ds_read_b64_tr_b16 v[108:109], v124 offset:6272\n\t"
        "ds_read_b64_tr_b16 v[110:111], v124 offset:9344\n\t"
        "s_waitcnt lgkmcnt(12)\n\t"
        "v_mfma_f32_32x32x16_bf16 v[64:79], v[132:135], v[112:115], 0\n\t"
        "ds_read_b64_tr_b16 v[112:113], v123 offset:6400\n\t"
        "ds_read_b64_tr_b16 v[114:115], v123 offset:9472\n\t"
        "s_waitcnt lgkmcnt(10)\n\t"
        "v_mfma_f32_32x32x16_bf16 v[80:95], v[132:135], v[116:119], 0\n\t"
        "v_cmp_gt_u32 vcc, s47, v126\n\t"
        "ds_read_b64_tr_b16 v[116:117], v124 offset:6400\n\t"
        "ds_read_b64_tr_b16 v[118:119], v124 offset:9472\n\t"
        "s_waitcnt vmcnt(14) lgkmcnt(10)\n\t"
        "v_mfma_f32_32x32x16_bf16 v[0:15], v[96:99], v[136:139], v[0:15]\n\t"
        "s_cbranch_vccnz 12f\n\t"
        "13:\n\t"
        "ds_read_b64_tr_b16 v[96:97], v129 offset:0\n\t"
        "ds_read_b64_tr_b16 v[98:99], v129 offset:3072\n\t"
        "s_waitcnt lgkmcnt(10)\n\t"
        "v_mfma_f32_32x32x16_bf16 v[16:31], v[100:103], v[136:139], v[16:31]\n\t"
        "v_add_u32 v128, 1, v128\n\t"
        "ds_read_b64_tr_b16 v[100:101], v130 offset:0\n\t"
        "ds_read_b64_tr_b16 v[102:103], v130 offset:3072\n\t"
        "s_waitcnt lgkmcnt(10)\n\t"
        "v_mfma_f32_32x32x16_bf16 v[32:47], v[104:107], v[136:139], v[32:47]\n\t"
        "ds_read_b64_tr_b16 v[104:105], v129 offset:128\n\t"
        "ds_read_b64_tr_b16 v[106:107], v129 offset:3200\n\t"
        "s_waitcnt lgkmcnt(10)\n\t"
        "v_mfma_f32_32x32x16_bf16 v[48:63], v[108:111], v[136:139], v[48:63]\n\t"
        "s_add_u32 s46, s46, 12288\n\t"
        "s_add_u32 s47, s47, 1\n\t"
        "ds_read_b64_tr_b16 v[108:109], v130 offset:128\n\t"
        "ds_read_b64_tr_b16 v[110:111], v130 offset:3200\n\t"
        "s_waitcnt lgkmcnt(10)\n\t"
        "v_mfma_f32_32x32x16_bf16 v[64:79], v[136:139], v[112:115], v[64:79]\n\t"
        "s_cmp_eq_u32 s46, 110592\n\t"
        "s_cselect_b32 s46, 0, s46\n\t"
        "ds_read_b64_tr_b16 v[112:113], v129 offset:256\n\t"
        "ds_read_b64_tr_b16 v[114:115], v129 offset:3328\n\t"
        "s_waitcnt lgkmcnt(10)\n\t"
        "v_mfma_f32_32x32x16_bf16 v[80:95], v[136:139], v[116:119], v[80:95]\n\t"
        "ds_write_b32 v127, v128\n\t"
        "ds_read_b64_tr_b16 v[116:117], v130 offset:256\n\t"
        "ds_read_b64_tr_b16 v[118:119], v130 offset:3328\n\t"
        "s_waitcnt vmcnt(13) lgkmcnt(11)\n\t"
        "v_mfma_f32_32x32x16_bf16 v[0:15], v[96:99], v[140:143], v[0:15]\n\t"
        "ds_read_b32 v126, v125\n\t"
        "ds_read_b64_tr_b16 v[96:97], v129 offset:6144\n\t"
        "ds_read_b64_tr_b16 v[98:99], v129 offset:9216\n\t"
        "s_waitcnt lgkmcnt(12)\n\t"
        "v_mfma_f32_32x32x16_bf16 v[16:31], v[100:103], v[140:143], v[16:31]\n\t"
        "v_add_u32 v123, s46, v121\n\t"
        "ds_read_b64_tr_b16 v[100:101], v130 offset:6144\n\t"
        "ds_read_b64_tr_b16 v[102:103], v130 offset:9216\n\t"
        "s_waitcnt lgkmcnt(12)\n\t"
        "v_mfma_f32_32x32x16_bf16 v[32:47], v[104:107], v[140:143], v[32:47]\n\t"
        "ds_read_b64_tr_b16 v[104:105], v129 offset:6272\n\t"
        "ds_read_b64_tr_b16 v[106:107], v129 offset:9344\n\t"
        "s_waitcnt lgkmcnt(12)\n\t"
        "v_mfma_f32_32x32x16_bf16 v[48:63], v[108:111], v[140:143], v[48:63]\n\t"
        "v_add_u32 v124, s46, v122\n\t"
        "ds_read_b64_tr_b16 v[108:109], v130 offset:6272\n\t"
        "ds_read_b64_tr_b16 v[110:111], v130 offset:9344\n\t"
        "s_waitcnt lgkmcnt(12)\n\t"
        "v_mfma_f32_32x32x16_bf16 v[64:79], v[140:143], v[112:115], v[64:79]\n\t"
        "ds_read_b64_tr_b16 v[112:113], v129 offset:6400\n\t"
        "ds_read_b64_tr_b16 v[114:115], v129 offset:9472\n\t"
        "s_waitcnt lgkmcnt(10)\n\t"
        "v_mfma_f32_32x32x16_bf16 v[80:95], v[140:143], v[116:119], v[80:95]\n\t"
        "v_cmp_gt_u32 vcc, s47, v126\n\t"
        "ds_read_b64_tr_b16 v[116:117], v130 offset:6400\n\t"
        "ds_read_b64_tr_b16 v[118:119], v130 offset:9472\n\t"
        "s_waitcnt vmcnt(12) lgkmcnt(10)\n\t"
        "v_mfma_f32_32x32x16_bf16 v[0:15], v[96:99], v[144:147], v[0:15]\n\t"
        "s_cbranch_vccnz 14f\n\t"
        "15:\n\t"
        "ds_read_b64_tr_b16 v[96:97], v123 offset:0\n\t"
        "ds_read_b64_tr_b16 v[98:99], v123 offset:3072\n\t"
        "s_waitcnt lgkmcnt(10)\n\t"
        "v_mfma_f32_32x32x16_bf16 v[16:31], v[100:103], v[144:147], v[16:31]\n\t"
        "v_add_u32 v128, 1, v128\n\t"
        "ds_read_b64_tr_b16 v[100:101], v124 offset:0\n\t"
        "ds_read_b64_tr_b16 v[102:103], v124 offset:3072\n\t"
        "s_waitcnt lgkmcnt(10)\n\t"
        "v_mfma_f32_32x32x16_bf16 v[32:47], v[104:107], v[144:147], v[32:47]\n\t"
        "ds_read_b64_tr_b16 v[104:105], v123 offset:128\n\t"
        "ds_read_b64_tr_b16 v[106:107], v123 offset:3200\n\t"
        "s_waitcnt lgkmcnt(10)\n\t"
        "v_mfma_f32_32x32x16_bf16 v[48:63], v[108:111], v[144:147], v[48:63]\n\t"
        "s_add_u32 s46, s46, 12288\n\t"
        "s_add_u32 s47, s47, 1\n\t"
        "ds_read_b64_tr_b16 v[108:109], v124 offset:128\n\t"
        "ds_read_b64_tr_b16 v[110:111], v124 offset:3200\n\t"
        "s_waitcnt lgkmcnt(10)\n\t"
        "v_mfma_f32_32x32x16_bf16 v[64:79], v[144:147], v[112:115], v[64:79]\n\t"
        "s_cmp_eq_u32 s46, 110592\n\t"
        "s_cselect_b32 s46, 0, s46\n\t"
        "ds_read_b64_tr_b16 v[112:113], v123 offset:256\n\t"
        "ds_read_b64_tr_b16 v[114:115], v123 offset:3328\n\t"
        "s_waitcnt lgkmcnt(10)\n\t"
        "v_mfma_f32_32x32x16_bf16 v[80:95], v[144:147], v[116:119], v[80:95]\n\t"
        "ds_write_b32 v127, v128\n\t"
        "ds_read_b64_tr_b16 v[116:117], v124 offset:256\n\t"
        "ds_read_b64_tr_b16 v[118:119], v124 offset:3328\n\t"
        "s_waitcnt lgkmcnt(0)\n\t"
        "s_nop 7\n\t"
        "s_branch 9f\n\t"
        "10:\n\t"
        "s_mov_b32 s48, 0x10000\n\t"
        "8:\n\t"
        "ds_read_b32 v126, v125\n\t"
        "s_sub_u32 s48, s48, 1\n\t"
        "s_waitcnt lgkmcnt(0)\n\t"
        "s_cbranch_scc1 11b\n\t"
        "v_cmp_gt_u32 vcc, s47, v126\n\t"
        "s_cbranch_vccnz 8b\n\t"
        "s_branch 11b\n\t"
        "12:\n\t"
        "s_mov_b32 s48, 0x10000\n\t"
        "8:\n\t"
        "ds_read_b32 v126, v125\n\t"
        "s_sub_u32 s48, s48, 1\n\t"
        "s_waitcnt lgkmcnt(0)\n\t"
        "s_cbranch_scc1 13b\n\t"
        "v_cmp_gt_u32 vcc, s47, v126\n\t"
        "s_cbranch_vccnz 8b\n\t"
        "s_branch 13b\n\t"
        "14:\n\t"
        "s_mov_b32 s48, 0x10000\n\t"
        "8:\n\t"
        "ds_read_b32 v126, v125\n\t"
        "s_sub_u32 s48, s48, 1\n\t"
        "s_waitcnt lgkmcnt(0)\n\t"
        "s_cbranch_scc1 15b\n\t"
        "v_cmp_gt_u32 vcc, s47, v126\n\t"
        "s_cbranch_vccnz 8b\n\t"
        "s_branch 15b\n\t"
        "9:\n\t"
        : "={v[0:15]}"(acc[0]), "={v[16:31]}"(acc[1]), "={v[32:47]}"(acc[2]), "={v[48:63]}"(acc[3]), "={v[64:79]}"(acc[4]), "={v[80:95]}"(acc[5]),
          "={v[96:111]}"(stA), "+{v[112:127]}"(stB), "+{v[128:131]}"(stC), "={s46}"(slot), "+{s47}"(need)
        : "{v[132:147]}"(xr0) : "s48", "vcc", "scc", "memory");
      STAMPV(2 + 16 * L);
      float al = 1.f;
      if (L == 1) {
        PHASE_VARS;
        float S, Q, Dd;
        row_stats_sum(rs, OFF_SX, tile, lane, S, Q, Dd);
        STAMPV(36);
        lds_wait_ge<0>(lbase + L_W + 24, 2);
        soff = ((tn + 1) & 15) * 4096;
        asm volatile(
          "buffer_load_dwordx4 v[148:151], v120, s[40:43], s44 offen offset:0 sc1\n\t"
          "buffer_load_dwordx4 v[152:155], v120, s[40:43], s44 offen offset:1024 sc1\n\t"
          "buffer_load_dwordx4 v[156:159], v120, s[40:43], s44 offen offset:2048 sc1\n\t"
          "buffer_load_dwordx4 v[160:163], v120, s[40:43], s44 offen offset:3072 sc1\n\t"
          : "={v[148:163]}"(xr1), "={v[164:179]}"(xr2), "={v[180:195]}"(xr3), "+{s44}"(soff), "+v"(S), "+v"(Q), "+v"(Dd) : "{v[112:127]}"(stB), "{s[40:43]}"(srd) : "scc", "memory");
        float mean, var, mb1, rd1, mb2, den2;
        row_stats_fin(smem, 1, tile, lane, S, Q, Dd, U2F(words[0]), x1p, mean, var, mb1, rd1);
        ln_scalars(mean + (mean - mb1) * rd1, var * (1.0f + rd1) * (1.0f + rd1), mb2, den2);
        const float rd2 = RCP(den2);
        al = (1.0f + rd1) * rd2;
        const float be = -(mb1 * rd1 + mb2) * RCP(1.0f + rd1);
        reinterpret_cast<float*>(smem + L_AB)[wid * 64 + lane] = hi ? be : al;
        reinterpret_cast<float*>(smem + L_AB)[256 + wid * 64 + lane] = hi ? rd1 : mb1;
        soff = ((tn + 1 + 0) & 15) * 4096;
        asm volatile(
          "s_nop 0\n\t"
          "s_add_u32 s44, s44, 0x1000\n\t"
          "s_and_b32 s44, s44, 0xffff\n\t"
          "buffer_load_dwordx4 v[164:167], v120, s[40:43], s44 offen offset:0 sc1\n\t"
          "buffer_load_dwordx4 v[168:171], v120, s[40:43], s44 offen offset:1024 sc1\n\t"
          "buffer_load_dwordx4 v[172:175], v120, s[40:43], s44 offen offset:2048 sc1\n\t"
          "buffer_load_dwordx4 v[176:179], v120, s[40:43], s44 offen offset:3072 sc1\n\t"
          "s_add_u32 s44, s44, 0x1000\n\t"
          "s_and_b32 s44, s44, 0xffff\n\t"
          "buffer_load_dwordx4 v[180:183], v120, s[40:43], s44 offen offset:0 sc1\n\t"
          "buffer_load_dwordx4 v[184:187], v120, s[40:43], s44 offen offset:1024 sc1\n\t"
          "buffer_load_dwordx4 v[188:191], v120, s[40:43], s44 offen offset:2048 sc1\n\t"
          "buffer_load_dwordx4 v[192:195], v120, s[40:43], s44 offen offset:3072 sc1\n\t"
          : "+{v[148:163]}"(xr1), "+{v[164:179]}"(xr2), "+{v[180:195]}"(xr3), "+{s44}"(soff) : "{v[112:127]}"(stB), "{s[40:43]}"(srd) : "scc", "memory");
#pragma unroll
        for (int jj = 0; jj < 3; ++jj) {
          f32x4 cs[2][4];
          float cv[2];
#pragma unroll
          for (int j = 0; j < 2; ++j) {
            const int cb = jj * 64 + (j ^ hh) * 32;
            if (jj == 2) cv[j] = *reinterpret_cast<const float*>(smem + L_CS + (cb + r32) * 4);
#pragma unroll
            for (int g = 0; g < 4; ++g)
              if (jj < 2 || j == 0) cs[j][g] = *reinterpret_cast<const f32x4*>(smem + (jj < 2 ? L_CS + cb * 4 : L_AB + wid * 256 + 128) + (8 * g + 4 * hi) * 4);
          }
          SBAR();
#pragma unroll
          for (int j = 0; j < 2; ++j)
#pragma unroll
            for (int e = 0; e < 16; ++e) acc[2 * jj + j][e] += jj < 2 ? be * cs[j][e >> 2][e & 3] : cs[0][e >> 2][e & 3] * cv[j];
          SBAR();
        }
      }
      u32x4 ep;
      { PHASE_VARS;
        if (L == 0) reinterpret_cast<float*>(smem + L_AB)[wid * 64 + lane] = 1.0f;
        const unsigned dst = OFF_KV + L * (10 * MBY) + tn * 2 * HS + tile * 4096 + lane * 16;
        ep = u32x4{__float_as_uint(al), dst + hh * HS, dst + (hh ^ 1) * HS, lbase + L_AB + wid * 256 + hi * 16}; }
      STAMPV(3 + 16 * L);
      soff = ((tn + 1 + 2) & 15) * 4096;
      asm volatile(
        "s_nop 0\n\t"
        "s_add_u32 s44, s44, 0x1000\n\t"
        "s_and_b32 s44, s44, 0xffff\n\t"
        "buffer_load_dwordx4 v[132:135], v120, s[40:43], s44 offen offset:0 sc1\n\t"
        "buffer_load_dwordx4 v[136:139], v120, s[40:43], s44 offen offset:1024 sc1\n\t"
        "buffer_load_dwordx4 v[140:143], v120, s[40:43], s44 offen offset:2048 sc1\n\t"
        "s_mov_b32 s45, 3\n\t"
        "1:\n\t"
        "s_waitcnt vmcnt(14) lgkmcnt(11)\n\t"
        "v_mfma_f32_32x32x16_bf16 v[0:15], v[96:99], v[148:151], v[0:15]\n\t"
        "ds_read_b32 v126, v125\n\t"
        "ds_read_b64_tr_b16 v[96:97], v123 offset:6144\n\t"
        "ds_read_b64_tr_b16 v[98:99], v123 offset:9216\n\t"
        "s_waitcnt lgkmcnt(12)\n\t"
        "v_mfma_f32_32x32x16_bf16 v[16:31], v[100:103], v[148:151], v[16:31]\n\t"
        "v_add_u32 v129, s46, v121\n\t"
        "ds_read_b64_tr_b16 v[100:101], v124 offset:6144\n\t"
        "ds_read_b64_tr_b16 v[102:103], v124 offset:9216\n\t"
        "s_waitcnt lgkmcnt(12)\n\t"
        "v_mfma_f32_32x32x16_bf16 v[32:47], v[104:107], v[148:151], v[32:47]\n\t"
        "ds_read_b64_tr_b16 v[104:105], v123 offset:6272\n\t"
        "ds_read_b64_tr_b16 v[106:107], v123 offset:9344\n\t"
        "buffer_load_dwordx4 v[144:147], v120, s[40:43], s44 offen offset:3072 sc1\n\t"
        "s_waitcnt lgkmcnt(12)\n\t"
        "v_mfma_f32_32x32x16_bf16 v[48:63], v[108:111], v[148:151], v[48:63]\n\t"
        "v_add_u32 v130, s46, v122\n\t"
        "ds_read_b64_tr_b16 v[108:109], v124 offset:6272\n\t"
        "ds_read_b64_tr_b16 v[110:111], v124 offset:9344\n\t"
        "s_waitcnt lgkmcnt(12)\n\t"
        "v_mfma_f32_32x32x16_bf16 v[64:79], v[148:151], v[112:115], v[64:79]\n\t"
        "ds_read_b64_tr_b16 v[112:113], v123 offset:6400\n\t"
        "ds_read_b64_tr_b16 v[114:115], v123 offset:9472\n\t"
        "s_waitcnt lgkmcnt(10)\n\t"
        "v_mfma_f32_32x32x16_bf16 v[80:95], v[148:151], v[116:119], v[80:95]\n\t"
        "v_cmp_gt_u32 vcc, s47, v126\n\t"
        "ds_read_b64_tr_b16 v[116:117], v124 offset:6400\n\t"
        "ds_read_b64_tr_b16 v[118:119], v124 offset:9472\n\t"
        "s_waitcnt vmcnt(14) lgkmcnt(10)\n\t"
        "v_mfma_f32_32x32x16_bf16 v[0:15], v[96:99], v[152:155], v[0:15]\n\t"
        "s_cbranch_vccnz 16f\n\t"
        "17:\n\t"
        "ds_read_b64_tr_b16 v[96:97], v129 offset:0\n\t"
        "ds_read_b64_tr_b16 v[98:99], v129 offset:3072\n\t"
        "s_waitcnt lgkmcnt(10)\n\t"
        "v_mfma_f32_32x32x16_bf16 v[16:31], v[100:103], v[152:155], v[16:31]\n\t"
        "v_add_u32 v128, 1, v128\n\t"
        "ds_read_b64_tr_b16 v[100:101], v130 offset:0\n\t"
        "ds_read_b64_tr_b16 v[102:103], v130 offset:3072\n\t"
        "s_waitcnt lgkmcnt(10)\n\t"
        "v_mfma_f32_32x32x16_bf16 v[32:47], v[104:107], v[152:155], v[32:47]\n\t"
        "ds_read_b64_tr_b16 v[104:105], v129 offset:128\n\t"
        "ds_read_b64_tr_b16 v[106:107], v129 offset:3200\n\t"
        "s_add_u32 s44, s44, 0x1000\n\t"
        "s_and_b32 s44, s44, 0xffff\n\t"
        "buffer_load_dwordx4 v[148:151], v120, s[40:43], s44 offen offset:0 sc1\n\t"
        "s_waitcnt lgkmcnt(10)\n\t"
        "v_mfma_f32_32x32x16_bf16 v[48:63], v[108:111], v[152:155], v[48:63]\n\t"
        "s_add_u32 s46, s46, 12288\n\t"
        "s_add_u32 s47, s47, 1\n\t"
        "ds_read_b64_tr_b16 v[108:109], v130 offset:128\n\t"
        "ds_read_b64_tr_b16 v[110:111], v130 offset:3200\n\t"
        "s_waitcnt lgkmcnt(10)\n\t"
        "v_mfma_f32_32x32x16_bf16 v[64:79], v[152:155], v[112:115], v[64:79]\n\t"
        "s_cmp_eq_u32 s46, 110592\n\t"
        "s_cselect_b32 s46, 0, s46\n\t"
        "ds_read_b64_tr_b16 v[112:113], v129 offset:256\n\t"
        "ds_read_b64_tr_b16 v[114:115], v129 offset:3328\n\t"
        "s_waitcnt lgkmcnt(10)\n\t"
        "v_mfma_f32_32x32x16_bf16 v[80:95], v[152:155], v[116:119], v[80:95]\n\t"
        "ds_write_b32 v127, v128\n\t"
        "ds_read_b64_tr_b16 v[116:117], v130 offset:256\n\t"
        "ds_read_b64_tr_b16 v[118:119], v130 offset:3328\n\t"
        "s_waitcnt vmcnt(14) lgkmcnt(11)\n\t"
        "v_mfma_f32_32x32x16_bf16 v[0:15], v[96:99], v[156:159], v[0:15]\n\t"
        "ds_read_b32 v126, v125\n\t"
        "ds_read_b64_tr_b16 v[96:97], v129 offset:6144\n\t"
        "ds_read_b64_tr_b16 v[98:99], v129 offset:9216\n\t"
        "s_waitcnt lgkmcnt(12)\n\t"
        "v_mfma_f32_32x32x16_bf16 v[16:31], v[100:103], v[156:159], v[16:31]\n\t"
        "v_add_u32 v123, s46, v121\n\t"
        "ds_read_b64_tr_b16 v[100:101], v130 offset:6144\n\t"
        "ds_read_b64_tr_b16 v[102:103], v130 offset:9216\n\t"
        "s_waitcnt lgkmcnt(12)\n\t"
        "v_mfma_f32_32x32x16_bf16 v[32:47], v[104:107], v[156:159], v[32:47]\n\t"
        "ds_read_b64_tr_b16 v[104:105], v129 offset:6272\n\t"
        "ds_read_b64_tr_b16 v[106:107], v129 offset:9344\n\t"
        "buffer_load_dwordx4 v[152:155], v120, s[40:43], s44 offen offset:1024 sc1\n\t"
        "s_waitcnt lgkmcnt(12)\n\t"
        "v_mfma_f32_32x32x16_bf16 v[48:63], v[108:111], v[156:159], v[48:63]\n\t"
        "v_add_u32 v124, s46, v122\n\t"
        "ds_read_b64_tr_b16 v[108:109], v130 offset:6272\n\t"
        "ds_read_b64_tr_b16 v[110:111], v130 offset:9344\n\t"
        "s_waitcnt lgkmcnt(12)\n\t"
        "v_mfma_f32_32x32x16_bf16 v[64:79], v[156:159], v[112:115], v[64:79]\n\t"
        "ds_read_b64_tr_b16 v[112:113], v129 offset:6400\n\t"
        "ds_read_b64_tr_b16 v[114:115], v129 offset:9472\n\t"
        "s_waitcnt lgkmcnt(10)\n\t"
        "v_mfma_f32_32x32x16_bf16 v[80:95], v[156:159], v[116:119], v[80:95]\n\t"
        "v_cmp_gt_u32 vcc, s47, v126\n\t"
        "ds_read_b64_tr_b16 v[116:117], v130 offset:6400\n\t"
        "ds_read_b64_tr_b16 v[118:119], v130 offset:9472\n\t"
        "s_waitcnt vmcnt(14) lgkmcnt(10)\n\t"
        "v_mfma_f32_32x32x16_bf16 v[0:15], v[96:99], v[160:163], v[0:15]\n\t"
        "s_cbranch_vccnz 18f\n\t"
        "19:\n\t"
        "ds_read_b64_tr_b16 v[96:97], v123 offset:0\n\t"
        "ds_read_b64_tr_b16 v[98:99], v123 offset:3072\n\t"
        "s_waitcnt lgkmcnt(10)\n\t"
        "v_mfma_f32_32x32x16_bf16 v[16:31], v[100:103], v[160:163], v[16:31]\n\t"
        "v_add_u32 v128, 1, v128\n\t"
        "ds_read_b64_tr_b16 v[100:101], v124 offset:0\n\t"
        "ds_read_b64_tr_b16 v[102:103], v124 offset:3072\n\t"
        "s_waitcnt lgkmcnt(10)\n\t"
        "v_mfma_f32_32x32x16_bf16 v[32:47], v[104:107], v[160:163], v[32:47]\n\t"
        "ds_read_b64_tr_b16 v[104:105], v123 offset:128\n\t"
        "ds_read_b64_tr_b16 v[106:107], v123 offset:3200\n\t"
        "buffer_load_dwordx4 v[156:159], v120, s[40:43], s44 offen offset:2048 sc1\n\t"
        "s_waitcnt lgkmcnt(10)\n\t"
        "v_mfma_f32_32x32x16_bf16 v[48:63], v[108:111], v[160:163], v[48:63]\n\t"
        "s_add_u32 s46, s46, 12288\n\t"
        "s_add_u32 s47, s47, 1\n\t"
        "ds_read_b64_tr_b16 v[108:109], v124 offset:128\n\t"
        "ds_read_b64_tr_b16 v[110:111], v124 offset:3200\n\t"
        "s_waitcnt lgkmcnt(10)\n\t"
        "v_mfma_f32_32x32x16_bf16 v[64:79], v[160:163], v[112:115], v[64:79]\n\t"
        "s_cmp_eq_u32 s46, 110592\n\t"
        "s_cselect_b32 s46, 0, s46\n\t"
        "ds_read_b64_tr_b16 v[112:113], v123 offset:256\n\t"
        "ds_read_b64_tr_b16 v[114:115], v123 offset:3328\n\t"
        "s_waitcnt lgkmcnt(10)\n\t"
        "v_mfma_f32_32x32x16_bf16 v[80:95], v[160:163], v[116:119], v[80:95]\n\t"
        "ds_write_b32 v127, v128\n\t"
        "ds_read_b64_tr_b16 v[116:117], v124 offset:256\n\t"
        "ds_read_b64_tr_b16 v[118:119], v124 offset:3328\n\t"
        "s_waitcnt vmcnt(14) lgkmcnt(11)\n\t"
        "v_mfma_f32_32x32x16_bf16 v[0:15], v[96:99], v[164:167], v[0:15]\n\t"
        "ds_read_b32 v126, v125\n\t"
        "ds_read_b64_tr_b16 v[96:97], v123 offset:6144\n\t"
        "ds_read_b64_tr_b16 v[98:99], v123 offset:9216\n\t"
        "s_waitcnt lgkmcnt(12)\n\t"
        "v_mfma_f32_32x32x16_bf16 v[16:31], v[100:103], v[164:167], v[16:31]\n\t"
        "v_add_u32 v129, s46, v121\n\t"
        "ds_read_b64_tr_b16 v[100:101], v124 offset:6144\n\t"
        "ds_read_b64_tr_b16 v[102:103], v124 offset:9216\n\t"
        "s_waitcnt lgkmcnt(12)\n\t"
        "v_mfma_f32_32x32x16_bf16 v[32:47], v[104:107], v[164:167], v[32:47]\n\t"
        "ds_read_b64_tr_b16 v[104:105], v123 offset:6272\n\t"
        "ds_read_b64_tr_b16 v[106:107], v123 offset:9344\n\t"
        "buffer_load_dwordx4 v[160:163], v120, s[40:43], s44 offen offset:3072 sc1\n\t"
        "s_waitcnt lgkmcnt(12)\n\t"
        "v_mfma_f32_32x32x16_bf16 v[48:63], v[108:111], v[164:167], v[48:63]\n\t"
        "v_add_u32 v130, s46, v122\n\t"
        "ds_read_b64_tr_b16 v[108:109], v124 offset:6272\n\t"
        "ds_read_b64_tr_b16 v[110:111], v124 offset:9344\n\t"
        "s_waitcnt lgkmcnt(12)\n\t"
        "v_mfma_f32_32x32x16_bf16 v[64:79], v[164:167], v[112:115], v[64:79]\n\t"
        "ds_read_b64_tr_b16 v[112:113], v123 offset:6400\n\t"
        "ds_read_b64_tr_b16 v[114:115], v123 offset:9472\n\t"
        "s_waitcnt lgkmcnt(10)\n\t"
        "v_mfma_f32_32x32x16_bf16 v[80:95], v[164:167], v[116:119], v[80:95]\n\t"
        "v_cmp_gt_u32 vcc, s47, v126\n\t"
        "ds_read_b64_tr_b16 v[116:117], v124 offset:6400\n\t"
        "ds_read_b64_tr_b16 v[118:119], v124 offset:9472\n\t"
        "s_waitcnt vmcnt(14) lgkmcnt(10)\n\t"
        "v_mfma_f32_32x32x16_bf16 v[0:15], v[96:99], v[168:171], v[0:15]\n\t"
        "s_cbranch_vccnz 20f\n\t"
        "21:\n\t"
        "ds_read_b64_tr_b16 v[96:97], v129 offset:0\n\t"
        "ds_read_b64_tr_b16 v[98:99], v129 offset:3072\n\t"
        "s_waitcnt lgkmcnt(10)\n\t"
        "v_mfma_f32_32x32x16_bf16 v[16:31], v[100:103], v[168:171], v[16:31]\n\t"
        "v_add_u32 v128, 1, v128\n\t"
        "ds_read_b64_tr_b16 v[100:101], v130 offset:0\n\t"
        "ds_read_b64_tr_b16 v[102:103], v130 offset:3072\n\t"
        "s_waitcnt lgkmcnt(10)\n\t"
        "v_mfma_f32_32x32x16_bf16 v[32:47], v[104:107], v[168:171], v[32:47]\n\t"
        "ds_read_b64_tr_b16 v[104:105], v129 offset:128\n\t"
        "ds_read_b64_tr_b16 v[106:107], v129 offset:3200\n\t"
        "s_add_u32 s44, s44, 0x1000\n\t"
        "s_and_b32 s44, s44, 0xffff\n\t"
        "buffer_load_dwordx4 v[164:167], v120, s[40:43], s44 offen offset:0 sc1\n\t"
        "s_waitcnt lgkmcnt(10)\n\t"
        "v_mfma_f32_32x32x16_bf16 v[48:63], v[108:111], v[168:171], v[48:63]\n\t"
        "s_add_u32 s46, s46, 12288\n\t"
        "s_add_u32 s47, s47, 1\n\t"
        "ds_read_b64_tr_b16 v[108:109], v130 offset:128\n\t"
        "ds_read_b64_tr_b16 v[110:111], v130 offset:3200\n\t"
        "s_waitcnt lgkmcnt(10)\n\t"
        "v_mfma_f32_32x32x16_bf16 v[64:79], v[168:171], v[112:115], v[64:79]\n\t"
        "s_cmp_eq_u32 s46, 110592\n\t"
        "s_cselect_b32 s46, 0, s46\n\t"
        "ds_read_b64_tr_b16 v[112:113], v129 offset:256\n\t"
        "ds_read_b64_tr_b16 v[114:115], v129 offset:3328\n\t"
        "s_waitcnt lgkmcnt(10)\n\t"
        "v_mfma_f32_32x32x16_bf16 v[80:95], v[168:171], v[116:119], v[80:95]\n\t"
        "ds_write_b32 v127, v128\n\t"
        "ds_read_b64_tr_b16 v[116:117], v130 offset:256\n\t"
        "ds_read_b64_tr_b16 v[118:119], v130 offset:3328\n\t"
        "s_waitcnt vmcnt(14) lgkmcnt(11)\n\t"
        "v_mfma_f32_32x32x16_bf16 v[0:15], v[96:99], v[172:175], v[0:15]\n\t"
        "ds_read_b32 v126, v125\n\t"
        "ds_read_b64_tr_b16 v[96:97], v129 offset:6144\n\t"
        "ds_read_b64_tr_b16 v[98:99], v129 offset:9216\n\t"
        "s_waitcnt lgkmcnt(12)\n\t"
        "v_mfma_f32_32x32x16_bf16 v[16:31], v[100:103], v[172:175], v[16:31]\n\t"
        "v_add_u32 v123, s46, v121\n\t"
        "ds_read_b64_tr_b16 v[100:101], v130 offset:6144\n\t"
        "ds_read_b64_tr_b16 v[102:103], v130 offset:9216\n\t"
        "s_waitcnt lgkmcnt(12)\n\t"
        "v_mfma_f32_32x32x16_bf16 v[32:47], v[104:107], v[172:175], v[32:47]\n\t"
        "ds_read_b64_tr_b16 v[104:105], v129 offset:6272\n\t"
        "ds_read_b64_tr_b16 v[106:107], v129 offset:9344\n\t"
        "buffer_load_dwordx4 v[168:171], v120, s[40:43], s44 offen offset:1024 sc1\n\t"
        "s_waitcnt lgkmcnt(12)\n\t"
        "v_mfma_f32_32x32x16_bf16 v[48:63], v[108:111], v[172:175], v[48:63]\n\t"
        "v_add_u32 v124, s46, v122\n\t"
        "ds_read_b64_tr_b16 v[108:109], v130 offset:6272\n\t"
        "ds_read_b64_tr_b16 v[110:111], v130 offset:9344\n\t"
        "s_waitcnt lgkmcnt(12)\n\t"
        "v_mfma_f32_32x32x16_bf16 v[64:79], v[172:175], v[112:115], v[64:79]\n\t"
        "ds_read_b64_tr_b16 v[112:113], v129 offset:6400\n\t"
        "ds_read_b64_tr_b16 v[114:115], v129 offset:9472\n\t"
        "s_waitcnt lgkmcnt(10)\n\t"
        "v_mfma_f32_32x32x16_bf16 v[80:95], v[172:175], v[116:119], v[80:95]\n\t"
        "v_cmp_gt_u32 vcc, s47, v126\n\t"
        "ds_read_b64_tr_b16 v[116:117], v130 offset:6400\n\t"
        "ds_read_b64_tr_b16 v[118:119], v130 offset:9472\n\t"
        "s_waitcnt vmcnt(14) lgkmcnt(10)\n\t"
        "v_mfma_f32_32x32x16_bf16 v[0:15], v[96:99], v[176:179], v[0:15]\n\t"
        "s_cbranch_vccnz 22f\n\t"
        "23:\n\t"
        "ds_read_b64_tr_b16 v[96:97], v123 offset:0\n\t"
        "ds_read_b64_tr_b16 v[98:99], v123 offset:3072\n\t"
        "s_waitcnt lgkmcnt(10)\n\t"
        "v_mfma_f32_32x32x16_bf16 v[16:31], v[100:103], v[176:179], v[16:31]\n\t"
        "v_add_u32 v128, 1, v128\n\t"
        "ds_read_b64_tr_b16 v[100:101], v124 offset:0\n\t"
        "ds_read_b64_tr_b16 v[102:103], v124 offset:3072\n\t"
        "s_waitcnt lgkmcnt(10)\n\t"
        "v_mfma_f32_32x32x16_bf16 v[32:47], v[104:107], v[176:179], v[32:47]\n\t"
        "ds_read_b64_tr_b16 v[104:105], v123 offset:128\n\t"
        "ds_read_b64_tr_b16 v[106:107], v123 offset:3200\n\t"
        "buffer_load_dwordx4 v[172:175], v120, s[40:43], s44 offen offset:2048 sc1\n\t"
        "s_waitcnt lgkmcnt(10)\n\t"
        "v_mfma_f32_32x32x16_bf16 v[48:63], v[108:111], v[176:179], v[48:63]\n\t"
        "s_add_u32 s46, s46, 12288\n\t"
        "s_add_u32 s47, s47, 1\n\t"
        "ds_read_b64_tr_b16 v[108:109], v124 offset:128\n\t"
        "ds_read_b64_tr_b16 v[110:111], v124 offset:3200\n\t"
        "s_waitcnt lgkmcnt(10)\n\t"
        "v_mfma_f32_32x32x16_bf16 v[64:79], v[176:179], v[112:115], v[64:79]\n\t"
        "s_cmp_eq_u32 s46, 110592\n\t"
        "s_cselect_b32 s46, 0, s46\n\t"
        "ds_read_b64_tr_b16 v[112:113], v123 offset:256\n\t"
        "ds_read_b64_tr_b16 v[114:115], v123 offset:3328\n\t"
        "s_waitcnt lgkmcnt(10)\n\t"
        "v_mfma_f32_32x32x16_bf16 v[80:95], v[176:179], v[116:119], v[80:95]\n\t"
        "ds_write_b32 v127, v128\n\t"
        "ds_read_b64_tr_b16 v[116:117], v124 offset:256\n\t"
        "ds_read_b64_tr_b16 v[118:119], v124 offset:3328\n\t"
        "s_waitcnt vmcnt(14) lgkmcnt(11)\n\t"
        "v_mfma_f32_32x32x16_bf16 v[0:15], v[96:99], v[180:183], v[0:15]\n\t"
        "ds_read_b32 v126, v125\n\t"
        "ds_read_b64_tr_b16 v[96:97], v123 offset:6144\n\t"
        "ds_read_b64_tr_b16 v[98:99], v123 offset:9216\n\t"
        "s_waitcnt lgkmcnt(12)\n\t"
        "v_mfma_f32_32x32x16_bf16 v[16:31], v[100:103], v[180:183], v[16:31]\n\t"
        "v_add_u32 v129, s46, v121\n\t"
        "ds_read_b64_tr_b16 v[100:101], v124 offset:6144\n\t"
        "ds_read_b64_tr_b16 v[102:103], v124 offset:9216\n\t"
        "s_waitcnt lgkmcnt(12)\n\t"
        "v_mfma_f32_32x32x16_bf16 v[32:47], v[104:107], v[180:183], v[32:47]\n\t"
        "ds_read_b64_tr_b16 v[104:105], v123 offset:6272\n\t"
        "ds_read_b64_tr_b16 v[106:107], v123 offset:9344\n\t"
        "buffer_load_dwordx4 v[176:179], v120, s[40:43], s44 offen offset:3072 sc1\n\t"
        "s_waitcnt lgkmcnt(12)\n\t"
        "v_mfma_f32_32x32x16_bf16 v[48:63], v[108:111], v[180:183], v[48:63]\n\t"
        "v_add_u32 v130, s46, v122\n\t"
        "ds_read_b64_tr_b16 v[108:109], v124 offset:6272\n\t"
        "ds_read_b64_tr_b16 v[110:111], v124 offset:9344\n\t"
        "s_waitcnt lgkmcnt(12)\n\t"
        "v_mfma_f32_32x32x16_bf16 v[64:79], v[180:183], v[112:115], v[64:79]\n\t"
        "ds_read_b64_tr_b16 v[112:113], v123 offset:6400\n\t"
        "ds_read_b64_tr_b16 v[114:115], v123 offset:9472\n\t"
        "s_waitcnt lgkmcnt(10)\n\t"
        "v_mfma_f32_32x32x16_bf16 v[80:95], v[180:183], v[116:119], v[80:95]\n\t"
        "v_cmp_gt_u32 vcc, s47, v126\n\t"
        "ds_read_b64_tr_b16 v[116:117], v124 offset:6400\n\t"
        "ds_read_b64_tr_b16 v[118:119], v124 offset:9472\n\t"
        "s_waitcnt vmcnt(14) lgkmcnt(10)\n\t"
        "v_mfma_f32_32x32x16_bf16 v[0:15], v[96:99], v[184:187], v[0:15]\n\t"
        "s_cbranch_vccnz 24f\n\t"
        "25:\n\t"
        "ds_read_b64_tr_b16 v[96:97], v129 offset:0\n\t"
        "ds_read_b64_tr_b16 v[98:99], v129 offset:3072\n\t"
        "s_waitcnt lgkmcnt(10)\n\t"
        "v_mfma_f32_32x32x16_bf16 v[16:31], v[100:103], v[184:187], v[16:31]\n\t"
        "v_add_u32 v128, 1, v128\n\t"
        "ds_read_b64_tr_b16 v[100:101], v130 offset:0\n\t"
        "ds_read_b64_tr_b16 v[102:103], v130 offset:3072\n\t"
        "s_waitcnt lgkmcnt(10)\n\t"
        "v_mfma_f32_32x32x16_bf16 v[32:47], v[104:107], v[184:187], v[32:47]\n\t"
        "ds_read_b64_tr_b16 v[104:105], v129 offset:128\n\t"
        "ds_read_b64_tr_b16 v[106:107], v129 offset:3200\n\t"
        "s_add_u32 s44, s44, 0x1000\n\t"
        "s_and_b32 s44, s44, 0xffff\n\t"
        "buffer_load_dwordx4 v[180:183], v120, s[40:43], s44 offen offset:0 sc1\n\t"
        "s_waitcnt lgkmcnt(10)\n\t"
        "v_mfma_f32_32x32x16_bf16 v[48:63], v[108:111], v[184:187], v[48:63]\n\t"
        "s_add_u32 s46, s46, 12288\n\t"
        "s_add_u32 s47, s47, 1\n\t"
        "ds_read_b64_tr_b16 v[108:109], v130 offset:128\n\t"
        "ds_read_b64_tr_b16 v[110:111], v130 offset:3200\n\t"
        "s_waitcnt lgkmcnt(10)\n\t"
        "v_mfma_f32_32x32x16_bf16 v[64:79], v[184:187], v[112:115], v[64:79]\n\t"
        "s_cmp_eq_u32 s46, 110592\n\t"
        "s_cselect_b32 s46, 0, s46\n\t"
        "ds_read_b64_tr_b16 v[112:113], v129 offset:256\n\t"
        "ds_read_b64_tr_b16 v[114:115], v129 offset:3328\n\t"
        "s_waitcnt lgkmcnt(10)\n\t"
        "v_mfma_f32_32x32x16_bf16 v[80:95], v[184:187], v[116:119], v[80:95]\n\t"
        "ds_write_b32 v127, v128\n\t"
        "ds_read_b64_tr_b16 v[116:117], v130 offset:256\n\t"
        "ds_read_b64_tr_b16 v[118:119], v130 offset:3328\n\t"
        "s_waitcnt vmcnt(14) lgkmcnt(11)\n\t"
        "v_mfma_f32_32x32x16_bf16 v[0:15], v[96:99], v[188:191], v[0:15]\n\t"
        "ds_read_b32 v126, v125\n\t"
        "ds_read_b64_tr_b16 v[96:97], v129 offset:6144\n\t"
        "ds_read_b64_tr_b16 v[98:99], v129 offset:9216\n\t"
        "s_waitcnt lgkmcnt(12)\n\t"
        "v_mfma_f32_32x32x16_bf16 v[16:31], v[100:103], v[188:191], v[16:31]\n\t"
        "v_add_u32 v123, s46, v121\n\t"
        "ds_read_b64_tr_b16 v[100:101], v130 offset:6144\n\t"
        "ds_read_b64_tr_b16 v[102:103], v130 offset:9216\n\t"
        "s_waitcnt lgkmcnt(12)\n\t"
        "v_mfma_f32_32x32x16_bf16 v[32:47], v[104:107], v[188:191], v[32:47]\n\t"
        "ds_read_b64_tr_b16 v[104:105], v129 offset:6272\n\t"
        "ds_read_b64_tr_b16 v[106:107], v129 offset:9344\n\t"
        "buffer_load_dwordx4 v[184:187], v120, s[40:43], s44 offen offset:1024 sc1\n\t"
        "s_waitcnt lgkmcnt(12)\n\t"
        "v_mfma_f32_32x32x16_bf16 v[48:63], v[108:111], v[188:191], v[48:63]\n\t"
        "v_add_u32 v124, s46, v122\n\t"
        "ds_read_b64_tr_b16 v[108:109], v130 offset:6272\n\t"
        "ds_read_b64_tr_b16 v[110:111], v130 offset:9344\n\t"
        "s_waitcnt lgkmcnt(12)\n\t"
        "v_mfma_f32_32x32x16_bf16 v[64:79], v[188:191], v[112:115], v[64:79]\n\t"
        "ds_read_b64_tr_b16 v[112:113], v129 offset:6400\n\t"
        "ds_read_b64_tr_b16 v[114:115], v129 offset:9472\n\t"
        "s_waitcnt lgkmcnt(10)\n\t"
        "v_mfma_f32_32x32x16_bf16 v[80:95], v[188:191], v[116:119], v[80:95]\n\t"
        "v_cmp_gt_u32 vcc, s47, v126\n\t"
        "ds_read_b64_tr_b16 v[116:117], v130 offset:6400\n\t"
        "ds_read_b64_tr_b16 v[118:119], v130 offset:9472\n\t"
        "s_waitcnt vmcnt(14) lgkmcnt(10)\n\t"
        "v_mfma_f32_32x32x16_bf16 v[0:15], v[96:99], v[192:195], v[0:15]\n\t"
        "s_cbranch_vccnz 26f\n\t"
        "27:\n\t"
        "ds_read_b64_tr_b16 v[96:97], v123 offset:0\n\t"
        "ds_read_b64_tr_b16 v[98:99], v123 offset:3072\n\t"
        "s_waitcnt lgkmcnt(10)\n\t"
        "v_mfma_f32_32x32x16_bf16 v[16:31], v[100:103], v[192:195], v[16:31]\n\t"
        "v_add_u32 v128, 1, v128\n\t"
        "ds_read_b64_tr_b16 v[100:101], v124 offset:0\n\t"
        "ds_read_b64_tr_b16 v[102:103], v124 offset:3072\n\t"
        "s_waitcnt lgkmcnt(10)\n\t"
        "v_mfma_f32_32x32x16_bf16 v[32:47], v[104:107], v[192:195], v[32:47]\n\t"
        "ds_read_b64_tr_b16 v[104:105], v123 offset:128\n\t"
        "ds_read_b64_tr_b16 v[106:107], v123 offset:3200\n\t"
        "buffer_load_dwordx4 v[188:191], v120, s[40:43], s44 offen offset:2048 sc1\n\t"
        "s_waitcnt lgkmcnt(10)\n\t"
        "v_mfma_f32_32x32x16_bf16 v[48:63], v[108:111], v[192:195], v[48:63]\n\t"
        "s_add_u32 s46, s46, 12288\n\t"
        "s_add_u32 s47, s47, 1\n\t"
        "ds_read_b64_tr_b16 v[108:109], v124 offset:128\n\t"
        "ds_read_b64_tr_b16 v[110:111], v124 offset:3200\n\t"
        "s_waitcnt lgkmcnt(10)\n\t"
        "v_mfma_f32_32x32x16_bf16 v[64:79], v[192:195], v[112:115], v[64:79]\n\t"
        "s_cmp_eq_u32 s46, 110592\n\t"
        "s_cselect_b32 s46, 0, s46\n\t"
        "ds_read_b64_tr_b16 v[112:113], v123 offset:256\n\t"
        "ds_read_b64_tr_b16 v[114:115], v123 offset:3328\n\t"
        "s_waitcnt lgkmcnt(10)\n\t"
        "v_mfma_f32_32x32x16_bf16 v[80:95], v[192:195], v[116:119], v[80:95]\n\t"
        "ds_write_b32 v127, v128\n\t"
        "ds_read_b64_tr_b16 v[116:117], v124 offset:256\n\t"
        "ds_read_b64_tr_b16 v[118:119], v124 offset:3328\n\t"
        "s_waitcnt vmcnt(14) lgkmcnt(11)\n\t"
        "v_mfma_f32_32x32x16_bf16 v[0:15], v[96:99], v[132:135], v[0:15]\n\t"
        "ds_read_b32 v126, v125\n\t"
        "ds_read_b64_tr_b16 v[96:97], v123 offset:6144\n\t"
        "ds_read_b64_tr_b16 v[98:99], v123 offset:9216\n\t"
        "s_waitcnt lgkmcnt(12)\n\t"
        "v_mfma_f32_32x32x16_bf16 v[16:31], v[100:103], v[132:135], v[16:31]\n\t"
        "v_add_u32 v129, s46, v121\n\t"
        "ds_read_b64_tr_b16 v[100:101], v124 offset:6144\n\t"
        "ds_read_b64_tr_b16 v[102:103], v124 offset:9216\n\t"
        "s_waitcnt lgkmcnt(12)\n\t"
        "v_mfma_f32_32x32x16_bf16 v[32:47], v[104:107], v[132:135], v[32:47]\n\t"
        "ds_read_b64_tr_b16 v[104:105], v123 offset:6272\n\t"
        "ds_read_b64_tr_b16 v[106:107], v123 offset:9344\n\t"
        "buffer_load_dwordx4 v[192:195], v120, s[40:43], s44 offen offset:3072 sc1\n\t"
        "s_waitcnt lgkmcnt(12)\n\t"
        "v_mfma_f32_32x32x16_bf16 v[48:63], v[108:111], v[132:135], v[48:63]\n\t"
        "v_add_u32 v130, s46, v122\n\t"
        "ds_read_b64_tr_b16 v[108:109], v124 offset:6272\n\t"
        "ds_read_b64_tr_b16 v[110:111], v124 offset:9344\n\t"
        "s_waitcnt lgkmcnt(12)\n\t"
        "v_mfma_f32_32x32x16_bf16 v[64:79], v[132:135], v[112:115], v[64:79]\n\t"
        "ds_read_b64_tr_b16 v[112:113], v123 offset:6400\n\t"
        "ds_read_b64_tr_b16 v[114:115], v123 offset:9472\n\t"
        "s_waitcnt lgkmcnt(10)\n\t"
        "v_mfma_f32_32x32x16_bf16 v[80:95], v[132:135], v[116:119], v[80:95]\n\t"
        "v_cmp_gt_u32 vcc, s47, v126\n\t"
        "ds_read_b64_tr_b16 v[116:117], v124 offset:6400\n\t"
        "ds_read_b64_tr_b16 v[118:119], v124 offset:9472\n\t"
        "s_waitcnt vmcnt(14) lgkmcnt(10)\n\t"
        "v_mfma_f32_32x32x16_bf16 v[0:15], v[96:99], v[136:139], v[0:15]\n\t"
        "s_cbranch_vccnz 28f\n\t"
        "29:\n\t"
        "ds_read_b64_tr_b16 v[96:97], v129 offset:0\n\t"
        "ds_read_b64_tr_b16 v[98:99], v129 offset:3072\n\t"
        "s_waitcnt lgkmcnt(10)\n\t"
        "v_mfma_f32_32x32x16_bf16 v[16:31], v[100:103], v[136:139], v[16:31]\n\t"
        "v_add_u32 v128, 1, v128\n\t"
        "ds_read_b64_tr_b16 v[100:101], v130 offset:0\n\t"
        "ds_read_b64_tr_b16 v[102:103], v130 offset:3072\n\t"
        "s_waitcnt lgkmcnt(10)\n\t"
        "v_mfma_f32_32x32x16_bf16 v[32:47], v[104:107], v[136:139], v[32:47]\n\t"
        "ds_read_b64_tr_b16 v[104:105], v129 offset:128\n\t"
        "ds_read_b64_tr_b16 v[106:107], v129 offset:3200\n\t"
        "s_add_u32 s44, s44, 0x1000\n\t"
        "s_and_b32 s44, s44, 0xffff\n\t"
        "buffer_load_dwordx4 v[132:135], v120, s[40:43], s44 offen offset:0 sc1\n\t"
        "s_waitcnt lgkmcnt(10)\n\t"
        "v_mfma_f32_32x32x16_bf16 v[48:63], v[108:111], v[136:139], v[48:63]\n\t"
        "s_add_u32 s46, s46, 12288\n\t"
        "s_add_u32 s47, s47, 1\n\t"
        "ds_read_b64_tr_b16 v[108:109], v130 offset:128\n\t"
        "ds_read_b64_tr_b16 v[110:111], v130 offset:3200\n\t"
        "s_waitcnt lgkmcnt(10)\n\t"
        "v_mfma_f32_32x32x16_bf16 v[64:79], v[136:139], v[112:115], v[64:79]\n\t"
        "s_cmp_eq_u32 s46, 110592\n\t"
        "s_cselect_b32 s46, 0, s46\n\t"
        "ds_read_b64_tr_b16 v[112:113], v129 offset:256\n\t"
        "ds_read_b64_tr_b16 v[114:115], v129 offset:3328\n\t"
        "s_waitcnt lgkmcnt(10)\n\t"
        "v_mfma_f32_32x32x16_bf16 v[80:95], v[136:139], v[116:119], v[80:95]\n\t"
        "ds_write_b32 v127, v128\n\t"
        "ds_read_b64_tr_b16 v[116:117], v130 offset:256\n\t"
        "ds_read_b64_tr_b16 v[118:119], v130 offset:3328\n\t"
        "s_waitcnt vmcnt(14) lgkmcnt(11)\n\t"
        "v_mfma_f32_32x32x16_bf16 v[0:15], v[96:99], v[140:143], v[0:15]\n\t"
        "ds_read_b32 v126, v125\n\t"
        "ds_read_b64_tr_b16 v[96:97], v129 offset:6144\n\t"
        "ds_read_b64_tr_b16 v[98:99], v129 offset:9216\n\t"
        "s_waitcnt lgkmcnt(12)\n\t"
        "v_mfma_f32_32x32x16_bf16 v[16:31], v[100:103], v[140:143], v[16:31]\n\t"
        "v_add_u32 v123, s46, v121\n\t"
        "ds_read_b64_tr_b16 v[100:101], v130 offset:6144\n\t"
        "ds_read_b64_tr_b16 v[102:103], v130 offset:9216\n\t"
        "s_waitcnt lgkmcnt(12)\n\t"
        "v_mfma_f32_32x32x16_bf16 v[32:47], v[104:107], v[140:143], v[32:47]\n\t"
        "ds_read_b64_tr_b16 v[104:105], v129 offset:6272\n\t"
        "ds_read_b64_tr_b16 v[106:107], v129 offset:9344\n\t"
        "buffer_load_dwordx4 v[136:139], v120, s[40:43], s44 offen offset:1024 sc1\n\t"
        "s_waitcnt lgkmcnt(12)\n\t"
        "v_mfma_f32_32x32x16_bf16 v[48:63], v[108:111], v[140:143], v[48:63]\n\t"
        "v_add_u32 v124, s46, v122\n\t"
        "ds_read_b64_tr_b16 v[108:109], v130 offset:6272\n\t"
        "ds_read_b64_tr_b16 v[110:111], v130 offset:9344\n\t"
        "s_waitcnt lgkmcnt(12)\n\t"
        "v_mfma_f32_32x32x16_bf16 v[64:79], v[140:143], v[112:115], v[64:79]\n\t"
        "ds_read_b64_tr_b16 v[112:113], v129 offset:6400\n\t"
        "ds_read_b64_tr_b16 v[114:115], v129 offset:9472\n\t"
        "s_waitcnt lgkmcnt(10)\n\t"
        "v_mfma_f32_32x32x16_bf16 v[80:95], v[140:143], v[116:119], v[80:95]\n\t"
        "v_cmp_gt_u32 vcc, s47, v126\n\t"
        "ds_read_b64_tr_b16 v[116:117], v130 offset:6400\n\t"
        "ds_read_b64_tr_b16 v[118:119], v130 offset:9472\n\t"
        "s_waitcnt vmcnt(14) lgkmcnt(10)\n\t"
        "v_mfma_f32_32x32x16_bf16 v[0:15], v[96:99], v[144:147], v[0:15]\n\t"
        "s_cbranch_vccnz 30f\n\t"
        "31:\n\t"
        "ds_read_b64_tr_b16 v[96:97], v123 offset:0\n\t"
        "ds_read_b64_tr_b16 v[98:99], v123 offset:3072\n\t"
        "s_waitcnt lgkmcnt(10)\n\t"
        "v_mfma_f32_32x32x16_bf16 v[16:31], v[100:103], v[144:147], v[16:31]\n\t"
        "v_add_u32 v128, 1, v128\n\t"
        "ds_read_b64_tr_b16 v[100:101], v124 offset:0\n\t"
        "ds_read_b64_tr_b16 v[102:103], v124 offset:3072\n\t"
        "s_waitcnt lgkmcnt(10)\n\t"
        "v_mfma_f32_32x32x16_bf16 v[32:47], v[104:107], v[144:147], v[32:47]\n\t"
        "ds_read_b64_tr_b16 v[104:105], v123 offset:128\n\t"
        "ds_read_b64_tr_b16 v[106:107], v123 offset:3200\n\t"
        "buffer_load_dwordx4 v[140:143], v120, s[40:43], s44 offen offset:2048 sc1\n\t"
        "s_waitcnt lgkmcnt(10)\n\t"
        "v_mfma_f32_32x32x16_bf16 v[48:63], v[108:111], v[144:147], v[48:63]\n\t"
        "s_add_u32 s46, s46, 12288\n\t"
        "s_add_u32 s47, s47, 1\n\t"
        "ds_read_b64_tr_b16 v[108:109], v124 offset:128\n\t"
        "ds_read_b64_tr_b16 v[110:111], v124 offset:3200\n\t"
        "s_waitcnt lgkmcnt(10)\n\t"
        "v_mfma_f32_32x32x16_bf16 v[64:79], v[144:147], v[112:115], v[64:79]\n\t"
        "s_cmp_eq_u32 s46, 110592\n\t"
        "s_cselect_b32 s46, 0, s46\n\t"
        "ds_read_b64_tr_b16 v[112:113], v123 offset:256\n\t"
        "ds_read_b64_tr_b16 v[114:115], v123 offset:3328\n\t"
        "s_waitcnt lgkmcnt(10)\n\t"
        "v_mfma_f32_32x32x16_bf16 v[80:95], v[144:147], v[116:119], v[80:95]\n\t"
        "ds_write_b32 v127, v128\n\t"
        "ds_read_b64_tr_b16 v[116:117], v124 offset:256\n\t"
        "ds_read_b64_tr_b16 v[118:119], v124 offset:3328\n\t"
        "s_sub_u32 s45, s45, 1\n\t"
        "s_cmp_lg_u32 s45, 0\n\t"
        "s_cbranch_scc1 1b\n\t"
        "s_waitcnt vmcnt(14) lgkmcnt(11)\n\t"
        "v_mfma_f32_32x32x16_bf16 v[0:15], v[96:99], v[148:151], v[0:15]\n\t"
        "ds_read_b32 v126, v125\n\t"
        "s_add_u32 s47, s47, 4\n\t"
        "ds_read_b64_tr_b16 v[96:97], v123 offset:6400\n\t"
        "ds_read_b64_tr_b16 v[98:99], v123 offset:9472\n\t"
        "s_waitcnt lgkmcnt(12)\n\t"
        "v_mfma_f32_32x32x16_bf16 v[16:31], v[100:103], v[148:151], v[16:31]\n\t"
        "ds_read_b64_tr_b16 v[100:101], v124 offset:6400\n\t"
        "ds_read_b64_tr_b16 v[102:103], v124 offset:9472\n\t"
        "s_waitcnt lgkmcnt(4)\n\t"
        "v_cmp_gt_u32 vcc, s47, v126\n\t"
        "v_mfma_f32_32x32x16_bf16 v[32:47], v[104:107], v[148:151], v[32:47]\n\t"
        "s_cbranch_vccnz 74f\n\t"
        "75:\n\t"
        "ds_read_b64_tr_b16 v[104:105], v121 offset:256\n\t"
        "ds_read_b64_tr_b16 v[106:107], v121 offset:3328\n\t"
        "s_waitcnt lgkmcnt(12)\n\t"
        "v_mfma_f32_32x32x16_bf16 v[48:63], v[108:111], v[148:151], v[48:63]\n\t"
        "ds_read_b64_tr_b16 v[108:109], v122 offset:256\n\t"
        "ds_read_b64_tr_b16 v[110:111], v122 offset:3328\n\t"
        "s_waitcnt lgkmcnt(12)\n\t"
        "v_mfma_f32_32x32x16_bf16 v[64:79], v[148:151], v[112:115], v[64:79]\n\t"
        "ds_read_b64_tr_b16 v[112:113], v121 offset:6400\n\t"
        "ds_read_b64_tr_b16 v[114:115], v121 offset:9472\n\t"
        "s_waitcnt lgkmcnt(11)\n\t"
        "v_mfma_f32_32x32x16_bf16 v[80:95], v[148:151], v[116:119], v[80:95]\n\t"
        "ds_read_b64_tr_b16 v[116:117], v122 offset:6400\n\t"
        "ds_read_b64_tr_b16 v[118:119], v122 offset:9472\n\t"
        "s_waitcnt vmcnt(13) lgkmcnt(10)\n\t"
        "v_mfma_f32_32x32x16_bf16 v[64:79], v[152:155], v[96:99], v[64:79]\n\t"
        "ds_read_b64_tr_b16 v[96:97], v121 offset:12544\n\t"
        "ds_read_b64_tr_b16 v[98:99], v121 offset:15616\n\t"
        "s_waitcnt lgkmcnt(10)\n\t"
        "v_mfma_f32_32x32x16_bf16 v[80:95], v[152:155], v[100:103], v[80:95]\n\t"
        "ds_read_b64_tr_b16 v[100:101], v122 offset:12544\n\t"
        "ds_read_b64_tr_b16 v[102:103], v122 offset:15616\n\t"
        "s_waitcnt vmcnt(12) lgkmcnt(10)\n\t"
        "v_mfma_f32_32x32x16_bf16 v[64:79], v[156:159], v[104:107], v[64:79]\n\t"
        "ds_read_b128 v[196:199], v215 offset:0\n\t"
        "ds_read_b64_tr_b16 v[104:105], v121 offset:18688\n\t"
        "ds_read_b64_tr_b16 v[106:107], v121 offset:21760\n\t"
        "s_waitcnt lgkmcnt(11)\n\t"
        "v_mfma_f32_32x32x16_bf16 v[80:95], v[156:159], v[108:111], v[80:95]\n\t"
        "ds_read_b128 v[200:203], v215 offset:32\n\t"
        "ds_read_b64_tr_b16 v[108:109], v122 offset:18688\n\t"
        "ds_read_b64_tr_b16 v[110:111], v122 offset:21760\n\t"
        "s_waitcnt vmcnt(11) lgkmcnt(12)\n\t"
        "v_mfma_f32_32x32x16_bf16 v[64:79], v[160:163], v[112:115], v[64:79]\n\t"
        "ds_read_b128 v[204:207], v215 offset:64\n\t"
        "ds_read_b64_tr_b16 v[112:113], v121 offset:24832\n\t"
        "ds_read_b64_tr_b16 v[114:115], v121 offset:27904\n\t"
        "s_waitcnt lgkmcnt(13)\n\t"
        "v_mfma_f32_32x32x16_bf16 v[80:95], v[160:163], v[116:119], v[80:95]\n\t"
        "ds_read_b128 v[208:211], v215 offset:96\n\t"
        "ds_read_b64_tr_b16 v[116:117], v122 offset:24832\n\t"
        "ds_read_b64_tr_b16 v[118:119], v122 offset:27904\n\t"
        "s_waitcnt vmcnt(10) lgkmcnt(14)\n\t"
        "v_mfma_f32_32x32x16_bf16 v[64:79], v[164:167], v[96:99], v[64:79]\n\t"
        "ds_read_b64_tr_b16 v[96:97], v121 offset:30976\n\t"
        "ds_read_b64_tr_b16 v[98:99], v121 offset:34048\n\t"
        "s_waitcnt lgkmcnt(14)\n\t"
        "v_mfma_f32_32x32x16_bf16 v[80:95], v[164:167], v[100:103], v[80:95]\n\t"
        "ds_read_b64_tr_b16 v[100:101], v122 offset:30976\n\t"
        "ds_read_b64_tr_b16 v[102:103], v122 offset:34048\n\t"
        "s_waitcnt vmcnt(9) lgkmcnt(13)\n\t"
        "v_mfma_f32_32x32x16_bf16 v[64:79], v[168:171], v[104:107], v[64:79]\n\t"
        "ds_read_b64_tr_b16 v[104:105], v121 offset:37120\n\t"
        "ds_read_b64_tr_b16 v[106:107], v121 offset:40192\n\t"
        "s_waitcnt lgkmcnt(12)\n\t"
        "v_mfma_f32_32x32x16_bf16 v[80:95], v[168:171], v[108:111], v[80:95]\n\t"
        "ds_read_b64_tr_b16 v[108:109], v122 offset:37120\n\t"
        "ds_read_b64_tr_b16 v[110:111], v122 offset:40192\n\t"
        "s_waitcnt vmcnt(8) lgkmcnt(11)\n\t"
        "v_mfma_f32_32x32x16_bf16 v[64:79], v[172:175], v[112:115], v[64:79]\n\t"
        "ds_read_b64_tr_b16 v[112:113], v121 offset:43264\n\t"
        "ds_read_b64_tr_b16 v[114:115], v121 offset:46336\n\t"
        "s_waitcnt lgkmcnt(10)\n\t"
        "v_mfma_f32_32x32x16_bf16 v[80:95], v[172:175], v[116:119], v[80:95]\n\t"
        "ds_read_b64_tr_b16 v[116:117], v122 offset:43264\n\t"
        "ds_read_b64_tr_b16 v[118:119], v122 offset:46336\n\t"
        "s_waitcnt vmcnt(7) lgkmcnt(10)\n\t"
        "v_mfma_f32_32x32x16_bf16 v[64:79], v[176:179], v[96:99], v[64:79]\n\t"
        "ds_read_b64_tr_b16 v[96:97], v121 offset:49408\n\t"
        "ds_read_b64_tr_b16 v[98:99], v121 offset:52480\n\t"
        "s_waitcnt lgkmcnt(10)\n\t"
        "v_mfma_f32_32x32x16_bf16 v[80:95], v[176:179], v[100:103], v[80:95]\n\t"
        "ds_read_b64_tr_b16 v[100:101], v122 offset:49408\n\t"
        "ds_read_b64_tr_b16 v[102:103], v122 offset:52480\n\t"
        "s_waitcnt vmcnt(6) lgkmcnt(10)\n\t"
        "v_mfma_f32_32x32x16_bf16 v[64:79], v[180:183], v[104:107], v[64:79]\n\t"
        "ds_read_b64_tr_b16 v[104:105], v121 offset:55552\n\t"
        "ds_read_b64_tr_b16 v[106:107], v121 offset:58624\n\t"
        "s_waitcnt lgkmcnt(10)\n\t"
        "v_mfma_f32_32x32x16_bf16 v[80:95], v[180:183], v[108:111], v[80:95]\n\t"
        "ds_read_b64_tr_b16 v[108:109], v122 offset:55552\n\t"
        "ds_read_b64_tr_b16 v[110:111], v122 offset:58624\n\t"
        "s_waitcnt vmcnt(5) lgkmcnt(10)\n\t"
        "v_mfma_f32_32x32x16_bf16 v[64:79], v[184:187], v[112:115], v[64:79]\n\t"
        "ds_read_b64_tr_b16 v[112:113], v123 offset:6272\n\t"
        "ds_read_b64_tr_b16 v[114:115], v123 offset:9344\n\t"
        "s_waitcnt lgkmcnt(10)\n\t"
        "v_mfma_f32_32x32x16_bf16 v[80:95], v[184:187], v[116:119], v[80:95]\n\t"
        "ds_read_b64_tr_b16 v[116:117], v124 offset:6272\n\t"
        "ds_read_b64_tr_b16 v[118:119], v124 offset:9344\n\t"
        "s_waitcnt vmcnt(4) lgkmcnt(10)\n\t"
        "v_mfma_f32_32x32x16_bf16 v[64:79], v[188:191], v[96:99], v[64:79]\n\t"
        "ds_read_b64_tr_b16 v[96:97], v121 offset:128\n\t"
        "ds_read_b64_tr_b16 v[98:99], v121 offset:3200\n\t"
        "s_waitcnt lgkmcnt(10)\n\t"
        "v_mfma_f32_32x32x16_bf16 v[80:95], v[188:191], v[100:103], v[80:95]\n\t"
        "ds_read_b64_tr_b16 v[100:101], v122 offset:128\n\t"
        "ds_read_b64_tr_b16 v[102:103], v122 offset:3200\n\t"
        "s_waitcnt vmcnt(0) lgkmcnt(10)\n\t"
        "v_mfma_f32_32x32x16_bf16 v[64:79], v[192:195], v[104:107], v[64:79]\n\t"
        "ds_read_b64_tr_b16 v[104:105], v121 offset:6272\n\t"
        "ds_read_b64_tr_b16 v[106:107], v121 offset:9344\n\t"
        "s_waitcnt lgkmcnt(10)\n\t"
        "v_mfma_f32_32x32x16_bf16 v[80:95], v[192:195], v[108:111], v[80:95]\n\t"
        "ds_read_b64_tr_b16 v[108:109], v122 offset:6272\n\t"
        "ds_read_b64_tr_b16 v[110:111], v122 offset:9344\n\t"
        "s_waitcnt lgkmcnt(10)\n\t"
        "v_mfma_f32_32x32x16_bf16 v[32:47], v[112:115], v[152:155], v[32:47]\n\t"
        "ds_read_b64_tr_b16 v[112:113], v121 offset:12416\n\t"
        "ds_read_b64_tr_b16 v[114:115], v121 offset:15488\n\t"
        "s_waitcnt lgkmcnt(10)\n\t"
        "v_mfma_f32_32x32x16_bf16 v[48:63], v[116:119], v[152:155], v[48:63]\n\t"
        "ds_read_b64_tr_b16 v[116:117], v122 offset:12416\n\t"
        "ds_read_b64_tr_b16 v[118:119], v122 offset:15488\n\t"
        "v_mul_f32 v64, v196, v64\n\t"
        "v_mul_f32 v65, v197, v65\n\t"
        "v_mul_f32 v66, v198, v66\n\t"
        "s_waitcnt lgkmcnt(10)\n\t"
        "v_mfma_f32_32x32x16_bf16 v[32:47], v[96:99], v[156:159], v[32:47]\n\t"
        "ds_read_b64_tr_b16 v[96:97], v121 offset:18560\n\t"
        "ds_read_b64_tr_b16 v[98:99], v121 offset:21632\n\t"
        "v_mul_f32 v67, v199, v67\n\t"
        "v_mul_f32 v68, v200, v68\n\t"
        "v_mul_f32 v69, v201, v69\n\t"
        "s_waitcnt lgkmcnt(10)\n\t"
        "v_mfma_f32_32x32x16_bf16 v[48:63], v[100:103], v[156:159], v[48:63]\n\t"
        "ds_read_b64_tr_b16 v[100:101], v122 offset:18560\n\t"
        "ds_read_b64_tr_b16 v[102:103], v122 offset:21632\n\t"
        "v_mul_f32 v70, v202, v70\n\t"
        "v_mul_f32 v71, v203, v71\n\t"
        "v_cvt_pk_bf16_f32 v64, v64, v65\n\t"
        "s_waitcnt lgkmcnt(10)\n\t"
        "v_mfma_f32_32x32x16_bf16 v[32:47], v[104:107], v[160:163], v[32:47]\n\t"
        "ds_read_b64_tr_b16 v[104:105], v121 offset:24704\n\t"
        "ds_read_b64_tr_b16 v[106:107], v121 offset:27776\n\t"
        "v_cvt_pk_bf16_f32 v65, v66, v67\n\t"
        "v_cvt_pk_bf16_f32 v66, v68, v69\n\t"
        "v_cvt_pk_bf16_f32 v67, v70, v71\n\t"
        "s_waitcnt lgkmcnt(10)\n\t"
        "v_mfma_f32_32x32x16_bf16 v[48:63], v[108:111], v[160:163], v[48:63]\n\t"
        "ds_read_b64_tr_b16 v[108:109], v122 offset:24704\n\t"
        "ds_read_b64_tr_b16 v[110:111], v122 offset:27776\n\t"
        "v_mul_f32 v72, v204, v72\n\t"
        "v_mul_f32 v73, v205, v73\n\t"
        "v_mul_f32 v74, v206, v74\n\t"
        "s_waitcnt lgkmcnt(10)\n\t"
        "v_mfma_f32_32x32x16_bf16 v[32:47], v[112:115], v[164:167], v[32:47]\n\t"
        "ds_read_b64_tr_b16 v[112:113], v121 offset:30848\n\t"
        "ds_read_b64_tr_b16 v[114:115], v121 offset:33920\n\t"
        "v_mul_f32 v75, v207, v75\n\t"
        "v_mul_f32 v76, v208, v76\n\t"
        "v_mul_f32 v77, v209, v77\n\t"
        "s_waitcnt lgkmcnt(10)\n\t"
        "v_mfma_f32_32x32x16_bf16 v[48:63], v[116:119], v[164:167], v[48:63]\n\t"
        "ds_read_b64_tr_b16 v[116:117], v122 offset:30848\n\t"
        "ds_read_b64_tr_b16 v[118:119], v122 offset:33920\n\t"
        "v_mul_f32 v78, v210, v78\n\t"
        "v_mul_f32 v79, v211, v79\n\t"
        "v_cvt_pk_bf16_f32 v68, v72, v73\n\t"
        "s_waitcnt lgkmcnt(10)\n\t"
        "v_mfma_f32_32x32x16_bf16 v[32:47], v[96:99], v[168:171], v[32:47]\n\t"
        "ds_read_b64_tr_b16 v[96:97], v121 offset:36992\n\t"
        "ds_read_b64_tr_b16 v[98:99], v121 offset:40064\n\t"
        "v_cvt_pk_bf16_f32 v69, v74, v75\n\t"
        "v_cvt_pk_bf16_f32 v70, v76, v77\n\t"
        "v_cvt_pk_bf16_f32 v71, v78, v79\n\t"
        "s_waitcnt lgkmcnt(10)\n\t"
        "v_mfma_f32_32x32x16_bf16 v[48:63], v[100:103], v[168:171], v[48:63]\n\t"
        "ds_read_b64_tr_b16 v[100:101], v122 offset:36992\n\t"
        "ds_read_b64_tr_b16 v[102:103], v122 offset:40064\n\t"
        "v_permlane32_swap_b32 v64, v66\n\t"
        "v_permlane32_swap_b32 v65, v67\n\t"
        "v_permlane32_swap_b32 v68, v70\n\t"
        "s_waitcnt lgkmcnt(10)\n\t"
        "v_mfma_f32_32x32x16_bf16 v[32:47], v[104:107], v[172:175], v[32:47]\n\t"
        "ds_read_b64_tr_b16 v[104:105], v121 offset:43136\n\t"
        "ds_read_b64_tr_b16 v[106:107], v121 offset:46208\n\t"
        "v_permlane32_swap_b32 v69, v71\n\t"
        "buffer_store_dwordx4 v[64:67], v213, s[40:43], 0 offen offset:2048 sc1\n\t"
        "buffer_store_dwordx4 v[68:71], v213, s[40:43], 0 offen offset:3072 sc1\n\t"
        "s_waitcnt lgkmcnt(10)\n\t"
        "v_mfma_f32_32x32x16_bf16 v[48:63], v[108:111], v[172:175], v[48:63]\n\t"
        "ds_read_b64_tr_b16 v[108:109], v122 offset:43136\n\t"
        "ds_read_b64_tr_b16 v[110:111], v122 offset:46208\n\t"
        "v_mul_f32 v80, v196, v80\n\t"
        "v_mul_f32 v81, v197, v81\n\t"
        "v_mul_f32 v82, v198, v82\n\t"
        "s_waitcnt lgkmcnt(10)\n\t"
        "v_mfma_f32_32x32x16_bf16 v[32:47], v[112:115], v[176:179], v[32:47]\n\t"
        "ds_read_b64_tr_b16 v[112:113], v121 offset:49280\n\t"
        "ds_read_b64_tr_b16 v[114:115], v121 offset:52352\n\t"
        "v_mul_f32 v83, v199, v83\n\t"
        "v_mul_f32 v84, v200, v84\n\t"
        "v_mul_f32 v85, v201, v85\n\t"
        "s_waitcnt lgkmcnt(10)\n\t"
        "v_mfma_f32_32x32x16_bf16 v[48:63], v[116:119], v[176:179], v[48:63]\n\t"
        "ds_read_b64_tr_b16 v[116:117], v122 offset:49280\n\t"
        "ds_read_b64_tr_b16 v[118:119], v122 offset:52352\n\t"
        "v_mul_f32 v86, v202, v86\n\t"
        "v_mul_f32 v87, v203, v87\n\t"
        "v_cvt_pk_bf16_f32 v80, v80, v81\n\t"
        "s_waitcnt lgkmcnt(10)\n\t"
        "v_mfma_f32_32x32x16_bf16 v[32:47], v[96:99], v[180:183], v[32:47]\n\t"
        "ds_read_b64_tr_b16 v[96:97], v121 offset:55424\n\t"
        "ds_read_b64_tr_b16 v[98:99], v121 offset:58496\n\t"
        "v_cvt_pk_bf16_f32 v81, v82, v83\n\t"
        "v_cvt_pk_bf16_f32 v82, v84, v85\n\t"
        "v_cvt_pk_bf16_f32 v83, v86, v87\n\t"
        "s_waitcnt lgkmcnt(10)\n\t"
        "v_mfma_f32_32x32x16_bf16 v[48:63], v[100:103], v[180:183], v[48:63]\n\t"
        "ds_read_b64_tr_b16 v[100:101], v122 offset:55424\n\t"
        "ds_read_b64_tr_b16 v[102:103], v122 offset:58496\n\t"
        "v_mul_f32 v88, v204, v88\n\t"
        "v_mul_f32 v89, v205, v89\n\t"
        "v_mul_f32 v90, v206, v90\n\t"
        "s_waitcnt lgkmcnt(10)\n\t"
        "v_mfma_f32_32x32x16_bf16 v[32:47], v[104:107], v[184:187], v[32:47]\n\t"
        "ds_read_b64_tr_b16 v[104:105], v123 offset:6144\n\t"
        "ds_read_b64_tr_b16 v[106:107], v123 offset:9216\n\t"
        "v_mul_f32 v91, v207, v91\n\t"
        "v_mul_f32 v92, v208, v92\n\t"
        "v_mul_f32 v93, v209, v93\n\t"
        "s_waitcnt lgkmcnt(10)\n\t"
        "v_mfma_f32_32x32x16_bf16 v[48:63], v[108:111], v[184:187], v[48:63]\n\t"
        "ds_read_b64_tr_b16 v[108:109], v124 offset:6144\n\t"
        "ds_read_b64_tr_b16 v[110:111], v124 offset:9216\n\t"
        "v_mul_f32 v94, v210, v94\n\t"
        "v_mul_f32 v95, v211, v95\n\t"
        "v_cvt_pk_bf16_f32 v84, v88, v89\n\t"
        "s_waitcnt lgkmcnt(10)\n\t"
        "v_mfma_f32_32x32x16_bf16 v[32:47], v[112:115], v[188:191], v[32:47]\n\t"
        "ds_read_b64_tr_b16 v[112:113], v121 offset:0\n\t"
        "ds_read_b64_tr_b16 v[114:115], v121 offset:3072\n\t"
        "v_cvt_pk_bf16_f32 v85, v90, v91\n\t"
        "v_cvt_pk_bf16_f32 v86, v92, v93\n\t"
        "v_cvt_pk_bf16_f32 v87, v94, v95\n\t"
        "s_waitcnt lgkmcnt(10)\n\t"
        "v_mfma_f32_32x32x16_bf16 v[48:63], v[116:119], v[188:191], v[48:63]\n\t"
        "ds_read_b64_tr_b16 v[116:117], v122 offset:0\n\t"
        "ds_read_b64_tr_b16 v[118:119], v122 offset:3072\n\t"
        "v_permlane32_swap_b32 v80, v82\n\t"
        "v_permlane32_swap_b32 v81, v83\n\t"
        "v_permlane32_swap_b32 v84, v86\n\t"
        "s_waitcnt lgkmcnt(10)\n\t"
        "v_mfma_f32_32x32x16_bf16 v[32:47], v[96:99], v[192:195], v[32:47]\n\t"
        "ds_read_b64_tr_b16 v[96:97], v121 offset:6144\n\t"
        "ds_read_b64_tr_b16 v[98:99], v121 offset:9216\n\t"
        "v_permlane32_swap_b32 v85, v87\n\t"
        "buffer_store_dwordx4 v[80:83], v214, s[40:43], 0 offen offset:2048 sc1\n\t"
        "buffer_store_dwordx4 v[84:87], v214, s[40:43], 0 offen offset:3072 sc1\n\t"
        "s_waitcnt lgkmcnt(10)\n\t"
        "v_mfma_f32_32x32x16_bf16 v[48:63], v[100:103], v[192:195], v[48:63]\n\t"
        "ds_read_b64_tr_b16 v[100:101], v122 offset:6144\n\t"
        "ds_read_b64_tr_b16 v[102:103], v122 offset:9216\n\t"
        "s_waitcnt lgkmcnt(10)\n\t"
        "v_mfma_f32_32x32x16_bf16 v[0:15], v[104:107], v[152:155], v[0:15]\n\t"
        "ds_read_b64_tr_b16 v[104:105], v121 offset:12288\n\t"
        "ds_read_b64_tr_b16 v[106:107], v121 offset:15360\n\t"
        "s_waitcnt lgkmcnt(10)\n\t"
        "v_mfma_f32_32x32x16_bf16 v[16:31], v[108:111], v[152:155], v[16:31]\n\t"
        "ds_read_b64_tr_b16 v[108:109], v122 offset:12288\n\t"
        "ds_read_b64_tr_b16 v[110:111], v122 offset:15360\n\t"
        "v_mul_f32 v32, v212, v32\n\t"
        "v_mul_f32 v33, v212, v33\n\t"
        "v_mul_f32 v34, v212, v34\n\t"
        "s_waitcnt lgkmcnt(10)\n\t"
        "v_mfma_f32_32x32x16_bf16 v[0:15], v[112:115], v[156:159], v[0:15]\n\t"
        "ds_read_b64_tr_b16 v[112:113], v121 offset:18432\n\t"
        "ds_read_b64_tr_b16 v[114:115], v121 offset:21504\n\t"
        "v_mul_f32 v35, v212, v35\n\t"
        "v_mul_f32 v36, v212, v36\n\t"
        "v_mul_f32 v37, v212, v37\n\t"
        "s_waitcnt lgkmcnt(10)\n\t"
        "v_mfma_f32_32x32x16_bf16 v[16:31], v[116:119], v[156:159], v[16:31]\n\t"
        "ds_read_b64_tr_b16 v[116:117], v122 offset:18432\n\t"
        "ds_read_b64_tr_b16 v[118:119], v122 offset:21504\n\t"
        "v_mul_f32 v38, v212, v38\n\t"
        "v_mul_f32 v39, v212, v39\n\t"
        "v_cvt_pk_bf16_f32 v32, v32, v33\n\t"
        "s_waitcnt lgkmcnt(10)\n\t"
        "v_mfma_f32_32x32x16_bf16 v[0:15], v[96:99], v[160:163], v[0:15]\n\t"
        "ds_read_b64_tr_b16 v[96:97], v121 offset:24576\n\t"
        "ds_read_b64_tr_b16 v[98:99], v121 offset:27648\n\t"
        "v_cvt_pk_bf16_f32 v33, v34, v35\n\t"
        "v_cvt_pk_bf16_f32 v34, v36, v37\n\t"
        "v_cvt_pk_bf16_f32 v35, v38, v39\n\t"
        "s_waitcnt lgkmcnt(10)\n\t"
        "v_mfma_f32_32x32x16_bf16 v[16:31], v[100:103], v[160:163], v[16:31]\n\t"
        "ds_read_b64_tr_b16 v[100:101], v122 offset:24576\n\t"
        "ds_read_b64_tr_b16 v[102:103], v122 offset:27648\n\t"
        "v_mul_f32 v40, v212, v40\n\t"
        "v_mul_f32 v41, v212, v41\n\t"
        "v_mul_f32 v42, v212, v42\n\t"
        "s_waitcnt lgkmcnt(10)\n\t"
        "v_mfma_f32_32x32x16_bf16 v[0:15], v[104:107], v[164:167], v[0:15]\n\t"
        "ds_read_b64_tr_b16 v[104:105], v121 offset:30720\n\t"
        "ds_read_b64_tr_b16 v[106:107], v121 offset:33792\n\t"
        "v_mul_f32 v43, v212, v43\n\t"
        "v_mul_f32 v44, v212, v44\n\t"
        "v_mul_f32 v45, v212, v45\n\t"
        "s_waitcnt lgkmcnt(10)\n\t"
        "v_mfma_f32_32x32x16_bf16 v[16:31], v[108:111], v[164:167], v[16:31]\n\t"
        "ds_read_b64_tr_b16 v[108:109], v122 offset:30720\n\t"
        "ds_read_b64_tr_b16 v[110:111], v122 offset:33792\n\t"
        "v_mul_f32 v46, v212, v46\n\t"
        "v_mul_f32 v47, v212, v47\n\t"
        "v_cvt_pk_bf16_f32 v36, v40, v41\n\t"
        "s_waitcnt lgkmcnt(10)\n\t"
        "v_mfma_f32_32x32x16_bf16 v[0:15], v[112:115], v[168:171], v[0:15]\n\t"
        "ds_read_b64_tr_b16 v[112:113], v121 offset:36864\n\t"
        "ds_read_b64_tr_b16 v[114:115], v121 offset:39936\n\t"
        "v_cvt_pk_bf16_f32 v37, v42, v43\n\t"
        "v_cvt_pk_bf16_f32 v38, v44, v45\n\t"
        "v_cvt_pk_bf16_f32 v39, v46, v47\n\t"
        "s_waitcnt lgkmcnt(10)\n\t"
        "v_mfma_f32_32x32x16_bf16 v[16:31], v[116:119], v[168:171], v[16:31]\n\t"
        "ds_read_b64_tr_b16 v[116:117], v122 offset:36864\n\t"
        "ds_read_b64_tr_b16 v[118:119], v122 offset:39936\n\t"
        "buffer_store_dwordx4 v[32:35], v213, s[40:43], 0 offen offset:0 sc1\n\t"
        "buffer_store_dwordx4 v[36:39], v213, s[40:43], 0 offen offset:1024 sc1\n\t"
        "v_mul_f32 v48, v212, v48\n\t"
        "s_waitcnt lgkmcnt(10)\n\t"
        "v_mfma_f32_32x32x16_bf16 v[0:15], v[96:99], v[172:175], v[0:15]\n\t"
        "ds_read_b64_tr_b16 v[96:97], v121 offset:43008\n\t"
        "ds_read_b64_tr_b16 v[98:99], v121 offset:46080\n\t"
        "v_mul_f32 v49, v212, v49\n\t"
        "v_mul_f32 v50, v212, v50\n\t"
        "v_mul_f32 v51, v212, v51\n\t"
        "s_waitcnt lgkmcnt(10)\n\t"
        "v_mfma_f32_32x32x16_bf16 v[16:31], v[100:103], v[172:175], v[16:31]\n\t"
        "ds_read_b64_tr_b16 v[100:101], v122 offset:43008\n\t"
        "ds_read_b64_tr_b16 v[102:103], v122 offset:46080\n\t"
        "v_mul_f32 v52, v212, v52\n\t"
        "v_mul_f32 v53, v212, v53\n\t"
        "v_mul_f32 v54, v212, v54\n\t"
        "s_waitcnt lgkmcnt(10)\n\t"
        "v_mfma_f32_32x32x16_bf16 v[0:15], v[104:107], v[176:179], v[0:15]\n\t"
        "ds_read_b64_tr_b16 v[104:105], v121 offset:49152\n\t"
        "ds_read_b64_tr_b16 v[106:107], v121 offset:52224\n\t"
        "v_mul_f32 v55, v212, v55\n\t"
        "v_cvt_pk_bf16_f32 v48, v48, v49\n\t"
        "v_cvt_pk_bf16_f32 v49, v50, v51\n\t"
        "s_waitcnt lgkmcnt(10)\n\t"
        "v_mfma_f32_32x32x16_bf16 v[16:31], v[108:111], v[176:179], v[16:31]\n\t"
        "ds_read_b64_tr_b16 v[108:109], v122 offset:49152\n\t"
        "ds_read_b64_tr_b16 v[110:111], v122 offset:52224\n\t"
        "v_cvt_pk_bf16_f32 v50, v52, v53\n\t"
        "v_cvt_pk_bf16_f32 v51, v54, v55\n\t"
        "v_mul_f32 v56, v212, v56\n\t"
        "s_waitcnt lgkmcnt(10)\n\t"
        "v_mfma_f32_32x32x16_bf16 v[0:15], v[112:115], v[180:183], v[0:15]\n\t"
        "ds_read_b64_tr_b16 v[112:113], v121 offset:55296\n\t"
        "ds_read_b64_tr_b16 v[114:115], v121 offset:58368\n\t"
        "v_mul_f32 v57, v212, v57\n\t"
        "v_mul_f32 v58, v212, v58\n\t"
        "v_mul_f32 v59, v212, v59\n\t"
        "s_waitcnt lgkmcnt(10)\n\t"
        "v_mfma_f32_32x32x16_bf16 v[16:31], v[116:119], v[180:183], v[16:31]\n\t"
        "ds_read_b64_tr_b16 v[116:117], v122 offset:55296\n\t"
        "ds_read_b64_tr_b16 v[118:119], v122 offset:58368\n\t"
        "v_mul_f32 v60, v212, v60\n\t"
        "v_mul_f32 v61, v212, v61\n\t"
        "v_mul_f32 v62, v212, v62\n\t"
        "s_waitcnt lgkmcnt(10)\n\t"
        "v_mfma_f32_32x32x16_bf16 v[0:15], v[96:99], v[184:187], v[0:15]\n\t"
        "v_mul_f32 v63, v212, v63\n\t"
        "v_cvt_pk_bf16_f32 v52, v56, v57\n\t"
        "v_cvt_pk_bf16_f32 v53, v58, v59\n\t"
        "v_cvt_pk_bf16_f32 v54, v60, v61\n\t"
        "v_cvt_pk_bf16_f32 v55, v62, v63\n\t"
        "s_waitcnt lgkmcnt(8)\n\t"
        "v_mfma_f32_32x32x16_bf16 v[16:31], v[100:103], v[184:187], v[16:31]\n\t"
        "buffer_store_dwordx4 v[48:51], v214, s[40:43], 0 offen offset:0 sc1\n\t"
        "buffer_store_dwordx4 v[52:55], v214, s[40:43], 0 offen offset:1024 sc1\n\t"
        "s_waitcnt lgkmcnt(6)\n\t"
        "v_mfma_f32_32x32x16_bf16 v[0:15], v[104:107], v[188:191], v[0:15]\n\t"
        "s_waitcnt lgkmcnt(4)\n\t"
        "v_mfma_f32_32x32x16_bf16 v[16:31], v[108:111], v[188:191], v[16:31]\n\t"
        "s_waitcnt lgkmcnt(2)\n\t"
        "v_mfma_f32_32x32x16_bf16 v[0:15], v[112:115], v[192:195], v[0:15]\n\t"
        "s_waitcnt lgkmcnt(0)\n\t"
        "v_mfma_f32_32x32x16_bf16 v[16:31], v[116:119], v[192:195], v[16:31]\n\t"
        "s_nop 11\n\t"
        "s_branch 9f\n\t"
        "16:\n\t"
        "s_mov_b32 s48, 0x10000\n\t"
        "8:\n\t"
        "ds_read_b32 v126, v125\n\t"
        "s_sub_u32 s48, s48, 1\n\t"
        "s_waitcnt lgkmcnt(0)\n\t"
        "s_cbranch_scc1 17b\n\t"
        "v_cmp_gt_u32 vcc, s47, v126\n\t"
        "s_cbranch_vccnz 8b\n\t"
        "s_branch 17b\n\t"
        "18:\n\t"
        "s_mov_b32 s48, 0x10000\n\t"
        "8:\n\t"
        "ds_read_b32 v126, v125\n\t"
        "s_sub_u32 s48, s48, 1\n\t"
        "s_waitcnt lgkmcnt(0)\n\t"
        "s_cbranch_scc1 19b\n\t"
        "v_cmp_gt_u32 vcc, s47, v126\n\t"
        "s_cbranch_vccnz 8b\n\t"
        "s_branch 19b\n\t"
        "20:\n\t"
        "s_mov_b32 s48, 0x10000\n\t"
        "8:\n\t"
        "ds_read_b32 v126, v125\n\t"
        "s_sub_u32 s48, s48, 1\n\t"
        "s_waitcnt lgkmcnt(0)\n\t"
        "s_cbranch_scc1 21b\n\t"
        "v_cmp_gt_u32 vcc, s47, v126\n\t"
        "s_cbranch_vccnz 8b\n\t"
        "s_branch 21b\n\t"
        "22:\n\t"
        "s_mov_b32 s48, 0x10000\n\t"
        "8:\n\t"
        "ds_read_b32 v126, v125\n\t"
        "s_sub_u32 s48, s48, 1\n\t"
        "s_waitcnt lgkmcnt(0)\n\t"
        "s_cbranch_scc1 23b\n\t"
        "v_cmp_gt_u32 vcc, s47, v126\n\t"
        "s_cbranch_vccnz 8b\n\t"
        "s_branch 23b\n\t"
        "24:\n\t"
        "s_mov_b32 s48, 0x10000\n\t"
        "8:\n\t"
        "ds_read_b32 v126, v125\n\t"
        "s_sub_u32 s48, s48, 1\n\t"
        "s_waitcnt lgkmcnt(0)\n\t"
        "s_cbranch_scc1 25b\n\t"
        "v_cmp_gt_u32 vcc, s47, v126\n\t"
        "s_cbranch_vccnz 8b\n\t"
        "s_branch 25b\n\t"
        "26:\n\t"
        "s_mov_b32 s48, 0x10000\n\t"
        "8:\n\t"
        "ds_read_b32 v126, v125\n\t"
        "s_sub_u32 s48, s48, 1\n\t"
        "s_waitcnt lgkmcnt(0)\n\t"
        "s_cbranch_scc1 27b\n\t"
        "v_cmp_gt_u32 vcc, s47, v126\n\t"
        "s_cbranch_vccnz 8b\n\t"
        "s_branch 27b\n\t"
        "28:\n\t"
        "s_mov_b32 s48, 0x10000\n\t"
        "8:\n\t"
        "ds_read_b32 v126, v125\n\t"
        "s_sub_u32 s48, s48, 1\n\t"
        "s_waitcnt lgkmcnt(0)\n\t"
        "s_cbranch_scc1 29b\n\t"
        "v_cmp_gt_u32 vcc, s47, v126\n\t"
        "s_cbranch_vccnz 8b\n\t"
        "s_branch 29b\n\t"
        "30:\n\t"
        "s_mov_b32 s48, 0x10000\n\t"
        "8:\n\t"
        "ds_read_b32 v126, v125\n\t"
        "s_sub_u32 s48, s48, 1\n\t"
        "s_waitcnt lgkmcnt(0)\n\t"
        "s_cbranch_scc1 31b\n\t"
        "v_cmp_gt_u32 vcc, s47, v126\n\t"
        "s_cbranch_vccnz 8b\n\t"
        "s_branch 31b\n\t"
        "74:\n\t"
        "s_mov_b32 s48, 0x10000\n\t"
        "8:\n\t"
        "ds_read_b32 v126, v125\n\t"
        "s_sub_u32 s48, s48, 1\n\t"
        "s_waitcnt lgkmcnt(0)\n\t"
        "s_cbranch_scc1 75b\n\t"
        "v_cmp_gt_u32 vcc, s47, v126\n\t"
        "s_cbranch_vccnz 8b\n\t"
        "s_branch 75b\n\t"
        "9:\n\t"
        : "+{v[0:15]}"(acc[0]), "+{v[16:31]}"(acc[1]), "+{v[32:47]}"(acc[2]), "+{v[48:63]}"(acc[3]), "+{v[64:79]}"(acc[4]), "+{v[80:95]}"(acc[5]),
          "+{v[96:111]}"(stA), "+{v[112:127]}"(stB), "+{v[128:131]}"(stC), "+{v[148:163]}"(xr1), "+{v[164:179]}"(xr2), "+{v[180:195]}"(xr3),
          "+{s44}"(soff), "+{s46}"(slot), "+{s47}"(need)
        : "{s[40:43]}"(srd), "{v[212:215]}"(ep) : "v196", "v197", "v198", "v199", "v200", "v201", "v202", "v203", "v204", "v205", "v206", "v207", "v208", "v209", "v210", "v211", "v132", "v133", "v134", "v135", "v136", "v137", "v138", "v139", "v140", "v141", "v142", "v143", "v144", "v145", "v146", "v147", "s45", "s48", "vcc", "scc", "memory");
      STAMPV(4 + 16 * L);
      PHASE_VARS;
      u32x4 kv[4][2];
#pragma unroll
      for (int j = 0; j < 4; ++j)
#pragma unroll
        for (int e = 0; e < 8; ++e) kv[j][e >> 2][e & 3] = __float_as_uint(acc[2 + j][e]);
      constexpr float QS = 0.17677669529663687f * 1.4426950408889634f;
      u32x4 qo[2][2];
      auto drained = [&]() { asm volatile("s_waitcnt vmcnt(0)\n\tds_write_b32 %0, %1" :: "v"(lbase + L_W + 80 + 4 * wid), "v"(L + 1) : "memory"); };
      if (L == 0) {
#pragma unroll
        for (int e = 0; e < 16; ++e) {
          if (e == 8) { SBAR(); drained(); SBAR(); }
          const unsigned w = pack2(acc[e >> 3][2 * (e & 7)], acc[e >> 3][2 * (e & 7) + 1]);
          qo[e >> 3][(e >> 2) & 1][e & 3] = pack2(lo16(w) * QS, hi16(w) * QS);
        }
      } else {
        const float aq = U2F(ep[0]) * QS;
#pragma unroll
        for (int e = 0; e < 16; ++e) qo[e >> 3][(e >> 2) & 1][e & 3] = pack2(acc[e >> 3][2 * (e & 7)] * aq, acc[e >> 3][2 * (e & 7) + 1] * aq);
        SBAR();
        lds_wait_ge<0>(lbase + L_W + 4 + 4 * (lane & 1), 1);
        const int idx = (int)__builtin_amdgcn_readfirstlane(words[3]);
        if ((idx >> 5) == tile) {
          f32x4 cs[4][4];
          float cv[2];
#pragma unroll
          for (int j = 0; j < 6; ++j) {
            const int cb = (j >> 1) * 64 + ((j & 1) ^ hh) * 32;
            if (j >= 4) cv[j - 4] = *reinterpret_cast<const float*>(smem + L_PATCH + (cb + r32) * 4);
            else {
#pragma unroll
              for (int g = 0; g < 4; ++g) cs[j][g] = *reinterpret_cast<const f32x4*>(smem + L_PATCH + (cb + 8 * g + 4 * hi) * 4);
            }
          }
          SBAR();
          const bool me = r32 == (idx & 31);
          const int rel = (idx & 31) - 8 * hi;
#pragma unroll
          for (int j = 0; j < 2; ++j) {
            const unsigned pv = pack2(cv[j], cv[j]);
#pragma unroll
            for (int e = 0; e < 8; ++e) {
              unsigned a = pack2(cs[j][e >> 1][2 * (e & 1)] * QS, cs[j][e >> 1][2 * (e & 1) + 1] * QS), b = pack2(cs[2 + j][e >> 1][2 * (e & 1)], cs[2 + j][e >> 1][2 * (e & 1) + 1]);
              PIN(a); PIN(b);
              qo[j][e >> 2][e & 3] = me ? a : qo[j][e >> 2][e & 3];
              kv[j][e >> 2][e & 3] = me ? b : kv[j][e >> 2][e & 3];
              const int t0 = 16 * (e >> 2) + 2 * (e & 3);
              const unsigned mask = rel == t0 ? 0xffffu : rel == t0 + 1 ? 0xffff0000u : 0u;
              kv[2 + j][e >> 2][e & 3] = (kv[2 + j][e >> 2][e & 3] & ~mask) | (pv & mask);
            }
          }
          DRAIN();
#pragma unroll
          for (int j = 0; j < 4; ++j) { stg(rs, ep[1 + (j & 1)] + (j >> 1) * 2048, kv[j][0]); stg(rs, ep[1 + (j & 1)] + (j >> 1) * 2048 + 1024, kv[j][1]); }
        }
        drained();
      }
      SBAR();
#pragma unroll
      for (int j = 0; j < 2; ++j) {
        char* p = smem + L_XCH + ((j ^ hh) * 4 + wid) * 2048 + lane * 16;
        *reinterpret_cast<u32x4*>(p) = qo[j][0]; *reinterpret_cast<u32x4*>(p + 1024) = qo[j][1];
        p = smem + L_XCH + 16384 + wid * 4096 + j * 2048 + lane * 16;
        *reinterpret_cast<u32x4*>(p) = kv[2 * j + 1][0]; *reinterpret_cast<u32x4*>(p + 1024) = kv[2 * j + 1][1];
      }
      STAMPV(41 + 4 * L);
      BAR_LGKM();
      STAMPV(43 + 4 * L);
      kva = cat4(kv[0][0], kv[0][1], kv[2][0], kv[2][1]);
      const u32x4* pk = reinterpret_cast<const u32x4*>(smem + L_XCH + 16384 + (wid ^ 1) * 4096 + lane * 16);
#pragma unroll
      for (int u = 0; u < 4; ++u) {
        const u32x4* p = reinterpret_cast<const u32x4*>(smem + L_XCH + (hh * 4 + ((2 * kh + 2 + u) & 3)) * 2048 + lane * 16);
        q[u] = __builtin_shufflevector(p[0], p[64], 0, 1, 2, 3, 4, 5, 6, 7);
      }
      kvb = cat4(pk[0], pk[64], pk[128], pk[192]);
      SBAR();
      asm volatile(
        "v_mov_b64 v[0:1], 0\n\t"
        "v_mov_b64 v[2:3], 0\n\t"
        "v_mov_b64 v[4:5], 0\n\t"
        "v_mov_b64 v[6:7], 0\n\t"
        "v_mov_b64 v[8:9], 0\n\t"
        "v_mov_b64 v[10:11], 0\n\t"
        "v_mov_b64 v[12:13], 0\n\t"
        "v_mov_b64 v[14:15], 0\n\t"
        "v_mov_b64 v[16:17], 0\n\t"
        "v_mov_b64 v[18:19], 0\n\t"
        "v_mov_b64 v[20:21], 0\n\t"
        "v_mov_b64 v[22:23], 0\n\t"
        "v_mov_b64 v[24:25], 0\n\t"
        "v_mov_b64 v[26:27], 0\n\t"
        "v_mov_b64 v[28:29], 0\n\t"
        "v_mov_b64 v[30:31], 0\n\t"
        "v_mov_b64 v[32:33], 0\n\t"
        "v_mov_b64 v[34:35], 0\n\t"
        "v_mov_b64 v[36:37], 0\n\t"
        "v_mov_b64 v[38:39], 0\n\t"
        "v_mov_b64 v[40:41], 0\n\t"
        "v_mov_b64 v[42:43], 0\n\t"
        "v_mov_b64 v[44:45], 0\n\t"
        "v_mov_b64 v[46:47], 0\n\t"
        "v_mov_b64 v[48:49], 0\n\t"
        "v_mov_b64 v[50:51], 0\n\t"
        "v_mov_b64 v[52:53], 0\n\t"
        "v_mov_b64 v[54:55], 0\n\t"
        "v_mov_b64 v[56:57], 0\n\t"
        "v_mov_b64 v[58:59], 0\n\t"
        "v_mov_b64 v[60:61], 0\n\t"
        "v_mov_b64 v[62:63], 0\n\t"
        "v_mov_b64 v[128:129], 0\n\t"
        "v_mov_b64 v[130:131], 0\n\t"
        "v_mov_b64 v[132:133], 0\n\t"
        "v_mov_b64 v[134:135], 0\n\t"
        "v_mov_b64 v[136:137], 0\n\t"
        "v_mov_b64 v[138:139], 0\n\t"
        "v_mov_b64 v[140:141], 0\n\t"
        "v_mov_b64 v[142:143], 0\n\t"
        : "={v[0:15]}"(oz[0]), "={v[16:31]}"(oz[1]), "={v[32:47]}"(oz[2]), "={v[48:63]}"(oz[3]), "={v[128:143]}"(oz[4]));
    }
    STAMPV(5 + 16 * L);
    {
      u32x4 ones;
      unsigned kvoff;
      u32x8 fv, cv;
      asm volatile("" : "={v[184:191]}"(fv), "={v[216:223]}"(cv));
      { PHASE_VARS;
        const unsigned v = (lane & 3) == 0 ? 0x3F803F80u : 0u; const bool up = (lane & 4) != 0; ones[0] = ones[1] = up ? 0u : v; ones[2] = ones[3] = up ? v : 0u;
        kvoff = OFF_KV + L * (10 * MBY) + (tn * 2 + hh) * HS + kh * 8192 + lane * 16;
        fv[1] = lbase + L_FREE + wid * 256 + lane * 4; fv[2] = 32 * L + 32;
        cv[0] = lbase + L_XCH + wid * 8192 + lane * 16; cv[1] = lbase + L_XCH + (wid ^ 2) * 8192 + lane * 16;
        cv[2] = lbase + L_XCH + 32768 + wid * 512 + lane * 4; cv[3] = lbase + L_XCH + 32768 + (wid ^ 2) * 512 + lane * 4;
        cv[4] = lbase + L_W + 48 + wid * 4; cv[5] = lbase + L_W + 48 + (wid ^ 2) * 4; cv[6] = L + 1;
        cv[7] = ((tile * 32 + r32) * D_ + tn * 64 + hi * 8) * 2; }
      f32x16 o0 = oz[0], o1 = oz[1], o2 = oz[2], o3 = oz[3], lv = oz[4];
      unsigned soff = tm * 16384;
      STAMP_DECL;
      asm volatile(
        "s_nop 1\n\t"
        "v_mfma_f32_32x32x16_bf16 v[96:111], v[152:155], v[64:67], 0\n\t"
        "v_mfma_f32_32x32x16_bf16 v[96:111], v[156:159], v[68:71], v[96:111]\n\t"
        "s_mov_b32 s46, 0\n\t"
        "ds_write_b32 v185, v186\n\t"
        "s_nop 9\n\t"
        "v_exp_f32 v96, v96\n\t"
        "v_exp_f32 v97, v97\n\t"
        "v_exp_f32 v98, v98\n\t"
        "v_exp_f32 v99, v99\n\t"
        "v_exp_f32 v100, v100\n\t"
        "v_exp_f32 v101, v101\n\t"
        "v_mfma_f32_32x32x16_bf16 v[112:127], v[152:155], v[72:75], 0\n\t"
        "v_exp_f32 v102, v102\n\t"
        "v_exp_f32 v103, v103\n\t"
        "v_exp_f32 v104, v104\n\t"
        "v_mfma_f32_32x32x16_bf16 v[112:127], v[156:159], v[76:79], v[112:127]\n\t"
        "v_exp_f32 v105, v105\n\t"
        "v_exp_f32 v106, v106\n\t"
        "v_exp_f32 v107, v107\n\t"
        "v_smfmac_f32_32x32x32_bf16 v[0:15], v[160:163], v[96:103], v148\n\t"
        "v_exp_f32 v108, v108\n\t"
        "v_exp_f32 v109, v109\n\t"
        "v_exp_f32 v110, v110\n\t"
        "v_smfmac_f32_16x16x64_bf16 v[128:131], v[144:147], v[96:103], v148\n\t"
        "v_exp_f32 v111, v111\n\t"
        "v_exp_f32 v112, v112\n\t"
        "v_exp_f32 v113, v113\n\t"
        "v_smfmac_f32_32x32x32_bf16 v[0:15], v[164:167], v[104:111], v148\n\t"
        "v_exp_f32 v114, v114\n\t"
        "v_exp_f32 v115, v115\n\t"
        "v_exp_f32 v116, v116\n\t"
        "v_smfmac_f32_16x16x64_bf16 v[128:131], v[144:147], v[104:111], v148\n\t"
        "v_exp_f32 v117, v117\n\t"
        "v_mfma_f32_32x32x16_bf16 v[96:111], v[152:155], v[80:83], 0\n\t"
        "v_exp_f32 v118, v118\n\t"
        "v_exp_f32 v119, v119\n\t"
        "v_exp_f32 v120, v120\n\t"
        "ds_read_b32 v151, v224\n\t"
        "v_mfma_f32_32x32x16_bf16 v[96:111], v[156:159], v[84:87], v[96:111]\n\t"
        "v_exp_f32 v121, v121\n\t"
        "v_exp_f32 v122, v122\n\t"
        "v_exp_f32 v123, v123\n\t"
        "v_smfmac_f32_32x32x32_bf16 v[16:31], v[160:163], v[112:119], v148\n\t"
        "v_exp_f32 v124, v124\n\t"
        "v_exp_f32 v125, v125\n\t"
        "v_exp_f32 v126, v126\n\t"
        "v_smfmac_f32_16x16x64_bf16 v[132:135], v[144:147], v[112:119], v148\n\t"
        "v_exp_f32 v127, v127\n\t"
        "s_cmp_lg_u32 s46, 0\n\t"
        "s_cbranch_scc1 25f\n\t"
        "s_waitcnt lgkmcnt(0)\n\t"
        "v_cmp_ne_u32 vcc, v222, v151\n\t"
        "s_cbranch_vccz 24f\n\t"
        "25:\n\t"
        "v_exp_f32 v96, v96\n\t"
        "v_exp_f32 v97, v97\n\t"
        "v_smfmac_f32_32x32x32_bf16 v[16:31], v[164:167], v[120:127], v148\n\t"
        "v_exp_f32 v98, v98\n\t"
        "v_exp_f32 v99, v99\n\t"
        "v_exp_f32 v100, v100\n\t"
        "v_smfmac_f32_16x16x64_bf16 v[132:135], v[144:147], v[120:127], v148\n\t"
        "v_exp_f32 v101, v101\n\t"
        "v_mfma_f32_32x32x16_bf16 v[112:127], v[152:155], v[88:91], 0\n\t"
        "v_exp_f32 v102, v102\n\t"
        "v_exp_f32 v103, v103\n\t"
        "v_exp_f32 v104, v104\n\t"
        "ds_read_b32 v151, v224\n\t"
        "v_mfma_f32_32x32x16_bf16 v[112:127], v[156:159], v[92:95], v[112:127]\n\t"
        "v_exp_f32 v105, v105\n\t"
        "v_exp_f32 v106, v106\n\t"
        "v_exp_f32 v107, v107\n\t"
        "v_smfmac_f32_32x32x32_bf16 v[32:47], v[160:163], v[96:103], v148\n\t"
        "v_exp_f32 v108, v108\n\t"
        "v_exp_f32 v109, v109\n\t"
        "v_exp_f32 v110, v110\n\t"
        "v_smfmac_f32_16x16x64_bf16 v[136:139], v[144:147], v[96:103], v148\n\t"
        "v_exp_f32 v111, v111\n\t"
        "s_cmp_lg_u32 s46, 0\n\t"
        "s_cbranch_scc1 27f\n\t"
        "s_waitcnt lgkmcnt(0)\n\t"
        "v_cmp_ne_u32 vcc, v222, v151\n\t"
        "s_cbranch_vccz 26f\n\t"
        "27:\n\t"
        "v_exp_f32 v112, v112\n\t"
        "v_exp_f32 v113, v113\n\t"
        "v_smfmac_f32_32x32x32_bf16 v[32:47], v[164:167], v[104:111], v148\n\t"
        "v_exp_f32 v114, v114\n\t"
        "v_exp_f32 v115, v115\n\t"
        "v_exp_f32 v116, v116\n\t"
        "v_smfmac_f32_16x16x64_bf16 v[136:139], v[144:147], v[104:111], v148\n\t"
        "v_exp_f32 v117, v117\n\t"
        "v_mfma_f32_32x32x16_bf16 v[96:111], v[168:171], v[64:67], 0\n\t"
        "v_exp_f32 v118, v118\n\t"
        "v_exp_f32 v119, v119\n\t"
        "v_exp_f32 v120, v120\n\t"
        "ds_read_b32 v151, v224\n\t"
        "v_mfma_f32_32x32x16_bf16 v[96:111], v[172:175], v[68:71], v[96:111]\n\t"
        "v_exp_f32 v121, v121\n\t"
        "v_exp_f32 v122, v122\n\t"
        "v_exp_f32 v123, v123\n\t"
        "v_smfmac_f32_32x32x32_bf16 v[48:63], v[160:163], v[112:119], v148\n\t"
        "v_exp_f32 v124, v124\n\t"
        "v_exp_f32 v125, v125\n\t"
        "v_exp_f32 v126, v126\n\t"
        "v_smfmac_f32_16x16x64_bf16 v[140:143], v[144:147], v[112:119], v148\n\t"
        "v_exp_f32 v127, v127\n\t"
        "s_cmp_lg_u32 s46, 0\n\t"
        "s_cbranch_scc1 29f\n\t"
        "s_waitcnt lgkmcnt(0)\n\t"
        "v_cmp_ne_u32 vcc, v222, v151\n\t"
        "s_cbranch_vccz 28f\n\t"
        "29:\n\t"
        "v_exp_f32 v96, v96\n\t"
        "v_exp_f32 v97, v97\n\t"
        "v_smfmac_f32_32x32x32_bf16 v[48:63], v[164:167], v[120:127], v148\n\t"
        "v_exp_f32 v98, v98\n\t"
        "v_exp_f32 v99, v99\n\t"
        "v_exp_f32 v100, v100\n\t"
        "v_smfmac_f32_16x16x64_bf16 v[140:143], v[144:147], v[120:127], v148\n\t"
        "v_exp_f32 v101, v101\n\t"
        "v_mfma_f32_32x32x16_bf16 v[112:127], v[168:171], v[72:75], 0\n\t"
        "v_exp_f32 v102, v102\n\t"
        "v_exp_f32 v103, v103\n\t"
        "v_exp_f32 v104, v104\n\t"
        "ds_read_b32 v151, v224\n\t"
        "v_mfma_f32_32x32x16_bf16 v[112:127], v[172:175], v[76:79], v[112:127]\n\t"
        "v_exp_f32 v105, v105\n\t"
        "v_exp_f32 v106, v106\n\t"
        "v_exp_f32 v107, v107\n\t"
        "v_smfmac_f32_32x32x32_bf16 v[0:15], v[176:179], v[96:103], v148\n\t"
        "v_exp_f32 v108, v108\n\t"
        "v_exp_f32 v109, v109\n\t"
        "v_exp_f32 v110, v110\n\t"
        "v_smfmac_f32_16x16x64_bf16 v[128:131], v[144:147], v[96:103], v148\n\t"
        "v_exp_f32 v111, v111\n\t"
        "s_cmp_lg_u32 s46, 0\n\t"
        "s_cbranch_scc1 31f\n\t"
        "s_waitcnt lgkmcnt(0)\n\t"
        "v_cmp_ne_u32 vcc, v222, v151\n\t"
        "s_cbranch_vccz 30f\n\t"
        "31:\n\t"
        "v_exp_f32 v112, v112\n\t"
        "v_exp_f32 v113, v113\n\t"
        "v_smfmac_f32_32x32x32_bf16 v[0:15], v[180:183], v[104:111], v148\n\t"
        "v_exp_f32 v114, v114\n\t"
        "v_exp_f32 v115, v115\n\t"
        "v_exp_f32 v116, v116\n\t"
        "v_smfmac_f32_16x16x64_bf16 v[128:131], v[144:147], v[104:111], v148\n\t"
        "v_exp_f32 v117, v117\n\t"
        "v_mfma_f32_32x32x16_bf16 v[96:111], v[168:171], v[80:83], 0\n\t"
        "v_exp_f32 v118, v118\n\t"
        "v_exp_f32 v119, v119\n\t"
        "v_exp_f32 v120, v120\n\t"
        "ds_read_b32 v151, v224\n\t"
        "v_mfma_f32_32x32x16_bf16 v[96:111], v[172:175], v[84:87], v[96:111]\n\t"
        "v_exp_f32 v121, v121\n\t"
        "v_exp_f32 v122, v122\n\t"
        "v_exp_f32 v123, v123\n\t"
        "v_smfmac_f32_32x32x32_bf16 v[16:31], v[176:179], v[112:119], v148\n\t"
        "v_exp_f32 v124, v124\n\t"
        "v_exp_f32 v125, v125\n\t"
        "v_exp_f32 v126, v126\n\t"
        "v_smfmac_f32_16x16x64_bf16 v[132:135], v[144:147], v[112:119], v148\n\t"
        "v_exp_f32 v127, v127\n\t"
        "s_cmp_lg_u32 s46, 0\n\t"
        "s_cbranch_scc1 33f\n\t"
        "s_waitcnt lgkmcnt(0)\n\t"
        "v_cmp_ne_u32 vcc, v222, v151\n\t"
        "s_cbranch_vccz 32f\n\t"
        "33:\n\t"
        "v_exp_f32 v96, v96\n\t"
        "v_exp_f32 v97, v97\n\t"
        "v_smfmac_f32_32x32x32_bf16 v[16:31], v[180:183], v[120:127], v148\n\t"
        "v_exp_f32 v98, v98\n\t"
        "v_exp_f32 v99, v99\n\t"
        "v_exp_f32 v100, v100\n\t"
        "v_smfmac_f32_16x16x64_bf16 v[132:135], v[144:147], v[120:127], v148\n\t"
        "v_exp_f32 v101, v101\n\t"
        "v_mfma_f32_32x32x16_bf16 v[112:127], v[168:171], v[88:91], 0\n\t"
        "v_exp_f32 v102, v102\n\t"
        "v_exp_f32 v103, v103\n\t"
        "v_exp_f32 v104, v104\n\t"
        "v_mfma_f32_32x32x16_bf16 v[112:127], v[172:175], v[92:95], v[112:127]\n\t"
        "v_exp_f32 v105, v105\n\t"
        "v_exp_f32 v106, v106\n\t"
        "v_exp_f32 v107, v107\n\t"
        "v_smfmac_f32_32x32x32_bf16 v[32:47], v[176:179], v[96:103], v148\n\t"
        "v_exp_f32 v108, v108\n\t"
        "v_exp_f32 v109, v109\n\t"
        "v_exp_f32 v110, v110\n\t"
        "v_smfmac_f32_16x16x64_bf16 v[136:139], v[144:147], v[96:103], v148\n\t"
        "v_exp_f32 v111, v111\n\t"
        "s_cmp_lg_u32 s46, 0\n\t"
        "s_cbranch_scc1 5f\n\t"
        "s_mov_b32 s45, 0x200000\n\t"
        "2:\n\t"
        "ds_read_b32 v151, v224\n\t"
        "s_sub_u32 s45, s45, 1\n\t"
        "s_waitcnt lgkmcnt(0)\n\t"
        "s_cbranch_scc1 3f\n\t"
        "v_cmp_ne_u32 vcc, v222, v151\n\t"
        "s_cbranch_vccnz 2b\n\t"
        "3:\n\t"
        "s_add_u32 s44, s44, 0x4000\n\t"
        "s_and_b32 s44, s44, 0x3ffff\n\t"
        "buffer_load_dwordx4 v[184:187], v149, s[40:43], s44 offen offset:0 sc1\n\t"
        "buffer_load_dwordx4 v[188:191], v149, s[40:43], s44 offen offset:1024 sc1\n\t"
        "buffer_load_dwordx4 v[192:195], v149, s[40:43], s44 offen offset:2048 sc1\n\t"
        "buffer_load_dwordx4 v[196:199], v149, s[40:43], s44 offen offset:3072 sc1\n\t"
        "buffer_load_dwordx4 v[200:203], v150, s[40:43], s44 offen offset:0 sc1\n\t"
        "buffer_load_dwordx4 v[204:207], v150, s[40:43], s44 offen offset:1024 sc1\n\t"
        "buffer_load_dwordx4 v[208:211], v150, s[40:43], s44 offen offset:2048 sc1\n\t"
        "buffer_load_dwordx4 v[212:215], v150, s[40:43], s44 offen offset:3072 sc1\n\t"
        "5:\n\t"
        "s_add_u32 s44, s44, 0x4000\n\t"
        "s_and_b32 s44, s44, 0x3ffff\n\t"
        "buffer_load_dwordx4 v[152:155], v149, s[40:43], s44 offen offset:0 sc1\n\t"
        "buffer_load_dwordx4 v[156:159], v149, s[40:43], s44 offen offset:1024 sc1\n\t"
        "buffer_load_dwordx4 v[160:163], v149, s[40:43], s44 offen offset:2048 sc1\n\t"
        "buffer_load_dwordx4 v[164:167], v149, s[40:43], s44 offen offset:3072 sc1\n\t"
        "s_mov_b32 s45, 7\n\t"
        "1:\n\t"
        "v_exp_f32 v112, v112\n\t"
        "v_exp_f32 v113, v113\n\t"
        "v_smfmac_f32_32x32x32_bf16 v[32:47], v[180:183], v[104:111], v148\n\t"
        "v_exp_f32 v114, v114\n\t"
        "v_exp_f32 v115, v115\n\t"
        "v_exp_f32 v116, v116\n\t"
        "v_smfmac_f32_16x16x64_bf16 v[136:139], v[144:147], v[104:111], v148\n\t"
        "v_exp_f32 v117, v117\n\t"
        "s_waitcnt vmcnt(8)\n\t"
        "v_mfma_f32_32x32x16_bf16 v[96:111], v[184:187], v[64:67], 0\n\t"
        "v_exp_f32 v118, v118\n\t"
        "v_exp_f32 v119, v119\n\t"
        "v_exp_f32 v120, v120\n\t"
        "v_mfma_f32_32x32x16_bf16 v[96:111], v[188:191], v[68:71], v[96:111]\n\t"
        "v_exp_f32 v121, v121\n\t"
        "v_exp_f32 v122, v122\n\t"
        "v_exp_f32 v123, v123\n\t"
        "v_smfmac_f32_32x32x32_bf16 v[48:63], v[176:179], v[112:119], v148\n\t"
        "v_exp_f32 v124, v124\n\t"
        "v_exp_f32 v125, v125\n\t"
        "v_exp_f32 v126, v126\n\t"
        "v_smfmac_f32_16x16x64_bf16 v[140:143], v[144:147], v[112:119], v148\n\t"
        "buffer_load_dwordx4 v[168:171], v150, s[40:43], s44 offen offset:0 sc1\n\t"
        "v_exp_f32 v127, v127\n\t"
        "v_exp_f32 v96, v96\n\t"
        "v_exp_f32 v97, v97\n\t"
        "v_smfmac_f32_32x32x32_bf16 v[48:63], v[180:183], v[120:127], v148\n\t"
        "v_exp_f32 v98, v98\n\t"
        "v_exp_f32 v99, v99\n\t"
        "v_exp_f32 v100, v100\n\t"
        "v_smfmac_f32_16x16x64_bf16 v[140:143], v[144:147], v[120:127], v148\n\t"
        "v_exp_f32 v101, v101\n\t"
        "v_mfma_f32_32x32x16_bf16 v[112:127], v[184:187], v[72:75], 0\n\t"
        "v_exp_f32 v102, v102\n\t"
        "v_exp_f32 v103, v103\n\t"
        "v_exp_f32 v104, v104\n\t"
        "v_mfma_f32_32x32x16_bf16 v[112:127], v[188:191], v[76:79], v[112:127]\n\t"
        "v_exp_f32 v105, v105\n\t"
        "v_exp_f32 v106, v106\n\t"
        "v_exp_f32 v107, v107\n\t"
        "v_smfmac_f32_32x32x32_bf16 v[0:15], v[192:195], v[96:103], v148\n\t"
        "v_exp_f32 v108, v108\n\t"
        "v_exp_f32 v109, v109\n\t"
        "v_exp_f32 v110, v110\n\t"
        "v_smfmac_f32_16x16x64_bf16 v[128:131], v[144:147], v[96:103], v148\n\t"
        "buffer_load_dwordx4 v[172:175], v150, s[40:43], s44 offen offset:1024 sc1\n\t"
        "v_exp_f32 v111, v111\n\t"
        "v_exp_f32 v112, v112\n\t"
        "v_exp_f32 v113, v113\n\t"
        "v_smfmac_f32_32x32x32_bf16 v[0:15], v[196:199], v[104:111], v148\n\t"
        "v_exp_f32 v114, v114\n\t"
        "v_exp_f32 v115, v115\n\t"
        "v_exp_f32 v116, v116\n\t"
        "v_smfmac_f32_16x16x64_bf16 v[128:131], v[144:147], v[104:111], v148\n\t"
        "v_exp_f32 v117, v117\n\t"
        "v_mfma_f32_32x32x16_bf16 v[96:111], v[184:187], v[80:83], 0\n\t"
        "v_exp_f32 v118, v118\n\t"
        "v_exp_f32 v119, v119\n\t"
        "v_exp_f32 v120, v120\n\t"
        "v_mfma_f32_32x32x16_bf16 v[96:111], v[188:191], v[84:87], v[96:111]\n\t"
        "v_exp_f32 v121, v121\n\t"
        "v_exp_f32 v122, v122\n\t"
        "v_exp_f32 v123, v123\n\t"
        "v_smfmac_f32_32x32x32_bf16 v[16:31], v[192:195], v[112:119], v148\n\t"
        "v_exp_f32 v124, v124\n\t"
        "v_exp_f32 v125, v125\n\t"
        "v_exp_f32 v126, v126\n\t"
        "v_smfmac_f32_16x16x64_bf16 v[132:135], v[144:147], v[112:119], v148\n\t"
        "buffer_load_dwordx4 v[176:179], v150, s[40:43], s44 offen offset:2048 sc1\n\t"
        "v_exp_f32 v127, v127\n\t"
        "v_exp_f32 v96, v96\n\t"
        "v_exp_f32 v97, v97\n\t"
        "v_smfmac_f32_32x32x32_bf16 v[16:31], v[196:199], v[120:127], v148\n\t"
        "v_exp_f32 v98, v98\n\t"
        "v_exp_f32 v99, v99\n\t"
        "v_exp_f32 v100, v100\n\t"
        "v_smfmac_f32_16x16x64_bf16 v[132:135], v[144:147], v[120:127], v148\n\t"
        "v_exp_f32 v101, v101\n\t"
        "v_mfma_f32_32x32x16_bf16 v[112:127], v[184:187], v[88:91], 0\n\t"
        "v_exp_f32 v102, v102\n\t"
        "v_exp_f32 v103, v103\n\t"
        "v_exp_f32 v104, v104\n\t"
        "v_mfma_f32_32x32x16_bf16 v[112:127], v[188:191], v[92:95], v[112:127]\n\t"
        "v_exp_f32 v105, v105\n\t"
        "v_exp_f32 v106, v106\n\t"
        "v_exp_f32 v107, v107\n\t"
        "v_smfmac_f32_32x32x32_bf16 v[32:47], v[192:195], v[96:103], v148\n\t"
        "v_exp_f32 v108, v108\n\t"
        "v_exp_f32 v109, v109\n\t"
        "v_exp_f32 v110, v110\n\t"
        "v_smfmac_f32_16x16x64_bf16 v[136:139], v[144:147], v[96:103], v148\n\t"
        "buffer_load_dwordx4 v[180:183], v150, s[40:43], s44 offen offset:3072 sc1\n\t"
        "v_exp_f32 v111, v111\n\t"
        "v_exp_f32 v112, v112\n\t"
        "v_exp_f32 v113, v113\n\t"
        "v_smfmac_f32_32x32x32_bf16 v[32:47], v[196:199], v[104:111], v148\n\t"
        "s_add_u32 s44, s44, 0x4000\n\t"
        "s_and_b32 s44, s44, 0x3ffff\n\t"
        "v_exp_f32 v114, v114\n\t"
        "v_exp_f32 v115, v115\n\t"
        "v_exp_f32 v116, v116\n\t"
        "v_smfmac_f32_16x16x64_bf16 v[136:139], v[144:147], v[104:111], v148\n\t"
        "v_exp_f32 v117, v117\n\t"
        "s_waitcnt vmcnt(8)\n\t"
        "v_mfma_f32_32x32x16_bf16 v[96:111], v[200:203], v[64:67], 0\n\t"
        "v_exp_f32 v118, v118\n\t"
        "v_exp_f32 v119, v119\n\t"
        "v_exp_f32 v120, v120\n\t"
        "v_mfma_f32_32x32x16_bf16 v[96:111], v[204:207], v[68:71], v[96:111]\n\t"
        "v_exp_f32 v121, v121\n\t"
        "v_exp_f32 v122, v122\n\t"
        "v_exp_f32 v123, v123\n\t"
        "v_smfmac_f32_32x32x32_bf16 v[48:63], v[192:195], v[112:119], v148\n\t"
        "v_exp_f32 v124, v124\n\t"
        "v_exp_f32 v125, v125\n\t"
        "v_exp_f32 v126, v126\n\t"
        "v_smfmac_f32_16x16x64_bf16 v[140:143], v[144:147], v[112:119], v148\n\t"
        "buffer_load_dwordx4 v[184:187], v149, s[40:43], s44 offen offset:0 sc1\n\t"
        "v_exp_f32 v127, v127\n\t"
        "v_exp_f32 v96, v96\n\t"
        "v_exp_f32 v97, v97\n\t"
        "v_smfmac_f32_32x32x32_bf16 v[48:63], v[196:199], v[120:127], v148\n\t"
        "v_exp_f32 v98, v98\n\t"
        "v_exp_f32 v99, v99\n\t"
        "v_exp_f32 v100, v100\n\t"
        "v_smfmac_f32_16x16x64_bf16 v[140:143], v[144:147], v[120:127], v148\n\t"
        "v_exp_f32 v101, v101\n\t"
        "v_mfma_f32_32x32x16_bf16 v[112:127], v[200:203], v[72:75], 0\n\t"
        "v_exp_f32 v102, v102\n\t"
        "v_exp_f32 v103, v103\n\t"
        "v_exp_f32 v104, v104\n\t"
        "v_mfma_f32_32x32x16_bf16 v[112:127], v[204:207], v[76:79], v[112:127]\n\t"
        "v_exp_f32 v105, v105\n\t"
        "v_exp_f32 v106, v106\n\t"
        "v_exp_f32 v107, v107\n\t"
        "v_smfmac_f32_32x32x32_bf16 v[0:15], v[208:211], v[96:103], v148\n\t"
        "v_exp_f32 v108, v108\n\t"
        "v_exp_f32 v109, v109\n\t"
        "v_exp_f32 v110, v110\n\t"
        "v_smfmac_f32_16x16x64_bf16 v[128:131], v[144:147], v[96:103], v148\n\t"
        "buffer_load_dwordx4 v[188:191], v149, s[40:43], s44 offen offset:1024 sc1\n\t"
        "v_exp_f32 v111, v111\n\t"
        "v_exp_f32 v112, v112\n\t"
        "v_exp_f32 v113, v113\n\t"
        "v_smfmac_f32_32x32x32_bf16 v[0:15], v[212:215], v[104:111], v148\n\t"
        "v_exp_f32 v114, v114\n\t"
        "v_exp_f32 v115, v115\n\t"
        "v_exp_f32 v116, v116\n\t"
        "v_smfmac_f32_16x16x64_bf16 v[128:131], v[144:147], v[104:111], v148\n\t"
        "v_exp_f32 v117, v117\n\t"
        "v_mfma_f32_32x32x16_bf16 v[96:111], v[200:203], v[80:83], 0\n\t"
        "v_exp_f32 v118, v118\n\t"
        "v_exp_f32 v119, v119\n\t"
        "v_exp_f32 v120, v120\n\t"
        "v_mfma_f32_32x32x16_bf16 v[96:111], v[204:207], v[84:87], v[96:111]\n\t"
        "v_exp_f32 v121, v121\n\t"
        "v_exp_f32 v122, v122\n\t"
        "v_exp_f32 v123, v123\n\t"
        "v_smfmac_f32_32x32x32_bf16 v[16:31], v[208:211], v[112:119], v148\n\t"
        "v_exp_f32 v124, v124\n\t"
        "v_exp_f32 v125, v125\n\t"
        "v_exp_f32 v126, v126\n\t"
        "v_smfmac_f32_16x16x64_bf16 v[132:135], v[144:147], v[112:119], v148\n\t"
        "buffer_load_dwordx4 v[192:195], v149, s[40:43], s44 offen offset:2048 sc1\n\t"
        "v_exp_f32 v127, v127\n\t"
        "v_exp_f32 v96, v96\n\t"
        "v_exp_f32 v97, v97\n\t"
        "v_smfmac_f32_32x32x32_bf16 v[16:31], v[212:215], v[120:127], v148\n\t"
        "v_exp_f32 v98, v98\n\t"
        "v_exp_f32 v99, v99\n\t"
        "v_exp_f32 v100, v100\n\t"
        "v_smfmac_f32_16x16x64_bf16 v[132:135], v[144:147], v[120:127], v148\n\t"
        "v_exp_f32 v101, v101\n\t"
        "v_mfma_f32_32x32x16_bf16 v[112:127], v[200:203], v[88:91], 0\n\t"
        "v_exp_f32 v102, v102\n\t"
        "v_exp_f32 v103, v103\n\t"
        "v_exp_f32 v104, v104\n\t"
        "v_mfma_f32_32x32x16_bf16 v[112:127], v[204:207], v[92:95], v[112:127]\n\t"
        "v_exp_f32 v105, v105\n\t"
        "v_exp_f32 v106, v106\n\t"
        "v_exp_f32 v107, v107\n\t"
        "v_smfmac_f32_32x32x32_bf16 v[32:47], v[208:211], v[96:103], v148\n\t"
        "v_exp_f32 v108, v108\n\t"
        "v_exp_f32 v109, v109\n\t"
        "v_exp_f32 v110, v110\n\t"
        "v_smfmac_f32_16x16x64_bf16 v[136:139], v[144:147], v[96:103], v148\n\t"
        "buffer_load_dwordx4 v[196:199], v149, s[40:43], s44 offen offset:3072 sc1\n\t"
        "v_exp_f32 v111, v111\n\t"
        "v_exp_f32 v112, v112\n\t"
        "v_exp_f32 v113, v113\n\t"
        "v_smfmac_f32_32x32x32_bf16 v[32:47], v[212:215], v[104:111], v148\n\t"
        "v_exp_f32 v114, v114\n\t"
        "v_exp_f32 v115, v115\n\t"
        "v_exp_f32 v116, v116\n\t"
        "v_smfmac_f32_16x16x64_bf16 v[136:139], v[144:147], v[104:111], v148\n\t"
        "v_exp_f32 v117, v117\n\t"
        "s_waitcnt vmcnt(8)\n\t"
        "v_mfma_f32_32x32x16_bf16 v[96:111], v[152:155], v[64:67], 0\n\t"
        "v_exp_f32 v118, v118\n\t"
        "v_exp_f32 v119, v119\n\t"
        "v_exp_f32 v120, v120\n\t"
        "v_mfma_f32_32x32x16_bf16 v[96:111], v[156:159], v[68:71], v[96:111]\n\t"
        "v_exp_f32 v121, v121\n\t"
        "v_exp_f32 v122, v122\n\t"
        "v_exp_f32 v123, v123\n\t"
        "v_smfmac_f32_32x32x32_bf16 v[48:63], v[208:211], v[112:119], v148\n\t"
        "v_exp_f32 v124, v124\n\t"
        "v_exp_f32 v125, v125\n\t"
        "v_exp_f32 v126, v126\n\t"
        "v_smfmac_f32_16x16x64_bf16 v[140:143], v[144:147], v[112:119], v148\n\t"
        "buffer_load_dwordx4 v[200:203], v150, s[40:43], s44 offen offset:0 sc1\n\t"
        "v_exp_f32 v127, v127\n\t"
        "v_exp_f32 v96, v96\n\t"
        "v_exp_f32 v97, v97\n\t"
        "v_smfmac_f32_32x32x32_bf16 v[48:63], v[212:215], v[120:127], v148\n\t"
        "v_exp_f32 v98, v98\n\t"
        "v_exp_f32 v99, v99\n\t"
        "v_exp_f32 v100, v100\n\t"
        "v_smfmac_f32_16x16x64_bf16 v[140:143], v[144:147], v[120:127], v148\n\t"
        "v_exp_f32 v101, v101\n\t"
        "v_mfma_f32_32x32x16_bf16 v[112:127], v[152:155], v[72:75], 0\n\t"
        "v_exp_f32 v102, v102\n\t"
        "v_exp_f32 v103, v103\n\t"
        "v_exp_f32 v104, v104\n\t"
        "v_mfma_f32_32x32x16_bf16 v[112:127], v[156:159], v[76:79], v[112:127]\n\t"
        "v_exp_f32 v105, v105\n\t"
        "v_exp_f32 v106, v106\n\t"
        "v_exp_f32 v107, v107\n\t"
        "v_smfmac_f32_32x32x32_bf16 v[0:15], v[160:163], v[96:103], v148\n\t"
        "v_exp_f32 v108, v108\n\t"
        "v_exp_f32 v109, v109\n\t"
        "v_exp_f32 v110, v110\n\t"
        "v_smfmac_f32_16x16x64_bf16 v[128:131], v[144:147], v[96:103], v148\n\t"
        "buffer_load_dwordx4 v[204:207], v150, s[40:43], s44 offen offset:1024 sc1\n\t"
        "v_exp_f32 v111, v111\n\t"
        "v_exp_f32 v112, v112\n\t"
        "v_exp_f32 v113, v113\n\t"
        "v_smfmac_f32_32x32x32_bf16 v[0:15], v[164:167], v[104:111], v148\n\t"
        "v_exp_f32 v114, v114\n\t"
        "v_exp_f32 v115, v115\n\t"
        "v_exp_f32 v116, v116\n\t"
        "v_smfmac_f32_16x16x64_bf16 v[128:131], v[144:147], v[104:111], v148\n\t"
        "v_exp_f32 v117, v117\n\t"
        "v_mfma_f32_32x32x16_bf16 v[96:111], v[152:155], v[80:83], 0\n\t"
        "v_exp_f32 v118, v118\n\t"
        "v_exp_f32 v119, v119\n\t"
        "v_exp_f32 v120, v120\n\t"
        "v_mfma_f32_32x32x16_bf16 v[96:111], v[156:159], v[84:87], v[96:111]\n\t"
        "v_exp_f32 v121, v121\n\t"
        "v_exp_f32 v122, v122\n\t"
        "v_exp_f32 v123, v123\n\t"
        "v_smfmac_f32_32x32x32_bf16 v[16:31], v[160:163], v[112:119], v148\n\t"
        "v_exp_f32 v124, v124\n\t"
        "v_exp_f32 v125, v125\n\t"
        "v_exp_f32 v126, v126\n\t"
        "v_smfmac_f32_16x16x64_bf16 v[132:135], v[144:147], v[112:119], v148\n\t"
        "buffer_load_dwordx4 v[208:211], v150, s[40:43], s44 offen offset:2048 sc1\n\t"
        "v_exp_f32 v127, v127\n\t"
        "v_exp_f32 v96, v96\n\t"
        "v_exp_f32 v97, v97\n\t"
        "v_smfmac_f32_32x32x32_bf16 v[16:31], v[164:167], v[120:127], v148\n\t"
        "v_exp_f32 v98, v98\n\t"
        "v_exp_f32 v99, v99\n\t"
        "v_exp_f32 v100, v100\n\t"
        "v_smfmac_f32_16x16x64_bf16 v[132:135], v[144:147], v[120:127], v148\n\t"
        "v_exp_f32 v101, v101\n\t"
        "v_mfma_f32_32x32x16_bf16 v[112:127], v[152:155], v[88:91], 0\n\t"
        "v_exp_f32 v102, v102\n\t"
        "v_exp_f32 v103, v103\n\t"
        "v_exp_f32 v104, v104\n\t"
        "v_mfma_f32_32x32x16_bf16 v[112:127], v[156:159], v[92:95], v[112:127]\n\t"
        "v_exp_f32 v105, v105\n\t"
        "v_exp_f32 v106, v106\n\t"
        "v_exp_f32 v107, v107\n\t"
        "v_smfmac_f32_32x32x32_bf16 v[32:47], v[160:163], v[96:103], v148\n\t"
        "v_exp_f32 v108, v108\n\t"
        "v_exp_f32 v109, v109\n\t"
        "v_exp_f32 v110, v110\n\t"
        "v_smfmac_f32_16x16x64_bf16 v[136:139], v[144:147], v[96:103], v148\n\t"
        "buffer_load_dwordx4 v[212:215], v150, s[40:43], s44 offen offset:3072 sc1\n\t"
        "v_exp_f32 v111, v111\n\t"
        "v_exp_f32 v112, v112\n\t"
        "v_exp_f32 v113, v113\n\t"
        "v_smfmac_f32_32x32x32_bf16 v[32:47], v[164:167], v[104:111], v148\n\t"
        "s_add_u32 s44, s44, 0x4000\n\t"
        "s_and_b32 s44, s44, 0x3ffff\n\t"
        "v_exp_f32 v114, v114\n\t"
        "v_exp_f32 v115, v115\n\t"
        "v_exp_f32 v116, v116\n\t"
        "v_smfmac_f32_16x16x64_bf16 v[136:139], v[144:147], v[104:111], v148\n\t"
        "v_exp_f32 v117, v117\n\t"
        "s_waitcnt vmcnt(8)\n\t"
        "v_mfma_f32_32x32x16_bf16 v[96:111], v[168:171], v[64:67], 0\n\t"
        "v_exp_f32 v118, v118\n\t"
        "v_exp_f32 v119, v119\n\t"
        "v_exp_f32 v120, v120\n\t"
        "v_mfma_f32_32x32x16_bf16 v[96:111], v[172:175], v[68:71], v[96:111]\n\t"
        "v_exp_f32 v121, v121\n\t"
        "v_exp_f32 v122, v122\n\t"
        "v_exp_f32 v123, v123\n\t"
        "v_smfmac_f32_32x32x32_bf16 v[48:63], v[160:163], v[112:119], v148\n\t"
        "v_exp_f32 v124, v124\n\t"
        "v_exp_f32 v125, v125\n\t"
        "v_exp_f32 v126, v126\n\t"
        "v_smfmac_f32_16x16x64_bf16 v[140:143], v[144:147], v[112:119], v148\n\t"
        "buffer_load_dwordx4 v[152:155], v149, s[40:43], s44 offen offset:0 sc1\n\t"
        "v_exp_f32 v127, v127\n\t"
        "v_exp_f32 v96, v96\n\t"
        "v_exp_f32 v97, v97\n\t"
        "v_smfmac_f32_32x32x32_bf16 v[48:63], v[164:167], v[120:127], v148\n\t"
        "v_exp_f32 v98, v98\n\t"
        "v_exp_f32 v99, v99\n\t"
        "v_exp_f32 v100, v100\n\t"
        "v_smfmac_f32_16x16x64_bf16 v[140:143], v[144:147], v[120:127], v148\n\t"
        "v_exp_f32 v101, v101\n\t"
        "v_mfma_f32_32x32x16_bf16 v[112:127], v[168:171], v[72:75], 0\n\t"
        "v_exp_f32 v102, v102\n\t"
        "v_exp_f32 v103, v103\n\t"
        "v_exp_f32 v104, v104\n\t"
        "v_mfma_f32_32x32x16_bf16 v[112:127], v[172:175], v[76:79], v[112:127]\n\t"
        "v_exp_f32 v105, v105\n\t"
        "v_exp_f32 v106, v106\n\t"
        "v_exp_f32 v107, v107\n\t"
        "v_smfmac_f32_32x32x32_bf16 v[0:15], v[176:179], v[96:103], v148\n\t"
        "v_exp_f32 v108, v108\n\t"
        "v_exp_f32 v109, v109\n\t"
        "v_exp_f32 v110, v110\n\t"
        "v_smfmac_f32_16x16x64_bf16 v[128:131], v[144:147], v[96:103], v148\n\t"
        "buffer_load_dwordx4 v[156:159], v149, s[40:43], s44 offen offset:1024 sc1\n\t"
        "v_exp_f32 v111, v111\n\t"
        "v_exp_f32 v112, v112\n\t"
        "v_exp_f32 v113, v113\n\t"
        "v_smfmac_f32_32x32x32_bf16 v[0:15], v[180:183], v[104:111], v148\n\t"
        "v_exp_f32 v114, v114\n\t"
        "v_exp_f32 v115, v115\n\t"
        "v_exp_f32 v116, v116\n\t"
        "v_smfmac_f32_16x16x64_bf16 v[128:131], v[144:147], v[104:111], v148\n\t"
        "v_exp_f32 v117, v117\n\t"
        "v_mfma_f32_32x32x16_bf16 v[96:111], v[168:171], v[80:83], 0\n\t"
        "v_exp_f32 v118, v118\n\t"
        "v_exp_f32 v119, v119\n\t"
        "v_exp_f32 v120, v120\n\t"
        "v_mfma_f32_32x32x16_bf16 v[96:111], v[172:175], v[84:87], v[96:111]\n\t"
        "v_exp_f32 v121, v121\n\t"
        "v_exp_f32 v122, v122\n\t"
        "v_exp_f32 v123, v123\n\t"
        "v_smfmac_f32_32x32x32_bf16 v[16:31], v[176:179], v[112:119], v148\n\t"
        "v_exp_f32 v124, v124\n\t"
        "v_exp_f32 v125, v125\n\t"
        "v_exp_f32 v126, v126\n\t"
        "v_smfmac_f32_16x16x64_bf16 v[132:135], v[144:147], v[112:119], v148\n\t"
        "buffer_load_dwordx4 v[160:163], v149, s[40:43], s44 offen offset:2048 sc1\n\t"
        "v_exp_f32 v127, v127\n\t"
        "v_exp_f32 v96, v96\n\t"
        "v_exp_f32 v97, v97\n\t"
        "v_smfmac_f32_32x32x32_bf16 v[16:31], v[180:183], v[120:127], v148\n\t"
        "v_exp_f32 v98, v98\n\t"
        "v_exp_f32 v99, v99\n\t"
        "v_exp_f32 v100, v100\n\t"
        "v_smfmac_f32_16x16x64_bf16 v[132:135], v[144:147], v[120:127], v148\n\t"
        "v_exp_f32 v101, v101\n\t"
        "v_mfma_f32_32x32x16_bf16 v[112:127], v[168:171], v[88:91], 0\n\t"
        "v_exp_f32 v102, v102\n\t"
        "v_exp_f32 v103, v103\n\t"
        "v_exp_f32 v104, v104\n\t"
        "v_mfma_f32_32x32x16_bf16 v[112:127], v[172:175], v[92:95], v[112:127]\n\t"
        "v_exp_f32 v105, v105\n\t"
        "v_exp_f32 v106, v106\n\t"
        "v_exp_f32 v107, v107\n\t"
        "v_smfmac_f32_32x32x32_bf16 v[32:47], v[176:179], v[96:103], v148\n\t"
        "v_exp_f32 v108, v108\n\t"
        "v_exp_f32 v109, v109\n\t"
        "v_exp_f32 v110, v110\n\t"
        "v_smfmac_f32_16x16x64_bf16 v[136:139], v[144:147], v[96:103], v148\n\t"
        "buffer_load_dwordx4 v[164:167], v149, s[40:43], s44 offen offset:3072 sc1\n\t"
        "v_exp_f32 v111, v111\n\t"
        "s_sub_u32 s45, s45, 1\n\t"
        "s_cmp_lg_u32 s45, 0\n\t"
        "s_cbranch_scc1 1b\n\t"
        "v_exp_f32 v112, v112\n\t"
        "v_exp_f32 v113, v113\n\t"
        "v_smfmac_f32_32x32x32_bf16 v[32:47], v[180:183], v[104:111], v148\n\t"
        "v_exp_f32 v114, v114\n\t"
        "v_exp_f32 v115, v115\n\t"
        "v_exp_f32 v116, v116\n\t"
        "v_smfmac_f32_16x16x64_bf16 v[136:139], v[144:147], v[104:111], v148\n\t"
        "v_exp_f32 v117, v117\n\t"
        "s_waitcnt vmcnt(8)\n\t"
        "v_mfma_f32_32x32x16_bf16 v[96:111], v[184:187], v[64:67], 0\n\t"
        "v_exp_f32 v118, v118\n\t"
        "v_exp_f32 v119, v119\n\t"
        "v_exp_f32 v120, v120\n\t"
        "v_mfma_f32_32x32x16_bf16 v[96:111], v[188:191], v[68:71], v[96:111]\n\t"
        "v_exp_f32 v121, v121\n\t"
        "v_exp_f32 v122, v122\n\t"
        "v_exp_f32 v123, v123\n\t"
        "v_smfmac_f32_32x32x32_bf16 v[48:63], v[176:179], v[112:119], v148\n\t"
        "v_exp_f32 v124, v124\n\t"
        "v_exp_f32 v125, v125\n\t"
        "v_exp_f32 v126, v126\n\t"
        "v_smfmac_f32_16x16x64_bf16 v[140:143], v[144:147], v[112:119], v148\n\t"
        "v_exp_f32 v127, v127\n\t"
        "v_exp_f32 v96, v96\n\t"
        "v_exp_f32 v97, v97\n\t"
        "v_smfmac_f32_32x32x32_bf16 v[48:63], v[180:183], v[120:127], v148\n\t"
        "v_exp_f32 v98, v98\n\t"
        "v_exp_f32 v99, v99\n\t"
        "v_exp_f32 v100, v100\n\t"
        "v_smfmac_f32_16x16x64_bf16 v[140:143], v[144:147], v[120:127], v148\n\t"
        "v_exp_f32 v101, v101\n\t"
        "v_mfma_f32_32x32x16_bf16 v[112:127], v[184:187], v[72:75], 0\n\t"
        "v_exp_f32 v102, v102\n\t"
        "v_exp_f32 v103, v103\n\t"
        "v_exp_f32 v104, v104\n\t"
        "v_mfma_f32_32x32x16_bf16 v[112:127], v[188:191], v[76:79], v[112:127]\n\t"
        "v_exp_f32 v105, v105\n\t"
        "v_exp_f32 v106, v106\n\t"
        "v_exp_f32 v107, v107\n\t"
        "v_smfmac_f32_32x32x32_bf16 v[0:15], v[192:195], v[96:103], v148\n\t"
        "v_exp_f32 v108, v108\n\t"
        "v_exp_f32 v109, v109\n\t"
        "v_exp_f32 v110, v110\n\t"
        "v_smfmac_f32_16x16x64_bf16 v[128:131], v[144:147], v[96:103], v148\n\t"
        "v_exp_f32 v111, v111\n\t"
        "v_exp_f32 v112, v112\n\t"
        "v_exp_f32 v113, v113\n\t"
        "v_smfmac_f32_32x32x32_bf16 v[0:15], v[196:199], v[104:111], v148\n\t"
        "v_exp_f32 v114, v114\n\t"
        "v_exp_f32 v115, v115\n\t"
        "v_exp_f32 v116, v116\n\t"
        "v_smfmac_f32_16x16x64_bf16 v[128:131], v[144:147], v[104:111], v148\n\t"
        "v_exp_f32 v117, v117\n\t"
        "s_waitcnt vmcnt(4)\n\t"
        "v_mfma_f32_32x32x16_bf16 v[96:111], v[200:203], v[64:67], 0\n\t"
        "v_exp_f32 v118, v118\n\t"
        "v_exp_f32 v119, v119\n\t"
        "v_exp_f32 v120, v120\n\t"
        "v_mfma_f32_32x32x16_bf16 v[96:111], v[204:207], v[68:71], v[96:111]\n\t"
        "v_exp_f32 v121, v121\n\t"
        "v_exp_f32 v122, v122\n\t"
        "v_exp_f32 v123, v123\n\t"
        "v_smfmac_f32_32x32x32_bf16 v[16:31], v[192:195], v[112:119], v148\n\t"
        "v_exp_f32 v124, v124\n\t"
        "v_exp_f32 v125, v125\n\t"
        "v_exp_f32 v126, v126\n\t"
        "v_smfmac_f32_16x16x64_bf16 v[132:135], v[144:147], v[112:119], v148\n\t"
        "v_exp_f32 v127, v127\n\t"
        "s_cmp_lg_u32 s47, 0\n\t"
        "s_cbranch_scc1 4f\n\t"
        "global_load_dwordx4 %[r0], v223, %[xb] offset:0\n\t"
        "global_load_dwordx4 %[r1], v223, %[xb] offset:32\n\t"
        "global_load_dwordx4 %[r2], v223, %[xb] offset:64\n\t"
        "global_load_dwordx4 %[r3], v223, %[xb] offset:96\n\t"
        "4:\n\t"
        "v_exp_f32 v96, v96\n\t"
        "v_exp_f32 v97, v97\n\t"
        "v_smfmac_f32_32x32x32_bf16 v[16:31], v[196:199], v[120:127], v148\n\t"
        "v_exp_f32 v98, v98\n\t"
        "v_exp_f32 v99, v99\n\t"
        "v_exp_f32 v100, v100\n\t"
        "v_smfmac_f32_16x16x64_bf16 v[132:135], v[144:147], v[120:127], v148\n\t"
        "v_exp_f32 v101, v101\n\t"
        "v_mfma_f32_32x32x16_bf16 v[112:127], v[200:203], v[72:75], 0\n\t"
        "v_exp_f32 v102, v102\n\t"
        "v_exp_f32 v103, v103\n\t"
        "v_exp_f32 v104, v104\n\t"
        "v_mfma_f32_32x32x16_bf16 v[112:127], v[204:207], v[76:79], v[112:127]\n\t"
        "v_exp_f32 v105, v105\n\t"
        "v_exp_f32 v106, v106\n\t"
        "v_exp_f32 v107, v107\n\t"
        "v_smfmac_f32_32x32x32_bf16 v[0:15], v[208:211], v[96:103], v148\n\t"
        "v_exp_f32 v108, v108\n\t"
        "v_exp_f32 v109, v109\n\t"
        "v_exp_f32 v110, v110\n\t"
        "v_smfmac_f32_16x16x64_bf16 v[128:131], v[144:147], v[96:103], v148\n\t"
        "v_exp_f32 v111, v111\n\t"
        "v_exp_f32 v112, v112\n\t"
        "v_exp_f32 v113, v113\n\t"
        "v_smfmac_f32_32x32x32_bf16 v[0:15], v[212:215], v[104:111], v148\n\t"
        "v_exp_f32 v114, v114\n\t"
        "v_exp_f32 v115, v115\n\t"
        "v_exp_f32 v116, v116\n\t"
        "v_smfmac_f32_16x16x64_bf16 v[128:131], v[144:147], v[104:111], v148\n\t"
        "v_exp_f32 v117, v117\n\t"
        "v_mfma_f32_32x32x16_bf16 v[96:111], v[184:187], v[80:83], 0\n\t"
        "v_exp_f32 v118, v118\n\t"
        "v_exp_f32 v119, v119\n\t"
        "v_exp_f32 v120, v120\n\t"
        "ds_write_b128 v216, v[0:3] offset:0\n\t"
        "v_mfma_f32_32x32x16_bf16 v[96:111], v[188:191], v[84:87], v[96:111]\n\t"
        "v_exp_f32 v121, v121\n\t"
        "ds_write_b128 v216, v[4:7] offset:1024\n\t"
        "v_exp_f32 v122, v122\n\t"
        "ds_write_b128 v216, v[8:11] offset:2048\n\t"
        "v_exp_f32 v123, v123\n\t"
        "ds_write_b128 v216, v[12:15] offset:3072\n\t"
        "v_smfmac_f32_32x32x32_bf16 v[16:31], v[208:211], v[112:119], v148\n\t"
        "v_exp_f32 v124, v124\n\t"
        "ds_write_b32 v218, v128 offset:0\n\t"
        "v_exp_f32 v125, v125\n\t"
        "v_exp_f32 v126, v126\n\t"
        "v_smfmac_f32_16x16x64_bf16 v[132:135], v[144:147], v[112:119], v148\n\t"
        "v_exp_f32 v127, v127\n\t"
        "v_exp_f32 v96, v96\n\t"
        "v_exp_f32 v97, v97\n\t"
        "v_smfmac_f32_32x32x32_bf16 v[16:31], v[212:215], v[120:127], v148\n\t"
        "v_exp_f32 v98, v98\n\t"
        "v_exp_f32 v99, v99\n\t"
        "v_exp_f32 v100, v100\n\t"
        "v_smfmac_f32_16x16x64_bf16 v[132:135], v[144:147], v[120:127], v148\n\t"
        "v_exp_f32 v101, v101\n\t"
        "v_mfma_f32_32x32x16_bf16 v[112:127], v[184:187], v[88:91], 0\n\t"
        "v_exp_f32 v102, v102\n\t"
        "v_exp_f32 v103, v103\n\t"
        "v_exp_f32 v104, v104\n\t"
        "ds_write_b128 v216, v[16:19] offset:4096\n\t"
        "v_mfma_f32_32x32x16_bf16 v[112:127], v[188:191], v[92:95], v[112:127]\n\t"
        "v_exp_f32 v105, v105\n\t"
        "ds_write_b128 v216, v[20:23] offset:5120\n\t"
        "v_exp_f32 v106, v106\n\t"
        "ds_write_b128 v216, v[24:27] offset:6144\n\t"
        "v_exp_f32 v107, v107\n\t"
        "ds_write_b128 v216, v[28:31] offset:7168\n\t"
        "v_smfmac_f32_32x32x32_bf16 v[32:47], v[192:195], v[96:103], v148\n\t"
        "v_exp_f32 v108, v108\n\t"
        "ds_write_b32 v218, v132 offset:256\n\t"
        "v_exp_f32 v109, v109\n\t"
        "ds_write_b32 v220, v222\n\t"
        "v_exp_f32 v110, v110\n\t"
        "v_smfmac_f32_16x16x64_bf16 v[136:139], v[144:147], v[96:103], v148\n\t"
        "v_exp_f32 v111, v111\n\t"
        "v_exp_f32 v112, v112\n\t"
        "v_exp_f32 v113, v113\n\t"
        "v_smfmac_f32_32x32x32_bf16 v[32:47], v[196:199], v[104:111], v148\n\t"
        "v_exp_f32 v114, v114\n\t"
        "v_exp_f32 v115, v115\n\t"
        "v_exp_f32 v116, v116\n\t"
        "v_smfmac_f32_16x16x64_bf16 v[136:139], v[144:147], v[104:111], v148\n\t"
        "v_exp_f32 v117, v117\n\t"
        "v_mfma_f32_32x32x16_bf16 v[96:111], v[200:203], v[80:83], 0\n\t"
        "v_exp_f32 v118, v118\n\t"
        "v_exp_f32 v119, v119\n\t"
        "v_exp_f32 v120, v120\n\t"
        "v_mfma_f32_32x32x16_bf16 v[96:111], v[204:207], v[84:87], v[96:111]\n\t"
        "v_exp_f32 v121, v121\n\t"
        "v_exp_f32 v122, v122\n\t"
        "v_exp_f32 v123, v123\n\t"
        "v_smfmac_f32_32x32x32_bf16 v[48:63], v[192:195], v[112:119], v148\n\t"
        "v_exp_f32 v124, v124\n\t"
        "v_exp_f32 v125, v125\n\t"
        "v_exp_f32 v126, v126\n\t"
        "v_smfmac_f32_16x16x64_bf16 v[140:143], v[144:147], v[112:119], v148\n\t"
        "v_exp_f32 v127, v127\n\t"
        "v_exp_f32 v96, v96\n\t"
        "v_exp_f32 v97, v97\n\t"
        "v_smfmac_f32_32x32x32_bf16 v[48:63], v[196:199], v[120:127], v148\n\t"
        "v_exp_f32 v98, v98\n\t"
        "v_exp_f32 v99, v99\n\t"
        "v_exp_f32 v100, v100\n\t"
        "v_smfmac_f32_16x16x64_bf16 v[140:143], v[144:147], v[120:127], v148\n\t"
        "v_exp_f32 v101, v101\n\t"
        "v_mfma_f32_32x32x16_bf16 v[112:127], v[200:203], v[88:91], 0\n\t"
        "v_exp_f32 v102, v102\n\t"
        "v_exp_f32 v103, v103\n\t"
        "v_exp_f32 v104, v104\n\t"
        "ds_read_b32 v151, v221\n\t"
        "v_mfma_f32_32x32x16_bf16 v[112:127], v[204:207], v[92:95], v[112:127]\n\t"
        "v_exp_f32 v105, v105\n\t"
        "v_exp_f32 v106, v106\n\t"
        "v_exp_f32 v107, v107\n\t"
        "v_smfmac_f32_32x32x32_bf16 v[32:47], v[208:211], v[96:103], v148\n\t"
        "v_exp_f32 v108, v108\n\t"
        "v_exp_f32 v109, v109\n\t"
        "v_exp_f32 v110, v110\n\t"
        "v_smfmac_f32_16x16x64_bf16 v[136:139], v[144:147], v[96:103], v148\n\t"
        "v_exp_f32 v111, v111\n\t"
        "s_waitcnt lgkmcnt(0)\n\t"
        "v_cmp_ne_u32 vcc, v222, v151\n\t"
        "s_cbranch_vccnz 40f\n\t"
        "41:\n\t"
        "v_exp_f32 v112, v112\n\t"
        "ds_read_b128 v[0:3], v217 offset:0\n\t"
        "v_exp_f32 v113, v113\n\t"
        "ds_read_b128 v[4:7], v217 offset:1024\n\t"
        "v_smfmac_f32_32x32x32_bf16 v[32:47], v[212:215], v[104:111], v148\n\t"
        "v_exp_f32 v114, v114\n\t"
        "ds_read_b128 v[8:11], v217 offset:2048\n\t"
        "v_exp_f32 v115, v115\n\t"
        "ds_read_b128 v[12:15], v217 offset:3072\n\t"
        "v_exp_f32 v116, v116\n\t"
        "ds_read_b32 v128, v219 offset:0\n\t"
        "v_smfmac_f32_16x16x64_bf16 v[136:139], v[144:147], v[104:111], v148\n\t"
        "v_exp_f32 v117, v117\n\t"
        "ds_read_b128 v[16:19], v217 offset:4096\n\t"
        "v_exp_f32 v118, v118\n\t"
        "ds_read_b128 v[20:23], v217 offset:5120\n\t"
        "v_exp_f32 v119, v119\n\t"
        "ds_read_b128 v[24:27], v217 offset:6144\n\t"
        "v_exp_f32 v120, v120\n\t"
        "ds_read_b128 v[28:31], v217 offset:7168\n\t"
        "v_exp_f32 v121, v121\n\t"
        "ds_read_b32 v132, v219 offset:256\n\t"
        "v_exp_f32 v122, v122\n\t"
        "v_exp_f32 v123, v123\n\t"
        "v_smfmac_f32_32x32x32_bf16 v[48:63], v[208:211], v[112:119], v148\n\t"
        "v_exp_f32 v124, v124\n\t"
        "v_exp_f32 v125, v125\n\t"
        "v_exp_f32 v126, v126\n\t"
        "v_smfmac_f32_16x16x64_bf16 v[140:143], v[144:147], v[112:119], v148\n\t"
        "v_exp_f32 v127, v127\n\t"
        "s_nop 1\n\t"
        "v_smfmac_f32_32x32x32_bf16 v[48:63], v[212:215], v[120:127], v148\n\t"
        "v_smfmac_f32_16x16x64_bf16 v[140:143], v[144:147], v[120:127], v148\n\t"
        "s_waitcnt vmcnt(0) lgkmcnt(0)\n\t"
        "s_nop 6\n\t"
        "s_branch 9f\n\t"
        "24:\n\t"
        "s_add_u32 s44, s44, 0x4000\n\t"
        "s_and_b32 s44, s44, 0x3ffff\n\t"
        "buffer_load_dwordx4 v[184:187], v149, s[40:43], s44 offen offset:0 sc1\n\t"
        "buffer_load_dwordx4 v[188:191], v149, s[40:43], s44 offen offset:1024 sc1\n\t"
        "buffer_load_dwordx4 v[192:195], v149, s[40:43], s44 offen offset:2048 sc1\n\t"
        "buffer_load_dwordx4 v[196:199], v149, s[40:43], s44 offen offset:3072 sc1\n\t"
        "buffer_load_dwordx4 v[200:203], v150, s[40:43], s44 offen offset:0 sc1\n\t"
        "buffer_load_dwordx4 v[204:207], v150, s[40:43], s44 offen offset:1024 sc1\n\t"
        "buffer_load_dwordx4 v[208:211], v150, s[40:43], s44 offen offset:2048 sc1\n\t"
        "buffer_load_dwordx4 v[212:215], v150, s[40:43], s44 offen offset:3072 sc1\n\t"
        "s_mov_b32 s46, 1\n\t"
        "s_branch 25b\n\t"
        "26:\n\t"
        "s_add_u32 s44, s44, 0x4000\n\t"
        "s_and_b32 s44, s44, 0x3ffff\n\t"
        "buffer_load_dwordx4 v[184:187], v149, s[40:43], s44 offen offset:0 sc1\n\t"
        "buffer_load_dwordx4 v[188:191], v149, s[40:43], s44 offen offset:1024 sc1\n\t"
        "buffer_load_dwordx4 v[192:195], v149, s[40:43], s44 offen offset:2048 sc1\n\t"
        "buffer_load_dwordx4 v[196:199], v149, s[40:43], s44 offen offset:3072 sc1\n\t"
        "buffer_load_dwordx4 v[200:203], v150, s[40:43], s44 offen offset:0 sc1\n\t"
        "buffer_load_dwordx4 v[204:207], v150, s[40:43], s44 offen offset:1024 sc1\n\t"
        "buffer_load_dwordx4 v[208:211], v150, s[40:43], s44 offen offset:2048 sc1\n\t"
        "buffer_load_dwordx4 v[212:215], v150, s[40:43], s44 offen offset:3072 sc1\n\t"
        "s_mov_b32 s46, 1\n\t"
        "s_branch 27b\n\t"
        "28:\n\t"
        "s_add_u32 s44, s44, 0x4000\n\t"
        "s_and_b32 s44, s44, 0x3ffff\n\t"
        "buffer_load_dwordx4 v[184:187], v149, s[40:43], s44 offen offset:0 sc1\n\t"
        "buffer_load_dwordx4 v[188:191], v149, s[40:43], s44 offen offset:1024 sc1\n\t"
        "buffer_load_dwordx4 v[192:195], v149, s[40:43], s44 offen offset:2048 sc1\n\t"
        "buffer_load_dwordx4 v[196:199], v149, s[40:43], s44 offen offset:3072 sc1\n\t"
        "buffer_load_dwordx4 v[200:203], v150, s[40:43], s44 offen offset:0 sc1\n\t"
        "buffer_load_dwordx4 v[204:207], v150, s[40:43], s44 offen offset:1024 sc1\n\t"
        "buffer_load_dwordx4 v[208:211], v150, s[40:43], s44 offen offset:2048 sc1\n\t"
        "buffer_load_dwordx4 v[212:215], v150, s[40:43], s44 offen offset:3072 sc1\n\t"
        "s_mov_b32 s46, 1\n\t"
        "s_branch 29b\n\t"
        "30:\n\t"
        "s_add_u32 s44, s44, 0x4000\n\t"
        "s_and_b32 s44, s44, 0x3ffff\n\t"
        "buffer_load_dwordx4 v[184:187], v149, s[40:43], s44 offen offset:0 sc1\n\t"
        "buffer_load_dwordx4 v[188:191], v149, s[40:43], s44 offen offset:1024 sc1\n\t"
        "buffer_load_dwordx4 v[192:195], v149, s[40:43], s44 offen offset:2048 sc1\n\t"
        "buffer_load_dwordx4 v[196:199], v149, s[40:43], s44 offen offset:3072 sc1\n\t"
        "buffer_load_dwordx4 v[200:203], v150, s[40:43], s44 offen offset:0 sc1\n\t"
        "buffer_load_dwordx4 v[204:207], v150, s[40:43], s44 offen offset:1024 sc1\n\t"
        "buffer_load_dwordx4 v[208:211], v150, s[40:43], s44 offen offset:2048 sc1\n\t"
        "buffer_load_dwordx4 v[212:215], v150, s[40:43], s44 offen offset:3072 sc1\n\t"
        "s_mov_b32 s46, 1\n\t"
        "s_branch 31b\n\t"
        "32:\n\t"
        "s_add_u32 s44, s44, 0x4000\n\t"
        "s_and_b32 s44, s44, 0x3ffff\n\t"
        "buffer_load_dwordx4 v[184:187], v149, s[40:43], s44 offen offset:0 sc1\n\t"
        "buffer_load_dwordx4 v[188:191], v149, s[40:43], s44 offen offset:1024 sc1\n\t"
        "buffer_load_dwordx4 v[192:195], v149, s[40:43], s44 offen offset:2048 sc1\n\t"
        "buffer_load_dwordx4 v[196:199], v149, s[40:43], s44 offen offset:3072 sc1\n\t"
        "buffer_load_dwordx4 v[200:203], v150, s[40:43], s44 offen offset:0 sc1\n\t"
        "buffer_load_dwordx4 v[204:207], v150, s[40:43], s44 offen offset:1024 sc1\n\t"
        "buffer_load_dwordx4 v[208:211], v150, s[40:43], s44 offen offset:2048 sc1\n\t"
        "buffer_load_dwordx4 v[212:215], v150, s[40:43], s44 offen offset:3072 sc1\n\t"
        "s_mov_b32 s46, 1\n\t"
        "s_branch 33b\n\t"
        "40:\n\t"
        "s_mov_b32 s45, 0x100000\n\t"
        "42:\n\t"
        "ds_read_b32 v151, v221\n\t"
        "s_sub_u32 s45, s45, 1\n\t"
        "s_waitcnt lgkmcnt(0)\n\t"
        "s_cbranch_scc1 41b\n\t"
        "v_cmp_ne_u32 vcc, v222, v151\n\t"
        "s_cbranch_vccnz 42b\n\t"
        "s_branch 41b\n\t"
        "9:\n\t"
        : "+{v[0:15]}"(o0), "+{v[16:31]}"(o1), "+{v[32:47]}"(o2), "+{v[48:63]}"(o3), "+{v[128:143]}"(lv), "+{s44}"(soff), "+{v[152:167]}"(kva), "+{v[168:183]}"(kvb), "+{v[184:191]}"(fv),
          [r0] "+v"(x1p[0]), [r1] "+v"(x1p[1]), [r2] "+v"(x1p[2]), [r3] "+v"(x1p[3]) STAMP_OUTS
        : "{v[64:71]}"(q[0]), "{v[72:79]}"(q[1]), "{v[80:87]}"(q[2]), "{v[88:95]}"(q[3]), "{v[144:147]}"(ones), "{v148}"(0xddddu), "{v149}"(kvoff), "{v150}"(kvoff + 4096),
          "{v[216:223]}"(cv), "{v224}"(lbase + L_W + 16), "{s[40:43]}"(srd), "{s47}"(L), [xb] "s"(x0)
        : "v96", "v97", "v98", "v99", "v100", "v101", "v102", "v103", "v104", "v105", "v106", "v107", "v108", "v109", "v110", "v111", "v112", "v113", "v114", "v115", "v116", "v117", "v118", "v119", "v120", "v121", "v122", "v123", "v124", "v125", "v126", "v127", "v151", "v192", "v193", "v194", "v195", "v196", "v197", "v198", "v199", "v200", "v201", "v202", "v203", "v204", "v205", "v206", "v207", "v208", "v209", "v210", "v211", "v212", "v213", "v214", "v215", "s45", "s46", "vcc", "scc", "memory");
      STAMP_PUT(6 + 16 * L);
      STAMPV(9 + 16 * L);
      PHASE_VARS;
      const unsigned dst = OFF_OF + L * (5 * MBY) + (tm * 4 + 2 * kh) * TS + (tn * 2 + hh) * 2048 + lane * 16;
      auto put = [&](int u, const f32x16& o) {
#pragma unroll
        for (int c = 0; c < 2; ++c) {
          u32x4 w;
#pragma unroll
          for (int e = 0; e < 4; ++e) w[e] = pack2(o[8 * c + 2 * e], o[8 * c + 2 * e + 1]);
          stg(rs, dst + u * TS + c * 1024, w);
        }
      };
      const float la = lv[8] + lv[0], lb = lv[12] + lv[4];
      o2 = (o2 + o0) * RCP(la); put(0, o2);
      SBAR();
      o3 = (o3 + o1) * RCP(lb); put(1, o3);
      SBAR();
      {
        const f32x16 t = o2 + o3;
        float sum = 0.f;
#pragma unroll
        for (int e = 0; e < 16; e += 4) sum += (t[e] + t[e + 1]) + (t[e + 2] + t[e + 3]);
        if (__any(!(fabsf(sum) < 1e37f) | !(fmaxf(la, lb) < 1e27f) | !(fminf(la, lb) > 1e-9f) | !(la + lb == la + lb))) {
          DRAIN();
          attn_safe(rs, kvoff - kh * 8192, q[2], ones, o2); put(0, o2);
          attn_safe(rs, kvoff - kh * 8192, q[3], ones, o3); put(1, o3);
        }
      }
    }
    STAMPV(10 + 16 * L);
    {
      f32x16 acc[2];
      u32x4 res[4];
      u32x2 wrc[8];
      STAMP_DECL;
      { PHASE_VARS;
      BAR_ALL();
      STAMPV(11 + 16 * L);
#pragma unroll
      for (int g = 0; g < 8; ++g) wrc[g] = *reinterpret_cast<const u32x2*>(smem + L_WR + g * 16 + hi * 8);
      if (L == 0) {
#pragma unroll
        for (int i = 0; i < 4; ++i) res[i] = fo_swap(x1p[i]);
      } else {
        const bool routed = (int)words[3] == tile * 32 + r32;
        const float mb1 = reinterpret_cast<const float*>(smem + L_AB)[256 + wid * 64 + r32], rd1 = reinterpret_cast<const float*>(smem + L_AB)[256 + wid * 64 + 32 + r32];
#pragma unroll
        for (int i = 0; i < 4; ++i) {
          const u32x4 pr = *reinterpret_cast<const u32x4*>(smem + L_X2P + (i * 2 + hi) * 16);
#pragma unroll
          for (int e = 0; e < 4; ++e) {
            const unsigned h2 = ln_pair(x1p[i][e], mb1, rd1);
            unsigned t = pack2(lo16(x1p[i][e]) + lo16(h2), hi16(x1p[i][e]) + hi16(h2));
            PIN(t);
            res[i][e] = routed ? pr[e] : t;
          }
        }
      }
#pragma unroll
      for (int g = 0; g < 8; ++g) PIN(wrc[g]);
#pragma unroll
      for (int e = 0; e < 16; ++e) { acc[e >> 3][2 * (e & 7)] = lo16(res[e >> 2][e & 3]); acc[e >> 3][2 * (e & 7) + 1] = hi16(res[e >> 2][e & 3]); }
      unsigned soff = tn * 4096;
      const unsigned a0 = lbase + wrow * 128 + ((wch ^ wsw) << 4) + 8 * (tp & 1), a1 = lbase + wrow * 128 + (((4 + wch) ^ wsw) << 4) + 8 * (tp & 1);
      asm volatile(
        "ds_read_b64_tr_b16 v[32:33], v193 offset:0\n\t"
        "ds_read_b64_tr_b16 v[34:35], v193 offset:1024\n\t"
        "ds_read_b64_tr_b16 v[36:37], v194 offset:0\n\t"
        "ds_read_b64_tr_b16 v[38:39], v194 offset:1024\n\t"
        "ds_read_b64_tr_b16 v[40:41], v193 offset:2048\n\t"
        "ds_read_b64_tr_b16 v[42:43], v193 offset:3072\n\t"
        "ds_read_b64_tr_b16 v[44:45], v194 offset:2048\n\t"
        "ds_read_b64_tr_b16 v[46:47], v194 offset:3072\n\t"
        "ds_read_b64_tr_b16 v[48:49], v193 offset:4096\n\t"
        "ds_read_b64_tr_b16 v[50:51], v193 offset:5120\n\t"
        "ds_read_b64_tr_b16 v[52:53], v194 offset:4096\n\t"
        "ds_read_b64_tr_b16 v[54:55], v194 offset:5120\n\t"
        "ds_read_b64_tr_b16 v[56:57], v193 offset:6144\n\t"
        "ds_read_b64_tr_b16 v[58:59], v193 offset:7168\n\t"
        "ds_read_b64_tr_b16 v[60:61], v194 offset:6144\n\t"
        "ds_read_b64_tr_b16 v[62:63], v194 offset:7168\n\t"
        "s_mov_b32 s45, 0x200000\n\t"
        "2:\n\t"
        "ds_read_b32 v200, v198\n\t"
        "s_sub_u32 s45, s45, 1\n\t"
        "s_waitcnt lgkmcnt(0)\n\t"
        "s_cbranch_scc1 3f\n\t"
        "v_cmp_ne_u32 vcc, v199, v200\n\t"
        "s_cbranch_vccnz 2b\n\t"
        "3:\n\t"
        "buffer_load_dwordx4 v[64:67], v192, s[40:43], s44 offen offset:0 sc1\n\t"
        "buffer_load_dwordx4 v[68:71], v192, s[40:43], s44 offen offset:1024 sc1\n\t"
        "buffer_load_dwordx4 v[72:75], v192, s[40:43], s44 offen offset:2048 sc1\n\t"
        "buffer_load_dwordx4 v[76:79], v192, s[40:43], s44 offen offset:3072 sc1\n\t"
        "s_add_u32 s44, s44, 0x1000\n\t"
        "s_and_b32 s44, s44, 0xffff\n\t"
        "buffer_load_dwordx4 v[80:83], v192, s[40:43], s44 offen offset:0 sc1\n\t"
        "buffer_load_dwordx4 v[84:87], v192, s[40:43], s44 offen offset:1024 sc1\n\t"
        "buffer_load_dwordx4 v[88:91], v192, s[40:43], s44 offen offset:2048 sc1\n\t"
        "buffer_load_dwordx4 v[92:95], v192, s[40:43], s44 offen offset:3072 sc1\n\t"
        "s_add_u32 s44, s44, 0x1000\n\t"
        "s_and_b32 s44, s44, 0xffff\n\t"
        "buffer_load_dwordx4 v[96:99], v192, s[40:43], s44 offen offset:0 sc1\n\t"
        "buffer_load_dwordx4 v[100:103], v192, s[40:43], s44 offen offset:1024 sc1\n\t"
        "buffer_load_dwordx4 v[104:107], v192, s[40:43], s44 offen offset:2048 sc1\n\t"
        "buffer_load_dwordx4 v[108:111], v192, s[40:43], s44 offen offset:3072 sc1\n\t"
        "s_waitcnt vmcnt(11) lgkmcnt(14)\n\t"
        "v_mfma_f32_32x32x16_bf16 v[0:15], v[32:35], v[64:67], v[0:15]\n\t"
        "ds_read_b64_tr_b16 v[32:33], v193 offset:8192\n\t"
        "ds_read_b64_tr_b16 v[34:35], v193 offset:9216\n\t"
        "s_waitcnt lgkmcnt(14)\n\t"
        "v_mfma_f32_32x32x16_bf16 v[16:31], v[36:39], v[64:67], v[16:31]\n\t"
        "ds_read_b64_tr_b16 v[36:37], v194 offset:8192\n\t"
        "ds_read_b64_tr_b16 v[38:39], v194 offset:9216\n\t"
        "s_add_u32 s44, s44, 0x1000\n\t"
        "s_and_b32 s44, s44, 0xffff\n\t"
        "buffer_load_dwordx4 v[112:115], v192, s[40:43], s44 offen offset:0 sc1\n\t"
        "s_waitcnt vmcnt(11) lgkmcnt(14)\n\t"
        "v_mfma_f32_32x32x16_bf16 v[0:15], v[40:43], v[68:71], v[0:15]\n\t"
        "ds_read_b64_tr_b16 v[40:41], v193 offset:10240\n\t"
        "ds_read_b64_tr_b16 v[42:43], v193 offset:11264\n\t"
        "s_waitcnt lgkmcnt(14)\n\t"
        "v_mfma_f32_32x32x16_bf16 v[16:31], v[44:47], v[68:71], v[16:31]\n\t"
        "ds_read_b64_tr_b16 v[44:45], v194 offset:10240\n\t"
        "ds_read_b64_tr_b16 v[46:47], v194 offset:11264\n\t"
        "buffer_load_dwordx4 v[116:119], v192, s[40:43], s44 offen offset:1024 sc1\n\t"
        "s_waitcnt vmcnt(11) lgkmcnt(14)\n\t"
        "v_mfma_f32_32x32x16_bf16 v[0:15], v[48:51], v[72:75], v[0:15]\n\t"
        "ds_read_b64_tr_b16 v[48:49], v193 offset:12288\n\t"
        "ds_read_b64_tr_b16 v[50:51], v193 offset:13312\n\t"
        "s_waitcnt lgkmcnt(14)\n\t"
        "v_mfma_f32_32x32x16_bf16 v[16:31], v[52:55], v[72:75], v[16:31]\n\t"
        "ds_read_b64_tr_b16 v[52:53], v194 offset:12288\n\t"
        "ds_read_b64_tr_b16 v[54:55], v194 offset:13312\n\t"
        "buffer_load_dwordx4 v[120:123], v192, s[40:43], s44 offen offset:2048 sc1\n\t"
        "s_waitcnt vmcnt(11) lgkmcnt(14)\n\t"
        "v_mfma_f32_32x32x16_bf16 v[0:15], v[56:59], v[76:79], v[0:15]\n\t"
        "ds_read_b64_tr_b16 v[56:57], v193 offset:14336\n\t"
        "ds_read_b64_tr_b16 v[58:59], v193 offset:15360\n\t"
        "s_waitcnt lgkmcnt(14)\n\t"
        "v_mfma_f32_32x32x16_bf16 v[16:31], v[60:63], v[76:79], v[16:31]\n\t"
        "ds_read_b64_tr_b16 v[60:61], v194 offset:14336\n\t"
        "ds_read_b64_tr_b16 v[62:63], v194 offset:15360\n\t"
        "buffer_load_dwordx4 v[124:127], v192, s[40:43], s44 offen offset:3072 sc1\n\t"
        "s_waitcnt vmcnt(11) lgkmcnt(14)\n\t"
        "v_mfma_f32_32x32x16_bf16 v[0:15], v[32:35], v[80:83], v[0:15]\n\t"
        "ds_read_b64_tr_b16 v[32:33], v193 offset:16384\n\t"
        "ds_read_b64_tr_b16 v[34:35], v193 offset:17408\n\t"
        "s_waitcnt lgkmcnt(14)\n\t"
        "v_mfma_f32_32x32x16_bf16 v[16:31], v[36:39], v[80:83], v[16:31]\n\t"
        "ds_read_b64_tr_b16 v[36:37], v194 offset:16384\n\t"
        "ds_read_b64_tr_b16 v[38:39], v194 offset:17408\n\t"
        "s_add_u32 s44, s44, 0x1000\n\t"
        "s_and_b32 s44, s44, 0xffff\n\t"
        "buffer_load_dwordx4 v[128:131], v192, s[40:43], s44 offen offset:0 sc1\n\t"
        "s_waitcnt vmcnt(11) lgkmcnt(14)\n\t"
        "v_mfma_f32_32x32x16_bf16 v[0:15], v[40:43], v[84:87], v[0:15]\n\t"
        "ds_read_b64_tr_b16 v[40:41], v193 offset:18432\n\t"
        "ds_read_b64_tr_b16 v[42:43], v193 offset:19456\n\t"
        "s_waitcnt lgkmcnt(14)\n\t"
        "v_mfma_f32_32x32x16_bf16 v[16:31], v[44:47], v[84:87], v[16:31]\n\t"
        "ds_read_b64_tr_b16 v[44:45], v194 offset:18432\n\t"
        "ds_read_b64_tr_b16 v[46:47], v194 offset:19456\n\t"
        "buffer_load_dwordx4 v[132:135], v192, s[40:43], s44 offen offset:1024 sc1\n\t"
        "s_waitcnt vmcnt(11) lgkmcnt(14)\n\t"
        "v_mfma_f32_32x32x16_bf16 v[0:15], v[48:51], v[88:91], v[0:15]\n\t"
        "ds_read_b64_tr_b16 v[48:49], v193 offset:20480\n\t"
        "ds_read_b64_tr_b16 v[50:51], v193 offset:21504\n\t"
        "s_waitcnt lgkmcnt(14)\n\t"
        "v_mfma_f32_32x32x16_bf16 v[16:31], v[52:55], v[88:91], v[16:31]\n\t"
        "ds_read_b64_tr_b16 v[52:53], v194 offset:20480\n\t"
        "ds_read_b64_tr_b16 v[54:55], v194 offset:21504\n\t"
        "buffer_load_dwordx4 v[136:139], v192, s[40:43], s44 offen offset:2048 sc1\n\t"
        "s_waitcnt vmcnt(11) lgkmcnt(14)\n\t"
        "v_mfma_f32_32x32x16_bf16 v[0:15], v[56:59], v[92:95], v[0:15]\n\t"
        "ds_read_b64_tr_b16 v[56:57], v193 offset:22528\n\t"
        "ds_read_b64_tr_b16 v[58:59], v193 offset:23552\n\t"
        "s_waitcnt lgkmcnt(14)\n\t"
        "v_mfma_f32_32x32x16_bf16 v[16:31], v[60:63], v[92:95], v[16:31]\n\t"
        "ds_read_b64_tr_b16 v[60:61], v194 offset:22528\n\t"
        "ds_read_b64_tr_b16 v[62:63], v194 offset:23552\n\t"
        "buffer_load_dwordx4 v[140:143], v192, s[40:43], s44 offen offset:3072 sc1\n\t"
        "s_waitcnt vmcnt(11) lgkmcnt(14)\n\t"
        "v_mfma_f32_32x32x16_bf16 v[0:15], v[32:35], v[96:99], v[0:15]\n\t"
        "ds_read_b64_tr_b16 v[32:33], v193 offset:24576\n\t"
        "ds_read_b64_tr_b16 v[34:35], v193 offset:25600\n\t"
        "s_waitcnt lgkmcnt(14)\n\t"
        "v_mfma_f32_32x32x16_bf16 v[16:31], v[36:39], v[96:99], v[16:31]\n\t"
        "ds_read_b64_tr_b16 v[36:37], v194 offset:24576\n\t"
        "ds_read_b64_tr_b16 v[38:39], v194 offset:25600\n\t"
        "s_add_u32 s44, s44, 0x1000\n\t"
        "s_and_b32 s44, s44, 0xffff\n\t"
        "buffer_load_dwordx4 v[144:147], v192, s[40:43], s44 offen offset:0 sc1\n\t"
        "s_waitcnt vmcnt(11) lgkmcnt(14)\n\t"
        "v_mfma_f32_32x32x16_bf16 v[0:15], v[40:43], v[100:103], v[0:15]\n\t"
        "ds_read_b64_tr_b16 v[40:41], v193 offset:26624\n\t"
        "ds_read_b64_tr_b16 v[42:43], v193 offset:27648\n\t"
        "s_waitcnt lgkmcnt(14)\n\t"
        "v_mfma_f32_32x32x16_bf16 v[16:31], v[44:47], v[100:103], v[16:31]\n\t"
        "ds_read_b64_tr_b16 v[44:45], v194 offset:26624\n\t"
        "ds_read_b64_tr_b16 v[46:47], v194 offset:27648\n\t"
        "buffer_load_dwordx4 v[148:151], v192, s[40:43], s44 offen offset:1024 sc1\n\t"
        "s_waitcnt vmcnt(11) lgkmcnt(14)\n\t"
        "v_mfma_f32_32x32x16_bf16 v[0:15], v[48:51], v[104:107], v[0:15]\n\t"
        "ds_read_b64_tr_b16 v[48:49], v193 offset:28672\n\t"
        "ds_read_b64_tr_b16 v[50:51], v193 offset:29696\n\t"
        "s_waitcnt lgkmcnt(14)\n\t"
        "v_mfma_f32_32x32x16_bf16 v[16:31], v[52:55], v[104:107], v[16:31]\n\t"
        "ds_read_b64_tr_b16 v[52:53], v194 offset:28672\n\t"
        "ds_read_b64_tr_b16 v[54:55], v194 offset:29696\n\t"
        "buffer_load_dwordx4 v[152:155], v192, s[40:43], s44 offen offset:2048 sc1\n\t"
        "s_waitcnt vmcnt(11) lgkmcnt(14)\n\t"
        "v_mfma_f32_32x32x16_bf16 v[0:15], v[56:59], v[108:111], v[0:15]\n\t"
        "ds_read_b64_tr_b16 v[56:57], v193 offset:30720\n\t"
        "ds_read_b64_tr_b16 v[58:59], v193 offset:31744\n\t"
        "s_waitcnt lgkmcnt(14)\n\t"
        "v_mfma_f32_32x32x16_bf16 v[16:31], v[60:63], v[108:111], v[16:31]\n\t"
        "ds_read_b64_tr_b16 v[60:61], v194 offset:30720\n\t"
        "ds_read_b64_tr_b16 v[62:63], v194 offset:31744\n\t"
        "buffer_load_dwordx4 v[156:159], v192, s[40:43], s44 offen offset:3072 sc1\n\t"
        "s_waitcnt vmcnt(11) lgkmcnt(14)\n\t"
        "v_mfma_f32_32x32x16_bf16 v[0:15], v[32:35], v[112:115], v[0:15]\n\t"
        "ds_read_b64_tr_b16 v[32:33], v193 offset:32768\n\t"
        "ds_read_b64_tr_b16 v[34:35], v193 offset:33792\n\t"
        "s_waitcnt lgkmcnt(14)\n\t"
        "v_mfma_f32_32x32x16_bf16 v[16:31], v[36:39], v[112:115], v[16:31]\n\t"
        "ds_read_b64_tr_b16 v[36:37], v194 offset:32768\n\t"
        "ds_read_b64_tr_b16 v[38:39], v194 offset:33792\n\t"
        "s_add_u32 s44, s44, 0x1000\n\t"
        "s_and_b32 s44, s44, 0xffff\n\t"
        "buffer_load_dwordx4 v[160:163], v192, s[40:43], s44 offen offset:0 sc1\n\t"
        "s_waitcnt vmcnt(11) lgkmcnt(14)\n\t"
        "v_mfma_f32_32x32x16_bf16 v[0:15], v[40:43], v[116:119], v[0:15]\n\t"
        "ds_read_b64_tr_b16 v[40:41], v193 offset:34816\n\t"
        "ds_read_b64_tr_b16 v[42:43], v193 offset:35840\n\t"
        "s_waitcnt lgkmcnt(14)\n\t"
        "v_mfma_f32_32x32x16_bf16 v[16:31], v[44:47], v[116:119], v[16:31]\n\t"
        "ds_read_b64_tr_b16 v[44:45], v194 offset:34816\n\t"
        "ds_read_b64_tr_b16 v[46:47], v194 offset:35840\n\t"
        "buffer_load_dwordx4 v[164:167], v192, s[40:43], s44 offen offset:1024 sc1\n\t"
        "s_waitcnt vmcnt(11) lgkmcnt(14)\n\t"
        "v_mfma_f32_32x32x16_bf16 v[0:15], v[48:51], v[120:123], v[0:15]\n\t"
        "ds_read_b64_tr_b16 v[48:49], v193 offset:36864\n\t"
        "ds_read_b64_tr_b16 v[50:51], v193 offset:37888\n\t"
        "s_waitcnt lgkmcnt(14)\n\t"
        "v_mfma_f32_32x32x16_bf16 v[16:31], v[52:55], v[120:123], v[16:31]\n\t"
        "ds_read_b64_tr_b16 v[52:53], v194 offset:36864\n\t"
        "ds_read_b64_tr_b16 v[54:55], v194 offset:37888\n\t"
        "buffer_load_dwordx4 v[168:171], v192, s[40:43], s44 offen offset:2048 sc1\n\t"
        "s_waitcnt vmcnt(11) lgkmcnt(14)\n\t"
        "v_mfma_f32_32x32x16_bf16 v[0:15], v[56:59], v[124:127], v[0:15]\n\t"
        "ds_read_b64_tr_b16 v[56:57], v193 offset:38912\n\t"
        "ds_read_b64_tr_b16 v[58:59], v193 offset:39936\n\t"
        "s_waitcnt lgkmcnt(14)\n\t"
        "v_mfma_f32_32x32x16_bf16 v[16:31], v[60:63], v[124:127], v[16:31]\n\t"
        "ds_read_b64_tr_b16 v[60:61], v194 offset:38912\n\t"
        "ds_read_b64_tr_b16 v[62:63], v194 offset:39936\n\t"
        "buffer_load_dwordx4 v[172:175], v192, s[40:43], s44 offen offset:3072 sc1\n\t"
        "s_waitcnt vmcnt(11) lgkmcnt(14)\n\t"
        "v_mfma_f32_32x32x16_bf16 v[0:15], v[32:35], v[128:131], v[0:15]\n\t"
        "ds_read_b64_tr_b16 v[32:33], v193 offset:40960\n\t"
        "ds_read_b64_tr_b16 v[34:35], v193 offset:41984\n\t"
        "s_waitcnt lgkmcnt(14)\n\t"
        "v_mfma_f32_32x32x16_bf16 v[16:31], v[36:39], v[128:131], v[16:31]\n\t"
        "ds_read_b64_tr_b16 v[36:37], v194 offset:40960\n\t"
        "ds_read_b64_tr_b16 v[38:39], v194 offset:41984\n\t"
        "s_add_u32 s44, s44, 0x1000\n\t"
        "s_and_b32 s44, s44, 0xffff\n\t"
        "buffer_load_dwordx4 v[176:179], v192, s[40:43], s44 offen offset:0 sc1\n\t"
        "s_waitcnt vmcnt(11) lgkmcnt(14)\n\t"
        "v_mfma_f32_32x32x16_bf16 v[0:15], v[40:43], v[132:135], v[0:15]\n\t"
        "ds_read_b64_tr_b16 v[40:41], v193 offset:43008\n\t"
        "ds_read_b64_tr_b16 v[42:43], v193 offset:44032\n\t"
        "s_waitcnt lgkmcnt(14)\n\t"
        "v_mfma_f32_32x32x16_bf16 v[16:31], v[44:47], v[132:135], v[16:31]\n\t"
        "ds_read_b64_tr_b16 v[44:45], v194 offset:43008\n\t"
        "ds_read_b64_tr_b16 v[46:47], v194 offset:44032\n\t"
        "buffer_load_dwordx4 v[180:183], v192, s[40:43], s44 offen offset:1024 sc1\n\t"
        "s_waitcnt vmcnt(11) lgkmcnt(14)\n\t"
        "v_mfma_f32_32x32x16_bf16 v[0:15], v[48:51], v[136:139], v[0:15]\n\t"
        "ds_read_b64_tr_b16 v[48:49], v193 offset:45056\n\t"
        "ds_read_b64_tr_b16 v[50:51], v193 offset:46080\n\t"
        "s_waitcnt lgkmcnt(14)\n\t"
        "v_mfma_f32_32x32x16_bf16 v[16:31], v[52:55], v[136:139], v[16:31]\n\t"
        "ds_read_b64_tr_b16 v[52:53], v194 offset:45056\n\t"
        "ds_read_b64_tr_b16 v[54:55], v194 offset:46080\n\t"
        "buffer_load_dwordx4 v[184:187], v192, s[40:43], s44 offen offset:2048 sc1\n\t"
        "s_waitcnt vmcnt(11) lgkmcnt(14)\n\t"
        "v_mfma_f32_32x32x16_bf16 v[0:15], v[56:59], v[140:143], v[0:15]\n\t"
        "ds_read_b64_tr_b16 v[56:57], v193 offset:47104\n\t"
        "ds_read_b64_tr_b16 v[58:59], v193 offset:48128\n\t"
        "s_waitcnt lgkmcnt(14)\n\t"
        "v_mfma_f32_32x32x16_bf16 v[16:31], v[60:63], v[140:143], v[16:31]\n\t"
        "ds_read_b64_tr_b16 v[60:61], v194 offset:47104\n\t"
        "ds_read_b64_tr_b16 v[62:63], v194 offset:48128\n\t"
        "buffer_load_dwordx4 v[188:191], v192, s[40:43], s44 offen offset:3072 sc1\n\t"
        "s_waitcnt vmcnt(11) lgkmcnt(14)\n\t"
        "v_mfma_f32_32x32x16_bf16 v[0:15], v[32:35], v[144:147], v[0:15]\n\t"
        "ds_read_b64_tr_b16 v[32:33], v193 offset:49152\n\t"
        "ds_read_b64_tr_b16 v[34:35], v193 offset:50176\n\t"
        "s_waitcnt lgkmcnt(14)\n\t"
        "v_mfma_f32_32x32x16_bf16 v[16:31], v[36:39], v[144:147], v[16:31]\n\t"
        "ds_read_b64_tr_b16 v[36:37], v194 offset:49152\n\t"
        "ds_read_b64_tr_b16 v[38:39], v194 offset:50176\n\t"
        "s_add_u32 s44, s44, 0x1000\n\t"
        "s_and_b32 s44, s44, 0xffff\n\t"
        "buffer_load_dwordx4 v[64:67], v192, s[40:43], s44 offen offset:0 sc1\n\t"
        "s_waitcnt vmcnt(11) lgkmcnt(14)\n\t"
        "v_mfma_f32_32x32x16_bf16 v[0:15], v[40:43], v[148:151], v[0:15]\n\t"
        "ds_read_b64_tr_b16 v[40:41], v193 offset:51200\n\t"
        "ds_read_b64_tr_b16 v[42:43], v193 offset:52224\n\t"
        "s_waitcnt lgkmcnt(14)\n\t"
        "v_mfma_f32_32x32x16_bf16 v[16:31], v[44:47], v[148:151], v[16:31]\n\t"
        "ds_read_b64_tr_b16 v[44:45], v194 offset:51200\n\t"
        "ds_read_b64_tr_b16 v[46:47], v194 offset:52224\n\t"
        "buffer_load_dwordx4 v[68:71], v192, s[40:43], s44 offen offset:1024 sc1\n\t"
        "s_waitcnt vmcnt(11) lgkmcnt(14)\n\t"
        "v_mfma_f32_32x32x16_bf16 v[0:15], v[48:51], v[152:155], v[0:15]\n\t"
        "ds_read_b64_tr_b16 v[48:49], v193 offset:53248\n\t"
        "ds_read_b64_tr_b16 v[50:51], v193 offset:54272\n\t"
        "s_waitcnt lgkmcnt(14)\n\t"
        "v_mfma_f32_32x32x16_bf16 v[16:31], v[52:55], v[152:155], v[16:31]\n\t"
        "ds_read_b64_tr_b16 v[52:53], v194 offset:53248\n\t"
        "ds_read_b64_tr_b16 v[54:55], v194 offset:54272\n\t"
        "buffer_load_dwordx4 v[72:75], v192, s[40:43], s44 offen offset:2048 sc1\n\t"
        "s_waitcnt vmcnt(11) lgkmcnt(14)\n\t"
        "v_mfma_f32_32x32x16_bf16 v[0:15], v[56:59], v[156:159], v[0:15]\n\t"
        "ds_read_b64_tr_b16 v[56:57], v193 offset:55296\n\t"
        "ds_read_b64_tr_b16 v[58:59], v193 offset:56320\n\t"
        "s_waitcnt lgkmcnt(14)\n\t"
        "v_mfma_f32_32x32x16_bf16 v[16:31], v[60:63], v[156:159], v[16:31]\n\t"
        "ds_read_b64_tr_b16 v[60:61], v194 offset:55296\n\t"
        "ds_read_b64_tr_b16 v[62:63], v194 offset:56320\n\t"
        "buffer_load_dwordx4 v[76:79], v192, s[40:43], s44 offen offset:3072 sc1\n\t"
        "s_waitcnt vmcnt(11) lgkmcnt(14)\n\t"
        "v_mfma_f32_32x32x16_bf16 v[0:15], v[32:35], v[160:163], v[0:15]\n\t"
        "ds_read_b64_tr_b16 v[32:33], v193 offset:57344\n\t"
        "ds_read_b64_tr_b16 v[34:35], v193 offset:58368\n\t"
        "s_waitcnt lgkmcnt(14)\n\t"
        "v_mfma_f32_32x32x16_bf16 v[16:31], v[36:39], v[160:163], v[16:31]\n\t"
        "ds_read_b64_tr_b16 v[36:37], v194 offset:57344\n\t"
        "ds_read_b64_tr_b16 v[38:39], v194 offset:58368\n\t"
        "s_add_u32 s44, s44, 0x1000\n\t"
        "s_and_b32 s44, s44, 0xffff\n\t"
        "buffer_load_dwordx4 v[80:83], v192, s[40:43], s44 offen offset:0 sc1\n\t"
        "s_waitcnt vmcnt(11) lgkmcnt(14)\n\t"
        "v_mfma_f32_32x32x16_bf16 v[0:15], v[40:43], v[164:167], v[0:15]\n\t"
        "ds_read_b64_tr_b16 v[40:41], v193 offset:59392\n\t"
        "ds_read_b64_tr_b16 v[42:43], v193 offset:60416\n\t"
        "s_waitcnt lgkmcnt(14)\n\t"
        "v_mfma_f32_32x32x16_bf16 v[16:31], v[44:47], v[164:167], v[16:31]\n\t"
        "ds_read_b64_tr_b16 v[44:45], v194 offset:59392\n\t"
        "ds_read_b64_tr_b16 v[46:47], v194 offset:60416\n\t"
        "buffer_load_dwordx4 v[84:87], v192, s[40:43], s44 offen offset:1024 sc1\n\t"
        "s_waitcnt vmcnt(11) lgkmcnt(14)\n\t"
        "v_mfma_f32_32x32x16_bf16 v[0:15], v[48:51], v[168:171], v[0:15]\n\t"
        "ds_read_b64_tr_b16 v[48:49], v193 offset:61440\n\t"
        "ds_read_b64_tr_b16 v[50:51], v193 offset:62464\n\t"
        "s_waitcnt lgkmcnt(14)\n\t"
        "v_mfma_f32_32x32x16_bf16 v[16:31], v[52:55], v[168:171], v[16:31]\n\t"
        "ds_read_b64_tr_b16 v[52:53], v194 offset:61440\n\t"
        "ds_read_b64_tr_b16 v[54:55], v194 offset:62464\n\t"
        "buffer_load_dwordx4 v[88:91], v192, s[40:43], s44 offen offset:2048 sc1\n\t"
        "s_waitcnt vmcnt(11) lgkmcnt(14)\n\t"
        "v_mfma_f32_32x32x16_bf16 v[0:15], v[56:59], v[172:175], v[0:15]\n\t"
        "ds_read_b64_tr_b16 v[56:57], v193 offset:63488\n\t"
        "ds_read_b64_tr_b16 v[58:59], v193 offset:64512\n\t"
        "s_waitcnt lgkmcnt(14)\n\t"
        "v_mfma_f32_32x32x16_bf16 v[16:31], v[60:63], v[172:175], v[16:31]\n\t"
        "ds_read_b64_tr_b16 v[60:61], v194 offset:63488\n\t"
        "ds_read_b64_tr_b16 v[62:63], v194 offset:64512\n\t"
        "buffer_load_dwordx4 v[92:95], v192, s[40:43], s44 offen offset:3072 sc1\n\t"
        "s_waitcnt vmcnt(11) lgkmcnt(14)\n\t"
        "v_mfma_f32_32x32x16_bf16 v[0:15], v[32:35], v[176:179], v[0:15]\n\t"
        "ds_read_b64_tr_b16 v[32:33], v195 offset:0\n\t"
        "ds_read_b64_tr_b16 v[34:35], v195 offset:1024\n\t"
        "s_waitcnt lgkmcnt(14)\n\t"
        "v_mfma_f32_32x32x16_bf16 v[16:31], v[36:39], v[176:179], v[16:31]\n\t"
        "ds_read_b64_tr_b16 v[36:37], v196 offset:0\n\t"
        "ds_read_b64_tr_b16 v[38:39], v196 offset:1024\n\t"
        "s_add_u32 s44, s44, 0x1000\n\t"
        "s_and_b32 s44, s44, 0xffff\n\t"
        "buffer_load_dwordx4 v[96:99], v192, s[40:43], s44 offen offset:0 sc1\n\t"
        "s_waitcnt vmcnt(11) lgkmcnt(14)\n\t"
        "v_mfma_f32_32x32x16_bf16 v[0:15], v[40:43], v[180:183], v[0:15]\n\t"
        "ds_read_b64_tr_b16 v[40:41], v195 offset:2048\n\t"
        "ds_read_b64_tr_b16 v[42:43], v195 offset:3072\n\t"
        "s_waitcnt lgkmcnt(14)\n\t"
        "v_mfma_f32_32x32x16_bf16 v[16:31], v[44:47], v[180:183], v[16:31]\n\t"
        "ds_read_b64_tr_b16 v[44:45], v196 offset:2048\n\t"
        "ds_read_b64_tr_b16 v[46:47], v196 offset:3072\n\t"
        "buffer_load_dwordx4 v[100:103], v192, s[40:43], s44 offen offset:1024 sc1\n\t"
        "s_waitcnt vmcnt(11) lgkmcnt(14)\n\t"
        "v_mfma_f32_32x32x16_bf16 v[0:15], v[48:51], v[184:187], v[0:15]\n\t"
        "ds_read_b64_tr_b16 v[48:49], v195 offset:4096\n\t"
        "ds_read_b64_tr_b16 v[50:51], v195 offset:5120\n\t"
        "s_waitcnt lgkmcnt(14)\n\t"
        "v_mfma_f32_32x32x16_bf16 v[16:31], v[52:55], v[184:187], v[16:31]\n\t"
        "ds_read_b64_tr_b16 v[52:53], v196 offset:4096\n\t"
        "ds_read_b64_tr_b16 v[54:55], v196 offset:5120\n\t"
        "buffer_load_dwordx4 v[104:107], v192, s[40:43], s44 offen offset:2048 sc1\n\t"
        "s_waitcnt vmcnt(11) lgkmcnt(14)\n\t"
        "v_mfma_f32_32x32x16_bf16 v[0:15], v[56:59], v[188:191], v[0:15]\n\t"
        "ds_read_b64_tr_b16 v[56:57], v195 offset:6144\n\t"
        "ds_read_b64_tr_b16 v[58:59], v195 offset:7168\n\t"
        "s_waitcnt lgkmcnt(14)\n\t"
        "v_mfma_f32_32x32x16_bf16 v[16:31], v[60:63], v[188:191], v[16:31]\n\t"
        "ds_read_b64_tr_b16 v[60:61], v196 offset:6144\n\t"
        "ds_read_b64_tr_b16 v[62:63], v196 offset:7168\n\t"
        "buffer_load_dwordx4 v[108:111], v192, s[40:43], s44 offen offset:3072 sc1\n\t"
        "s_waitcnt vmcnt(11) lgkmcnt(14)\n\t"
        "v_mfma_f32_32x32x16_bf16 v[0:15], v[32:35], v[64:67], v[0:15]\n\t"
        "ds_read_b64_tr_b16 v[32:33], v195 offset:8192\n\t"
        "ds_read_b64_tr_b16 v[34:35], v195 offset:9216\n\t"
        "s_waitcnt lgkmcnt(14)\n\t"
        "v_mfma_f32_32x32x16_bf16 v[16:31], v[36:39], v[64:67], v[16:31]\n\t"
        "ds_read_b64_tr_b16 v[36:37], v196 offset:8192\n\t"
        "ds_read_b64_tr_b16 v[38:39], v196 offset:9216\n\t"
        "s_add_u32 s44, s44, 0x1000\n\t"
        "s_and_b32 s44, s44, 0xffff\n\t"
        "buffer_load_dwordx4 v[112:115], v192, s[40:43], s44 offen offset:0 sc1\n\t"
        "s_waitcnt vmcnt(11) lgkmcnt(14)\n\t"
        "v_mfma_f32_32x32x16_bf16 v[0:15], v[40:43], v[68:71], v[0:15]\n\t"
        "ds_read_b64_tr_b16 v[40:41], v195 offset:10240\n\t"
        "ds_read_b64_tr_b16 v[42:43], v195 offset:11264\n\t"
        "s_waitcnt lgkmcnt(14)\n\t"
        "v_mfma_f32_32x32x16_bf16 v[16:31], v[44:47], v[68:71], v[16:31]\n\t"
        "ds_read_b64_tr_b16 v[44:45], v196 offset:10240\n\t"
        "ds_read_b64_tr_b16 v[46:47], v196 offset:11264\n\t"
        "buffer_load_dwordx4 v[116:119], v192, s[40:43], s44 offen offset:1024 sc1\n\t"
        "s_waitcnt vmcnt(11) lgkmcnt(14)\n\t"
        "v_mfma_f32_32x32x16_bf16 v[0:15], v[48:51], v[72:75], v[0:15]\n\t"
        "ds_read_b64_tr_b16 v[48:49], v195 offset:12288\n\t"
        "ds_read_b64_tr_b16 v[50:51], v195 offset:13312\n\t"
        "s_waitcnt lgkmcnt(14)\n\t"
        "v_mfma_f32_32x32x16_bf16 v[16:31], v[52:55], v[72:75], v[16:31]\n\t"
        "ds_read_b64_tr_b16 v[52:53], v196 offset:12288\n\t"
        "ds_read_b64_tr_b16 v[54:55], v196 offset:13312\n\t"
        "buffer_load_dwordx4 v[120:123], v192, s[40:43], s44 offen offset:2048 sc1\n\t"
        "s_waitcnt vmcnt(11) lgkmcnt(14)\n\t"
        "v_mfma_f32_32x32x16_bf16 v[0:15], v[56:59], v[76:79], v[0:15]\n\t"
        "ds_read_b64_tr_b16 v[56:57], v195 offset:14336\n\t"
        "ds_read_b64_tr_b16 v[58:59], v195 offset:15360\n\t"
        "s_waitcnt lgkmcnt(14)\n\t"
        "v_mfma_f32_32x32x16_bf16 v[16:31], v[60:63], v[76:79], v[16:31]\n\t"
        "ds_read_b64_tr_b16 v[60:61], v196 offset:14336\n\t"
        "ds_read_b64_tr_b16 v[62:63], v196 offset:15360\n\t"
        "buffer_load_dwordx4 v[124:127], v192, s[40:43], s44 offen offset:3072 sc1\n\t"
        "s_waitcnt vmcnt(11) lgkmcnt(14)\n\t"
        "v_mfma_f32_32x32x16_bf16 v[0:15], v[32:35], v[80:83], v[0:15]\n\t"
        "ds_read_b64_tr_b16 v[32:33], v195 offset:16384\n\t"
        "ds_read_b64_tr_b16 v[34:35], v195 offset:17408\n\t"
        "s_waitcnt lgkmcnt(14)\n\t"
        "v_mfma_f32_32x32x16_bf16 v[16:31], v[36:39], v[80:83], v[16:31]\n\t"
        "ds_read_b64_tr_b16 v[36:37], v196 offset:16384\n\t"
        "ds_read_b64_tr_b16 v[38:39], v196 offset:17408\n\t"
        "s_add_u32 s44, s44, 0x1000\n\t"
        "s_and_b32 s44, s44, 0xffff\n\t"
        "buffer_load_dwordx4 v[128:131], v192, s[40:43], s44 offen offset:0 sc1\n\t"
        "s_waitcnt vmcnt(11) lgkmcnt(14)\n\t"
        "v_mfma_f32_32x32x16_bf16 v[0:15], v[40:43], v[84:87], v[0:15]\n\t"
        "ds_read_b64_tr_b16 v[40:41], v195 offset:18432\n\t"
        "ds_read_b64_tr_b16 v[42:43], v195 offset:19456\n\t"
        "s_waitcnt lgkmcnt(14)\n\t"
        "v_mfma_f32_32x32x16_bf16 v[16:31], v[44:47], v[84:87], v[16:31]\n\t"
        "ds_read_b64_tr_b16 v[44:45], v196 offset:18432\n\t"
        "ds_read_b64_tr_b16 v[46:47], v196 offset:19456\n\t"
        "buffer_load_dwordx4 v[132:135], v192, s[40:43], s44 offen offset:1024 sc1\n\t"
        "s_waitcnt vmcnt(11) lgkmcnt(14)\n\t"
        "v_mfma_f32_32x32x16_bf16 v[0:15], v[48:51], v[88:91], v[0:15]\n\t"
        "ds_read_b64_tr_b16 v[48:49], v195 offset:20480\n\t"
        "ds_read_b64_tr_b16 v[50:51], v195 offset:21504\n\t"
        "s_waitcnt lgkmcnt(14)\n\t"
        "v_mfma_f32_32x32x16_bf16 v[16:31], v[52:55], v[88:91], v[16:31]\n\t"
        "ds_read_b64_tr_b16 v[52:53], v196 offset:20480\n\t"
        "ds_read_b64_tr_b16 v[54:55], v196 offset:21504\n\t"
        "buffer_load_dwordx4 v[136:139], v192, s[40:43], s44 offen offset:2048 sc1\n\t"
        "s_waitcnt vmcnt(11) lgkmcnt(14)\n\t"
        "v_mfma_f32_32x32x16_bf16 v[0:15], v[56:59], v[92:95], v[0:15]\n\t"
        "ds_read_b64_tr_b16 v[56:57], v195 offset:22528\n\t"
        "ds_read_b64_tr_b16 v[58:59], v195 offset:23552\n\t"
        "s_waitcnt lgkmcnt(14)\n\t"
        "v_mfma_f32_32x32x16_bf16 v[16:31], v[60:63], v[92:95], v[16:31]\n\t"
        "ds_read_b64_tr_b16 v[60:61], v196 offset:22528\n\t"
        "ds_read_b64_tr_b16 v[62:63], v196 offset:23552\n\t"
        "buffer_load_dwordx4 v[140:143], v192, s[40:43], s44 offen offset:3072 sc1\n\t"
        "s_waitcnt vmcnt(11) lgkmcnt(14)\n\t"
        "v_mfma_f32_32x32x16_bf16 v[0:15], v[32:35], v[96:99], v[0:15]\n\t"
        "ds_read_b64_tr_b16 v[32:33], v195 offset:24576\n\t"
        "ds_read_b64_tr_b16 v[34:35], v195 offset:25600\n\t"
        "s_waitcnt lgkmcnt(14)\n\t"
        "v_mfma_f32_32x32x16_bf16 v[16:31], v[36:39], v[96:99], v[16:31]\n\t"
        "ds_read_b64_tr_b16 v[36:37], v196 offset:24576\n\t"
        "ds_read_b64_tr_b16 v[38:39], v196 offset:25600\n\t"
        "s_add_u32 s44, s44, 0x1000\n\t"
        "s_and_b32 s44, s44, 0xffff\n\t"
        "buffer_load_dwordx4 v[144:147], v192, s[40:43], s44 offen offset:0 sc1\n\t"
        "s_waitcnt vmcnt(11) lgkmcnt(14)\n\t"
        "v_mfma_f32_32x32x16_bf16 v[0:15], v[40:43], v[100:103], v[0:15]\n\t"
        "ds_read_b64_tr_b16 v[40:41], v195 offset:26624\n\t"
        "ds_read_b64_tr_b16 v[42:43], v195 offset:27648\n\t"
        "s_waitcnt lgkmcnt(14)\n\t"
        "v_mfma_f32_32x32x16_bf16 v[16:31], v[44:47], v[100:103], v[16:31]\n\t"
        "ds_read_b64_tr_b16 v[44:45], v196 offset:26624\n\t"
        "ds_read_b64_tr_b16 v[46:47], v196 offset:27648\n\t"
        "buffer_load_dwordx4 v[148:151], v192, s[40:43], s44 offen offset:1024 sc1\n\t"
        "s_waitcnt vmcnt(11) lgkmcnt(14)\n\t"
        "v_mfma_f32_32x32x16_bf16 v[0:15], v[48:51], v[104:107], v[0:15]\n\t"
        "ds_read_b64_tr_b16 v[48:49], v195 offset:28672\n\t"
        "ds_read_b64_tr_b16 v[50:51], v195 offset:29696\n\t"
        "s_waitcnt lgkmcnt(14)\n\t"
        "v_mfma_f32_32x32x16_bf16 v[16:31], v[52:55], v[104:107], v[16:31]\n\t"
        "ds_read_b64_tr_b16 v[52:53], v196 offset:28672\n\t"
        "ds_read_b64_tr_b16 v[54:55], v196 offset:29696\n\t"
        "buffer_load_dwordx4 v[152:155], v192, s[40:43], s44 offen offset:2048 sc1\n\t"
        "s_waitcnt vmcnt(11) lgkmcnt(14)\n\t"
        "v_mfma_f32_32x32x16_bf16 v[0:15], v[56:59], v[108:111], v[0:15]\n\t"
        "ds_read_b64_tr_b16 v[56:57], v195 offset:30720\n\t"
        "ds_read_b64_tr_b16 v[58:59], v195 offset:31744\n\t"
        "s_waitcnt lgkmcnt(14)\n\t"
        "v_mfma_f32_32x32x16_bf16 v[16:31], v[60:63], v[108:111], v[16:31]\n\t"
        "ds_read_b64_tr_b16 v[60:61], v196 offset:30720\n\t"
        "ds_read_b64_tr_b16 v[62:63], v196 offset:31744\n\t"
        "buffer_load_dwordx4 v[156:159], v192, s[40:43], s44 offen offset:3072 sc1\n\t"
        "s_waitcnt vmcnt(11) lgkmcnt(14)\n\t"
        "v_mfma_f32_32x32x16_bf16 v[0:15], v[32:35], v[112:115], v[0:15]\n\t"
        "ds_read_b64_tr_b16 v[32:33], v195 offset:32768\n\t"
        "ds_read_b64_tr_b16 v[34:35], v195 offset:33792\n\t"
        "s_waitcnt lgkmcnt(14)\n\t"
        "v_mfma_f32_32x32x16_bf16 v[16:31], v[36:39], v[112:115], v[16:31]\n\t"
        "ds_read_b64_tr_b16 v[36:37], v196 offset:32768\n\t"
        "ds_read_b64_tr_b16 v[38:39], v196 offset:33792\n\t"
        "s_add_u32 s44, s44, 0x1000\n\t"
        "s_and_b32 s44, s44, 0xffff\n\t"
        "buffer_load_dwordx4 v[160:163], v192, s[40:43], s44 offen offset:0 sc1\n\t"
        "s_waitcnt vmcnt(11) lgkmcnt(14)\n\t"
        "v_mfma_f32_32x32x16_bf16 v[0:15], v[40:43], v[116:119], v[0:15]\n\t"
        "ds_read_b64_tr_b16 v[40:41], v195 offset:34816\n\t"
        "ds_read_b64_tr_b16 v[42:43], v195 offset:35840\n\t"
        "s_waitcnt lgkmcnt(14)\n\t"
        "v_mfma_f32_32x32x16_bf16 v[16:31], v[44:47], v[116:119], v[16:31]\n\t"
        "ds_read_b64_tr_b16 v[44:45], v196 offset:34816\n\t"
        "ds_read_b64_tr_b16 v[46:47], v196 offset:35840\n\t"
        "buffer_load_dwordx4 v[164:167], v192, s[40:43], s44 offen offset:1024 sc1\n\t"
        "s_waitcnt vmcnt(11) lgkmcnt(14)\n\t"
        "v_mfma_f32_32x32x16_bf16 v[0:15], v[48:51], v[120:123], v[0:15]\n\t"
        "ds_read_b64_tr_b16 v[48:49], v195 offset:36864\n\t"
        "ds_read_b64_tr_b16 v[50:51], v195 offset:37888\n\t"
        "s_waitcnt lgkmcnt(14)\n\t"
        "v_mfma_f32_32x32x16_bf16 v[16:31], v[52:55], v[120:123], v[16:31]\n\t"
        "ds_read_b64_tr_b16 v[52:53], v196 offset:36864\n\t"
        "ds_read_b64_tr_b16 v[54:55], v196 offset:37888\n\t"
        "buffer_load_dwordx4 v[168:171], v192, s[40:43], s44 offen offset:2048 sc1\n\t"
        "s_waitcnt vmcnt(11) lgkmcnt(14)\n\t"
        "v_mfma_f32_32x32x16_bf16 v[0:15], v[56:59], v[124:127], v[0:15]\n\t"
        "ds_read_b64_tr_b16 v[56:57], v195 offset:38912\n\t"
        "ds_read_b64_tr_b16 v[58:59], v195 offset:39936\n\t"
        "s_waitcnt lgkmcnt(14)\n\t"
        "v_mfma_f32_32x32x16_bf16 v[16:31], v[60:63], v[124:127], v[16:31]\n\t"
        "ds_read_b64_tr_b16 v[60:61], v196 offset:38912\n\t"
        "ds_read_b64_tr_b16 v[62:63], v196 offset:39936\n\t"
        "buffer_load_dwordx4 v[172:175], v192, s[40:43], s44 offen offset:3072 sc1\n\t"
        "s_waitcnt vmcnt(11) lgkmcnt(14)\n\t"
        "v_mfma_f32_32x32x16_bf16 v[0:15], v[32:35], v[128:131], v[0:15]\n\t"
        "ds_read_b64_tr_b16 v[32:33], v195 offset:40960\n\t"
        "ds_read_b64_tr_b16 v[34:35], v195 offset:41984\n\t"
        "s_waitcnt lgkmcnt(14)\n\t"
        "v_mfma_f32_32x32x16_bf16 v[16:31], v[36:39], v[128:131], v[16:31]\n\t"
        "ds_read_b64_tr_b16 v[36:37], v196 offset:40960\n\t"
        "ds_read_b64_tr_b16 v[38:39], v196 offset:41984\n\t"
        "s_add_u32 s44, s44, 0x1000\n\t"
        "s_and_b32 s44, s44, 0xffff\n\t"
        "buffer_load_dwordx4 v[176:179], v192, s[40:43], s44 offen offset:0 sc1\n\t"
        "s_waitcnt vmcnt(11) lgkmcnt(14)\n\t"
        "v_mfma_f32_32x32x16_bf16 v[0:15], v[40:43], v[132:135], v[0:15]\n\t"
        "ds_read_b64_tr_b16 v[40:41], v195 offset:43008\n\t"
        "ds_read_b64_tr_b16 v[42:43], v195 offset:44032\n\t"
        "s_waitcnt lgkmcnt(14)\n\t"
        "v_mfma_f32_32x32x16_bf16 v[16:31], v[44:47], v[132:135], v[16:31]\n\t"
        "ds_read_b64_tr_b16 v[44:45], v196 offset:43008\n\t"
        "ds_read_b64_tr_b16 v[46:47], v196 offset:44032\n\t"
        "buffer_load_dwordx4 v[180:183], v192, s[40:43], s44 offen offset:1024 sc1\n\t"
        "s_waitcnt vmcnt(11) lgkmcnt(14)\n\t"
        "v_mfma_f32_32x32x16_bf16 v[0:15], v[48:51], v[136:139], v[0:15]\n\t"
        "ds_read_b64_tr_b16 v[48:49], v195 offset:45056\n\t"
        "ds_read_b64_tr_b16 v[50:51], v195 offset:46080\n\t"
        "s_waitcnt lgkmcnt(14)\n\t"
        "v_mfma_f32_32x32x16_bf16 v[16:31], v[52:55], v[136:139], v[16:31]\n\t"
        "ds_read_b64_tr_b16 v[52:53], v196 offset:45056\n\t"
        "ds_read_b64_tr_b16 v[54:55], v196 offset:46080\n\t"
        "buffer_load_dwordx4 v[184:187], v192, s[40:43], s44 offen offset:2048 sc1\n\t"
        "s_waitcnt vmcnt(11) lgkmcnt(14)\n\t"
        "v_mfma_f32_32x32x16_bf16 v[0:15], v[56:59], v[140:143], v[0:15]\n\t"
        "ds_read_b64_tr_b16 v[56:57], v195 offset:47104\n\t"
        "ds_read_b64_tr_b16 v[58:59], v195 offset:48128\n\t"
        "s_waitcnt lgkmcnt(14)\n\t"
        "v_mfma_f32_32x32x16_bf16 v[16:31], v[60:63], v[140:143], v[16:31]\n\t"
        "ds_read_b64_tr_b16 v[60:61], v196 offset:47104\n\t"
        "ds_read_b64_tr_b16 v[62:63], v196 offset:48128\n\t"
        "buffer_load_dwordx4 v[188:191], v192, s[40:43], s44 offen offset:3072 sc1\n\t"
        "s_waitcnt vmcnt(11) lgkmcnt(14)\n\t"
        "v_mfma_f32_32x32x16_bf16 v[0:15], v[32:35], v[144:147], v[0:15]\n\t"
        "ds_read_b64_tr_b16 v[32:33], v195 offset:49152\n\t"
        "ds_read_b64_tr_b16 v[34:35], v195 offset:50176\n\t"
        "s_waitcnt lgkmcnt(14)\n\t"
        "v_mfma_f32_32x32x16_bf16 v[16:31], v[36:39], v[144:147], v[16:31]\n\t"
        "ds_read_b64_tr_b16 v[36:37], v196 offset:49152\n\t"
        "ds_read_b64_tr_b16 v[38:39], v196 offset:50176\n\t"
        "s_waitcnt vmcnt(10) lgkmcnt(14)\n\t"
        "v_mfma_f32_32x32x16_bf16 v[0:15], v[40:43], v[148:151], v[0:15]\n\t"
        "ds_read_b64_tr_b16 v[40:41], v195 offset:51200\n\t"
        "ds_read_b64_tr_b16 v[42:43], v195 offset:52224\n\t"
        "s_waitcnt lgkmcnt(14)\n\t"
        "v_mfma_f32_32x32x16_bf16 v[16:31], v[44:47], v[148:151], v[16:31]\n\t"
        "ds_read_b64_tr_b16 v[44:45], v196 offset:51200\n\t"
        "ds_read_b64_tr_b16 v[46:47], v196 offset:52224\n\t"
        "s_waitcnt vmcnt(9) lgkmcnt(14)\n\t"
        "v_mfma_f32_32x32x16_bf16 v[0:15], v[48:51], v[152:155], v[0:15]\n\t"
        "ds_read_b64_tr_b16 v[48:49], v195 offset:53248\n\t"
        "ds_read_b64_tr_b16 v[50:51], v195 offset:54272\n\t"
        "s_waitcnt lgkmcnt(14)\n\t"
        "v_mfma_f32_32x32x16_bf16 v[16:31], v[52:55], v[152:155], v[16:31]\n\t"
        "ds_read_b64_tr_b16 v[52:53], v196 offset:53248\n\t"
        "ds_read_b64_tr_b16 v[54:55], v196 offset:54272\n\t"
        "s_waitcnt vmcnt(8) lgkmcnt(14)\n\t"
        "v_mfma_f32_32x32x16_bf16 v[0:15], v[56:59], v[156:159], v[0:15]\n\t"
        "ds_read_b64_tr_b16 v[56:57], v195 offset:55296\n\t"
        "ds_read_b64_tr_b16 v[58:59], v195 offset:56320\n\t"
        "s_waitcnt lgkmcnt(14)\n\t"
        "v_mfma_f32_32x32x16_bf16 v[16:31], v[60:63], v[156:159], v[16:31]\n\t"
        "ds_read_b64_tr_b16 v[60:61], v196 offset:55296\n\t"
        "ds_read_b64_tr_b16 v[62:63], v196 offset:56320\n\t"
        "s_waitcnt vmcnt(7) lgkmcnt(14)\n\t"
        "v_mfma_f32_32x32x16_bf16 v[0:15], v[32:35], v[160:163], v[0:15]\n\t"
        "s_barrier\n\t"
        "ds_read_b64_tr_b16 v[32:33], v195 offset:57344\n\t"
        "ds_read_b64_tr_b16 v[34:35], v195 offset:58368\n\t"
        "s_waitcnt lgkmcnt(14)\n\t"
        "v_mfma_f32_32x32x16_bf16 v[16:31], v[36:39], v[160:163], v[16:31]\n\t"
        "ds_read_b64_tr_b16 v[36:37], v196 offset:57344\n\t"
        "ds_read_b64_tr_b16 v[38:39], v196 offset:58368\n\t"
        "s_waitcnt vmcnt(6) lgkmcnt(14)\n\t"
        "v_mfma_f32_32x32x16_bf16 v[0:15], v[40:43], v[164:167], v[0:15]\n\t"
        "ds_read_b64_tr_b16 v[40:41], v195 offset:59392\n\t"
        "ds_read_b64_tr_b16 v[42:43], v195 offset:60416\n\t"
        "s_waitcnt lgkmcnt(14)\n\t"
        "v_mfma_f32_32x32x16_bf16 v[16:31], v[44:47], v[164:167], v[16:31]\n\t"
        "ds_read_b64_tr_b16 v[44:45], v196 offset:59392\n\t"
        "ds_read_b64_tr_b16 v[46:47], v196 offset:60416\n\t"
        "s_waitcnt vmcnt(5) lgkmcnt(14)\n\t"
        "v_mfma_f32_32x32x16_bf16 v[0:15], v[48:51], v[168:171], v[0:15]\n\t"
        "ds_read_b64_tr_b16 v[48:49], v195 offset:61440\n\t"
        "ds_read_b64_tr_b16 v[50:51], v195 offset:62464\n\t"
        "s_waitcnt lgkmcnt(14)\n\t"
        "v_mfma_f32_32x32x16_bf16 v[16:31], v[52:55], v[168:171], v[16:31]\n\t"
        "ds_read_b64_tr_b16 v[52:53], v196 offset:61440\n\t"
        "ds_read_b64_tr_b16 v[54:55], v196 offset:62464\n\t"
        "s_waitcnt vmcnt(4) lgkmcnt(14)\n\t"
        "v_mfma_f32_32x32x16_bf16 v[0:15], v[56:59], v[172:175], v[0:15]\n\t"
        "ds_read_b64_tr_b16 v[56:57], v195 offset:63488\n\t"
        "ds_read_b64_tr_b16 v[58:59], v195 offset:64512\n\t"
        "s_waitcnt lgkmcnt(14)\n\t"
        "v_mfma_f32_32x32x16_bf16 v[16:31], v[60:63], v[172:175], v[16:31]\n\t"
        "ds_read_b64_tr_b16 v[60:61], v196 offset:63488\n\t"
        "ds_read_b64_tr_b16 v[62:63], v196 offset:64512\n\t"
        "s_waitcnt vmcnt(3) lgkmcnt(14)\n\t"
        "v_mfma_f32_32x32x16_bf16 v[0:15], v[32:35], v[176:179], v[0:15]\n\t"
        "s_waitcnt lgkmcnt(12)\n\t"
        "v_mfma_f32_32x32x16_bf16 v[16:31], v[36:39], v[176:179], v[16:31]\n\t"
        "s_waitcnt vmcnt(2) lgkmcnt(10)\n\t"
        "v_mfma_f32_32x32x16_bf16 v[0:15], v[40:43], v[180:183], v[0:15]\n\t"
        "s_waitcnt lgkmcnt(8)\n\t"
        "v_mfma_f32_32x32x16_bf16 v[16:31], v[44:47], v[180:183], v[16:31]\n\t"
        "s_waitcnt vmcnt(1) lgkmcnt(6)\n\t"
        "v_mfma_f32_32x32x16_bf16 v[0:15], v[48:51], v[184:187], v[0:15]\n\t"
        "s_waitcnt lgkmcnt(4)\n\t"
        "v_mfma_f32_32x32x16_bf16 v[16:31], v[52:55], v[184:187], v[16:31]\n\t"
        "s_waitcnt vmcnt(0) lgkmcnt(2)\n\t"
        "v_mfma_f32_32x32x16_bf16 v[0:15], v[56:59], v[188:191], v[0:15]\n\t"
        "s_waitcnt lgkmcnt(0)\n\t"
        "v_mfma_f32_32x32x16_bf16 v[16:31], v[60:63], v[188:191], v[16:31]\n\t"
        "s_nop 11\n\t"
        : "+{v[0:15]}"(acc[0]), "+{v[16:31]}"(acc[1]), "+{s44}"(soff) STAMP_OUTS
        : "{v192}"(OFF_OF + L * (5 * MBY) + tile * TS + lane * 16), "{v193}"(a0), "{v194}"(a1), "{v195}"(a0 + 65536), "{v196}"(a1 + 65536),
          "{v198}"(lbase + L_W + 20), "{v199}"(L + 1), "{s[40:43]}"(srd)
        : "v32", "v33", "v34", "v35", "v36", "v37", "v38", "v39", "v40", "v41", "v42", "v43", "v44", "v45", "v46", "v47", "v48", "v49", "v50", "v51", "v52", "v53", "v54", "v55", "v56", "v57", "v58", "v59", "v60", "v61", "v62", "v63", "v64", "v65", "v66", "v67", "v68", "v69", "v70", "v71", "v72", "v73", "v74", "v75", "v76", "v77", "v78", "v79", "v80", "v81", "v82", "v83", "v84", "v85", "v86", "v87", "v88", "v89", "v90", "v91", "v92", "v93", "v94", "v95", "v96", "v97", "v98", "v99", "v100", "v101", "v102", "v103", "v104", "v105", "v106", "v107", "v108", "v109", "v110", "v111", "v112", "v113", "v114", "v115", "v116", "v117", "v118", "v119", "v120", "v121", "v122", "v123", "v124", "v125", "v126", "v127", "v128", "v129", "v130", "v131", "v132", "v133", "v134", "v135", "v136", "v137", "v138", "v139", "v140", "v141", "v142", "v143", "v144", "v145", "v146", "v147", "v148", "v149", "v150", "v151", "v152", "v153", "v154", "v155", "v156", "v157", "v158", "v159", "v160", "v161", "v162", "v163", "v164", "v165", "v166", "v167", "v168", "v169", "v170", "v171", "v172", "v173", "v174", "v175", "v176", "v177", "v178", "v179", "v180", "v181", "v182", "v183", "v184", "v185", "v186", "v187", "v188", "v189", "v190", "v191", "v200", "s45", "vcc", "scc", "memory");
      }
      PHASE_VARS;
      STAMP_PUT(12 + 16 * L);
      STAMPV(15 + 16 * L);
      float s = 0.f, sq = 0.f, d = 0.f;
      const unsigned xdst = (L ? OOB : OFF_X1 + tile * TS + tn * 4096) + lane * 16;
#pragma unroll
      for (int i = 0; i < 4; ++i) {
#pragma unroll
        for (int e = 0; e < 4; ++e) x1p[i][e] = pack2(acc[i >> 1][8 * (i & 1) + 2 * e], acc[i >> 1][8 * (i & 1) + 2 * e + 1]);
        stg(rs, xdst + i * 1024, x1p[i]);
        SBAR();
      }
#pragma unroll
      for (int g = 0; g < 8; ++g)
#pragma unroll
        for (int e = 0; e < 2; ++e) {
          const unsigned o = x1p[g >> 1][2 * (g & 1) + e];
          s = dot2(o, 0x3f803f80u, s); sq = dot2(o, o, sq); d = dot2(o, wrc[g][e], d);
        }
      const u32x4 pst = {nzf(half_swap_sum(s)), nzf(half_swap_sum(sq)), nzf(half_swap_sum(d)), 0u};
      asm volatile("buffer_store_dwordx4 %0, %1, %2, 0 offen sc1\n\ts_waitcnt vmcnt(1)\n\tds_write_b32 %3, %4"
                   :: "v"(pst), "v"(stats_dst(sx, tile, tn, lane)), "s"(srd), "v"(lbase + L_W + 64 + 4 * wid), "v"(3) : "memory");
    }
    STAMPV(16 + 16 * L);
  }
  {
    PHASE_VARS;
    float S, Q, Dd, mean, var, mb1, rd1;
    __builtin_amdgcn_s_sleep(13);
    row_stats_sum(rs, OFF_SX + SX_LAYER, tile, lane, S, Q, Dd);
    STAMPV(33);
    row_stats_fin(smem, 2, tile, lane, S, Q, Dd, U2F(words[0]), x1p, mean, var, mb1, rd1);
    STAMPV(34);
    u32x4 xo[4];
#pragma unroll
    for (int i = 0; i < 4; ++i) {
#pragma unroll
      for (int e = 0; e < 4; ++e) {
        const unsigned h2 = ln_pair(x1p[i][e], mb1, rd1);
        xo[i][e] = pack2(lo16(x1p[i][e]) + lo16(h2), hi16(x1p[i][e]) + hi16(h2));
      }
      *reinterpret_cast<u32x4*>(smem + wid * 4096 + r32 * 128 + ((2 * i + hi) ^ (r32 & 7)) * 16) = fo_swap(xo[i]);
    }
    asm volatile("s_waitcnt lgkmcnt(0)" ::: "memory");
#pragma unroll
    for (int i = 0; i < 4; ++i) { xo[i] = *reinterpret_cast<const u32x4*>(smem + wid * 4096 + i * 1024 + (lane >> 3) * 128 + ((lane ^ (lane >> 3)) & 7) * 16); PIN(xo[i]); }
    u16* dst = out + (size_t)(tile * 32 + (lane >> 3)) * D_ + tn * 64 + (lane & 7) * 8;
    lds_wait_ge(lbase + L_W + 4 + 4 * (lane & 1), 2);
    const int rel = (int)words[3] - tile * 32 - (lane >> 3);
#pragma unroll
    for (int i = 0; i < 4; ++i)
      if (rel != 8 * i) st16(dst + (size_t)i * 8 * D_, xo[i]);
    STAMPV(35);
    STAMP_END;
  }
}

extern "C" __global__ void end_marker() {}

extern "C" void kernel_launch(void* const* d_in, const int* in_sizes, int n_in, void* d_out, int out_size,
                              void* d_ws, size_t ws_size, hipStream_t stream) {
  char* ws = (char*)d_ws;
  mega_kernel<<<256, 512, 0, stream>>>((const u16*)d_in[0], (const u16*)d_in[1], (const u16*)d_in[2], (const u16*)d_in[4], (const u16*)d_in[5],
                                       (const u16*)d_in[6], (const u16*)d_in[7], ws, (u16*)d_out);
}
```

```cpp
#include <hip/hip_runtime.h>
#include <hip/hip_bf16.h>
#include <stdint.h>

typedef unsigned short u16;
typedef short s16x4 __attribute__((ext_vector_type(4)));
typedef short s16x8 __attribute__((ext_vector_type(8)));
typedef float f32x16 __attribute__((ext_vector_type(16)));
typedef float f32x8 __attribute__((ext_vector_type(8)));
typedef float f32x4 __attribute__((ext_vector_type(4)));
typedef float f32x2 __attribute__((ext_vector_type(2)));
typedef unsigned int u32x16 __attribute__((ext_vector_type(16)));
typedef unsigned int u32x8 __attribute__((ext_vector_type(8)));
typedef unsigned int u32x4 __attribute__((ext_vector_type(4)));
typedef unsigned int u32x2 __attribute__((ext_vector_type(2)));
typedef __bf16 bf16x2 __attribute__((ext_vector_type(2)));
typedef __bf16 bf16x8 __attribute__((ext_vector_type(8)));
typedef __bf16 bf16x16 __attribute__((ext_vector_type(16)));

#define DI __device__ __forceinline__
#define LDS_PTR(T, p) ((__attribute__((address_space(3))) T*)(p))
#define BARRIER() asm volatile("s_barrier" ::: "memory")
#define BAR_LGKM() asm volatile("s_waitcnt lgkmcnt(0)\n\ts_barrier" ::: "memory")
#define BAR_ALL() asm volatile("s_waitcnt vmcnt(0) lgkmcnt(0)\n\ts_barrier" ::: "memory")
#define DRAIN() asm volatile("s_waitcnt vmcnt(0)" ::: "memory")
#define PACE() ({ asm volatile("s_sleep 1\n\ts_waitcnt vmcnt(3)" ::: "memory"); })
#define MFMA32(a, b, c) __builtin_amdgcn_mfma_f32_32x32x16_bf16(a, b, c, 0, 0, 0)
#define MFMA16(a, b, c) __builtin_amdgcn_mfma_f32_16x16x32_bf16(a, b, c, 0, 0, 0)
#define PIN(x) asm volatile("" : "+v"(x))
#define EX2(x) __builtin_amdgcn_exp2f(x)
#define RCP(x) __builtin_amdgcn_rcpf(x)
#define SBAR() __builtin_amdgcn_sched_barrier(0)

#define STAMP_INIT
#define STAMPV(i)
#define STAMP_END
#define STAMP_DECL
#define STAMP_OUTS
#define STAMP_PUT(i)
constexpr int D_ = 1024, F_ = 3584;

constexpr unsigned MBY = 1u << 20;
constexpr unsigned TS = 65536 + 4096, HS = 262144 + 4096;
constexpr unsigned OFF_HF = 0, OFF_X1 = 5 * MBY;
constexpr unsigned OFF_OF = 10 * MBY;
constexpr unsigned OFF_KV = 20 * MBY;
constexpr unsigned OFF_FL = 40 * MBY;
constexpr unsigned FL_EPOCH = OFF_FL;
constexpr unsigned FL_KV = OFF_FL + 4096;
constexpr unsigned FL_O = FL_KV + 65536;
constexpr unsigned FL_X = FL_O + 65536;
constexpr unsigned OFF_CS = 41 * MBY;
constexpr unsigned OFF_SX = 42 * MBY;
constexpr unsigned SX_ST = 0;
constexpr unsigned SX_ROW = 512 * 1024;
constexpr unsigned SX_CAND = SX_ROW + 256 * 1024;
constexpr unsigned SX_GP = SX_CAND + 32 * 1024;
constexpr unsigned SX_YP = SX_GP + 224 * 1024;
constexpr unsigned SX_LAYER = SX_YP + 224 * 1024;
constexpr unsigned SX_QP = 2 * SX_LAYER;
constexpr unsigned SX_BYTES = SX_QP + 512 * 1024;
static_assert(SX_BYTES <= 6 * 256 * 128 * 16, "sentinel fill");
constexpr unsigned WS_BYTES = 48 * MBY, OOB = 0x80000000u;
constexpr unsigned SENT = 0xFFFFFFFFu;

constexpr int NS = 9, DD = 3, SLOTB = 12288;
constexpr int L_XCH = 122880;
constexpr int L_SL = L_XCH + 8192;
constexpr int L_CAND = L_XCH + 32768;
constexpr int L_AB = L_XCH + 34816;
constexpr int L_FULL = 159744, L_FREE = L_FULL + 512;
constexpr int L_CS = L_FREE + 1024, L_PATCH = L_CS + 768;
constexpr int L_GU = L_PATCH + 768, L_TAG = L_GU + 256, L_WR = L_TAG + 256;
constexpr int L_X2F = L_WR + 128, L_X2P = L_X2F + 128;
constexpr int L_W = L_X2P + 128;
constexpr int LDS_BYTES = 163840;
static_assert(NS * SLOTB <= 54 * 2048 && L_W + 128 <= LDS_BYTES && (NS + 1) * SLOTB <= L_XCH && L_AB + 2048 <= L_FULL, "LDS map");

DI int fresh_lane() { int l; asm volatile("v_mbcnt_lo_u32_b32 %0, -1, 0\n\tv_mbcnt_hi_u32_b32 %0, -1, %0" : "=v"(l)); return l; }
#define LANE_VARS const int lane = fresh_lane(), r32 = lane & 31, hi = lane >> 5, tq = (lane & 15) >> 2, tp = lane & 3, tblk = (lane >> 4) & 1, \
  wrow = 4 * hi + tq, wsw = 4 * ((wrow >> 1) & 1), wch = 2 * tblk + (tp >> 1), c24 = r32 < 24 ? r32 : 23; \
  (void)r32; (void)hi; (void)wsw; (void)wch; (void)c24
DI int fresh_s(int v) { asm volatile("" : "+s"(v)); return v; }
#define PHASE_VARS const int tm = fresh_s(tm_), tn = fresh_s(tn_), wid = fresh_s(wid_), tile = tm * 4 + wid, hh = wid & 1, kh = wid >> 1; (void)tile; (void)hh; (void)kh; LANE_VARS
DI float bf2f(u16 v) { return __uint_as_float(((unsigned)v) << 16); }
DI unsigned pack2(float lo, float hi) { f32x2 v = {lo, hi}; return __builtin_bit_cast(unsigned, __builtin_convertvector(v, bf16x2)); }
DI float rbf(float f) { return __uint_as_float(pack2(f, 0.f) << 16); }
DI float lo16(unsigned u) { return __uint_as_float(u << 16); }
DI float hi16(unsigned u) { return __uint_as_float(u & 0xffff0000u); }
DI float opq(float r) { asm("" : "+v"(r)); return r; }
DI float fadd(float a, float b) { return opq(a + b); }
DI float fmul(float a, float b) { return opq(a * b); }
DI float ffma(float a, float b, float c) { return opq(__builtin_fmaf(a, b, c)); }
DI float U2F(unsigned u) { return __uint_as_float(u); }
DI float half_swap_sum(float v) {
  auto r = __builtin_amdgcn_permlane32_swap(__float_as_uint(v), __float_as_uint(v), false, false);
  return fadd(U2F(r[0]), U2F(r[1]));
}
DI float half_swap_max(float v) {
  auto r = __builtin_amdgcn_permlane32_swap(__float_as_uint(v), __float_as_uint(v), false, false);
  return fmaxf(__uint_as_float(r[0]), __uint_as_float(r[1]));
}
template <int CTRL> DI unsigned dpp(unsigned v) { return (unsigned)__builtin_amdgcn_update_dpp(0, (int)v, CTRL, 0xf, 0xf, false); }
DI float row_swap_sum(float v) {
  auto r = __builtin_amdgcn_permlane16_swap(__float_as_uint(v), __float_as_uint(v), false, false);
  return fadd(U2F(r[0]), U2F(r[1]));
}
DI float half_sum(float v) {
  v = fadd(v, U2F(dpp<0xB1>(__float_as_uint(v)))); v = fadd(v, U2F(dpp<0x4E>(__float_as_uint(v))));
  v = fadd(v, U2F(dpp<0x141>(__float_as_uint(v)))); v = fadd(v, U2F(dpp<0x140>(__float_as_uint(v))));
  return row_swap_sum(v);
}
DI float wave_sum(float v) { return half_swap_sum(half_sum(v)); }
DI unsigned half_max_u32(unsigned v) {
  v = max(v, dpp<0xB1>(v)); v = max(v, dpp<0x4E>(v)); v = max(v, dpp<0x141>(v)); v = max(v, dpp<0x140>(v));
  auto r = __builtin_amdgcn_permlane16_swap(v, v, false, false);
  return max(r[0], r[1]);
}
DI unsigned wave_max_u32(unsigned v) {
  v = half_max_u32(v);
  auto r = __builtin_amdgcn_permlane32_swap(v, v, false, false);
  return max(r[0], r[1]);
}
DI u32x4 fo_swap(u32x4 v) {
  auto a = __builtin_amdgcn_permlane32_swap(v[0], v[2], false, false);
  auto b = __builtin_amdgcn_permlane32_swap(v[1], v[3], false, false);
  u32x4 o; o[0] = a[0]; o[1] = b[0]; o[2] = a[1]; o[3] = b[1];
  return o;
}
DI void glds16(const void* gsrc, unsigned lds_dst) {
  unsigned keep;
  asm volatile("s_mov_b32 %0, m0\n\ts_mov_b32 m0, %2\n\ts_nop 0\n\tglobal_load_lds_dwordx4 %1, off\n\ts_mov_b32 m0, %0"
               : "=&s"(keep) : "v"(gsrc), "s"(lds_dst) : "memory");
}
#define GL_ "global_load_lds_dwordx4 "
DI void glds_v4(const void* sb, const unsigned (&v)[4], unsigned m0v) {
  unsigned keep;
  asm volatile("s_mov_b32 %0, m0\n\ts_mov_b32 m0, %6\n\ts_nop 0\n\t" GL_ "%1, %5\n\t" GL_ "%2, %5 offset:1024\n\t" GL_ "%3, %5 offset:2048\n\t"
               GL_ "%4, %5 offset:3072\n\ts_mov_b32 m0, %0" : "=&s"(keep) : "v"(v[0]), "v"(v[1]), "v"(v[2]), "v"(v[3]), "s"(sb), "s"(m0v) : "memory");
}
#define GP_ "\n\ts_sleep %8\n\ts_waitcnt vmcnt(%7)\n\t"
template <int TH, int SL> DI void glds_v4t(const void* sb, const unsigned (&v)[4], unsigned m0v) {
  unsigned keep;
  asm volatile("s_mov_b32 %0, m0\n\ts_mov_b32 m0, %6\n\ts_nop 0\n\t" GL_ "%1, %5" GP_ GL_ "%2, %5 offset:1024" GP_ GL_ "%3, %5 offset:2048" GP_ GL_ "%4, %5 offset:3072" GP_ "s_mov_b32 m0, %0"
               : "=&s"(keep) : "v"(v[0]), "v"(v[1]), "v"(v[2]), "v"(v[3]), "s"(sb), "s"(m0v), "n"(TH), "n"(SL) : "memory");
}
DI void glds_v3(const void* sb, const unsigned (&v)[3], unsigned m0v) {
  unsigned keep;
  asm volatile("s_mov_b32 %0, m0\n\ts_mov_b32 m0, %5\n\ts_nop 0\n\t" GL_ "%1, %4\n\t" GL_ "%2, %4 offset:1024\n\t" GL_ "%3, %4 offset:2048\n\t"
               "s_mov_b32 m0, %0" : "=&s"(keep) : "v"(v[0]), "v"(v[1]), "v"(v[2]), "s"(sb), "s"(m0v) : "memory");
}
DI float dot2(unsigned a, unsigned b, float c) {
  return __builtin_amdgcn_fdot2_f32_bf16(__builtin_bit_cast(bf16x2, a), __builtin_bit_cast(bf16x2, b), c, false);
}
#define CODE_ADDR(sym, out) do { unsigned long long pc_; unsigned off_; \
  asm volatile("s_getpc_b64 %0\n1:\n\ts_mov_b32 %1, " #sym "-1b" : "=s"(pc_), "=s"(off_)); out = (const char*)(pc_ + (long long)(int)off_); } while (0)
DI void st16(void* p, u32x4 v) { asm volatile("global_store_dwordx4 %0, %1, off sc0 sc1\n\ts_nop 1" :: "v"(p), "v"(v) : "memory"); }
DI void ln_scalars(float mean, float var, float& mb, float& den) { mb = rbf(mean); den = rbf(__builtin_amdgcn_sqrtf(rbf(rbf(var) + 1e-5f))); }
DI unsigned ln_pair(unsigned v, float mb, float rd) {
  const unsigned c = pack2(lo16(v) - mb, hi16(v) - mb);
  return pack2(lo16(c) * rd, hi16(c) * rd);
}
DI unsigned ln_pair_s(unsigned v, float nmb, float rd) {
  const unsigned c = pack2(fadd(lo16(v), nmb), fadd(hi16(v), nmb));
  return pack2(fmul(lo16(c), rd), fmul(hi16(c), rd));
}
template <int N, int M> DI f32x4 sum4(const u32x4 (&v)[M]) {
  f32x4 t;
#pragma unroll
  for (int e = 0; e < 4; ++e) {
    t[e] = U2F(v[0][e]);
#pragma unroll
    for (int i = 1; i < N; ++i) t[e] = fadd(t[e], U2F(v[i][e]));
  }
  return t;
}
#define PERM_LO(x, y) __builtin_amdgcn_perm(y, x, 0x05040100u)
#define PERM_HI(x, y) __builtin_amdgcn_perm(y, x, 0x07060302u)
constexpr unsigned SPIN_CAP = 1u << 15;
DI unsigned nz(unsigned v) { return min(v, SENT - 1u); }
DI unsigned nzf(float v) { return nz(__float_as_uint(v)); }
DI unsigned mx4(unsigned m, const u32x4& v) { return max(max(max(m, v[0]), v[1]), max(v[2], v[3])); }
template <int N> DI bool none_sent(const u32x4 (&v)[N]) {
  unsigned m = 0u;
#pragma unroll
  for (int i = 0; i < N; ++i) m = mx4(m, v[i]);
  return m != SENT;
}
typedef __amdgpu_buffer_rsrc_t rsrc_t;
DI u32x4 ldg(rsrc_t rs, unsigned off) { return __builtin_amdgcn_raw_buffer_load_b128(rs, (int)off, 0, 16); }
DI void stg(rsrc_t rs, unsigned off, u32x4 v) { __builtin_amdgcn_raw_buffer_store_b128(v, rs, (int)off, 0, 16); }
DI void poll_pause(bool slow) { if (slow) __builtin_amdgcn_s_sleep(4); asm volatile("s_sleep 1" ::: "memory"); }
template <int N, class LD, class OKF> DI void poll_all(u32x4 (&out)[N], bool slow, LD ld, OKF okf) {
  for (unsigned spins = 0;; ++spins) {
    ld(out);
    SBAR();
    if (__all(okf(out)) || spins > SPIN_CAP) break;
    poll_pause(slow);
  }
}
DI void lds_set(unsigned addr, unsigned v) { asm volatile("s_waitcnt lgkmcnt(0)\n\tds_write_b32 %0, %1" :: "v"(addr), "v"(v) : "memory"); }
template <int SLEEP = 1> DI void lds_wait_ge(unsigned addr, unsigned need) {
  unsigned spin = 0x100000, v;
  asm volatile("1:\n\tds_read_b32 %0, %2\n\ts_sub_u32 %1, %1, 1\n\ts_waitcnt lgkmcnt(0)\n\ts_cbranch_scc1 2f\n\tv_cmp_gt_u32 vcc, %3, %0\n\ts_cbranch_vccz 2f\n\ts_sleep %4\n\ts_branch 1b\n2:"
               : "=&v"(v), "+s"(spin) : "v"(addr), "s"(need), "n"(SLEEP) : "vcc", "scc", "memory");
}
DI s16x8 tr2(const char* p, int second) {
  s16x4 lo = __builtin_amdgcn_ds_read_tr16_b64_v4i16(LDS_PTR(s16x4, p)), hi = __builtin_amdgcn_ds_read_tr16_b64_v4i16(LDS_PTR(s16x4, p + second));
  return __builtin_shufflevector(lo, hi, 0, 1, 2, 3, 4, 5, 6, 7);
}
DI u32x16 cat4(const u32x4& a, const u32x4& b, const u32x4& c, const u32x4& d) {
  const u32x8 x = __builtin_shufflevector(a, b, 0, 1, 2, 3, 4, 5, 6, 7), y = __builtin_shufflevector(c, d, 0, 1, 2, 3, 4, 5, 6, 7);
  return __builtin_shufflevector(x, y, 0, 1, 2, 3, 4, 5, 6, 7, 8, 9, 10, 11, 12, 13, 14, 15);
}

DI unsigned stats_dst(unsigned sx, int tile, int tn, int lane) { return lane < 32 ? sx + SX_ST + tile * 8192 + tn * 512 + lane * 16 : OOB; }
DI void row_stats_sum(rsrc_t rs, unsigned sx, int tile, int lane, float& S, float& Q, float& Dd) {
  const unsigned exb = sx + SX_ST + tile * 8192 + (lane & 31) * 16;
  u32x4 pv[8];
  poll_all<8>(pv, false, [&](u32x4 (&v)[8]) {
#pragma unroll
    for (int i = 0; i < 8; ++i) v[i] = ldg(rs, exb + ((lane >> 5) * 8 + i) * 512);
  }, [&](const u32x4 (&v)[8]) { return none_sent(v); });
  S = 0.f; Q = 0.f; Dd = 0.f;
#pragma unroll
  for (int i = 0; i < 8; ++i) { S += __uint_as_float(pv[i][0]); Q += __uint_as_float(pv[i][1]); Dd += __uint_as_float(pv[i][2]); }
}
DI void row_stats_fin(rsrc_t rs, unsigned kdst, char* smem, int epoch, int tile, int lane, float S, float Q, float Dd, float wsum, const u32x4 (&x1p)[4],
                      float& mean, float& var, float& mb, float& rd) {
  S = half_swap_sum(S); Q = half_swap_sum(Q); Dd = half_swap_sum(Dd);
  mean = S * (1.0f / 1024.0f); var = fmaxf(Q * (1.0f / 1024.0f) - mean * mean, 0.f);
  float den;
  ln_scalars(mean, var, mb, den);
  rd = RCP(den);
  const unsigned rb = pack2((Dd - mb * wsum) * rd, 0.f) & 0xffffu;
  const unsigned mine = (((rb & 0x8000u) ? (~rb & 0xffffu) : (rb | 0x8000u)) << 16) | (unsigned)(2047 - (tile * 32 + (lane & 31)));
  const bool best = half_max_u32(mine) == mine;
  __builtin_amdgcn_raw_buffer_store_b32(mine, rs, (best && lane < 32) ? kdst : OOB, 0, 16);
  if (best) {
    char* c = smem + L_CAND + (tile & 3) * 144;
#pragma unroll
    for (int i = 0; i < 4; ++i) *reinterpret_cast<u32x4*>(c + (2 * i + (lane >> 5)) * 16) = x1p[i];
    *reinterpret_cast<u32x4*>(c + 128) = u32x4{mine, __float_as_uint(mb), __float_as_uint(rd), 0u};
  }
  lds_set((unsigned)(uintptr_t)smem + L_W + 32 + (tile & 3) * 4, epoch);
}

DI void attn_safe(rsrc_t rs, unsigned kvoff, const u32x8& q, const u32x4& ones, f32x16& o) {
  const s16x8 q0 = __builtin_bit_cast(s16x8, __builtin_shufflevector(q, q, 0, 1, 2, 3)), q1 = __builtin_bit_cast(s16x8, __builtin_shufflevector(q, q, 4, 5, 6, 7));
  f32x4 l = {0.f, 0.f, 0.f, 0.f};
#pragma unroll
  for (int e = 0; e < 16; ++e) o[e] = 0.f;
  float m = -INFINITY;
#pragma unroll 1
  for (int kt = 0; kt < 64; ++kt) {
    const unsigned p = kvoff + kt * 4096;
    const u32x4 k0 = ldg(rs, p), k1 = ldg(rs, p + 1024), v0 = ldg(rs, p + 2048), v1 = ldg(rs, p + 3072);
    f32x16 s = {};
    s = MFMA32(__builtin_bit_cast(s16x8, k0), q0, s);
    s = MFMA32(__builtin_bit_cast(s16x8, k1), q1, s);
    float mt = fmaxf(s[0], s[1]);
#pragma unroll
    for (int e = 2; e < 16; ++e) mt = fmaxf(mt, s[e]);
    const float mn = fmaxf(m, half_swap_max(mt));
    const float alpha = EX2(m - mn);
    m = mn;
#pragma unroll
    for (int e = 0; e < 16; ++e) { s[e] = EX2(s[e] - mn); o[e] *= alpha; }
    l[0] *= alpha;
    const bf16x16 pa = __builtin_bit_cast(bf16x16, __builtin_shufflevector(s, s, 0, 1, 2, 3, 4, 5, 6, 7));
    const bf16x16 pb = __builtin_bit_cast(bf16x16, __builtin_shufflevector(s, s, 8, 9, 10, 11, 12, 13, 14, 15));
    o = __builtin_amdgcn_smfmac_f32_32x32x32_bf16(__builtin_bit_cast(bf16x8, v0), pa, o, 0xdddd, 0, 0);
    l = __builtin_amdgcn_smfmac_f32_16x16x64_bf16(__builtin_bit_cast(bf16x8, ones), pa, l, 0xdddd, 0, 0);
    o = __builtin_amdgcn_smfmac_f32_32x32x32_bf16(__builtin_bit_cast(bf16x8, v1), pb, o, 0xdddd, 0, 0);
    l = __builtin_amdgcn_smfmac_f32_16x16x64_bf16(__builtin_bit_cast(bf16x8, ones), pb, l, 0xdddd, 0, 0);
  }
  o *= 1.0f / l[0];
}

extern "C" __global__ __launch_bounds__(512) void mega_kernel(const u16* __restrict__ x0, const u16* __restrict__ wqkv, const u16* __restrict__ wo,
                                                              const u16* __restrict__ w_r, const u16* __restrict__ wg, const u16* __restrict__ wu,
                                                              const u16* __restrict__ wd, char* ws, u16* __restrict__ out) {
  __shared__ __attribute__((aligned(16))) char smem[LDS_BYTES];
  const int wid = __builtin_amdgcn_readfirstlane(threadIdx.x >> 6);
  STAMP_INIT;
  STAMPV(0);
  const int bid = blockIdx.x, xcd = bid & 7, loc = bid >> 3;
  const int tm = 8 * (xcd >> 2) + (loc >> 2), tn = 4 * (xcd & 3) + (loc & 3);
  const unsigned lbase = (unsigned)(uintptr_t)smem;
  const rsrc_t rs = __builtin_amdgcn_make_buffer_rsrc(ws, 0, (int)WS_BYTES, 0x00020000);
  volatile __attribute__((address_space(3))) unsigned* words = LDS_PTR(volatile unsigned, smem + L_W);
  *reinterpret_cast<unsigned*>(smem + L_FULL + threadIdx.x * 4) = 0u;
  words[threadIdx.x & 31] = 0u;
  BAR_LGKM();
  const int lw = wid & 1;
  const char* wb = reinterpret_cast<const char*>(wqkv + (size_t)(16 * lw) * (3 * D_) + tn * 64);
  auto stage_lanes = [](unsigned (&wv)[3]) {
    const int lane = fresh_lane();
#pragma unroll
    for (int i = 0; i < 3; ++i) {
      const int li = lane + 64 * i, k = li / 24, cp = li % 24, c = cp ^ (4 * ((k >> 1) & 1));
      wv[i] = (unsigned)(k * (3 * D_ * 2) + (c >> 3) * (D_ * 2) + (c & 7) * 16 - i * 1024);
    }
  };
  auto stage = [&](const unsigned (&wv)[3], int s, int slot) {
    const char* p = wb + (size_t)((s + 2 * tn) & 31) * (32 * 3 * D_ * 2);
    glds_v3(p, wv, lbase + slot * SLOTB + lw * 6144);
    glds_v3(p + 8 * (3 * D_ * 2), wv, lbase + slot * SLOTB + lw * 6144 + 3072);
  };
  auto load_tag = [&]() {
    u32x2 t = __builtin_amdgcn_raw_buffer_load_b64(rs, FL_EPOCH, 0, 16);
    t[0] += 1u; t[1] += 0x9E3779B9u;
    PIN(t);
    return t;
  };
  auto exchange = [&](const u32x2& tag, unsigned line0, int me, bool bar = false) {
    const int lane = fresh_lane();
    __builtin_amdgcn_raw_buffer_store_b64(tag, rs, lane == 0 ? line0 + me * 128 : OOB, 0, 16);
    if (bar) BARRIER();
    if (1) {
      unsigned po = lane < 16 ? line0 + lane * 128 : OOB;
      const u32x4 srd = {(unsigned)(uintptr_t)ws, (unsigned)((uintptr_t)ws >> 32) & 0xffffu, WS_BYTES, 0x00020000u};
      for (unsigned spins = 0;; ++spins) {
        u32x2 v = {0u, 0u};
        asm volatile("buffer_atomic_or_x2 %0, %1, %2, 0 offen sc0 sc1\n\ts_waitcnt vmcnt(0)" : "+v"(v) : "v"(po), "s"(srd) : "memory");
        po = ((v[0] == tag[0]) & (v[1] == tag[1])) ? OOB : po;
        if (__all(po == OOB) || spins > SPIN_CAP) break;
      }
      return;
    }
    __builtin_amdgcn_s_sleep(8);
    for (unsigned spins = 0;; ++spins) {
      const u32x2 v = __builtin_amdgcn_raw_buffer_load_b64(rs, line0 + (lane & 15) * 128, 0, 16);
      if (__all((v[0] == tag[0]) & (v[1] == tag[1])) || spins > SPIN_CAP) break;
      asm volatile("s_sleep 1" ::: "memory");
    }
  };
  if (wid >= 4) {
    auto wo_rows = [&](int g0, int n, int gs) {
      const int lane = fresh_lane();
      unsigned wov[4];
#pragma unroll
      for (int i = 0; i < 4; ++i) wov[i] = (unsigned)((8 * i + (lane >> 3)) * (D_ * 2) + ((lane & 7) ^ (4 * ((lane >> 4) & 1))) * 16 - i * 1024);
#pragma unroll 1
      for (int i = 0, k = (2 * (tm & 7)) % n, g; i < n && !0; ++i, k = k + 1 == n ? 0 : k + 1) g = g0 + gs * k, glds_v4t<3, 1>(reinterpret_cast<const char*>(wo) + (size_t)((64 * tn + 32 * g) & 1023) * (D_ * 2) + tn * 128, wov, lbase + g * 4096);
    };
    if (wid < 6) {
      const int lane = fresh_lane();
      unsigned wv[3];
      stage_lanes(wv);
      __builtin_amdgcn_s_setprio(3);
      const unsigned fra = lbase + L_FREE + (lane & 3) * 256, fla = lbase + L_FULL + lw * 256 + lane * 4;
      const rsrc_t rw = __builtin_amdgcn_make_buffer_rsrc(const_cast<char*>(wb), 0, 0 ? 0 : 6 * MBY - (16 * lw * 3 * D_ + tn * 64) * 2, 0x00020000);
#pragma unroll 1
      for (int L = 0; L < 2; ++L) {
        const int s0 = NS, lead = L ? 8 : NS - 1;
        int slot = 0;
        if (L == 0) {
#pragma unroll 1
          for (int i = 0, s = tm & 7; i < 2 * NS; ++i) {
            glds_v3(wb + (size_t)((s + 2 * tn) & 31) * (32 * 3 * D_ * 2) + (i & 1) * (8 * 3 * D_ * 2), wv, lbase + s * SLOTB + lw * 6144 + (i & 1) * 3072);
            asm volatile("s_waitcnt vmcnt(6)" ::: "memory");
            if (i & 1) s = s + 1 == NS ? 0 : s + 1;
          }
          asm volatile("s_waitcnt vmcnt(0)\n\tds_write_b32 %0, %1" :: "v"(fla), "v"(NS) : "memory");
          STAMPV(1);
        }
#pragma unroll 1
        for (int s = NS; s < 32; ++s) {
          lds_wait_ge(fra, 32 * L + s - lead);
          stage(wv, s, slot);
          slot = (slot == NS - 1) ? 0 : slot + 1;
          if (s >= s0 + DD) asm volatile("s_waitcnt vmcnt(%0)\n\tds_write_b32 %1, %2" :: "n"(DD * 6), "v"(fla), "v"(32 * L + s - DD + 1) : "memory");
        }
#pragma unroll
        for (int d = DD - 1; d >= 0; --d) asm volatile("s_waitcnt vmcnt(%0)\n\tds_write_b32 %1, %2" :: "n"(d * 6), "v"(fla), "v"(32 * L + 32 - d) : "memory");
        u32x2 tag = *reinterpret_cast<const u32x2*>(smem + L_TAG);
        PIN(tag);
        if (lw == 0) {
          lds_wait_ge(lbase + L_W + 96 + 4 * (lane & 1), 1);
          lds_wait_ge<0>(lbase + L_W + 80 + 4 * (lane & 3), L + 1);
          exchange(tag, FL_KV + (L * 16 + tn) * 2048, tm, true);
          lds_wait_ge(fra, 32 * L + 32);
          if (0) { const unsigned long long t0 = __builtin_amdgcn_s_memrealtime(); while (__builtin_amdgcn_s_memrealtime() < t0 + 0) __builtin_amdgcn_s_sleep(2); }
          lds_set(lbase + L_W + 16, L + 1);
        } else { BAR_LGKM(); lds_wait_ge<8>(lbase + L_W + 16, L + 1); }
        __builtin_amdgcn_s_sleep(24);
        wo_rows(lw, 15, 2);
        u32x4 pre[NS * 6];
        if (L == 0) {
#pragma unroll
          for (int s = 0; s < NS; ++s) {
#pragma unroll
            for (int i = 0; i < 6; ++i)
              pre[s * 6 + i] = __builtin_amdgcn_raw_buffer_load_b128(rw, wv[i % 3], ((s + 2 * tn) & 31) * (32 * 3 * D_ * 2) + (i / 3) * (8 * 3 * D_ * 2) + (i % 3) * 1024, 0), PACE();
          }
        } else {
#pragma unroll
          for (int i = 0; i < NS * 6; ++i) asm volatile("" : "=v"(pre[i]));
        }
        BAR_ALL();
        if (lw == 0) {
          exchange(tag, FL_O + (L * 16 + tm) * 2048, tn); lds_set(lbase + L_W + 20, L + 1);
          __builtin_amdgcn_raw_buffer_store_b64(tag, rs, (bid == 0 && lane == 0) ? FL_EPOCH : OOB, 0, 16);
        }
        else wo_rows(30, 2, 1);
        BAR_ALL();
        if (L == 0) {
          char* d = smem + lw * 6144 + fresh_lane() * 16;
#pragma unroll
          for (int s = 0; s < NS; ++s) {
#pragma unroll
            for (int i = 0; i < 6; ++i) *reinterpret_cast<u32x4*>(d + s * SLOTB + i * 1024) = pre[s * 6 + i];
            lds_set(fla, 32 + s + 1);
          }
          STAMPV(2);
          lds_wait_ge<2>(lbase + L_W + 24, 2);
        }
      }
      STAMP_END;
      return;
    }
    const int a = wid - 6;
    const bool ffn = loc < 28;
    const int grp = xcd * 7 + (ffn ? loc >> 2 : 0), qa = (loc & 3) * 2 + a;
    const int kr = loc, cg = xcd;
    const unsigned wa = lbase + L_W;
    if (a) {
      const int lane = fresh_lane();
      unsigned t = __builtin_amdgcn_raw_buffer_load_b32(rs, 1 ? lane * MBY + MBY - 128 : OOB, 0, 16);
      if (1) {
        const char *c0, *c1;
        CODE_ADDR(mega_kernel, c0); CODE_ADDR(end_marker, c1);
        const long long o = (loc * 64 + lane) * 32;
        t |= *reinterpret_cast<const __attribute__((address_space(1))) unsigned*>((uintptr_t)(c0 + (o + 4 <= c1 - c0 ? o : 0)));
      }
      const u32x2 tag = load_tag();
      *reinterpret_cast<u32x2*>(smem + L_TAG) = tag;
      PIN(t);
      lds_wait_ge<0>(wa + 64 + 4 * (lane & 3), 2);
      exchange(tag, FL_X + tm * 2048, tn);
      lds_set(wa + 24, 1);
    } else {
      unsigned wv[3];
      stage_lanes(wv);
#pragma unroll 1
      for (int i = 0; i < 4 * 0; ++i) {
        glds_v3(reinterpret_cast<const char*>(wqkv + tn * 64) + (size_t)((4 * (NS + 2 * tn + 3 * (tm & 7)) + i) & 127) * (8 * 3 * D_ * 2), wv, lbase + NS * SLOTB);
        asm volatile("s_waitcnt vmcnt(6)" ::: "memory");
      }
      STAMPV(11);
      lds_wait_ge<2>(wa + 24, 1);
    }
    __builtin_amdgcn_s_sleep(16);
    auto transpose8 = [](u32x4* v) {
      u32x4 t[8];
#pragma unroll
      for (int j = 0; j < 8; ++j)
#pragma unroll
        for (int e = 0; e < 4; ++e) t[j][e] = (j & 1) ? PERM_HI(v[2 * e][j >> 1], v[2 * e + 1][j >> 1]) : PERM_LO(v[2 * e][j >> 1], v[2 * e + 1][j >> 1]);
#pragma unroll
      for (int j = 0; j < 8; ++j) { v[j] = t[j]; PIN(v[j]); }
    };
    u32x4 gf[32];
    u32x4 wdf[16];
    auto wait_go = [&](int L) {
      lds_wait_ge<8>(lbase + L_W + 16, L + 1);
      __builtin_amdgcn_s_sleep(24);
    };
    auto load_wd = [&](unsigned dv) {
      const rsrc_t rd = __builtin_amdgcn_make_buffer_rsrc(const_cast<u16*>(wd) + (size_t)grp * 64 * D_ + 128 * qa, 0, 0 ? 0 : 64 * D_ * 2 - 256 * qa, 0x00020000);
#pragma unroll
      for (int i = 0; i < 16; ++i) {
        wdf[i] = __builtin_amdgcn_raw_buffer_load_b128(rd, dv, (32 * (i >> 3) + (i & 7)) * (D_ * 2), 0);
        asm volatile("s_sleep 2\n\ts_waitcnt vmcnt(3)" ::: "memory");
      }
    };
    {
      LANE_VARS;
      const u32x4 sv = {SENT, SENT, SENT, SENT};
#pragma unroll
      for (int i = 0; i < 6; ++i) {
        const unsigned o = ((i * 256 + bid) * 128 + a * 64 + lane) * 16;
        stg(rs, o < SX_BYTES ? OFF_SX + o : OOB, sv);
      }
      if (a == 0) {
        const u32x4 w0 = *reinterpret_cast<const u32x4*>(w_r + lane * 8), w1 = *reinterpret_cast<const u32x4*>(w_r + 512 + lane * 8);
        float t = 0.f;
#pragma unroll
        for (int e = 0; e < 4; ++e) t = fadd(fadd(t, fadd(lo16(w0[e]), hi16(w0[e]))), fadd(lo16(w1[e]), hi16(w1[e])));
        words[0] = __float_as_uint(wave_sum(t));
        if ((lane >> 3) == (tn & 7)) *reinterpret_cast<u32x4*>(smem + L_WR + (lane & 7) * 16) = tn < 8 ? w0 : w1;
      }
      const u16* p = wqkv + (size_t)(64 * tm + 32 * a + hi) * (3 * D_) + (c24 >> 3) * D_ + tn * 64 + (c24 & 7) * 8;
      float cs[8] = {0.f, 0.f, 0.f, 0.f, 0.f, 0.f, 0.f, 0.f};
      u32x4 v[16];
#pragma unroll
      for (int i = 0; i < 16; ++i) v[i] = *reinterpret_cast<const u32x4*>(p + (size_t)i * (2 * 3 * D_));
      SBAR();
#pragma unroll
      for (int i = 0; i < 16; ++i)
#pragma unroll
        for (int e = 0; e < 4; ++e) { cs[2 * e] = fadd(cs[2 * e], lo16(v[i][e])); cs[2 * e + 1] = fadd(cs[2 * e + 1], hi16(v[i][e])); }
      u32x4 o;
#pragma unroll
      for (int e = 0; e < 4; ++e) { const float x = half_swap_sum(cs[e]), y = half_swap_sum(cs[4 + e]); o[e] = __float_as_uint(hi ? y : x); }
      stg(rs, r32 < 24 ? OFF_CS + (tn * 32 + tm * 2 + a) * 768 + r32 * 32 + hi * 16 : OOB, o);
    }
    {
      DRAIN();
      lds_set(wa + 96 + 4 * a, 1);
      BAR_LGKM();
      LANE_VARS;
      const char* gp = reinterpret_cast<const char*>(((lane & 8) ? wu : wg) + (size_t)(128 * qa + 16 * hi + 4 * tblk) * F_ + 64 * grp + (lane & 7) * 8);
      unsigned dv = (lane >> 4) * (8 * D_ * 2) + (lane & 15) * 16;
      PIN(gp); PIN(dv);
      unsigned sv[3];
#pragma unroll
      for (int i = 0; i < 3; ++i) { const int li = lane + 64 * i, k = li / 24, c = li % 24; sv[i] = (unsigned)(k * (3 * D_ * 2) + (c >> 3) * (D_ * 2) + (c & 7) * 16 - i * 1024); PIN(sv[i]); }
      wait_go(0);
#pragma unroll
      for (int i = 0; i < 32; ++i) {
        gf[i] = *reinterpret_cast<const u32x4*>(gp + (size_t)(0 ? 0 : 32 * (i >> 3) + 8 * ((i >> 2) & 1) + (i & 3)) * (F_ * 2));
        asm volatile("s_sleep 2\n\ts_waitcnt vmcnt(3)" ::: "memory");
      }
      load_wd(dv);
      STAMPV(7);
      BARRIER();
      __builtin_amdgcn_s_sleep(2);
#pragma unroll
      for (int g = 0; g < 4; ++g) glds_v3(wqkv + (size_t)(32 * kr + 8 * g) * (3 * D_) + (2 * cg + a) * 64, sv, lbase + L_SL + a * 12288 + g * 3072);
      transpose8(gf); transpose8(gf + 8); transpose8(gf + 16); transpose8(gf + 24);
      transpose8(wdf); transpose8(wdf + 8);
      {
        LANE_VARS;
        f32x4 t[2];
#pragma unroll
        for (int c = 0; c < 2; ++c) {
          u32x4 v[8];
#pragma unroll
          for (int i = 0; i < 8; ++i) v[i] = ldg(rs, OFF_CS + (tn * 32 + 16 * c + 2 * i + hi) * 768 + a * 384 + c24 * 16);
          SBAR();
          t[c] = sum4<8>(v);
          SBAR();
        }
#pragma unroll
        for (int e = 0; e < 4; ++e) t[0][e] = half_swap_sum(fadd(t[0][e], t[1][e]));
        if (lane < 24) *reinterpret_cast<f32x4*>(smem + L_CS + a * 384 + lane * 16) = t[0];
      }
      u32x2 tag = *reinterpret_cast<const u32x2*>(smem + L_TAG);
      PIN(tag);
      BAR_ALL();
      if (a) {
        lds_wait_ge<0>(wa + 64 + 4 * (lane & 3), 3);
        exchange(tag, FL_X + (16 + tm) * 2048, tn);
        lds_set(wa + 24, 2);
        STAMPV(12);
      }
    }
#pragma unroll 1
    for (int L = fresh_s(0);; ++L) {
      const unsigned sx = OFF_SX + L * SX_LAYER;
      STAMPV(1 + 16 * L);
      if (0 && L == 0) { words[3] = 5000u; lds_set(wa + 4 + 4 * a, 1); } else {
      u32x4 hf[4];
      unsigned key;
      { LANE_VARS;
      lds_wait_ge<0>(wa + 32 + 4 * tp, L + 1);
      if (a == 0) {
        const char* cp = smem + L_CAND + (r32 >> 3) * 144;
        const u32x4 c = *reinterpret_cast<const u32x4*>(cp + (lane & 7) * 16), rec = *reinterpret_cast<const u32x4*>(cp + 128);
        u32x4 o;
#pragma unroll
        for (int e = 0; e < 4; ++e) { unsigned h = ln_pair_s(c[e], -U2F(rec[1]), U2F(rec[2])); PIN(h); o[e] = nz(hi ? c[e] : h); }
        stg(rs, sx + SX_ROW + hi * 131072 + (tm * 4 + (r32 >> 3)) * 2048 + tn * 128 + (lane & 7) * 16, o);
        STAMPV(8 + 16 * L);
        lds_wait_ge<0>(wa + 104, L + 1);
        key = __builtin_amdgcn_readfirstlane(words[27]);
      } else {
        const u32x4 srd = {(unsigned)(uintptr_t)ws, (unsigned)((uintptr_t)ws >> 32) & 0xffffu, WS_BYTES, 0x00020000u};
        unsigned po = sx + SX_CAND + (tn * 16 + (lane >> 2)) * 128 + tp * 4;
        key = 0u;
        for (unsigned spins = 0;; ++spins) {
          unsigned v = 0u;
          asm volatile("buffer_atomic_or %0, %1, %2, 0 offen sc0 sc1\n\ts_waitcnt vmcnt(0)" : "+v"(v) : "v"(po), "s"(srd) : "memory");
          key = v == SENT ? key : max(key, v);
          po = v == SENT ? po : OOB;
          if (__all(po == OOB) || spins > SPIN_CAP) break;
        }
        key = __builtin_amdgcn_readfirstlane(wave_max_u32(key));
        words[27] = key;
        words[3] = 2047u - (key & 2047u);
        lds_set(wa + 104, L + 1);
        STAMPV(13 + 16 * L);
      }
      poll_all<4>(hf, false, [&](u32x4 (&t)[4]) {
#pragma unroll
        for (int i = 0; i < 4; ++i) t[i] = ldg(rs, sx + SX_ROW + ((2047 - (int)(key & 2047u)) >> 5) * 2048 + (16 * qa + 4 * i + (lane >> 4)) * 16);
      }, [&](const u32x4 (&t)[4]) { return none_sent(t); });
      if (L == 1) lds_set(wa + 4 + 4 * a, 2);
      }
      STAMPV(2 + 16 * L);
      f32x4 d[8];
      const f32x4 zero = {0.f, 0.f, 0.f, 0.f};
      SBAR();
      __builtin_amdgcn_s_setprio(3);
#pragma unroll
      for (int t = 0; t < 32; ++t) d[t & 7] = MFMA16(__builtin_bit_cast(s16x8, hf[t >> 3]), __builtin_bit_cast(s16x8, gf[t]), t < 8 ? zero : d[t & 7]);
      SBAR();
      __builtin_amdgcn_s_setprio(0);
      LANE_VARS;
      const int idx = 2047 - (int)(key & 2047u), win = idx >> 7;
      const bool owner = win == tm;
      auto pick2 = [&](const f32x4 (&d)[8]) {
        u32x2 o;
#pragma unroll
        for (int e = 0; e < 2; ++e) {
          const float x = tblk ? d[2 + e][0] : d[e][0], y = tblk ? d[6 + e][0] : d[4 + e][0];
          o[e] = nzf(hi ? y : x);
        }
        return o;
      };
      if (ffn) {
        const unsigned ppos = (lane & 15) * 32 + (lane >> 4) * 8;
        __builtin_amdgcn_raw_buffer_store_b64(pick2(d), rs, sx + SX_GP + (grp * 8 + qa) * 512 + ppos, 0, 16);
        STAMPV(9 + 16 * L);
        __builtin_amdgcn_s_sleep(13);
        if (L == 0) __builtin_amdgcn_s_sleep(0);
        u32x2 gu[8];
        for (unsigned spins = 0;; ++spins) {
#pragma unroll
          for (int i = 0; i < 8; ++i) gu[i] = __builtin_amdgcn_raw_buffer_load_b64(rs, sx + SX_GP + (grp * 8 + i) * 512 + lane * 8, 0, 16);
          SBAR();
          unsigned m = 0u;
#pragma unroll
          for (int i = 0; i < 8; ++i) m = max(max(m, gu[i][0]), gu[i][1]);
          if (__all(m != SENT) || spins > SPIN_CAP) break;
          poll_pause(L == 0);
        }
        STAMPV(10 + 16 * L);
        float t0 = opq(U2F(gu[0][0])), t1 = opq(U2F(gu[0][1]));
#pragma unroll
        for (int i = 1; i < 8; ++i) { t0 = fadd(t0, U2F(gu[i][0])); t1 = fadd(t1, U2F(gu[i][1])); }
        auto sw = __builtin_amdgcn_permlane32_swap(__float_as_uint(t0), __float_as_uint(t1), false, false);
        const float gv = rbf(U2F(sw[0])), uv = rbf(U2F(sw[1]));
        reinterpret_cast<u16*>(smem + L_GU + a * 128)[2 * r32 + hi] = (u16)pack2(fmul(rbf(fmul(gv, RCP(fadd(1.0f, __expf(-gv))))), uv), 0.f);
        asm volatile("s_waitcnt lgkmcnt(0)" ::: "memory");
        const s16x8 a0 = *reinterpret_cast<const s16x8*>(smem + L_GU + a * 128 + (lane >> 4) * 16), a1 = *reinterpret_cast<const s16x8*>(smem + L_GU + a * 128 + 64 + (lane >> 4) * 16);
        SBAR();
        __builtin_amdgcn_s_setprio(3);
#pragma unroll
        for (int t = 0; t < 16; ++t) d[t & 7] = MFMA16(t < 8 ? a0 : a1, __builtin_bit_cast(s16x8, wdf[t]), t < 8 ? zero : d[t & 7]);
        SBAR();
        __builtin_amdgcn_s_setprio(0);
        __builtin_amdgcn_raw_buffer_store_b64(pick2(d), rs, sx + SX_YP + grp * 4096 + qa * 512 + ppos, 0, 16);
      }
      STAMPV(3 + 16 * L);
      if (L == 1 && (cg != (kr & 7) || a)) { STAMP_END; __builtin_amdgcn_endpgm(); }
      s16x8 wpf[12];
#pragma unroll
      for (int t = 0; t < 12; ++t) wpf[t] = tr2(smem + L_SL + a * 12288 + (16 * hi + 4 * tblk + tq) * 384 + t * 32 + tp * 8, 8 * 384);
#pragma unroll
      for (int t = 0; t < 12; ++t) PIN(wpf[t]);
      if (L == 0 && !owner) lds_set(wa + 4 + 4 * a, 1);
      __builtin_amdgcn_s_sleep(13 - 4);
      if (L == 0) __builtin_amdgcn_s_sleep(0);
      if (L == 0 && !ffn) __builtin_amdgcn_s_sleep(64);
      const unsigned rk = key >> 16;
      float rsig = rbf(RCP(fadd(1.0f, __expf(-lo16((rk & 0x8000u) ? (rk & 0x7fffu) : (~rk & 0xffffu))))));
      PIN(rsig);
      const int n = 4 * (lane & 7) + ((lane >> 3) & 3);
      const int fo = ((n >> 4) * 2 + ((n >> 2) & 1)) * 8 + ((n >> 3) & 1) * 4 + (n & 3);
      u32x4 pv[8];
      poll_all<8>(pv, L == 0, [&](u32x4 (&t)[8]) {
#pragma unroll
        for (int i = 0; i < 7; ++i) t[i] = ldg(rs, sx + SX_YP + (8 * i + (lane >> 3)) * 4096 + kr * 128 + (lane & 7) * 16);
        t[7][0] = t[7][2] = __builtin_amdgcn_raw_buffer_load_b32(rs, sx + SX_ROW + (idx >> 5) * 2048 + kr * 64 + (fo >> 1) * 4, 0, 16);
        t[7][1] = t[7][3] = __builtin_amdgcn_raw_buffer_load_b32(rs, sx + SX_ROW + 131072 + (idx >> 5) * 2048 + kr * 64 + (fo >> 1) * 4, 0, 16);
      }, [&](const u32x4 (&t)[8]) { return none_sent(t); });
      const float hv = (fo & 1) ? hi16(pv[7][0]) : lo16(pv[7][0]), xv = (fo & 1) ? hi16(pv[7][1]) : lo16(pv[7][1]);
      STAMPV(4 + 16 * L);
      float ys;
      {
        f32x4 t = sum4<7>(pv);
#pragma unroll
        for (int e = 0; e < 4; ++e) {
          t[e] = fadd(t[e], U2F(dpp<0x128>(__float_as_uint(t[e]))));
          t[e] = half_swap_sum(row_swap_sum(t[e]));
        }
        const float x = (lane & 8) ? t[1] : t[0], y = (lane & 8) ? t[3] : t[2];
        ys = tblk ? y : x;
      }
      const unsigned ob = pack2(fadd(xv, rbf(fadd(hv, rbf(fmul(rbf(ys), rsig))))), 0.f) & 0xffffu;
      if (L == 1) {
        if (lane < 32) out[(size_t)idx * D_ + kr * 32 + n] = (u16)ob;
        STAMPV(5 + 16 * L);
        STAMP_END;
        __builtin_amdgcn_endpgm();
      }
      reinterpret_cast<u16*>(smem + L_X2F + a * 64)[fo] = (u16)ob;
      asm volatile("s_waitcnt lgkmcnt(0)" ::: "memory");
      {
        const s16x8 xa = *reinterpret_cast<const s16x8*>(smem + L_X2F + a * 64 + (lane >> 4) * 16);
        const u32x4 xf = *reinterpret_cast<const u32x4*>(smem + L_X2F + a * 64 + tp * 16);
        f32x4 z[12];
        SBAR();
        __builtin_amdgcn_s_setprio(3);
#pragma unroll
        for (int t = 0; t < 12; ++t) z[t] = MFMA16(xa, wpf[t], zero);
        SBAR();
        __builtin_amdgcn_s_setprio(0);
        const float x2 = lo16(ob), s1 = half_sum(x2), s2 = half_sum(fmul(x2, x2));
        u32x4 o;
#pragma unroll
        for (int e = 0; e < 4; ++e) {
          const unsigned sp = lane < 52 ? (e == 0 ? __float_as_uint(s1) : __float_as_uint(s2)) : xf[e];
          const unsigned x = __float_as_uint(tblk ? z[4 + e][0] : z[e][0]), y = tblk ? sp : __float_as_uint(z[8 + e][0]);
          o[e] = nz(hi ? y : x);
        }
        const unsigned pos = lane < 24 ? lane * 16 : lane < 48 ? lane * 16 + 128 : lane < 52 ? (lane & 1) * 512 + 384 : 400 + tp * 16;
        stg(rs, lane < 56 ? OFF_SX + SX_QP + ((2 * cg + a) * 32 + kr) * 1024 + pos : OOB, o);
      }
      STAMPV(5 + 16 * L);
      if (owner) {
        __builtin_amdgcn_s_sleep(13);
        const int c25 = r32 < 25 ? r32 : 24, pl = 24 * a + c24, col = 64 * (pl >> 4) + (pl & 15);
        float cs[4];
#pragma unroll
        for (int e = 0; e < 4; ++e) { cs[e] = reinterpret_cast<const float*>(smem + L_CS)[col + 16 * e]; PIN(cs[e]); }
        u32x4 qv[17];
        poll_all<17>(qv, true, [&](u32x4 (&t)[17]) {
#pragma unroll
          for (int i = 0; i < 16; ++i) t[i] = ldg(rs, OFF_SX + SX_QP + (tn * 32 + 2 * i + hi) * 1024 + a * 512 + c25 * 16);
          t[16] = ldg(rs, OFF_SX + SX_QP + (tn * 34 + hi) * 1024 + 400 + tp * 16);
        }, [&](const u32x4 (&t)[17]) { return none_sent(t); });
        *reinterpret_cast<u32x4*>(smem + L_X2P + hi * 64 + tp * 16) = qv[16];
        f32x4 t = sum4<16>(qv);
#pragma unroll
        for (int e = 0; e < 4; ++e) t[e] = half_swap_sum(t[e]);
        const float mean = fmul(U2F(__builtin_amdgcn_readlane(__float_as_uint(t[0]), 24)), 1.0f / 1024.0f);
        const float var = fmaxf(ffma(-mean, mean, fmul(U2F(__builtin_amdgcn_readlane(__float_as_uint(t[1]), 24)), 1.0f / 1024.0f)), 0.f);
        float mb, den;
        ln_scalars(mean, var, mb, den);
        const float rd = RCP(den);
        if (lane < 24) {
#pragma unroll
          for (int e = 0; e < 4; ++e) reinterpret_cast<float*>(smem + L_PATCH)[col + 16 * e] = fmul(ffma(-mb, cs[e], t[e]), rd);
        }
        lds_set(wa + 4 + 4 * a, L + 1);
      }
      STAMPV(6 + 16 * L);
      }
      BAR_ALL();
      unsigned dv = fresh_lane();
      dv = (dv >> 4) * (8 * D_ * 2) + (dv & 15) * 16;
      PIN(dv);
      wait_go(1);
      load_wd(dv);
      STAMPV(23);
      BARRIER();
      transpose8(wdf); transpose8(wdf + 8);
      BARRIER();
    }
  }
  const int tm_ = tm, tn_ = tn, wid_ = wid;
  {
    const int tid = threadIdx.x, lane = tid & 63, r32 = lane & 31, row = 2 * wid + (lane >> 5);
    char* sm = smem + L_XCH;
    u32x4 v[4];
    asm volatile("global_load_dwordx4 %0, %4, off\n\tglobal_load_dwordx4 %1, %4, off offset:512\n\tglobal_load_dwordx4 %2, %4, off offset:1024\n\tglobal_load_dwordx4 %3, %4, off offset:1536"
                 : "=&v"(v[0]), "=&v"(v[1]), "=&v"(v[2]), "=&v"(v[3]) : "v"(x0 + (size_t)(tm * 128 + tn * 8 + row) * D_ + r32 * 8) : "memory");
    int pass = fresh_s(0);
#pragma unroll 1
    for (;;) {
      asm volatile("s_cmp_eq_u32 %4, 0\n\ts_cbranch_scc1 1f\n\ts_waitcnt vmcnt(0)\n1:" : "+v"(v[0]), "+v"(v[1]), "+v"(v[2]), "+v"(v[3]) : "s"(pass) : "memory", "scc");
      float s = 0.f, s2 = 0.f;
#pragma unroll
      for (int i = 0; i < 4; ++i)
#pragma unroll
        for (int e = 0; e < 4; e += 2) { s = dot2(v[i][e], 0x3f803f80u, s); s2 = dot2(v[i][e + 1], 0x3f803f80u, s2); }
      const float mean = half_sum(s + s2) * (1.0f / 1024.0f);
      f32x2 m2 = {0.f, 0.f};
#pragma unroll
      for (int i = 0; i < 4; ++i)
#pragma unroll
        for (int e = 0; e < 4; ++e) { const f32x2 d = f32x2{lo16(v[i][e]), hi16(v[i][e])} - mean; m2 += d * d; }
      float mb, den;
      ln_scalars(mean, half_sum(m2[0] + m2[1]) * (1.0f / 1024.0f), mb, den);
      const float rd = 1.0f / den;
#pragma unroll
      for (int i = 0; i < 4; ++i) {
        char* p = sm + ((16 * i + (r32 >> 1)) * 2) * 144 + row * 16 + (r32 & 1) * 8;
        u32x2 lo = {ln_pair(v[i][0], mb, rd), ln_pair(v[i][1], mb, rd)}, hi = {ln_pair(v[i][2], mb, rd), ln_pair(v[i][3], mb, rd)};
        *reinterpret_cast<u32x2*>(p) = lo;
        *reinterpret_cast<u32x2*>(p + 144) = hi;
      }
      STAMPV(37);
      lds_set(lbase + L_W + 64 + 4 * wid, pass);
      lds_wait_ge<0>(lbase + L_W + 64 + 4 * (lane & 3), pass);
      u32x4 t[4];
#pragma unroll
      for (int i = 0; i < 4; ++i) t[i] = *reinterpret_cast<const u32x4*>(sm + (i * 32 + (tid >> 3)) * 144 + (tid & 7) * 16);
      SBAR();
      unsigned dst = OFF_HF + (tm * 4 + (tn >> 2)) * TS + (tn & 3) * 128 + (tid >> 3) * 512 + (tid & 7) * 16;
      PIN(dst);
      if (pass) {
#pragma unroll
        for (int i = 0; i < 4; ++i) stg(rs, dst + i * 16384, t[i]);
        break;
      }
#pragma unroll
      for (int i = 0; i < 4; ++i) PIN(t[i]);
      pass = fresh_s(1);
    }
    DRAIN();
    lds_set(lbase + L_W + 64 + 4 * wid, 2);
    STAMPV(38);
  }
  const u32x4 srd = {(unsigned)(uintptr_t)ws, (unsigned)((uintptr_t)ws >> 32) & 0xffffu, WS_BYTES, 0x00020000u};
  u32x4 x1p[4];
  asm volatile("" : "=v"(x1p[0]), "=v"(x1p[1]), "=v"(x1p[2]), "=v"(x1p[3]));
#pragma unroll 1
  for (int L = 0; L < 2; ++L) {
    const unsigned sx = OFF_SX + L * SX_LAYER;
    u32x8 q[4];
    u32x16 kva, kvb;
    f32x16 oz[5];
    {
      f32x16 acc[6];
      u32x16 stA, stB, xr0, xr1, xr2, xr3;
      u32x4 stC;
      asm volatile("" : "={v[112:127]}"(stB), "={v[128:131]}"(stC));
      { PHASE_VARS;
      stB[8] = lane * 16 + (L ? OFF_X1 : OFF_HF) + tile * TS;
      stB[9] = lbase + wrow * 384 + (((4 * hh + wch) ^ wsw) << 4) + 8 * (tp & 1);
      stB[10] = lbase + wrow * 384 + (((4 * (hh ^ 1) + wch) ^ wsw) << 4) + 8 * (tp & 1);
      stB[13] = lbase + L_FULL + (lane & 1) * 256;
      stB[15] = lbase + L_FREE + wid * 256 + lane * 4;
      stC[0] = 32 * L; }
      unsigned soff, slot, need = 32 * L + 1;
      if (L == 0) {
        lds_wait_ge<0>(lbase + L_W + 24, 1);
        STAMPV(39);
        soff = tn * 4096;
        asm volatile(
          "buffer_load_dwordx4 v[132:135], v120, s[40:43], s44 offen offset:0 sc1\n\t"
          "buffer_load_dwordx4 v[136:139], v120, s[40:43], s44 offen offset:1024 sc1\n\t"
          "buffer_load_dwordx4 v[140:143], v120, s[40:43], s44 offen offset:2048 sc1\n\t"
          "buffer_load_dwordx4 v[144:147], v120, s[40:43], s44 offen offset:3072 sc1\n\t"
          "s_add_u32 s44, s44, 0x1000\n\t"
          "s_and_b32 s44, s44, 0xffff\n\t"
          "buffer_load_dwordx4 v[148:151], v120, s[40:43], s44 offen offset:0 sc1\n\t"
          "buffer_load_dwordx4 v[152:155], v120, s[40:43], s44 offen offset:1024 sc1\n\t"
          "buffer_load_dwordx4 v[156:159], v120, s[40:43], s44 offen offset:2048 sc1\n\t"
          "buffer_load_dwordx4 v[160:163], v120, s[40:43], s44 offen offset:3072 sc1\n\t"
          "s_add_u32 s44, s44, 0x1000\n\t"
          "s_and_b32 s44, s44, 0xffff\n\t"
          "buffer_load_dwordx4 v[164:167], v120, s[40:43], s44 offen offset:0 sc1\n\t"
          "buffer_load_dwordx4 v[168:171], v120, s[40:43], s44 offen offset:1024 sc1\n\t"
          "buffer_load_dwordx4 v[172:175], v120, s[40:43], s44 offen offset:2048 sc1\n\t"
          "buffer_load_dwordx4 v[176:179], v120, s[40:43], s44 offen offset:3072 sc1\n\t"
          "s_add_u32 s44, s44, 0x1000\n\t"
          "s_and_b32 s44, s44, 0xffff\n\t"
          "buffer_load_dwordx4 v[180:183], v120, s[40:43], s44 offen offset:0 sc1\n\t"
          "buffer_load_dwordx4 v[184:187], v120, s[40:43], s44 offen offset:1024 sc1\n\t"
          "buffer_load_dwordx4 v[188:191], v120, s[40:43], s44 offen offset:2048 sc1\n\t"
          "buffer_load_dwordx4 v[192:195], v120, s[40:43], s44 offen offset:3072 sc1\n\t"
          : "={v[132:147]}"(xr0), "={v[148:163]}"(xr1), "={v[164:179]}"(xr2), "={v[180:195]}"(xr3), "+{s44}"(soff) : "{v[112:127]}"(stB), "{s[40:43]}"(srd) : "scc", "memory");
      } else {
        xr0 = cat4(x1p[0], x1p[1], x1p[2], x1p[3]);
        asm volatile("" : "={v[148:163]}"(xr1), "={v[164:179]}"(xr2), "={v[180:195]}"(xr3));
      }
      STAMPV(1 + 16 * L);
      asm volatile(
        "v_mov_b32 v123, v121\n\t"
        "v_mov_b32 v124, v122\n\t"
        "s_mov_b64 vcc, -1\n\t"
        "s_cbranch_vccnz 10f\n\t"
        "11:\n\t"
        "s_mov_b32 s46, 12288\n\t"
        "s_add_u32 s47, s47, 1\n\t"
        "ds_read_b64_tr_b16 v[96:97], v123 offset:0\n\t"
        "ds_read_b64_tr_b16 v[98:99], v123 offset:3072\n\t"
        "ds_read_b64_tr_b16 v[100:101], v124 offset:0\n\t"
        "ds_read_b64_tr_b16 v[102:103], v124 offset:3072\n\t"
        "ds_read_b64_tr_b16 v[104:105], v123 offset:128\n\t"
        "ds_read_b64_tr_b16 v[106:107], v123 offset:3200\n\t"
        "ds_read_b64_tr_b16 v[108:109], v124 offset:128\n\t"
        "ds_read_b64_tr_b16 v[110:111], v124 offset:3200\n\t"
        "ds_read_b64_tr_b16 v[112:113], v123 offset:256\n\t"
        "ds_read_b64_tr_b16 v[114:115], v123 offset:3328\n\t"
        "ds_write_b32 v127, v128\n\t"
        "ds_read_b64_tr_b16 v[116:117], v124 offset:256\n\t"
        "ds_read_b64_tr_b16 v[118:119], v124 offset:3328\n\t"
        "s_waitcnt vmcnt(15) lgkmcnt(11)\n\t"
        "v_mfma_f32_32x32x16_bf16 v[0:15], v[96:99], v[132:135], 0\n\t"
        "ds_read_b32 v126, v125\n\t"
        "ds_read_b64_tr_b16 v[96:97], v123 offset:6144\n\t"
        "ds_read_b64_tr_b16 v[98:99], v123 offset:9216\n\t"
        "s_waitcnt lgkmcnt(12)\n\t"
        "v_mfma_f32_32x32x16_bf16 v[16:31], v[100:103], v[132:135], 0\n\t"
        "v_add_u32 v129, s46, v121\n\t"
        "ds_read_b64_tr_b16 v[100:101], v124 offset:6144\n\t"
        "ds_read_b64_tr_b16 v[102:103], v124 offset:9216\n\t"
        "s_waitcnt lgkmcnt(12)\n\t"
        "v_mfma_f32_32x32x16_bf16 v[32:47], v[104:107], v[132:135], 0\n\t"
        "ds_read_b64_tr_b16 v[104:105], v123 offset:6272\n\t"
        "ds_read_b64_tr_b16 v[106:107], v123 offset:9344\n\t"
        "s_waitcnt lgkmcnt(12)\n\t"
        "v_mfma_f32_32x32x16_bf16 v[48:63], v[108:111], v[132:135], 0\n\t"
        "v_add_u32 v130, s46, v122\n\t"
        "ds_read_b64_tr_b16 v[108:109], v124 offset:6272\n\t"
        "ds_read_b64_tr_b16 v[110:111], v124 offset:9344\n\t"
        "s_waitcnt lgkmcnt(12)\n\t"
        "v_mfma_f32_32x32x16_bf16 v[64:79], v[132:135], v[112:115], 0\n\t"
        "ds_read_b64_tr_b16 v[112:113], v123 offset:6400\n\t"
        "ds_read_b64_tr_b16 v[114:115], v123 offset:9472\n\t"
        "s_waitcnt lgkmcnt(10)\n\t"
        "v_mfma_f32_32x32x16_bf16 v[80:95], v[132:135], v[116:119], 0\n\t"
        "v_cmp_gt_u32 vcc, s47, v126\n\t"
        "ds_read_b64_tr_b16 v[116:117], v124 offset:6400\n\t"
        "ds_read_b64_tr_b16 v[118:119], v124 offset:9472\n\t"
        "s_waitcnt vmcnt(14) lgkmcnt(10)\n\t"
        "v_mfma_f32_32x32x16_bf16 v[0:15], v[96:99], v[136:139], v[0:15]\n\t"
        "s_cbranch_vccnz 12f\n\t"
        "13:\n\t"
        "ds_read_b64_tr_b16 v[96:97], v129 offset:0\n\t"
        "ds_read_b64_tr_b16 v[98:99], v129 offset:3072\n\t"
        "s_waitcnt lgkmcnt(10)\n\t"
        "v_mfma_f32_32x32x16_bf16 v[16:31], v[100:103], v[136:139], v[16:31]\n\t"
        "v_add_u32 v128, 1, v128\n\t"
        "ds_read_b64_tr_b16 v[100:101], v130 offset:0\n\t"
        "ds_read_b64_tr_b16 v[102:103], v130 offset:3072\n\t"
        "s_waitcnt lgkmcnt(10)\n\t"
        "v_mfma_f32_32x32x16_bf16 v[32:47], v[104:107], v[136:139], v[32:47]\n\t"
        "ds_read_b64_tr_b16 v[104:105], v129 offset:128\n\t"
        "ds_read_b64_tr_b16 v[106:107], v129 offset:3200\n\t"
        "s_waitcnt lgkmcnt(10)\n\t"
        "v_mfma_f32_32x32x16_bf16 v[48:63], v[108:111], v[136:139], v[48:63]\n\t"
        "s_add_u32 s46, s46, 12288\n\t"
        "s_add_u32 s47, s47, 1\n\t"
        "ds_read_b64_tr_b16 v[108:109], v130 offset:128\n\t"
        "ds_read_b64_tr_b16 v[110:111], v130 offset:3200\n\t"
        "s_waitcnt lgkmcnt(10)\n\t"
        "v_mfma_f32_32x32x16_bf16 v[64:79], v[136:139], v[112:115], v[64:79]\n\t"
        "s_cmp_eq_u32 s46, 110592\n\t"
        "s_cselect_b32 s46, 0, s46\n\t"
        "ds_read_b64_tr_b16 v[112:113], v129 offset:256\n\t"
        "ds_read_b64_tr_b16 v[114:115], v129 offset:3328\n\t"
        "s_waitcnt lgkmcnt(10)\n\t"
        "v_mfma_f32_32x32x16_bf16 v[80:95], v[136:139], v[116:119], v[80:95]\n\t"
        "ds_write_b32 v127, v128\n\t"
        "ds_read_b64_tr_b16 v[116:117], v130 offset:256\n\t"
        "ds_read_b64_tr_b16 v[118:119], v130 offset:3328\n\t"
        "s_waitcnt vmcnt(13) lgkmcnt(11)\n\t"
        "v_mfma_f32_32x32x16_bf16 v[0:15], v[96:99], v[140:143], v[0:15]\n\t"
        "ds_read_b32 v126, v125\n\t"
        "ds_read_b64_tr_b16 v[96:97], v129 offset:6144\n\t"
        "ds_read_b64_tr_b16 v[98:99], v129 offset:9216\n\t"
        "s_waitcnt lgkmcnt(12)\n\t"
        "v_mfma_f32_32x32x16_bf16 v[16:31], v[100:103], v[140:143], v[16:31]\n\t"
        "v_add_u32 v123, s46, v121\n\t"
        "ds_read_b64_tr_b16 v[100:101], v130 offset:6144\n\t"
        "ds_read_b64_tr_b16 v[102:103], v130 offset:9216\n\t"
        "s_waitcnt lgkmcnt(12)\n\t"
        "v_mfma_f32_32x32x16_bf16 v[32:47], v[104:107], v[140:143], v[32:47]\n\t"
        "ds_read_b64_tr_b16 v[104:105], v129 offset:6272\n\t"
        "ds_read_b64_tr_b16 v[106:107], v129 offset:9344\n\t"
        "s_waitcnt lgkmcnt(12)\n\t"
        "v_mfma_f32_32x32x16_bf16 v[48:63], v[108:111], v[140:143], v[48:63]\n\t"
        "v_add_u32 v124, s46, v122\n\t"
        "ds_read_b64_tr_b16 v[108:109], v130 offset:6272\n\t"
        "ds_read_b64_tr_b16 v[110:111], v130 offset:9344\n\t"
        "s_waitcnt lgkmcnt(12)\n\t"
        "v_mfma_f32_32x32x16_bf16 v[64:79], v[140:143], v[112:115], v[64:79]\n\t"
        "ds_read_b64_tr_b16 v[112:113], v129 offset:6400\n\t"
        "ds_read_b64_tr_b16 v[114:115], v129 offset:9472\n\t"
        "s_waitcnt lgkmcnt(10)\n\t"
        "v_mfma_f32_32x32x16_bf16 v[80:95], v[140:143], v[116:119], v[80:95]\n\t"
        "v_cmp_gt_u32 vcc, s47, v126\n\t"
        "ds_read_b64_tr_b16 v[116:117], v130 offset:6400\n\t"
        "ds_read_b64_tr_b16 v[118:119], v130 offset:9472\n\t"
        "s_waitcnt vmcnt(12) lgkmcnt(10)\n\t"
        "v_mfma_f32_32x32x16_bf16 v[0:15], v[96:99], v[144:147], v[0:15]\n\t"
        "s_cbranch_vccnz 14f\n\t"
        "15:\n\t"
        "ds_read_b64_tr_b16 v[96:97], v123 offset:0\n\t"
        "ds_read_b64_tr_b16 v[98:99], v123 offset:3072\n\t"
        "s_waitcnt lgkmcnt(10)\n\t"
        "v_mfma_f32_32x32x16_bf16 v[16:31], v[100:103], v[144:147], v[16:31]\n\t"
        "v_add_u32 v128, 1, v128\n\t"
        "ds_read_b64_tr_b16 v[100:101], v124 offset:0\n\t"
        "ds_read_b64_tr_b16 v[102:103], v124 offset:3072\n\t"
        "s_waitcnt lgkmcnt(10)\n\t"
        "v_mfma_f32_32x32x16_bf16 v[32:47], v[104:107], v[144:147], v[32:47]\n\t"
        "ds_read_b64_tr_b16 v[104:105], v123 offset:128\n\t"
        "ds_read_b64_tr_b16 v[106:107], v123 offset:3200\n\t"
        "s_waitcnt lgkmcnt(10)\n\t"
        "v_mfma_f32_32x32x16_bf16 v[48:63], v[108:111], v[144:147], v[48:63]\n\t"
        "s_add_u32 s46, s46, 12288\n\t"
        "s_add_u32 s47, s47, 1\n\t"
        "ds_read_b64_tr_b16 v[108:109], v124 offset:128\n\t"
        "ds_read_b64_tr_b16 v[110:111], v124 offset:3200\n\t"
        "s_waitcnt lgkmcnt(10)\n\t"
        "v_mfma_f32_32x32x16_bf16 v[64:79], v[144:147], v[112:115], v[64:79]\n\t"
        "s_cmp_eq_u32 s46, 110592\n\t"
        "s_cselect_b32 s46, 0, s46\n\t"
        "ds_read_b64_tr_b16 v[112:113], v123 offset:256\n\t"
        "ds_read_b64_tr_b16 v[114:115], v123 offset:3328\n\t"
        "s_waitcnt lgkmcnt(10)\n\t"
        "v_mfma_f32_32x32x16_bf16 v[80:95], v[144:147], v[116:119], v[80:95]\n\t"
        "ds_write_b32 v127, v128\n\t"
        "ds_read_b64_tr_b16 v[116:117], v124 offset:256\n\t"
        "ds_read_b64_tr_b16 v[118:119], v124 offset:3328\n\t"
        "s_waitcnt lgkmcnt(0)\n\t"
        "s_nop 7\n\t"
        "s_branch 9f\n\t"
        "10:\n\t"
        "s_mov_b32 s48, 0x10000\n\t"
        "8:\n\t"
        "ds_read_b32 v126, v125\n\t"
        "s_sub_u32 s48, s48, 1\n\t"
        "s_waitcnt lgkmcnt(0)\n\t"
        "s_cbranch_scc1 11b\n\t"
        "v_cmp_gt_u32 vcc, s47, v126\n\t"
        "s_cbranch_vccnz 8b\n\t"
        "s_branch 11b\n\t"
        "12:\n\t"
        "s_mov_b32 s48, 0x10000\n\t"
        "8:\n\t"
        "ds_read_b32 v126, v125\n\t"
        "s_sub_u32 s48, s48, 1\n\t"
        "s_waitcnt lgkmcnt(0)\n\t"
        "s_cbranch_scc1 13b\n\t"
        "v_cmp_gt_u32 vcc, s47, v126\n\t"
        "s_cbranch_vccnz 8b\n\t"
        "s_branch 13b\n\t"
        "14:\n\t"
        "s_mov_b32 s48, 0x10000\n\t"
        "8:\n\t"
        "ds_read_b32 v126, v125\n\t"
        "s_sub_u32 s48, s48, 1\n\t"
        "s_waitcnt lgkmcnt(0)\n\t"
        "s_cbranch_scc1 15b\n\t"
        "v_cmp_gt_u32 vcc, s47, v126\n\t"
        "s_cbranch_vccnz 8b\n\t"
        "s_branch 15b\n\t"
        "9:\n\t"
        : "={v[0:15]}"(acc[0]), "={v[16:31]}"(acc[1]), "={v[32:47]}"(acc[2]), "={v[48:63]}"(acc[3]), "={v[64:79]}"(acc[4]), "={v[80:95]}"(acc[5]),
          "={v[96:111]}"(stA), "+{v[112:127]}"(stB), "+{v[128:131]}"(stC), "={s46}"(slot), "+{s47}"(need)
        : "{v[132:147]}"(xr0) : "s48", "vcc", "scc", "memory");
      STAMPV(2 + 16 * L);
      float al = 1.f;
      if (L == 1) {
        PHASE_VARS;
        float S, Q, Dd;
        row_stats_sum(rs, OFF_SX, tile, lane, S, Q, Dd);
        STAMPV(36);
        lds_wait_ge<0>(lbase + L_W + 24, 2);
        soff = ((tn + 1) & 15) * 4096;
        asm volatile(
          "buffer_load_dwordx4 v[148:151], v120, s[40:43], s44 offen offset:0 sc1\n\t"
          "buffer_load_dwordx4 v[152:155], v120, s[40:43], s44 offen offset:1024 sc1\n\t"
          "buffer_load_dwordx4 v[156:159], v120, s[40:43], s44 offen offset:2048 sc1\n\t"
          "buffer_load_dwordx4 v[160:163], v120, s[40:43], s44 offen offset:3072 sc1\n\t"
          : "={v[148:163]}"(xr1), "={v[164:179]}"(xr2), "={v[180:195]}"(xr3), "+{s44}"(soff), "+v"(S), "+v"(Q), "+v"(Dd) : "{v[112:127]}"(stB), "{s[40:43]}"(srd) : "scc", "memory");
        float mean, var, mb1, rd1, mb2, den2;
        row_stats_fin(rs, OFF_SX + SX_CAND + (tn * 16 + tm) * 128 + wid * 4, smem, 1, tile, lane, S, Q, Dd, U2F(words[0]), x1p, mean, var, mb1, rd1);
        ln_scalars(mean + (mean - mb1) * rd1, var * (1.0f + rd1) * (1.0f + rd1), mb2, den2);
        const float rd2 = RCP(den2);
        al = (1.0f + rd1) * rd2;
        const float be = -(mb1 * rd1 + mb2) * RCP(1.0f + rd1);
        reinterpret_cast<float*>(smem + L_AB)[wid * 64 + lane] = hi ? be : al;
        reinterpret_cast<float*>(smem + L_AB)[256 + wid * 64 + lane] = hi ? rd1 : mb1;
        soff = ((tn + 1 + 0) & 15) * 4096;
        asm volatile(
          "s_nop 0\n\t"
          "s_add_u32 s44, s44, 0x1000\n\t"
          "s_and_b32 s44, s44, 0xffff\n\t"
          "buffer_load_dwordx4 v[164:167], v120, s[40:43], s44 offen offset:0 sc1\n\t"
          "buffer_load_dwordx4 v[168:171], v120, s[40:43], s44 offen offset:1024 sc1\n\t"
          "buffer_load_dwordx4 v[172:175], v120, s[40:43], s44 offen offset:2048 sc1\n\t"
          "buffer_load_dwordx4 v[176:179], v120, s[40:43], s44 offen offset:3072 sc1\n\t"
          "s_add_u32 s44, s44, 0x1000\n\t"
          "s_and_b32 s44, s44, 0xffff\n\t"
          "buffer_load_dwordx4 v[180:183], v120, s[40:43], s44 offen offset:0 sc1\n\t"
          "buffer_load_dwordx4 v[184:187], v120, s[40:43], s44 offen offset:1024 sc1\n\t"
          "buffer_load_dwordx4 v[188:191], v120, s[40:43], s44 offen offset:2048 sc1\n\t"
          "buffer_load_dwordx4 v[192:195], v120, s[40:43], s44 offen offset:3072 sc1\n\t"
          : "+{v[148:163]}"(xr1), "+{v[164:179]}"(xr2), "+{v[180:195]}"(xr3), "+{s44}"(soff) : "{v[112:127]}"(stB), "{s[40:43]}"(srd) : "scc", "memory");
#pragma unroll
        for (int jj = 0; jj < 3; ++jj) {
          f32x4 cs[2][4];
          float cv[2];
#pragma unroll
          for (int j = 0; j < 2; ++j) {
            const int cb = jj * 64 + (j ^ hh) * 32;
            if (jj == 2) cv[j] = *reinterpret_cast<const float*>(smem + L_CS + (cb + r32) * 4);
#pragma unroll
            for (int g = 0; g < 4; ++g)
              if (jj < 2 || j == 0) cs[j][g] = *reinterpret_cast<const f32x4*>(smem + (jj < 2 ? L_CS + cb * 4 : L_AB + wid * 256 + 128) + (8 * g + 4 * hi) * 4);
          }
          SBAR();
#pragma unroll
          for (int j = 0; j < 2; ++j)
#pragma unroll
            for (int e = 0; e < 16; ++e) acc[2 * jj + j][e] += jj < 2 ? be * cs[j][e >> 2][e & 3] : cs[0][e >> 2][e & 3] * cv[j];
          SBAR();
        }
      }
      u32x4 ep;
      { PHASE_VARS;
        if (L == 0) reinterpret_cast<float*>(smem + L_AB)[wid * 64 + lane] = 1.0f;
        const unsigned dst = OFF_KV + L * (10 * MBY) + tn * 2 * HS + tile * 4096 + lane * 16;
        ep = u32x4{__float_as_uint(al), dst + hh * HS, dst + (hh ^ 1) * HS, lbase + L_AB + wid * 256 + hi * 16}; }
      STAMPV(3 + 16 * L);
      soff = ((tn + 1 + 2) & 15) * 4096;
      asm volatile(
        "s_nop 0\n\t"
        "s_add_u32 s44, s44, 0x1000\n\t"
        "s_and_b32 s44, s44, 0xffff\n\t"
        "buffer_load_dwordx4 v[132:135], v120, s[40:43], s44 offen offset:0 sc1\n\t"
        "buffer_load_dwordx4 v[136:139], v120, s[40:43], s44 offen offset:1024 sc1\n\t"
        "buffer_load_dwordx4 v[140:143], v120, s[40:43], s44 offen offset:2048 sc1\n\t"
        "s_mov_b32 s45, 3\n\t"
        "1:\n\t"
        "s_waitcnt vmcnt(14) lgkmcnt(11)\n\t"
        "v_mfma_f32_32x32x16_bf16 v[0:15], v[96:99], v[148:151], v[0:15]\n\t"
        "ds_read_b32 v126, v125\n\t"
        "ds_read_b64_tr_b16 v[96:97], v123 offset:6144\n\t"
        "ds_read_b64_tr_b16 v[98:99], v123 offset:9216\n\t"
        "s_waitcnt lgkmcnt(12)\n\t"
        "v_mfma_f32_32x32x16_bf16 v[16:31], v[100:103], v[148:151], v[16:31]\n\t"
        "v_add_u32 v129, s46, v121\n\t"
        "ds_read_b64_tr_b16 v[100:101], v124 offset:6144\n\t"
        "ds_read_b64_tr_b16 v[102:103], v124 offset:9216\n\t"
        "s_waitcnt lgkmcnt(12)\n\t"
        "v_mfma_f32_32x32x16_bf16 v[32:47], v[104:107], v[148:151], v[32:47]\n\t"
        "ds_read_b64_tr_b16 v[104:105], v123 offset:6272\n\t"
        "ds_read_b64_tr_b16 v[106:107], v123 offset:9344\n\t"
        "buffer_load_dwordx4 v[144:147], v120, s[40:43], s44 offen offset:3072 sc1\n\t"
        "s_waitcnt lgkmcnt(12)\n\t"
        "v_mfma_f32_32x32x16_bf16 v[48:63], v[108:111], v[148:151], v[48:63]\n\t"
        "v_add_u32 v130, s46, v122\n\t"
        "ds_read_b64_tr_b16 v[108:109], v124 offset:6272\n\t"
        "ds_read_b64_tr_b16 v[110:111], v124 offset:9344\n\t"
        "s_waitcnt lgkmcnt(12)\n\t"
        "v_mfma_f32_32x32x16_bf16 v[64:79], v[148:151], v[112:115], v[64:79]\n\t"
        "ds_read_b64_tr_b16 v[112:113], v123 offset:6400\n\t"
        "ds_read_b64_tr_b16 v[114:115], v123 offset:9472\n\t"
        "s_waitcnt lgkmcnt(10)\n\t"
        "v_mfma_f32_32x32x16_bf16 v[80:95], v[148:151], v[116:119], v[80:95]\n\t"
        "v_cmp_gt_u32 vcc, s47, v126\n\t"
        "ds_read_b64_tr_b16 v[116:117], v124 offset:6400\n\t"
        "ds_read_b64_tr_b16 v[118:119], v124 offset:9472\n\t"
        "s_waitcnt vmcnt(14) lgkmcnt(10)\n\t"
        "v_mfma_f32_32x32x16_bf16 v[0:15], v[96:99], v[152:155], v[0:15]\n\t"
        "s_cbranch_vccnz 16f\n\t"
        "17:\n\t"
        "ds_read_b64_tr_b16 v[96:97], v129 offset:0\n\t"
        "ds_read_b64_tr_b16 v[98:99], v129 offset:3072\n\t"
        "s_waitcnt lgkmcnt(10)\n\t"
        "v_mfma_f32_32x32x16_bf16 v[16:31], v[100:103], v[152:155], v[16:31]\n\t"
        "v_add_u32 v128, 1, v128\n\t"
        "ds_read_b64_tr_b16 v[100:101], v130 offset:0\n\t"
        "ds_read_b64_tr_b16 v[102:103], v130 offset:3072\n\t"
        "s_waitcnt lgkmcnt(10)\n\t"
        "v_mfma_f32_32x32x16_bf16 v[32:47], v[104:107], v[152:155], v[32:47]\n\t"
        "ds_read_b64_tr_b16 v[104:105], v129 offset:128\n\t"
        "ds_read_b64_tr_b16 v[106:107], v129 offset:3200\n\t"
        "s_add_u32 s44, s44, 0x1000\n\t"
        "s_and_b32 s44, s44, 0xffff\n\t"
        "buffer_load_dwordx4 v[148:151], v120, s[40:43], s44 offen offset:0 sc1\n\t"
        "s_waitcnt lgkmcnt(10)\n\t"
        "v_mfma_f32_32x32x16_bf16 v[48:63], v[108:111], v[152:155], v[48:63]\n\t"
        "s_add_u32 s46, s46, 12288\n\t"
        "s_add_u32 s47, s47, 1\n\t"
        "ds_read_b64_tr_b16 v[108:109], v130 offset:128\n\t"
        "ds_read_b64_tr_b16 v[110:111], v130 offset:3200\n\t"
        "s_waitcnt lgkmcnt(10)\n\t"
        "v_mfma_f32_32x32x16_bf16 v[64:79], v[152:155], v[112:115], v[64:79]\n\t"
        "s_cmp_eq_u32 s46, 110592\n\t"
        "s_cselect_b32 s46, 0, s46\n\t"
        "ds_read_b64_tr_b16 v[112:113], v129 offset:256\n\t"
        "ds_read_b64_tr_b16 v[114:115], v129 offset:3328\n\t"
        "s_waitcnt lgkmcnt(10)\n\t"
        "v_mfma_f32_32x32x16_bf16 v[80:95], v[152:155], v[116:119], v[80:95]\n\t"
        "ds_write_b32 v127, v128\n\t"
        "ds_read_b64_tr_b16 v[116:117], v130 offset:256\n\t"
        "ds_read_b64_tr_b16 v[118:119], v130 offset:3328\n\t"
        "s_waitcnt vmcnt(14) lgkmcnt(11)\n\t"
        "v_mfma_f32_32x32x16_bf16 v[0:15], v[96:99], v[156:159], v[0:15]\n\t"
        "ds_read_b32 v126, v125\n\t"
        "ds_read_b64_tr_b16 v[96:97], v129 offset:6144\n\t"
        "ds_read_b64_tr_b16 v[98:99], v129 offset:9216\n\t"
        "s_waitcnt lgkmcnt(12)\n\t"
        "v_mfma_f32_32x32x16_bf16 v[16:31], v[100:103], v[156:159], v[16:31]\n\t"
        "v_add_u32 v123, s46, v121\n\t"
        "ds_read_b64_tr_b16 v[100:101], v130 offset:6144\n\t"
        "ds_read_b64_tr_b16 v[102:103], v130 offset:9216\n\t"
        "s_waitcnt lgkmcnt(12)\n\t"
        "v_mfma_f32_32x32x16_bf16 v[32:47], v[104:107], v[156:159], v[32:47]\n\t"
        "ds_read_b64_tr_b16 v[104:105], v129 offset:6272\n\t"
        "ds_read_b64_tr_b16 v[106:107], v129 offset:9344\n\t"
        "buffer_load_dwordx4 v[152:155], v120, s[40:43], s44 offen offset:1024 sc1\n\t"
        "s_waitcnt lgkmcnt(12)\n\t"
        "v_mfma_f32_32x32x16_bf16 v[48:63], v[108:111], v[156:159], v[48:63]\n\t"
        "v_add_u32 v124, s46, v122\n\t"
        "ds_read_b64_tr_b16 v[108:109], v130 offset:6272\n\t"
        "ds_read_b64_tr_b16 v[110:111], v130 offset:9344\n\t"
        "s_waitcnt lgkmcnt(12)\n\t"
        "v_mfma_f32_32x32x16_bf16 v[64:79], v[156:159], v[112:115], v[64:79]\n\t"
        "ds_read_b64_tr_b16 v[112:113], v129 offset:6400\n\t"
        "ds_read_b64_tr_b16 v[114:115], v129 offset:9472\n\t"
        "s_waitcnt lgkmcnt(10)\n\t"
        "v_mfma_f32_32x32x16_bf16 v[80:95], v[156:159], v[116:119], v[80:95]\n\t"
        "v_cmp_gt_u32 vcc, s47, v126\n\t"
        "ds_read_b64_tr_b16 v[116:117], v130 offset:6400\n\t"
        "ds_read_b64_tr_b16 v[118:119], v130 offset:9472\n\t"
        "s_waitcnt vmcnt(14) lgkmcnt(10)\n\t"
        "v_mfma_f32_32x32x16_bf16 v[0:15], v[96:99], v[160:163], v[0:15]\n\t"
        "s_cbranch_vccnz 18f\n\t"
        "19:\n\t"
        "ds_read_b64_tr_b16 v[96:97], v123 offset:0\n\t"
        "ds_read_b64_tr_b16 v[98:99], v123 offset:3072\n\t"
        "s_waitcnt lgkmcnt(10)\n\t"
        "v_mfma_f32_32x32x16_bf16 v[16:31], v[100:103], v[160:163], v[16:31]\n\t"
        "v_add_u32 v128, 1, v128\n\t"
        "ds_read_b64_tr_b16 v[100:101], v124 offset:0\n\t"
        "ds_read_b64_tr_b16 v[102:103], v124 offset:3072\n\t"
        "s_waitcnt lgkmcnt(10)\n\t"
        "v_mfma_f32_32x32x16_bf16 v[32:47], v[104:107], v[160:163], v[32:47]\n\t"
        "ds_read_b64_tr_b16 v[104:105], v123 offset:128\n\t"
        "ds_read_b64_tr_b16 v[106:107], v123 offset:3200\n\t"
        "buffer_load_dwordx4 v[156:159], v120, s[40:43], s44 offen offset:2048 sc1\n\t"
        "s_waitcnt lgkmcnt(10)\n\t"
        "v_mfma_f32_32x32x16_bf16 v[48:63], v[108:111], v[160:163], v[48:63]\n\t"
        "s_add_u32 s46, s46, 12288\n\t"
        "s_add_u32 s47, s47, 1\n\t"
        "ds_read_b64_tr_b16 v[108:109], v124 offset:128\n\t"
        "ds_read_b64_tr_b16 v[110:111], v124 offset:3200\n\t"
        "s_waitcnt lgkmcnt(10)\n\t"
        "v_mfma_f32_32x32x16_bf16 v[64:79], v[160:163], v[112:115], v[64:79]\n\t"
        "s_cmp_eq_u32 s46, 110592\n\t"
        "s_cselect_b32 s46, 0, s46\n\t"
        "ds_read_b64_tr_b16 v[112:113], v123 offset:256\n\t"
        "ds_read_b64_tr_b16 v[114:115], v123 offset:3328\n\t"
        "s_waitcnt lgkmcnt(10)\n\t"
        "v_mfma_f32_32x32x16_bf16 v[80:95], v[160:163], v[116:119], v[80:95]\n\t"
        "ds_write_b32 v127, v128\n\t"
        "ds_read_b64_tr_b16 v[116:117], v124 offset:256\n\t"
        "ds_read_b64_tr_b16 v[118:119], v124 offset:3328\n\t"
        "s_waitcnt vmcnt(14) lgkmcnt(11)\n\t"
        "v_mfma_f32_32x32x16_bf16 v[0:15], v[96:99], v[164:167], v[0:15]\n\t"
        "ds_read_b32 v126, v125\n\t"
        "ds_read_b64_tr_b16 v[96:97], v123 offset:6144\n\t"
        "ds_read_b64_tr_b16 v[98:99], v123 offset:9216\n\t"
        "s_waitcnt lgkmcnt(12)\n\t"
        "v_mfma_f32_32x32x16_bf16 v[16:31], v[100:103], v[164:167], v[16:31]\n\t"
        "v_add_u32 v129, s46, v121\n\t"
        "ds_read_b64_tr_b16 v[100:101], v124 offset:6144\n\t"
        "ds_read_b64_tr_b16 v[102:103], v124 offset:9216\n\t"
        "s_waitcnt lgkmcnt(12)\n\t"
        "v_mfma_f32_32x32x16_bf16 v[32:47], v[104:107], v[164:167], v[32:47]\n\t"
        "ds_read_b64_tr_b16 v[104:105], v123 offset:6272\n\t"
        "ds_read_b64_tr_b16 v[106:107], v123 offset:9344\n\t"
        "buffer_load_dwordx4 v[160:163], v120, s[40:43], s44 offen offset:3072 sc1\n\t"
        "s_waitcnt lgkmcnt(12)\n\t"
        "v_mfma_f32_32x32x16_bf16 v[48:63], v[108:111], v[164:167], v[48:63]\n\t"
        "v_add_u32 v130, s46, v122\n\t"
        "ds_read_b64_tr_b16 v[108:109], v124 offset:6272\n\t"
        "ds_read_b64_tr_b16 v[110:111], v124 offset:9344\n\t"
        "s_waitcnt lgkmcnt(12)\n\t"
        "v_mfma_f32_32x32x16_bf16 v[64:79], v[164:167], v[112:115], v[64:79]\n\t"
        "ds_read_b64_tr_b16 v[112:113], v123 offset:6400\n\t"
        "ds_read_b64_tr_b16 v[114:115], v123 offset:9472\n\t"
        "s_waitcnt lgkmcnt(10)\n\t"
        "v_mfma_f32_32x32x16_bf16 v[80:95], v[164:167], v[116:119], v[80:95]\n\t"
        "v_cmp_gt_u32 vcc, s47, v126\n\t"
        "ds_read_b64_tr_b16 v[116:117], v124 offset:6400\n\t"
        "ds_read_b64_tr_b16 v[118:119], v124 offset:9472\n\t"
        "s_waitcnt vmcnt(14) lgkmcnt(10)\n\t"
        "v_mfma_f32_32x32x16_bf16 v[0:15], v[96:99], v[168:171], v[0:15]\n\t"
        "s_cbranch_vccnz 20f\n\t"
        "21:\n\t"
        "ds_read_b64_tr_b16 v[96:97], v129 offset:0\n\t"
        "ds_read_b64_tr_b16 v[98:99], v129 offset:3072\n\t"
        "s_waitcnt lgkmcnt(10)\n\t"
        "v_mfma_f32_32x32x16_bf16 v[16:31], v[100:103], v[168:171], v[16:31]\n\t"
        "v_add_u32 v128, 1, v128\n\t"
        "ds_read_b64_tr_b16 v[100:101], v130 offset:0\n\t"
        "ds_read_b64_tr_b16 v[102:103], v130 offset:3072\n\t"
        "s_waitcnt lgkmcnt(10)\n\t"
        "v_mfma_f32_32x32x16_bf16 v[32:47], v[104:107], v[168:171], v[32:47]\n\t"
        "ds_read_b64_tr_b16 v[104:105], v129 offset:128\n\t"
        "ds_read_b64_tr_b16 v[106:107], v129 offset:3200\n\t"
        "s_add_u32 s44, s44, 0x1000\n\t"
        "s_and_b32 s44, s44, 0xffff\n\t"
        "buffer_load_dwordx4 v[164:167], v120, s[40:43], s44 offen offset:0 sc1\n\t"
        "s_waitcnt lgkmcnt(10)\n\t"
        "v_mfma_f32_32x32x16_bf16 v[48:63], v[108:111], v[168:171], v[48:63]\n\t"
        "s_add_u32 s46, s46, 12288\n\t"
        "s_add_u32 s47, s47, 1\n\t"
        "ds_read_b64_tr_b16 v[108:109], v130 offset:128\n\t"
        "ds_read_b64_tr_b16 v[110:111], v130 offset:3200\n\t"
        "s_waitcnt lgkmcnt(10)\n\t"
        "v_mfma_f32_32x32x16_bf16 v[64:79], v[168:171], v[112:115], v[64:79]\n\t"
        "s_cmp_eq_u32 s46, 110592\n\t"
        "s_cselect_b32 s46, 0, s46\n\t"
        "ds_read_b64_tr_b16 v[112:113], v129 offset:256\n\t"
        "ds_read_b64_tr_b16 v[114:115], v129 offset:3328\n\t"
        "s_waitcnt lgkmcnt(10)\n\t"
        "v_mfma_f32_32x32x16_bf16 v[80:95], v[168:171], v[116:119], v[80:95]\n\t"
        "ds_write_b32 v127, v128\n\t"
        "ds_read_b64_tr_b16 v[116:117], v130 offset:256\n\t"
        "ds_read_b64_tr_b16 v[118:119], v130 offset:3328\n\t"
        "s_waitcnt vmcnt(14) lgkmcnt(11)\n\t"
        "v_mfma_f32_32x32x16_bf16 v[0:15], v[96:99], v[172:175], v[0:15]\n\t"
        "ds_read_b32 v126, v125\n\t"
        "ds_read_b64_tr_b16 v[96:97], v129 offset:6144\n\t"
        "ds_read_b64_tr_b16 v[98:99], v129 offset:9216\n\t"
        "s_waitcnt lgkmcnt(12)\n\t"
        "v_mfma_f32_32x32x16_bf16 v[16:31], v[100:103], v[172:175], v[16:31]\n\t"
        "v_add_u32 v123, s46, v121\n\t"
        "ds_read_b64_tr_b16 v[100:101], v130 offset:6144\n\t"
        "ds_read_b64_tr_b16 v[102:103], v130 offset:9216\n\t"
        "s_waitcnt lgkmcnt(12)\n\t"
        "v_mfma_f32_32x32x16_bf16 v[32:47], v[104:107], v[172:175], v[32:47]\n\t"
        "ds_read_b64_tr_b16 v[104:105], v129 offset:6272\n\t"
        "ds_read_b64_tr_b16 v[106:107], v129 offset:9344\n\t"
        "buffer_load_dwordx4 v[168:171], v120, s[40:43], s44 offen offset:1024 sc1\n\t"
        "s_waitcnt lgkmcnt(12)\n\t"
        "v_mfma_f32_32x32x16_bf16 v[48:63], v[108:111], v[172:175], v[48:63]\n\t"
        "v_add_u32 v124, s46, v122\n\t"
        "ds_read_b64_tr_b16 v[108:109], v130 offset:6272\n\t"
        "ds_read_b64_tr_b16 v[110:111], v130 offset:9344\n\t"
        "s_waitcnt lgkmcnt(12)\n\t"
        "v_mfma_f32_32x32x16_bf16 v[64:79], v[172:175], v[112:115], v[64:79]\n\t"
        "ds_read_b64_tr_b16 v[112:113], v129 offset:6400\n\t"
        "ds_read_b64_tr_b16 v[114:115], v129 offset:9472\n\t"
        "s_waitcnt lgkmcnt(10)\n\t"
        "v_mfma_f32_32x32x16_bf16 v[80:95], v[172:175], v[116:119], v[80:95]\n\t"
        "v_cmp_gt_u32 vcc, s47, v126\n\t"
        "ds_read_b64_tr_b16 v[116:117], v130 offset:6400\n\t"
        "ds_read_b64_tr_b16 v[118:119], v130 offset:9472\n\t"
        "s_waitcnt vmcnt(14) lgkmcnt(10)\n\t"
        "v_mfma_f32_32x32x16_bf16 v[0:15], v[96:99], v[176:179], v[0:15]\n\t"
        "s_cbranch_vccnz 22f\n\t"
        "23:\n\t"
        "ds_read_b64_tr_b16 v[96:97], v123 offset:0\n\t"
        "ds_read_b64_tr_b16 v[98:99], v123 offset:3072\n\t"
        "s_waitcnt lgkmcnt(10)\n\t"
        "v_mfma_f32_32x32x16_bf16 v[16:31], v[100:103], v[176:179], v[16:31]\n\t"
        "v_add_u32 v128, 1, v128\n\t"
        "ds_read_b64_tr_b16 v[100:101], v124 offset:0\n\t"
        "ds_read_b64_tr_b16 v[102:103], v124 offset:3072\n\t"
        "s_waitcnt lgkmcnt(10)\n\t"
        "v_mfma_f32_32x32x16_bf16 v[32:47], v[104:107], v[176:179], v[32:47]\n\t"
        "ds_read_b64_tr_b16 v[104:105], v123 offset:128\n\t"
        "ds_read_b64_tr_b16 v[106:107], v123 offset:3200\n\t"
        "buffer_load_dwordx4 v[172:175], v120, s[40:43], s44 offen offset:2048 sc1\n\t"
        "s_waitcnt lgkmcnt(10)\n\t"
        "v_mfma_f32_32x32x16_bf16 v[48:63], v[108:111], v[176:179], v[48:63]\n\t"
        "s_add_u32 s46, s46, 12288\n\t"
        "s_add_u32 s47, s47, 1\n\t"
        "ds_read_b64_tr_b16 v[108:109], v124 offset:128\n\t"
        "ds_read_b64_tr_b16 v[110:111], v124 offset:3200\n\t"
        "s_waitcnt lgkmcnt(10)\n\t"
        "v_mfma_f32_32x32x16_bf16 v[64:79], v[176:179], v[112:115], v[64:79]\n\t"
        "s_cmp_eq_u32 s46, 110592\n\t"
        "s_cselect_b32 s46, 0, s46\n\t"
        "ds_read_b64_tr_b16 v[112:113], v123 offset:256\n\t"
        "ds_read_b64_tr_b16 v[114:115], v123 offset:3328\n\t"
        "s_waitcnt lgkmcnt(10)\n\t"
        "v_mfma_f32_32x32x16_bf16 v[80:95], v[176:179], v[116:119], v[80:95]\n\t"
        "ds_write_b32 v127, v128\n\t"
        "ds_read_b64_tr_b16 v[116:117], v124 offset:256\n\t"
        "ds_read_b64_tr_b16 v[118:119], v124 offset:3328\n\t"
        "s_waitcnt vmcnt(14) lgkmcnt(11)\n\t"
        "v_mfma_f32_32x32x16_bf16 v[0:15], v[96:99], v[180:183], v[0:15]\n\t"
        "ds_read_b32 v126, v125\n\t"
        "ds_read_b64_tr_b16 v[96:97], v123 offset:6144\n\t"
        "ds_read_b64_tr_b16 v[98:99], v123 offset:9216\n\t"
        "s_waitcnt lgkmcnt(12)\n\t"
        "v_mfma_f32_32x32x16_bf16 v[16:31], v[100:103], v[180:183], v[16:31]\n\t"
        "v_add_u32 v129, s46, v121\n\t"
        "ds_read_b64_tr_b16 v[100:101], v124 offset:6144\n\t"
        "ds_read_b64_tr_b16 v[102:103], v124 offset:9216\n\t"
        "s_waitcnt lgkmcnt(12)\n\t"
        "v_mfma_f32_32x32x16_bf16 v[32:47], v[104:107], v[180:183], v[32:47]\n\t"
        "ds_read_b64_tr_b16 v[104:105], v123 offset:6272\n\t"
        "ds_read_b64_tr_b16 v[106:107], v123 offset:9344\n\t"
        "buffer_load_dwordx4 v[176:179], v120, s[40:43], s44 offen offset:3072 sc1\n\t"
        "s_waitcnt lgkmcnt(12)\n\t"
        "v_mfma_f32_32x32x16_bf16 v[48:63], v[108:111], v[180:183], v[48:63]\n\t"
        "v_add_u32 v130, s46, v122\n\t"
        "ds_read_b64_tr_b16 v[108:109], v124 offset:6272\n\t"
        "ds_read_b64_tr_b16 v[110:111], v124 offset:9344\n\t"
        "s_waitcnt lgkmcnt(12)\n\t"
        "v_mfma_f32_32x32x16_bf16 v[64:79], v[180:183], v[112:115], v[64:79]\n\t"
        "ds_read_b64_tr_b16 v[112:113], v123 offset:6400\n\t"
        "ds_read_b64_tr_b16 v[114:115], v123 offset:9472\n\t"
        "s_waitcnt lgkmcnt(10)\n\t"
        "v_mfma_f32_32x32x16_bf16 v[80:95], v[180:183], v[116:119], v[80:95]\n\t"
        "v_cmp_gt_u32 vcc, s47, v126\n\t"
        "ds_read_b64_tr_b16 v[116:117], v124 offset:6400\n\t"
        "ds_read_b64_tr_b16 v[118:119], v124 offset:9472\n\t"
        "s_waitcnt vmcnt(14) lgkmcnt(10)\n\t"
        "v_mfma_f32_32x32x16_bf16 v[0:15], v[96:99], v[184:187], v[0:15]\n\t"
        "s_cbranch_vccnz 24f\n\t"
        "25:\n\t"
        "ds_read_b64_tr_b16 v[96:97], v129 offset:0\n\t"
        "ds_read_b64_tr_b16 v[98:99], v129 offset:3072\n\t"
        "s_waitcnt lgkmcnt(10)\n\t"
        "v_mfma_f32_32x32x16_bf16 v[16:31], v[100:103], v[184:187], v[16:31]\n\t"
        "v_add_u32 v128, 1, v128\n\t"
        "ds_read_b64_tr_b16 v[100:101], v130 offset:0\n\t"
        "ds_read_b64_tr_b16 v[102:103], v130 offset:3072\n\t"
        "s_waitcnt lgkmcnt(10)\n\t"
        "v_mfma_f32_32x32x16_bf16 v[32:47], v[104:107], v[184:187], v[32:47]\n\t"
        "ds_read_b64_tr_b16 v[104:105], v129 offset:128\n\t"
        "ds_read_b64_tr_b16 v[106:107], v129 offset:3200\n\t"
        "s_add_u32 s44, s44, 0x1000\n\t"
        "s_and_b32 s44, s44, 0xffff\n\t"
        "buffer_load_dwordx4 v[180:183], v120, s[40:43], s44 offen offset:0 sc1\n\t"
        "s_waitcnt lgkmcnt(10)\n\t"
        "v_mfma_f32_32x32x16_bf16 v[48:63], v[108:111], v[184:187], v[48:63]\n\t"
        "s_add_u32 s46, s46, 12288\n\t"
        "s_add_u32 s47, s47, 1\n\t"
        "ds_read_b64_tr_b16 v[108:109], v130 offset:128\n\t"
        "ds_read_b64_tr_b16 v[110:111], v130 offset:3200\n\t"
        "s_waitcnt lgkmcnt(10)\n\t"
        "v_mfma_f32_32x32x16_bf16 v[64:79], v[184:187], v[112:115], v[64:79]\n\t"
        "s_cmp_eq_u32 s46, 110592\n\t"
        "s_cselect_b32 s46, 0, s46\n\t"
        "ds_read_b64_tr_b16 v[112:113], v129 offset:256\n\t"
        "ds_read_b64_tr_b16 v[114:115], v129 offset:3328\n\t"
        "s_waitcnt lgkmcnt(10)\n\t"
        "v_mfma_f32_32x32x16_bf16 v[80:95], v[184:187], v[116:119], v[80:95]\n\t"
        "ds_write_b32 v127, v128\n\t"
        "ds_read_b64_tr_b16 v[116:117], v130 offset:256\n\t"
        "ds_read_b64_tr_b16 v[118:119], v130 offset:3328\n\t"
        "s_waitcnt vmcnt(14) lgkmcnt(11)\n\t"
        "v_mfma_f32_32x32x16_bf16 v[0:15], v[96:99], v[188:191], v[0:15]\n\t"
        "ds_read_b32 v126, v125\n\t"
        "ds_read_b64_tr_b16 v[96:97], v129 offset:6144\n\t"
        "ds_read_b64_tr_b16 v[98:99], v129 offset:9216\n\t"
        "s_waitcnt lgkmcnt(12)\n\t"
        "v_mfma_f32_32x32x16_bf16 v[16:31], v[100:103], v[188:191], v[16:31]\n\t"
        "v_add_u32 v123, s46, v121\n\t"
        "ds_read_b64_tr_b16 v[100:101], v130 offset:6144\n\t"
        "ds_read_b64_tr_b16 v[102:103], v130 offset:9216\n\t"
        "s_waitcnt lgkmcnt(12)\n\t"
        "v_mfma_f32_32x32x16_bf16 v[32:47], v[104:107], v[188:191], v[32:47]\n\t"
        "ds_read_b64_tr_b16 v[104:105], v129 offset:6272\n\t"
        "ds_read_b64_tr_b16 v[106:107], v129 offset:9344\n\t"
        "buffer_load_dwordx4 v[184:187], v120, s[40:43], s44 offen offset:1024 sc1\n\t"
        "s_waitcnt lgkmcnt(12)\n\t"
        "v_mfma_f32_32x32x16_bf16 v[48:63], v[108:111], v[188:191], v[48:63]\n\t"
        "v_add_u32 v124, s46, v122\n\t"
        "ds_read_b64_tr_b16 v[108:109], v130 offset:6272\n\t"
        "ds_read_b64_tr_b16 v[110:111], v130 offset:9344\n\t"
        "s_waitcnt lgkmcnt(12)\n\t"
        "v_mfma_f32_32x32x16_bf16 v[64:79], v[188:191], v[112:115], v[64:79]\n\t"
        "ds_read_b64_tr_b16 v[112:113], v129 offset:6400\n\t"
        "ds_read_b64_tr_b16 v[114:115], v129 offset:9472\n\t"
        "s_waitcnt lgkmcnt(10)\n\t"
        "v_mfma_f32_32x32x16_bf16 v[80:95], v[188:191], v[116:119], v[80:95]\n\t"
        "v_cmp_gt_u32 vcc, s47, v126\n\t"
        "ds_read_b64_tr_b16 v[116:117], v130 offset:6400\n\t"
        "ds_read_b64_tr_b16 v[118:119], v130 offset:9472\n\t"
        "s_waitcnt vmcnt(14) lgkmcnt(10)\n\t"
        "v_mfma_f32_32x32x16_bf16 v[0:15], v[96:99], v[192:195], v[0:15]\n\t"
        "s_cbranch_vccnz 26f\n\t"
        "27:\n\t"
        "ds_read_b64_tr_b16 v[96:97], v123 offset:0\n\t"
        "ds_read_b64_tr_b16 v[98:99], v123 offset:3072\n\t"
        "s_waitcnt lgkmcnt(10)\n\t"
        "v_mfma_f32_32x32x16_bf16 v[16:31], v[100:103], v[192:195], v[16:31]\n\t"
        "v_add_u32 v128, 1, v128\n\t"
        "ds_read_b64_tr_b16 v[100:101], v124 offset:0\n\t"
        "ds_read_b64_tr_b16 v[102:103], v124 offset:3072\n\t"
        "s_waitcnt lgkmcnt(10)\n\t"
        "v_mfma_f32_32x32x16_bf16 v[32:47], v[104:107], v[192:195], v[32:47]\n\t"
        "ds_read_b64_tr_b16 v[104:105], v123 offset:128\n\t"
        "ds_read_b64_tr_b16 v[106:107], v123 offset:3200\n\t"
        "buffer_load_dwordx4 v[188:191], v120, s[40:43], s44 offen offset:2048 sc1\n\t"
        "s_waitcnt lgkmcnt(10)\n\t"
        "v_mfma_f32_32x32x16_bf16 v[48:63], v[108:111], v[192:195], v[48:63]\n\t"
        "s_add_u32 s46, s46, 12288\n\t"
        "s_add_u32 s47, s47, 1\n\t"
        "ds_read_b64_tr_b16 v[108:109], v124 offset:128\n\t"
        "ds_read_b64_tr_b16 v[110:111], v124 offset:3200\n\t"
        "s_waitcnt lgkmcnt(10)\n\t"
        "v_mfma_f32_32x32x16_bf16 v[64:79], v[192:195], v[112:115], v[64:79]\n\t"
        "s_cmp_eq_u32 s46, 110592\n\t"
        "s_cselect_b32 s46, 0, s46\n\t"
        "ds_read_b64_tr_b16 v[112:113], v123 offset:256\n\t"
        "ds_read_b64_tr_b16 v[114:115], v123 offset:3328\n\t"
        "s_waitcnt lgkmcnt(10)\n\t"
        "v_mfma_f32_32x32x16_bf16 v[80:95], v[192:195], v[116:119], v[80:95]\n\t"
        "ds_write_b32 v127, v128\n\t"
        "ds_read_b64_tr_b16 v[116:117], v124 offset:256\n\t"
        "ds_read_b64_tr_b16 v[118:119], v124 offset:3328\n\t"
        "s_waitcnt vmcnt(14) lgkmcnt(11)\n\t"
        "v_mfma_f32_32x32x16_bf16 v[0:15], v[96:99], v[132:135], v[0:15]\n\t"
        "ds_read_b32 v126, v125\n\t"
        "ds_read_b64_tr_b16 v[96:97], v123 offset:6144\n\t"
        "ds_read_b64_tr_b16 v[98:99], v123 offset:9216\n\t"
        "s_waitcnt lgkmcnt(12)\n\t"
        "v_mfma_f32_32x32x16_bf16 v[16:31], v[100:103], v[132:135], v[16:31]\n\t"
        "v_add_u32 v129, s46, v121\n\t"
        "ds_read_b64_tr_b16 v[100:101], v124 offset:6144\n\t"
        "ds_read_b64_tr_b16 v[102:103], v124 offset:9216\n\t"
        "s_waitcnt lgkmcnt(12)\n\t"
        "v_mfma_f32_32x32x16_bf16 v[32:47], v[104:107], v[132:135], v[32:47]\n\t"
        "ds_read_b64_tr_b16 v[104:105], v123 offset:6272\n\t"
        "ds_read_b64_tr_b16 v[106:107], v123 offset:9344\n\t"
        "buffer_load_dwordx4 v[192:195], v120, s[40:43], s44 offen offset:3072 sc1\n\t"
        "s_waitcnt lgkmcnt(12)\n\t"
        "v_mfma_f32_32x32x16_bf16 v[48:63], v[108:111], v[132:135], v[48:63]\n\t"
        "v_add_u32 v130, s46, v122\n\t"
        "ds_read_b64_tr_b16 v[108:109], v124 offset:6272\n\t"
        "ds_read_b64_tr_b16 v[110:111], v124 offset:9344\n\t"
        "s_waitcnt lgkmcnt(12)\n\t"
        "v_mfma_f32_32x32x16_bf16 v[64:79], v[132:135], v[112:115], v[64:79]\n\t"
        "ds_read_b64_tr_b16 v[112:113], v123 offset:6400\n\t"
        "ds_read_b64_tr_b16 v[114:115], v123 offset:9472\n\t"
        "s_waitcnt lgkmcnt(10)\n\t"
        "v_mfma_f32_32x32x16_bf16 v[80:95], v[132:135], v[116:119], v[80:95]\n\t"
        "v_cmp_gt_u32 vcc, s47, v126\n\t"
        "ds_read_b64_tr_b16 v[116:117], v124 offset:6400\n\t"
        "ds_read_b64_tr_b16 v[118:119], v124 offset:9472\n\t"
        "s_waitcnt vmcnt(14) lgkmcnt(10)\n\t"
        "v_mfma_f32_32x32x16_bf16 v[0:15], v[96:99], v[136:139], v[0:15]\n\t"
        "s_cbranch_vccnz 28f\n\t"
        "29:\n\t"
        "ds_read_b64_tr_b16 v[96:97], v129 offset:0\n\t"
        "ds_read_b64_tr_b16 v[98:99], v129 offset:3072\n\t"
        "s_waitcnt lgkmcnt(10)\n\t"
        "v_mfma_f32_32x32x16_bf16 v[16:31], v[100:103], v[136:139], v[16:31]\n\t"
        "v_add_u32 v128, 1, v128\n\t"
        "ds_read_b64_tr_b16 v[100:101], v130 offset:0\n\t"
        "ds_read_b64_tr_b16 v[102:103], v130 offset:3072\n\t"
        "s_waitcnt lgkmcnt(10)\n\t"
        "v_mfma_f32_32x32x16_bf16 v[32:47], v[104:107], v[136:139], v[32:47]\n\t"
        "ds_read_b64_tr_b16 v[104:105], v129 offset:128\n\t"
        "ds_read_b64_tr_b16 v[106:107], v129 offset:3200\n\t"
        "s_add_u32 s44, s44, 0x1000\n\t"
        "s_and_b32 s44, s44, 0xffff\n\t"
        "buffer_load_dwordx4 v[132:135], v120, s[40:43], s44 offen offset:0 sc1\n\t"
        "s_waitcnt lgkmcnt(10)\n\t"
        "v_mfma_f32_32x32x16_bf16 v[48:63], v[108:111], v[136:139], v[48:63]\n\t"
        "s_add_u32 s46, s46, 12288\n\t"
        "s_add_u32 s47, s47, 1\n\t"
        "ds_read_b64_tr_b16 v[108:109], v130 offset:128\n\t"
        "ds_read_b64_tr_b16 v[110:111], v130 offset:3200\n\t"
        "s_waitcnt lgkmcnt(10)\n\t"
        "v_mfma_f32_32x32x16_bf16 v[64:79], v[136:139], v[112:115], v[64:79]\n\t"
        "s_cmp_eq_u32 s46, 110592\n\t"
        "s_cselect_b32 s46, 0, s46\n\t"
        "ds_read_b64_tr_b16 v[112:113], v129 offset:256\n\t"
        "ds_read_b64_tr_b16 v[114:115], v129 offset:3328\n\t"
        "s_waitcnt lgkmcnt(10)\n\t"
        "v_mfma_f32_32x32x16_bf16 v[80:95], v[136:139], v[116:119], v[80:95]\n\t"
        "ds_write_b32 v127, v128\n\t"
        "ds_read_b64_tr_b16 v[116:117], v130 offset:256\n\t"
        "ds_read_b64_tr_b16 v[118:119], v130 offset:3328\n\t"
        "s_waitcnt vmcnt(14) lgkmcnt(11)\n\t"
        "v_mfma_f32_32x32x16_bf16 v[0:15], v[96:99], v[140:143], v[0:15]\n\t"
        "ds_read_b32 v126, v125\n\t"
        "ds_read_b64_tr_b16 v[96:97], v129 offset:6144\n\t"
        "ds_read_b64_tr_b16 v[98:99], v129 offset:9216\n\t"
        "s_waitcnt lgkmcnt(12)\n\t"
        "v_mfma_f32_32x32x16_bf16 v[16:31], v[100:103], v[140:143], v[16:31]\n\t"
        "v_add_u32 v123, s46, v121\n\t"
        "ds_read_b64_tr_b16 v[100:101], v130 offset:6144\n\t"
        "ds_read_b64_tr_b16 v[102:103], v130 offset:9216\n\t"
        "s_waitcnt lgkmcnt(12)\n\t"
        "v_mfma_f32_32x32x16_bf16 v[32:47], v[104:107], v[140:143], v[32:47]\n\t"
        "ds_read_b64_tr_b16 v[104:105], v129 offset:6272\n\t"
        "ds_read_b64_tr_b16 v[106:107], v129 offset:9344\n\t"
        "buffer_load_dwordx4 v[136:139], v120, s[40:43], s44 offen offset:1024 sc1\n\t"
        "s_waitcnt lgkmcnt(12)\n\t"
        "v_mfma_f32_32x32x16_bf16 v[48:63], v[108:111], v[140:143], v[48:63]\n\t"
        "v_add_u32 v124, s46, v122\n\t"
        "ds_read_b64_tr_b16 v[108:109], v130 offset:6272\n\t"
        "ds_read_b64_tr_b16 v[110:111], v130 offset:9344\n\t"
        "s_waitcnt lgkmcnt(12)\n\t"
        "v_mfma_f32_32x32x16_bf16 v[64:79], v[140:143], v[112:115], v[64:79]\n\t"
        "ds_read_b64_tr_b16 v[112:113], v129 offset:6400\n\t"
        "ds_read_b64_tr_b16 v[114:115], v129 offset:9472\n\t"
        "s_waitcnt lgkmcnt(10)\n\t"
        "v_mfma_f32_32x32x16_bf16 v[80:95], v[140:143], v[116:119], v[80:95]\n\t"
        "v_cmp_gt_u32 vcc, s47, v126\n\t"
        "ds_read_b64_tr_b16 v[116:117], v130 offset:6400\n\t"
        "ds_read_b64_tr_b16 v[118:119], v130 offset:9472\n\t"
        "s_waitcnt vmcnt(14) lgkmcnt(10)\n\t"
        "v_mfma_f32_32x32x16_bf16 v[0:15], v[96:99], v[144:147], v[0:15]\n\t"
        "s_cbranch_vccnz 30f\n\t"
        "31:\n\t"
        "ds_read_b64_tr_b16 v[96:97], v123 offset:0\n\t"
        "ds_read_b64_tr_b16 v[98:99], v123 offset:3072\n\t"
        "s_waitcnt lgkmcnt(10)\n\t"
        "v_mfma_f32_32x32x16_bf16 v[16:31], v[100:103], v[144:147], v[16:31]\n\t"
        "v_add_u32 v128, 1, v128\n\t"
        "ds_read_b64_tr_b16 v[100:101], v124 offset:0\n\t"
        "ds_read_b64_tr_b16 v[102:103], v124 offset:3072\n\t"
        "s_waitcnt lgkmcnt(10)\n\t"
        "v_mfma_f32_32x32x16_bf16 v[32:47], v[104:107], v[144:147], v[32:47]\n\t"
        "ds_read_b64_tr_b16 v[104:105], v123 offset:128\n\t"
        "ds_read_b64_tr_b16 v[106:107], v123 offset:3200\n\t"
        "buffer_load_dwordx4 v[140:143], v120, s[40:43], s44 offen offset:2048 sc1\n\t"
        "s_waitcnt lgkmcnt(10)\n\t"
        "v_mfma_f32_32x32x16_bf16 v[48:63], v[108:111], v[144:147], v[48:63]\n\t"
        "s_add_u32 s46, s46, 12288\n\t"
        "s_add_u32 s47, s47, 1\n\t"
        "ds_read_b64_tr_b16 v[108:109], v124 offset:128\n\t"
        "ds_read_b64_tr_b16 v[110:111], v124 offset:3200\n\t"
        "s_waitcnt lgkmcnt(10)\n\t"
        "v_mfma_f32_32x32x16_bf16 v[64:79], v[144:147], v[112:115], v[64:79]\n\t"
        "s_cmp_eq_u32 s46, 110592\n\t"
        "s_cselect_b32 s46, 0, s46\n\t"
        "ds_read_b64_tr_b16 v[112:113], v123 offset:256\n\t"
        "ds_read_b64_tr_b16 v[114:115], v123 offset:3328\n\t"
        "s_waitcnt lgkmcnt(10)\n\t"
        "v_mfma_f32_32x32x16_bf16 v[80:95], v[144:147], v[116:119], v[80:95]\n\t"
        "ds_write_b32 v127, v128\n\t"
        "ds_read_b64_tr_b16 v[116:117], v124 offset:256\n\t"
        "ds_read_b64_tr_b16 v[118:119], v124 offset:3328\n\t"
        "s_sub_u32 s45, s45, 1\n\t"
        "s_cmp_lg_u32 s45, 0\n\t"
        "s_cbranch_scc1 1b\n\t"
        "s_waitcnt vmcnt(14) lgkmcnt(11)\n\t"
        "v_mfma_f32_32x32x16_bf16 v[0:15], v[96:99], v[148:151], v[0:15]\n\t"
        "ds_read_b32 v126, v125\n\t"
        "s_add_u32 s47, s47, 4\n\t"
        "ds_read_b64_tr_b16 v[96:97], v123 offset:6400\n\t"
        "ds_read_b64_tr_b16 v[98:99], v123 offset:9472\n\t"
        "s_waitcnt lgkmcnt(12)\n\t"
        "v_mfma_f32_32x32x16_bf16 v[16:31], v[100:103], v[148:151], v[16:31]\n\t"
        "ds_read_b64_tr_b16 v[100:101], v124 offset:6400\n\t"
        "ds_read_b64_tr_b16 v[102:103], v124 offset:9472\n\t"
        "s_waitcnt lgkmcnt(4)\n\t"
        "v_cmp_gt_u32 vcc, s47, v126\n\t"
        "v_mfma_f32_32x32x16_bf16 v[32:47], v[104:107], v[148:151], v[32:47]\n\t"
        "s_cbranch_vccnz 74f\n\t"
        "75:\n\t"
        "ds_read_b64_tr_b16 v[104:105], v121 offset:256\n\t"
        "ds_read_b64_tr_b16 v[106:107], v121 offset:3328\n\t"
        "s_waitcnt lgkmcnt(12)\n\t"
        "v_mfma_f32_32x32x16_bf16 v[48:63], v[108:111], v[148:151], v[48:63]\n\t"
        "ds_read_b64_tr_b16 v[108:109], v122 offset:256\n\t"
        "ds_read_b64_tr_b16 v[110:111], v122 offset:3328\n\t"
        "s_waitcnt lgkmcnt(12)\n\t"
        "v_mfma_f32_32x32x16_bf16 v[64:79], v[148:151], v[112:115], v[64:79]\n\t"
        "ds_read_b64_tr_b16 v[112:113], v121 offset:6400\n\t"
        "ds_read_b64_tr_b16 v[114:115], v121 offset:9472\n\t"
        "s_waitcnt lgkmcnt(11)\n\t"
        "v_mfma_f32_32x32x16_bf16 v[80:95], v[148:151], v[116:119], v[80:95]\n\t"
        "ds_read_b64_tr_b16 v[116:117], v122 offset:6400\n\t"
        "ds_read_b64_tr_b16 v[118:119], v122 offset:9472\n\t"
        "s_waitcnt vmcnt(13) lgkmcnt(10)\n\t"
        "v_mfma_f32_32x32x16_bf16 v[64:79], v[152:155], v[96:99], v[64:79]\n\t"
        "ds_read_b64_tr_b16 v[96:97], v121 offset:12544\n\t"
        "ds_read_b64_tr_b16 v[98:99], v121 offset:15616\n\t"
        "s_waitcnt lgkmcnt(10)\n\t"
        "v_mfma_f32_32x32x16_bf16 v[80:95], v[152:155], v[100:103], v[80:95]\n\t"
        "ds_read_b64_tr_b16 v[100:101], v122 offset:12544\n\t"
        "ds_read_b64_tr_b16 v[102:103], v122 offset:15616\n\t"
        "s_waitcnt vmcnt(12) lgkmcnt(10)\n\t"
        "v_mfma_f32_32x32x16_bf16 v[64:79], v[156:159], v[104:107], v[64:79]\n\t"
        "ds_read_b128 v[196:199], v215 offset:0\n\t"
        "ds_read_b64_tr_b16 v[104:105], v121 offset:18688\n\t"
        "ds_read_b64_tr_b16 v[106:107], v121 offset:21760\n\t"
        "s_waitcnt lgkmcnt(11)\n\t"
        "v_mfma_f32_32x32x16_bf16 v[80:95], v[156:159], v[108:111], v[80:95]\n\t"
        "ds_read_b128 v[200:203], v215 offset:32\n\t"
        "ds_read_b64_tr_b16 v[108:109], v122 offset:18688\n\t"
        "ds_read_b64_tr_b16 v[110:111], v122 offset:21760\n\t"
        "s_waitcnt vmcnt(11) lgkmcnt(12)\n\t"
        "v_mfma_f32_32x32x16_bf16 v[64:79], v[160:163], v[112:115], v[64:79]\n\t"
        "ds_read_b128 v[204:207], v215 offset:64\n\t"
        "ds_read_b64_tr_b16 v[112:113], v121 offset:24832\n\t"
        "ds_read_b64_tr_b16 v[114:115], v121 offset:27904\n\t"
        "s_waitcnt lgkmcnt(13)\n\t"
        "v_mfma_f32_32x32x16_bf16 v[80:95], v[160:163], v[116:119], v[80:95]\n\t"
        "ds_read_b128 v[208:211], v215 offset:96\n\t"
        "ds_read_b64_tr_b16 v[116:117], v122 offset:24832\n\t"
        "ds_read_b64_tr_b16 v[118:119], v122 offset:27904\n\t"
        "s_waitcnt vmcnt(10) lgkmcnt(14)\n\t"
        "v_mfma_f32_32x32x16_bf16 v[64:79], v[164:167], v[96:99], v[64:79]\n\t"
        "ds_read_b64_tr_b16 v[96:97], v121 offset:30976\n\t"
        "ds_read_b64_tr_b16 v[98:99], v121 offset:34048\n\t"
        "s_waitcnt lgkmcnt(14)\n\t"
        "v_mfma_f32_32x32x16_bf16 v[80:95], v[164:167], v[100:103], v[80:95]\n\t"
        "ds_read_b64_tr_b16 v[100:101], v122 offset:30976\n\t"
        "ds_read_b64_tr_b16 v[102:103], v122 offset:34048\n\t"
        "s_waitcnt vmcnt(9) lgkmcnt(13)\n\t"
        "v_mfma_f32_32x32x16_bf16 v[64:79], v[168:171], v[104:107], v[64:79]\n\t"
        "ds_read_b64_tr_b16 v[104:105], v121 offset:37120\n\t"
        "ds_read_b64_tr_b16 v[106:107], v121 offset:40192\n\t"
        "s_waitcnt lgkmcnt(12)\n\t"
        "v_mfma_f32_32x32x16_bf16 v[80:95], v[168:171], v[108:111], v[80:95]\n\t"
        "ds_read_b64_tr_b16 v[108:109], v122 offset:37120\n\t"
        "ds_read_b64_tr_b16 v[110:111], v122 offset:40192\n\t"
        "s_waitcnt vmcnt(8) lgkmcnt(11)\n\t"
        "v_mfma_f32_32x32x16_bf16 v[64:79], v[172:175], v[112:115], v[64:79]\n\t"
        "ds_read_b64_tr_b16 v[112:113], v121 offset:43264\n\t"
        "ds_read_b64_tr_b16 v[114:115], v121 offset:46336\n\t"
        "s_waitcnt lgkmcnt(10)\n\t"
        "v_mfma_f32_32x32x16_bf16 v[80:95], v[172:175], v[116:119], v[80:95]\n\t"
        "ds_read_b64_tr_b16 v[116:117], v122 offset:43264\n\t"
        "ds_read_b64_tr_b16 v[118:119], v122 offset:46336\n\t"
        "s_waitcnt vmcnt(7) lgkmcnt(10)\n\t"
        "v_mfma_f32_32x32x16_bf16 v[64:79], v[176:179], v[96:99], v[64:79]\n\t"
        "ds_read_b64_tr_b16 v[96:97], v121 offset:49408\n\t"
        "ds_read_b64_tr_b16 v[98:99], v121 offset:52480\n\t"
        "s_waitcnt lgkmcnt(10)\n\t"
        "v_mfma_f32_32x32x16_bf16 v[80:95], v[176:179], v[100:103], v[80:95]\n\t"
        "ds_read_b64_tr_b16 v[100:101], v122 offset:49408\n\t"
        "ds_read_b64_tr_b16 v[102:103], v122 offset:52480\n\t"
        "s_waitcnt vmcnt(6) lgkmcnt(10)\n\t"
        "v_mfma_f32_32x32x16_bf16 v[64:79], v[180:183], v[104:107], v[64:79]\n\t"
        "ds_read_b64_tr_b16 v[104:105], v121 offset:55552\n\t"
        "ds_read_b64_tr_b16 v[106:107], v121 offset:58624\n\t"
        "s_waitcnt lgkmcnt(10)\n\t"
        "v_mfma_f32_32x32x16_bf16 v[80:95], v[180:183], v[108:111], v[80:95]\n\t"
        "ds_read_b64_tr_b16 v[108:109], v122 offset:55552\n\t"
        "ds_read_b64_tr_b16 v[110:111], v122 offset:58624\n\t"
        "s_waitcnt vmcnt(5) lgkmcnt(10)\n\t"
        "v_mfma_f32_32x32x16_bf16 v[64:79], v[184:187], v[112:115], v[64:79]\n\t"
        "ds_read_b64_tr_b16 v[112:113], v123 offset:6272\n\t"
        "ds_read_b64_tr_b16 v[114:115], v123 offset:9344\n\t"
        "s_waitcnt lgkmcnt(10)\n\t"
        "v_mfma_f32_32x32x16_bf16 v[80:95], v[184:187], v[116:119], v[80:95]\n\t"
        "ds_read_b64_tr_b16 v[116:117], v124 offset:6272\n\t"
        "ds_read_b64_tr_b16 v[118:119], v124 offset:9344\n\t"
        "s_waitcnt vmcnt(4) lgkmcnt(10)\n\t"
        "v_mfma_f32_32x32x16_bf16 v[64:79], v[188:191], v[96:99], v[64:79]\n\t"
        "ds_read_b64_tr_b16 v[96:97], v121 offset:128\n\t"
        "ds_read_b64_tr_b16 v[98:99], v121 offset:3200\n\t"
        "s_waitcnt lgkmcnt(10)\n\t"
        "v_mfma_f32_32x32x16_bf16 v[80:95], v[188:191], v[100:103], v[80:95]\n\t"
        "ds_read_b64_tr_b16 v[100:101], v122 offset:128\n\t"
        "ds_read_b64_tr_b16 v[102:103], v122 offset:3200\n\t"
        "s_waitcnt vmcnt(0) lgkmcnt(10)\n\t"
        "v_mfma_f32_32x32x16_bf16 v[64:79], v[192:195], v[104:107], v[64:79]\n\t"
        "ds_read_b64_tr_b16 v[104:105], v121 offset:6272\n\t"
        "ds_read_b64_tr_b16 v[106:107], v121 offset:9344\n\t"
        "s_waitcnt lgkmcnt(10)\n\t"
        "v_mfma_f32_32x32x16_bf16 v[80:95], v[192:195], v[108:111], v[80:95]\n\t"
        "ds_read_b64_tr_b16 v[108:109], v122 offset:6272\n\t"
        "ds_read_b64_tr_b16 v[110:111], v122 offset:9344\n\t"
        "s_waitcnt lgkmcnt(10)\n\t"
        "v_mfma_f32_32x32x16_bf16 v[32:47], v[112:115], v[152:155], v[32:47]\n\t"
        "ds_read_b64_tr_b16 v[112:113], v121 offset:12416\n\t"
        "ds_read_b64_tr_b16 v[114:115], v121 offset:15488\n\t"
        "s_waitcnt lgkmcnt(10)\n\t"
        "v_mfma_f32_32x32x16_bf16 v[48:63], v[116:119], v[152:155], v[48:63]\n\t"
        "ds_read_b64_tr_b16 v[116:117], v122 offset:12416\n\t"
        "ds_read_b64_tr_b16 v[118:119], v122 offset:15488\n\t"
        "v_mul_f32 v64, v196, v64\n\t"
        "v_mul_f32 v65, v197, v65\n\t"
        "v_mul_f32 v66, v198, v66\n\t"
        "s_waitcnt lgkmcnt(10)\n\t"
        "v_mfma_f32_32x32x16_bf16 v[32:47], v[96:99], v[156:159], v[32:47]\n\t"
        "ds_read_b64_tr_b16 v[96:97], v121 offset:18560\n\t"
        "ds_read_b64_tr_b16 v[98:99], v121 offset:21632\n\t"
        "v_mul_f32 v67, v199, v67\n\t"
        "v_mul_f32 v68, v200, v68\n\t"
        "v_mul_f32 v69, v201, v69\n\t"
        "s_waitcnt lgkmcnt(10)\n\t"
        "v_mfma_f32_32x32x16_bf16 v[48:63], v[100:103], v[156:159], v[48:63]\n\t"
        "ds_read_b64_tr_b16 v[100:101], v122 offset:18560\n\t"
        "ds_read_b64_tr_b16 v[102:103], v122 offset:21632\n\t"
        "v_mul_f32 v70, v202, v70\n\t"
        "v_mul_f32 v71, v203, v71\n\t"
        "v_cvt_pk_bf16_f32 v64, v64, v65\n\t"
        "s_waitcnt lgkmcnt(10)\n\t"
        "v_mfma_f32_32x32x16_bf16 v[32:47], v[104:107], v[160:163], v[32:47]\n\t"
        "ds_read_b64_tr_b16 v[104:105], v121 offset:24704\n\t"
        "ds_read_b64_tr_b16 v[106:107], v121 offset:27776\n\t"
        "v_cvt_pk_bf16_f32 v65, v66, v67\n\t"
        "v_cvt_pk_bf16_f32 v66, v68, v69\n\t"
        "v_cvt_pk_bf16_f32 v67, v70, v71\n\t"
        "s_waitcnt lgkmcnt(10)\n\t"
        "v_mfma_f32_32x32x16_bf16 v[48:63], v[108:111], v[160:163], v[48:63]\n\t"
        "ds_read_b64_tr_b16 v[108:109], v122 offset:24704\n\t"
        "ds_read_b64_tr_b16 v[110:111], v122 offset:27776\n\t"
        "v_mul_f32 v72, v204, v72\n\t"
        "v_mul_f32 v73, v205, v73\n\t"
        "v_mul_f32 v74, v206, v74\n\t"
        "s_waitcnt lgkmcnt(10)\n\t"
        "v_mfma_f32_32x32x16_bf16 v[32:47], v[112:115], v[164:167], v[32:47]\n\t"
        "ds_read_b64_tr_b16 v[112:113], v121 offset:30848\n\t"
        "ds_read_b64_tr_b16 v[114:115], v121 offset:33920\n\t"
        "v_mul_f32 v75, v207, v75\n\t"
        "v_mul_f32 v76, v208, v76\n\t"
        "v_mul_f32 v77, v209, v77\n\t"
        "s_waitcnt lgkmcnt(10)\n\t"
        "v_mfma_f32_32x32x16_bf16 v[48:63], v[116:119], v[164:167], v[48:63]\n\t"
        "ds_read_b64_tr_b16 v[116:117], v122 offset:30848\n\t"
        "ds_read_b64_tr_b16 v[118:119], v122 offset:33920\n\t"
        "v_mul_f32 v78, v210, v78\n\t"
        "v_mul_f32 v79, v211, v79\n\t"
        "v_cvt_pk_bf16_f32 v68, v72, v73\n\t"
        "s_waitcnt lgkmcnt(10)\n\t"
        "v_mfma_f32_32x32x16_bf16 v[32:47], v[96:99], v[168:171], v[32:47]\n\t"
        "ds_read_b64_tr_b16 v[96:97], v121 offset:36992\n\t"
        "ds_read_b64_tr_b16 v[98:99], v121 offset:40064\n\t"
        "v_cvt_pk_bf16_f32 v69, v74, v75\n\t"
        "v_cvt_pk_bf16_f32 v70, v76, v77\n\t"
        "v_cvt_pk_bf16_f32 v71, v78, v79\n\t"
        "s_waitcnt lgkmcnt(10)\n\t"
        "v_mfma_f32_32x32x16_bf16 v[48:63], v[100:103], v[168:171], v[48:63]\n\t"
        "ds_read_b64_tr_b16 v[100:101], v122 offset:36992\n\t"
        "ds_read_b64_tr_b16 v[102:103], v122 offset:40064\n\t"
        "v_permlane32_swap_b32 v64, v66\n\t"
        "v_permlane32_swap_b32 v65, v67\n\t"
        "v_permlane32_swap_b32 v68, v70\n\t"
        "s_waitcnt lgkmcnt(10)\n\t"
        "v_mfma_f32_32x32x16_bf16 v[32:47], v[104:107], v[172:175], v[32:47]\n\t"
        "ds_read_b64_tr_b16 v[104:105], v121 offset:43136\n\t"
        "ds_read_b64_tr_b16 v[106:107], v121 offset:46208\n\t"
        "v_permlane32_swap_b32 v69, v71\n\t"
        "buffer_store_dwordx4 v[64:67], v213, s[40:43], 0 offen offset:2048 sc1\n\t"
        "buffer_store_dwordx4 v[68:71], v213, s[40:43], 0 offen offset:3072 sc1\n\t"
        "s_waitcnt lgkmcnt(10)\n\t"
        "v_mfma_f32_32x32x16_bf16 v[48:63], v[108:111], v[172:175], v[48:63]\n\t"
        "ds_read_b64_tr_b16 v[108:109], v122 offset:43136\n\t"
        "ds_read_b64_tr_b16 v[110:111], v122 offset:46208\n\t"
        "v_mul_f32 v80, v196, v80\n\t"
        "v_mul_f32 v81, v197, v81\n\t"
        "v_mul_f32 v82, v198, v82\n\t"
        "s_waitcnt lgkmcnt(10)\n\t"
        "v_mfma_f32_32x32x16_bf16 v[32:47], v[112:115], v[176:179], v[32:47]\n\t"
        "ds_read_b64_tr_b16 v[112:113], v121 offset:49280\n\t"
        "ds_read_b64_tr_b16 v[114:115], v121 offset:52352\n\t"
        "v_mul_f32 v83, v199, v83\n\t"
        "v_mul_f32 v84, v200, v84\n\t"
        "v_mul_f32 v85, v201, v85\n\t"
        "s_waitcnt lgkmcnt(10)\n\t"
        "v_mfma_f32_32x32x16_bf16 v[48:63], v[116:119], v[176:179], v[48:63]\n\t"
        "ds_read_b64_tr_b16 v[116:117], v122 offset:49280\n\t"
        "ds_read_b64_tr_b16 v[118:119], v122 offset:52352\n\t"
        "v_mul_f32 v86, v202, v86\n\t"
        "v_mul_f32 v87, v203, v87\n\t"
        "v_cvt_pk_bf16_f32 v80, v80, v81\n\t"
        "s_waitcnt lgkmcnt(10)\n\t"
        "v_mfma_f32_32x32x16_bf16 v[32:47], v[96:99], v[180:183], v[32:47]\n\t"
        "ds_read_b64_tr_b16 v[96:97], v121 offset:55424\n\t"
        "ds_read_b64_tr_b16 v[98:99], v121 offset:58496\n\t"
        "v_cvt_pk_bf16_f32 v81, v82, v83\n\t"
        "v_cvt_pk_bf16_f32 v82, v84, v85\n\t"
        "v_cvt_pk_bf16_f32 v83, v86, v87\n\t"
        "s_waitcnt lgkmcnt(10)\n\t"
        "v_mfma_f32_32x32x16_bf16 v[48:63], v[100:103], v[180:183], v[48:63]\n\t"
        "ds_read_b64_tr_b16 v[100:101], v122 offset:55424\n\t"
        "ds_read_b64_tr_b16 v[102:103], v122 offset:58496\n\t"
        "v_mul_f32 v88, v204, v88\n\t"
        "v_mul_f32 v89, v205, v89\n\t"
        "v_mul_f32 v90, v206, v90\n\t"
        "s_waitcnt lgkmcnt(10)\n\t"
        "v_mfma_f32_32x32x16_bf16 v[32:47], v[104:107], v[184:187], v[32:47]\n\t"
        "ds_read_b64_tr_b16 v[104:105], v123 offset:6144\n\t"
        "ds_read_b64_tr_b16 v[106:107], v123 offset:9216\n\t"
        "v_mul_f32 v91, v207, v91\n\t"
        "v_mul_f32 v92, v208, v92\n\t"
        "v_mul_f32 v93, v209, v93\n\t"
        "s_waitcnt lgkmcnt(10)\n\t"
        "v_mfma_f32_32x32x16_bf16 v[48:63], v[108:111], v[184:187], v[48:63]\n\t"
        "ds_read_b64_tr_b16 v[108:109], v124 offset:6144\n\t"
        "ds_read_b64_tr_b16 v[110:111], v124 offset:9216\n\t"
        "v_mul_f32 v94, v210, v94\n\t"
        "v_mul_f32 v95, v211, v95\n\t"
        "v_cvt_pk_bf16_f32 v84, v88, v89\n\t"
        "s_waitcnt lgkmcnt(10)\n\t"
        "v_mfma_f32_32x32x16_bf16 v[32:47], v[112:115], v[188:191], v[32:47]\n\t"
        "ds_read_b64_tr_b16 v[112:113], v121 offset:0\n\t"
        "ds_read_b64_tr_b16 v[114:115], v121 offset:3072\n\t"
        "v_cvt_pk_bf16_f32 v85, v90, v91\n\t"
        "v_cvt_pk_bf16_f32 v86, v92, v93\n\t"
        "v_cvt_pk_bf16_f32 v87, v94, v95\n\t"
        "s_waitcnt lgkmcnt(10)\n\t"
        "v_mfma_f32_32x32x16_bf16 v[48:63], v[116:119], v[188:191], v[48:63]\n\t"
        "ds_read_b64_tr_b16 v[116:117], v122 offset:0\n\t"
        "ds_read_b64_tr_b16 v[118:119], v122 offset:3072\n\t"
        "v_permlane32_swap_b32 v80, v82\n\t"
        "v_permlane32_swap_b32 v81, v83\n\t"
        "v_permlane32_swap_b32 v84, v86\n\t"
        "s_waitcnt lgkmcnt(10)\n\t"
        "v_mfma_f32_32x32x16_bf16 v[32:47], v[96:99], v[192:195], v[32:47]\n\t"
        "ds_read_b64_tr_b16 v[96:97], v121 offset:6144\n\t"
        "ds_read_b64_tr_b16 v[98:99], v121 offset:9216\n\t"
        "v_permlane32_swap_b32 v85, v87\n\t"
        "buffer_store_dwordx4 v[80:83], v214, s[40:43], 0 offen offset:2048 sc1\n\t"
        "buffer_store_dwordx4 v[84:87], v214, s[40:43], 0 offen offset:3072 sc1\n\t"
        "s_waitcnt lgkmcnt(10)\n\t"
        "v_mfma_f32_32x32x16_bf16 v[48:63], v[100:103], v[192:195], v[48:63]\n\t"
        "ds_read_b64_tr_b16 v[100:101], v122 offset:6144\n\t"
        "ds_read_b64_tr_b16 v[102:103], v122 offset:9216\n\t"
        "s_waitcnt lgkmcnt(10)\n\t"
        "v_mfma_f32_32x32x16_bf16 v[0:15], v[104:107], v[152:155], v[0:15]\n\t"
        "ds_read_b64_tr_b16 v[104:105], v121 offset:12288\n\t"
        "ds_read_b64_tr_b16 v[106:107], v121 offset:15360\n\t"
        "s_waitcnt lgkmcnt(10)\n\t"
        "v_mfma_f32_32x32x16_bf16 v[16:31], v[108:111], v[152:155], v[16:31]\n\t"
        "ds_read_b64_tr_b16 v[108:109], v122 offset:12288\n\t"
        "ds_read_b64_tr_b16 v[110:111], v122 offset:15360\n\t"
        "v_mul_f32 v32, v212, v32\n\t"
        "v_mul_f32 v33, v212, v33\n\t"
        "v_mul_f32 v34, v212, v34\n\t"
        "s_waitcnt lgkmcnt(10)\n\t"
        "v_mfma_f32_32x32x16_bf16 v[0:15], v[112:115], v[156:159], v[0:15]\n\t"
        "ds_read_b64_tr_b16 v[112:113], v121 offset:18432\n\t"
        "ds_read_b64_tr_b16 v[114:115], v121 offset:21504\n\t"
        "v_mul_f32 v35, v212, v35\n\t"
        "v_mul_f32 v36, v212, v36\n\t"
        "v_mul_f32 v37, v212, v37\n\t"
        "s_waitcnt lgkmcnt(10)\n\t"
        "v_mfma_f32_32x32x16_bf16 v[16:31], v[116:119], v[156:159], v[16:31]\n\t"
        "ds_read_b64_tr_b16 v[116:117], v122 offset:18432\n\t"
        "ds_read_b64_tr_b16 v[118:119], v122 offset:21504\n\t"
        "v_mul_f32 v38, v212, v38\n\t"
        "v_mul_f32 v39, v212, v39\n\t"
        "v_cvt_pk_bf16_f32 v32, v32, v33\n\t"
        "s_waitcnt lgkmcnt(10)\n\t"
        "v_mfma_f32_32x32x16_bf16 v[0:15], v[96:99], v[160:163], v[0:15]\n\t"
        "ds_read_b64_tr_b16 v[96:97], v121 offset:24576\n\t"
        "ds_read_b64_tr_b16 v[98:99], v121 offset:27648\n\t"
        "v_cvt_pk_bf16_f32 v33, v34, v35\n\t"
        "v_cvt_pk_bf16_f32 v34, v36, v37\n\t"
        "v_cvt_pk_bf16_f32 v35, v38, v39\n\t"
        "s_waitcnt lgkmcnt(10)\n\t"
        "v_mfma_f32_32x32x16_bf16 v[16:31], v[100:103], v[160:163], v[16:31]\n\t"
        "ds_read_b64_tr_b16 v[100:101], v122 offset:24576\n\t"
        "ds_read_b64_tr_b16 v[102:103], v122 offset:27648\n\t"
        "v_mul_f32 v40, v212, v40\n\t"
        "v_mul_f32 v41, v212, v41\n\t"
        "v_mul_f32 v42, v212, v42\n\t"
        "s_waitcnt lgkmcnt(10)\n\t"
        "v_mfma_f32_32x32x16_bf16 v[0:15], v[104:107], v[164:167], v[0:15]\n\t"
        "ds_read_b64_tr_b16 v[104:105], v121 offset:30720\n\t"
        "ds_read_b64_tr_b16 v[106:107], v121 offset:33792\n\t"
        "v_mul_f32 v43, v212, v43\n\t"
        "v_mul_f32 v44, v212, v44\n\t"
        "v_mul_f32 v45, v212, v45\n\t"
        "s_waitcnt lgkmcnt(10)\n\t"
        "v_mfma_f32_32x32x16_bf16 v[16:31], v[108:111], v[164:167], v[16:31]\n\t"
        "ds_read_b64_tr_b16 v[108:109], v122 offset:30720\n\t"
        "ds_read_b64_tr_b16 v[110:111], v122 offset:33792\n\t"
        "v_mul_f32 v46, v212, v46\n\t"
        "v_mul_f32 v47, v212, v47\n\t"
        "v_cvt_pk_bf16_f32 v36, v40, v41\n\t"
        "s_waitcnt lgkmcnt(10)\n\t"
        "v_mfma_f32_32x32x16_bf16 v[0:15], v[112:115], v[168:171], v[0:15]\n\t"
        "ds_read_b64_tr_b16 v[112:113], v121 offset:36864\n\t"
        "ds_read_b64_tr_b16 v[114:115], v121 offset:39936\n\t"
        "v_cvt_pk_bf16_f32 v37, v42, v43\n\t"
        "v_cvt_pk_bf16_f32 v38, v44, v45\n\t"
        "v_cvt_pk_bf16_f32 v39, v46, v47\n\t"
        "s_waitcnt lgkmcnt(10)\n\t"
        "v_mfma_f32_32x32x16_bf16 v[16:31], v[116:119], v[168:171], v[16:31]\n\t"
        "ds_read_b64_tr_b16 v[116:117], v122 offset:36864\n\t"
        "ds_read_b64_tr_b16 v[118:119], v122 offset:39936\n\t"
        "buffer_store_dwordx4 v[32:35], v213, s[40:43], 0 offen offset:0 sc1\n\t"
        "buffer_store_dwordx4 v[36:39], v213, s[40:43], 0 offen offset:1024 sc1\n\t"
        "v_mul_f32 v48, v212, v48\n\t"
        "s_waitcnt lgkmcnt(10)\n\t"
        "v_mfma_f32_32x32x16_bf16 v[0:15], v[96:99], v[172:175], v[0:15]\n\t"
        "ds_read_b64_tr_b16 v[96:97], v121 offset:43008\n\t"
        "ds_read_b64_tr_b16 v[98:99], v121 offset:46080\n\t"
        "v_mul_f32 v49, v212, v49\n\t"
        "v_mul_f32 v50, v212, v50\n\t"
        "v_mul_f32 v51, v212, v51\n\t"
        "s_waitcnt lgkmcnt(10)\n\t"
        "v_mfma_f32_32x32x16_bf16 v[16:31], v[100:103], v[172:175], v[16:31]\n\t"
        "ds_read_b64_tr_b16 v[100:101], v122 offset:43008\n\t"
        "ds_read_b64_tr_b16 v[102:103], v122 offset:46080\n\t"
        "v_mul_f32 v52, v212, v52\n\t"
        "v_mul_f32 v53, v212, v53\n\t"
        "v_mul_f32 v54, v212, v54\n\t"
        "s_waitcnt lgkmcnt(10)\n\t"
        "v_mfma_f32_32x32x16_bf16 v[0:15], v[104:107], v[176:179], v[0:15]\n\t"
        "ds_read_b64_tr_b16 v[104:105], v121 offset:49152\n\t"
        "ds_read_b64_tr_b16 v[106:107], v121 offset:52224\n\t"
        "v_mul_f32 v55, v212, v55\n\t"
        "v_cvt_pk_bf16_f32 v48, v48, v49\n\t"
        "v_cvt_pk_bf16_f32 v49, v50, v51\n\t"
        "s_waitcnt lgkmcnt(10)\n\t"
        "v_mfma_f32_32x32x16_bf16 v[16:31], v[108:111], v[176:179], v[16:31]\n\t"
        "ds_read_b64_tr_b16 v[108:109], v122 offset:49152\n\t"
        "ds_read_b64_tr_b16 v[110:111], v122 offset:52224\n\t"
        "v_cvt_pk_bf16_f32 v50, v52, v53\n\t"
        "v_cvt_pk_bf16_f32 v51, v54, v55\n\t"
        "v_mul_f32 v56, v212, v56\n\t"
        "s_waitcnt lgkmcnt(10)\n\t"
        "v_mfma_f32_32x32x16_bf16 v[0:15], v[112:115], v[180:183], v[0:15]\n\t"
        "ds_read_b64_tr_b16 v[112:113], v121 offset:55296\n\t"
        "ds_read_b64_tr_b16 v[114:115], v121 offset:58368\n\t"
        "v_mul_f32 v57, v212, v57\n\t"
        "v_mul_f32 v58, v212, v58\n\t"
        "v_mul_f32 v59, v212, v59\n\t"
        "s_waitcnt lgkmcnt(10)\n\t"
        "v_mfma_f32_32x32x16_bf16 v[16:31], v[116:119], v[180:183], v[16:31]\n\t"
        "ds_read_b64_tr_b16 v[116:117], v122 offset:55296\n\t"
        "ds_read_b64_tr_b16 v[118:119], v122 offset:58368\n\t"
        "v_mul_f32 v60, v212, v60\n\t"
        "v_mul_f32 v61, v212, v61\n\t"
        "v_mul_f32 v62, v212, v62\n\t"
        "s_waitcnt lgkmcnt(10)\n\t"
        "v_mfma_f32_32x32x16_bf16 v[0:15], v[96:99], v[184:187], v[0:15]\n\t"
        "v_mul_f32 v63, v212, v63\n\t"
        "v_cvt_pk_bf16_f32 v52, v56, v57\n\t"
        "v_cvt_pk_bf16_f32 v53, v58, v59\n\t"
        "v_cvt_pk_bf16_f32 v54, v60, v61\n\t"
        "v_cvt_pk_bf16_f32 v55, v62, v63\n\t"
        "s_waitcnt lgkmcnt(8)\n\t"
        "v_mfma_f32_32x32x16_bf16 v[16:31], v[100:103], v[184:187], v[16:31]\n\t"
        "buffer_store_dwordx4 v[48:51], v214, s[40:43], 0 offen offset:0 sc1\n\t"
        "buffer_store_dwordx4 v[52:55], v214, s[40:43], 0 offen offset:1024 sc1\n\t"
        "s_waitcnt lgkmcnt(6)\n\t"
        "v_mfma_f32_32x32x16_bf16 v[0:15], v[104:107], v[188:191], v[0:15]\n\t"
        "s_waitcnt lgkmcnt(4)\n\t"
        "v_mfma_f32_32x32x16_bf16 v[16:31], v[108:111], v[188:191], v[16:31]\n\t"
        "s_waitcnt lgkmcnt(2)\n\t"
        "v_mfma_f32_32x32x16_bf16 v[0:15], v[112:115], v[192:195], v[0:15]\n\t"
        "s_waitcnt lgkmcnt(0)\n\t"
        "v_mfma_f32_32x32x16_bf16 v[16:31], v[116:119], v[192:195], v[16:31]\n\t"
        "s_nop 11\n\t"
        "s_branch 9f\n\t"
        "16:\n\t"
        "s_mov_b32 s48, 0x10000\n\t"
        "8:\n\t"
        "ds_read_b32 v126, v125\n\t"
        "s_sub_u32 s48, s48, 1\n\t"
        "s_waitcnt lgkmcnt(0)\n\t"
        "s_cbranch_scc1 17b\n\t"
        "v_cmp_gt_u32 vcc, s47, v126\n\t"
        "s_cbranch_vccnz 8b\n\t"
        "s_branch 17b\n\t"
        "18:\n\t"
        "s_mov_b32 s48, 0x10000\n\t"
        "8:\n\t"
        "ds_read_b32 v126, v125\n\t"
        "s_sub_u32 s48, s48, 1\n\t"
        "s_waitcnt lgkmcnt(0)\n\t"
        "s_cbranch_scc1 19b\n\t"
        "v_cmp_gt_u32 vcc, s47, v126\n\t"
        "s_cbranch_vccnz 8b\n\t"
        "s_branch 19b\n\t"
        "20:\n\t"
        "s_mov_b32 s48, 0x10000\n\t"
        "8:\n\t"
        "ds_read_b32 v126, v125\n\t"
        "s_sub_u32 s48, s48, 1\n\t"
        "s_waitcnt lgkmcnt(0)\n\t"
        "s_cbranch_scc1 21b\n\t"
        "v_cmp_gt_u32 vcc, s47, v126\n\t"
        "s_cbranch_vccnz 8b\n\t"
        "s_branch 21b\n\t"
        "22:\n\t"
        "s_mov_b32 s48, 0x10000\n\t"
        "8:\n\t"
        "ds_read_b32 v126, v125\n\t"
        "s_sub_u32 s48, s48, 1\n\t"
        "s_waitcnt lgkmcnt(0)\n\t"
        "s_cbranch_scc1 23b\n\t"
        "v_cmp_gt_u32 vcc, s47, v126\n\t"
        "s_cbranch_vccnz 8b\n\t"
        "s_branch 23b\n\t"
        "24:\n\t"
        "s_mov_b32 s48, 0x10000\n\t"
        "8:\n\t"
        "ds_read_b32 v126, v125\n\t"
        "s_sub_u32 s48, s48, 1\n\t"
        "s_waitcnt lgkmcnt(0)\n\t"
        "s_cbranch_scc1 25b\n\t"
        "v_cmp_gt_u32 vcc, s47, v126\n\t"
        "s_cbranch_vccnz 8b\n\t"
        "s_branch 25b\n\t"
        "26:\n\t"
        "s_mov_b32 s48, 0x10000\n\t"
        "8:\n\t"
        "ds_read_b32 v126, v125\n\t"
        "s_sub_u32 s48, s48, 1\n\t"
        "s_waitcnt lgkmcnt(0)\n\t"
        "s_cbranch_scc1 27b\n\t"
        "v_cmp_gt_u32 vcc, s47, v126\n\t"
        "s_cbranch_vccnz 8b\n\t"
        "s_branch 27b\n\t"
        "28:\n\t"
        "s_mov_b32 s48, 0x10000\n\t"
        "8:\n\t"
        "ds_read_b32 v126, v125\n\t"
        "s_sub_u32 s48, s48, 1\n\t"
        "s_waitcnt lgkmcnt(0)\n\t"
        "s_cbranch_scc1 29b\n\t"
        "v_cmp_gt_u32 vcc, s47, v126\n\t"
        "s_cbranch_vccnz 8b\n\t"
        "s_branch 29b\n\t"
        "30:\n\t"
        "s_mov_b32 s48, 0x10000\n\t"
        "8:\n\t"
        "ds_read_b32 v126, v125\n\t"
        "s_sub_u32 s48, s48, 1\n\t"
        "s_waitcnt lgkmcnt(0)\n\t"
        "s_cbranch_scc1 31b\n\t"
        "v_cmp_gt_u32 vcc, s47, v126\n\t"
        "s_cbranch_vccnz 8b\n\t"
        "s_branch 31b\n\t"
        "74:\n\t"
        "s_mov_b32 s48, 0x10000\n\t"
        "8:\n\t"
        "ds_read_b32 v126, v125\n\t"
        "s_sub_u32 s48, s48, 1\n\t"
        "s_waitcnt lgkmcnt(0)\n\t"
        "s_cbranch_scc1 75b\n\t"
        "v_cmp_gt_u32 vcc, s47, v126\n\t"
        "s_cbranch_vccnz 8b\n\t"
        "s_branch 75b\n\t"
        "9:\n\t"
        : "+{v[0:15]}"(acc[0]), "+{v[16:31]}"(acc[1]), "+{v[32:47]}"(acc[2]), "+{v[48:63]}"(acc[3]), "+{v[64:79]}"(acc[4]), "+{v[80:95]}"(acc[5]),
          "+{v[96:111]}"(stA), "+{v[112:127]}"(stB), "+{v[128:131]}"(stC), "+{v[148:163]}"(xr1), "+{v[164:179]}"(xr2), "+{v[180:195]}"(xr3),
          "+{s44}"(soff), "+{s46}"(slot), "+{s47}"(need)
        : "{s[40:43]}"(srd), "{v[212:215]}"(ep) : "v196", "v197", "v198", "v199", "v200", "v201", "v202", "v203", "v204", "v205", "v206", "v207", "v208", "v209", "v210", "v211", "v132", "v133", "v134", "v135", "v136", "v137", "v138", "v139", "v140", "v141", "v142", "v143", "v144", "v145", "v146", "v147", "s45", "s48", "vcc", "scc", "memory");
      STAMPV(4 + 16 * L);
      PHASE_VARS;
      u32x4 kv[4][2];
#pragma unroll
      for (int j = 0; j < 4; ++j)
#pragma unroll
        for (int e = 0; e < 8; ++e) kv[j][e >> 2][e & 3] = __float_as_uint(acc[2 + j][e]);
      constexpr float QS = 0.17677669529663687f * 1.4426950408889634f;
      u32x4 qo[2][2];
      auto drained = [&]() { asm volatile("s_waitcnt vmcnt(0)\n\tds_write_b32 %0, %1" :: "v"(lbase + L_W + 80 + 4 * wid), "v"(L + 1) : "memory"); };
      if (L == 0) {
#pragma unroll
        for (int e = 0; e < 16; ++e) {
          if (e == 8) { SBAR(); drained(); SBAR(); }
          const unsigned w = pack2(acc[e >> 3][2 * (e & 7)], acc[e >> 3][2 * (e & 7) + 1]);
          qo[e >> 3][(e >> 2) & 1][e & 3] = pack2(lo16(w) * QS, hi16(w) * QS);
        }
      } else {
        const float aq = U2F(ep[0]) * QS;
#pragma unroll
        for (int e = 0; e < 16; ++e) qo[e >> 3][(e >> 2) & 1][e & 3] = pack2(acc[e >> 3][2 * (e & 7)] * aq, acc[e >> 3][2 * (e & 7) + 1] * aq);
        SBAR();
        lds_wait_ge<0>(lbase + L_W + 4 + 4 * (lane & 1), 1);
        const int idx = (int)__builtin_amdgcn_readfirstlane(words[3]);
        if ((idx >> 5) == tile) {
          f32x4 cs[4][4];
          float cv[2];
#pragma unroll
          for (int j = 0; j < 6; ++j) {
            const int cb = (j >> 1) * 64 + ((j & 1) ^ hh) * 32;
            if (j >= 4) cv[j - 4] = *reinterpret_cast<const float*>(smem + L_PATCH + (cb + r32) * 4);
            else {
#pragma unroll
              for (int g = 0; g < 4; ++g) cs[j][g] = *reinterpret_cast<const f32x4*>(smem + L_PATCH + (cb + 8 * g + 4 * hi) * 4);
            }
          }
          SBAR();
          const bool me = r32 == (idx & 31);
          const int rel = (idx & 31) - 8 * hi;
#pragma unroll
          for (int j = 0; j < 2; ++j) {
            const unsigned pv = pack2(cv[j], cv[j]);
#pragma unroll
            for (int e = 0; e < 8; ++e) {
              unsigned a = pack2(cs[j][e >> 1][2 * (e & 1)] * QS, cs[j][e >> 1][2 * (e & 1) + 1] * QS), b = pack2(cs[2 + j][e >> 1][2 * (e & 1)], cs[2 + j][e >> 1][2 * (e & 1) + 1]);
              PIN(a); PIN(b);
              qo[j][e >> 2][e & 3] = me ? a : qo[j][e >> 2][e & 3];
              kv[j][e >> 2][e & 3] = me ? b : kv[j][e >> 2][e & 3];
              const int t0 = 16 * (e >> 2) + 2 * (e & 3);
              const unsigned mask = rel == t0 ? 0xffffu : rel == t0 + 1 ? 0xffff0000u : 0u;
              kv[2 + j][e >> 2][e & 3] = (kv[2 + j][e >> 2][e & 3] & ~mask) | (pv & mask);
            }
          }
          DRAIN();
#pragma unroll
          for (int j = 0; j < 4; ++j) { stg(rs, ep[1 + (j & 1)] + (j >> 1) * 2048, kv[j][0]); stg(rs, ep[1 + (j & 1)] + (j >> 1) * 2048 + 1024, kv[j][1]); }
        }
        drained();
      }
      SBAR();
#pragma unroll
      for (int j = 0; j < 2; ++j) {
        char* p = smem + L_XCH + ((j ^ hh) * 4 + wid) * 2048 + lane * 16;
        *reinterpret_cast<u32x4*>(p) = qo[j][0]; *reinterpret_cast<u32x4*>(p + 1024) = qo[j][1];
        p = smem + L_XCH + 16384 + wid * 4096 + j * 2048 + lane * 16;
        *reinterpret_cast<u32x4*>(p) = kv[2 * j + 1][0]; *reinterpret_cast<u32x4*>(p + 1024) = kv[2 * j + 1][1];
      }
      STAMPV(41 + 4 * L);
      BAR_LGKM();
      STAMPV(43 + 4 * L);
      kva = cat4(kv[0][0], kv[0][1], kv[2][0], kv[2][1]);
      const u32x4* pk = reinterpret_cast<const u32x4*>(smem + L_XCH + 16384 + (wid ^ 1) * 4096 + lane * 16);
#pragma unroll
      for (int u = 0; u < 4; ++u) {
        const u32x4* p = reinterpret_cast<const u32x4*>(smem + L_XCH + (hh * 4 + ((2 * kh + 2 + u) & 3)) * 2048 + lane * 16);
        q[u] = __builtin_shufflevector(p[0], p[64], 0, 1, 2, 3, 4, 5, 6, 7);
      }
      kvb = cat4(pk[0], pk[64], pk[128], pk[192]);
      SBAR();
      asm volatile(
        "v_mov_b64 v[0:1], 0\n\t"
        "v_mov_b64 v[2:3], 0\n\t"
        "v_mov_b64 v[4:5], 0\n\t"
        "v_mov_b64 v[6:7], 0\n\t"
        "v_mov_b64 v[8:9], 0\n\t"
        "v_mov_b64 v[10:11], 0\n\t"
        "v_mov_b64 v[12:13], 0\n\t"
        "v_mov_b64 v[14:15], 0\n\t"
        "v_mov_b64 v[16:17], 0\n\t"
        "v_mov_b64 v[18:19], 0\n\t"
        "v_mov_b64 v[20:21], 0\n\t"
        "v_mov_b64 v[22:23], 0\n\t"
        "v_mov_b64 v[24:25], 0\n\t"
        "v_mov_b64 v[26:27], 0\n\t"
        "v_mov_b64 v[28:29], 0\n\t"
        "v_mov_b64 v[30:31], 0\n\t"
        "v_mov_b64 v[32:33], 0\n\t"
        "v_mov_b64 v[34:35], 0\n\t"
        "v_mov_b64 v[36:37], 0\n\t"
        "v_mov_b64 v[38:39], 0\n\t"
        "v_mov_b64 v[40:41], 0\n\t"
        "v_mov_b64 v[42:43], 0\n\t"
        "v_mov_b64 v[44:45], 0\n\t"
        "v_mov_b64 v[46:47], 0\n\t"
        "v_mov_b64 v[48:49], 0\n\t"
        "v_mov_b64 v[50:51], 0\n\t"
        "v_mov_b64 v[52:53], 0\n\t"
        "v_mov_b64 v[54:55], 0\n\t"
        "v_mov_b64 v[56:57], 0\n\t"
        "v_mov_b64 v[58:59], 0\n\t"
        "v_mov_b64 v[60:61], 0\n\t"
        "v_mov_b64 v[62:63], 0\n\t"
        "v_mov_b64 v[128:129], 0\n\t"
        "v_mov_b64 v[130:131], 0\n\t"
        "v_mov_b64 v[132:133], 0\n\t"
        "v_mov_b64 v[134:135], 0\n\t"
        "v_mov_b64 v[136:137], 0\n\t"
        "v_mov_b64 v[138:139], 0\n\t"
        "v_mov_b64 v[140:141], 0\n\t"
        "v_mov_b64 v[142:143], 0\n\t"
        : "={v[0:15]}"(oz[0]), "={v[16:31]}"(oz[1]), "={v[32:47]}"(oz[2]), "={v[48:63]}"(oz[3]), "={v[128:143]}"(oz[4]));
    }
    STAMPV(5 + 16 * L);
    {
      u32x4 ones;
      unsigned kvoff;
      u32x8 fv, cv;
      asm volatile("" : "={v[184:191]}"(fv), "={v[216:223]}"(cv));
      { PHASE_VARS;
        const unsigned v = (lane & 3) == 0 ? 0x3F803F80u : 0u; const bool up = (lane & 4) != 0; ones[0] = ones[1] = up ? 0u : v; ones[2] = ones[3] = up ? v : 0u;
        kvoff = OFF_KV + L * (10 * MBY) + (tn * 2 + hh) * HS + kh * 8192 + lane * 16;
        fv[1] = lbase + L_FREE + wid * 256 + lane * 4; fv[2] = 32 * L + 32;
        cv[0] = lbase + L_XCH + wid * 8192 + lane * 16; cv[1] = lbase + L_XCH + (wid ^ 2) * 8192 + lane * 16;
        cv[2] = lbase + L_XCH + 32768 + wid * 512 + lane * 4; cv[3] = lbase + L_XCH + 32768 + (wid ^ 2) * 512 + lane * 4;
        cv[4] = lbase + L_W + 48 + wid * 4; cv[5] = lbase + L_W + 48 + (wid ^ 2) * 4; cv[6] = L + 1;
        cv[7] = ((tile * 32 + r32) * D_ + tn * 64 + hi * 8) * 2; }
      f32x16 o0 = oz[0], o1 = oz[1], o2 = oz[2], o3 = oz[3], lv = oz[4];
      unsigned soff = tm * 16384;
      STAMP_DECL;
      asm volatile(
        "s_nop 1\n\t"
        "v_mfma_f32_32x32x16_bf16 v[96:111], v[152:155], v[64:67], 0\n\t"
        "v_mfma_f32_32x32x16_bf16 v[96:111], v[156:159], v[68:71], v[96:111]\n\t"
        "s_mov_b32 s46, 0\n\t"
        "ds_write_b32 v185, v186\n\t"
        "s_nop 9\n\t"
        "v_exp_f32 v96, v96\n\t"
        "v_exp_f32 v97, v97\n\t"
        "v_exp_f32 v98, v98\n\t"
        "v_exp_f32 v99, v99\n\t"
        "v_exp_f32 v100, v100\n\t"
        "v_exp_f32 v101, v101\n\t"
        "v_mfma_f32_32x32x16_bf16 v[112:127], v[152:155], v[72:75], 0\n\t"
        "v_exp_f32 v102, v102\n\t"
        "v_exp_f32 v103, v103\n\t"
        "v_exp_f32 v104, v104\n\t"
        "v_mfma_f32_32x32x16_bf16 v[112:127], v[156:159], v[76:79], v[112:127]\n\t"
        "v_exp_f32 v105, v105\n\t"
        "v_exp_f32 v106, v106\n\t"
        "v_exp_f32 v107, v107\n\t"
        "v_smfmac_f32_32x32x32_bf16 v[0:15], v[160:163], v[96:103], v148\n\t"
        "v_exp_f32 v108, v108\n\t"
        "v_exp_f32 v109, v109\n\t"
        "v_exp_f32 v110, v110\n\t"
        "v_smfmac_f32_16x16x64_bf16 v[128:131], v[144:147], v[96:103], v148\n\t"
        "v_exp_f32 v111, v111\n\t"
        "v_exp_f32 v112, v112\n\t"
        "v_exp_f32 v113, v113\n\t"
        "v_smfmac_f32_32x32x32_bf16 v[0:15], v[164:167], v[104:111], v148\n\t"
        "v_exp_f32 v114, v114\n\t"
        "v_exp_f32 v115, v115\n\t"
        "v_exp_f32 v116, v116\n\t"
        "v_smfmac_f32_16x16x64_bf16 v[128:131], v[144:147], v[104:111], v148\n\t"
        "v_exp_f32 v117, v117\n\t"
        "v_mfma_f32_32x32x16_bf16 v[96:111], v[152:155], v[80:83], 0\n\t"
        "v_exp_f32 v118, v118\n\t"
        "v_exp_f32 v119, v119\n\t"
        "v_exp_f32 v120, v120\n\t"
        "ds_read_b32 v151, v224\n\t"
        "v_mfma_f32_32x32x16_bf16 v[96:111], v[156:159], v[84:87], v[96:111]\n\t"
        "v_exp_f32 v121, v121\n\t"
        "v_exp_f32 v122, v122\n\t"
        "v_exp_f32 v123, v123\n\t"
        "v_smfmac_f32_32x32x32_bf16 v[16:31], v[160:163], v[112:119], v148\n\t"
        "v_exp_f32 v124, v124\n\t"
        "v_exp_f32 v125, v125\n\t"
        "v_exp_f32 v126, v126\n\t"
        "v_smfmac_f32_16x16x64_bf16 v[132:135], v[144:147], v[112:119], v148\n\t"
        "v_exp_f32 v127, v127\n\t"
        "s_cmp_lg_u32 s46, 0\n\t"
        "s_cbranch_scc1 25f\n\t"
        "s_waitcnt lgkmcnt(0)\n\t"
        "v_cmp_ne_u32 vcc, v222, v151\n\t"
        "s_cbranch_vccz 24f\n\t"
        "25:\n\t"
        "v_exp_f32 v96, v96\n\t"
        "v_exp_f32 v97, v97\n\t"
        "v_smfmac_f32_32x32x32_bf16 v[16:31], v[164:167], v[120:127], v148\n\t"
        "v_exp_f32 v98, v98\n\t"
        "v_exp_f32 v99, v99\n\t"
        "v_exp_f32 v100, v100\n\t"
        "v_smfmac_f32_16x16x64_bf16 v[132:135], v[144:147], v[120:127], v148\n\t"
        "v_exp_f32 v101, v101\n\t"
        "v_mfma_f32_32x32x16_bf16 v[112:127], v[152:155], v[88:91], 0\n\t"
        "v_exp_f32 v102, v102\n\t"
        "v_exp_f32 v103, v103\n\t"
        "v_exp_f32 v104, v104\n\t"
        "ds_read_b32 v151, v224\n\t"
        "v_mfma_f32_32x32x16_bf16 v[112:127], v[156:159], v[92:95], v[112:127]\n\t"
        "v_exp_f32 v105, v105\n\t"
        "v_exp_f32 v106, v106\n\t"
        "v_exp_f32 v107, v107\n\t"
        "v_smfmac_f32_32x32x32_bf16 v[32:47], v[160:163], v[96:103], v148\n\t"
        "v_exp_f32 v108, v108\n\t"
        "v_exp_f32 v109, v109\n\t"
        "v_exp_f32 v110, v110\n\t"
        "v_smfmac_f32_16x16x64_bf16 v[136:139], v[144:147], v[96:103], v148\n\t"
        "v_exp_f32 v111, v111\n\t"
        "s_cmp_lg_u32 s46, 0\n\t"
        "s_cbranch_scc1 27f\n\t"
        "s_waitcnt lgkmcnt(0)\n\t"
        "v_cmp_ne_u32 vcc, v222, v151\n\t"
        "s_cbranch_vccz 26f\n\t"
        "27:\n\t"
        "v_exp_f32 v112, v112\n\t"
        "v_exp_f32 v113, v113\n\t"
        "v_smfmac_f32_32x32x32_bf16 v[32:47], v[164:167], v[104:111], v148\n\t"
        "v_exp_f32 v114, v114\n\t"
        "v_exp_f32 v115, v115\n\t"
        "v_exp_f32 v116, v116\n\t"
        "v_smfmac_f32_16x16x64_bf16 v[136:139], v[144:147], v[104:111], v148\n\t"
        "v_exp_f32 v117, v117\n\t"
        "v_mfma_f32_32x32x16_bf16 v[96:111], v[168:171], v[64:67], 0\n\t"
        "v_exp_f32 v118, v118\n\t"
        "v_exp_f32 v119, v119\n\t"
        "v_exp_f32 v120, v120\n\t"
        "ds_read_b32 v151, v224\n\t"
        "v_mfma_f32_32x32x16_bf16 v[96:111], v[172:175], v[68:71], v[96:111]\n\t"
        "v_exp_f32 v121, v121\n\t"
        "v_exp_f32 v122, v122\n\t"
        "v_exp_f32 v123, v123\n\t"
        "v_smfmac_f32_32x32x32_bf16 v[48:63], v[160:163], v[112:119], v148\n\t"
        "v_exp_f32 v124, v124\n\t"
        "v_exp_f32 v125, v125\n\t"
        "v_exp_f32 v126, v126\n\t"
        "v_smfmac_f32_16x16x64_bf16 v[140:143], v[144:147], v[112:119], v148\n\t"
        "v_exp_f32 v127, v127\n\t"
        "s_cmp_lg_u32 s46, 0\n\t"
        "s_cbranch_scc1 29f\n\t"
        "s_waitcnt lgkmcnt(0)\n\t"
        "v_cmp_ne_u32 vcc, v222, v151\n\t"
        "s_cbranch_vccz 28f\n\t"
        "29:\n\t"
        "v_exp_f32 v96, v96\n\t"
        "v_exp_f32 v97, v97\n\t"
        "v_smfmac_f32_32x32x32_bf16 v[48:63], v[164:167], v[120:127], v148\n\t"
        "v_exp_f32 v98, v98\n\t"
        "v_exp_f32 v99, v99\n\t"
        "v_exp_f32 v100, v100\n\t"
        "v_smfmac_f32_16x16x64_bf16 v[140:143], v[144:147], v[120:127], v148\n\t"
        "v_exp_f32 v101, v101\n\t"
        "v_mfma_f32_32x32x16_bf16 v[112:127], v[168:171], v[72:75], 0\n\t"
        "v_exp_f32 v102, v102\n\t"
        "v_exp_f32 v103, v103\n\t"
        "v_exp_f32 v104, v104\n\t"
        "ds_read_b32 v151, v224\n\t"
        "v_mfma_f32_32x32x16_bf16 v[112:127], v[172:175], v[76:79], v[112:127]\n\t"
        "v_exp_f32 v105, v105\n\t"
        "v_exp_f32 v106, v106\n\t"
        "v_exp_f32 v107, v107\n\t"
        "v_smfmac_f32_32x32x32_bf16 v[0:15], v[176:179], v[96:103], v148\n\t"
        "v_exp_f32 v108, v108\n\t"
        "v_exp_f32 v109, v109\n\t"
        "v_exp_f32 v110, v110\n\t"
        "v_smfmac_f32_16x16x64_bf16 v[128:131], v[144:147], v[96:103], v148\n\t"
        "v_exp_f32 v111, v111\n\t"
        "s_cmp_lg_u32 s46, 0\n\t"
        "s_cbranch_scc1 31f\n\t"
        "s_waitcnt lgkmcnt(0)\n\t"
        "v_cmp_ne_u32 vcc, v222, v151\n\t"
        "s_cbranch_vccz 30f\n\t"
        "31:\n\t"
        "v_exp_f32 v112, v112\n\t"
        "v_exp_f32 v113, v113\n\t"
        "v_smfmac_f32_32x32x32_bf16 v[0:15], v[180:183], v[104:111], v148\n\t"
        "v_exp_f32 v114, v114\n\t"
        "v_exp_f32 v115, v115\n\t"
        "v_exp_f32 v116, v116\n\t"
        "v_smfmac_f32_16x16x64_bf16 v[128:131], v[144:147], v[104:111], v148\n\t"
        "v_exp_f32 v117, v117\n\t"
        "v_mfma_f32_32x32x16_bf16 v[96:111], v[168:171], v[80:83], 0\n\t"
        "v_exp_f32 v118, v118\n\t"
        "v_exp_f32 v119, v119\n\t"
        "v_exp_f32 v120, v120\n\t"
        "ds_read_b32 v151, v224\n\t"
        "v_mfma_f32_32x32x16_bf16 v[96:111], v[172:175], v[84:87], v[96:111]\n\t"
        "v_exp_f32 v121, v121\n\t"
        "v_exp_f32 v122, v122\n\t"
        "v_exp_f32 v123, v123\n\t"
        "v_smfmac_f32_32x32x32_bf16 v[16:31], v[176:179], v[112:119], v148\n\t"
        "v_exp_f32 v124, v124\n\t"
        "v_exp_f32 v125, v125\n\t"
        "v_exp_f32 v126, v126\n\t"
        "v_smfmac_f32_16x16x64_bf16 v[132:135], v[144:147], v[112:119], v148\n\t"
        "v_exp_f32 v127, v127\n\t"
        "s_cmp_lg_u32 s46, 0\n\t"
        "s_cbranch_scc1 33f\n\t"
        "s_waitcnt lgkmcnt(0)\n\t"
        "v_cmp_ne_u32 vcc, v222, v151\n\t"
        "s_cbranch_vccz 32f\n\t"
        "33:\n\t"
        "v_exp_f32 v96, v96\n\t"
        "v_exp_f32 v97, v97\n\t"
        "v_smfmac_f32_32x32x32_bf16 v[16:31], v[180:183], v[120:127], v148\n\t"
        "v_exp_f32 v98, v98\n\t"
        "v_exp_f32 v99, v99\n\t"
        "v_exp_f32 v100, v100\n\t"
        "v_smfmac_f32_16x16x64_bf16 v[132:135], v[144:147], v[120:127], v148\n\t"
        "v_exp_f32 v101, v101\n\t"
        "v_mfma_f32_32x32x16_bf16 v[112:127], v[168:171], v[88:91], 0\n\t"
        "v_exp_f32 v102, v102\n\t"
        "v_exp_f32 v103, v103\n\t"
        "v_exp_f32 v104, v104\n\t"
        "v_mfma_f32_32x32x16_bf16 v[112:127], v[172:175], v[92:95], v[112:127]\n\t"
        "v_exp_f32 v105, v105\n\t"
        "v_exp_f32 v106, v106\n\t"
        "v_exp_f32 v107, v107\n\t"
        "v_smfmac_f32_32x32x32_bf16 v[32:47], v[176:179], v[96:103], v148\n\t"
        "v_exp_f32 v108, v108\n\t"
        "v_exp_f32 v109, v109\n\t"
        "v_exp_f32 v110, v110\n\t"
        "v_smfmac_f32_16x16x64_bf16 v[136:139], v[144:147], v[96:103], v148\n\t"
        "v_exp_f32 v111, v111\n\t"
        "s_cmp_lg_u32 s46, 0\n\t"
        "s_cbranch_scc1 5f\n\t"
        "s_mov_b32 s45, 0x200000\n\t"
        "2:\n\t"
        "ds_read_b32 v151, v224\n\t"
        "s_sub_u32 s45, s45, 1\n\t"
        "s_waitcnt lgkmcnt(0)\n\t"
        "s_cbranch_scc1 3f\n\t"
        "v_cmp_ne_u32 vcc, v222, v151\n\t"
        "s_cbranch_vccnz 2b\n\t"
        "3:\n\t"
        "s_add_u32 s44, s44, 0x4000\n\t"
        "s_and_b32 s44, s44, 0x3ffff\n\t"
        "buffer_load_dwordx4 v[184:187], v149, s[40:43], s44 offen offset:0 sc1\n\t"
        "buffer_load_dwordx4 v[188:191], v149, s[40:43], s44 offen offset:1024 sc1\n\t"
        "buffer_load_dwordx4 v[192:195], v149, s[40:43], s44 offen offset:2048 sc1\n\t"
        "buffer_load_dwordx4 v[196:199], v149, s[40:43], s44 offen offset:3072 sc1\n\t"
        "buffer_load_dwordx4 v[200:203], v150, s[40:43], s44 offen offset:0 sc1\n\t"
        "buffer_load_dwordx4 v[204:207], v150, s[40:43], s44 offen offset:1024 sc1\n\t"
        "buffer_load_dwordx4 v[208:211], v150, s[40:43], s44 offen offset:2048 sc1\n\t"
        "buffer_load_dwordx4 v[212:215], v150, s[40:43], s44 offen offset:3072 sc1\n\t"
        "5:\n\t"
        "s_add_u32 s44, s44, 0x4000\n\t"
        "s_and_b32 s44, s44, 0x3ffff\n\t"
        "buffer_load_dwordx4 v[152:155], v149, s[40:43], s44 offen offset:0 sc1\n\t"
        "buffer_load_dwordx4 v[156:159], v149, s[40:43], s44 offen offset:1024 sc1\n\t"
        "buffer_load_dwordx4 v[160:163], v149, s[40:43], s44 offen offset:2048 sc1\n\t"
        "buffer_load_dwordx4 v[164:167], v149, s[40:43], s44 offen offset:3072 sc1\n\t"
        "s_mov_b32 s45, 7\n\t"
        "1:\n\t"
        "v_exp_f32 v112, v112\n\t"
        "v_exp_f32 v113, v113\n\t"
        "v_smfmac_f32_32x32x32_bf16 v[32:47], v[180:183], v[104:111], v148\n\t"
        "v_exp_f32 v114, v114\n\t"
        "v_exp_f32 v115, v115\n\t"
        "v_exp_f32 v116, v116\n\t"
        "v_smfmac_f32_16x16x64_bf16 v[136:139], v[144:147], v[104:111], v148\n\t"
        "v_exp_f32 v117, v117\n\t"
        "s_waitcnt vmcnt(8)\n\t"
        "v_mfma_f32_32x32x16_bf16 v[96:111], v[184:187], v[64:67], 0\n\t"
        "v_exp_f32 v118, v118\n\t"
        "v_exp_f32 v119, v119\n\t"
        "v_exp_f32 v120, v120\n\t"
        "v_mfma_f32_32x32x16_bf16 v[96:111], v[188:191], v[68:71], v[96:111]\n\t"
        "v_exp_f32 v121, v121\n\t"
        "v_exp_f32 v122, v122\n\t"
        "v_exp_f32 v123, v123\n\t"
        "v_smfmac_f32_32x32x32_bf16 v[48:63], v[176:179], v[112:119], v148\n\t"
        "v_exp_f32 v124, v124\n\t"
        "v_exp_f32 v125, v125\n\t"
        "v_exp_f32 v126, v126\n\t"
        "v_smfmac_f32_16x16x64_bf16 v[140:143], v[144:147], v[112:119], v148\n\t"
        "buffer_load_dwordx4 v[168:171], v150, s[40:43], s44 offen offset:0 sc1\n\t"
        "v_exp_f32 v127, v127\n\t"
        "v_exp_f32 v96, v96\n\t"
        "v_exp_f32 v97, v97\n\t"
        "v_smfmac_f32_32x32x32_bf16 v[48:63], v[180:183], v[120:127], v148\n\t"
        "v_exp_f32 v98, v98\n\t"
        "v_exp_f32 v99, v99\n\t"
        "v_exp_f32 v100, v100\n\t"
        "v_smfmac_f32_16x16x64_bf16 v[140:143], v[144:147], v[120:127], v148\n\t"
        "v_exp_f32 v101, v101\n\t"
        "v_mfma_f32_32x32x16_bf16 v[112:127], v[184:187], v[72:75], 0\n\t"
        "v_exp_f32 v102, v102\n\t"
        "v_exp_f32 v103, v103\n\t"
        "v_exp_f32 v104, v104\n\t"
        "v_mfma_f32_32x32x16_bf16 v[112:127], v[188:191], v[76:79], v[112:127]\n\t"
        "v_exp_f32 v105, v105\n\t"
        "v_exp_f32 v106, v106\n\t"
        "v_exp_f32 v107, v107\n\t"
        "v_smfmac_f32_32x32x32_bf16 v[0:15], v[192:195], v[96:103], v148\n\t"
        "v_exp_f32 v108, v108\n\t"
        "v_exp_f32 v109, v109\n\t"
        "v_exp_f32 v110, v110\n\t"
        "v_smfmac_f32_16x16x64_bf16 v[128:131], v[144:147], v[96:103], v148\n\t"
        "buffer_load_dwordx4 v[172:175], v150, s[40:43], s44 offen offset:1024 sc1\n\t"
        "v_exp_f32 v111, v111\n\t"
        "v_exp_f32 v112, v112\n\t"
        "v_exp_f32 v113, v113\n\t"
        "v_smfmac_f32_32x32x32_bf16 v[0:15], v[196:199], v[104:111], v148\n\t"
        "v_exp_f32 v114, v114\n\t"
        "v_exp_f32 v115, v115\n\t"
        "v_exp_f32 v116, v116\n\t"
        "v_smfmac_f32_16x16x64_bf16 v[128:131], v[144:147], v[104:111], v148\n\t"
        "v_exp_f32 v117, v117\n\t"
        "v_mfma_f32_32x32x16_bf16 v[96:111], v[184:187], v[80:83], 0\n\t"
        "v_exp_f32 v118, v118\n\t"
        "v_exp_f32 v119, v119\n\t"
        "v_exp_f32 v120, v120\n\t"
        "v_mfma_f32_32x32x16_bf16 v[96:111], v[188:191], v[84:87], v[96:111]\n\t"
        "v_exp_f32 v121, v121\n\t"
        "v_exp_f32 v122, v122\n\t"
        "v_exp_f32 v123, v123\n\t"
        "v_smfmac_f32_32x32x32_bf16 v[16:31], v[192:195], v[112:119], v148\n\t"
        "v_exp_f32 v124, v124\n\t"
        "v_exp_f32 v125, v125\n\t"
        "v_exp_f32 v126, v126\n\t"
        "v_smfmac_f32_16x16x64_bf16 v[132:135], v[144:147], v[112:119], v148\n\t"
        "buffer_load_dwordx4 v[176:179], v150, s[40:43], s44 offen offset:2048 sc1\n\t"
        "v_exp_f32 v127, v127\n\t"
        "v_exp_f32 v96, v96\n\t"
        "v_exp_f32 v97, v97\n\t"
        "v_smfmac_f32_32x32x32_bf16 v[16:31], v[196:199], v[120:127], v148\n\t"
        "v_exp_f32 v98, v98\n\t"
        "v_exp_f32 v99, v99\n\t"
        "v_exp_f32 v100, v100\n\t"
        "v_smfmac_f32_16x16x64_bf16 v[132:135], v[144:147], v[120:127], v148\n\t"
        "v_exp_f32 v101, v101\n\t"
        "v_mfma_f32_32x32x16_bf16 v[112:127], v[184:187], v[88:91], 0\n\t"
        "v_exp_f32 v102, v102\n\t"
        "v_exp_f32 v103, v103\n\t"
        "v_exp_f32 v104, v104\n\t"
        "v_mfma_f32_32x32x16_bf16 v[112:127], v[188:191], v[92:95], v[112:127]\n\t"
        "v_exp_f32 v105, v105\n\t"
        "v_exp_f32 v106, v106\n\t"
        "v_exp_f32 v107, v107\n\t"
        "v_smfmac_f32_32x32x32_bf16 v[32:47], v[192:195], v[96:103], v148\n\t"
        "v_exp_f32 v108, v108\n\t"
        "v_exp_f32 v109, v109\n\t"
        "v_exp_f32 v110, v110\n\t"
        "v_smfmac_f32_16x16x64_bf16 v[136:139], v[144:147], v[96:103], v148\n\t"
        "buffer_load_dwordx4 v[180:183], v150, s[40:43], s44 offen offset:3072 sc1\n\t"
        "v_exp_f32 v111, v111\n\t"
        "v_exp_f32 v112, v112\n\t"
        "v_exp_f32 v113, v113\n\t"
        "v_smfmac_f32_32x32x32_bf16 v[32:47], v[196:199], v[104:111], v148\n\t"
        "s_add_u32 s44, s44, 0x4000\n\t"
        "s_and_b32 s44, s44, 0x3ffff\n\t"
        "v_exp_f32 v114, v114\n\t"
        "v_exp_f32 v115, v115\n\t"
        "v_exp_f32 v116, v116\n\t"
        "v_smfmac_f32_16x16x64_bf16 v[136:139], v[144:147], v[104:111], v148\n\t"
        "v_exp_f32 v117, v117\n\t"
        "s_waitcnt vmcnt(8)\n\t"
        "v_mfma_f32_32x32x16_bf16 v[96:111], v[200:203], v[64:67], 0\n\t"
        "v_exp_f32 v118, v118\n\t"
        "v_exp_f32 v119, v119\n\t"
        "v_exp_f32 v120, v120\n\t"
        "v_mfma_f32_32x32x16_bf16 v[96:111], v[204:207], v[68:71], v[96:111]\n\t"
        "v_exp_f32 v121, v121\n\t"
        "v_exp_f32 v122, v122\n\t"
        "v_exp_f32 v123, v123\n\t"
        "v_smfmac_f32_32x32x32_bf16 v[48:63], v[192:195], v[112:119], v148\n\t"
        "v_exp_f32 v124, v124\n\t"
        "v_exp_f32 v125, v125\n\t"
        "v_exp_f32 v126, v126\n\t"
        "v_smfmac_f32_16x16x64_bf16 v[140:143], v[144:147], v[112:119], v148\n\t"
        "buffer_load_dwordx4 v[184:187], v149, s[40:43], s44 offen offset:0 sc1\n\t"
        "v_exp_f32 v127, v127\n\t"
        "v_exp_f32 v96, v96\n\t"
        "v_exp_f32 v97, v97\n\t"
        "v_smfmac_f32_32x32x32_bf16 v[48:63], v[196:199], v[120:127], v148\n\t"
        "v_exp_f32 v98, v98\n\t"
        "v_exp_f32 v99, v99\n\t"
        "v_exp_f32 v100, v100\n\t"
        "v_smfmac_f32_16x16x64_bf16 v[140:143], v[144:147], v[120:127], v148\n\t"
        "v_exp_f32 v101, v101\n\t"
        "v_mfma_f32_32x32x16_bf16 v[112:127], v[200:203], v[72:75], 0\n\t"
        "v_exp_f32 v102, v102\n\t"
        "v_exp_f32 v103, v103\n\t"
        "v_exp_f32 v104, v104\n\t"
        "v_mfma_f32_32x32x16_bf16 v[112:127], v[204:207], v[76:79], v[112:127]\n\t"
        "v_exp_f32 v105, v105\n\t"
        "v_exp_f32 v106, v106\n\t"
        "v_exp_f32 v107, v107\n\t"
        "v_smfmac_f32_32x32x32_bf16 v[0:15], v[208:211], v[96:103], v148\n\t"
        "v_exp_f32 v108, v108\n\t"
        "v_exp_f32 v109, v109\n\t"
        "v_exp_f32 v110, v110\n\t"
        "v_smfmac_f32_16x16x64_bf16 v[128:131], v[144:147], v[96:103], v148\n\t"
        "buffer_load_dwordx4 v[188:191], v149, s[40:43], s44 offen offset:1024 sc1\n\t"
        "v_exp_f32 v111, v111\n\t"
        "v_exp_f32 v112, v112\n\t"
        "v_exp_f32 v113, v113\n\t"
        "v_smfmac_f32_32x32x32_bf16 v[0:15], v[212:215], v[104:111], v148\n\t"
        "v_exp_f32 v114, v114\n\t"
        "v_exp_f32 v115, v115\n\t"
        "v_exp_f32 v116, v116\n\t"
        "v_smfmac_f32_16x16x64_bf16 v[128:131], v[144:147], v[104:111], v148\n\t"
        "v_exp_f32 v117, v117\n\t"
        "v_mfma_f32_32x32x16_bf16 v[96:111], v[200:203], v[80:83], 0\n\t"
        "v_exp_f32 v118, v118\n\t"
        "v_exp_f32 v119, v119\n\t"
        "v_exp_f32 v120, v120\n\t"
        "v_mfma_f32_32x32x16_bf16 v[96:111], v[204:207], v[84:87], v[96:111]\n\t"
        "v_exp_f32 v121, v121\n\t"
        "v_exp_f32 v122, v122\n\t"
        "v_exp_f32 v123, v123\n\t"
        "v_smfmac_f32_32x32x32_bf16 v[16:31], v[208:211], v[112:119], v148\n\t"
        "v_exp_f32 v124, v124\n\t"
        "v_exp_f32 v125, v125\n\t"
        "v_exp_f32 v126, v126\n\t"
        "v_smfmac_f32_16x16x64_bf16 v[132:135], v[144:147], v[112:119], v148\n\t"
        "buffer_load_dwordx4 v[192:195], v149, s[40:43], s44 offen offset:2048 sc1\n\t"
        "v_exp_f32 v127, v127\n\t"
        "v_exp_f32 v96, v96\n\t"
        "v_exp_f32 v97, v97\n\t"
        "v_smfmac_f32_32x32x32_bf16 v[16:31], v[212:215], v[120:127], v148\n\t"
        "v_exp_f32 v98, v98\n\t"
        "v_exp_f32 v99, v99\n\t"
        "v_exp_f32 v100, v100\n\t"
        "v_smfmac_f32_16x16x64_bf16 v[132:135], v[144:147], v[120:127], v148\n\t"
        "v_exp_f32 v101, v101\n\t"
        "v_mfma_f32_32x32x16_bf16 v[112:127], v[200:203], v[88:91], 0\n\t"
        "v_exp_f32 v102, v102\n\t"
        "v_exp_f32 v103, v103\n\t"
        "v_exp_f32 v104, v104\n\t"
        "v_mfma_f32_32x32x16_bf16 v[112:127], v[204:207], v[92:95], v[112:127]\n\t"
        "v_exp_f32 v105, v105\n\t"
        "v_exp_f32 v106, v106\n\t"
        "v_exp_f32 v107, v107\n\t"
        "v_smfmac_f32_32x32x32_bf16 v[32:47], v[208:211], v[96:103], v148\n\t"
        "v_exp_f32 v108, v108\n\t"
        "v_exp_f32 v109, v109\n\t"
        "v_exp_f32 v110, v110\n\t"
        "v_smfmac_f32_16x16x64_bf16 v[136:139], v[144:147], v[96:103], v148\n\t"
        "buffer_load_dwordx4 v[196:199], v149, s[40:43], s44 offen offset:3072 sc1\n\t"
        "v_exp_f32 v111, v111\n\t"
        "v_exp_f32 v112, v112\n\t"
        "v_exp_f32 v113, v113\n\t"
        "v_smfmac_f32_32x32x32_bf16 v[32:47], v[212:215], v[104:111], v148\n\t"
        "v_exp_f32 v114, v114\n\t"
        "v_exp_f32 v115, v115\n\t"
        "v_exp_f32 v116, v116\n\t"
        "v_smfmac_f32_16x16x64_bf16 v[136:139], v[144:147], v[104:111], v148\n\t"
        "v_exp_f32 v117, v117\n\t"
        "s_waitcnt vmcnt(8)\n\t"
        "v_mfma_f32_32x32x16_bf16 v[96:111], v[152:155], v[64:67], 0\n\t"
        "v_exp_f32 v118, v118\n\t"
        "v_exp_f32 v119, v119\n\t"
        "v_exp_f32 v120, v120\n\t"
        "v_mfma_f32_32x32x16_bf16 v[96:111], v[156:159], v[68:71], v[96:111]\n\t"
        "v_exp_f32 v121, v121\n\t"
        "v_exp_f32 v122, v122\n\t"
        "v_exp_f32 v123, v123\n\t"
        "v_smfmac_f32_32x32x32_bf16 v[48:63], v[208:211], v[112:119], v148\n\t"
        "v_exp_f32 v124, v124\n\t"
        "v_exp_f32 v125, v125\n\t"
        "v_exp_f32 v126, v126\n\t"
        "v_smfmac_f32_16x16x64_bf16 v[140:143], v[144:147], v[112:119], v148\n\t"
        "buffer_load_dwordx4 v[200:203], v150, s[40:43], s44 offen offset:0 sc1\n\t"
        "v_exp_f32 v127, v127\n\t"
        "v_exp_f32 v96, v96\n\t"
        "v_exp_f32 v97, v97\n\t"
        "v_smfmac_f32_32x32x32_bf16 v[48:63], v[212:215], v[120:127], v148\n\t"
        "v_exp_f32 v98, v98\n\t"
        "v_exp_f32 v99, v99\n\t"
        "v_exp_f32 v100, v100\n\t"
        "v_smfmac_f32_16x16x64_bf16 v[140:143], v[144:147], v[120:127], v148\n\t"
        "v_exp_f32 v101, v101\n\t"
        "v_mfma_f32_32x32x16_bf16 v[112:127], v[152:155], v[72:75], 0\n\t"
        "v_exp_f32 v102, v102\n\t"
        "v_exp_f32 v103, v103\n\t"
        "v_exp_f32 v104, v104\n\t"
        "v_mfma_f32_32x32x16_bf16 v[112:127], v[156:159], v[76:79], v[112:127]\n\t"
        "v_exp_f32 v105, v105\n\t"
        "v_exp_f32 v106, v106\n\t"
        "v_exp_f32 v107, v107\n\t"
        "v_smfmac_f32_32x32x32_bf16 v[0:15], v[160:163], v[96:103], v148\n\t"
        "v_exp_f32 v108, v108\n\t"
        "v_exp_f32 v109, v109\n\t"
        "v_exp_f32 v110, v110\n\t"
        "v_smfmac_f32_16x16x64_bf16 v[128:131], v[144:147], v[96:103], v148\n\t"
        "buffer_load_dwordx4 v[204:207], v150, s[40:43], s44 offen offset:1024 sc1\n\t"
        "v_exp_f32 v111, v111\n\t"
        "v_exp_f32 v112, v112\n\t"
        "v_exp_f32 v113, v113\n\t"
        "v_smfmac_f32_32x32x32_bf16 v[0:15], v[164:167], v[104:111], v148\n\t"
        "v_exp_f32 v114, v114\n\t"
        "v_exp_f32 v115, v115\n\t"
        "v_exp_f32 v116, v116\n\t"
        "v_smfmac_f32_16x16x64_bf16 v[128:131], v[144:147], v[104:111], v148\n\t"
        "v_exp_f32 v117, v117\n\t"
        "v_mfma_f32_32x32x16_bf16 v[96:111], v[152:155], v[80:83], 0\n\t"
        "v_exp_f32 v118, v118\n\t"
        "v_exp_f32 v119, v119\n\t"
        "v_exp_f32 v120, v120\n\t"
        "v_mfma_f32_32x32x16_bf16 v[96:111], v[156:159], v[84:87], v[96:111]\n\t"
        "v_exp_f32 v121, v121\n\t"
        "v_exp_f32 v122, v122\n\t"
        "v_exp_f32 v123, v123\n\t"
        "v_smfmac_f32_32x32x32_bf16 v[16:31], v[160:163], v[112:119], v148\n\t"
        "v_exp_f32 v124, v124\n\t"
        "v_exp_f32 v125, v125\n\t"
        "v_exp_f32 v126, v126\n\t"
        "v_smfmac_f32_16x16x64_bf16 v[132:135], v[144:147], v[112:119], v148\n\t"
        "buffer_load_dwordx4 v[208:211], v150, s[40:43], s44 offen offset:2048 sc1\n\t"
        "v_exp_f32 v127, v127\n\t"
        "v_exp_f32 v96, v96\n\t"
        "v_exp_f32 v97, v97\n\t"
        "v_smfmac_f32_32x32x32_bf16 v[16:31], v[164:167], v[120:127], v148\n\t"
        "v_exp_f32 v98, v98\n\t"
        "v_exp_f32 v99, v99\n\t"
        "v_exp_f32 v100, v100\n\t"
        "v_smfmac_f32_16x16x64_bf16 v[132:135], v[144:147], v[120:127], v148\n\t"
        "v_exp_f32 v101, v101\n\t"
        "v_mfma_f32_32x32x16_bf16 v[112:127], v[152:155], v[88:91], 0\n\t"
        "v_exp_f32 v102, v102\n\t"
        "v_exp_f32 v103, v103\n\t"
        "v_exp_f32 v104, v104\n\t"
        "v_mfma_f32_32x32x16_bf16 v[112:127], v[156:159], v[92:95], v[112:127]\n\t"
        "v_exp_f32 v105, v105\n\t"
        "v_exp_f32 v106, v106\n\t"
        "v_exp_f32 v107, v107\n\t"
        "v_smfmac_f32_32x32x32_bf16 v[32:47], v[160:163], v[96:103], v148\n\t"
        "v_exp_f32 v108, v108\n\t"
        "v_exp_f32 v109, v109\n\t"
        "v_exp_f32 v110, v110\n\t"
        "v_smfmac_f32_16x16x64_bf16 v[136:139], v[144:147], v[96:103], v148\n\t"
        "buffer_load_dwordx4 v[212:215], v150, s[40:43], s44 offen offset:3072 sc1\n\t"
        "v_exp_f32 v111, v111\n\t"
        "v_exp_f32 v112, v112\n\t"
        "v_exp_f32 v113, v113\n\t"
        "v_smfmac_f32_32x32x32_bf16 v[32:47], v[164:167], v[104:111], v148\n\t"
        "s_add_u32 s44, s44, 0x4000\n\t"
        "s_and_b32 s44, s44, 0x3ffff\n\t"
        "v_exp_f32 v114, v114\n\t"
        "v_exp_f32 v115, v115\n\t"
        "v_exp_f32 v116, v116\n\t"
        "v_smfmac_f32_16x16x64_bf16 v[136:139], v[144:147], v[104:111], v148\n\t"
        "v_exp_f32 v117, v117\n\t"
        "s_waitcnt vmcnt(8)\n\t"
        "v_mfma_f32_32x32x16_bf16 v[96:111], v[168:171], v[64:67], 0\n\t"
        "v_exp_f32 v118, v118\n\t"
        "v_exp_f32 v119, v119\n\t"
        "v_exp_f32 v120, v120\n\t"
        "v_mfma_f32_32x32x16_bf16 v[96:111], v[172:175], v[68:71], v[96:111]\n\t"
        "v_exp_f32 v121, v121\n\t"
        "v_exp_f32 v122, v122\n\t"
        "v_exp_f32 v123, v123\n\t"
        "v_smfmac_f32_32x32x32_bf16 v[48:63], v[160:163], v[112:119], v148\n\t"
        "v_exp_f32 v124, v124\n\t"
        "v_exp_f32 v125, v125\n\t"
        "v_exp_f32 v126, v126\n\t"
        "v_smfmac_f32_16x16x64_bf16 v[140:143], v[144:147], v[112:119], v148\n\t"
        "buffer_load_dwordx4 v[152:155], v149, s[40:43], s44 offen offset:0 sc1\n\t"
        "v_exp_f32 v127, v127\n\t"
        "v_exp_f32 v96, v96\n\t"
        "v_exp_f32 v97, v97\n\t"
        "v_smfmac_f32_32x32x32_bf16 v[48:63], v[164:167], v[120:127], v148\n\t"
        "v_exp_f32 v98, v98\n\t"
        "v_exp_f32 v99, v99\n\t"
        "v_exp_f32 v100, v100\n\t"
        "v_smfmac_f32_16x16x64_bf16 v[140:143], v[144:147], v[120:127], v148\n\t"
        "v_exp_f32 v101, v101\n\t"
        "v_mfma_f32_32x32x16_bf16 v[112:127], v[168:171], v[72:75], 0\n\t"
        "v_exp_f32 v102, v102\n\t"
        "v_exp_f32 v103, v103\n\t"
        "v_exp_f32 v104, v104\n\t"
        "v_mfma_f32_32x32x16_bf16 v[112:127], v[172:175], v[76:79], v[112:127]\n\t"
        "v_exp_f32 v105, v105\n\t"
        "v_exp_f32 v106, v106\n\t"
        "v_exp_f32 v107, v107\n\t"
        "v_smfmac_f32_32x32x32_bf16 v[0:15], v[176:179], v[96:103], v148\n\t"
        "v_exp_f32 v108, v108\n\t"
        "v_exp_f32 v109, v109\n\t"
        "v_exp_f32 v110, v110\n\t"
        "v_smfmac_f32_16x16x64_bf16 v[128:131], v[144:147], v[96:103], v148\n\t"
        "buffer_load_dwordx4 v[156:159], v149, s[40:43], s44 offen offset:1024 sc1\n\t"
        "v_exp_f32 v111, v111\n\t"
        "v_exp_f32 v112, v112\n\t"
        "v_exp_f32 v113, v113\n\t"
        "v_smfmac_f32_32x32x32_bf16 v[0:15], v[180:183], v[104:111], v148\n\t"
        "v_exp_f32 v114, v114\n\t"
        "v_exp_f32 v115, v115\n\t"
        "v_exp_f32 v116, v116\n\t"
        "v_smfmac_f32_16x16x64_bf16 v[128:131], v[144:147], v[104:111], v148\n\t"
        "v_exp_f32 v117, v117\n\t"
        "v_mfma_f32_32x32x16_bf16 v[96:111], v[168:171], v[80:83], 0\n\t"
        "v_exp_f32 v118, v118\n\t"
        "v_exp_f32 v119, v119\n\t"
        "v_exp_f32 v120, v120\n\t"
        "v_mfma_f32_32x32x16_bf16 v[96:111], v[172:175], v[84:87], v[96:111]\n\t"
        "v_exp_f32 v121, v121\n\t"
        "v_exp_f32 v122, v122\n\t"
        "v_exp_f32 v123, v123\n\t"
        "v_smfmac_f32_32x32x32_bf16 v[16:31], v[176:179], v[112:119], v148\n\t"
        "v_exp_f32 v124, v124\n\t"
        "v_exp_f32 v125, v125\n\t"
        "v_exp_f32 v126, v126\n\t"
        "v_smfmac_f32_16x16x64_bf16 v[132:135], v[144:147], v[112:119], v148\n\t"
        "buffer_load_dwordx4 v[160:163], v149, s[40:43], s44 offen offset:2048 sc1\n\t"
        "v_exp_f32 v127, v127\n\t"
        "v_exp_f32 v96, v96\n\t"
        "v_exp_f32 v97, v97\n\t"
        "v_smfmac_f32_32x32x32_bf16 v[16:31], v[180:183], v[120:127], v148\n\t"
        "v_exp_f32 v98, v98\n\t"
        "v_exp_f32 v99, v99\n\t"
        "v_exp_f32 v100, v100\n\t"
        "v_smfmac_f32_16x16x64_bf16 v[132:135], v[144:147], v[120:127], v148\n\t"
        "v_exp_f32 v101, v101\n\t"
        "v_mfma_f32_32x32x16_bf16 v[112:127], v[168:171], v[88:91], 0\n\t"
        "v_exp_f32 v102, v102\n\t"
        "v_exp_f32 v103, v103\n\t"
        "v_exp_f32 v104, v104\n\t"
        "v_mfma_f32_32x32x16_bf16 v[112:127], v[172:175], v[92:95], v[112:127]\n\t"
        "v_exp_f32 v105, v105\n\t"
        "v_exp_f32 v106, v106\n\t"
        "v_exp_f32 v107, v107\n\t"
        "v_smfmac_f32_32x32x32_bf16 v[32:47], v[176:179], v[96:103], v148\n\t"
        "v_exp_f32 v108, v108\n\t"
        "v_exp_f32 v109, v109\n\t"
        "v_exp_f32 v110, v110\n\t"
        "v_smfmac_f32_16x16x64_bf16 v[136:139], v[144:147], v[96:103], v148\n\t"
        "buffer_load_dwordx4 v[164:167], v149, s[40:43], s44 offen offset:3072 sc1\n\t"
        "v_exp_f32 v111, v111\n\t"
        "s_sub_u32 s45, s45, 1\n\t"
        "s_cmp_lg_u32 s45, 0\n\t"
        "s_cbranch_scc1 1b\n\t"
        "v_exp_f32 v112, v112\n\t"
        "v_exp_f32 v113, v113\n\t"
        "v_smfmac_f32_32x32x32_bf16 v[32:47], v[180:183], v[104:111], v148\n\t"
        "v_exp_f32 v114, v114\n\t"
        "v_exp_f32 v115, v115\n\t"
        "v_exp_f32 v116, v116\n\t"
        "v_smfmac_f32_16x16x64_bf16 v[136:139], v[144:147], v[104:111], v148\n\t"
        "v_exp_f32 v117, v117\n\t"
        "s_waitcnt vmcnt(8)\n\t"
        "v_mfma_f32_32x32x16_bf16 v[96:111], v[184:187], v[64:67], 0\n\t"
        "v_exp_f32 v118, v118\n\t"
        "v_exp_f32 v119, v119\n\t"
        "v_exp_f32 v120, v120\n\t"
        "v_mfma_f32_32x32x16_bf16 v[96:111], v[188:191], v[68:71], v[96:111]\n\t"
        "v_exp_f32 v121, v121\n\t"
        "v_exp_f32 v122, v122\n\t"
        "v_exp_f32 v123, v123\n\t"
        "v_smfmac_f32_32x32x32_bf16 v[48:63], v[176:179], v[112:119], v148\n\t"
        "v_exp_f32 v124, v124\n\t"
        "v_exp_f32 v125, v125\n\t"
        "v_exp_f32 v126, v126\n\t"
        "v_smfmac_f32_16x16x64_bf16 v[140:143], v[144:147], v[112:119], v148\n\t"
        "v_exp_f32 v127, v127\n\t"
        "v_exp_f32 v96, v96\n\t"
        "v_exp_f32 v97, v97\n\t"
        "v_smfmac_f32_32x32x32_bf16 v[48:63], v[180:183], v[120:127], v148\n\t"
        "v_exp_f32 v98, v98\n\t"
        "v_exp_f32 v99, v99\n\t"
        "v_exp_f32 v100, v100\n\t"
        "v_smfmac_f32_16x16x64_bf16 v[140:143], v[144:147], v[120:127], v148\n\t"
        "v_exp_f32 v101, v101\n\t"
        "v_mfma_f32_32x32x16_bf16 v[112:127], v[184:187], v[72:75], 0\n\t"
        "v_exp_f32 v102, v102\n\t"
        "v_exp_f32 v103, v103\n\t"
        "v_exp_f32 v104, v104\n\t"
        "v_mfma_f32_32x32x16_bf16 v[112:127], v[188:191], v[76:79], v[112:127]\n\t"
        "v_exp_f32 v105, v105\n\t"
        "v_exp_f32 v106, v106\n\t"
        "v_exp_f32 v107, v107\n\t"
        "v_smfmac_f32_32x32x32_bf16 v[0:15], v[192:195], v[96:103], v148\n\t"
        "v_exp_f32 v108, v108\n\t"
        "v_exp_f32 v109, v109\n\t"
        "v_exp_f32 v110, v110\n\t"
        "v_smfmac_f32_16x16x64_bf16 v[128:131], v[144:147], v[96:103], v148\n\t"
        "v_exp_f32 v111, v111\n\t"
        "v_exp_f32 v112, v112\n\t"
        "v_exp_f32 v113, v113\n\t"
        "v_smfmac_f32_32x32x32_bf16 v[0:15], v[196:199], v[104:111], v148\n\t"
        "v_exp_f32 v114, v114\n\t"
        "v_exp_f32 v115, v115\n\t"
        "v_exp_f32 v116, v116\n\t"
        "v_smfmac_f32_16x16x64_bf16 v[128:131], v[144:147], v[104:111], v148\n\t"
        "v_exp_f32 v117, v117\n\t"
        "s_waitcnt vmcnt(4)\n\t"
        "v_mfma_f32_32x32x16_bf16 v[96:111], v[200:203], v[64:67], 0\n\t"
        "v_exp_f32 v118, v118\n\t"
        "v_exp_f32 v119, v119\n\t"
        "v_exp_f32 v120, v120\n\t"
        "v_mfma_f32_32x32x16_bf16 v[96:111], v[204:207], v[68:71], v[96:111]\n\t"
        "v_exp_f32 v121, v121\n\t"
        "v_exp_f32 v122, v122\n\t"
        "v_exp_f32 v123, v123\n\t"
        "v_smfmac_f32_32x32x32_bf16 v[16:31], v[192:195], v[112:119], v148\n\t"
        "v_exp_f32 v124, v124\n\t"
        "v_exp_f32 v125, v125\n\t"
        "v_exp_f32 v126, v126\n\t"
        "v_smfmac_f32_16x16x64_bf16 v[132:135], v[144:147], v[112:119], v148\n\t"
        "v_exp_f32 v127, v127\n\t"
        "s_cmp_lg_u32 s47, 0\n\t"
        "s_cbranch_scc1 4f\n\t"
        "global_load_dwordx4 %[r0], v223, %[xb] offset:0\n\t"
        "global_load_dwordx4 %[r1], v223, %[xb] offset:32\n\t"
        "global_load_dwordx4 %[r2], v223, %[xb] offset:64\n\t"
        "global_load_dwordx4 %[r3], v223, %[xb] offset:96\n\t"
        "4:\n\t"
        "v_exp_f32 v96, v96\n\t"
        "v_exp_f32 v97, v97\n\t"
        "v_smfmac_f32_32x32x32_bf16 v[16:31], v[196:199], v[120:127], v148\n\t"
        "v_exp_f32 v98, v98\n\t"
        "v_exp_f32 v99, v99\n\t"
        "v_exp_f32 v100, v100\n\t"
        "v_smfmac_f32_16x16x64_bf16 v[132:135], v[144:147], v[120:127], v148\n\t"
        "v_exp_f32 v101, v101\n\t"
        "v_mfma_f32_32x32x16_bf16 v[112:127], v[200:203], v[72:75], 0\n\t"
        "v_exp_f32 v102, v102\n\t"
        "v_exp_f32 v103, v103\n\t"
        "v_exp_f32 v104, v104\n\t"
        "v_mfma_f32_32x32x16_bf16 v[112:127], v[204:207], v[76:79], v[112:127]\n\t"
        "v_exp_f32 v105, v105\n\t"
        "v_exp_f32 v106, v106\n\t"
        "v_exp_f32 v107, v107\n\t"
        "v_smfmac_f32_32x32x32_bf16 v[0:15], v[208:211], v[96:103], v148\n\t"
        "v_exp_f32 v108, v108\n\t"
        "v_exp_f32 v109, v109\n\t"
        "v_exp_f32 v110, v110\n\t"
        "v_smfmac_f32_16x16x64_bf16 v[128:131], v[144:147], v[96:103], v148\n\t"
        "v_exp_f32 v111, v111\n\t"
        "v_exp_f32 v112, v112\n\t"
        "v_exp_f32 v113, v113\n\t"
        "v_smfmac_f32_32x32x32_bf16 v[0:15], v[212:215], v[104:111], v148\n\t"
        "v_exp_f32 v114, v114\n\t"
        "v_exp_f32 v115, v115\n\t"
        "v_exp_f32 v116, v116\n\t"
        "v_smfmac_f32_16x16x64_bf16 v[128:131], v[144:147], v[104:111], v148\n\t"
        "v_exp_f32 v117, v117\n\t"
        "v_mfma_f32_32x32x16_bf16 v[96:111], v[184:187], v[80:83], 0\n\t"
        "v_exp_f32 v118, v118\n\t"
        "v_exp_f32 v119, v119\n\t"
        "v_exp_f32 v120, v120\n\t"
        "ds_write_b128 v216, v[0:3] offset:0\n\t"
        "v_mfma_f32_32x32x16_bf16 v[96:111], v[188:191], v[84:87], v[96:111]\n\t"
        "v_exp_f32 v121, v121\n\t"
        "ds_write_b128 v216, v[4:7] offset:1024\n\t"
        "v_exp_f32 v122, v122\n\t"
        "ds_write_b128 v216, v[8:11] offset:2048\n\t"
        "v_exp_f32 v123, v123\n\t"
        "ds_write_b128 v216, v[12:15] offset:3072\n\t"
        "v_smfmac_f32_32x32x32_bf16 v[16:31], v[208:211], v[112:119], v148\n\t"
        "v_exp_f32 v124, v124\n\t"
        "ds_write_b32 v218, v128 offset:0\n\t"
        "v_exp_f32 v125, v125\n\t"
        "v_exp_f32 v126, v126\n\t"
        "v_smfmac_f32_16x16x64_bf16 v[132:135], v[144:147], v[112:119], v148\n\t"
        "v_exp_f32 v127, v127\n\t"
        "v_exp_f32 v96, v96\n\t"
        "v_exp_f32 v97, v97\n\t"
        "v_smfmac_f32_32x32x32_bf16 v[16:31], v[212:215], v[120:127], v148\n\t"
        "v_exp_f32 v98, v98\n\t"
        "v_exp_f32 v99, v99\n\t"
        "v_exp_f32 v100, v100\n\t"
        "v_smfmac_f32_16x16x64_bf16 v[132:135], v[144:147], v[120:127], v148\n\t"
        "v_exp_f32 v101, v101\n\t"
        "v_mfma_f32_32x32x16_bf16 v[112:127], v[184:187], v[88:91], 0\n\t"
        "v_exp_f32 v102, v102\n\t"
        "v_exp_f32 v103, v103\n\t"
        "v_exp_f32 v104, v104\n\t"
        "ds_write_b128 v216, v[16:19] offset:4096\n\t"
        "v_mfma_f32_32x32x16_bf16 v[112:127], v[188:191], v[92:95], v[112:127]\n\t"
        "v_exp_f32 v105, v105\n\t"
        "ds_write_b128 v216, v[20:23] offset:5120\n\t"
        "v_exp_f32 v106, v106\n\t"
        "ds_write_b128 v216, v[24:27] offset:6144\n\t"
        "v_exp_f32 v107, v107\n\t"
        "ds_write_b128 v216, v[28:31] offset:7168\n\t"
        "v_smfmac_f32_32x32x32_bf16 v[32:47], v[192:195], v[96:103], v148\n\t"
        "v_exp_f32 v108, v108\n\t"
        "ds_write_b32 v218, v132 offset:256\n\t"
        "v_exp_f32 v109, v109\n\t"
        "ds_write_b32 v220, v222\n\t"
        "v_exp_f32 v110, v110\n\t"
        "v_smfmac_f32_16x16x64_bf16 v[136:139], v[144:147], v[96:103], v148\n\t"
        "v_exp_f32 v111, v111\n\t"
        "v_exp_f32 v112, v112\n\t"
        "v_exp_f32 v113, v113\n\t"
        "v_smfmac_f32_32x32x32_bf16 v[32:47], v[196:199], v[104:111], v148\n\t"
        "v_exp_f32 v114, v114\n\t"
        "v_exp_f32 v115, v115\n\t"
        "v_exp_f32 v116, v116\n\t"
        "v_smfmac_f32_16x16x64_bf16 v[136:139], v[144:147], v[104:111], v148\n\t"
        "v_exp_f32 v117, v117\n\t"
        "v_mfma_f32_32x32x16_bf16 v[96:111], v[200:203], v[80:83], 0\n\t"
        "v_exp_f32 v118, v118\n\t"
        "v_exp_f32 v119, v119\n\t"
        "v_exp_f32 v120, v120\n\t"
        "v_mfma_f32_32x32x16_bf16 v[96:111], v[204:207], v[84:87], v[96:111]\n\t"
        "v_exp_f32 v121, v121\n\t"
        "v_exp_f32 v122, v122\n\t"
        "v_exp_f32 v123, v123\n\t"
        "v_smfmac_f32_32x32x32_bf16 v[48:63], v[192:195], v[112:119], v148\n\t"
        "v_exp_f32 v124, v124\n\t"
        "v_exp_f32 v125, v125\n\t"
        "v_exp_f32 v126, v126\n\t"
        "v_smfmac_f32_16x16x64_bf16 v[140:143], v[144:147], v[112:119], v148\n\t"
        "v_exp_f32 v127, v127\n\t"
        "v_exp_f32 v96, v96\n\t"
        "v_exp_f32 v97, v97\n\t"
        "v_smfmac_f32_32x32x32_bf16 v[48:63], v[196:199], v[120:127], v148\n\t"
        "v_exp_f32 v98, v98\n\t"
        "v_exp_f32 v99, v99\n\t"
        "v_exp_f32 v100, v100\n\t"
        "v_smfmac_f32_16x16x64_bf16 v[140:143], v[144:147], v[120:127], v148\n\t"
        "v_exp_f32 v101, v101\n\t"
        "v_mfma_f32_32x32x16_bf16 v[112:127], v[200:203], v[88:91], 0\n\t"
        "v_exp_f32 v102, v102\n\t"
        "v_exp_f32 v103, v103\n\t"
        "v_exp_f32 v104, v104\n\t"
        "ds_read_b32 v151, v221\n\t"
        "v_mfma_f32_32x32x16_bf16 v[112:127], v[204:207], v[92:95], v[112:127]\n\t"
        "v_exp_f32 v105, v105\n\t"
        "v_exp_f32 v106, v106\n\t"
        "v_exp_f32 v107, v107\n\t"
        "v_smfmac_f32_32x32x32_bf16 v[32:47], v[208:211], v[96:103], v148\n\t"
        "v_exp_f32 v108, v108\n\t"
        "v_exp_f32 v109, v109\n\t"
        "v_exp_f32 v110, v110\n\t"
        "v_smfmac_f32_16x16x64_bf16 v[136:139], v[144:147], v[96:103], v148\n\t"
        "v_exp_f32 v111, v111\n\t"
        "s_waitcnt lgkmcnt(0)\n\t"
        "v_cmp_ne_u32 vcc, v222, v151\n\t"
        "s_cbranch_vccnz 40f\n\t"
        "41:\n\t"
        "v_exp_f32 v112, v112\n\t"
        "ds_read_b128 v[0:3], v217 offset:0\n\t"
        "v_exp_f32 v113, v113\n\t"
        "ds_read_b128 v[4:7], v217 offset:1024\n\t"
        "v_smfmac_f32_32x32x32_bf16 v[32:47], v[212:215], v[104:111], v148\n\t"
        "v_exp_f32 v114, v114\n\t"
        "ds_read_b128 v[8:11], v217 offset:2048\n\t"
        "v_exp_f32 v115, v115\n\t"
        "ds_read_b128 v[12:15], v217 offset:3072\n\t"
        "v_exp_f32 v116, v116\n\t"
        "ds_read_b32 v128, v219 offset:0\n\t"
        "v_smfmac_f32_16x16x64_bf16 v[136:139], v[144:147], v[104:111], v148\n\t"
        "v_exp_f32 v117, v117\n\t"
        "ds_read_b128 v[16:19], v217 offset:4096\n\t"
        "v_exp_f32 v118, v118\n\t"
        "ds_read_b128 v[20:23], v217 offset:5120\n\t"
        "v_exp_f32 v119, v119\n\t"
        "ds_read_b128 v[24:27], v217 offset:6144\n\t"
        "v_exp_f32 v120, v120\n\t"
        "ds_read_b128 v[28:31], v217 offset:7168\n\t"
        "v_exp_f32 v121, v121\n\t"
        "ds_read_b32 v132, v219 offset:256\n\t"
        "v_exp_f32 v122, v122\n\t"
        "v_exp_f32 v123, v123\n\t"
        "v_smfmac_f32_32x32x32_bf16 v[48:63], v[208:211], v[112:119], v148\n\t"
        "v_exp_f32 v124, v124\n\t"
        "v_exp_f32 v125, v125\n\t"
        "v_exp_f32 v126, v126\n\t"
        "v_smfmac_f32_16x16x64_bf16 v[140:143], v[144:147], v[112:119], v148\n\t"
        "v_exp_f32 v127, v127\n\t"
        "s_nop 1\n\t"
        "v_smfmac_f32_32x32x32_bf16 v[48:63], v[212:215], v[120:127], v148\n\t"
        "v_smfmac_f32_16x16x64_bf16 v[140:143], v[144:147], v[120:127], v148\n\t"
        "s_waitcnt vmcnt(0) lgkmcnt(0)\n\t"
        "s_nop 6\n\t"
        "s_branch 9f\n\t"
        "24:\n\t"
        "s_add_u32 s44, s44, 0x4000\n\t"
        "s_and_b32 s44, s44, 0x3ffff\n\t"
        "buffer_load_dwordx4 v[184:187], v149, s[40:43], s44 offen offset:0 sc1\n\t"
        "buffer_load_dwordx4 v[188:191], v149, s[40:43], s44 offen offset:1024 sc1\n\t"
        "buffer_load_dwordx4 v[192:195], v149, s[40:43], s44 offen offset:2048 sc1\n\t"
        "buffer_load_dwordx4 v[196:199], v149, s[40:43], s44 offen offset:3072 sc1\n\t"
        "buffer_load_dwordx4 v[200:203], v150, s[40:43], s44 offen offset:0 sc1\n\t"
        "buffer_load_dwordx4 v[204:207], v150, s[40:43], s44 offen offset:1024 sc1\n\t"
        "buffer_load_dwordx4 v[208:211], v150, s[40:43], s44 offen offset:2048 sc1\n\t"
        "buffer_load_dwordx4 v[212:215], v150, s[40:43], s44 offen offset:3072 sc1\n\t"
        "s_mov_b32 s46, 1\n\t"
        "s_branch 25b\n\t"
        "26:\n\t"
        "s_add_u32 s44, s44, 0x4000\n\t"
        "s_and_b32 s44, s44, 0x3ffff\n\t"
        "buffer_load_dwordx4 v[184:187], v149, s[40:43], s44 offen offset:0 sc1\n\t"
        "buffer_load_dwordx4 v[188:191], v149, s[40:43], s44 offen offset:1024 sc1\n\t"
        "buffer_load_dwordx4 v[192:195], v149, s[40:43], s44 offen offset:2048 sc1\n\t"
        "buffer_load_dwordx4 v[196:199], v149, s[40:43], s44 offen offset:3072 sc1\n\t"
        "buffer_load_dwordx4 v[200:203], v150, s[40:43], s44 offen offset:0 sc1\n\t"
        "buffer_load_dwordx4 v[204:207], v150, s[40:43], s44 offen offset:1024 sc1\n\t"
        "buffer_load_dwordx4 v[208:211], v150, s[40:43], s44 offen offset:2048 sc1\n\t"
        "buffer_load_dwordx4 v[212:215], v150, s[40:43], s44 offen offset:3072 sc1\n\t"
        "s_mov_b32 s46, 1\n\t"
        "s_branch 27b\n\t"
        "28:\n\t"
        "s_add_u32 s44, s44, 0x4000\n\t"
        "s_and_b32 s44, s44, 0x3ffff\n\t"
        "buffer_load_dwordx4 v[184:187], v149, s[40:43], s44 offen offset:0 sc1\n\t"
        "buffer_load_dwordx4 v[188:191], v149, s[40:43], s44 offen offset:1024 sc1\n\t"
        "buffer_load_dwordx4 v[192:195], v149, s[40:43], s44 offen offset:2048 sc1\n\t"
        "buffer_load_dwordx4 v[196:199], v149, s[40:43], s44 offen offset:3072 sc1\n\t"
        "buffer_load_dwordx4 v[200:203], v150, s[40:43], s44 offen offset:0 sc1\n\t"
        "buffer_load_dwordx4 v[204:207], v150, s[40:43], s44 offen offset:1024 sc1\n\t"
        "buffer_load_dwordx4 v[208:211], v150, s[40:43], s44 offen offset:2048 sc1\n\t"
        "buffer_load_dwordx4 v[212:215], v150, s[40:43], s44 offen offset:3072 sc1\n\t"
        "s_mov_b32 s46, 1\n\t"
        "s_branch 29b\n\t"
        "30:\n\t"
        "s_add_u32 s44, s44, 0x4000\n\t"
        "s_and_b32 s44, s44, 0x3ffff\n\t"
        "buffer_load_dwordx4 v[184:187], v149, s[40:43], s44 offen offset:0 sc1\n\t"
        "buffer_load_dwordx4 v[188:191], v149, s[40:43], s44 offen offset:1024 sc1\n\t"
        "buffer_load_dwordx4 v[192:195], v149, s[40:43], s44 offen offset:2048 sc1\n\t"
        "buffer_load_dwordx4 v[196:199], v149, s[40:43], s44 offen offset:3072 sc1\n\t"
        "buffer_load_dwordx4 v[200:203], v150, s[40:43], s44 offen offset:0 sc1\n\t"
        "buffer_load_dwordx4 v[204:207], v150, s[40:43], s44 offen offset:1024 sc1\n\t"
        "buffer_load_dwordx4 v[208:211], v150, s[40:43], s44 offen offset:2048 sc1\n\t"
        "buffer_load_dwordx4 v[212:215], v150, s[40:43], s44 offen offset:3072 sc1\n\t"
        "s_mov_b32 s46, 1\n\t"
        "s_branch 31b\n\t"
        "32:\n\t"
        "s_add_u32 s44, s44, 0x4000\n\t"
        "s_and_b32 s44, s44, 0x3ffff\n\t"
        "buffer_load_dwordx4 v[184:187], v149, s[40:43], s44 offen offset:0 sc1\n\t"
        "buffer_load_dwordx4 v[188:191], v149, s[40:43], s44 offen offset:1024 sc1\n\t"
        "buffer_load_dwordx4 v[192:195], v149, s[40:43], s44 offen offset:2048 sc1\n\t"
        "buffer_load_dwordx4 v[196:199], v149, s[40:43], s44 offen offset:3072 sc1\n\t"
        "buffer_load_dwordx4 v[200:203], v150, s[40:43], s44 offen offset:0 sc1\n\t"
        "buffer_load_dwordx4 v[204:207], v150, s[40:43], s44 offen offset:1024 sc1\n\t"
        "buffer_load_dwordx4 v[208:211], v150, s[40:43], s44 offen offset:2048 sc1\n\t"
        "buffer_load_dwordx4 v[212:215], v150, s[40:43], s44 offen offset:3072 sc1\n\t"
        "s_mov_b32 s46, 1\n\t"
        "s_branch 33b\n\t"
        "40:\n\t"
        "s_mov_b32 s45, 0x100000\n\t"
        "42:\n\t"
        "ds_read_b32 v151, v221\n\t"
        "s_sub_u32 s45, s45, 1\n\t"
        "s_waitcnt lgkmcnt(0)\n\t"
        "s_cbranch_scc1 41b\n\t"
        "v_cmp_ne_u32 vcc, v222, v151\n\t"
        "s_cbranch_vccnz 42b\n\t"
        "s_branch 41b\n\t"
        "9:\n\t"
        : "+{v[0:15]}"(o0), "+{v[16:31]}"(o1), "+{v[32:47]}"(o2), "+{v[48:63]}"(o3), "+{v[128:143]}"(lv), "+{s44}"(soff), "+{v[152:167]}"(kva), "+{v[168:183]}"(kvb), "+{v[184:191]}"(fv),
          [r0] "+v"(x1p[0]), [r1] "+v"(x1p[1]), [r2] "+v"(x1p[2]), [r3] "+v"(x1p[3]) STAMP_OUTS
        : "{v[64:71]}"(q[0]), "{v[72:79]}"(q[1]), "{v[80:87]}"(q[2]), "{v[88:95]}"(q[3]), "{v[144:147]}"(ones), "{v148}"(0xddddu), "{v149}"(kvoff), "{v150}"(kvoff + 4096),
          "{v[216:223]}"(cv), "{v224}"(lbase + L_W + 16), "{s[40:43]}"(srd), "{s47}"(L), [xb] "s"(x0)
        : "v96", "v97", "v98", "v99", "v100", "v101", "v102", "v103", "v104", "v105", "v106", "v107", "v108", "v109", "v110", "v111", "v112", "v113", "v114", "v115", "v116", "v117", "v118", "v119", "v120", "v121", "v122", "v123", "v124", "v125", "v126", "v127", "v151", "v192", "v193", "v194", "v195", "v196", "v197", "v198", "v199", "v200", "v201", "v202", "v203", "v204", "v205", "v206", "v207", "v208", "v209", "v210", "v211", "v212", "v213", "v214", "v215", "s45", "s46", "vcc", "scc", "memory");
      STAMP_PUT(6 + 16 * L);
      STAMPV(9 + 16 * L);
      PHASE_VARS;
      const unsigned dst = OFF_OF + L * (5 * MBY) + (tm * 4 + 2 * kh) * TS + (tn * 2 + hh) * 2048 + lane * 16;
      auto put = [&](int u, const f32x16& o) {
#pragma unroll
        for (int c = 0; c < 2; ++c) {
          u32x4 w;
#pragma unroll
          for (int e = 0; e < 4; ++e) w[e] = pack2(o[8 * c + 2 * e], o[8 * c + 2 * e + 1]);
          stg(rs, dst + u * TS + c * 1024, w);
        }
      };
      const float la = lv[8] + lv[0], lb = lv[12] + lv[4];
      o2 = (o2 + o0) * RCP(la); put(0, o2);
      SBAR();
      o3 = (o3 + o1) * RCP(lb); put(1, o3);
      SBAR();
      {
        const f32x16 t = o2 + o3;
        float sum = 0.f;
#pragma unroll
        for (int e = 0; e < 16; e += 4) sum += (t[e] + t[e + 1]) + (t[e + 2] + t[e + 3]);
        if (__any(!(fabsf(sum) < 1e37f) | !(fmaxf(la, lb) < 1e27f) | !(fminf(la, lb) > 1e-9f) | !(la + lb == la + lb))) {
          DRAIN();
          attn_safe(rs, kvoff - kh * 8192, q[2], ones, o2); put(0, o2);
          attn_safe(rs, kvoff - kh * 8192, q[3], ones, o3); put(1, o3);
        }
      }
    }
    STAMPV(10 + 16 * L);
    {
      f32x16 acc[2];
      u32x4 res[4];
      u32x2 wrc[8];
      STAMP_DECL;
      { PHASE_VARS;
      BAR_ALL();
      STAMPV(11 + 16 * L);
#pragma unroll
      for (int g = 0; g < 8; ++g) wrc[g] = *reinterpret_cast<const u32x2*>(smem + L_WR + g * 16 + hi * 8);
      if (L == 0) {
#pragma unroll
        for (int i = 0; i < 4; ++i) res[i] = fo_swap(x1p[i]);
      } else {
        const bool routed = (int)words[3] == tile * 32 + r32;
        const float mb1 = reinterpret_cast<const float*>(smem + L_AB)[256 + wid * 64 + r32], rd1 = reinterpret_cast<const float*>(smem + L_AB)[256 + wid * 64 + 32 + r32];
#pragma unroll
        for (int i = 0; i < 4; ++i) {
          const u32x4 pr = *reinterpret_cast<const u32x4*>(smem + L_X2P + (i * 2 + hi) * 16);
#pragma unroll
          for (int e = 0; e < 4; ++e) {
            const unsigned h2 = ln_pair(x1p[i][e], mb1, rd1);
            unsigned t = pack2(lo16(x1p[i][e]) + lo16(h2), hi16(x1p[i][e]) + hi16(h2));
            PIN(t);
            res[i][e] = routed ? pr[e] : t;
          }
        }
      }
#pragma unroll
      for (int g = 0; g < 8; ++g) PIN(wrc[g]);
#pragma unroll
      for (int e = 0; e < 16; ++e) { acc[e >> 3][2 * (e & 7)] = lo16(res[e >> 2][e & 3]); acc[e >> 3][2 * (e & 7) + 1] = hi16(res[e >> 2][e & 3]); }
      unsigned soff = tn * 4096;
      const unsigned a0 = lbase + wrow * 128 + ((wch ^ wsw) << 4) + 8 * (tp & 1), a1 = lbase + wrow * 128 + (((4 + wch) ^ wsw) << 4) + 8 * (tp & 1);
      asm volatile(
        "ds_read_b64_tr_b16 v[32:33], v193 offset:0\n\t"
        "ds_read_b64_tr_b16 v[34:35], v193 offset:1024\n\t"
        "ds_read_b64_tr_b16 v[36:37], v194 offset:0\n\t"
        "ds_read_b64_tr_b16 v[38:39], v194 offset:1024\n\t"
        "ds_read_b64_tr_b16 v[40:41], v193 offset:2048\n\t"
        "ds_read_b64_tr_b16 v[42:43], v193 offset:3072\n\t"
        "ds_read_b64_tr_b16 v[44:45], v194 offset:2048\n\t"
        "ds_read_b64_tr_b16 v[46:47], v194 offset:3072\n\t"
        "ds_read_b64_tr_b16 v[48:49], v193 offset:4096\n\t"
        "ds_read_b64_tr_b16 v[50:51], v193 offset:5120\n\t"
        "ds_read_b64_tr_b16 v[52:53], v194 offset:4096\n\t"
        "ds_read_b64_tr_b16 v[54:55], v194 offset:5120\n\t"
        "ds_read_b64_tr_b16 v[56:57], v193 offset:6144\n\t"
        "ds_read_b64_tr_b16 v[58:59], v193 offset:7168\n\t"
        "ds_read_b64_tr_b16 v[60:61], v194 offset:6144\n\t"
        "ds_read_b64_tr_b16 v[62:63], v194 offset:7168\n\t"
        "s_mov_b32 s45, 0x200000\n\t"
        "2:\n\t"
        "ds_read_b32 v200, v198\n\t"
        "s_sub_u32 s45, s45, 1\n\t"
        "s_waitcnt lgkmcnt(0)\n\t"
        "s_cbranch_scc1 3f\n\t"
        "v_cmp_ne_u32 vcc, v199, v200\n\t"
        "s_cbranch_vccnz 2b\n\t"
        "3:\n\t"
        "buffer_load_dwordx4 v[64:67], v192, s[40:43], s44 offen offset:0 sc1\n\t"
        "buffer_load_dwordx4 v[68:71], v192, s[40:43], s44 offen offset:1024 sc1\n\t"
        "buffer_load_dwordx4 v[72:75], v192, s[40:43], s44 offen offset:2048 sc1\n\t"
        "buffer_load_dwordx4 v[76:79], v192, s[40:43], s44 offen offset:3072 sc1\n\t"
        "s_add_u32 s44, s44, 0x1000\n\t"
        "s_and_b32 s44, s44, 0xffff\n\t"
        "buffer_load_dwordx4 v[80:83], v192, s[40:43], s44 offen offset:0 sc1\n\t"
        "buffer_load_dwordx4 v[84:87], v192, s[40:43], s44 offen offset:1024 sc1\n\t"
        "buffer_load_dwordx4 v[88:91], v192, s[40:43], s44 offen offset:2048 sc1\n\t"
        "buffer_load_dwordx4 v[92:95], v192, s[40:43], s44 offen offset:3072 sc1\n\t"
        "s_add_u32 s44, s44, 0x1000\n\t"
        "s_and_b32 s44, s44, 0xffff\n\t"
        "buffer_load_dwordx4 v[96:99], v192, s[40:43], s44 offen offset:0 sc1\n\t"
        "buffer_load_dwordx4 v[100:103], v192, s[40:43], s44 offen offset:1024 sc1\n\t"
        "buffer_load_dwordx4 v[104:107], v192, s[40:43], s44 offen offset:2048 sc1\n\t"
        "buffer_load_dwordx4 v[108:111], v192, s[40:43], s44 offen offset:3072 sc1\n\t"
        "s_waitcnt vmcnt(11) lgkmcnt(14)\n\t"
        "v_mfma_f32_32x32x16_bf16 v[0:15], v[32:35], v[64:67], v[0:15]\n\t"
        "ds_read_b64_tr_b16 v[32:33], v193 offset:8192\n\t"
        "ds_read_b64_tr_b16 v[34:35], v193 offset:9216\n\t"
        "s_waitcnt lgkmcnt(14)\n\t"
        "v_mfma_f32_32x32x16_bf16 v[16:31], v[36:39], v[64:67], v[16:31]\n\t"
        "ds_read_b64_tr_b16 v[36:37], v194 offset:8192\n\t"
        "ds_read_b64_tr_b16 v[38:39], v194 offset:9216\n\t"
        "s_add_u32 s44, s44, 0x1000\n\t"
        "s_and_b32 s44, s44, 0xffff\n\t"
        "buffer_load_dwordx4 v[112:115], v192, s[40:43], s44 offen offset:0 sc1\n\t"
        "s_waitcnt vmcnt(11) lgkmcnt(14)\n\t"
        "v_mfma_f32_32x32x16_bf16 v[0:15], v[40:43], v[68:71], v[0:15]\n\t"
        "ds_read_b64_tr_b16 v[40:41], v193 offset:10240\n\t"
        "ds_read_b64_tr_b16 v[42:43], v193 offset:11264\n\t"
        "s_waitcnt lgkmcnt(14)\n\t"
        "v_mfma_f32_32x32x16_bf16 v[16:31], v[44:47], v[68:71], v[16:31]\n\t"
        "ds_read_b64_tr_b16 v[44:45], v194 offset:10240\n\t"
        "ds_read_b64_tr_b16 v[46:47], v194 offset:11264\n\t"
        "buffer_load_dwordx4 v[116:119], v192, s[40:43], s44 offen offset:1024 sc1\n\t"
        "s_waitcnt vmcnt(11) lgkmcnt(14)\n\t"
        "v_mfma_f32_32x32x16_bf16 v[0:15], v[48:51], v[72:75], v[0:15]\n\t"
        "ds_read_b64_tr_b16 v[48:49], v193 offset:12288\n\t"
        "ds_read_b64_tr_b16 v[50:51], v193 offset:13312\n\t"
        "s_waitcnt lgkmcnt(14)\n\t"
        "v_mfma_f32_32x32x16_bf16 v[16:31], v[52:55], v[72:75], v[16:31]\n\t"
        "ds_read_b64_tr_b16 v[52:53], v194 offset:12288\n\t"
        "ds_read_b64_tr_b16 v[54:55], v194 offset:13312\n\t"
        "buffer_load_dwordx4 v[120:123], v192, s[40:43], s44 offen offset:2048 sc1\n\t"
        "s_waitcnt vmcnt(11) lgkmcnt(14)\n\t"
        "v_mfma_f32_32x32x16_bf16 v[0:15], v[56:59], v[76:79], v[0:15]\n\t"
        "ds_read_b64_tr_b16 v[56:57], v193 offset:14336\n\t"
        "ds_read_b64_tr_b16 v[58:59], v193 offset:15360\n\t"
        "s_waitcnt lgkmcnt(14)\n\t"
        "v_mfma_f32_32x32x16_bf16 v[16:31], v[60:63], v[76:79], v[16:31]\n\t"
        "ds_read_b64_tr_b16 v[60:61], v194 offset:14336\n\t"
        "ds_read_b64_tr_b16 v[62:63], v194 offset:15360\n\t"
        "buffer_load_dwordx4 v[124:127], v192, s[40:43], s44 offen offset:3072 sc1\n\t"
        "s_waitcnt vmcnt(11) lgkmcnt(14)\n\t"
        "v_mfma_f32_32x32x16_bf16 v[0:15], v[32:35], v[80:83], v[0:15]\n\t"
        "ds_read_b64_tr_b16 v[32:33], v193 offset:16384\n\t"
        "ds_read_b64_tr_b16 v[34:35], v193 offset:17408\n\t"
        "s_waitcnt lgkmcnt(14)\n\t"
        "v_mfma_f32_32x32x16_bf16 v[16:31], v[36:39], v[80:83], v[16:31]\n\t"
        "ds_read_b64_tr_b16 v[36:37], v194 offset:16384\n\t"
        "ds_read_b64_tr_b16 v[38:39], v194 offset:17408\n\t"
        "s_add_u32 s44, s44, 0x1000\n\t"
        "s_and_b32 s44, s44, 0xffff\n\t"
        "buffer_load_dwordx4 v[128:131], v192, s[40:43], s44 offen offset:0 sc1\n\t"
        "s_waitcnt vmcnt(11) lgkmcnt(14)\n\t"
        "v_mfma_f32_32x32x16_bf16 v[0:15], v[40:43], v[84:87], v[0:15]\n\t"
        "ds_read_b64_tr_b16 v[40:41], v193 offset:18432\n\t"
        "ds_read_b64_tr_b16 v[42:43], v193 offset:19456\n\t"
        "s_waitcnt lgkmcnt(14)\n\t"
        "v_mfma_f32_32x32x16_bf16 v[16:31], v[44:47], v[84:87], v[16:31]\n\t"
        "ds_read_b64_tr_b16 v[44:45], v194 offset:18432\n\t"
        "ds_read_b64_tr_b16 v[46:47], v194 offset:19456\n\t"
        "buffer_load_dwordx4 v[132:135], v192, s[40:43], s44 offen offset:1024 sc1\n\t"
        "s_waitcnt vmcnt(11) lgkmcnt(14)\n\t"
        "v_mfma_f32_32x32x16_bf16 v[0:15], v[48:51], v[88:91], v[0:15]\n\t"
        "ds_read_b64_tr_b16 v[48:49], v193 offset:20480\n\t"
        "ds_read_b64_tr_b16 v[50:51], v193 offset:21504\n\t"
        "s_waitcnt lgkmcnt(14)\n\t"
        "v_mfma_f32_32x32x16_bf16 v[16:31], v[52:55], v[88:91], v[16:31]\n\t"
        "ds_read_b64_tr_b16 v[52:53], v194 offset:20480\n\t"
        "ds_read_b64_tr_b16 v[54:55], v194 offset:21504\n\t"
        "buffer_load_dwordx4 v[136:139], v192, s[40:43], s44 offen offset:2048 sc1\n\t"
        "s_waitcnt vmcnt(11) lgkmcnt(14)\n\t"
        "v_mfma_f32_32x32x16_bf16 v[0:15], v[56:59], v[92:95], v[0:15]\n\t"
        "ds_read_b64_tr_b16 v[56:57], v193 offset:22528\n\t"
        "ds_read_b64_tr_b16 v[58:59], v193 offset:23552\n\t"
        "s_waitcnt lgkmcnt(14)\n\t"
        "v_mfma_f32_32x32x16_bf16 v[16:31], v[60:63], v[92:95], v[16:31]\n\t"
        "ds_read_b64_tr_b16 v[60:61], v194 offset:22528\n\t"
        "ds_read_b64_tr_b16 v[62:63], v194 offset:23552\n\t"
        "buffer_load_dwordx4 v[140:143], v192, s[40:43], s44 offen offset:3072 sc1\n\t"
        "s_waitcnt vmcnt(11) lgkmcnt(14)\n\t"
        "v_mfma_f32_32x32x16_bf16 v[0:15], v[32:35], v[96:99], v[0:15]\n\t"
        "ds_read_b64_tr_b16 v[32:33], v193 offset:24576\n\t"
        "ds_read_b64_tr_b16 v[34:35], v193 offset:25600\n\t"
        "s_waitcnt lgkmcnt(14)\n\t"
        "v_mfma_f32_32x32x16_bf16 v[16:31], v[36:39], v[96:99], v[16:31]\n\t"
        "ds_read_b64_tr_b16 v[36:37], v194 offset:24576\n\t"
        "ds_read_b64_tr_b16 v[38:39], v194 offset:25600\n\t"
        "s_add_u32 s44, s44, 0x1000\n\t"
        "s_and_b32 s44, s44, 0xffff\n\t"
        "buffer_load_dwordx4 v[144:147], v192, s[40:43], s44 offen offset:0 sc1\n\t"
        "s_waitcnt vmcnt(11) lgkmcnt(14)\n\t"
        "v_mfma_f32_32x32x16_bf16 v[0:15], v[40:43], v[100:103], v[0:15]\n\t"
        "ds_read_b64_tr_b16 v[40:41], v193 offset:26624\n\t"
        "ds_read_b64_tr_b16 v[42:43], v193 offset:27648\n\t"
        "s_waitcnt lgkmcnt(14)\n\t"
        "v_mfma_f32_32x32x16_bf16 v[16:31], v[44:47], v[100:103], v[16:31]\n\t"
        "ds_read_b64_tr_b16 v[44:45], v194 offset:26624\n\t"
        "ds_read_b64_tr_b16 v[46:47], v194 offset:27648\n\t"
        "buffer_load_dwordx4 v[148:151], v192, s[40:43], s44 offen offset:1024 sc1\n\t"
        "s_waitcnt vmcnt(11) lgkmcnt(14)\n\t"
        "v_mfma_f32_32x32x16_bf16 v[0:15], v[48:51], v[104:107], v[0:15]\n\t"
        "ds_read_b64_tr_b16 v[48:49], v193 offset:28672\n\t"
        "ds_read_b64_tr_b16 v[50:51], v193 offset:29696\n\t"
        "s_waitcnt lgkmcnt(14)\n\t"
        "v_mfma_f32_32x32x16_bf16 v[16:31], v[52:55], v[104:107], v[16:31]\n\t"
        "ds_read_b64_tr_b16 v[52:53], v194 offset:28672\n\t"
        "ds_read_b64_tr_b16 v[54:55], v194 offset:29696\n\t"
        "buffer_load_dwordx4 v[152:155], v192, s[40:43], s44 offen offset:2048 sc1\n\t"
        "s_waitcnt vmcnt(11) lgkmcnt(14)\n\t"
        "v_mfma_f32_32x32x16_bf16 v[0:15], v[56:59], v[108:111], v[0:15]\n\t"
        "ds_read_b64_tr_b16 v[56:57], v193 offset:30720\n\t"
        "ds_read_b64_tr_b16 v[58:59], v193 offset:31744\n\t"
        "s_waitcnt lgkmcnt(14)\n\t"
        "v_mfma_f32_32x32x16_bf16 v[16:31], v[60:63], v[108:111], v[16:31]\n\t"
        "ds_read_b64_tr_b16 v[60:61], v194 offset:30720\n\t"
        "ds_read_b64_tr_b16 v[62:63], v194 offset:31744\n\t"
        "buffer_load_dwordx4 v[156:159], v192, s[40:43], s44 offen offset:3072 sc1\n\t"
        "s_waitcnt vmcnt(11) lgkmcnt(14)\n\t"
        "v_mfma_f32_32x32x16_bf16 v[0:15], v[32:35], v[112:115], v[0:15]\n\t"
        "ds_read_b64_tr_b16 v[32:33], v193 offset:32768\n\t"
        "ds_read_b64_tr_b16 v[34:35], v193 offset:33792\n\t"
        "s_waitcnt lgkmcnt(14)\n\t"
        "v_mfma_f32_32x32x16_bf16 v[16:31], v[36:39], v[112:115], v[16:31]\n\t"
        "ds_read_b64_tr_b16 v[36:37], v194 offset:32768\n\t"
        "ds_read_b64_tr_b16 v[38:39], v194 offset:33792\n\t"
        "s_add_u32 s44, s44, 0x1000\n\t"
        "s_and_b32 s44, s44, 0xffff\n\t"
        "buffer_load_dwordx4 v[160:163], v192, s[40:43], s44 offen offset:0 sc1\n\t"
        "s_waitcnt vmcnt(11) lgkmcnt(14)\n\t"
        "v_mfma_f32_32x32x16_bf16 v[0:15], v[40:43], v[116:119], v[0:15]\n\t"
        "ds_read_b64_tr_b16 v[40:41], v193 offset:34816\n\t"
        "ds_read_b64_tr_b16 v[42:43], v193 offset:35840\n\t"
        "s_waitcnt lgkmcnt(14)\n\t"
        "v_mfma_f32_32x32x16_bf16 v[16:31], v[44:47], v[116:119], v[16:31]\n\t"
        "ds_read_b64_tr_b16 v[44:45], v194 offset:34816\n\t"
        "ds_read_b64_tr_b16 v[46:47], v194 offset:35840\n\t"
        "buffer_load_dwordx4 v[164:167], v192, s[40:43], s44 offen offset:1024 sc1\n\t"
        "s_waitcnt vmcnt(11) lgkmcnt(14)\n\t"
        "v_mfma_f32_32x32x16_bf16 v[0:15], v[48:51], v[120:123], v[0:15]\n\t"
        "ds_read_b64_tr_b16 v[48:49], v193 offset:36864\n\t"
        "ds_read_b64_tr_b16 v[50:51], v193 offset:37888\n\t"
        "s_waitcnt lgkmcnt(14)\n\t"
        "v_mfma_f32_32x32x16_bf16 v[16:31], v[52:55], v[120:123], v[16:31]\n\t"
        "ds_read_b64_tr_b16 v[52:53], v194 offset:36864\n\t"
        "ds_read_b64_tr_b16 v[54:55], v194 offset:37888\n\t"
        "buffer_load_dwordx4 v[168:171], v192, s[40:43], s44 offen offset:2048 sc1\n\t"
        "s_waitcnt vmcnt(11) lgkmcnt(14)\n\t"
        "v_mfma_f32_32x32x16_bf16 v[0:15], v[56:59], v[124:127], v[0:15]\n\t"
        "ds_read_b64_tr_b16 v[56:57], v193 offset:38912\n\t"
        "ds_read_b64_tr_b16 v[58:59], v193 offset:39936\n\t"
        "s_waitcnt lgkmcnt(14)\n\t"
        "v_mfma_f32_32x32x16_bf16 v[16:31], v[60:63], v[124:127], v[16:31]\n\t"
        "ds_read_b64_tr_b16 v[60:61], v194 offset:38912\n\t"
        "ds_read_b64_tr_b16 v[62:63], v194 offset:39936\n\t"
        "buffer_load_dwordx4 v[172:175], v192, s[40:43], s44 offen offset:3072 sc1\n\t"
        "s_waitcnt vmcnt(11) lgkmcnt(14)\n\t"
        "v_mfma_f32_32x32x16_bf16 v[0:15], v[32:35], v[128:131], v[0:15]\n\t"
        "ds_read_b64_tr_b16 v[32:33], v193 offset:40960\n\t"
        "ds_read_b64_tr_b16 v[34:35], v193 offset:41984\n\t"
        "s_waitcnt lgkmcnt(14)\n\t"
        "v_mfma_f32_32x32x16_bf16 v[16:31], v[36:39], v[128:131], v[16:31]\n\t"
        "ds_read_b64_tr_b16 v[36:37], v194 offset:40960\n\t"
        "ds_read_b64_tr_b16 v[38:39], v194 offset:41984\n\t"
        "s_add_u32 s44, s44, 0x1000\n\t"
        "s_and_b32 s44, s44, 0xffff\n\t"
        "buffer_load_dwordx4 v[176:179], v192, s[40:43], s44 offen offset:0 sc1\n\t"
        "s_waitcnt vmcnt(11) lgkmcnt(14)\n\t"
        "v_mfma_f32_32x32x16_bf16 v[0:15], v[40:43], v[132:135], v[0:15]\n\t"
        "ds_read_b64_tr_b16 v[40:41], v193 offset:43008\n\t"
        "ds_read_b64_tr_b16 v[42:43], v193 offset:44032\n\t"
        "s_waitcnt lgkmcnt(14)\n\t"
        "v_mfma_f32_32x32x16_bf16 v[16:31], v[44:47], v[132:135], v[16:31]\n\t"
        "ds_read_b64_tr_b16 v[44:45], v194 offset:43008\n\t"
        "ds_read_b64_tr_b16 v[46:47], v194 offset:44032\n\t"
        "buffer_load_dwordx4 v[180:183], v192, s[40:43], s44 offen offset:1024 sc1\n\t"
        "s_waitcnt vmcnt(11) lgkmcnt(14)\n\t"
        "v_mfma_f32_32x32x16_bf16 v[0:15], v[48:51], v[136:139], v[0:15]\n\t"
        "ds_read_b64_tr_b16 v[48:49], v193 offset:45056\n\t"
        "ds_read_b64_tr_b16 v[50:51], v193 offset:46080\n\t"
        "s_waitcnt lgkmcnt(14)\n\t"
        "v_mfma_f32_32x32x16_bf16 v[16:31], v[52:55], v[136:139], v[16:31]\n\t"
        "ds_read_b64_tr_b16 v[52:53], v194 offset:45056\n\t"
        "ds_read_b64_tr_b16 v[54:55], v194 offset:46080\n\t"
        "buffer_load_dwordx4 v[184:187], v192, s[40:43], s44 offen offset:2048 sc1\n\t"
        "s_waitcnt vmcnt(11) lgkmcnt(14)\n\t"
        "v_mfma_f32_32x32x16_bf16 v[0:15], v[56:59], v[140:143], v[0:15]\n\t"
        "ds_read_b64_tr_b16 v[56:57], v193 offset:47104\n\t"
        "ds_read_b64_tr_b16 v[58:59], v193 offset:48128\n\t"
        "s_waitcnt lgkmcnt(14)\n\t"
        "v_mfma_f32_32x32x16_bf16 v[16:31], v[60:63], v[140:143], v[16:31]\n\t"
        "ds_read_b64_tr_b16 v[60:61], v194 offset:47104\n\t"
        "ds_read_b64_tr_b16 v[62:63], v194 offset:48128\n\t"
        "buffer_load_dwordx4 v[188:191], v192, s[40:43], s44 offen offset:3072 sc1\n\t"
        "s_waitcnt vmcnt(11) lgkmcnt(14)\n\t"
        "v_mfma_f32_32x32x16_bf16 v[0:15], v[32:35], v[144:147], v[0:15]\n\t"
        "ds_read_b64_tr_b16 v[32:33], v193 offset:49152\n\t"
        "ds_read_b64_tr_b16 v[34:35], v193 offset:50176\n\t"
        "s_waitcnt lgkmcnt(14)\n\t"
        "v_mfma_f32_32x32x16_bf16 v[16:31], v[36:39], v[144:147], v[16:31]\n\t"
        "ds_read_b64_tr_b16 v[36:37], v194 offset:49152\n\t"
        "ds_read_b64_tr_b16 v[38:39], v194 offset:50176\n\t"
        "s_add_u32 s44, s44, 0x1000\n\t"
        "s_and_b32 s44, s44, 0xffff\n\t"
        "buffer_load_dwordx4 v[64:67], v192, s[40:43], s44 offen offset:0 sc1\n\t"
        "s_waitcnt vmcnt(11) lgkmcnt(14)\n\t"
        "v_mfma_f32_32x32x16_bf16 v[0:15], v[40:43], v[148:151], v[0:15]\n\t"
        "ds_read_b64_tr_b16 v[40:41], v193 offset:51200\n\t"
        "ds_read_b64_tr_b16 v[42:43], v193 offset:52224\n\t"
        "s_waitcnt lgkmcnt(14)\n\t"
        "v_mfma_f32_32x32x16_bf16 v[16:31], v[44:47], v[148:151], v[16:31]\n\t"
        "ds_read_b64_tr_b16 v[44:45], v194 offset:51200\n\t"
        "ds_read_b64_tr_b16 v[46:47], v194 offset:52224\n\t"
        "buffer_load_dwordx4 v[68:71], v192, s[40:43], s44 offen offset:1024 sc1\n\t"
        "s_waitcnt vmcnt(11) lgkmcnt(14)\n\t"
        "v_mfma_f32_32x32x16_bf16 v[0:15], v[48:51], v[152:155], v[0:15]\n\t"
        "ds_read_b64_tr_b16 v[48:49], v193 offset:53248\n\t"
        "ds_read_b64_tr_b16 v[50:51], v193 offset:54272\n\t"
        "s_waitcnt lgkmcnt(14)\n\t"
        "v_mfma_f32_32x32x16_bf16 v[16:31], v[52:55], v[152:155], v[16:31]\n\t"
        "ds_read_b64_tr_b16 v[52:53], v194 offset:53248\n\t"
        "ds_read_b64_tr_b16 v[54:55], v194 offset:54272\n\t"
        "buffer_load_dwordx4 v[72:75], v192, s[40:43], s44 offen offset:2048 sc1\n\t"
        "s_waitcnt vmcnt(11) lgkmcnt(14)\n\t"
        "v_mfma_f32_32x32x16_bf16 v[0:15], v[56:59], v[156:159], v[0:15]\n\t"
        "ds_read_b64_tr_b16 v[56:57], v193 offset:55296\n\t"
        "ds_read_b64_tr_b16 v[58:59], v193 offset:56320\n\t"
        "s_waitcnt lgkmcnt(14)\n\t"
        "v_mfma_f32_32x32x16_bf16 v[16:31], v[60:63], v[156:159], v[16:31]\n\t"
        "ds_read_b64_tr_b16 v[60:61], v194 offset:55296\n\t"
        "ds_read_b64_tr_b16 v[62:63], v194 offset:56320\n\t"
        "buffer_load_dwordx4 v[76:79], v192, s[40:43], s44 offen offset:3072 sc1\n\t"
        "s_waitcnt vmcnt(11) lgkmcnt(14)\n\t"
        "v_mfma_f32_32x32x16_bf16 v[0:15], v[32:35], v[160:163], v[0:15]\n\t"
        "ds_read_b64_tr_b16 v[32:33], v193 offset:57344\n\t"
        "ds_read_b64_tr_b16 v[34:35], v193 offset:58368\n\t"
        "s_waitcnt lgkmcnt(14)\n\t"
        "v_mfma_f32_32x32x16_bf16 v[16:31], v[36:39], v[160:163], v[16:31]\n\t"
        "ds_read_b64_tr_b16 v[36:37], v194 offset:57344\n\t"
        "ds_read_b64_tr_b16 v[38:39], v194 offset:58368\n\t"
        "s_add_u32 s44, s44, 0x1000\n\t"
        "s_and_b32 s44, s44, 0xffff\n\t"
        "buffer_load_dwordx4 v[80:83], v192, s[40:43], s44 offen offset:0 sc1\n\t"
        "s_waitcnt vmcnt(11) lgkmcnt(14)\n\t"
        "v_mfma_f32_32x32x16_bf16 v[0:15], v[40:43], v[164:167], v[0:15]\n\t"
        "ds_read_b64_tr_b16 v[40:41], v193 offset:59392\n\t"
        "ds_read_b64_tr_b16 v[42:43], v193 offset:60416\n\t"
        "s_waitcnt lgkmcnt(14)\n\t"
        "v_mfma_f32_32x32x16_bf16 v[16:31], v[44:47], v[164:167], v[16:31]\n\t"
        "ds_read_b64_tr_b16 v[44:45], v194 offset:59392\n\t"
        "ds_read_b64_tr_b16 v[46:47], v194 offset:60416\n\t"
        "buffer_load_dwordx4 v[84:87], v192, s[40:43], s44 offen offset:1024 sc1\n\t"
        "s_waitcnt vmcnt(11) lgkmcnt(14)\n\t"
        "v_mfma_f32_32x32x16_bf16 v[0:15], v[48:51], v[168:171], v[0:15]\n\t"
        "ds_read_b64_tr_b16 v[48:49], v193 offset:61440\n\t"
        "ds_read_b64_tr_b16 v[50:51], v193 offset:62464\n\t"
        "s_waitcnt lgkmcnt(14)\n\t"
        "v_mfma_f32_32x32x16_bf16 v[16:31], v[52:55], v[168:171], v[16:31]\n\t"
        "ds_read_b64_tr_b16 v[52:53], v194 offset:61440\n\t"
        "ds_read_b64_tr_b16 v[54:55], v194 offset:62464\n\t"
        "buffer_load_dwordx4 v[88:91], v192, s[40:43], s44 offen offset:2048 sc1\n\t"
        "s_waitcnt vmcnt(11) lgkmcnt(14)\n\t"
        "v_mfma_f32_32x32x16_bf16 v[0:15], v[56:59], v[172:175], v[0:15]\n\t"
        "ds_read_b64_tr_b16 v[56:57], v193 offset:63488\n\t"
        "ds_read_b64_tr_b16 v[58:59], v193 offset:64512\n\t"
        "s_waitcnt lgkmcnt(14)\n\t"
        "v_mfma_f32_32x32x16_bf16 v[16:31], v[60:63], v[172:175], v[16:31]\n\t"
        "ds_read_b64_tr_b16 v[60:61], v194 offset:63488\n\t"
        "ds_read_b64_tr_b16 v[62:63], v194 offset:64512\n\t"
        "buffer_load_dwordx4 v[92:95], v192, s[40:43], s44 offen offset:3072 sc1\n\t"
        "s_waitcnt vmcnt(11) lgkmcnt(14)\n\t"
        "v_mfma_f32_32x32x16_bf16 v[0:15], v[32:35], v[176:179], v[0:15]\n\t"
        "ds_read_b64_tr_b16 v[32:33], v195 offset:0\n\t"
        "ds_read_b64_tr_b16 v[34:35], v195 offset:1024\n\t"
        "s_waitcnt lgkmcnt(14)\n\t"
        "v_mfma_f32_32x32x16_bf16 v[16:31], v[36:39], v[176:179], v[16:31]\n\t"
        "ds_read_b64_tr_b16 v[36:37], v196 offset:0\n\t"
        "ds_read_b64_tr_b16 v[38:39], v196 offset:1024\n\t"
        "s_add_u32 s44, s44, 0x1000\n\t"
        "s_and_b32 s44, s44, 0xffff\n\t"
        "buffer_load_dwordx4 v[96:99], v192, s[40:43], s44 offen offset:0 sc1\n\t"
        "s_waitcnt vmcnt(11) lgkmcnt(14)\n\t"
        "v_mfma_f32_32x32x16_bf16 v[0:15], v[40:43], v[180:183], v[0:15]\n\t"
        "ds_read_b64_tr_b16 v[40:41], v195 offset:2048\n\t"
        "ds_read_b64_tr_b16 v[42:43], v195 offset:3072\n\t"
        "s_waitcnt lgkmcnt(14)\n\t"
        "v_mfma_f32_32x32x16_bf16 v[16:31], v[44:47], v[180:183], v[16:31]\n\t"
        "ds_read_b64_tr_b16 v[44:45], v196 offset:2048\n\t"
        "ds_read_b64_tr_b16 v[46:47], v196 offset:3072\n\t"
        "buffer_load_dwordx4 v[100:103], v192, s[40:43], s44 offen offset:1024 sc1\n\t"
        "s_waitcnt vmcnt(11) lgkmcnt(14)\n\t"
        "v_mfma_f32_32x32x16_bf16 v[0:15], v[48:51], v[184:187], v[0:15]\n\t"
        "ds_read_b64_tr_b16 v[48:49], v195 offset:4096\n\t"
        "ds_read_b64_tr_b16 v[50:51], v195 offset:5120\n\t"
        "s_waitcnt lgkmcnt(14)\n\t"
        "v_mfma_f32_32x32x16_bf16 v[16:31], v[52:55], v[184:187], v[16:31]\n\t"
        "ds_read_b64_tr_b16 v[52:53], v196 offset:4096\n\t"
        "ds_read_b64_tr_b16 v[54:55], v196 offset:5120\n\t"
        "buffer_load_dwordx4 v[104:107], v192, s[40:43], s44 offen offset:2048 sc1\n\t"
        "s_waitcnt vmcnt(11) lgkmcnt(14)\n\t"
        "v_mfma_f32_32x32x16_bf16 v[0:15], v[56:59], v[188:191], v[0:15]\n\t"
        "ds_read_b64_tr_b16 v[56:57], v195 offset:6144\n\t"
        "ds_read_b64_tr_b16 v[58:59], v195 offset:7168\n\t"
        "s_waitcnt lgkmcnt(14)\n\t"
        "v_mfma_f32_32x32x16_bf16 v[16:31], v[60:63], v[188:191], v[16:31]\n\t"
        "ds_read_b64_tr_b16 v[60:61], v196 offset:6144\n\t"
        "ds_read_b64_tr_b16 v[62:63], v196 offset:7168\n\t"
        "buffer_load_dwordx4 v[108:111], v192, s[40:43], s44 offen offset:3072 sc1\n\t"
        "s_waitcnt vmcnt(11) lgkmcnt(14)\n\t"
        "v_mfma_f32_32x32x16_bf16 v[0:15], v[32:35], v[64:67], v[0:15]\n\t"
        "ds_read_b64_tr_b16 v[32:33], v195 offset:8192\n\t"
        "ds_read_b64_tr_b16 v[34:35], v195 offset:9216\n\t"
        "s_waitcnt lgkmcnt(14)\n\t"
        "v_mfma_f32_32x32x16_bf16 v[16:31], v[36:39], v[64:67], v[16:31]\n\t"
        "ds_read_b64_tr_b16 v[36:37], v196 offset:8192\n\t"
        "ds_read_b64_tr_b16 v[38:39], v196 offset:9216\n\t"
        "s_add_u32 s44, s44, 0x1000\n\t"
        "s_and_b32 s44, s44, 0xffff\n\t"
        "buffer_load_dwordx4 v[112:115], v192, s[40:43], s44 offen offset:0 sc1\n\t"
        "s_waitcnt vmcnt(11) lgkmcnt(14)\n\t"
        "v_mfma_f32_32x32x16_bf16 v[0:15], v[40:43], v[68:71], v[0:15]\n\t"
        "ds_read_b64_tr_b16 v[40:41], v195 offset:10240\n\t"
        "ds_read_b64_tr_b16 v[42:43], v195 offset:11264\n\t"
        "s_waitcnt lgkmcnt(14)\n\t"
        "v_mfma_f32_32x32x16_bf16 v[16:31], v[44:47], v[68:71], v[16:31]\n\t"
        "ds_read_b64_tr_b16 v[44:45], v196 offset:10240\n\t"
        "ds_read_b64_tr_b16 v[46:47], v196 offset:11264\n\t"
        "buffer_load_dwordx4 v[116:119], v192, s[40:43], s44 offen offset:1024 sc1\n\t"
        "s_waitcnt vmcnt(11) lgkmcnt(14)\n\t"
        "v_mfma_f32_32x32x16_bf16 v[0:15], v[48:51], v[72:75], v[0:15]\n\t"
        "ds_read_b64_tr_b16 v[48:49], v195 offset:12288\n\t"
        "ds_read_b64_tr_b16 v[50:51], v195 offset:13312\n\t"
        "s_waitcnt lgkmcnt(14)\n\t"
        "v_mfma_f32_32x32x16_bf16 v[16:31], v[52:55], v[72:75], v[16:31]\n\t"
        "ds_read_b64_tr_b16 v[52:53], v196 offset:12288\n\t"
        "ds_read_b64_tr_b16 v[54:55], v196 offset:13312\n\t"
        "buffer_load_dwordx4 v[120:123], v192, s[40:43], s44 offen offset:2048 sc1\n\t"
        "s_waitcnt vmcnt(11) lgkmcnt(14)\n\t"
        "v_mfma_f32_32x32x16_bf16 v[0:15], v[56:59], v[76:79], v[0:15]\n\t"
        "ds_read_b64_tr_b16 v[56:57], v195 offset:14336\n\t"
        "ds_read_b64_tr_b16 v[58:59], v195 offset:15360\n\t"
        "s_waitcnt lgkmcnt(14)\n\t"
        "v_mfma_f32_32x32x16_bf16 v[16:31], v[60:63], v[76:79], v[16:31]\n\t"
        "ds_read_b64_tr_b16 v[60:61], v196 offset:14336\n\t"
        "ds_read_b64_tr_b16 v[62:63], v196 offset:15360\n\t"
        "buffer_load_dwordx4 v[124:127], v192, s[40:43], s44 offen offset:3072 sc1\n\t"
        "s_waitcnt vmcnt(11) lgkmcnt(14)\n\t"
        "v_mfma_f32_32x32x16_bf16 v[0:15], v[32:35], v[80:83], v[0:15]\n\t"
        "ds_read_b64_tr_b16 v[32:33], v195 offset:16384\n\t"
        "ds_read_b64_tr_b16 v[34:35], v195 offset:17408\n\t"
        "s_waitcnt lgkmcnt(14)\n\t"
        "v_mfma_f32_32x32x16_bf16 v[16:31], v[36:39], v[80:83], v[16:31]\n\t"
        "ds_read_b64_tr_b16 v[36:37], v196 offset:16384\n\t"
        "ds_read_b64_tr_b16 v[38:39], v196 offset:17408\n\t"
        "s_add_u32 s44, s44, 0x1000\n\t"
        "s_and_b32 s44, s44, 0xffff\n\t"
        "buffer_load_dwordx4 v[128:131], v192, s[40:43], s44 offen offset:0 sc1\n\t"
        "s_waitcnt vmcnt(11) lgkmcnt(14)\n\t"
        "v_mfma_f32_32x32x16_bf16 v[0:15], v[40:43], v[84:87], v[0:15]\n\t"
        "ds_read_b64_tr_b16 v[40:41], v195 offset:18432\n\t"
        "ds_read_b64_tr_b16 v[42:43], v195 offset:19456\n\t"
        "s_waitcnt lgkmcnt(14)\n\t"
        "v_mfma_f32_32x32x16_bf16 v[16:31], v[44:47], v[84:87], v[16:31]\n\t"
        "ds_read_b64_tr_b16 v[44:45], v196 offset:18432\n\t"
        "ds_read_b64_tr_b16 v[46:47], v196 offset:19456\n\t"
        "buffer_load_dwordx4 v[132:135], v192, s[40:43], s44 offen offset:1024 sc1\n\t"
        "s_waitcnt vmcnt(11) lgkmcnt(14)\n\t"
        "v_mfma_f32_32x32x16_bf16 v[0:15], v[48:51], v[88:91], v[0:15]\n\t"
        "ds_read_b64_tr_b16 v[48:49], v195 offset:20480\n\t"
        "ds_read_b64_tr_b16 v[50:51], v195 offset:21504\n\t"
        "s_waitcnt lgkmcnt(14)\n\t"
        "v_mfma_f32_32x32x16_bf16 v[16:31], v[52:55], v[88:91], v[16:31]\n\t"
        "ds_read_b64_tr_b16 v[52:53], v196 offset:20480\n\t"
        "ds_read_b64_tr_b16 v[54:55], v196 offset:21504\n\t"
        "buffer_load_dwordx4 v[136:139], v192, s[40:43], s44 offen offset:2048 sc1\n\t"
        "s_waitcnt vmcnt(11) lgkmcnt(14)\n\t"
        "v_mfma_f32_32x32x16_bf16 v[0:15], v[56:59], v[92:95], v[0:15]\n\t"
        "ds_read_b64_tr_b16 v[56:57], v195 offset:22528\n\t"
        "ds_read_b64_tr_b16 v[58:59], v195 offset:23552\n\t"
        "s_waitcnt lgkmcnt(14)\n\t"
        "v_mfma_f32_32x32x16_bf16 v[16:31], v[60:63], v[92:95], v[16:31]\n\t"
        "ds_read_b64_tr_b16 v[60:61], v196 offset:22528\n\t"
        "ds_read_b64_tr_b16 v[62:63], v196 offset:23552\n\t"
        "buffer_load_dwordx4 v[140:143], v192, s[40:43], s44 offen offset:3072 sc1\n\t"
        "s_waitcnt vmcnt(11) lgkmcnt(14)\n\t"
        "v_mfma_f32_32x32x16_bf16 v[0:15], v[32:35], v[96:99], v[0:15]\n\t"
        "ds_read_b64_tr_b16 v[32:33], v195 offset:24576\n\t"
        "ds_read_b64_tr_b16 v[34:35], v195 offset:25600\n\t"
        "s_waitcnt lgkmcnt(14)\n\t"
        "v_mfma_f32_32x32x16_bf16 v[16:31], v[36:39], v[96:99], v[16:31]\n\t"
        "ds_read_b64_tr_b16 v[36:37], v196 offset:24576\n\t"
        "ds_read_b64_tr_b16 v[38:39], v196 offset:25600\n\t"
        "s_add_u32 s44, s44, 0x1000\n\t"
        "s_and_b32 s44, s44, 0xffff\n\t"
        "buffer_load_dwordx4 v[144:147], v192, s[40:43], s44 offen offset:0 sc1\n\t"
        "s_waitcnt vmcnt(11) lgkmcnt(14)\n\t"
        "v_mfma_f32_32x32x16_bf16 v[0:15], v[40:43], v[100:103], v[0:15]\n\t"
        "ds_read_b64_tr_b16 v[40:41], v195 offset:26624\n\t"
        "ds_read_b64_tr_b16 v[42:43], v195 offset:27648\n\t"
        "s_waitcnt lgkmcnt(14)\n\t"
        "v_mfma_f32_32x32x16_bf16 v[16:31], v[44:47], v[100:103], v[16:31]\n\t"
        "ds_read_b64_tr_b16 v[44:45], v196 offset:26624\n\t"
        "ds_read_b64_tr_b16 v[46:47], v196 offset:27648\n\t"
        "buffer_load_dwordx4 v[148:151], v192, s[40:43], s44 offen offset:1024 sc1\n\t"
        "s_waitcnt vmcnt(11) lgkmcnt(14)\n\t"
        "v_mfma_f32_32x32x16_bf16 v[0:15], v[48:51], v[104:107], v[0:15]\n\t"
        "ds_read_b64_tr_b16 v[48:49], v195 offset:28672\n\t"
        "ds_read_b64_tr_b16 v[50:51], v195 offset:29696\n\t"
        "s_waitcnt lgkmcnt(14)\n\t"
        "v_mfma_f32_32x32x16_bf16 v[16:31], v[52:55], v[104:107], v[16:31]\n\t"
        "ds_read_b64_tr_b16 v[52:53], v196 offset:28672\n\t"
        "ds_read_b64_tr_b16 v[54:55], v196 offset:29696\n\t"
        "buffer_load_dwordx4 v[152:155], v192, s[40:43], s44 offen offset:2048 sc1\n\t"
        "s_waitcnt vmcnt(11) lgkmcnt(14)\n\t"
        "v_mfma_f32_32x32x16_bf16 v[0:15], v[56:59], v[108:111], v[0:15]\n\t"
        "ds_read_b64_tr_b16 v[56:57], v195 offset:30720\n\t"
        "ds_read_b64_tr_b16 v[58:59], v195 offset:31744\n\t"
        "s_waitcnt lgkmcnt(14)\n\t"
        "v_mfma_f32_32x32x16_bf16 v[16:31], v[60:63], v[108:111], v[16:31]\n\t"
        "ds_read_b64_tr_b16 v[60:61], v196 offset:30720\n\t"
        "ds_read_b64_tr_b16 v[62:63], v196 offset:31744\n\t"
        "buffer_load_dwordx4 v[156:159], v192, s[40:43], s44 offen offset:3072 sc1\n\t"
        "s_waitcnt vmcnt(11) lgkmcnt(14)\n\t"
        "v_mfma_f32_32x32x16_bf16 v[0:15], v[32:35], v[112:115], v[0:15]\n\t"
        "ds_read_b64_tr_b16 v[32:33], v195 offset:32768\n\t"
        "ds_read_b64_tr_b16 v[34:35], v195 offset:33792\n\t"
        "s_waitcnt lgkmcnt(14)\n\t"
        "v_mfma_f32_32x32x16_bf16 v[16:31], v[36:39], v[112:115], v[16:31]\n\t"
        "ds_read_b64_tr_b16 v[36:37], v196 offset:32768\n\t"
        "ds_read_b64_tr_b16 v[38:39], v196 offset:33792\n\t"
        "s_add_u32 s44, s44, 0x1000\n\t"
        "s_and_b32 s44, s44, 0xffff\n\t"
        "buffer_load_dwordx4 v[160:163], v192, s[40:43], s44 offen offset:0 sc1\n\t"
        "s_waitcnt vmcnt(11) lgkmcnt(14)\n\t"
        "v_mfma_f32_32x32x16_bf16 v[0:15], v[40:43], v[116:119], v[0:15]\n\t"
        "ds_read_b64_tr_b16 v[40:41], v195 offset:34816\n\t"
        "ds_read_b64_tr_b16 v[42:43], v195 offset:35840\n\t"
        "s_waitcnt lgkmcnt(14)\n\t"
        "v_mfma_f32_32x32x16_bf16 v[16:31], v[44:47], v[116:119], v[16:31]\n\t"
        "ds_read_b64_tr_b16 v[44:45], v196 offset:34816\n\t"
        "ds_read_b64_tr_b16 v[46:47], v196 offset:35840\n\t"
        "buffer_load_dwordx4 v[164:167], v192, s[40:43], s44 offen offset:1024 sc1\n\t"
        "s_waitcnt vmcnt(11) lgkmcnt(14)\n\t"
        "v_mfma_f32_32x32x16_bf16 v[0:15], v[48:51], v[120:123], v[0:15]\n\t"
        "ds_read_b64_tr_b16 v[48:49], v195 offset:36864\n\t"
        "ds_read_b64_tr_b16 v[50:51], v195 offset:37888\n\t"
        "s_waitcnt lgkmcnt(14)\n\t"
        "v_mfma_f32_32x32x16_bf16 v[16:31], v[52:55], v[120:123], v[16:31]\n\t"
        "ds_read_b64_tr_b16 v[52:53], v196 offset:36864\n\t"
        "ds_read_b64_tr_b16 v[54:55], v196 offset:37888\n\t"
        "buffer_load_dwordx4 v[168:171], v192, s[40:43], s44 offen offset:2048 sc1\n\t"
        "s_waitcnt vmcnt(11) lgkmcnt(14)\n\t"
        "v_mfma_f32_32x32x16_bf16 v[0:15], v[56:59], v[124:127], v[0:15]\n\t"
        "ds_read_b64_tr_b16 v[56:57], v195 offset:38912\n\t"
        "ds_read_b64_tr_b16 v[58:59], v195 offset:39936\n\t"
        "s_waitcnt lgkmcnt(14)\n\t"
        "v_mfma_f32_32x32x16_bf16 v[16:31], v[60:63], v[124:127], v[16:31]\n\t"
        "ds_read_b64_tr_b16 v[60:61], v196 offset:38912\n\t"
        "ds_read_b64_tr_b16 v[62:63], v196 offset:39936\n\t"
        "buffer_load_dwordx4 v[172:175], v192, s[40:43], s44 offen offset:3072 sc1\n\t"
        "s_waitcnt vmcnt(11) lgkmcnt(14)\n\t"
        "v_mfma_f32_32x32x16_bf16 v[0:15], v[32:35], v[128:131], v[0:15]\n\t"
        "ds_read_b64_tr_b16 v[32:33], v195 offset:40960\n\t"
        "ds_read_b64_tr_b16 v[34:35], v195 offset:41984\n\t"
        "s_waitcnt lgkmcnt(14)\n\t"
        "v_mfma_f32_32x32x16_bf16 v[16:31], v[36:39], v[128:131], v[16:31]\n\t"
        "ds_read_b64_tr_b16 v[36:37], v196 offset:40960\n\t"
        "ds_read_b64_tr_b16 v[38:39], v196 offset:41984\n\t"
        "s_add_u32 s44, s44, 0x1000\n\t"
        "s_and_b32 s44, s44, 0xffff\n\t"
        "buffer_load_dwordx4 v[176:179], v192, s[40:43], s44 offen offset:0 sc1\n\t"
        "s_waitcnt vmcnt(11) lgkmcnt(14)\n\t"
        "v_mfma_f32_32x32x16_bf16 v[0:15], v[40:43], v[132:135], v[0:15]\n\t"
        "ds_read_b64_tr_b16 v[40:41], v195 offset:43008\n\t"
        "ds_read_b64_tr_b16 v[42:43], v195 offset:44032\n\t"
        "s_waitcnt lgkmcnt(14)\n\t"
        "v_mfma_f32_32x32x16_bf16 v[16:31], v[44:47], v[132:135], v[16:31]\n\t"
        "ds_read_b64_tr_b16 v[44:45], v196 offset:43008\n\t"
        "ds_read_b64_tr_b16 v[46:47], v196 offset:44032\n\t"
        "buffer_load_dwordx4 v[180:183], v192, s[40:43], s44 offen offset:1024 sc1\n\t"
        "s_waitcnt vmcnt(11) lgkmcnt(14)\n\t"
        "v_mfma_f32_32x32x16_bf16 v[0:15], v[48:51], v[136:139], v[0:15]\n\t"
        "ds_read_b64_tr_b16 v[48:49], v195 offset:45056\n\t"
        "ds_read_b64_tr_b16 v[50:51], v195 offset:46080\n\t"
        "s_waitcnt lgkmcnt(14)\n\t"
        "v_mfma_f32_32x32x16_bf16 v[16:31], v[52:55], v[136:139], v[16:31]\n\t"
        "ds_read_b64_tr_b16 v[52:53], v196 offset:45056\n\t"
        "ds_read_b64_tr_b16 v[54:55], v196 offset:46080\n\t"
        "buffer_load_dwordx4 v[184:187], v192, s[40:43], s44 offen offset:2048 sc1\n\t"
        "s_waitcnt vmcnt(11) lgkmcnt(14)\n\t"
        "v_mfma_f32_32x32x16_bf16 v[0:15], v[56:59], v[140:143], v[0:15]\n\t"
        "ds_read_b64_tr_b16 v[56:57], v195 offset:47104\n\t"
        "ds_read_b64_tr_b16 v[58:59], v195 offset:48128\n\t"
        "s_waitcnt lgkmcnt(14)\n\t"
        "v_mfma_f32_32x32x16_bf16 v[16:31], v[60:63], v[140:143], v[16:31]\n\t"
        "ds_read_b64_tr_b16 v[60:61], v196 offset:47104\n\t"
        "ds_read_b64_tr_b16 v[62:63], v196 offset:48128\n\t"
        "buffer_load_dwordx4 v[188:191], v192, s[40:43], s44 offen offset:3072 sc1\n\t"
        "s_waitcnt vmcnt(11) lgkmcnt(14)\n\t"
        "v_mfma_f32_32x32x16_bf16 v[0:15], v[32:35], v[144:147], v[0:15]\n\t"
        "ds_read_b64_tr_b16 v[32:33], v195 offset:49152\n\t"
        "ds_read_b64_tr_b16 v[34:35], v195 offset:50176\n\t"
        "s_waitcnt lgkmcnt(14)\n\t"
        "v_mfma_f32_32x32x16_bf16 v[16:31], v[36:39], v[144:147], v[16:31]\n\t"
        "ds_read_b64_tr_b16 v[36:37], v196 offset:49152\n\t"
        "ds_read_b64_tr_b16 v[38:39], v196 offset:50176\n\t"
        "s_waitcnt vmcnt(10) lgkmcnt(14)\n\t"
        "v_mfma_f32_32x32x16_bf16 v[0:15], v[40:43], v[148:151], v[0:15]\n\t"
        "ds_read_b64_tr_b16 v[40:41], v195 offset:51200\n\t"
        "ds_read_b64_tr_b16 v[42:43], v195 offset:52224\n\t"
        "s_waitcnt lgkmcnt(14)\n\t"
        "v_mfma_f32_32x32x16_bf16 v[16:31], v[44:47], v[148:151], v[16:31]\n\t"
        "ds_read_b64_tr_b16 v[44:45], v196 offset:51200\n\t"
        "ds_read_b64_tr_b16 v[46:47], v196 offset:52224\n\t"
        "s_waitcnt vmcnt(9) lgkmcnt(14)\n\t"
        "v_mfma_f32_32x32x16_bf16 v[0:15], v[48:51], v[152:155], v[0:15]\n\t"
        "ds_read_b64_tr_b16 v[48:49], v195 offset:53248\n\t"
        "ds_read_b64_tr_b16 v[50:51], v195 offset:54272\n\t"
        "s_waitcnt lgkmcnt(14)\n\t"
        "v_mfma_f32_32x32x16_bf16 v[16:31], v[52:55], v[152:155], v[16:31]\n\t"
        "ds_read_b64_tr_b16 v[52:53], v196 offset:53248\n\t"
        "ds_read_b64_tr_b16 v[54:55], v196 offset:54272\n\t"
        "s_waitcnt vmcnt(8) lgkmcnt(14)\n\t"
        "v_mfma_f32_32x32x16_bf16 v[0:15], v[56:59], v[156:159], v[0:15]\n\t"
        "ds_read_b64_tr_b16 v[56:57], v195 offset:55296\n\t"
        "ds_read_b64_tr_b16 v[58:59], v195 offset:56320\n\t"
        "s_waitcnt lgkmcnt(14)\n\t"
        "v_mfma_f32_32x32x16_bf16 v[16:31], v[60:63], v[156:159], v[16:31]\n\t"
        "ds_read_b64_tr_b16 v[60:61], v196 offset:55296\n\t"
        "ds_read_b64_tr_b16 v[62:63], v196 offset:56320\n\t"
        "s_waitcnt vmcnt(7) lgkmcnt(14)\n\t"
        "v_mfma_f32_32x32x16_bf16 v[0:15], v[32:35], v[160:163], v[0:15]\n\t"
        "s_barrier\n\t"
        "ds_read_b64_tr_b16 v[32:33], v195 offset:57344\n\t"
        "ds_read_b64_tr_b16 v[34:35], v195 offset:58368\n\t"
        "s_waitcnt lgkmcnt(14)\n\t"
        "v_mfma_f32_32x32x16_bf16 v[16:31], v[36:39], v[160:163], v[16:31]\n\t"
        "ds_read_b64_tr_b16 v[36:37], v196 offset:57344\n\t"
        "ds_read_b64_tr_b16 v[38:39], v196 offset:58368\n\t"
        "s_waitcnt vmcnt(6) lgkmcnt(14)\n\t"
        "v_mfma_f32_32x32x16_bf16 v[0:15], v[40:43], v[164:167], v[0:15]\n\t"
        "ds_read_b64_tr_b16 v[40:41], v195 offset:59392\n\t"
        "ds_read_b64_tr_b16 v[42:43], v195 offset:60416\n\t"
        "s_waitcnt lgkmcnt(14)\n\t"
        "v_mfma_f32_32x32x16_bf16 v[16:31], v[44:47], v[164:167], v[16:31]\n\t"
        "ds_read_b64_tr_b16 v[44:45], v196 offset:59392\n\t"
        "ds_read_b64_tr_b16 v[46:47], v196 offset:60416\n\t"
        "s_waitcnt vmcnt(5) lgkmcnt(14)\n\t"
        "v_mfma_f32_32x32x16_bf16 v[0:15], v[48:51], v[168:171], v[0:15]\n\t"
        "ds_read_b64_tr_b16 v[48:49], v195 offset:61440\n\t"
        "ds_read_b64_tr_b16 v[50:51], v195 offset:62464\n\t"
        "s_waitcnt lgkmcnt(14)\n\t"
        "v_mfma_f32_32x32x16_bf16 v[16:31], v[52:55], v[168:171], v[16:31]\n\t"
        "ds_read_b64_tr_b16 v[52:53], v196 offset:61440\n\t"
        "ds_read_b64_tr_b16 v[54:55], v196 offset:62464\n\t"
        "s_waitcnt vmcnt(4) lgkmcnt(14)\n\t"
        "v_mfma_f32_32x32x16_bf16 v[0:15], v[56:59], v[172:175], v[0:15]\n\t"
        "ds_read_b64_tr_b16 v[56:57], v195 offset:63488\n\t"
        "ds_read_b64_tr_b16 v[58:59], v195 offset:64512\n\t"
        "s_waitcnt lgkmcnt(14)\n\t"
        "v_mfma_f32_32x32x16_bf16 v[16:31], v[60:63], v[172:175], v[16:31]\n\t"
        "ds_read_b64_tr_b16 v[60:61], v196 offset:63488\n\t"
        "ds_read_b64_tr_b16 v[62:63], v196 offset:64512\n\t"
        "s_waitcnt vmcnt(3) lgkmcnt(14)\n\t"
        "v_mfma_f32_32x32x16_bf16 v[0:15], v[32:35], v[176:179], v[0:15]\n\t"
        "s_waitcnt lgkmcnt(12)\n\t"
        "v_mfma_f32_32x32x16_bf16 v[16:31], v[36:39], v[176:179], v[16:31]\n\t"
        "s_waitcnt vmcnt(2) lgkmcnt(10)\n\t"
        "v_mfma_f32_32x32x16_bf16 v[0:15], v[40:43], v[180:183], v[0:15]\n\t"
        "s_waitcnt lgkmcnt(8)\n\t"
        "v_mfma_f32_32x32x16_bf16 v[16:31], v[44:47], v[180:183], v[16:31]\n\t"
        "s_waitcnt vmcnt(1) lgkmcnt(6)\n\t"
        "v_mfma_f32_32x32x16_bf16 v[0:15], v[48:51], v[184:187], v[0:15]\n\t"
        "s_waitcnt lgkmcnt(4)\n\t"
        "v_mfma_f32_32x32x16_bf16 v[16:31], v[52:55], v[184:187], v[16:31]\n\t"
        "s_waitcnt vmcnt(0) lgkmcnt(2)\n\t"
        "v_mfma_f32_32x32x16_bf16 v[0:15], v[56:59], v[188:191], v[0:15]\n\t"
        "s_waitcnt lgkmcnt(0)\n\t"
        "v_mfma_f32_32x32x16_bf16 v[16:31], v[60:63], v[188:191], v[16:31]\n\t"
        "s_nop 11\n\t"
        : "+{v[0:15]}"(acc[0]), "+{v[16:31]}"(acc[1]), "+{s44}"(soff) STAMP_OUTS
        : "{v192}"(OFF_OF + L * (5 * MBY) + tile * TS + lane * 16), "{v193}"(a0), "{v194}"(a1), "{v195}"(a0 + 65536), "{v196}"(a1 + 65536),
          "{v198}"(lbase + L_W + 20), "{v199}"(L + 1), "{s[40:43]}"(srd)
        : "v32", "v33", "v34", "v35", "v36", "v37", "v38", "v39", "v40", "v41", "v42", "v43", "v44", "v45", "v46", "v47", "v48", "v49", "v50", "v51", "v52", "v53", "v54", "v55", "v56", "v57", "v58", "v59", "v60", "v61", "v62", "v63", "v64", "v65", "v66", "v67", "v68", "v69", "v70", "v71", "v72", "v73", "v74", "v75", "v76", "v77", "v78", "v79", "v80", "v81", "v82", "v83", "v84", "v85", "v86", "v87", "v88", "v89", "v90", "v91", "v92", "v93", "v94", "v95", "v96", "v97", "v98", "v99", "v100", "v101", "v102", "v103", "v104", "v105", "v106", "v107", "v108", "v109", "v110", "v111", "v112", "v113", "v114", "v115", "v116", "v117", "v118", "v119", "v120", "v121", "v122", "v123", "v124", "v125", "v126", "v127", "v128", "v129", "v130", "v131", "v132", "v133", "v134", "v135", "v136", "v137", "v138", "v139", "v140", "v141", "v142", "v143", "v144", "v145", "v146", "v147", "v148", "v149", "v150", "v151", "v152", "v153", "v154", "v155", "v156", "v157", "v158", "v159", "v160", "v161", "v162", "v163", "v164", "v165", "v166", "v167", "v168", "v169", "v170", "v171", "v172", "v173", "v174", "v175", "v176", "v177", "v178", "v179", "v180", "v181", "v182", "v183", "v184", "v185", "v186", "v187", "v188", "v189", "v190", "v191", "v200", "s45", "vcc", "scc", "memory");
      }
      PHASE_VARS;
      STAMP_PUT(12 + 16 * L);
      STAMPV(15 + 16 * L);
      float s = 0.f, sq = 0.f, d = 0.f;
      const unsigned xdst = (L ? OOB : OFF_X1 + tile * TS + tn * 4096) + lane * 16;
#pragma unroll
      for (int i = 0; i < 4; ++i) {
#pragma unroll
        for (int e = 0; e < 4; ++e) x1p[i][e] = pack2(acc[i >> 1][8 * (i & 1) + 2 * e], acc[i >> 1][8 * (i & 1) + 2 * e + 1]);
        stg(rs, xdst + i * 1024, x1p[i]);
        SBAR();
      }
#pragma unroll
      for (int g = 0; g < 8; ++g)
#pragma unroll
        for (int e = 0; e < 2; ++e) {
          const unsigned o = x1p[g >> 1][2 * (g & 1) + e];
          s = dot2(o, 0x3f803f80u, s); sq = dot2(o, o, sq); d = dot2(o, wrc[g][e], d);
        }
      const u32x4 pst = {nzf(half_swap_sum(s)), nzf(half_swap_sum(sq)), nzf(half_swap_sum(d)), 0u};
      asm volatile("buffer_store_dwordx4 %0, %1, %2, 0 offen sc1\n\ts_waitcnt vmcnt(1)\n\tds_write_b32 %3, %4"
                   :: "v"(pst), "v"(stats_dst(sx, tile, tn, lane)), "s"(srd), "v"(lbase + L_W + 64 + 4 * wid), "v"(3) : "memory");
    }
    STAMPV(16 + 16 * L);
  }
  {
    PHASE_VARS;
    float S, Q, Dd, mean, var, mb1, rd1;
    __builtin_amdgcn_s_sleep(13);
    row_stats_sum(rs, OFF_SX + SX_LAYER, tile, lane, S, Q, Dd);
    STAMPV(33);
    row_stats_fin(rs, OFF_SX + SX_LAYER + SX_CAND + (tn * 16 + tm) * 128 + wid * 4, smem, 2, tile, lane, S, Q, Dd, U2F(words[0]), x1p, mean, var, mb1, rd1);
    STAMPV(34);
    u32x4 xo[4];
#pragma unroll
    for (int i = 0; i < 4; ++i) {
#pragma unroll
      for (int e = 0; e < 4; ++e) {
        const unsigned h2 = ln_pair(x1p[i][e], mb1, rd1);
        xo[i][e] = pack2(lo16(x1p[i][e]) + lo16(h2), hi16(x1p[i][e]) + hi16(h2));
      }
      *reinterpret_cast<u32x4*>(smem + wid * 4096 + r32 * 128 + ((2 * i + hi) ^ (r32 & 7)) * 16) = fo_swap(xo[i]);
    }
    asm volatile("s_waitcnt lgkmcnt(0)" ::: "memory");
#pragma unroll
    for (int i = 0; i < 4; ++i) { xo[i] = *reinterpret_cast<const u32x4*>(smem + wid * 4096 + i * 1024 + (lane >> 3) * 128 + ((lane ^ (lane >> 3)) & 7) * 16); PIN(xo[i]); }
    u16* dst = out + (size_t)(tile * 32 + (lane >> 3)) * D_ + tn * 64 + (lane & 7) * 8;
    lds_wait_ge(lbase + L_W + 4 + 4 * (lane & 1), 2);
    const int rel = (int)words[3] - tile * 32 - (lane >> 3);
#pragma unroll
    for (int i = 0; i < 4; ++i)
      if (rel != 8 * i) st16(dst + (size_t)i * 8 * D_, xo[i]);
    STAMPV(35);
    STAMP_END;
  }
}

extern "C" __global__ void end_marker() {}

extern "C" void kernel_launch(void* const* d_in, const int* in_sizes, int n_in, void* d_out, int out_size,
                              void* d_ws, size_t ws_size, hipStream_t stream) {
  char* ws = (char*)d_ws;
  mega_kernel<<<256, 512, 0, stream>>>((const u16*)d_in[0], (const u16*)d_in[1], (const u16*)d_in[2], (const u16*)d_in[4], (const u16*)d_in[5],
                                       (const u16*)d_in[6], (const u16*)d_in[7], ws, (u16*)d_out);
}
```

```cpp
#include <hip/hip_runtime.h>
#include <hip/hip_bf16.h>
#include <stdint.h>

typedef unsigned short u16;
typedef short s16x4 __attribute__((ext_vector_type(4)));
typedef short s16x8 __attribute__((ext_vector_type(8)));
typedef float f32x16 __attribute__((ext_vector_type(16)));
typedef float f32x8 __attribute__((ext_vector_type(8)));
typedef float f32x4 __attribute__((ext_vector_type(4)));
typedef float f32x2 __attribute__((ext_vector_type(2)));
typedef unsigned int u32x16 __attribute__((ext_vector_type(16)));
typedef unsigned int u32x8 __attribute__((ext_vector_type(8)));
typedef unsigned int u32x4 __attribute__((ext_vector_type(4)));
typedef unsigned int u32x2 __attribute__((ext_vector_type(2)));
typedef __bf16 bf16x2 __attribute__((ext_vector_type(2)));
typedef __bf16 bf16x8 __attribute__((ext_vector_type(8)));
typedef __bf16 bf16x16 __attribute__((ext_vector_type(16)));

#define DI __device__ __forceinline__
#define LDS_PTR(T, p) ((__attribute__((address_space(3))) T*)(p))
#define BARRIER() asm volatile("s_barrier" ::: "memory")
#define BAR_LGKM() asm volatile("s_waitcnt lgkmcnt(0)\n\ts_barrier" ::: "memory")
#define BAR_ALL() asm volatile("s_waitcnt vmcnt(0) lgkmcnt(0)\n\ts_barrier" ::: "memory")
#define DRAIN() asm volatile("s_waitcnt vmcnt(0)" ::: "memory")
#define PACE() ({ asm volatile("s_sleep 1\n\ts_waitcnt vmcnt(3)" ::: "memory"); })
#define MFMA32(a, b, c) __builtin_amdgcn_mfma_f32_32x32x16_bf16(a, b, c, 0, 0, 0)
#define MFMA16(a, b, c) __builtin_amdgcn_mfma_f32_16x16x32_bf16(a, b, c, 0, 0, 0)
#define PIN(x) asm volatile("" : "+v"(x))
#define EX2(x) __builtin_amdgcn_exp2f(x)
#define RCP(x) __builtin_amdgcn_rcpf(x)
#define SBAR() __builtin_amdgcn_sched_barrier(0)

#define STAMP_INIT
#define STAMPV(i)
#define STAMP_END
#define STAMP_DECL
#define STAMP_OUTS
#define STAMP_PUT(i)
constexpr int D_ = 1024, F_ = 3584;

constexpr unsigned MBY = 1u << 20;
constexpr unsigned TS = 65536 + 4096, HS = 262144 + 4096;
constexpr unsigned OFF_HF = 0, OFF_X1 = 5 * MBY;
constexpr unsigned OFF_OF = 10 * MBY;
constexpr unsigned OFF_KV = 20 * MBY;
constexpr unsigned OFF_FL = 40 * MBY;
constexpr unsigned FL_EPOCH = OFF_FL;
constexpr unsigned FL_KV = OFF_FL + 4096;
constexpr unsigned FL_O = FL_KV + 65536;
constexpr unsigned FL_X = FL_O + 65536;
constexpr unsigned OFF_CS = 41 * MBY;
constexpr unsigned OFF_SX = 42 * MBY;
constexpr unsigned SX_ST = 0;
constexpr unsigned SX_ROW = 512 * 1024;
constexpr unsigned SX_CAND = SX_ROW + 256 * 1024;
constexpr unsigned SX_GP = SX_CAND + 32 * 1024;
constexpr unsigned SX_YP = SX_GP + 224 * 1024;
constexpr unsigned SX_LAYER = SX_YP + 224 * 1024;
constexpr unsigned SX_QP = 2 * SX_LAYER;
constexpr unsigned SX_BYTES = SX_QP + 512 * 1024;
static_assert(SX_BYTES <= 6 * 256 * 128 * 16, "sentinel fill");
constexpr unsigned WS_BYTES = 48 * MBY, OOB = 0x80000000u;
constexpr unsigned SENT = 0xFFFFFFFFu;

constexpr int NS = 9, DD = 3, SLOTB = 12288;
constexpr int L_XCH = 122880;
constexpr int L_SL = L_XCH + 8192;
constexpr int L_CAND = L_XCH + 32768;
constexpr int L_AB = L_XCH + 34816;
constexpr int L_FULL = 159744, L_FREE = L_FULL + 512;
constexpr int L_CS = L_FREE + 1024, L_PATCH = L_CS + 768;
constexpr int L_GU = L_PATCH + 768, L_TAG = L_GU + 256, L_WR = L_TAG + 256;
constexpr int L_X2F = L_WR + 128, L_X2P = L_X2F + 128;
constexpr int L_W = L_X2P + 128;
constexpr int LDS_BYTES = 163840;
static_assert(NS * SLOTB <= 54 * 2048 && L_W + 128 <= LDS_BYTES && (NS + 1) * SLOTB <= L_XCH && L_AB + 2048 <= L_FULL, "LDS map");

DI int fresh_lane() { int l; asm volatile("v_mbcnt_lo_u32_b32 %0, -1, 0\n\tv_mbcnt_hi_u32_b32 %0, -1, %0" : "=v"(l)); return l; }
#define LANE_VARS const int lane = fresh_lane(), r32 = lane & 31, hi = lane >> 5, tq = (lane & 15) >> 2, tp = lane & 3, tblk = (lane >> 4) & 1, \
  wrow = 4 * hi + tq, wsw = 4 * ((wrow >> 1) & 1), wch = 2 * tblk + (tp >> 1), c24 = r32 < 24 ? r32 : 23; \
  (void)r32; (void)hi; (void)wsw; (void)wch; (void)c24
DI int fresh_s(int v) { asm volatile("" : "+s"(v)); return v; }
#define PHASE_VARS const int tm = fresh_s(tm_), tn = fresh_s(tn_), wid = fresh_s(wid_), tile = tm * 4 + wid, hh = wid & 1, kh = wid >> 1; (void)tile; (void)hh; (void)kh; LANE_VARS
DI float bf2f(u16 v) { return __uint_as_float(((unsigned)v) << 16); }
DI unsigned pack2(float lo, float hi) { f32x2 v = {lo, hi}; return __builtin_bit_cast(unsigned, __builtin_convertvector(v, bf16x2)); }
DI float rbf(float f) { return __uint_as_float(pack2(f, 0.f) << 16); }
DI float lo16(unsigned u) { return __uint_as_float(u << 16); }
DI float hi16(unsigned u) { return __uint_as_float(u & 0xffff0000u); }
DI float opq(float r) { asm("" : "+v"(r)); return r; }
DI float fadd(float a, float b) { return opq(a + b); }
DI float fmul(float a, float b) { return opq(a * b); }
DI float ffma(float a, float b, float c) { return opq(__builtin_fmaf(a, b, c)); }
DI float U2F(unsigned u) { return __uint_as_float(u); }
DI float half_swap_sum(float v) {
  auto r = __builtin_amdgcn_permlane32_swap(__float_as_uint(v), __float_as_uint(v), false, false);
  return fadd(U2F(r[0]), U2F(r[1]));
}
DI float half_swap_max(float v) {
  auto r = __builtin_amdgcn_permlane32_swap(__float_as_uint(v), __float_as_uint(v), false, false);
  return fmaxf(__uint_as_float(r[0]), __uint_as_float(r[1]));
}
template <int CTRL> DI unsigned dpp(unsigned v) { return (unsigned)__builtin_amdgcn_update_dpp(0, (int)v, CTRL, 0xf, 0xf, false); }
DI float row_swap_sum(float v) {
  auto r = __builtin_amdgcn_permlane16_swap(__float_as_uint(v), __float_as_uint(v), false, false);
  return fadd(U2F(r[0]), U2F(r[1]));
}
DI float half_sum(float v) {
  v = fadd(v, U2F(dpp<0xB1>(__float_as_uint(v)))); v = fadd(v, U2F(dpp<0x4E>(__float_as_uint(v))));
  v = fadd(v, U2F(dpp<0x141>(__float_as_uint(v)))); v = fadd(v, U2F(dpp<0x140>(__float_as_uint(v))));
  return row_swap_sum(v);
}
DI float wave_sum(float v) { return half_swap_sum(half_sum(v)); }
DI unsigned half_max_u32(unsigned v) {
  v = max(v, dpp<0xB1>(v)); v = max(v, dpp<0x4E>(v)); v = max(v, dpp<0x141>(v)); v = max(v, dpp<0x140>(v));
  auto r = __builtin_amdgcn_permlane16_swap(v, v, false, false);
  return max(r[0], r[1]);
}
DI unsigned wave_max_u32(unsigned v) {
  v = half_max_u32(v);
  auto r = __builtin_amdgcn_permlane32_swap(v, v, false, false);
  return max(r[0], r[1]);
}
DI u32x4 fo_swap(u32x4 v) {
  auto a = __builtin_amdgcn_permlane32_swap(v[0], v[2], false, false);
  auto b = __builtin_amdgcn_permlane32_swap(v[1], v[3], false, false);
  u32x4 o; o[0] = a[0]; o[1] = b[0]; o[2] = a[1]; o[3] = b[1];
  return o;
}
DI void glds16(const void* gsrc, unsigned lds_dst) {
  unsigned keep;
  asm volatile("s_mov_b32 %0, m0\n\ts_mov_b32 m0, %2\n\ts_nop 0\n\tglobal_load_lds_dwordx4 %1, off\n\ts_mov_b32 m0, %0"
               : "=&s"(keep) : "v"(gsrc), "s"(lds_dst) : "memory");
}
#define GL_ "global_load_lds_dwordx4 "
DI void glds_v4(const void* sb, const unsigned (&v)[4], unsigned m0v) {
  unsigned keep;
  asm volatile("s_mov_b32 %0, m0\n\ts_mov_b32 m0, %6\n\ts_nop 0\n\t" GL_ "%1, %5\n\t" GL_ "%2, %5 offset:1024\n\t" GL_ "%3, %5 offset:2048\n\t"
               GL_ "%4, %5 offset:3072\n\ts_mov_b32 m0, %0" : "=&s"(keep) : "v"(v[0]), "v"(v[1]), "v"(v[2]), "v"(v[3]), "s"(sb), "s"(m0v) : "memory");
}
#define GP_ "\n\ts_sleep %8\n\ts_waitcnt vmcnt(%7)\n\t"
template <int TH, int SL> DI void glds_v4t(const void* sb, const unsigned (&v)[4], unsigned m0v) {
  unsigned keep;
  asm volatile("s_mov_b32 %0, m0\n\ts_mov_b32 m0, %6\n\ts_nop 0\n\t" GL_ "%1, %5" GP_ GL_ "%2, %5 offset:1024" GP_ GL_ "%3, %5 offset:2048" GP_ GL_ "%4, %5 offset:3072" GP_ "s_mov_b32 m0, %0"
               : "=&s"(keep) : "v"(v[0]), "v"(v[1]), "v"(v[2]), "v"(v[3]), "s"(sb), "s"(m0v), "n"(TH), "n"(SL) : "memory");
}
DI void glds_v3(const void* sb, const unsigned (&v)[3], unsigned m0v) {
  unsigned keep;
  asm volatile("s_mov_b32 %0, m0\n\ts_mov_b32 m0, %5\n\ts_nop 0\n\t" GL_ "%1, %4\n\t" GL_ "%2, %4 offset:1024\n\t" GL_ "%3, %4 offset:2048\n\t"
               "s_mov_b32 m0, %0" : "=&s"(keep) : "v"(v[0]), "v"(v[1]), "v"(v[2]), "s"(sb), "s"(m0v) : "memory");
}
DI float dot2(unsigned a, unsigned b, float c) {
  return __builtin_amdgcn_fdot2_f32_bf16(__builtin_bit_cast(bf16x2, a), __builtin_bit_cast(bf16x2, b), c, false);
}
#define CODE_ADDR(sym, out) do { unsigned long long pc_; unsigned off_; \
  asm volatile("s_getpc_b64 %0\n1:\n\ts_mov_b32 %1, " #sym "-1b" : "=s"(pc_), "=s"(off_)); out = (const char*)(pc_ + (long long)(int)off_); } while (0)
DI void st16(void* p, u32x4 v) { asm volatile("global_store_dwordx4 %0, %1, off sc0 sc1\n\ts_nop 1" :: "v"(p), "v"(v) : "memory"); }
DI void ln_scalars(float mean, float var, float& mb, float& den) { mb = rbf(mean); den = rbf(__builtin_amdgcn_sqrtf(rbf(rbf(var) + 1e-5f))); }
DI unsigned ln_pair(unsigned v, float mb, float rd) {
  const unsigned c = pack2(lo16(v) - mb, hi16(v) - mb);
  return pack2(lo16(c) * rd, hi16(c) * rd);
}
DI unsigned ln_pair_s(unsigned v, float nmb, float rd) {
  const unsigned c = pack2(fadd(lo16(v), nmb), fadd(hi16(v), nmb));
  return pack2(fmul(lo16(c), rd), fmul(hi16(c), rd));
}
template <int N, int M> DI f32x4 sum4(const u32x4 (&v)[M]) {
  f32x4 t;
#pragma unroll
  for (int e = 0; e < 4; ++e) {
    t[e] = U2F(v[0][e]);
#pragma unroll
    for (int i = 1; i < N; ++i) t[e] = fadd(t[e], U2F(v[i][e]));
  }
  return t;
}
#define PERM_LO(x, y) __builtin_amdgcn_perm(y, x, 0x05040100u)
#define PERM_HI(x, y) __builtin_amdgcn_perm(y, x, 0x07060302u)
constexpr unsigned SPIN_CAP = 1u << 15;
DI unsigned nz(unsigned v) { return min(v, SENT - 1u); }
DI unsigned nzf(float v) { return nz(__float_as_uint(v)); }
DI unsigned mx4(unsigned m, const u32x4& v) { return max(max(max(m, v[0]), v[1]), max(v[2], v[3])); }
template <int N> DI bool none_sent(const u32x4 (&v)[N]) {
  unsigned m = 0u;
#pragma unroll
  for (int i = 0; i < N; ++i) m = mx4(m, v[i]);
  return m != SENT;
}
typedef __amdgpu_buffer_rsrc_t rsrc_t;
DI u32x4 ldg(rsrc_t rs, unsigned off) { return __builtin_amdgcn_raw_buffer_load_b128(rs, (int)off, 0, 16); }
DI void stg(rsrc_t rs, unsigned off, u32x4 v) { __builtin_amdgcn_raw_buffer_store_b128(v, rs, (int)off, 0, 16); }
DI void poll_pause(bool slow) { if (slow) __builtin_amdgcn_s_sleep(4); asm volatile("s_sleep 1" ::: "memory"); }
template <int N, class LD, class OKF> DI void poll_all(u32x4 (&out)[N], bool slow, LD ld, OKF okf) {
  for (unsigned spins = 0;; ++spins) {
    ld(out);
    SBAR();
    if (__all(okf(out)) || spins > SPIN_CAP) break;
    poll_pause(slow);
  }
}
DI void lds_set(unsigned addr, unsigned v) { asm volatile("s_waitcnt lgkmcnt(0)\n\tds_write_b32 %0, %1" :: "v"(addr), "v"(v) : "memory"); }
template <int SLEEP = 1> DI void lds_wait_ge(unsigned addr, unsigned need) {
  unsigned spin = 0x100000, v;
  asm volatile("1:\n\tds_read_b32 %0, %2\n\ts_sub_u32 %1, %1, 1\n\ts_waitcnt lgkmcnt(0)\n\ts_cbranch_scc1 2f\n\tv_cmp_gt_u32 vcc, %3, %0\n\ts_cbranch_vccz 2f\n\ts_sleep %4\n\ts_branch 1b\n2:"
               : "=&v"(v), "+s"(spin) : "v"(addr), "s"(need), "n"(SLEEP) : "vcc", "scc", "memory");
}
DI s16x8 tr2(const char* p, int second) {
  s16x4 lo = __builtin_amdgcn_ds_read_tr16_b64_v4i16(LDS_PTR(s16x4, p)), hi = __builtin_amdgcn_ds_read_tr16_b64_v4i16(LDS_PTR(s16x4, p + second));
  return __builtin_shufflevector(lo, hi, 0, 1, 2, 3, 4, 5, 6, 7);
}
DI u32x16 cat4(const u32x4& a, const u32x4& b, const u32x4& c, const u32x4& d) {
  const u32x8 x = __builtin_shufflevector(a, b, 0, 1, 2, 3, 4, 5, 6, 7), y = __builtin_shufflevector(c, d, 0, 1, 2, 3, 4, 5, 6, 7);
  return __builtin_shufflevector(x, y, 0, 1, 2, 3, 4, 5, 6, 7, 8, 9, 10, 11, 12, 13, 14, 15);
}

DI unsigned stats_dst(unsigned sx, int tile, int tn, int lane) { return lane < 32 ? sx + SX_ST + tile * 8192 + tn * 512 + lane * 16 : OOB; }
DI void row_stats_sum(rsrc_t rs, unsigned sx, int tile, int lane, float& S, float& Q, float& Dd) {
  const unsigned exb = sx + SX_ST + tile * 8192 + (lane & 31) * 16;
  u32x4 pv[8];
  poll_all<8>(pv, false, [&](u32x4 (&v)[8]) {
#pragma unroll
    for (int i = 0; i < 8; ++i) v[i] = ldg(rs, exb + ((lane >> 5) * 8 + i) * 512);
  }, [&](const u32x4 (&v)[8]) { return none_sent(v); });
  S = 0.f; Q = 0.f; Dd = 0.f;
#pragma unroll
  for (int i = 0; i < 8; ++i) { S += __uint_as_float(pv[i][0]); Q += __uint_as_float(pv[i][1]); Dd += __uint_as_float(pv[i][2]); }
}
DI void row_stats_fin(rsrc_t rs, unsigned kdst, char* smem, int epoch, int tile, int lane, float S, float Q, float Dd, float wsum, const u32x4 (&x1p)[4],
                      float& mean, float& var, float& mb, float& rd) {
  S = half_swap_sum(S); Q = half_swap_sum(Q); Dd = half_swap_sum(Dd);
  mean = S * (1.0f / 1024.0f); var = fmaxf(Q * (1.0f / 1024.0f) - mean * mean, 0.f);
  float den;
  ln_scalars(mean, var, mb, den);
  rd = RCP(den);
  const unsigned rb = pack2((Dd - mb * wsum) * rd, 0.f) & 0xffffu;
  const unsigned mine = (((rb & 0x8000u) ? (~rb & 0xffffu) : (rb | 0x8000u)) << 16) | (unsigned)(2047 - (tile * 32 + (lane & 31)));
  const bool best = half_max_u32(mine) == mine;
  __builtin_amdgcn_raw_buffer_store_b32(mine, rs, (best && lane < 32) ? kdst : OOB, 0, 16);
  if (best) {
    char* c = smem + L_CAND + (tile & 3) * 144;
#pragma unroll
    for (int i = 0; i < 4; ++i) *reinterpret_cast<u32x4*>(c + (2 * i + (lane >> 5)) * 16) = x1p[i];
    *reinterpret_cast<u32x4*>(c + 128) = u32x4{mine, __float_as_uint(mb), __float_as_uint(rd), 0u};
  }
  lds_set((unsigned)(uintptr_t)smem + L_W + 32 + (tile & 3) * 4, epoch);
}

DI void attn_safe(rsrc_t rs, unsigned kvoff, const u32x8& q, const u32x4& ones, f32x16& o) {
  const s16x8 q0 = __builtin_bit_cast(s16x8, __builtin_shufflevector(q, q, 0, 1, 2, 3)), q1 = __builtin_bit_cast(s16x8, __builtin_shufflevector(q, q, 4, 5, 6, 7));
  f32x4 l = {0.f, 0.f, 0.f, 0.f};
#pragma unroll
  for (int e = 0; e < 16; ++e) o[e] = 0.f;
  float m = -INFINITY;
#pragma unroll 1
  for (int kt = 0; kt < 64; ++kt) {
    const unsigned p = kvoff + kt * 4096;
    const u32x4 k0 = ldg(rs, p), k1 = ldg(rs, p + 1024), v0 = ldg(rs, p + 2048), v1 = ldg(rs, p + 3072);
    f32x16 s = {};
    s = MFMA32(__builtin_bit_cast(s16x8, k0), q0, s);
    s = MFMA32(__builtin_bit_cast(s16x8, k1), q1, s);
    float mt = fmaxf(s[0], s[1]);
#pragma unroll
    for (int e = 2; e < 16; ++e) mt = fmaxf(mt, s[e]);
    const float mn = fmaxf(m, half_swap_max(mt));
    const float alpha = EX2(m - mn);
    m = mn;
#pragma unroll
    for (int e = 0; e < 16; ++e) { s[e] = EX2(s[e] - mn); o[e] *= alpha; }
    l[0] *= alpha;
    const bf16x16 pa = __builtin_bit_cast(bf16x16, __builtin_shufflevector(s, s, 0, 1, 2, 3, 4, 5, 6, 7));
    const bf16x16 pb = __builtin_bit_cast(bf16x16, __builtin_shufflevector(s, s, 8, 9, 10, 11, 12, 13, 14, 15));
    o = __builtin_amdgcn_smfmac_f32_32x32x32_bf16(__builtin_bit_cast(bf16x8, v0), pa, o, 0xdddd, 0, 0);
    l = __builtin_amdgcn_smfmac_f32_16x16x64_bf16(__builtin_bit_cast(bf16x8, ones), pa, l, 0xdddd, 0, 0);
    o = __builtin_amdgcn_smfmac_f32_32x32x32_bf16(__builtin_bit_cast(bf16x8, v1), pb, o, 0xdddd, 0, 0);
    l = __builtin_amdgcn_smfmac_f32_16x16x64_bf16(__builtin_bit_cast(bf16x8, ones), pb, l, 0xdddd, 0, 0);
  }
  o *= 1.0f / l[0];
}

extern "C" __global__ __launch_bounds__(512) void mega_kernel(const u16* __restrict__ x0, const u16* __restrict__ wqkv, const u16* __restrict__ wo,
                                                              const u16* __restrict__ w_r, const u16* __restrict__ wg, const u16* __restrict__ wu,
                                                              const u16* __restrict__ wd, char* ws, u16* __restrict__ out) {
  __shared__ __attribute__((aligned(16))) char smem[LDS_BYTES];
  const int wid = __builtin_amdgcn_readfirstlane(threadIdx.x >> 6);
  STAMP_INIT;
  STAMPV(0);
  const int bid = blockIdx.x, xcd = bid & 7, loc = bid >> 3;
  const int tm = 8 * (xcd >> 2) + (loc >> 2), tn = 4 * (xcd & 3) + (loc & 3);
  const unsigned lbase = (unsigned)(uintptr_t)smem;
  const rsrc_t rs = __builtin_amdgcn_make_buffer_rsrc(ws, 0, (int)WS_BYTES, 0x00020000);
  volatile __attribute__((address_space(3))) unsigned* words = LDS_PTR(volatile unsigned, smem + L_W);
  *reinterpret_cast<unsigned*>(smem + L_FULL + threadIdx.x * 4) = 0u;
  words[threadIdx.x & 31] = 0u;
  BAR_LGKM();
  const int lw = wid & 1;
  const char* wb = reinterpret_cast<const char*>(wqkv + (size_t)(16 * lw) * (3 * D_) + tn * 64);
  auto stage_lanes = [](unsigned (&wv)[3]) {
    const int lane = fresh_lane();
#pragma unroll
    for (int i = 0; i < 3; ++i) {
      const int li = lane + 64 * i, k = li / 24, cp = li % 24, c = cp ^ (4 * ((k >> 1) & 1));
      wv[i] = (unsigned)(k * (3 * D_ * 2) + (c >> 3) * (D_ * 2) + (c & 7) * 16 - i * 1024);
    }
  };
  auto stage = [&](const unsigned (&wv)[3], int s, int slot) {
    const char* p = wb + (size_t)((s + 2 * tn) & 31) * (32 * 3 * D_ * 2);
    glds_v3(p, wv, lbase + slot * SLOTB + lw * 6144);
    glds_v3(p + 8 * (3 * D_ * 2), wv, lbase + slot * SLOTB + lw * 6144 + 3072);
  };
  auto load_tag = [&]() {
    u32x2 t = __builtin_amdgcn_raw_buffer_load_b64(rs, FL_EPOCH, 0, 16);
    t[0] += 1u; t[1] += 0x9E3779B9u;
    PIN(t);
    return t;
  };
  auto exchange = [&](const u32x2& tag, unsigned line0, int me, bool bar = false) {
    const int lane = fresh_lane();
    __builtin_amdgcn_raw_buffer_store_b64(tag, rs, lane == 0 ? line0 + me * 128 : OOB, 0, 16);
    if (bar) BARRIER();
    if (1) {
      unsigned po = lane < 16 ? line0 + lane * 128 : OOB;
      const u32x4 srd = {(unsigned)(uintptr_t)ws, (unsigned)((uintptr_t)ws >> 32) & 0xffffu, WS_BYTES, 0x00020000u};
      for (unsigned spins = 0;; ++spins) {
        u32x2 v = {0u, 0u};
        asm volatile("buffer_atomic_or_x2 %0, %1, %2, 0 offen sc0 sc1\n\ts_waitcnt vmcnt(0)" : "+v"(v) : "v"(po), "s"(srd) : "memory");
        po = ((v[0] == tag[0]) & (v[1] == tag[1])) ? OOB : po;
        if (__all(po == OOB) || spins > SPIN_CAP) break;
      }
      return;
    }
    __builtin_amdgcn_s_sleep(8);
    for (unsigned spins = 0;; ++spins) {
      const u32x2 v = __builtin_amdgcn_raw_buffer_load_b64(rs, line0 + (lane & 15) * 128, 0, 16);
      if (__all((v[0] == tag[0]) & (v[1] == tag[1])) || spins > SPIN_CAP) break;
      asm volatile("s_sleep 1" ::: "memory");
    }
  };
  if (wid >= 4) {
    auto wo_rows = [&](int g0, int n, int gs) {
      const int lane = fresh_lane();
      unsigned wov[4];
#pragma unroll
      for (int i = 0; i < 4; ++i) wov[i] = (unsigned)((8 * i + (lane >> 3)) * (D_ * 2) + ((lane & 7) ^ (4 * ((lane >> 4) & 1))) * 16 - i * 1024);
#pragma unroll 1
      for (int i = 0, k = (2 * (tm & 7)) % n, g; i < n && !0; ++i, k = k + 1 == n ? 0 : k + 1) g = g0 + gs * k, glds_v4t<3, 1>(reinterpret_cast<const char*>(wo) + (size_t)((((tn >> 2) + 1 + (g >> 3)) & 3) * 256 + ((0 * 64 * tn + 32 * g) & 255)) * (D_ * 2) + tn * 128, wov, lbase + g * 4096);
    };
    if (wid < 6) {
      const int lane = fresh_lane();
      unsigned wv[3];
      stage_lanes(wv);
      __builtin_amdgcn_s_setprio(3);
      const unsigned fra = lbase + L_FREE + (lane & 3) * 256, fla = lbase + L_FULL + lw * 256 + lane * 4;
      const rsrc_t rw = __builtin_amdgcn_make_buffer_rsrc(const_cast<char*>(wb), 0, 0 ? 0 : 6 * MBY - (16 * lw * 3 * D_ + tn * 64) * 2, 0x00020000);
#pragma unroll 1
      for (int L = 0; L < 2; ++L) {
        const int s0 = NS, lead = L ? 8 : NS - 1;
        int slot = 0;
        if (L == 0) {
#pragma unroll 1
          for (int i = 0, s = tm & 7; i < 2 * NS; ++i) {
            glds_v3(wb + (size_t)((s + 2 * tn) & 31) * (32 * 3 * D_ * 2) + (i & 1) * (8 * 3 * D_ * 2), wv, lbase + s * SLOTB + lw * 6144 + (i & 1) * 3072);
            asm volatile("s_waitcnt vmcnt(6)" ::: "memory");
            if (i & 1) s = s + 1 == NS ? 0 : s + 1;
          }
          asm volatile("s_waitcnt vmcnt(0)\n\tds_write_b32 %0, %1" :: "v"(fla), "v"(NS) : "memory");
          STAMPV(1);
        }
#pragma unroll 1
        for (int s = NS; s < 32; ++s) {
          lds_wait_ge(fra, 32 * L + s - lead);
          stage(wv, s, slot);
          slot = (slot == NS - 1) ? 0 : slot + 1;
          if (s >= s0 + DD) asm volatile("s_waitcnt vmcnt(%0)\n\tds_write_b32 %1, %2" :: "n"(DD * 6), "v"(fla), "v"(32 * L + s - DD + 1) : "memory");
        }
#pragma unroll
        for (int d = DD - 1; d >= 0; --d) asm volatile("s_waitcnt vmcnt(%0)\n\tds_write_b32 %1, %2" :: "n"(d * 6), "v"(fla), "v"(32 * L + 32 - d) : "memory");
        u32x2 tag = *reinterpret_cast<const u32x2*>(smem + L_TAG);
        PIN(tag);
        if (lw == 0) {
          lds_wait_ge(lbase + L_W + 96 + 4 * (lane & 1), 1);
          lds_wait_ge<0>(lbase + L_W + 80 + 4 * (lane & 3), 2 * L + 1);
          exchange(tag, FL_KV + (L * 16 + tn) * 2048, tm, true);
          lds_wait_ge(fra, 32 * L + 32);
          if (0) { const unsigned long long t0 = __builtin_amdgcn_s_memrealtime(); while (__builtin_amdgcn_s_memrealtime() < t0 + 0) __builtin_amdgcn_s_sleep(2); }
          lds_set(lbase + L_W + 16, L + 1);
        } else { BAR_LGKM(); lds_wait_ge<8>(lbase + L_W + 16, L + 1); }
        __builtin_amdgcn_s_sleep(24);
        wo_rows(lw, 15, 2);
        u32x4 pre[NS * 6];
        if (L == 0) {
#pragma unroll
          for (int s = 0; s < NS; ++s) {
#pragma unroll
            for (int i = 0; i < 6; ++i)
              pre[s * 6 + i] = __builtin_amdgcn_raw_buffer_load_b128(rw, wv[i % 3], ((s + 2 * tn) & 31) * (32 * 3 * D_ * 2) + (i / 3) * (8 * 3 * D_ * 2) + (i % 3) * 1024, 0), PACE();
          }
        } else {
#pragma unroll
          for (int i = 0; i < NS * 6; ++i) asm volatile("" : "=v"(pre[i]));
        }
        DRAIN();
        const unsigned fo = FL_O + (L * 16 + tm) * 2048;
        if (lw) {
          lds_set(lbase + L_W + 112, 2 * L + 1);
          lds_wait_ge<4>(lbase + L_W + 48 + 4 * (lane & 3), L + 1);
          lds_wait_ge<0>(lbase + L_W + 80 + 4 * (lane & 3), 2 * L + 2);
          __builtin_amdgcn_raw_buffer_store_b64(tag, rs, lane == 0 ? fo + tn * 128 : OOB, 0, 16);
          wo_rows(30, 2, 1);
          DRAIN();
          lds_set(lbase + L_W + 112, 2 * L + 2);
        } else {
          lds_wait_ge<4>(lbase + L_W + 112, 2 * L + 1);
          lds_wait_ge<4>(lbase + L_W + 48 + 4 * (lane & 3), L + 1);
          unsigned po = lane < 16 ? fo + ((lane + 4 + (tn & 12)) & 15) * 128 : OOB;
          const u32x4 srd = {(unsigned)(uintptr_t)ws, (unsigned)((uintptr_t)ws >> 32) & 0xffffu, WS_BYTES, 0x00020000u};
          for (unsigned spins = 0;; ++spins) {
            u32x2 v = {0u, 0u};
            asm volatile("buffer_atomic_or_x2 %0, %1, %2, 0 offen sc0 sc1\n\ts_waitcnt vmcnt(0)" : "+v"(v) : "v"(po), "s"(srd) : "memory");
            po = ((v[0] == tag[0]) & (v[1] == tag[1])) ? OOB : po;
            unsigned m = (unsigned)__ballot(po == OOB);
            m &= m >> 1; m &= m >> 2;
            m = (m & 1u) | ((m >> 3) & 2u) | ((m >> 6) & 4u) | ((m >> 9) & 8u);
            if (m == 15u || spins > SPIN_CAP) break;
            words[5 + 2 * L] = words[28] == 2u * L + 2u ? m : m & 7u;
          }
          words[5 + 2 * L] = words[28] == 2u * L + 2u ? 15u : 7u;
          lds_wait_ge<0>(lbase + L_W + 112, 2 * L + 2);
          words[5 + 2 * L] = 15u;
          __builtin_amdgcn_raw_buffer_store_b64(tag, rs, (bid == 0 && lane == 0) ? FL_EPOCH : OOB, 0, 16);
        }
        BAR_ALL();
        if (L == 0) {
          char* d = smem + lw * 6144 + fresh_lane() * 16;
#pragma unroll
          for (int s = 0; s < NS; ++s) {
#pragma unroll
            for (int i = 0; i < 6; ++i) *reinterpret_cast<u32x4*>(d + s * SLOTB + i * 1024) = pre[s * 6 + i];
            lds_set(fla, 32 + s + 1);
          }
          STAMPV(2);
          lds_wait_ge<2>(lbase + L_W + 24, 2);
        }
      }
      STAMP_END;
      return;
    }
    const int a = wid - 6;
    const bool ffn = loc < 28;
    const int grp = xcd * 7 + (ffn ? loc >> 2 : 0), qa = (loc & 3) * 2 + a;
    const int kr = loc, cg = xcd;
    const unsigned wa = lbase + L_W;
    if (a) {
      const int lane = fresh_lane();
      unsigned t = __builtin_amdgcn_raw_buffer_load_b32(rs, 1 ? lane * MBY + MBY - 128 : OOB, 0, 16);
      if (1) {
        const char *c0, *c1;
        CODE_ADDR(mega_kernel, c0); CODE_ADDR(end_marker, c1);
        const long long o = (loc * 64 + lane) * 32;
        t |= *reinterpret_cast<const __attribute__((address_space(1))) unsigned*>((uintptr_t)(c0 + (o + 4 <= c1 - c0 ? o : 0)));
      }
      const u32x2 tag = load_tag();
      *reinterpret_cast<u32x2*>(smem + L_TAG) = tag;
      PIN(t);
      lds_wait_ge<0>(wa + 64 + 4 * (lane & 3), 2);
      exchange(tag, FL_X + tm * 2048, tn);
      lds_set(wa + 24, 1);
    } else {
      unsigned wv[3];
      stage_lanes(wv);
#pragma unroll 1
      for (int i = 0; i < 4 * 0; ++i) {
        glds_v3(reinterpret_cast<const char*>(wqkv + tn * 64) + (size_t)((4 * (NS + 2 * tn + 3 * (tm & 7)) + i) & 127) * (8 * 3 * D_ * 2), wv, lbase + NS * SLOTB);
        asm volatile("s_waitcnt vmcnt(6)" ::: "memory");
      }
      STAMPV(11);
      lds_wait_ge<2>(wa + 24, 1);
    }
    __builtin_amdgcn_s_sleep(16);
    auto transpose8 = [](u32x4* v) {
      u32x4 t[8];
#pragma unroll
      for (int j = 0; j < 8; ++j)
#pragma unroll
        for (int e = 0; e < 4; ++e) t[j][e] = (j & 1) ? PERM_HI(v[2 * e][j >> 1], v[2 * e + 1][j >> 1]) : PERM_LO(v[2 * e][j >> 1], v[2 * e + 1][j >> 1]);
#pragma unroll
      for (int j = 0; j < 8; ++j) { v[j] = t[j]; PIN(v[j]); }
    };
    u32x4 gf[32];
    u32x4 wdf[16];
    auto wait_go = [&](int L) {
      lds_wait_ge<8>(lbase + L_W + 16, L + 1);
      __builtin_amdgcn_s_sleep(24);
    };
    auto load_wd = [&](unsigned dv) {
      const rsrc_t rd = __builtin_amdgcn_make_buffer_rsrc(const_cast<u16*>(wd) + (size_t)grp * 64 * D_ + 128 * qa, 0, 0 ? 0 : 64 * D_ * 2 - 256 * qa, 0x00020000);
#pragma unroll
      for (int i = 0; i < 16; ++i) {
        wdf[i] = __builtin_amdgcn_raw_buffer_load_b128(rd, dv, (32 * (i >> 3) + (i & 7)) * (D_ * 2), 0);
        asm volatile("s_sleep 2\n\ts_waitcnt vmcnt(3)" ::: "memory");
      }
    };
    {
      LANE_VARS;
      const u32x4 sv = {SENT, SENT, SENT, SENT};
#pragma unroll
      for (int i = 0; i < 6; ++i) {
        const unsigned o = ((i * 256 + bid) * 128 + a * 64 + lane) * 16;
        stg(rs, o < SX_BYTES ? OFF_SX + o : OOB, sv);
      }
      if (a == 0) {
        const u32x4 w0 = *reinterpret_cast<const u32x4*>(w_r + lane * 8), w1 = *reinterpret_cast<const u32x4*>(w_r + 512 + lane * 8);
        float t = 0.f;
#pragma unroll
        for (int e = 0; e < 4; ++e) t = fadd(fadd(t, fadd(lo16(w0[e]), hi16(w0[e]))), fadd(lo16(w1[e]), hi16(w1[e])));
        words[0] = __float_as_uint(wave_sum(t));
        if ((lane >> 3) == (tn & 7)) *reinterpret_cast<u32x4*>(smem + L_WR + (lane & 7) * 16) = tn < 8 ? w0 : w1;
      }
      const u16* p = wqkv + (size_t)(64 * tm + 32 * a + hi) * (3 * D_) + (c24 >> 3) * D_ + tn * 64 + (c24 & 7) * 8;
      float cs[8] = {0.f, 0.f, 0.f, 0.f, 0.f, 0.f, 0.f, 0.f};
      u32x4 v[16];
#pragma unroll
      for (int i = 0; i < 16; ++i) v[i] = *reinterpret_cast<const u32x4*>(p + (size_t)i * (2 * 3 * D_));
      SBAR();
#pragma unroll
      for (int i = 0; i < 16; ++i)
#pragma unroll
        for (int e = 0; e < 4; ++e) { cs[2 * e] = fadd(cs[2 * e], lo16(v[i][e])); cs[2 * e + 1] = fadd(cs[2 * e + 1], hi16(v[i][e])); }
      u32x4 o;
#pragma unroll
      for (int e = 0; e < 4; ++e) { const float x = half_swap_sum(cs[e]), y = half_swap_sum(cs[4 + e]); o[e] = __float_as_uint(hi ? y : x); }
      stg(rs, r32 < 24 ? OFF_CS + (tn * 32 + tm * 2 + a) * 768 + r32 * 32 + hi * 16 : OOB, o);
    }
    {
      DRAIN();
      lds_set(wa + 96 + 4 * a, 1);
      BAR_LGKM();
      LANE_VARS;
      const char* gp = reinterpret_cast<const char*>(((lane & 8) ? wu : wg) + (size_t)(128 * qa + 16 * hi + 4 * tblk) * F_ + 64 * grp + (lane & 7) * 8);
      unsigned dv = (lane >> 4) * (8 * D_ * 2) + (lane & 15) * 16;
      PIN(gp); PIN(dv);
      unsigned sv[3];
#pragma unroll
      for (int i = 0; i < 3; ++i) { const int li = lane + 64 * i, k = li / 24, c = li % 24; sv[i] = (unsigned)(k * (3 * D_ * 2) + (c >> 3) * (D_ * 2) + (c & 7) * 16 - i * 1024); PIN(sv[i]); }
      wait_go(0);
#pragma unroll
      for (int i = 0; i < 32; ++i) {
        gf[i] = *reinterpret_cast<const u32x4*>(gp + (size_t)(0 ? 0 : 32 * (i >> 3) + 8 * ((i >> 2) & 1) + (i & 3)) * (F_ * 2));
        asm volatile("s_sleep 2\n\ts_waitcnt vmcnt(3)" ::: "memory");
      }
      load_wd(dv);
      STAMPV(7);
      lds_wait_ge<4>(wa + 80 + 4 * (lane & 3), 2);
#pragma unroll
      for (int g = 0; g < 4; ++g) glds_v3(wqkv + (size_t)(32 * kr + 8 * g) * (3 * D_) + (2 * cg + a) * 64, sv, lbase + L_SL + a * 12288 + g * 3072);
      transpose8(gf); transpose8(gf + 8); transpose8(gf + 16); transpose8(gf + 24);
      transpose8(wdf); transpose8(wdf + 8);
      {
        LANE_VARS;
        f32x4 t[2];
#pragma unroll
        for (int c = 0; c < 2; ++c) {
          u32x4 v[8];
#pragma unroll
          for (int i = 0; i < 8; ++i) v[i] = ldg(rs, OFF_CS + (tn * 32 + 16 * c + 2 * i + hi) * 768 + a * 384 + c24 * 16);
          SBAR();
          t[c] = sum4<8>(v);
          SBAR();
        }
#pragma unroll
        for (int e = 0; e < 4; ++e) t[0][e] = half_swap_sum(fadd(t[0][e], t[1][e]));
        if (lane < 24) *reinterpret_cast<f32x4*>(smem + L_CS + a * 384 + lane * 16) = t[0];
      }
      u32x2 tag = *reinterpret_cast<const u32x2*>(smem + L_TAG);
      PIN(tag);
      BAR_ALL();
      if (a) {
        lds_wait_ge<0>(wa + 64 + 4 * (lane & 3), 3);
        exchange(tag, FL_X + (16 + tm) * 2048, tn);
        lds_set(wa + 24, 2);
        STAMPV(12);
      }
    }
#pragma unroll 1
    for (int L = fresh_s(0);; ++L) {
      const unsigned sx = OFF_SX + L * SX_LAYER;
      STAMPV(1 + 16 * L);
      if (0 && L == 0) { words[3] = 5000u; lds_set(wa + 4 + 4 * a, 1); } else {
      u32x4 hf[4];
      unsigned key;
      { LANE_VARS;
      lds_wait_ge<0>(wa + 32 + 4 * tp, L + 1);
      if (a == 0) {
        const char* cp = smem + L_CAND + (r32 >> 3) * 144;
        const u32x4 c = *reinterpret_cast<const u32x4*>(cp + (lane & 7) * 16), rec = *reinterpret_cast<const u32x4*>(cp + 128);
        u32x4 o;
#pragma unroll
        for (int e = 0; e < 4; ++e) { unsigned h = ln_pair_s(c[e], -U2F(rec[1]), U2F(rec[2])); PIN(h); o[e] = nz(hi ? c[e] : h); }
        stg(rs, sx + SX_ROW + hi * 131072 + (tm * 4 + (r32 >> 3)) * 2048 + tn * 128 + (lane & 7) * 16, o);
        STAMPV(8 + 16 * L);
        lds_wait_ge<0>(wa + 104, L + 1);
        key = __builtin_amdgcn_readfirstlane(words[27]);
      } else {
        const u32x4 srd = {(unsigned)(uintptr_t)ws, (unsigned)((uintptr_t)ws >> 32) & 0xffffu, WS_BYTES, 0x00020000u};
        unsigned po = sx + SX_CAND + (tn * 16 + (lane >> 2)) * 128 + tp * 4;
        key = 0u;
        for (unsigned spins = 0;; ++spins) {
          unsigned v = 0u;
          asm volatile("buffer_atomic_or %0, %1, %2, 0 offen sc0 sc1\n\ts_waitcnt vmcnt(0)" : "+v"(v) : "v"(po), "s"(srd) : "memory");
          key = v == SENT ? key : max(key, v);
          po = v == SENT ? po : OOB;
          if (__all(po == OOB) || spins > SPIN_CAP) break;
        }
        key = __builtin_amdgcn_readfirstlane(wave_max_u32(key));
        words[27] = key;
        words[3] = 2047u - (key & 2047u);
        lds_set(wa + 104, L + 1);
        STAMPV(13 + 16 * L);
      }
      poll_all<4>(hf, false, [&](u32x4 (&t)[4]) {
#pragma unroll
        for (int i = 0; i < 4; ++i) t[i] = ldg(rs, sx + SX_ROW + ((2047 - (int)(key & 2047u)) >> 5) * 2048 + (16 * qa + 4 * i + (lane >> 4)) * 16);
      }, [&](const u32x4 (&t)[4]) { return none_sent(t); });
      if (L == 1) lds_set(wa + 4 + 4 * a, 2);
      }
      STAMPV(2 + 16 * L);
      f32x4 d[8];
      const f32x4 zero = {0.f, 0.f, 0.f, 0.f};
      SBAR();
      __builtin_amdgcn_s_setprio(3);
#pragma unroll
      for (int t = 0; t < 32; ++t) d[t & 7] = MFMA16(__builtin_bit_cast(s16x8, hf[t >> 3]), __builtin_bit_cast(s16x8, gf[t]), t < 8 ? zero : d[t & 7]);
      SBAR();
      __builtin_amdgcn_s_setprio(0);
      LANE_VARS;
      const int idx = 2047 - (int)(key & 2047u), win = idx >> 7;
      const bool owner = win == tm;
      auto pick2 = [&](const f32x4 (&d)[8]) {
        u32x2 o;
#pragma unroll
        for (int e = 0; e < 2; ++e) {
          const float x = tblk ? d[2 + e][0] : d[e][0], y = tblk ? d[6 + e][0] : d[4 + e][0];
          o[e] = nzf(hi ? y : x);
        }
        return o;
      };
      if (ffn) {
        const unsigned ppos = (lane & 15) * 32 + (lane >> 4) * 8;
        __builtin_amdgcn_raw_buffer_store_b64(pick2(d), rs, sx + SX_GP + (grp * 8 + qa) * 512 + ppos, 0, 16);
        STAMPV(9 + 16 * L);
        __builtin_amdgcn_s_sleep(13);
        if (L == 0) __builtin_amdgcn_s_sleep(0);
        u32x2 gu[8];
        for (unsigned spins = 0;; ++spins) {
#pragma unroll
          for (int i = 0; i < 8; ++i) gu[i] = __builtin_amdgcn_raw_buffer_load_b64(rs, sx + SX_GP + (grp * 8 + i) * 512 + lane * 8, 0, 16);
          SBAR();
          unsigned m = 0u;
#pragma unroll
          for (int i = 0; i < 8; ++i) m = max(max(m, gu[i][0]), gu[i][1]);
          if (__all(m != SENT) || spins > SPIN_CAP) break;
          poll_pause(L == 0);
        }
        STAMPV(10 + 16 * L);
        float t0 = opq(U2F(gu[0][0])), t1 = opq(U2F(gu[0][1]));
#pragma unroll
        for (int i = 1; i < 8; ++i) { t0 = fadd(t0, U2F(gu[i][0])); t1 = fadd(t1, U2F(gu[i][1])); }
        auto sw = __builtin_amdgcn_permlane32_swap(__float_as_uint(t0), __float_as_uint(t1), false, false);
        const float gv = rbf(U2F(sw[0])), uv = rbf(U2F(sw[1]));
        reinterpret_cast<u16*>(smem + L_GU + a * 128)[2 * r32 + hi] = (u16)pack2(fmul(rbf(fmul(gv, RCP(fadd(1.0f, __expf(-gv))))), uv), 0.f);
        asm volatile("s_waitcnt lgkmcnt(0)" ::: "memory");
        const s16x8 a0 = *reinterpret_cast<const s16x8*>(smem + L_GU + a * 128 + (lane >> 4) * 16), a1 = *reinterpret_cast<const s16x8*>(smem + L_GU + a * 128 + 64 + (lane >> 4) * 16);
        SBAR();
        __builtin_amdgcn_s_setprio(3);
#pragma unroll
        for (int t = 0; t < 16; ++t) d[t & 7] = MFMA16(t < 8 ? a0 : a1, __builtin_bit_cast(s16x8, wdf[t]), t < 8 ? zero : d[t & 7]);
        SBAR();
        __builtin_amdgcn_s_setprio(0);
        __builtin_amdgcn_raw_buffer_store_b64(pick2(d), rs, sx + SX_YP + grp * 4096 + qa * 512 + ppos, 0, 16);
      }
      STAMPV(3 + 16 * L);
      if (L == 1 && (cg != (kr & 7) || a)) { STAMP_END; __builtin_amdgcn_endpgm(); }
      s16x8 wpf[12];
#pragma unroll
      for (int t = 0; t < 12; ++t) wpf[t] = tr2(smem + L_SL + a * 12288 + (16 * hi + 4 * tblk + tq) * 384 + t * 32 + tp * 8, 8 * 384);
#pragma unroll
      for (int t = 0; t < 12; ++t) PIN(wpf[t]);
      if (L == 0 && !owner) lds_set(wa + 4 + 4 * a, 1);
      __builtin_amdgcn_s_sleep(13 - 4);
      if (L == 0) __builtin_amdgcn_s_sleep(0);
      if (L == 0 && !ffn) __builtin_amdgcn_s_sleep(64);
      const unsigned rk = key >> 16;
      float rsig = rbf(RCP(fadd(1.0f, __expf(-lo16((rk & 0x8000u) ? (rk & 0x7fffu) : (~rk & 0xffffu))))));
      PIN(rsig);
      const int n = 4 * (lane & 7) + ((lane >> 3) & 3);
      const int fo = ((n >> 4) * 2 + ((n >> 2) & 1)) * 8 + ((n >> 3) & 1) * 4 + (n & 3);
      u32x4 pv[8];
      poll_all<8>(pv, L == 0, [&](u32x4 (&t)[8]) {
#pragma unroll
        for (int i = 0; i < 7; ++i) t[i] = ldg(rs, sx + SX_YP + (8 * i + (lane >> 3)) * 4096 + kr * 128 + (lane & 7) * 16);
        t[7][0] = t[7][2] = __builtin_amdgcn_raw_buffer_load_b32(rs, sx + SX_ROW + (idx >> 5) * 2048 + kr * 64 + (fo >> 1) * 4, 0, 16);
        t[7][1] = t[7][3] = __builtin_amdgcn_raw_buffer_load_b32(rs, sx + SX_ROW + 131072 + (idx >> 5) * 2048 + kr * 64 + (fo >> 1) * 4, 0, 16);
      }, [&](const u32x4 (&t)[8]) { return none_sent(t); });
      const float hv = (fo & 1) ? hi16(pv[7][0]) : lo16(pv[7][0]), xv = (fo & 1) ? hi16(pv[7][1]) : lo16(pv[7][1]);
      STAMPV(4 + 16 * L);
      float ys;
      {
        f32x4 t = sum4<7>(pv);
#pragma unroll
        for (int e = 0; e < 4; ++e) {
          t[e] = fadd(t[e], U2F(dpp<0x128>(__float_as_uint(t[e]))));
          t[e] = half_swap_sum(row_swap_sum(t[e]));
        }
        const float x = (lane & 8) ? t[1] : t[0], y = (lane & 8) ? t[3] : t[2];
        ys = tblk ? y : x;
      }
      const unsigned ob = pack2(fadd(xv, rbf(fadd(hv, rbf(fmul(rbf(ys), rsig))))), 0.f) & 0xffffu;
      if (L == 1) {
        if (lane < 32) out[(size_t)idx * D_ + kr * 32 + n] = (u16)ob;
        STAMPV(5 + 16 * L);
        STAMP_END;
        __builtin_amdgcn_endpgm();
      }
      reinterpret_cast<u16*>(smem + L_X2F + a * 64)[fo] = (u16)ob;
      asm volatile("s_waitcnt lgkmcnt(0)" ::: "memory");
      {
        const s16x8 xa = *reinterpret_cast<const s16x8*>(smem + L_X2F + a * 64 + (lane >> 4) * 16);
        const u32x4 xf = *reinterpret_cast<const u32x4*>(smem + L_X2F + a * 64 + tp * 16);
        f32x4 z[12];
        SBAR();
        __builtin_amdgcn_s_setprio(3);
#pragma unroll
        for (int t = 0; t < 12; ++t) z[t] = MFMA16(xa, wpf[t], zero);
        SBAR();
        __builtin_amdgcn_s_setprio(0);
        const float x2 = lo16(ob), s1 = half_sum(x2), s2 = half_sum(fmul(x2, x2));
        u32x4 o;
#pragma unroll
        for (int e = 0; e < 4; ++e) {
          const unsigned sp = lane < 52 ? (e == 0 ? __float_as_uint(s1) : __float_as_uint(s2)) : xf[e];
          const unsigned x = __float_as_uint(tblk ? z[4 + e][0] : z[e][0]), y = tblk ? sp : __float_as_uint(z[8 + e][0]);
          o[e] = nz(hi ? y : x);
        }
        const unsigned pos = lane < 24 ? lane * 16 : lane < 48 ? lane * 16 + 128 : lane < 52 ? (lane & 1) * 512 + 384 : 400 + tp * 16;
        stg(rs, lane < 56 ? OFF_SX + SX_QP + ((2 * cg + a) * 32 + kr) * 1024 + pos : OOB, o);
      }
      STAMPV(5 + 16 * L);
      if (owner) {
        __builtin_amdgcn_s_sleep(13);
        const int c25 = r32 < 25 ? r32 : 24, pl = 24 * a + c24, col = 64 * (pl >> 4) + (pl & 15);
        float cs[4];
#pragma unroll
        for (int e = 0; e < 4; ++e) { cs[e] = reinterpret_cast<const float*>(smem + L_CS)[col + 16 * e]; PIN(cs[e]); }
        u32x4 qv[17];
        poll_all<17>(qv, true, [&](u32x4 (&t)[17]) {
#pragma unroll
          for (int i = 0; i < 16; ++i) t[i] = ldg(rs, OFF_SX + SX_QP + (tn * 32 + 2 * i + hi) * 1024 + a * 512 + c25 * 16);
          t[16] = ldg(rs, OFF_SX + SX_QP + (tn * 34 + hi) * 1024 + 400 + tp * 16);
        }, [&](const u32x4 (&t)[17]) { return none_sent(t); });
        *reinterpret_cast<u32x4*>(smem + L_X2P + hi * 64 + tp * 16) = qv[16];
        f32x4 t = sum4<16>(qv);
#pragma unroll
        for (int e = 0; e < 4; ++e) t[e] = half_swap_sum(t[e]);
        const float mean = fmul(U2F(__builtin_amdgcn_readlane(__float_as_uint(t[0]), 24)), 1.0f / 1024.0f);
        const float var = fmaxf(ffma(-mean, mean, fmul(U2F(__builtin_amdgcn_readlane(__float_as_uint(t[1]), 24)), 1.0f / 1024.0f)), 0.f);
        float mb, den;
        ln_scalars(mean, var, mb, den);
        const float rd = RCP(den);
        if (lane < 24) {
#pragma unroll
          for (int e = 0; e < 4; ++e) reinterpret_cast<float*>(smem + L_PATCH)[col + 16 * e] = fmul(ffma(-mb, cs[e], t[e]), rd);
        }
        lds_set(wa + 4 + 4 * a, L + 1);
      }
      STAMPV(6 + 16 * L);
      }
      BAR_ALL();
      unsigned dv = fresh_lane();
      dv = (dv >> 4) * (8 * D_ * 2) + (dv & 15) * 16;
      PIN(dv);
      wait_go(1);
      load_wd(dv);
      STAMPV(23);
      lds_wait_ge<4>(wa + 80 + 4 * (fresh_lane() & 3), 4);
      transpose8(wdf); transpose8(wdf + 8);
      BARRIER();
    }
  }
  const int tm_ = tm, tn_ = tn, wid_ = wid;
  {
    const int tid = threadIdx.x, lane = tid & 63, r32 = lane & 31, row = 2 * wid + (lane >> 5);
    char* sm = smem + L_XCH;
    u32x4 v[4];
    asm volatile("global_load_dwordx4 %0, %4, off\n\tglobal_load_dwordx4 %1, %4, off offset:512\n\tglobal_load_dwordx4 %2, %4, off offset:1024\n\tglobal_load_dwordx4 %3, %4, off offset:1536"
                 : "=&v"(v[0]), "=&v"(v[1]), "=&v"(v[2]), "=&v"(v[3]) : "v"(x0 + (size_t)(tm * 128 + tn * 8 + row) * D_ + r32 * 8) : "memory");
    int pass = fresh_s(0);
#pragma unroll 1
    for (;;) {
      asm volatile("s_cmp_eq_u32 %4, 0\n\ts_cbranch_scc1 1f\n\ts_waitcnt vmcnt(0)\n1:" : "+v"(v[0]), "+v"(v[1]), "+v"(v[2]), "+v"(v[3]) : "s"(pass) : "memory", "scc");
      float s = 0.f, s2 = 0.f;
#pragma unroll
      for (int i = 0; i < 4; ++i)
#pragma unroll
        for (int e = 0; e < 4; e += 2) { s = dot2(v[i][e], 0x3f803f80u, s); s2 = dot2(v[i][e + 1], 0x3f803f80u, s2); }
      const float mean = half_sum(s + s2) * (1.0f / 1024.0f);
      f32x2 m2 = {0.f, 0.f};
#pragma unroll
      for (int i = 0; i < 4; ++i)
#pragma unroll
        for (int e = 0; e < 4; ++e) { const f32x2 d = f32x2{lo16(v[i][e]), hi16(v[i][e])} - mean; m2 += d * d; }
      float mb, den;
      ln_scalars(mean, half_sum(m2[0] + m2[1]) * (1.0f / 1024.0f), mb, den);
      const float rd = 1.0f / den;
#pragma unroll
      for (int i = 0; i < 4; ++i) {
        char* p = sm + ((16 * i + (r32 >> 1)) * 2) * 144 + row * 16 + (r32 & 1) * 8;
        u32x2 lo = {ln_pair(v[i][0], mb, rd), ln_pair(v[i][1], mb, rd)}, hi = {ln_pair(v[i][2], mb, rd), ln_pair(v[i][3], mb, rd)};
        *reinterpret_cast<u32x2*>(p) = lo;
        *reinterpret_cast<u32x2*>(p + 144) = hi;
      }
      STAMPV(37);
      lds_set(lbase + L_W + 64 + 4 * wid, pass);
      lds_wait_ge<0>(lbase + L_W + 64 + 4 * (lane & 3), pass);
      u32x4 t[4];
#pragma unroll
      for (int i = 0; i < 4; ++i) t[i] = *reinterpret_cast<const u32x4*>(sm + (i * 32 + (tid >> 3)) * 144 + (tid & 7) * 16);
      SBAR();
      unsigned dst = OFF_HF + (tm * 4 + (tn >> 2)) * TS + (tn & 3) * 128 + (tid >> 3) * 512 + (tid & 7) * 16;
      PIN(dst);
      if (pass) {
#pragma unroll
        for (int i = 0; i < 4; ++i) stg(rs, dst + i * 16384, t[i]);
        break;
      }
#pragma unroll
      for (int i = 0; i < 4; ++i) PIN(t[i]);
      pass = fresh_s(1);
    }
    DRAIN();
    lds_set(lbase + L_W + 64 + 4 * wid, 2);
    STAMPV(38);
  }
  const u32x4 srd = {(unsigned)(uintptr_t)ws, (unsigned)((uintptr_t)ws >> 32) & 0xffffu, WS_BYTES, 0x00020000u};
  u32x4 x1p[4];
  asm volatile("" : "=v"(x1p[0]), "=v"(x1p[1]), "=v"(x1p[2]), "=v"(x1p[3]));
#pragma unroll 1
  for (int L = 0; L < 2; ++L) {
    const unsigned sx = OFF_SX + L * SX_LAYER;
    u32x8 q[4];
    u32x16 kva, kvb;
    f32x16 oz[5];
    {
      f32x16 acc[6];
      u32x16 stA, stB, xr0, xr1, xr2, xr3;
      u32x4 stC;
      asm volatile("" : "={v[112:127]}"(stB), "={v[128:131]}"(stC));
      { PHASE_VARS;
      stB[8] = lane * 16 + (L ? OFF_X1 : OFF_HF) + tile * TS;
      stB[9] = lbase + wrow * 384 + (((4 * hh + wch) ^ wsw) << 4) + 8 * (tp & 1);
      stB[10] = lbase + wrow * 384 + (((4 * (hh ^ 1) + wch) ^ wsw) << 4) + 8 * (tp & 1);
      stB[13] = lbase + L_FULL + (lane & 1) * 256;
      stB[15] = lbase + L_FREE + wid * 256 + lane * 4;
      stC[0] = 32 * L; }
      unsigned soff, slot, need = 32 * L + 1;
      if (L == 0) {
        lds_wait_ge<0>(lbase + L_W + 24, 1);
        STAMPV(39);
        soff = tn * 4096;
        asm volatile(
          "buffer_load_dwordx4 v[132:135], v120, s[40:43], s44 offen offset:0 sc1\n\t"
          "buffer_load_dwordx4 v[136:139], v120, s[40:43], s44 offen offset:1024 sc1\n\t"
          "buffer_load_dwordx4 v[140:143], v120, s[40:43], s44 offen offset:2048 sc1\n\t"
          "buffer_load_dwordx4 v[144:147], v120, s[40:43], s44 offen offset:3072 sc1\n\t"
          "s_add_u32 s44, s44, 0x1000\n\t"
          "s_and_b32 s44, s44, 0xffff\n\t"
          "buffer_load_dwordx4 v[148:151], v120, s[40:43], s44 offen offset:0 sc1\n\t"
          "buffer_load_dwordx4 v[152:155], v120, s[40:43], s44 offen offset:1024 sc1\n\t"
          "buffer_load_dwordx4 v[156:159], v120, s[40:43], s44 offen offset:2048 sc1\n\t"
          "buffer_load_dwordx4 v[160:163], v120, s[40:43], s44 offen offset:3072 sc1\n\t"
          "s_add_u32 s44, s44, 0x1000\n\t"
          "s_and_b32 s44, s44, 0xffff\n\t"
          "buffer_load_dwordx4 v[164:167], v120, s[40:43], s44 offen offset:0 sc1\n\t"
          "buffer_load_dwordx4 v[168:171], v120, s[40:43], s44 offen offset:1024 sc1\n\t"
          "buffer_load_dwordx4 v[172:175], v120, s[40:43], s44 offen offset:2048 sc1\n\t"
          "buffer_load_dwordx4 v[176:179], v120, s[40:43], s44 offen offset:3072 sc1\n\t"
          "s_add_u32 s44, s44, 0x1000\n\t"
          "s_and_b32 s44, s44, 0xffff\n\t"
          "buffer_load_dwordx4 v[180:183], v120, s[40:43], s44 offen offset:0 sc1\n\t"
          "buffer_load_dwordx4 v[184:187], v120, s[40:43], s44 offen offset:1024 sc1\n\t"
          "buffer_load_dwordx4 v[188:191], v120, s[40:43], s44 offen offset:2048 sc1\n\t"
          "buffer_load_dwordx4 v[192:195], v120, s[40:43], s44 offen offset:3072 sc1\n\t"
          : "={v[132:147]}"(xr0), "={v[148:163]}"(xr1), "={v[164:179]}"(xr2), "={v[180:195]}"(xr3), "+{s44}"(soff) : "{v[112:127]}"(stB), "{s[40:43]}"(srd) : "scc", "memory");
      } else {
        xr0 = cat4(x1p[0], x1p[1], x1p[2], x1p[3]);
        asm volatile("" : "={v[148:163]}"(xr1), "={v[164:179]}"(xr2), "={v[180:195]}"(xr3));
      }
      STAMPV(1 + 16 * L);
      asm volatile(
        "v_mov_b32 v123, v121\n\t"
        "v_mov_b32 v124, v122\n\t"
        "s_mov_b64 vcc, -1\n\t"
        "s_cbranch_vccnz 10f\n\t"
        "11:\n\t"
        "s_mov_b32 s46, 12288\n\t"
        "s_add_u32 s47, s47, 1\n\t"
        "ds_read_b64_tr_b16 v[96:97], v123 offset:0\n\t"
        "ds_read_b64_tr_b16 v[98:99], v123 offset:3072\n\t"
        "ds_read_b64_tr_b16 v[100:101], v124 offset:0\n\t"
        "ds_read_b64_tr_b16 v[102:103], v124 offset:3072\n\t"
        "ds_read_b64_tr_b16 v[104:105], v123 offset:128\n\t"
        "ds_read_b64_tr_b16 v[106:107], v123 offset:3200\n\t"
        "ds_read_b64_tr_b16 v[108:109], v124 offset:128\n\t"
        "ds_read_b64_tr_b16 v[110:111], v124 offset:3200\n\t"
        "ds_read_b64_tr_b16 v[112:113], v123 offset:256\n\t"
        "ds_read_b64_tr_b16 v[114:115], v123 offset:3328\n\t"
        "ds_write_b32 v127, v128\n\t"
        "ds_read_b64_tr_b16 v[116:117], v124 offset:256\n\t"
        "ds_read_b64_tr_b16 v[118:119], v124 offset:3328\n\t"
        "s_waitcnt vmcnt(15) lgkmcnt(11)\n\t"
        "v_mfma_f32_32x32x16_bf16 v[0:15], v[96:99], v[132:135], 0\n\t"
        "ds_read_b32 v126, v125\n\t"
        "ds_read_b64_tr_b16 v[96:97], v123 offset:6144\n\t"
        "ds_read_b64_tr_b16 v[98:99], v123 offset:9216\n\t"
        "s_waitcnt lgkmcnt(12)\n\t"
        "v_mfma_f32_32x32x16_bf16 v[16:31], v[100:103], v[132:135], 0\n\t"
        "v_add_u32 v129, s46, v121\n\t"
        "ds_read_b64_tr_b16 v[100:101], v124 offset:6144\n\t"
        "ds_read_b64_tr_b16 v[102:103], v124 offset:9216\n\t"
        "s_waitcnt lgkmcnt(12)\n\t"
        "v_mfma_f32_32x32x16_bf16 v[32:47], v[104:107], v[132:135], 0\n\t"
        "ds_read_b64_tr_b16 v[104:105], v123 offset:6272\n\t"
        "ds_read_b64_tr_b16 v[106:107], v123 offset:9344\n\t"
        "s_waitcnt lgkmcnt(12)\n\t"
        "v_mfma_f32_32x32x16_bf16 v[48:63], v[108:111], v[132:135], 0\n\t"
        "v_add_u32 v130, s46, v122\n\t"
        "ds_read_b64_tr_b16 v[108:109], v124 offset:6272\n\t"
        "ds_read_b64_tr_b16 v[110:111], v124 offset:9344\n\t"
        "s_waitcnt lgkmcnt(12)\n\t"
        "v_mfma_f32_32x32x16_bf16 v[64:79], v[132:135], v[112:115], 0\n\t"
        "ds_read_b64_tr_b16 v[112:113], v123 offset:6400\n\t"
        "ds_read_b64_tr_b16 v[114:115], v123 offset:9472\n\t"
        "s_waitcnt lgkmcnt(10)\n\t"
        "v_mfma_f32_32x32x16_bf16 v[80:95], v[132:135], v[116:119], 0\n\t"
        "v_cmp_gt_u32 vcc, s47, v126\n\t"
        "ds_read_b64_tr_b16 v[116:117], v124 offset:6400\n\t"
        "ds_read_b64_tr_b16 v[118:119], v124 offset:9472\n\t"
        "s_waitcnt vmcnt(14) lgkmcnt(10)\n\t"
        "v_mfma_f32_32x32x16_bf16 v[0:15], v[96:99], v[136:139], v[0:15]\n\t"
        "s_cbranch_vccnz 12f\n\t"
        "13:\n\t"
        "ds_read_b64_tr_b16 v[96:97], v129 offset:0\n\t"
        "ds_read_b64_tr_b16 v[98:99], v129 offset:3072\n\t"
        "s_waitcnt lgkmcnt(10)\n\t"
        "v_mfma_f32_32x32x16_bf16 v[16:31], v[100:103], v[136:139], v[16:31]\n\t"
        "v_add_u32 v128, 1, v128\n\t"
        "ds_read_b64_tr_b16 v[100:101], v130 offset:0\n\t"
        "ds_read_b64_tr_b16 v[102:103], v130 offset:3072\n\t"
        "s_waitcnt lgkmcnt(10)\n\t"
        "v_mfma_f32_32x32x16_bf16 v[32:47], v[104:107], v[136:139], v[32:47]\n\t"
        "ds_read_b64_tr_b16 v[104:105], v129 offset:128\n\t"
        "ds_read_b64_tr_b16 v[106:107], v129 offset:3200\n\t"
        "s_waitcnt lgkmcnt(10)\n\t"
        "v_mfma_f32_32x32x16_bf16 v[48:63], v[108:111], v[136:139], v[48:63]\n\t"
        "s_add_u32 s46, s46, 12288\n\t"
        "s_add_u32 s47, s47, 1\n\t"
        "ds_read_b64_tr_b16 v[108:109], v130 offset:128\n\t"
        "ds_read_b64_tr_b16 v[110:111], v130 offset:3200\n\t"
        "s_waitcnt lgkmcnt(10)\n\t"
        "v_mfma_f32_32x32x16_bf16 v[64:79], v[136:139], v[112:115], v[64:79]\n\t"
        "s_cmp_eq_u32 s46, 110592\n\t"
        "s_cselect_b32 s46, 0, s46\n\t"
        "ds_read_b64_tr_b16 v[112:113], v129 offset:256\n\t"
        "ds_read_b64_tr_b16 v[114:115], v129 offset:3328\n\t"
        "s_waitcnt lgkmcnt(10)\n\t"
        "v_mfma_f32_32x32x16_bf16 v[80:95], v[136:139], v[116:119], v[80:95]\n\t"
        "ds_write_b32 v127, v128\n\t"
        "ds_read_b64_tr_b16 v[116:117], v130 offset:256\n\t"
        "ds_read_b64_tr_b16 v[118:119], v130 offset:3328\n\t"
        "s_waitcnt vmcnt(13) lgkmcnt(11)\n\t"
        "v_mfma_f32_32x32x16_bf16 v[0:15], v[96:99], v[140:143], v[0:15]\n\t"
        "ds_read_b32 v126, v125\n\t"
        "ds_read_b64_tr_b16 v[96:97], v129 offset:6144\n\t"
        "ds_read_b64_tr_b16 v[98:99], v129 offset:9216\n\t"
        "s_waitcnt lgkmcnt(12)\n\t"
        "v_mfma_f32_32x32x16_bf16 v[16:31], v[100:103], v[140:143], v[16:31]\n\t"
        "v_add_u32 v123, s46, v121\n\t"
        "ds_read_b64_tr_b16 v[100:101], v130 offset:6144\n\t"
        "ds_read_b64_tr_b16 v[102:103], v130 offset:9216\n\t"
        "s_waitcnt lgkmcnt(12)\n\t"
        "v_mfma_f32_32x32x16_bf16 v[32:47], v[104:107], v[140:143], v[32:47]\n\t"
        "ds_read_b64_tr_b16 v[104:105], v129 offset:6272\n\t"
        "ds_read_b64_tr_b16 v[106:107], v129 offset:9344\n\t"
        "s_waitcnt lgkmcnt(12)\n\t"
        "v_mfma_f32_32x32x16_bf16 v[48:63], v[108:111], v[140:143], v[48:63]\n\t"
        "v_add_u32 v124, s46, v122\n\t"
        "ds_read_b64_tr_b16 v[108:109], v130 offset:6272\n\t"
        "ds_read_b64_tr_b16 v[110:111], v130 offset:9344\n\t"
        "s_waitcnt lgkmcnt(12)\n\t"
        "v_mfma_f32_32x32x16_bf16 v[64:79], v[140:143], v[112:115], v[64:79]\n\t"
        "ds_read_b64_tr_b16 v[112:113], v129 offset:6400\n\t"
        "ds_read_b64_tr_b16 v[114:115], v129 offset:9472\n\t"
        "s_waitcnt lgkmcnt(10)\n\t"
        "v_mfma_f32_32x32x16_bf16 v[80:95], v[140:143], v[116:119], v[80:95]\n\t"
        "v_cmp_gt_u32 vcc, s47, v126\n\t"
        "ds_read_b64_tr_b16 v[116:117], v130 offset:6400\n\t"
        "ds_read_b64_tr_b16 v[118:119], v130 offset:9472\n\t"
        "s_waitcnt vmcnt(12) lgkmcnt(10)\n\t"
        "v_mfma_f32_32x32x16_bf16 v[0:15], v[96:99], v[144:147], v[0:15]\n\t"
        "s_cbranch_vccnz 14f\n\t"
        "15:\n\t"
        "ds_read_b64_tr_b16 v[96:97], v123 offset:0\n\t"
        "ds_read_b64_tr_b16 v[98:99], v123 offset:3072\n\t"
        "s_waitcnt lgkmcnt(10)\n\t"
        "v_mfma_f32_32x32x16_bf16 v[16:31], v[100:103], v[144:147], v[16:31]\n\t"
        "v_add_u32 v128, 1, v128\n\t"
        "ds_read_b64_tr_b16 v[100:101], v124 offset:0\n\t"
        "ds_read_b64_tr_b16 v[102:103], v124 offset:3072\n\t"
        "s_waitcnt lgkmcnt(10)\n\t"
        "v_mfma_f32_32x32x16_bf16 v[32:47], v[104:107], v[144:147], v[32:47]\n\t"
        "ds_read_b64_tr_b16 v[104:105], v123 offset:128\n\t"
        "ds_read_b64_tr_b16 v[106:107], v123 offset:3200\n\t"
        "s_waitcnt lgkmcnt(10)\n\t"
        "v_mfma_f32_32x32x16_bf16 v[48:63], v[108:111], v[144:147], v[48:63]\n\t"
        "s_add_u32 s46, s46, 12288\n\t"
        "s_add_u32 s47, s47, 1\n\t"
        "ds_read_b64_tr_b16 v[108:109], v124 offset:128\n\t"
        "ds_read_b64_tr_b16 v[110:111], v124 offset:3200\n\t"
        "s_waitcnt lgkmcnt(10)\n\t"
        "v_mfma_f32_32x32x16_bf16 v[64:79], v[144:147], v[112:115], v[64:79]\n\t"
        "s_cmp_eq_u32 s46, 110592\n\t"
        "s_cselect_b32 s46, 0, s46\n\t"
        "ds_read_b64_tr_b16 v[112:113], v123 offset:256\n\t"
        "ds_read_b64_tr_b16 v[114:115], v123 offset:3328\n\t"
        "s_waitcnt lgkmcnt(10)\n\t"
        "v_mfma_f32_32x32x16_bf16 v[80:95], v[144:147], v[116:119], v[80:95]\n\t"
        "ds_write_b32 v127, v128\n\t"
        "ds_read_b64_tr_b16 v[116:117], v124 offset:256\n\t"
        "ds_read_b64_tr_b16 v[118:119], v124 offset:3328\n\t"
        "s_waitcnt lgkmcnt(0)\n\t"
        "s_nop 7\n\t"
        "s_branch 9f\n\t"
        "10:\n\t"
        "s_mov_b32 s48, 0x10000\n\t"
        "8:\n\t"
        "ds_read_b32 v126, v125\n\t"
        "s_sub_u32 s48, s48, 1\n\t"
        "s_waitcnt lgkmcnt(0)\n\t"
        "s_cbranch_scc1 11b\n\t"
        "v_cmp_gt_u32 vcc, s47, v126\n\t"
        "s_cbranch_vccnz 8b\n\t"
        "s_branch 11b\n\t"
        "12:\n\t"
        "s_mov_b32 s48, 0x10000\n\t"
        "8:\n\t"
        "ds_read_b32 v126, v125\n\t"
        "s_sub_u32 s48, s48, 1\n\t"
        "s_waitcnt lgkmcnt(0)\n\t"
        "s_cbranch_scc1 13b\n\t"
        "v_cmp_gt_u32 vcc, s47, v126\n\t"
        "s_cbranch_vccnz 8b\n\t"
        "s_branch 13b\n\t"
        "14:\n\t"
        "s_mov_b32 s48, 0x10000\n\t"
        "8:\n\t"
        "ds_read_b32 v126, v125\n\t"
        "s_sub_u32 s48, s48, 1\n\t"
        "s_waitcnt lgkmcnt(0)\n\t"
        "s_cbranch_scc1 15b\n\t"
        "v_cmp_gt_u32 vcc, s47, v126\n\t"
        "s_cbranch_vccnz 8b\n\t"
        "s_branch 15b\n\t"
        "9:\n\t"
        : "={v[0:15]}"(acc[0]), "={v[16:31]}"(acc[1]), "={v[32:47]}"(acc[2]), "={v[48:63]}"(acc[3]), "={v[64:79]}"(acc[4]), "={v[80:95]}"(acc[5]),
          "={v[96:111]}"(stA), "+{v[112:127]}"(stB), "+{v[128:131]}"(stC), "={s46}"(slot), "+{s47}"(need)
        : "{v[132:147]}"(xr0) : "s48", "vcc", "scc", "memory");
      STAMPV(2 + 16 * L);
      float al = 1.f;
      if (L == 1) {
        PHASE_VARS;
        float S, Q, Dd;
        row_stats_sum(rs, OFF_SX, tile, lane, S, Q, Dd);
        STAMPV(36);
        lds_wait_ge<0>(lbase + L_W + 24, 2);
        soff = ((tn + 1) & 15) * 4096;
        asm volatile(
          "buffer_load_dwordx4 v[148:151], v120, s[40:43], s44 offen offset:0 sc1\n\t"
          "buffer_load_dwordx4 v[152:155], v120, s[40:43], s44 offen offset:1024 sc1\n\t"
          "buffer_load_dwordx4 v[156:159], v120, s[40:43], s44 offen offset:2048 sc1\n\t"
          "buffer_load_dwordx4 v[160:163], v120, s[40:43], s44 offen offset:3072 sc1\n\t"
          : "={v[148:163]}"(xr1), "={v[164:179]}"(xr2), "={v[180:195]}"(xr3), "+{s44}"(soff), "+v"(S), "+v"(Q), "+v"(Dd) : "{v[112:127]}"(stB), "{s[40:43]}"(srd) : "scc", "memory");
        float mean, var, mb1, rd1, mb2, den2;
        row_stats_fin(rs, OFF_SX + SX_CAND + (tn * 16 + tm) * 128 + wid * 4, smem, 1, tile, lane, S, Q, Dd, U2F(words[0]), x1p, mean, var, mb1, rd1);
        ln_scalars(mean + (mean - mb1) * rd1, var * (1.0f + rd1) * (1.0f + rd1), mb2, den2);
        const float rd2 = RCP(den2);
        al = (1.0f + rd1) * rd2;
        const float be = -(mb1 * rd1 + mb2) * RCP(1.0f + rd1);
        reinterpret_cast<float*>(smem + L_AB)[wid * 64 + lane] = hi ? be : al;
        reinterpret_cast<float*>(smem + L_AB)[256 + wid * 64 + lane] = hi ? 1.0f + rd1 : -mb1 * rd1;
        soff = ((tn + 1 + 0) & 15) * 4096;
        asm volatile(
          "s_nop 0\n\t"
          "s_add_u32 s44, s44, 0x1000\n\t"
          "s_and_b32 s44, s44, 0xffff\n\t"
          "buffer_load_dwordx4 v[164:167], v120, s[40:43], s44 offen offset:0 sc1\n\t"
          "buffer_load_dwordx4 v[168:171], v120, s[40:43], s44 offen offset:1024 sc1\n\t"
          "buffer_load_dwordx4 v[172:175], v120, s[40:43], s44 offen offset:2048 sc1\n\t"
          "buffer_load_dwordx4 v[176:179], v120, s[40:43], s44 offen offset:3072 sc1\n\t"
          "s_add_u32 s44, s44, 0x1000\n\t"
          "s_and_b32 s44, s44, 0xffff\n\t"
          "buffer_load_dwordx4 v[180:183], v120, s[40:43], s44 offen offset:0 sc1\n\t"
          "buffer_load_dwordx4 v[184:187], v120, s[40:43], s44 offen offset:1024 sc1\n\t"
          "buffer_load_dwordx4 v[188:191], v120, s[40:43], s44 offen offset:2048 sc1\n\t"
          "buffer_load_dwordx4 v[192:195], v120, s[40:43], s44 offen offset:3072 sc1\n\t"
          : "+{v[148:163]}"(xr1), "+{v[164:179]}"(xr2), "+{v[180:195]}"(xr3), "+{s44}"(soff) : "{v[112:127]}"(stB), "{s[40:43]}"(srd) : "scc", "memory");
#pragma unroll
        for (int jj = 0; jj < 3; ++jj) {
          f32x4 cs[2][4];
          float cv[2];
#pragma unroll
          for (int j = 0; j < 2; ++j) {
            const int cb = jj * 64 + (j ^ hh) * 32;
            if (jj == 2) cv[j] = *reinterpret_cast<const float*>(smem + L_CS + (cb + r32) * 4);
#pragma unroll
            for (int g = 0; g < 4; ++g)
              if (jj < 2 || j == 0) cs[j][g] = *reinterpret_cast<const f32x4*>(smem + (jj < 2 ? L_CS + cb * 4 : L_AB + wid * 256 + 128) + (8 * g + 4 * hi) * 4);
          }
          SBAR();
#pragma unroll
          for (int j = 0; j < 2; ++j)
#pragma unroll
            for (int e = 0; e < 16; ++e) acc[2 * jj + j][e] += jj < 2 ? be * cs[j][e >> 2][e & 3] : cs[0][e >> 2][e & 3] * cv[j];
          SBAR();
        }
      }
      u32x4 ep;
      { PHASE_VARS;
        if (L == 0) reinterpret_cast<float*>(smem + L_AB)[wid * 64 + lane] = 1.0f;
        const unsigned dst = OFF_KV + L * (10 * MBY) + tn * 2 * HS + tile * 4096 + lane * 16;
        ep = u32x4{__float_as_uint(al), dst + hh * HS, dst + (hh ^ 1) * HS, lbase + L_AB + wid * 256 + hi * 16}; }
      STAMPV(3 + 16 * L);
      soff = ((tn + 1 + 2) & 15) * 4096;
      asm volatile(
        "s_nop 0\n\t"
        "s_add_u32 s44, s44, 0x1000\n\t"
        "s_and_b32 s44, s44, 0xffff\n\t"
        "buffer_load_dwordx4 v[132:135], v120, s[40:43], s44 offen offset:0 sc1\n\t"
        "buffer_load_dwordx4 v[136:139], v120, s[40:43], s44 offen offset:1024 sc1\n\t"
        "buffer_load_dwordx4 v[140:143], v120, s[40:43], s44 offen offset:2048 sc1\n\t"
        "s_mov_b32 s45, 3\n\t"
        "1:\n\t"
        "s_waitcnt vmcnt(14) lgkmcnt(11)\n\t"
        "v_mfma_f32_32x32x16_bf16 v[0:15], v[96:99], v[148:151], v[0:15]\n\t"
        "ds_read_b32 v126, v125\n\t"
        "ds_read_b64_tr_b16 v[96:97], v123 offset:6144\n\t"
        "ds_read_b64_tr_b16 v[98:99], v123 offset:9216\n\t"
        "s_waitcnt lgkmcnt(12)\n\t"
        "v_mfma_f32_32x32x16_bf16 v[16:31], v[100:103], v[148:151], v[16:31]\n\t"
        "v_add_u32 v129, s46, v121\n\t"
        "ds_read_b64_tr_b16 v[100:101], v124 offset:6144\n\t"
        "ds_read_b64_tr_b16 v[102:103], v124 offset:9216\n\t"
        "s_waitcnt lgkmcnt(12)\n\t"
        "v_mfma_f32_32x32x16_bf16 v[32:47], v[104:107], v[148:151], v[32:47]\n\t"
        "ds_read_b64_tr_b16 v[104:105], v123 offset:6272\n\t"
        "ds_read_b64_tr_b16 v[106:107], v123 offset:9344\n\t"
        "buffer_load_dwordx4 v[144:147], v120, s[40:43], s44 offen offset:3072 sc1\n\t"
        "s_waitcnt lgkmcnt(12)\n\t"
        "v_mfma_f32_32x32x16_bf16 v[48:63], v[108:111], v[148:151], v[48:63]\n\t"
        "v_add_u32 v130, s46, v122\n\t"
        "ds_read_b64_tr_b16 v[108:109], v124 offset:6272\n\t"
        "ds_read_b64_tr_b16 v[110:111], v124 offset:9344\n\t"
        "s_waitcnt lgkmcnt(12)\n\t"
        "v_mfma_f32_32x32x16_bf16 v[64:79], v[148:151], v[112:115], v[64:79]\n\t"
        "ds_read_b64_tr_b16 v[112:113], v123 offset:6400\n\t"
        "ds_read_b64_tr_b16 v[114:115], v123 offset:9472\n\t"
        "s_waitcnt lgkmcnt(10)\n\t"
        "v_mfma_f32_32x32x16_bf16 v[80:95], v[148:151], v[116:119], v[80:95]\n\t"
        "v_cmp_gt_u32 vcc, s47, v126\n\t"
        "ds_read_b64_tr_b16 v[116:117], v124 offset:6400\n\t"
        "ds_read_b64_tr_b16 v[118:119], v124 offset:9472\n\t"
        "s_waitcnt vmcnt(14) lgkmcnt(10)\n\t"
        "v_mfma_f32_32x32x16_bf16 v[0:15], v[96:99], v[152:155], v[0:15]\n\t"
        "s_cbranch_vccnz 16f\n\t"
        "17:\n\t"
        "ds_read_b64_tr_b16 v[96:97], v129 offset:0\n\t"
        "ds_read_b64_tr_b16 v[98:99], v129 offset:3072\n\t"
        "s_waitcnt lgkmcnt(10)\n\t"
        "v_mfma_f32_32x32x16_bf16 v[16:31], v[100:103], v[152:155], v[16:31]\n\t"
        "v_add_u32 v128, 1, v128\n\t"
        "ds_read_b64_tr_b16 v[100:101], v130 offset:0\n\t"
        "ds_read_b64_tr_b16 v[102:103], v130 offset:3072\n\t"
        "s_waitcnt lgkmcnt(10)\n\t"
        "v_mfma_f32_32x32x16_bf16 v[32:47], v[104:107], v[152:155], v[32:47]\n\t"
        "ds_read_b64_tr_b16 v[104:105], v129 offset:128\n\t"
        "ds_read_b64_tr_b16 v[106:107], v129 offset:3200\n\t"
        "s_add_u32 s44, s44, 0x1000\n\t"
        "s_and_b32 s44, s44, 0xffff\n\t"
        "buffer_load_dwordx4 v[148:151], v120, s[40:43], s44 offen offset:0 sc1\n\t"
        "s_waitcnt lgkmcnt(10)\n\t"
        "v_mfma_f32_32x32x16_bf16 v[48:63], v[108:111], v[152:155], v[48:63]\n\t"
        "s_add_u32 s46, s46, 12288\n\t"
        "s_add_u32 s47, s47, 1\n\t"
        "ds_read_b64_tr_b16 v[108:109], v130 offset:128\n\t"
        "ds_read_b64_tr_b16 v[110:111], v130 offset:3200\n\t"
        "s_waitcnt lgkmcnt(10)\n\t"
        "v_mfma_f32_32x32x16_bf16 v[64:79], v[152:155], v[112:115], v[64:79]\n\t"
        "s_cmp_eq_u32 s46, 110592\n\t"
        "s_cselect_b32 s46, 0, s46\n\t"
        "ds_read_b64_tr_b16 v[112:113], v129 offset:256\n\t"
        "ds_read_b64_tr_b16 v[114:115], v129 offset:3328\n\t"
        "s_waitcnt lgkmcnt(10)\n\t"
        "v_mfma_f32_32x32x16_bf16 v[80:95], v[152:155], v[116:119], v[80:95]\n\t"
        "ds_write_b32 v127, v128\n\t"
        "ds_read_b64_tr_b16 v[116:117], v130 offset:256\n\t"
        "ds_read_b64_tr_b16 v[118:119], v130 offset:3328\n\t"
        "s_waitcnt vmcnt(14) lgkmcnt(11)\n\t"
        "v_mfma_f32_32x32x16_bf16 v[0:15], v[96:99], v[156:159], v[0:15]\n\t"
        "ds_read_b32 v126, v125\n\t"
        "ds_read_b64_tr_b16 v[96:97], v129 offset:6144\n\t"
        "ds_read_b64_tr_b16 v[98:99], v129 offset:9216\n\t"
        "s_waitcnt lgkmcnt(12)\n\t"
        "v_mfma_f32_32x32x16_bf16 v[16:31], v[100:103], v[156:159], v[16:31]\n\t"
        "v_add_u32 v123, s46, v121\n\t"
        "ds_read_b64_tr_b16 v[100:101], v130 offset:6144\n\t"
        "ds_read_b64_tr_b16 v[102:103], v130 offset:9216\n\t"
        "s_waitcnt lgkmcnt(12)\n\t"
        "v_mfma_f32_32x32x16_bf16 v[32:47], v[104:107], v[156:159], v[32:47]\n\t"
        "ds_read_b64_tr_b16 v[104:105], v129 offset:6272\n\t"
        "ds_read_b64_tr_b16 v[106:107], v129 offset:9344\n\t"
        "buffer_load_dwordx4 v[152:155], v120, s[40:43], s44 offen offset:1024 sc1\n\t"
        "s_waitcnt lgkmcnt(12)\n\t"
        "v_mfma_f32_32x32x16_bf16 v[48:63], v[108:111], v[156:159], v[48:63]\n\t"
        "v_add_u32 v124, s46, v122\n\t"
        "ds_read_b64_tr_b16 v[108:109], v130 offset:6272\n\t"
        "ds_read_b64_tr_b16 v[110:111], v130 offset:9344\n\t"
        "s_waitcnt lgkmcnt(12)\n\t"
        "v_mfma_f32_32x32x16_bf16 v[64:79], v[156:159], v[112:115], v[64:79]\n\t"
        "ds_read_b64_tr_b16 v[112:113], v129 offset:6400\n\t"
        "ds_read_b64_tr_b16 v[114:115], v129 offset:9472\n\t"
        "s_waitcnt lgkmcnt(10)\n\t"
        "v_mfma_f32_32x32x16_bf16 v[80:95], v[156:159], v[116:119], v[80:95]\n\t"
        "v_cmp_gt_u32 vcc, s47, v126\n\t"
        "ds_read_b64_tr_b16 v[116:117], v130 offset:6400\n\t"
        "ds_read_b64_tr_b16 v[118:119], v130 offset:9472\n\t"
        "s_waitcnt vmcnt(14) lgkmcnt(10)\n\t"
        "v_mfma_f32_32x32x16_bf16 v[0:15], v[96:99], v[160:163], v[0:15]\n\t"
        "s_cbranch_vccnz 18f\n\t"
        "19:\n\t"
        "ds_read_b64_tr_b16 v[96:97], v123 offset:0\n\t"
        "ds_read_b64_tr_b16 v[98:99], v123 offset:3072\n\t"
        "s_waitcnt lgkmcnt(10)\n\t"
        "v_mfma_f32_32x32x16_bf16 v[16:31], v[100:103], v[160:163], v[16:31]\n\t"
        "v_add_u32 v128, 1, v128\n\t"
        "ds_read_b64_tr_b16 v[100:101], v124 offset:0\n\t"
        "ds_read_b64_tr_b16 v[102:103], v124 offset:3072\n\t"
        "s_waitcnt lgkmcnt(10)\n\t"
        "v_mfma_f32_32x32x16_bf16 v[32:47], v[104:107], v[160:163], v[32:47]\n\t"
        "ds_read_b64_tr_b16 v[104:105], v123 offset:128\n\t"
        "ds_read_b64_tr_b16 v[106:107], v123 offset:3200\n\t"
        "buffer_load_dwordx4 v[156:159], v120, s[40:43], s44 offen offset:2048 sc1\n\t"
        "s_waitcnt lgkmcnt(10)\n\t"
        "v_mfma_f32_32x32x16_bf16 v[48:63], v[108:111], v[160:163], v[48:63]\n\t"
        "s_add_u32 s46, s46, 12288\n\t"
        "s_add_u32 s47, s47, 1\n\t"
        "ds_read_b64_tr_b16 v[108:109], v124 offset:128\n\t"
        "ds_read_b64_tr_b16 v[110:111], v124 offset:3200\n\t"
        "s_waitcnt lgkmcnt(10)\n\t"
        "v_mfma_f32_32x32x16_bf16 v[64:79], v[160:163], v[112:115], v[64:79]\n\t"
        "s_cmp_eq_u32 s46, 110592\n\t"
        "s_cselect_b32 s46, 0, s46\n\t"
        "ds_read_b64_tr_b16 v[112:113], v123 offset:256\n\t"
        "ds_read_b64_tr_b16 v[114:115], v123 offset:3328\n\t"
        "s_waitcnt lgkmcnt(10)\n\t"
        "v_mfma_f32_32x32x16_bf16 v[80:95], v[160:163], v[116:119], v[80:95]\n\t"
        "ds_write_b32 v127, v128\n\t"
        "ds_read_b64_tr_b16 v[116:117], v124 offset:256\n\t"
        "ds_read_b64_tr_b16 v[118:119], v124 offset:3328\n\t"
        "s_waitcnt vmcnt(14) lgkmcnt(11)\n\t"
        "v_mfma_f32_32x32x16_bf16 v[0:15], v[96:99], v[164:167], v[0:15]\n\t"
        "ds_read_b32 v126, v125\n\t"
        "ds_read_b64_tr_b16 v[96:97], v123 offset:6144\n\t"
        "ds_read_b64_tr_b16 v[98:99], v123 offset:9216\n\t"
        "s_waitcnt lgkmcnt(12)\n\t"
        "v_mfma_f32_32x32x16_bf16 v[16:31], v[100:103], v[164:167], v[16:31]\n\t"
        "v_add_u32 v129, s46, v121\n\t"
        "ds_read_b64_tr_b16 v[100:101], v124 offset:6144\n\t"
        "ds_read_b64_tr_b16 v[102:103], v124 offset:9216\n\t"
        "s_waitcnt lgkmcnt(12)\n\t"
        "v_mfma_f32_32x32x16_bf16 v[32:47], v[104:107], v[164:167], v[32:47]\n\t"
        "ds_read_b64_tr_b16 v[104:105], v123 offset:6272\n\t"
        "ds_read_b64_tr_b16 v[106:107], v123 offset:9344\n\t"
        "buffer_load_dwordx4 v[160:163], v120, s[40:43], s44 offen offset:3072 sc1\n\t"
        "s_waitcnt lgkmcnt(12)\n\t"
        "v_mfma_f32_32x32x16_bf16 v[48:63], v[108:111], v[164:167], v[48:63]\n\t"
        "v_add_u32 v130, s46, v122\n\t"
        "ds_read_b64_tr_b16 v[108:109], v124 offset:6272\n\t"
        "ds_read_b64_tr_b16 v[110:111], v124 offset:9344\n\t"
        "s_waitcnt lgkmcnt(12)\n\t"
        "v_mfma_f32_32x32x16_bf16 v[64:79], v[164:167], v[112:115], v[64:79]\n\t"
        "ds_read_b64_tr_b16 v[112:113], v123 offset:6400\n\t"
        "ds_read_b64_tr_b16 v[114:115], v123 offset:9472\n\t"
        "s_waitcnt lgkmcnt(10)\n\t"
        "v_mfma_f32_32x32x16_bf16 v[80:95], v[164:167], v[116:119], v[80:95]\n\t"
        "v_cmp_gt_u32 vcc, s47, v126\n\t"
        "ds_read_b64_tr_b16 v[116:117], v124 offset:6400\n\t"
        "ds_read_b64_tr_b16 v[118:119], v124 offset:9472\n\t"
        "s_waitcnt vmcnt(14) lgkmcnt(10)\n\t"
        "v_mfma_f32_32x32x16_bf16 v[0:15], v[96:99], v[168:171], v[0:15]\n\t"
        "s_cbranch_vccnz 20f\n\t"
        "21:\n\t"
        "ds_read_b64_tr_b16 v[96:97], v129 offset:0\n\t"
        "ds_read_b64_tr_b16 v[98:99], v129 offset:3072\n\t"
        "s_waitcnt lgkmcnt(10)\n\t"
        "v_mfma_f32_32x32x16_bf16 v[16:31], v[100:103], v[168:171], v[16:31]\n\t"
        "v_add_u32 v128, 1, v128\n\t"
        "ds_read_b64_tr_b16 v[100:101], v130 offset:0\n\t"
        "ds_read_b64_tr_b16 v[102:103], v130 offset:3072\n\t"
        "s_waitcnt lgkmcnt(10)\n\t"
        "v_mfma_f32_32x32x16_bf16 v[32:47], v[104:107], v[168:171], v[32:47]\n\t"
        "ds_read_b64_tr_b16 v[104:105], v129 offset:128\n\t"
        "ds_read_b64_tr_b16 v[106:107], v129 offset:3200\n\t"
        "s_add_u32 s44, s44, 0x1000\n\t"
        "s_and_b32 s44, s44, 0xffff\n\t"
        "buffer_load_dwordx4 v[164:167], v120, s[40:43], s44 offen offset:0 sc1\n\t"
        "s_waitcnt lgkmcnt(10)\n\t"
        "v_mfma_f32_32x32x16_bf16 v[48:63], v[108:111], v[168:171], v[48:63]\n\t"
        "s_add_u32 s46, s46, 12288\n\t"
        "s_add_u32 s47, s47, 1\n\t"
        "ds_read_b64_tr_b16 v[108:109], v130 offset:128\n\t"
        "ds_read_b64_tr_b16 v[110:111], v130 offset:3200\n\t"
        "s_waitcnt lgkmcnt(10)\n\t"
        "v_mfma_f32_32x32x16_bf16 v[64:79], v[168:171], v[112:115], v[64:79]\n\t"
        "s_cmp_eq_u32 s46, 110592\n\t"
        "s_cselect_b32 s46, 0, s46\n\t"
        "ds_read_b64_tr_b16 v[112:113], v129 offset:256\n\t"
        "ds_read_b64_tr_b16 v[114:115], v129 offset:3328\n\t"
        "s_waitcnt lgkmcnt(10)\n\t"
        "v_mfma_f32_32x32x16_bf16 v[80:95], v[168:171], v[116:119], v[80:95]\n\t"
        "ds_write_b32 v127, v128\n\t"
        "ds_read_b64_tr_b16 v[116:117], v130 offset:256\n\t"
        "ds_read_b64_tr_b16 v[118:119], v130 offset:3328\n\t"
        "s_waitcnt vmcnt(14) lgkmcnt(11)\n\t"
        "v_mfma_f32_32x32x16_bf16 v[0:15], v[96:99], v[172:175], v[0:15]\n\t"
        "ds_read_b32 v126, v125\n\t"
        "ds_read_b64_tr_b16 v[96:97], v129 offset:6144\n\t"
        "ds_read_b64_tr_b16 v[98:99], v129 offset:9216\n\t"
        "s_waitcnt lgkmcnt(12)\n\t"
        "v_mfma_f32_32x32x16_bf16 v[16:31], v[100:103], v[172:175], v[16:31]\n\t"
        "v_add_u32 v123, s46, v121\n\t"
        "ds_read_b64_tr_b16 v[100:101], v130 offset:6144\n\t"
        "ds_read_b64_tr_b16 v[102:103], v130 offset:9216\n\t"
        "s_waitcnt lgkmcnt(12)\n\t"
        "v_mfma_f32_32x32x16_bf16 v[32:47], v[104:107], v[172:175], v[32:47]\n\t"
        "ds_read_b64_tr_b16 v[104:105], v129 offset:6272\n\t"
        "ds_read_b64_tr_b16 v[106:107], v129 offset:9344\n\t"
        "buffer_load_dwordx4 v[168:171], v120, s[40:43], s44 offen offset:1024 sc1\n\t"
        "s_waitcnt lgkmcnt(12)\n\t"
        "v_mfma_f32_32x32x16_bf16 v[48:63], v[108:111], v[172:175], v[48:63]\n\t"
        "v_add_u32 v124, s46, v122\n\t"
        "ds_read_b64_tr_b16 v[108:109], v130 offset:6272\n\t"
        "ds_read_b64_tr_b16 v[110:111], v130 offset:9344\n\t"
        "s_waitcnt lgkmcnt(12)\n\t"
        "v_mfma_f32_32x32x16_bf16 v[64:79], v[172:175], v[112:115], v[64:79]\n\t"
        "ds_read_b64_tr_b16 v[112:113], v129 offset:6400\n\t"
        "ds_read_b64_tr_b16 v[114:115], v129 offset:9472\n\t"
        "s_waitcnt lgkmcnt(10)\n\t"
        "v_mfma_f32_32x32x16_bf16 v[80:95], v[172:175], v[116:119], v[80:95]\n\t"
        "v_cmp_gt_u32 vcc, s47, v126\n\t"
        "ds_read_b64_tr_b16 v[116:117], v130 offset:6400\n\t"
        "ds_read_b64_tr_b16 v[118:119], v130 offset:9472\n\t"
        "s_waitcnt vmcnt(14) lgkmcnt(10)\n\t"
        "v_mfma_f32_32x32x16_bf16 v[0:15], v[96:99], v[176:179], v[0:15]\n\t"
        "s_cbranch_vccnz 22f\n\t"
        "23:\n\t"
        "ds_read_b64_tr_b16 v[96:97], v123 offset:0\n\t"
        "ds_read_b64_tr_b16 v[98:99], v123 offset:3072\n\t"
        "s_waitcnt lgkmcnt(10)\n\t"
        "v_mfma_f32_32x32x16_bf16 v[16:31], v[100:103], v[176:179], v[16:31]\n\t"
        "v_add_u32 v128, 1, v128\n\t"
        "ds_read_b64_tr_b16 v[100:101], v124 offset:0\n\t"
        "ds_read_b64_tr_b16 v[102:103], v124 offset:3072\n\t"
        "s_waitcnt lgkmcnt(10)\n\t"
        "v_mfma_f32_32x32x16_bf16 v[32:47], v[104:107], v[176:179], v[32:47]\n\t"
        "ds_read_b64_tr_b16 v[104:105], v123 offset:128\n\t"
        "ds_read_b64_tr_b16 v[106:107], v123 offset:3200\n\t"
        "buffer_load_dwordx4 v[172:175], v120, s[40:43], s44 offen offset:2048 sc1\n\t"
        "s_waitcnt lgkmcnt(10)\n\t"
        "v_mfma_f32_32x32x16_bf16 v[48:63], v[108:111], v[176:179], v[48:63]\n\t"
        "s_add_u32 s46, s46, 12288\n\t"
        "s_add_u32 s47, s47, 1\n\t"
        "ds_read_b64_tr_b16 v[108:109], v124 offset:128\n\t"
        "ds_read_b64_tr_b16 v[110:111], v124 offset:3200\n\t"
        "s_waitcnt lgkmcnt(10)\n\t"
        "v_mfma_f32_32x32x16_bf16 v[64:79], v[176:179], v[112:115], v[64:79]\n\t"
        "s_cmp_eq_u32 s46, 110592\n\t"
        "s_cselect_b32 s46, 0, s46\n\t"
        "ds_read_b64_tr_b16 v[112:113], v123 offset:256\n\t"
        "ds_read_b64_tr_b16 v[114:115], v123 offset:3328\n\t"
        "s_waitcnt lgkmcnt(10)\n\t"
        "v_mfma_f32_32x32x16_bf16 v[80:95], v[176:179], v[116:119], v[80:95]\n\t"
        "ds_write_b32 v127, v128\n\t"
        "ds_read_b64_tr_b16 v[116:117], v124 offset:256\n\t"
        "ds_read_b64_tr_b16 v[118:119], v124 offset:3328\n\t"
        "s_waitcnt vmcnt(14) lgkmcnt(11)\n\t"
        "v_mfma_f32_32x32x16_bf16 v[0:15], v[96:99], v[180:183], v[0:15]\n\t"
        "ds_read_b32 v126, v125\n\t"
        "ds_read_b64_tr_b16 v[96:97], v123 offset:6144\n\t"
        "ds_read_b64_tr_b16 v[98:99], v123 offset:9216\n\t"
        "s_waitcnt lgkmcnt(12)\n\t"
        "v_mfma_f32_32x32x16_bf16 v[16:31], v[100:103], v[180:183], v[16:31]\n\t"
        "v_add_u32 v129, s46, v121\n\t"
        "ds_read_b64_tr_b16 v[100:101], v124 offset:6144\n\t"
        "ds_read_b64_tr_b16 v[102:103], v124 offset:9216\n\t"
        "s_waitcnt lgkmcnt(12)\n\t"
        "v_mfma_f32_32x32x16_bf16 v[32:47], v[104:107], v[180:183], v[32:47]\n\t"
        "ds_read_b64_tr_b16 v[104:105], v123 offset:6272\n\t"
        "ds_read_b64_tr_b16 v[106:107], v123 offset:9344\n\t"
        "buffer_load_dwordx4 v[176:179], v120, s[40:43], s44 offen offset:3072 sc1\n\t"
        "s_waitcnt lgkmcnt(12)\n\t"
        "v_mfma_f32_32x32x16_bf16 v[48:63], v[108:111], v[180:183], v[48:63]\n\t"
        "v_add_u32 v130, s46, v122\n\t"
        "ds_read_b64_tr_b16 v[108:109], v124 offset:6272\n\t"
        "ds_read_b64_tr_b16 v[110:111], v124 offset:9344\n\t"
        "s_waitcnt lgkmcnt(12)\n\t"
        "v_mfma_f32_32x32x16_bf16 v[64:79], v[180:183], v[112:115], v[64:79]\n\t"
        "ds_read_b64_tr_b16 v[112:113], v123 offset:6400\n\t"
        "ds_read_b64_tr_b16 v[114:115], v123 offset:9472\n\t"
        "s_waitcnt lgkmcnt(10)\n\t"
        "v_mfma_f32_32x32x16_bf16 v[80:95], v[180:183], v[116:119], v[80:95]\n\t"
        "v_cmp_gt_u32 vcc, s47, v126\n\t"
        "ds_read_b64_tr_b16 v[116:117], v124 offset:6400\n\t"
        "ds_read_b64_tr_b16 v[118:119], v124 offset:9472\n\t"
        "s_waitcnt vmcnt(14) lgkmcnt(10)\n\t"
        "v_mfma_f32_32x32x16_bf16 v[0:15], v[96:99], v[184:187], v[0:15]\n\t"
        "s_cbranch_vccnz 24f\n\t"
        "25:\n\t"
        "ds_read_b64_tr_b16 v[96:97], v129 offset:0\n\t"
        "ds_read_b64_tr_b16 v[98:99], v129 offset:3072\n\t"
        "s_waitcnt lgkmcnt(10)\n\t"
        "v_mfma_f32_32x32x16_bf16 v[16:31], v[100:103], v[184:187], v[16:31]\n\t"
        "v_add_u32 v128, 1, v128\n\t"
        "ds_read_b64_tr_b16 v[100:101], v130 offset:0\n\t"
        "ds_read_b64_tr_b16 v[102:103], v130 offset:3072\n\t"
        "s_waitcnt lgkmcnt(10)\n\t"
        "v_mfma_f32_32x32x16_bf16 v[32:47], v[104:107], v[184:187], v[32:47]\n\t"
        "ds_read_b64_tr_b16 v[104:105], v129 offset:128\n\t"
        "ds_read_b64_tr_b16 v[106:107], v129 offset:3200\n\t"
        "s_add_u32 s44, s44, 0x1000\n\t"
        "s_and_b32 s44, s44, 0xffff\n\t"
        "buffer_load_dwordx4 v[180:183], v120, s[40:43], s44 offen offset:0 sc1\n\t"
        "s_waitcnt lgkmcnt(10)\n\t"
        "v_mfma_f32_32x32x16_bf16 v[48:63], v[108:111], v[184:187], v[48:63]\n\t"
        "s_add_u32 s46, s46, 12288\n\t"
        "s_add_u32 s47, s47, 1\n\t"
        "ds_read_b64_tr_b16 v[108:109], v130 offset:128\n\t"
        "ds_read_b64_tr_b16 v[110:111], v130 offset:3200\n\t"
        "s_waitcnt lgkmcnt(10)\n\t"
        "v_mfma_f32_32x32x16_bf16 v[64:79], v[184:187], v[112:115], v[64:79]\n\t"
        "s_cmp_eq_u32 s46, 110592\n\t"
        "s_cselect_b32 s46, 0, s46\n\t"
        "ds_read_b64_tr_b16 v[112:113], v129 offset:256\n\t"
        "ds_read_b64_tr_b16 v[114:115], v129 offset:3328\n\t"
        "s_waitcnt lgkmcnt(10)\n\t"
        "v_mfma_f32_32x32x16_bf16 v[80:95], v[184:187], v[116:119], v[80:95]\n\t"
        "ds_write_b32 v127, v128\n\t"
        "ds_read_b64_tr_b16 v[116:117], v130 offset:256\n\t"
        "ds_read_b64_tr_b16 v[118:119], v130 offset:3328\n\t"
        "s_waitcnt vmcnt(14) lgkmcnt(11)\n\t"
        "v_mfma_f32_32x32x16_bf16 v[0:15], v[96:99], v[188:191], v[0:15]\n\t"
        "ds_read_b32 v126, v125\n\t"
        "ds_read_b64_tr_b16 v[96:97], v129 offset:6144\n\t"
        "ds_read_b64_tr_b16 v[98:99], v129 offset:9216\n\t"
        "s_waitcnt lgkmcnt(12)\n\t"
        "v_mfma_f32_32x32x16_bf16 v[16:31], v[100:103], v[188:191], v[16:31]\n\t"
        "v_add_u32 v123, s46, v121\n\t"
        "ds_read_b64_tr_b16 v[100:101], v130 offset:6144\n\t"
        "ds_read_b64_tr_b16 v[102:103], v130 offset:9216\n\t"
        "s_waitcnt lgkmcnt(12)\n\t"
        "v_mfma_f32_32x32x16_bf16 v[32:47], v[104:107], v[188:191], v[32:47]\n\t"
        "ds_read_b64_tr_b16 v[104:105], v129 offset:6272\n\t"
        "ds_read_b64_tr_b16 v[106:107], v129 offset:9344\n\t"
        "buffer_load_dwordx4 v[184:187], v120, s[40:43], s44 offen offset:1024 sc1\n\t"
        "s_waitcnt lgkmcnt(12)\n\t"
        "v_mfma_f32_32x32x16_bf16 v[48:63], v[108:111], v[188:191], v[48:63]\n\t"
        "v_add_u32 v124, s46, v122\n\t"
        "ds_read_b64_tr_b16 v[108:109], v130 offset:6272\n\t"
        "ds_read_b64_tr_b16 v[110:111], v130 offset:9344\n\t"
        "s_waitcnt lgkmcnt(12)\n\t"
        "v_mfma_f32_32x32x16_bf16 v[64:79], v[188:191], v[112:115], v[64:79]\n\t"
        "ds_read_b64_tr_b16 v[112:113], v129 offset:6400\n\t"
        "ds_read_b64_tr_b16 v[114:115], v129 offset:9472\n\t"
        "s_waitcnt lgkmcnt(10)\n\t"
        "v_mfma_f32_32x32x16_bf16 v[80:95], v[188:191], v[116:119], v[80:95]\n\t"
        "v_cmp_gt_u32 vcc, s47, v126\n\t"
        "ds_read_b64_tr_b16 v[116:117], v130 offset:6400\n\t"
        "ds_read_b64_tr_b16 v[118:119], v130 offset:9472\n\t"
        "s_waitcnt vmcnt(14) lgkmcnt(10)\n\t"
        "v_mfma_f32_32x32x16_bf16 v[0:15], v[96:99], v[192:195], v[0:15]\n\t"
        "s_cbranch_vccnz 26f\n\t"
        "27:\n\t"
        "ds_read_b64_tr_b16 v[96:97], v123 offset:0\n\t"
        "ds_read_b64_tr_b16 v[98:99], v123 offset:3072\n\t"
        "s_waitcnt lgkmcnt(10)\n\t"
        "v_mfma_f32_32x32x16_bf16 v[16:31], v[100:103], v[192:195], v[16:31]\n\t"
        "v_add_u32 v128, 1, v128\n\t"
        "ds_read_b64_tr_b16 v[100:101], v124 offset:0\n\t"
        "ds_read_b64_tr_b16 v[102:103], v124 offset:3072\n\t"
        "s_waitcnt lgkmcnt(10)\n\t"
        "v_mfma_f32_32x32x16_bf16 v[32:47], v[104:107], v[192:195], v[32:47]\n\t"
        "ds_read_b64_tr_b16 v[104:105], v123 offset:128\n\t"
        "ds_read_b64_tr_b16 v[106:107], v123 offset:3200\n\t"
        "buffer_load_dwordx4 v[188:191], v120, s[40:43], s44 offen offset:2048 sc1\n\t"
        "s_waitcnt lgkmcnt(10)\n\t"
        "v_mfma_f32_32x32x16_bf16 v[48:63], v[108:111], v[192:195], v[48:63]\n\t"
        "s_add_u32 s46, s46, 12288\n\t"
        "s_add_u32 s47, s47, 1\n\t"
        "ds_read_b64_tr_b16 v[108:109], v124 offset:128\n\t"
        "ds_read_b64_tr_b16 v[110:111], v124 offset:3200\n\t"
        "s_waitcnt lgkmcnt(10)\n\t"
        "v_mfma_f32_32x32x16_bf16 v[64:79], v[192:195], v[112:115], v[64:79]\n\t"
        "s_cmp_eq_u32 s46, 110592\n\t"
        "s_cselect_b32 s46, 0, s46\n\t"
        "ds_read_b64_tr_b16 v[112:113], v123 offset:256\n\t"
        "ds_read_b64_tr_b16 v[114:115], v123 offset:3328\n\t"
        "s_waitcnt lgkmcnt(10)\n\t"
        "v_mfma_f32_32x32x16_bf16 v[80:95], v[192:195], v[116:119], v[80:95]\n\t"
        "ds_write_b32 v127, v128\n\t"
        "ds_read_b64_tr_b16 v[116:117], v124 offset:256\n\t"
        "ds_read_b64_tr_b16 v[118:119], v124 offset:3328\n\t"
        "s_waitcnt vmcnt(14) lgkmcnt(11)\n\t"
        "v_mfma_f32_32x32x16_bf16 v[0:15], v[96:99], v[132:135], v[0:15]\n\t"
        "ds_read_b32 v126, v125\n\t"
        "ds_read_b64_tr_b16 v[96:97], v123 offset:6144\n\t"
        "ds_read_b64_tr_b16 v[98:99], v123 offset:9216\n\t"
        "s_waitcnt lgkmcnt(12)\n\t"
        "v_mfma_f32_32x32x16_bf16 v[16:31], v[100:103], v[132:135], v[16:31]\n\t"
        "v_add_u32 v129, s46, v121\n\t"
        "ds_read_b64_tr_b16 v[100:101], v124 offset:6144\n\t"
        "ds_read_b64_tr_b16 v[102:103], v124 offset:9216\n\t"
        "s_waitcnt lgkmcnt(12)\n\t"
        "v_mfma_f32_32x32x16_bf16 v[32:47], v[104:107], v[132:135], v[32:47]\n\t"
        "ds_read_b64_tr_b16 v[104:105], v123 offset:6272\n\t"
        "ds_read_b64_tr_b16 v[106:107], v123 offset:9344\n\t"
        "buffer_load_dwordx4 v[192:195], v120, s[40:43], s44 offen offset:3072 sc1\n\t"
        "s_waitcnt lgkmcnt(12)\n\t"
        "v_mfma_f32_32x32x16_bf16 v[48:63], v[108:111], v[132:135], v[48:63]\n\t"
        "v_add_u32 v130, s46, v122\n\t"
        "ds_read_b64_tr_b16 v[108:109], v124 offset:6272\n\t"
        "ds_read_b64_tr_b16 v[110:111], v124 offset:9344\n\t"
        "s_waitcnt lgkmcnt(12)\n\t"
        "v_mfma_f32_32x32x16_bf16 v[64:79], v[132:135], v[112:115], v[64:79]\n\t"
        "ds_read_b64_tr_b16 v[112:113], v123 offset:6400\n\t"
        "ds_read_b64_tr_b16 v[114:115], v123 offset:9472\n\t"
        "s_waitcnt lgkmcnt(10)\n\t"
        "v_mfma_f32_32x32x16_bf16 v[80:95], v[132:135], v[116:119], v[80:95]\n\t"
        "v_cmp_gt_u32 vcc, s47, v126\n\t"
        "ds_read_b64_tr_b16 v[116:117], v124 offset:6400\n\t"
        "ds_read_b64_tr_b16 v[118:119], v124 offset:9472\n\t"
        "s_waitcnt vmcnt(14) lgkmcnt(10)\n\t"
        "v_mfma_f32_32x32x16_bf16 v[0:15], v[96:99], v[136:139], v[0:15]\n\t"
        "s_cbranch_vccnz 28f\n\t"
        "29:\n\t"
        "ds_read_b64_tr_b16 v[96:97], v129 offset:0\n\t"
        "ds_read_b64_tr_b16 v[98:99], v129 offset:3072\n\t"
        "s_waitcnt lgkmcnt(10)\n\t"
        "v_mfma_f32_32x32x16_bf16 v[16:31], v[100:103], v[136:139], v[16:31]\n\t"
        "v_add_u32 v128, 1, v128\n\t"
        "ds_read_b64_tr_b16 v[100:101], v130 offset:0\n\t"
        "ds_read_b64_tr_b16 v[102:103], v130 offset:3072\n\t"
        "s_waitcnt lgkmcnt(10)\n\t"
        "v_mfma_f32_32x32x16_bf16 v[32:47], v[104:107], v[136:139], v[32:47]\n\t"
        "ds_read_b64_tr_b16 v[104:105], v129 offset:128\n\t"
        "ds_read_b64_tr_b16 v[106:107], v129 offset:3200\n\t"
        "s_add_u32 s44, s44, 0x1000\n\t"
        "s_and_b32 s44, s44, 0xffff\n\t"
        "buffer_load_dwordx4 v[132:135], v120, s[40:43], s44 offen offset:0 sc1\n\t"
        "s_waitcnt lgkmcnt(10)\n\t"
        "v_mfma_f32_32x32x16_bf16 v[48:63], v[108:111], v[136:139], v[48:63]\n\t"
        "s_add_u32 s46, s46, 12288\n\t"
        "s_add_u32 s47, s47, 1\n\t"
        "ds_read_b64_tr_b16 v[108:109], v130 offset:128\n\t"
        "ds_read_b64_tr_b16 v[110:111], v130 offset:3200\n\t"
        "s_waitcnt lgkmcnt(10)\n\t"
        "v_mfma_f32_32x32x16_bf16 v[64:79], v[136:139], v[112:115], v[64:79]\n\t"
        "s_cmp_eq_u32 s46, 110592\n\t"
        "s_cselect_b32 s46, 0, s46\n\t"
        "ds_read_b64_tr_b16 v[112:113], v129 offset:256\n\t"
        "ds_read_b64_tr_b16 v[114:115], v129 offset:3328\n\t"
        "s_waitcnt lgkmcnt(10)\n\t"
        "v_mfma_f32_32x32x16_bf16 v[80:95], v[136:139], v[116:119], v[80:95]\n\t"
        "ds_write_b32 v127, v128\n\t"
        "ds_read_b64_tr_b16 v[116:117], v130 offset:256\n\t"
        "ds_read_b64_tr_b16 v[118:119], v130 offset:3328\n\t"
        "s_waitcnt vmcnt(14) lgkmcnt(11)\n\t"
        "v_mfma_f32_32x32x16_bf16 v[0:15], v[96:99], v[140:143], v[0:15]\n\t"
        "ds_read_b32 v126, v125\n\t"
        "ds_read_b64_tr_b16 v[96:97], v129 offset:6144\n\t"
        "ds_read_b64_tr_b16 v[98:99], v129 offset:9216\n\t"
        "s_waitcnt lgkmcnt(12)\n\t"
        "v_mfma_f32_32x32x16_bf16 v[16:31], v[100:103], v[140:143], v[16:31]\n\t"
        "v_add_u32 v123, s46, v121\n\t"
        "ds_read_b64_tr_b16 v[100:101], v130 offset:6144\n\t"
        "ds_read_b64_tr_b16 v[102:103], v130 offset:9216\n\t"
        "s_waitcnt lgkmcnt(12)\n\t"
        "v_mfma_f32_32x32x16_bf16 v[32:47], v[104:107], v[140:143], v[32:47]\n\t"
        "ds_read_b64_tr_b16 v[104:105], v129 offset:6272\n\t"
        "ds_read_b64_tr_b16 v[106:107], v129 offset:9344\n\t"
        "buffer_load_dwordx4 v[136:139], v120, s[40:43], s44 offen offset:1024 sc1\n\t"
        "s_waitcnt lgkmcnt(12)\n\t"
        "v_mfma_f32_32x32x16_bf16 v[48:63], v[108:111], v[140:143], v[48:63]\n\t"
        "v_add_u32 v124, s46, v122\n\t"
        "ds_read_b64_tr_b16 v[108:109], v130 offset:6272\n\t"
        "ds_read_b64_tr_b16 v[110:111], v130 offset:9344\n\t"
        "s_waitcnt lgkmcnt(12)\n\t"
        "v_mfma_f32_32x32x16_bf16 v[64:79], v[140:143], v[112:115], v[64:79]\n\t"
        "ds_read_b64_tr_b16 v[112:113], v129 offset:6400\n\t"
        "ds_read_b64_tr_b16 v[114:115], v129 offset:9472\n\t"
        "s_waitcnt lgkmcnt(10)\n\t"
        "v_mfma_f32_32x32x16_bf16 v[80:95], v[140:143], v[116:119], v[80:95]\n\t"
        "v_cmp_gt_u32 vcc, s47, v126\n\t"
        "ds_read_b64_tr_b16 v[116:117], v130 offset:6400\n\t"
        "ds_read_b64_tr_b16 v[118:119], v130 offset:9472\n\t"
        "s_waitcnt vmcnt(14) lgkmcnt(10)\n\t"
        "v_mfma_f32_32x32x16_bf16 v[0:15], v[96:99], v[144:147], v[0:15]\n\t"
        "s_cbranch_vccnz 30f\n\t"
        "31:\n\t"
        "ds_read_b64_tr_b16 v[96:97], v123 offset:0\n\t"
        "ds_read_b64_tr_b16 v[98:99], v123 offset:3072\n\t"
        "s_waitcnt lgkmcnt(10)\n\t"
        "v_mfma_f32_32x32x16_bf16 v[16:31], v[100:103], v[144:147], v[16:31]\n\t"
        "v_add_u32 v128, 1, v128\n\t"
        "ds_read_b64_tr_b16 v[100:101], v124 offset:0\n\t"
        "ds_read_b64_tr_b16 v[102:103], v124 offset:3072\n\t"
        "s_waitcnt lgkmcnt(10)\n\t"
        "v_mfma_f32_32x32x16_bf16 v[32:47], v[104:107], v[144:147], v[32:47]\n\t"
        "ds_read_b64_tr_b16 v[104:105], v123 offset:128\n\t"
        "ds_read_b64_tr_b16 v[106:107], v123 offset:3200\n\t"
        "buffer_load_dwordx4 v[140:143], v120, s[40:43], s44 offen offset:2048 sc1\n\t"
        "s_waitcnt lgkmcnt(10)\n\t"
        "v_mfma_f32_32x32x16_bf16 v[48:63], v[108:111], v[144:147], v[48:63]\n\t"
        "s_add_u32 s46, s46, 12288\n\t"
        "s_add_u32 s47, s47, 1\n\t"
        "ds_read_b64_tr_b16 v[108:109], v124 offset:128\n\t"
        "ds_read_b64_tr_b16 v[110:111], v124 offset:3200\n\t"
        "s_waitcnt lgkmcnt(10)\n\t"
        "v_mfma_f32_32x32x16_bf16 v[64:79], v[144:147], v[112:115], v[64:79]\n\t"
        "s_cmp_eq_u32 s46, 110592\n\t"
        "s_cselect_b32 s46, 0, s46\n\t"
        "ds_read_b64_tr_b16 v[112:113], v123 offset:256\n\t"
        "ds_read_b64_tr_b16 v[114:115], v123 offset:3328\n\t"
        "s_waitcnt lgkmcnt(10)\n\t"
        "v_mfma_f32_32x32x16_bf16 v[80:95], v[144:147], v[116:119], v[80:95]\n\t"
        "ds_write_b32 v127, v128\n\t"
        "ds_read_b64_tr_b16 v[116:117], v124 offset:256\n\t"
        "ds_read_b64_tr_b16 v[118:119], v124 offset:3328\n\t"
        "s_sub_u32 s45, s45, 1\n\t"
        "s_cmp_lg_u32 s45, 0\n\t"
        "s_cbranch_scc1 1b\n\t"
        "s_waitcnt vmcnt(14) lgkmcnt(11)\n\t"
        "v_mfma_f32_32x32x16_bf16 v[0:15], v[96:99], v[148:151], v[0:15]\n\t"
        "ds_read_b32 v126, v125\n\t"
        "s_add_u32 s47, s47, 4\n\t"
        "ds_read_b64_tr_b16 v[96:97], v123 offset:6400\n\t"
        "ds_read_b64_tr_b16 v[98:99], v123 offset:9472\n\t"
        "s_waitcnt lgkmcnt(12)\n\t"
        "v_mfma_f32_32x32x16_bf16 v[16:31], v[100:103], v[148:151], v[16:31]\n\t"
        "ds_read_b64_tr_b16 v[100:101], v124 offset:6400\n\t"
        "ds_read_b64_tr_b16 v[102:103], v124 offset:9472\n\t"
        "s_waitcnt lgkmcnt(4)\n\t"
        "v_cmp_gt_u32 vcc, s47, v126\n\t"
        "v_mfma_f32_32x32x16_bf16 v[32:47], v[104:107], v[148:151], v[32:47]\n\t"
        "s_cbranch_vccnz 74f\n\t"
        "75:\n\t"
        "ds_read_b64_tr_b16 v[104:105], v121 offset:256\n\t"
        "ds_read_b64_tr_b16 v[106:107], v121 offset:3328\n\t"
        "s_waitcnt lgkmcnt(12)\n\t"
        "v_mfma_f32_32x32x16_bf16 v[48:63], v[108:111], v[148:151], v[48:63]\n\t"
        "ds_read_b64_tr_b16 v[108:109], v122 offset:256\n\t"
        "ds_read_b64_tr_b16 v[110:111], v122 offset:3328\n\t"
        "s_waitcnt lgkmcnt(12)\n\t"
        "v_mfma_f32_32x32x16_bf16 v[64:79], v[148:151], v[112:115], v[64:79]\n\t"
        "ds_read_b64_tr_b16 v[112:113], v121 offset:6400\n\t"
        "ds_read_b64_tr_b16 v[114:115], v121 offset:9472\n\t"
        "s_waitcnt lgkmcnt(11)\n\t"
        "v_mfma_f32_32x32x16_bf16 v[80:95], v[148:151], v[116:119], v[80:95]\n\t"
        "ds_read_b64_tr_b16 v[116:117], v122 offset:6400\n\t"
        "ds_read_b64_tr_b16 v[118:119], v122 offset:9472\n\t"
        "s_waitcnt vmcnt(13) lgkmcnt(10)\n\t"
        "v_mfma_f32_32x32x16_bf16 v[64:79], v[152:155], v[96:99], v[64:79]\n\t"
        "ds_read_b64_tr_b16 v[96:97], v121 offset:12544\n\t"
        "ds_read_b64_tr_b16 v[98:99], v121 offset:15616\n\t"
        "s_waitcnt lgkmcnt(10)\n\t"
        "v_mfma_f32_32x32x16_bf16 v[80:95], v[152:155], v[100:103], v[80:95]\n\t"
        "ds_read_b64_tr_b16 v[100:101], v122 offset:12544\n\t"
        "ds_read_b64_tr_b16 v[102:103], v122 offset:15616\n\t"
        "s_waitcnt vmcnt(12) lgkmcnt(10)\n\t"
        "v_mfma_f32_32x32x16_bf16 v[64:79], v[156:159], v[104:107], v[64:79]\n\t"
        "ds_read_b128 v[196:199], v215 offset:0\n\t"
        "ds_read_b64_tr_b16 v[104:105], v121 offset:18688\n\t"
        "ds_read_b64_tr_b16 v[106:107], v121 offset:21760\n\t"
        "s_waitcnt lgkmcnt(11)\n\t"
        "v_mfma_f32_32x32x16_bf16 v[80:95], v[156:159], v[108:111], v[80:95]\n\t"
        "ds_read_b128 v[200:203], v215 offset:32\n\t"
        "ds_read_b64_tr_b16 v[108:109], v122 offset:18688\n\t"
        "ds_read_b64_tr_b16 v[110:111], v122 offset:21760\n\t"
        "s_waitcnt vmcnt(11) lgkmcnt(12)\n\t"
        "v_mfma_f32_32x32x16_bf16 v[64:79], v[160:163], v[112:115], v[64:79]\n\t"
        "ds_read_b128 v[204:207], v215 offset:64\n\t"
        "ds_read_b64_tr_b16 v[112:113], v121 offset:24832\n\t"
        "ds_read_b64_tr_b16 v[114:115], v121 offset:27904\n\t"
        "s_waitcnt lgkmcnt(13)\n\t"
        "v_mfma_f32_32x32x16_bf16 v[80:95], v[160:163], v[116:119], v[80:95]\n\t"
        "ds_read_b128 v[208:211], v215 offset:96\n\t"
        "ds_read_b64_tr_b16 v[116:117], v122 offset:24832\n\t"
        "ds_read_b64_tr_b16 v[118:119], v122 offset:27904\n\t"
        "s_waitcnt vmcnt(10) lgkmcnt(14)\n\t"
        "v_mfma_f32_32x32x16_bf16 v[64:79], v[164:167], v[96:99], v[64:79]\n\t"
        "ds_read_b64_tr_b16 v[96:97], v121 offset:30976\n\t"
        "ds_read_b64_tr_b16 v[98:99], v121 offset:34048\n\t"
        "s_waitcnt lgkmcnt(14)\n\t"
        "v_mfma_f32_32x32x16_bf16 v[80:95], v[164:167], v[100:103], v[80:95]\n\t"
        "ds_read_b64_tr_b16 v[100:101], v122 offset:30976\n\t"
        "ds_read_b64_tr_b16 v[102:103], v122 offset:34048\n\t"
        "s_waitcnt vmcnt(9) lgkmcnt(13)\n\t"
        "v_mfma_f32_32x32x16_bf16 v[64:79], v[168:171], v[104:107], v[64:79]\n\t"
        "ds_read_b64_tr_b16 v[104:105], v121 offset:37120\n\t"
        "ds_read_b64_tr_b16 v[106:107], v121 offset:40192\n\t"
        "s_waitcnt lgkmcnt(12)\n\t"
        "v_mfma_f32_32x32x16_bf16 v[80:95], v[168:171], v[108:111], v[80:95]\n\t"
        "ds_read_b64_tr_b16 v[108:109], v122 offset:37120\n\t"
        "ds_read_b64_tr_b16 v[110:111], v122 offset:40192\n\t"
        "s_waitcnt vmcnt(8) lgkmcnt(11)\n\t"
        "v_mfma_f32_32x32x16_bf16 v[64:79], v[172:175], v[112:115], v[64:79]\n\t"
        "ds_read_b64_tr_b16 v[112:113], v121 offset:43264\n\t"
        "ds_read_b64_tr_b16 v[114:115], v121 offset:46336\n\t"
        "s_waitcnt lgkmcnt(10)\n\t"
        "v_mfma_f32_32x32x16_bf16 v[80:95], v[172:175], v[116:119], v[80:95]\n\t"
        "ds_read_b64_tr_b16 v[116:117], v122 offset:43264\n\t"
        "ds_read_b64_tr_b16 v[118:119], v122 offset:46336\n\t"
        "s_waitcnt vmcnt(7) lgkmcnt(10)\n\t"
        "v_mfma_f32_32x32x16_bf16 v[64:79], v[176:179], v[96:99], v[64:79]\n\t"
        "ds_read_b64_tr_b16 v[96:97], v121 offset:49408\n\t"
        "ds_read_b64_tr_b16 v[98:99], v121 offset:52480\n\t"
        "s_waitcnt lgkmcnt(10)\n\t"
        "v_mfma_f32_32x32x16_bf16 v[80:95], v[176:179], v[100:103], v[80:95]\n\t"
        "ds_read_b64_tr_b16 v[100:101], v122 offset:49408\n\t"
        "ds_read_b64_tr_b16 v[102:103], v122 offset:52480\n\t"
        "s_waitcnt vmcnt(6) lgkmcnt(10)\n\t"
        "v_mfma_f32_32x32x16_bf16 v[64:79], v[180:183], v[104:107], v[64:79]\n\t"
        "ds_read_b64_tr_b16 v[104:105], v121 offset:55552\n\t"
        "ds_read_b64_tr_b16 v[106:107], v121 offset:58624\n\t"
        "s_waitcnt lgkmcnt(10)\n\t"
        "v_mfma_f32_32x32x16_bf16 v[80:95], v[180:183], v[108:111], v[80:95]\n\t"
        "ds_read_b64_tr_b16 v[108:109], v122 offset:55552\n\t"
        "ds_read_b64_tr_b16 v[110:111], v122 offset:58624\n\t"
        "s_waitcnt vmcnt(5) lgkmcnt(10)\n\t"
        "v_mfma_f32_32x32x16_bf16 v[64:79], v[184:187], v[112:115], v[64:79]\n\t"
        "ds_read_b64_tr_b16 v[112:113], v123 offset:6272\n\t"
        "ds_read_b64_tr_b16 v[114:115], v123 offset:9344\n\t"
        "s_waitcnt lgkmcnt(10)\n\t"
        "v_mfma_f32_32x32x16_bf16 v[80:95], v[184:187], v[116:119], v[80:95]\n\t"
        "ds_read_b64_tr_b16 v[116:117], v124 offset:6272\n\t"
        "ds_read_b64_tr_b16 v[118:119], v124 offset:9344\n\t"
        "s_waitcnt vmcnt(4) lgkmcnt(10)\n\t"
        "v_mfma_f32_32x32x16_bf16 v[64:79], v[188:191], v[96:99], v[64:79]\n\t"
        "ds_read_b64_tr_b16 v[96:97], v121 offset:128\n\t"
        "ds_read_b64_tr_b16 v[98:99], v121 offset:3200\n\t"
        "s_waitcnt lgkmcnt(10)\n\t"
        "v_mfma_f32_32x32x16_bf16 v[80:95], v[188:191], v[100:103], v[80:95]\n\t"
        "ds_read_b64_tr_b16 v[100:101], v122 offset:128\n\t"
        "ds_read_b64_tr_b16 v[102:103], v122 offset:3200\n\t"
        "s_waitcnt vmcnt(0) lgkmcnt(10)\n\t"
        "v_mfma_f32_32x32x16_bf16 v[64:79], v[192:195], v[104:107], v[64:79]\n\t"
        "ds_read_b64_tr_b16 v[104:105], v121 offset:6272\n\t"
        "ds_read_b64_tr_b16 v[106:107], v121 offset:9344\n\t"
        "s_waitcnt lgkmcnt(10)\n\t"
        "v_mfma_f32_32x32x16_bf16 v[80:95], v[192:195], v[108:111], v[80:95]\n\t"
        "ds_read_b64_tr_b16 v[108:109], v122 offset:6272\n\t"
        "ds_read_b64_tr_b16 v[110:111], v122 offset:9344\n\t"
        "s_waitcnt lgkmcnt(10)\n\t"
        "v_mfma_f32_32x32x16_bf16 v[32:47], v[112:115], v[152:155], v[32:47]\n\t"
        "ds_read_b64_tr_b16 v[112:113], v121 offset:12416\n\t"
        "ds_read_b64_tr_b16 v[114:115], v121 offset:15488\n\t"
        "s_waitcnt lgkmcnt(10)\n\t"
        "v_mfma_f32_32x32x16_bf16 v[48:63], v[116:119], v[152:155], v[48:63]\n\t"
        "ds_read_b64_tr_b16 v[116:117], v122 offset:12416\n\t"
        "ds_read_b64_tr_b16 v[118:119], v122 offset:15488\n\t"
        "v_mul_f32 v64, v196, v64\n\t"
        "v_mul_f32 v65, v197, v65\n\t"
        "v_mul_f32 v66, v198, v66\n\t"
        "s_waitcnt lgkmcnt(10)\n\t"
        "v_mfma_f32_32x32x16_bf16 v[32:47], v[96:99], v[156:159], v[32:47]\n\t"
        "ds_read_b64_tr_b16 v[96:97], v121 offset:18560\n\t"
        "ds_read_b64_tr_b16 v[98:99], v121 offset:21632\n\t"
        "v_mul_f32 v67, v199, v67\n\t"
        "v_mul_f32 v68, v200, v68\n\t"
        "v_mul_f32 v69, v201, v69\n\t"
        "s_waitcnt lgkmcnt(10)\n\t"
        "v_mfma_f32_32x32x16_bf16 v[48:63], v[100:103], v[156:159], v[48:63]\n\t"
        "ds_read_b64_tr_b16 v[100:101], v122 offset:18560\n\t"
        "ds_read_b64_tr_b16 v[102:103], v122 offset:21632\n\t"
        "v_mul_f32 v70, v202, v70\n\t"
        "v_mul_f32 v71, v203, v71\n\t"
        "v_cvt_pk_bf16_f32 v64, v64, v65\n\t"
        "s_waitcnt lgkmcnt(10)\n\t"
        "v_mfma_f32_32x32x16_bf16 v[32:47], v[104:107], v[160:163], v[32:47]\n\t"
        "ds_read_b64_tr_b16 v[104:105], v121 offset:24704\n\t"
        "ds_read_b64_tr_b16 v[106:107], v121 offset:27776\n\t"
        "v_cvt_pk_bf16_f32 v65, v66, v67\n\t"
        "v_cvt_pk_bf16_f32 v66, v68, v69\n\t"
        "v_cvt_pk_bf16_f32 v67, v70, v71\n\t"
        "s_waitcnt lgkmcnt(10)\n\t"
        "v_mfma_f32_32x32x16_bf16 v[48:63], v[108:111], v[160:163], v[48:63]\n\t"
        "ds_read_b64_tr_b16 v[108:109], v122 offset:24704\n\t"
        "ds_read_b64_tr_b16 v[110:111], v122 offset:27776\n\t"
        "v_mul_f32 v72, v204, v72\n\t"
        "v_mul_f32 v73, v205, v73\n\t"
        "v_mul_f32 v74, v206, v74\n\t"
        "s_waitcnt lgkmcnt(10)\n\t"
        "v_mfma_f32_32x32x16_bf16 v[32:47], v[112:115], v[164:167], v[32:47]\n\t"
        "ds_read_b64_tr_b16 v[112:113], v121 offset:30848\n\t"
        "ds_read_b64_tr_b16 v[114:115], v121 offset:33920\n\t"
        "v_mul_f32 v75, v207, v75\n\t"
        "v_mul_f32 v76, v208, v76\n\t"
        "v_mul_f32 v77, v209, v77\n\t"
        "s_waitcnt lgkmcnt(10)\n\t"
        "v_mfma_f32_32x32x16_bf16 v[48:63], v[116:119], v[164:167], v[48:63]\n\t"
        "ds_read_b64_tr_b16 v[116:117], v122 offset:30848\n\t"
        "ds_read_b64_tr_b16 v[118:119], v122 offset:33920\n\t"
        "v_mul_f32 v78, v210, v78\n\t"
        "v_mul_f32 v79, v211, v79\n\t"
        "v_cvt_pk_bf16_f32 v68, v72, v73\n\t"
        "s_waitcnt lgkmcnt(10)\n\t"
        "v_mfma_f32_32x32x16_bf16 v[32:47], v[96:99], v[168:171], v[32:47]\n\t"
        "ds_read_b64_tr_b16 v[96:97], v121 offset:36992\n\t"
        "ds_read_b64_tr_b16 v[98:99], v121 offset:40064\n\t"
        "v_cvt_pk_bf16_f32 v69, v74, v75\n\t"
        "v_cvt_pk_bf16_f32 v70, v76, v77\n\t"
        "v_cvt_pk_bf16_f32 v71, v78, v79\n\t"
        "s_waitcnt lgkmcnt(10)\n\t"
        "v_mfma_f32_32x32x16_bf16 v[48:63], v[100:103], v[168:171], v[48:63]\n\t"
        "ds_read_b64_tr_b16 v[100:101], v122 offset:36992\n\t"
        "ds_read_b64_tr_b16 v[102:103], v122 offset:40064\n\t"
        "v_permlane32_swap_b32 v64, v66\n\t"
        "v_permlane32_swap_b32 v65, v67\n\t"
        "v_permlane32_swap_b32 v68, v70\n\t"
        "s_waitcnt lgkmcnt(10)\n\t"
        "v_mfma_f32_32x32x16_bf16 v[32:47], v[104:107], v[172:175], v[32:47]\n\t"
        "ds_read_b64_tr_b16 v[104:105], v121 offset:43136\n\t"
        "ds_read_b64_tr_b16 v[106:107], v121 offset:46208\n\t"
        "v_permlane32_swap_b32 v69, v71\n\t"
        "buffer_store_dwordx4 v[64:67], v213, s[40:43], 0 offen offset:2048 sc1\n\t"
        "buffer_store_dwordx4 v[68:71], v213, s[40:43], 0 offen offset:3072 sc1\n\t"
        "s_waitcnt lgkmcnt(10)\n\t"
        "v_mfma_f32_32x32x16_bf16 v[48:63], v[108:111], v[172:175], v[48:63]\n\t"
        "ds_read_b64_tr_b16 v[108:109], v122 offset:43136\n\t"
        "ds_read_b64_tr_b16 v[110:111], v122 offset:46208\n\t"
        "v_mul_f32 v80, v196, v80\n\t"
        "v_mul_f32 v81, v197, v81\n\t"
        "v_mul_f32 v82, v198, v82\n\t"
        "s_waitcnt lgkmcnt(10)\n\t"
        "v_mfma_f32_32x32x16_bf16 v[32:47], v[112:115], v[176:179], v[32:47]\n\t"
        "ds_read_b64_tr_b16 v[112:113], v121 offset:49280\n\t"
        "ds_read_b64_tr_b16 v[114:115], v121 offset:52352\n\t"
        "v_mul_f32 v83, v199, v83\n\t"
        "v_mul_f32 v84, v200, v84\n\t"
        "v_mul_f32 v85, v201, v85\n\t"
        "s_waitcnt lgkmcnt(10)\n\t"
        "v_mfma_f32_32x32x16_bf16 v[48:63], v[116:119], v[176:179], v[48:63]\n\t"
        "ds_read_b64_tr_b16 v[116:117], v122 offset:49280\n\t"
        "ds_read_b64_tr_b16 v[118:119], v122 offset:52352\n\t"
        "v_mul_f32 v86, v202, v86\n\t"
        "v_mul_f32 v87, v203, v87\n\t"
        "v_cvt_pk_bf16_f32 v80, v80, v81\n\t"
        "s_waitcnt lgkmcnt(10)\n\t"
        "v_mfma_f32_32x32x16_bf16 v[32:47], v[96:99], v[180:183], v[32:47]\n\t"
        "ds_read_b64_tr_b16 v[96:97], v121 offset:55424\n\t"
        "ds_read_b64_tr_b16 v[98:99], v121 offset:58496\n\t"
        "v_cvt_pk_bf16_f32 v81, v82, v83\n\t"
        "v_cvt_pk_bf16_f32 v82, v84, v85\n\t"
        "v_cvt_pk_bf16_f32 v83, v86, v87\n\t"
        "s_waitcnt lgkmcnt(10)\n\t"
        "v_mfma_f32_32x32x16_bf16 v[48:63], v[100:103], v[180:183], v[48:63]\n\t"
        "ds_read_b64_tr_b16 v[100:101], v122 offset:55424\n\t"
        "ds_read_b64_tr_b16 v[102:103], v122 offset:58496\n\t"
        "v_mul_f32 v88, v204, v88\n\t"
        "v_mul_f32 v89, v205, v89\n\t"
        "v_mul_f32 v90, v206, v90\n\t"
        "s_waitcnt lgkmcnt(10)\n\t"
        "v_mfma_f32_32x32x16_bf16 v[32:47], v[104:107], v[184:187], v[32:47]\n\t"
        "ds_read_b64_tr_b16 v[104:105], v123 offset:6144\n\t"
        "ds_read_b64_tr_b16 v[106:107], v123 offset:9216\n\t"
        "v_mul_f32 v91, v207, v91\n\t"
        "v_mul_f32 v92, v208, v92\n\t"
        "v_mul_f32 v93, v209, v93\n\t"
        "s_waitcnt lgkmcnt(10)\n\t"
        "v_mfma_f32_32x32x16_bf16 v[48:63], v[108:111], v[184:187], v[48:63]\n\t"
        "ds_read_b64_tr_b16 v[108:109], v124 offset:6144\n\t"
        "ds_read_b64_tr_b16 v[110:111], v124 offset:9216\n\t"
        "v_mul_f32 v94, v210, v94\n\t"
        "v_mul_f32 v95, v211, v95\n\t"
        "v_cvt_pk_bf16_f32 v84, v88, v89\n\t"
        "s_waitcnt lgkmcnt(10)\n\t"
        "v_mfma_f32_32x32x16_bf16 v[32:47], v[112:115], v[188:191], v[32:47]\n\t"
        "ds_read_b64_tr_b16 v[112:113], v121 offset:0\n\t"
        "ds_read_b64_tr_b16 v[114:115], v121 offset:3072\n\t"
        "v_cvt_pk_bf16_f32 v85, v90, v91\n\t"
        "v_cvt_pk_bf16_f32 v86, v92, v93\n\t"
        "v_cvt_pk_bf16_f32 v87, v94, v95\n\t"
        "s_waitcnt lgkmcnt(10)\n\t"
        "v_mfma_f32_32x32x16_bf16 v[48:63], v[116:119], v[188:191], v[48:63]\n\t"
        "ds_read_b64_tr_b16 v[116:117], v122 offset:0\n\t"
        "ds_read_b64_tr_b16 v[118:119], v122 offset:3072\n\t"
        "v_permlane32_swap_b32 v80, v82\n\t"
        "v_permlane32_swap_b32 v81, v83\n\t"
        "v_permlane32_swap_b32 v84, v86\n\t"
        "s_waitcnt lgkmcnt(10)\n\t"
        "v_mfma_f32_32x32x16_bf16 v[32:47], v[96:99], v[192:195], v[32:47]\n\t"
        "ds_read_b64_tr_b16 v[96:97], v121 offset:6144\n\t"
        "ds_read_b64_tr_b16 v[98:99], v121 offset:9216\n\t"
        "v_permlane32_swap_b32 v85, v87\n\t"
        "buffer_store_dwordx4 v[80:83], v214, s[40:43], 0 offen offset:2048 sc1\n\t"
        "buffer_store_dwordx4 v[84:87], v214, s[40:43], 0 offen offset:3072 sc1\n\t"
        "s_waitcnt lgkmcnt(10)\n\t"
        "v_mfma_f32_32x32x16_bf16 v[48:63], v[100:103], v[192:195], v[48:63]\n\t"
        "ds_read_b64_tr_b16 v[100:101], v122 offset:6144\n\t"
        "ds_read_b64_tr_b16 v[102:103], v122 offset:9216\n\t"
        "s_waitcnt lgkmcnt(10)\n\t"
        "v_mfma_f32_32x32x16_bf16 v[0:15], v[104:107], v[152:155], v[0:15]\n\t"
        "ds_read_b64_tr_b16 v[104:105], v121 offset:12288\n\t"
        "ds_read_b64_tr_b16 v[106:107], v121 offset:15360\n\t"
        "s_waitcnt lgkmcnt(10)\n\t"
        "v_mfma_f32_32x32x16_bf16 v[16:31], v[108:111], v[152:155], v[16:31]\n\t"
        "ds_read_b64_tr_b16 v[108:109], v122 offset:12288\n\t"
        "ds_read_b64_tr_b16 v[110:111], v122 offset:15360\n\t"
        "v_mul_f32 v32, v212, v32\n\t"
        "v_mul_f32 v33, v212, v33\n\t"
        "v_mul_f32 v34, v212, v34\n\t"
        "s_waitcnt lgkmcnt(10)\n\t"
        "v_mfma_f32_32x32x16_bf16 v[0:15], v[112:115], v[156:159], v[0:15]\n\t"
        "ds_read_b64_tr_b16 v[112:113], v121 offset:18432\n\t"
        "ds_read_b64_tr_b16 v[114:115], v121 offset:21504\n\t"
        "v_mul_f32 v35, v212, v35\n\t"
        "v_mul_f32 v36, v212, v36\n\t"
        "v_mul_f32 v37, v212, v37\n\t"
        "s_waitcnt lgkmcnt(10)\n\t"
        "v_mfma_f32_32x32x16_bf16 v[16:31], v[116:119], v[156:159], v[16:31]\n\t"
        "ds_read_b64_tr_b16 v[116:117], v122 offset:18432\n\t"
        "ds_read_b64_tr_b16 v[118:119], v122 offset:21504\n\t"
        "v_mul_f32 v38, v212, v38\n\t"
        "v_mul_f32 v39, v212, v39\n\t"
        "v_cvt_pk_bf16_f32 v32, v32, v33\n\t"
        "s_waitcnt lgkmcnt(10)\n\t"
        "v_mfma_f32_32x32x16_bf16 v[0:15], v[96:99], v[160:163], v[0:15]\n\t"
        "ds_read_b64_tr_b16 v[96:97], v121 offset:24576\n\t"
        "ds_read_b64_tr_b16 v[98:99], v121 offset:27648\n\t"
        "v_cvt_pk_bf16_f32 v33, v34, v35\n\t"
        "v_cvt_pk_bf16_f32 v34, v36, v37\n\t"
        "v_cvt_pk_bf16_f32 v35, v38, v39\n\t"
        "s_waitcnt lgkmcnt(10)\n\t"
        "v_mfma_f32_32x32x16_bf16 v[16:31], v[100:103], v[160:163], v[16:31]\n\t"
        "ds_read_b64_tr_b16 v[100:101], v122 offset:24576\n\t"
        "ds_read_b64_tr_b16 v[102:103], v122 offset:27648\n\t"
        "v_mul_f32 v40, v212, v40\n\t"
        "v_mul_f32 v41, v212, v41\n\t"
        "v_mul_f32 v42, v212, v42\n\t"
        "s_waitcnt lgkmcnt(10)\n\t"
        "v_mfma_f32_32x32x16_bf16 v[0:15], v[104:107], v[164:167], v[0:15]\n\t"
        "ds_read_b64_tr_b16 v[104:105], v121 offset:30720\n\t"
        "ds_read_b64_tr_b16 v[106:107], v121 offset:33792\n\t"
        "v_mul_f32 v43, v212, v43\n\t"
        "v_mul_f32 v44, v212, v44\n\t"
        "v_mul_f32 v45, v212, v45\n\t"
        "s_waitcnt lgkmcnt(10)\n\t"
        "v_mfma_f32_32x32x16_bf16 v[16:31], v[108:111], v[164:167], v[16:31]\n\t"
        "ds_read_b64_tr_b16 v[108:109], v122 offset:30720\n\t"
        "ds_read_b64_tr_b16 v[110:111], v122 offset:33792\n\t"
        "v_mul_f32 v46, v212, v46\n\t"
        "v_mul_f32 v47, v212, v47\n\t"
        "v_cvt_pk_bf16_f32 v36, v40, v41\n\t"
        "s_waitcnt lgkmcnt(10)\n\t"
        "v_mfma_f32_32x32x16_bf16 v[0:15], v[112:115], v[168:171], v[0:15]\n\t"
        "ds_read_b64_tr_b16 v[112:113], v121 offset:36864\n\t"
        "ds_read_b64_tr_b16 v[114:115], v121 offset:39936\n\t"
        "v_cvt_pk_bf16_f32 v37, v42, v43\n\t"
        "v_cvt_pk_bf16_f32 v38, v44, v45\n\t"
        "v_cvt_pk_bf16_f32 v39, v46, v47\n\t"
        "s_waitcnt lgkmcnt(10)\n\t"
        "v_mfma_f32_32x32x16_bf16 v[16:31], v[116:119], v[168:171], v[16:31]\n\t"
        "ds_read_b64_tr_b16 v[116:117], v122 offset:36864\n\t"
        "ds_read_b64_tr_b16 v[118:119], v122 offset:39936\n\t"
        "buffer_store_dwordx4 v[32:35], v213, s[40:43], 0 offen offset:0 sc1\n\t"
        "buffer_store_dwordx4 v[36:39], v213, s[40:43], 0 offen offset:1024 sc1\n\t"
        "v_mul_f32 v48, v212, v48\n\t"
        "s_waitcnt lgkmcnt(10)\n\t"
        "v_mfma_f32_32x32x16_bf16 v[0:15], v[96:99], v[172:175], v[0:15]\n\t"
        "ds_read_b64_tr_b16 v[96:97], v121 offset:43008\n\t"
        "ds_read_b64_tr_b16 v[98:99], v121 offset:46080\n\t"
        "v_mul_f32 v49, v212, v49\n\t"
        "v_mul_f32 v50, v212, v50\n\t"
        "v_mul_f32 v51, v212, v51\n\t"
        "s_waitcnt lgkmcnt(10)\n\t"
        "v_mfma_f32_32x32x16_bf16 v[16:31], v[100:103], v[172:175], v[16:31]\n\t"
        "ds_read_b64_tr_b16 v[100:101], v122 offset:43008\n\t"
        "ds_read_b64_tr_b16 v[102:103], v122 offset:46080\n\t"
        "v_mul_f32 v52, v212, v52\n\t"
        "v_mul_f32 v53, v212, v53\n\t"
        "v_mul_f32 v54, v212, v54\n\t"
        "s_waitcnt lgkmcnt(10)\n\t"
        "v_mfma_f32_32x32x16_bf16 v[0:15], v[104:107], v[176:179], v[0:15]\n\t"
        "ds_read_b64_tr_b16 v[104:105], v121 offset:49152\n\t"
        "ds_read_b64_tr_b16 v[106:107], v121 offset:52224\n\t"
        "v_mul_f32 v55, v212, v55\n\t"
        "v_cvt_pk_bf16_f32 v48, v48, v49\n\t"
        "v_cvt_pk_bf16_f32 v49, v50, v51\n\t"
        "s_waitcnt lgkmcnt(10)\n\t"
        "v_mfma_f32_32x32x16_bf16 v[16:31], v[108:111], v[176:179], v[16:31]\n\t"
        "ds_read_b64_tr_b16 v[108:109], v122 offset:49152\n\t"
        "ds_read_b64_tr_b16 v[110:111], v122 offset:52224\n\t"
        "v_cvt_pk_bf16_f32 v50, v52, v53\n\t"
        "v_cvt_pk_bf16_f32 v51, v54, v55\n\t"
        "v_mul_f32 v56, v212, v56\n\t"
        "s_waitcnt lgkmcnt(10)\n\t"
        "v_mfma_f32_32x32x16_bf16 v[0:15], v[112:115], v[180:183], v[0:15]\n\t"
        "ds_read_b64_tr_b16 v[112:113], v121 offset:55296\n\t"
        "ds_read_b64_tr_b16 v[114:115], v121 offset:58368\n\t"
        "v_mul_f32 v57, v212, v57\n\t"
        "v_mul_f32 v58, v212, v58\n\t"
        "v_mul_f32 v59, v212, v59\n\t"
        "s_waitcnt lgkmcnt(10)\n\t"
        "v_mfma_f32_32x32x16_bf16 v[16:31], v[116:119], v[180:183], v[16:31]\n\t"
        "ds_read_b64_tr_b16 v[116:117], v122 offset:55296\n\t"
        "ds_read_b64_tr_b16 v[118:119], v122 offset:58368\n\t"
        "v_mul_f32 v60, v212, v60\n\t"
        "v_mul_f32 v61, v212, v61\n\t"
        "v_mul_f32 v62, v212, v62\n\t"
        "s_waitcnt lgkmcnt(10)\n\t"
        "v_mfma_f32_32x32x16_bf16 v[0:15], v[96:99], v[184:187], v[0:15]\n\t"
        "v_mul_f32 v63, v212, v63\n\t"
        "v_cvt_pk_bf16_f32 v52, v56, v57\n\t"
        "v_cvt_pk_bf16_f32 v53, v58, v59\n\t"
        "v_cvt_pk_bf16_f32 v54, v60, v61\n\t"
        "v_cvt_pk_bf16_f32 v55, v62, v63\n\t"
        "s_waitcnt lgkmcnt(8)\n\t"
        "v_mfma_f32_32x32x16_bf16 v[16:31], v[100:103], v[184:187], v[16:31]\n\t"
        "buffer_store_dwordx4 v[48:51], v214, s[40:43], 0 offen offset:0 sc1\n\t"
        "buffer_store_dwordx4 v[52:55], v214, s[40:43], 0 offen offset:1024 sc1\n\t"
        "s_waitcnt lgkmcnt(6)\n\t"
        "v_mfma_f32_32x32x16_bf16 v[0:15], v[104:107], v[188:191], v[0:15]\n\t"
        "s_waitcnt lgkmcnt(4)\n\t"
        "v_mfma_f32_32x32x16_bf16 v[16:31], v[108:111], v[188:191], v[16:31]\n\t"
        "s_waitcnt lgkmcnt(2)\n\t"
        "v_mfma_f32_32x32x16_bf16 v[0:15], v[112:115], v[192:195], v[0:15]\n\t"
        "s_waitcnt lgkmcnt(0)\n\t"
        "v_mfma_f32_32x32x16_bf16 v[16:31], v[116:119], v[192:195], v[16:31]\n\t"
        "s_nop 11\n\t"
        "s_branch 9f\n\t"
        "16:\n\t"
        "s_mov_b32 s48, 0x10000\n\t"
        "8:\n\t"
        "ds_read_b32 v126, v125\n\t"
        "s_sub_u32 s48, s48, 1\n\t"
        "s_waitcnt lgkmcnt(0)\n\t"
        "s_cbranch_scc1 17b\n\t"
        "v_cmp_gt_u32 vcc, s47, v126\n\t"
        "s_cbranch_vccnz 8b\n\t"
        "s_branch 17b\n\t"
        "18:\n\t"
        "s_mov_b32 s48, 0x10000\n\t"
        "8:\n\t"
        "ds_read_b32 v126, v125\n\t"
        "s_sub_u32 s48, s48, 1\n\t"
        "s_waitcnt lgkmcnt(0)\n\t"
        "s_cbranch_scc1 19b\n\t"
        "v_cmp_gt_u32 vcc, s47, v126\n\t"
        "s_cbranch_vccnz 8b\n\t"
        "s_branch 19b\n\t"
        "20:\n\t"
        "s_mov_b32 s48, 0x10000\n\t"
        "8:\n\t"
        "ds_read_b32 v126, v125\n\t"
        "s_sub_u32 s48, s48, 1\n\t"
        "s_waitcnt lgkmcnt(0)\n\t"
        "s_cbranch_scc1 21b\n\t"
        "v_cmp_gt_u32 vcc, s47, v126\n\t"
        "s_cbranch_vccnz 8b\n\t"
        "s_branch 21b\n\t"
        "22:\n\t"
        "s_mov_b32 s48, 0x10000\n\t"
        "8:\n\t"
        "ds_read_b32 v126, v125\n\t"
        "s_sub_u32 s48, s48, 1\n\t"
        "s_waitcnt lgkmcnt(0)\n\t"
        "s_cbranch_scc1 23b\n\t"
        "v_cmp_gt_u32 vcc, s47, v126\n\t"
        "s_cbranch_vccnz 8b\n\t"
        "s_branch 23b\n\t"
        "24:\n\t"
        "s_mov_b32 s48, 0x10000\n\t"
        "8:\n\t"
        "ds_read_b32 v126, v125\n\t"
        "s_sub_u32 s48, s48, 1\n\t"
        "s_waitcnt lgkmcnt(0)\n\t"
        "s_cbranch_scc1 25b\n\t"
        "v_cmp_gt_u32 vcc, s47, v126\n\t"
        "s_cbranch_vccnz 8b\n\t"
        "s_branch 25b\n\t"
        "26:\n\t"
        "s_mov_b32 s48, 0x10000\n\t"
        "8:\n\t"
        "ds_read_b32 v126, v125\n\t"
        "s_sub_u32 s48, s48, 1\n\t"
        "s_waitcnt lgkmcnt(0)\n\t"
        "s_cbranch_scc1 27b\n\t"
        "v_cmp_gt_u32 vcc, s47, v126\n\t"
        "s_cbranch_vccnz 8b\n\t"
        "s_branch 27b\n\t"
        "28:\n\t"
        "s_mov_b32 s48, 0x10000\n\t"
        "8:\n\t"
        "ds_read_b32 v126, v125\n\t"
        "s_sub_u32 s48, s48, 1\n\t"
        "s_waitcnt lgkmcnt(0)\n\t"
        "s_cbranch_scc1 29b\n\t"
        "v_cmp_gt_u32 vcc, s47, v126\n\t"
        "s_cbranch_vccnz 8b\n\t"
        "s_branch 29b\n\t"
        "30:\n\t"
        "s_mov_b32 s48, 0x10000\n\t"
        "8:\n\t"
        "ds_read_b32 v126, v125\n\t"
        "s_sub_u32 s48, s48, 1\n\t"
        "s_waitcnt lgkmcnt(0)\n\t"
        "s_cbranch_scc1 31b\n\t"
        "v_cmp_gt_u32 vcc, s47, v126\n\t"
        "s_cbranch_vccnz 8b\n\t"
        "s_branch 31b\n\t"
        "74:\n\t"
        "s_mov_b32 s48, 0x10000\n\t"
        "8:\n\t"
        "ds_read_b32 v126, v125\n\t"
        "s_sub_u32 s48, s48, 1\n\t"
        "s_waitcnt lgkmcnt(0)\n\t"
        "s_cbranch_scc1 75b\n\t"
        "v_cmp_gt_u32 vcc, s47, v126\n\t"
        "s_cbranch_vccnz 8b\n\t"
        "s_branch 75b\n\t"
        "9:\n\t"
        : "+{v[0:15]}"(acc[0]), "+{v[16:31]}"(acc[1]), "+{v[32:47]}"(acc[2]), "+{v[48:63]}"(acc[3]), "+{v[64:79]}"(acc[4]), "+{v[80:95]}"(acc[5]),
          "+{v[96:111]}"(stA), "+{v[112:127]}"(stB), "+{v[128:131]}"(stC), "+{v[148:163]}"(xr1), "+{v[164:179]}"(xr2), "+{v[180:195]}"(xr3),
          "+{s44}"(soff), "+{s46}"(slot), "+{s47}"(need)
        : "{s[40:43]}"(srd), "{v[212:215]}"(ep) : "v196", "v197", "v198", "v199", "v200", "v201", "v202", "v203", "v204", "v205", "v206", "v207", "v208", "v209", "v210", "v211", "v132", "v133", "v134", "v135", "v136", "v137", "v138", "v139", "v140", "v141", "v142", "v143", "v144", "v145", "v146", "v147", "s45", "s48", "vcc", "scc", "memory");
      STAMPV(4 + 16 * L);
      PHASE_VARS;
      u32x4 kv[4][2];
#pragma unroll
      for (int j = 0; j < 4; ++j)
#pragma unroll
        for (int e = 0; e < 8; ++e) kv[j][e >> 2][e & 3] = __float_as_uint(acc[2 + j][e]);
      constexpr float QS = 0.17677669529663687f * 1.4426950408889634f;
      u32x4 qo[2][2];
      auto drained = [&]() { asm volatile("s_waitcnt vmcnt(0)\n\tds_write_b32 %0, %1" :: "v"(lbase + L_W + 80 + 4 * wid), "v"(2 * L + 1) : "memory"); };
      if (L == 0) {
#pragma unroll
        for (int e = 0; e < 16; ++e) {
          if (e == 8) { SBAR(); drained(); SBAR(); }
          const unsigned w = pack2(acc[e >> 3][2 * (e & 7)], acc[e >> 3][2 * (e & 7) + 1]);
          qo[e >> 3][(e >> 2) & 1][e & 3] = pack2(lo16(w) * QS, hi16(w) * QS);
        }
      } else {
        const float aq = U2F(ep[0]) * QS;
#pragma unroll
        for (int e = 0; e < 16; ++e) qo[e >> 3][(e >> 2) & 1][e & 3] = pack2(acc[e >> 3][2 * (e & 7)] * aq, acc[e >> 3][2 * (e & 7) + 1] * aq);
        SBAR();
        lds_wait_ge<0>(lbase + L_W + 4 + 4 * (lane & 1), 1);
        const int idx = (int)__builtin_amdgcn_readfirstlane(words[3]);
        if ((idx >> 5) == tile) {
          f32x4 cs[4][4];
          float cv[2];
#pragma unroll
          for (int j = 0; j < 6; ++j) {
            const int cb = (j >> 1) * 64 + ((j & 1) ^ hh) * 32;
            if (j >= 4) cv[j - 4] = *reinterpret_cast<const float*>(smem + L_PATCH + (cb + r32) * 4);
            else {
#pragma unroll
              for (int g = 0; g < 4; ++g) cs[j][g] = *reinterpret_cast<const f32x4*>(smem + L_PATCH + (cb + 8 * g + 4 * hi) * 4);
            }
          }
          SBAR();
          const bool me = r32 == (idx & 31);
          const int rel = (idx & 31) - 8 * hi;
#pragma unroll
          for (int j = 0; j < 2; ++j) {
            const unsigned pv = pack2(cv[j], cv[j]);
#pragma unroll
            for (int e = 0; e < 8; ++e) {
              unsigned a = pack2(cs[j][e >> 1][2 * (e & 1)] * QS, cs[j][e >> 1][2 * (e & 1) + 1] * QS), b = pack2(cs[2 + j][e >> 1][2 * (e & 1)], cs[2 + j][e >> 1][2 * (e & 1) + 1]);
              PIN(a); PIN(b);
              qo[j][e >> 2][e & 3] = me ? a : qo[j][e >> 2][e & 3];
              kv[j][e >> 2][e & 3] = me ? b : kv[j][e >> 2][e & 3];
              const int t0 = 16 * (e >> 2) + 2 * (e & 3);
              const unsigned mask = rel == t0 ? 0xffffu : rel == t0 + 1 ? 0xffff0000u : 0u;
              kv[2 + j][e >> 2][e & 3] = (kv[2 + j][e >> 2][e & 3] & ~mask) | (pv & mask);
            }
          }
          if (me) {
#pragma unroll
            for (int i = 0; i < 4; ++i) x1p[i] = *reinterpret_cast<const u32x4*>(smem + L_X2P + (i * 2 + hi) * 16);
            reinterpret_cast<float*>(smem + L_AB)[256 + wid * 64 + lane] = hi ? 1.0f : 0.0f;
          }
          DRAIN();
#pragma unroll
          for (int j = 0; j < 4; ++j) { stg(rs, ep[1 + (j & 1)] + (j >> 1) * 2048, kv[j][0]); stg(rs, ep[1 + (j & 1)] + (j >> 1) * 2048 + 1024, kv[j][1]); }
        }
        drained();
      }
      SBAR();
#pragma unroll
      for (int j = 0; j < 2; ++j) {
        char* p = smem + L_XCH + ((j ^ hh) * 4 + wid) * 2048 + lane * 16;
        *reinterpret_cast<u32x4*>(p) = qo[j][0]; *reinterpret_cast<u32x4*>(p + 1024) = qo[j][1];
        p = smem + L_XCH + 16384 + wid * 4096 + j * 2048 + lane * 16;
        *reinterpret_cast<u32x4*>(p) = kv[2 * j + 1][0]; *reinterpret_cast<u32x4*>(p + 1024) = kv[2 * j + 1][1];
      }
      STAMPV(41 + 4 * L);
      BAR_LGKM();
      STAMPV(43 + 4 * L);
      kva = cat4(kv[0][0], kv[0][1], kv[2][0], kv[2][1]);
      const u32x4* pk = reinterpret_cast<const u32x4*>(smem + L_XCH + 16384 + (wid ^ 1) * 4096 + lane * 16);
#pragma unroll
      for (int u = 0; u < 4; ++u) {
        const u32x4* p = reinterpret_cast<const u32x4*>(smem + L_XCH + (hh * 4 + ((2 * kh + 2 + u) & 3)) * 2048 + lane * 16);
        q[u] = __builtin_shufflevector(p[0], p[64], 0, 1, 2, 3, 4, 5, 6, 7);
      }
      kvb = cat4(pk[0], pk[64], pk[128], pk[192]);
      SBAR();
      asm volatile(
        "v_mov_b64 v[0:1], 0\n\t"
        "v_mov_b64 v[2:3], 0\n\t"
        "v_mov_b64 v[4:5], 0\n\t"
        "v_mov_b64 v[6:7], 0\n\t"
        "v_mov_b64 v[8:9], 0\n\t"
        "v_mov_b64 v[10:11], 0\n\t"
        "v_mov_b64 v[12:13], 0\n\t"
        "v_mov_b64 v[14:15], 0\n\t"
        "v_mov_b64 v[16:17], 0\n\t"
        "v_mov_b64 v[18:19], 0\n\t"
        "v_mov_b64 v[20:21], 0\n\t"
        "v_mov_b64 v[22:23], 0\n\t"
        "v_mov_b64 v[24:25], 0\n\t"
        "v_mov_b64 v[26:27], 0\n\t"
        "v_mov_b64 v[28:29], 0\n\t"
        "v_mov_b64 v[30:31], 0\n\t"
        "v_mov_b64 v[32:33], 0\n\t"
        "v_mov_b64 v[34:35], 0\n\t"
        "v_mov_b64 v[36:37], 0\n\t"
        "v_mov_b64 v[38:39], 0\n\t"
        "v_mov_b64 v[40:41], 0\n\t"
        "v_mov_b64 v[42:43], 0\n\t"
        "v_mov_b64 v[44:45], 0\n\t"
        "v_mov_b64 v[46:47], 0\n\t"
        "v_mov_b64 v[48:49], 0\n\t"
        "v_mov_b64 v[50:51], 0\n\t"
        "v_mov_b64 v[52:53], 0\n\t"
        "v_mov_b64 v[54:55], 0\n\t"
        "v_mov_b64 v[56:57], 0\n\t"
        "v_mov_b64 v[58:59], 0\n\t"
        "v_mov_b64 v[60:61], 0\n\t"
        "v_mov_b64 v[62:63], 0\n\t"
        "v_mov_b64 v[128:129], 0\n\t"
        "v_mov_b64 v[130:131], 0\n\t"
        "v_mov_b64 v[132:133], 0\n\t"
        "v_mov_b64 v[134:135], 0\n\t"
        "v_mov_b64 v[136:137], 0\n\t"
        "v_mov_b64 v[138:139], 0\n\t"
        "v_mov_b64 v[140:141], 0\n\t"
        "v_mov_b64 v[142:143], 0\n\t"
        : "={v[0:15]}"(oz[0]), "={v[16:31]}"(oz[1]), "={v[32:47]}"(oz[2]), "={v[48:63]}"(oz[3]), "={v[128:143]}"(oz[4]));
    }
    STAMPV(5 + 16 * L);
    {
      u32x4 ones;
      unsigned kvoff;
      u32x8 fv, cv;
      asm volatile("" : "={v[184:191]}"(fv), "={v[216:223]}"(cv));
      { PHASE_VARS;
        const unsigned v = (lane & 3) == 0 ? 0x3F803F80u : 0u; const bool up = (lane & 4) != 0; ones[0] = ones[1] = up ? 0u : v; ones[2] = ones[3] = up ? v : 0u;
        kvoff = OFF_KV + L * (10 * MBY) + (tn * 2 + hh) * HS + kh * 8192 + lane * 16;
        fv[1] = lbase + L_FREE + wid * 256 + lane * 4; fv[2] = 32 * L + 32;
        cv[0] = lbase + L_XCH + wid * 8192 + lane * 16; cv[1] = lbase + L_XCH + (wid ^ 2) * 8192 + lane * 16;
        cv[2] = lbase + L_XCH + 32768 + wid * 512 + lane * 4; cv[3] = lbase + L_XCH + 32768 + (wid ^ 2) * 512 + lane * 4;
        cv[4] = lbase + L_W + 48 + wid * 4; cv[5] = lbase + L_W + 48 + (wid ^ 2) * 4; cv[6] = L + 1;
        cv[7] = ((tile * 32 + r32) * D_ + tn * 64 + hi * 8) * 2; }
      f32x16 o0 = oz[0], o1 = oz[1], o2 = oz[2], o3 = oz[3], lv = oz[4];
      unsigned soff = tm * 16384;
      STAMP_DECL;
      asm volatile(
        "s_nop 1\n\t"
        "v_mfma_f32_32x32x16_bf16 v[96:111], v[152:155], v[64:67], 0\n\t"
        "v_mfma_f32_32x32x16_bf16 v[96:111], v[156:159], v[68:71], v[96:111]\n\t"
        "s_mov_b32 s46, 0\n\t"
        "ds_write_b32 v185, v186\n\t"
        "s_nop 9\n\t"
        "v_exp_f32 v96, v96\n\t"
        "v_exp_f32 v97, v97\n\t"
        "v_exp_f32 v98, v98\n\t"
        "v_exp_f32 v99, v99\n\t"
        "v_exp_f32 v100, v100\n\t"
        "v_exp_f32 v101, v101\n\t"
        "v_mfma_f32_32x32x16_bf16 v[112:127], v[152:155], v[72:75], 0\n\t"
        "v_exp_f32 v102, v102\n\t"
        "v_exp_f32 v103, v103\n\t"
        "v_exp_f32 v104, v104\n\t"
        "v_mfma_f32_32x32x16_bf16 v[112:127], v[156:159], v[76:79], v[112:127]\n\t"
        "v_exp_f32 v105, v105\n\t"
        "v_exp_f32 v106, v106\n\t"
        "v_exp_f32 v107, v107\n\t"
        "v_smfmac_f32_32x32x32_bf16 v[0:15], v[160:163], v[96:103], v148\n\t"
        "v_exp_f32 v108, v108\n\t"
        "v_exp_f32 v109, v109\n\t"
        "v_exp_f32 v110, v110\n\t"
        "v_smfmac_f32_16x16x64_bf16 v[128:131], v[144:147], v[96:103], v148\n\t"
        "v_exp_f32 v111, v111\n\t"
        "v_exp_f32 v112, v112\n\t"
        "v_exp_f32 v113, v113\n\t"
        "v_smfmac_f32_32x32x32_bf16 v[0:15], v[164:167], v[104:111], v148\n\t"
        "v_exp_f32 v114, v114\n\t"
        "v_exp_f32 v115, v115\n\t"
        "v_exp_f32 v116, v116\n\t"
        "v_smfmac_f32_16x16x64_bf16 v[128:131], v[144:147], v[104:111], v148\n\t"
        "v_exp_f32 v117, v117\n\t"
        "v_mfma_f32_32x32x16_bf16 v[96:111], v[152:155], v[80:83], 0\n\t"
        "v_exp_f32 v118, v118\n\t"
        "v_exp_f32 v119, v119\n\t"
        "v_exp_f32 v120, v120\n\t"
        "ds_read_b32 v151, v224\n\t"
        "v_mfma_f32_32x32x16_bf16 v[96:111], v[156:159], v[84:87], v[96:111]\n\t"
        "v_exp_f32 v121, v121\n\t"
        "v_exp_f32 v122, v122\n\t"
        "v_exp_f32 v123, v123\n\t"
        "v_smfmac_f32_32x32x32_bf16 v[16:31], v[160:163], v[112:119], v148\n\t"
        "v_exp_f32 v124, v124\n\t"
        "v_exp_f32 v125, v125\n\t"
        "v_exp_f32 v126, v126\n\t"
        "v_smfmac_f32_16x16x64_bf16 v[132:135], v[144:147], v[112:119], v148\n\t"
        "v_exp_f32 v127, v127\n\t"
        "s_cmp_lg_u32 s46, 0\n\t"
        "s_cbranch_scc1 25f\n\t"
        "s_waitcnt lgkmcnt(0)\n\t"
        "v_cmp_ne_u32 vcc, v222, v151\n\t"
        "s_cbranch_vccz 24f\n\t"
        "25:\n\t"
        "v_exp_f32 v96, v96\n\t"
        "v_exp_f32 v97, v97\n\t"
        "v_smfmac_f32_32x32x32_bf16 v[16:31], v[164:167], v[120:127], v148\n\t"
        "v_exp_f32 v98, v98\n\t"
        "v_exp_f32 v99, v99\n\t"
        "v_exp_f32 v100, v100\n\t"
        "v_smfmac_f32_16x16x64_bf16 v[132:135], v[144:147], v[120:127], v148\n\t"
        "v_exp_f32 v101, v101\n\t"
        "v_mfma_f32_32x32x16_bf16 v[112:127], v[152:155], v[88:91], 0\n\t"
        "v_exp_f32 v102, v102\n\t"
        "v_exp_f32 v103, v103\n\t"
        "v_exp_f32 v104, v104\n\t"
        "ds_read_b32 v151, v224\n\t"
        "v_mfma_f32_32x32x16_bf16 v[112:127], v[156:159], v[92:95], v[112:127]\n\t"
        "v_exp_f32 v105, v105\n\t"
        "v_exp_f32 v106, v106\n\t"
        "v_exp_f32 v107, v107\n\t"
        "v_smfmac_f32_32x32x32_bf16 v[32:47], v[160:163], v[96:103], v148\n\t"
        "v_exp_f32 v108, v108\n\t"
        "v_exp_f32 v109, v109\n\t"
        "v_exp_f32 v110, v110\n\t"
        "v_smfmac_f32_16x16x64_bf16 v[136:139], v[144:147], v[96:103], v148\n\t"
        "v_exp_f32 v111, v111\n\t"
        "s_cmp_lg_u32 s46, 0\n\t"
        "s_cbranch_scc1 27f\n\t"
        "s_waitcnt lgkmcnt(0)\n\t"
        "v_cmp_ne_u32 vcc, v222, v151\n\t"
        "s_cbranch_vccz 26f\n\t"
        "27:\n\t"
        "v_exp_f32 v112, v112\n\t"
        "v_exp_f32 v113, v113\n\t"
        "v_smfmac_f32_32x32x32_bf16 v[32:47], v[164:167], v[104:111], v148\n\t"
        "v_exp_f32 v114, v114\n\t"
        "v_exp_f32 v115, v115\n\t"
        "v_exp_f32 v116, v116\n\t"
        "v_smfmac_f32_16x16x64_bf16 v[136:139], v[144:147], v[104:111], v148\n\t"
        "v_exp_f32 v117, v117\n\t"
        "v_mfma_f32_32x32x16_bf16 v[96:111], v[168:171], v[64:67], 0\n\t"
        "v_exp_f32 v118, v118\n\t"
        "v_exp_f32 v119, v119\n\t"
        "v_exp_f32 v120, v120\n\t"
        "ds_read_b32 v151, v224\n\t"
        "v_mfma_f32_32x32x16_bf16 v[96:111], v[172:175], v[68:71], v[96:111]\n\t"
        "v_exp_f32 v121, v121\n\t"
        "v_exp_f32 v122, v122\n\t"
        "v_exp_f32 v123, v123\n\t"
        "v_smfmac_f32_32x32x32_bf16 v[48:63], v[160:163], v[112:119], v148\n\t"
        "v_exp_f32 v124, v124\n\t"
        "v_exp_f32 v125, v125\n\t"
        "v_exp_f32 v126, v126\n\t"
        "v_smfmac_f32_16x16x64_bf16 v[140:143], v[144:147], v[112:119], v148\n\t"
        "v_exp_f32 v127, v127\n\t"
        "s_cmp_lg_u32 s46, 0\n\t"
        "s_cbranch_scc1 29f\n\t"
        "s_waitcnt lgkmcnt(0)\n\t"
        "v_cmp_ne_u32 vcc, v222, v151\n\t"
        "s_cbranch_vccz 28f\n\t"
        "29:\n\t"
        "v_exp_f32 v96, v96\n\t"
        "v_exp_f32 v97, v97\n\t"
        "v_smfmac_f32_32x32x32_bf16 v[48:63], v[164:167], v[120:127], v148\n\t"
        "v_exp_f32 v98, v98\n\t"
        "v_exp_f32 v99, v99\n\t"
        "v_exp_f32 v100, v100\n\t"
        "v_smfmac_f32_16x16x64_bf16 v[140:143], v[144:147], v[120:127], v148\n\t"
        "v_exp_f32 v101, v101\n\t"
        "v_mfma_f32_32x32x16_bf16 v[112:127], v[168:171], v[72:75], 0\n\t"
        "v_exp_f32 v102, v102\n\t"
        "v_exp_f32 v103, v103\n\t"
        "v_exp_f32 v104, v104\n\t"
        "ds_read_b32 v151, v224\n\t"
        "v_mfma_f32_32x32x16_bf16 v[112:127], v[172:175], v[76:79], v[112:127]\n\t"
        "v_exp_f32 v105, v105\n\t"
        "v_exp_f32 v106, v106\n\t"
        "v_exp_f32 v107, v107\n\t"
        "v_smfmac_f32_32x32x32_bf16 v[0:15], v[176:179], v[96:103], v148\n\t"
        "v_exp_f32 v108, v108\n\t"
        "v_exp_f32 v109, v109\n\t"
        "v_exp_f32 v110, v110\n\t"
        "v_smfmac_f32_16x16x64_bf16 v[128:131], v[144:147], v[96:103], v148\n\t"
        "v_exp_f32 v111, v111\n\t"
        "s_cmp_lg_u32 s46, 0\n\t"
        "s_cbranch_scc1 31f\n\t"
        "s_waitcnt lgkmcnt(0)\n\t"
        "v_cmp_ne_u32 vcc, v222, v151\n\t"
        "s_cbranch_vccz 30f\n\t"
        "31:\n\t"
        "v_exp_f32 v112, v112\n\t"
        "v_exp_f32 v113, v113\n\t"
        "v_smfmac_f32_32x32x32_bf16 v[0:15], v[180:183], v[104:111], v148\n\t"
        "v_exp_f32 v114, v114\n\t"
        "v_exp_f32 v115, v115\n\t"
        "v_exp_f32 v116, v116\n\t"
        "v_smfmac_f32_16x16x64_bf16 v[128:131], v[144:147], v[104:111], v148\n\t"
        "v_exp_f32 v117, v117\n\t"
        "v_mfma_f32_32x32x16_bf16 v[96:111], v[168:171], v[80:83], 0\n\t"
        "v_exp_f32 v118, v118\n\t"
        "v_exp_f32 v119, v119\n\t"
        "v_exp_f32 v120, v120\n\t"
        "ds_read_b32 v151, v224\n\t"
        "v_mfma_f32_32x32x16_bf16 v[96:111], v[172:175], v[84:87], v[96:111]\n\t"
        "v_exp_f32 v121, v121\n\t"
        "v_exp_f32 v122, v122\n\t"
        "v_exp_f32 v123, v123\n\t"
        "v_smfmac_f32_32x32x32_bf16 v[16:31], v[176:179], v[112:119], v148\n\t"
        "v_exp_f32 v124, v124\n\t"
        "v_exp_f32 v125, v125\n\t"
        "v_exp_f32 v126, v126\n\t"
        "v_smfmac_f32_16x16x64_bf16 v[132:135], v[144:147], v[112:119], v148\n\t"
        "v_exp_f32 v127, v127\n\t"
        "s_cmp_lg_u32 s46, 0\n\t"
        "s_cbranch_scc1 33f\n\t"
        "s_waitcnt lgkmcnt(0)\n\t"
        "v_cmp_ne_u32 vcc, v222, v151\n\t"
        "s_cbranch_vccz 32f\n\t"
        "33:\n\t"
        "v_exp_f32 v96, v96\n\t"
        "v_exp_f32 v97, v97\n\t"
        "v_smfmac_f32_32x32x32_bf16 v[16:31], v[180:183], v[120:127], v148\n\t"
        "v_exp_f32 v98, v98\n\t"
        "v_exp_f32 v99, v99\n\t"
        "v_exp_f32 v100, v100\n\t"
        "v_smfmac_f32_16x16x64_bf16 v[132:135], v[144:147], v[120:127], v148\n\t"
        "v_exp_f32 v101, v101\n\t"
        "v_mfma_f32_32x32x16_bf16 v[112:127], v[168:171], v[88:91], 0\n\t"
        "v_exp_f32 v102, v102\n\t"
        "v_exp_f32 v103, v103\n\t"
        "v_exp_f32 v104, v104\n\t"
        "v_mfma_f32_32x32x16_bf16 v[112:127], v[172:175], v[92:95], v[112:127]\n\t"
        "v_exp_f32 v105, v105\n\t"
        "v_exp_f32 v106, v106\n\t"
        "v_exp_f32 v107, v107\n\t"
        "v_smfmac_f32_32x32x32_bf16 v[32:47], v[176:179], v[96:103], v148\n\t"
        "v_exp_f32 v108, v108\n\t"
        "v_exp_f32 v109, v109\n\t"
        "v_exp_f32 v110, v110\n\t"
        "v_smfmac_f32_16x16x64_bf16 v[136:139], v[144:147], v[96:103], v148\n\t"
        "v_exp_f32 v111, v111\n\t"
        "s_cmp_lg_u32 s46, 0\n\t"
        "s_cbranch_scc1 5f\n\t"
        "s_mov_b32 s45, 0x200000\n\t"
        "2:\n\t"
        "ds_read_b32 v151, v224\n\t"
        "s_sub_u32 s45, s45, 1\n\t"
        "s_waitcnt lgkmcnt(0)\n\t"
        "s_cbranch_scc1 3f\n\t"
        "v_cmp_ne_u32 vcc, v222, v151\n\t"
        "s_cbranch_vccnz 2b\n\t"
        "3:\n\t"
        "s_add_u32 s44, s44, 0x4000\n\t"
        "s_and_b32 s44, s44, 0x3ffff\n\t"
        "buffer_load_dwordx4 v[184:187], v149, s[40:43], s44 offen offset:0 sc1\n\t"
        "buffer_load_dwordx4 v[188:191], v149, s[40:43], s44 offen offset:1024 sc1\n\t"
        "buffer_load_dwordx4 v[192:195], v149, s[40:43], s44 offen offset:2048 sc1\n\t"
        "buffer_load_dwordx4 v[196:199], v149, s[40:43], s44 offen offset:3072 sc1\n\t"
        "buffer_load_dwordx4 v[200:203], v150, s[40:43], s44 offen offset:0 sc1\n\t"
        "buffer_load_dwordx4 v[204:207], v150, s[40:43], s44 offen offset:1024 sc1\n\t"
        "buffer_load_dwordx4 v[208:211], v150, s[40:43], s44 offen offset:2048 sc1\n\t"
        "buffer_load_dwordx4 v[212:215], v150, s[40:43], s44 offen offset:3072 sc1\n\t"
        "5:\n\t"
        "s_add_u32 s44, s44, 0x4000\n\t"
        "s_and_b32 s44, s44, 0x3ffff\n\t"
        "buffer_load_dwordx4 v[152:155], v149, s[40:43], s44 offen offset:0 sc1\n\t"
        "buffer_load_dwordx4 v[156:159], v149, s[40:43], s44 offen offset:1024 sc1\n\t"
        "buffer_load_dwordx4 v[160:163], v149, s[40:43], s44 offen offset:2048 sc1\n\t"
        "buffer_load_dwordx4 v[164:167], v149, s[40:43], s44 offen offset:3072 sc1\n\t"
        "s_mov_b32 s45, 7\n\t"
        "1:\n\t"
        "v_exp_f32 v112, v112\n\t"
        "v_exp_f32 v113, v113\n\t"
        "v_smfmac_f32_32x32x32_bf16 v[32:47], v[180:183], v[104:111], v148\n\t"
        "v_exp_f32 v114, v114\n\t"
        "v_exp_f32 v115, v115\n\t"
        "v_exp_f32 v116, v116\n\t"
        "v_smfmac_f32_16x16x64_bf16 v[136:139], v[144:147], v[104:111], v148\n\t"
        "v_exp_f32 v117, v117\n\t"
        "s_waitcnt vmcnt(8)\n\t"
        "v_mfma_f32_32x32x16_bf16 v[96:111], v[184:187], v[64:67], 0\n\t"
        "v_exp_f32 v118, v118\n\t"
        "v_exp_f32 v119, v119\n\t"
        "v_exp_f32 v120, v120\n\t"
        "v_mfma_f32_32x32x16_bf16 v[96:111], v[188:191], v[68:71], v[96:111]\n\t"
        "v_exp_f32 v121, v121\n\t"
        "v_exp_f32 v122, v122\n\t"
        "v_exp_f32 v123, v123\n\t"
        "v_smfmac_f32_32x32x32_bf16 v[48:63], v[176:179], v[112:119], v148\n\t"
        "v_exp_f32 v124, v124\n\t"
        "v_exp_f32 v125, v125\n\t"
        "v_exp_f32 v126, v126\n\t"
        "v_smfmac_f32_16x16x64_bf16 v[140:143], v[144:147], v[112:119], v148\n\t"
        "buffer_load_dwordx4 v[168:171], v150, s[40:43], s44 offen offset:0 sc1\n\t"
        "v_exp_f32 v127, v127\n\t"
        "v_exp_f32 v96, v96\n\t"
        "v_exp_f32 v97, v97\n\t"
        "v_smfmac_f32_32x32x32_bf16 v[48:63], v[180:183], v[120:127], v148\n\t"
        "v_exp_f32 v98, v98\n\t"
        "v_exp_f32 v99, v99\n\t"
        "v_exp_f32 v100, v100\n\t"
        "v_smfmac_f32_16x16x64_bf16 v[140:143], v[144:147], v[120:127], v148\n\t"
        "v_exp_f32 v101, v101\n\t"
        "v_mfma_f32_32x32x16_bf16 v[112:127], v[184:187], v[72:75], 0\n\t"
        "v_exp_f32 v102, v102\n\t"
        "v_exp_f32 v103, v103\n\t"
        "v_exp_f32 v104, v104\n\t"
        "v_mfma_f32_32x32x16_bf16 v[112:127], v[188:191], v[76:79], v[112:127]\n\t"
        "v_exp_f32 v105, v105\n\t"
        "v_exp_f32 v106, v106\n\t"
        "v_exp_f32 v107, v107\n\t"
        "v_smfmac_f32_32x32x32_bf16 v[0:15], v[192:195], v[96:103], v148\n\t"
        "v_exp_f32 v108, v108\n\t"
        "v_exp_f32 v109, v109\n\t"
        "v_exp_f32 v110, v110\n\t"
        "v_smfmac_f32_16x16x64_bf16 v[128:131], v[144:147], v[96:103], v148\n\t"
        "buffer_load_dwordx4 v[172:175], v150, s[40:43], s44 offen offset:1024 sc1\n\t"
        "v_exp_f32 v111, v111\n\t"
        "v_exp_f32 v112, v112\n\t"
        "v_exp_f32 v113, v113\n\t"
        "v_smfmac_f32_32x32x32_bf16 v[0:15], v[196:199], v[104:111], v148\n\t"
        "v_exp_f32 v114, v114\n\t"
        "v_exp_f32 v115, v115\n\t"
        "v_exp_f32 v116, v116\n\t"
        "v_smfmac_f32_16x16x64_bf16 v[128:131], v[144:147], v[104:111], v148\n\t"
        "v_exp_f32 v117, v117\n\t"
        "v_mfma_f32_32x32x16_bf16 v[96:111], v[184:187], v[80:83], 0\n\t"
        "v_exp_f32 v118, v118\n\t"
        "v_exp_f32 v119, v119\n\t"
        "v_exp_f32 v120, v120\n\t"
        "v_mfma_f32_32x32x16_bf16 v[96:111], v[188:191], v[84:87], v[96:111]\n\t"
        "v_exp_f32 v121, v121\n\t"
        "v_exp_f32 v122, v122\n\t"
        "v_exp_f32 v123, v123\n\t"
        "v_smfmac_f32_32x32x32_bf16 v[16:31], v[192:195], v[112:119], v148\n\t"
        "v_exp_f32 v124, v124\n\t"
        "v_exp_f32 v125, v125\n\t"
        "v_exp_f32 v126, v126\n\t"
        "v_smfmac_f32_16x16x64_bf16 v[132:135], v[144:147], v[112:119], v148\n\t"
        "buffer_load_dwordx4 v[176:179], v150, s[40:43], s44 offen offset:2048 sc1\n\t"
        "v_exp_f32 v127, v127\n\t"
        "v_exp_f32 v96, v96\n\t"
        "v_exp_f32 v97, v97\n\t"
        "v_smfmac_f32_32x32x32_bf16 v[16:31], v[196:199], v[120:127], v148\n\t"
        "v_exp_f32 v98, v98\n\t"
        "v_exp_f32 v99, v99\n\t"
        "v_exp_f32 v100, v100\n\t"
        "v_smfmac_f32_16x16x64_bf16 v[132:135], v[144:147], v[120:127], v148\n\t"
        "v_exp_f32 v101, v101\n\t"
        "v_mfma_f32_32x32x16_bf16 v[112:127], v[184:187], v[88:91], 0\n\t"
        "v_exp_f32 v102, v102\n\t"
        "v_exp_f32 v103, v103\n\t"
        "v_exp_f32 v104, v104\n\t"
        "v_mfma_f32_32x32x16_bf16 v[112:127], v[188:191], v[92:95], v[112:127]\n\t"
        "v_exp_f32 v105, v105\n\t"
        "v_exp_f32 v106, v106\n\t"
        "v_exp_f32 v107, v107\n\t"
        "v_smfmac_f32_32x32x32_bf16 v[32:47], v[192:195], v[96:103], v148\n\t"
        "v_exp_f32 v108, v108\n\t"
        "v_exp_f32 v109, v109\n\t"
        "v_exp_f32 v110, v110\n\t"
        "v_smfmac_f32_16x16x64_bf16 v[136:139], v[144:147], v[96:103], v148\n\t"
        "buffer_load_dwordx4 v[180:183], v150, s[40:43], s44 offen offset:3072 sc1\n\t"
        "v_exp_f32 v111, v111\n\t"
        "v_exp_f32 v112, v112\n\t"
        "v_exp_f32 v113, v113\n\t"
        "v_smfmac_f32_32x32x32_bf16 v[32:47], v[196:199], v[104:111], v148\n\t"
        "s_add_u32 s44, s44, 0x4000\n\t"
        "s_and_b32 s44, s44, 0x3ffff\n\t"
        "v_exp_f32 v114, v114\n\t"
        "v_exp_f32 v115, v115\n\t"
        "v_exp_f32 v116, v116\n\t"
        "v_smfmac_f32_16x16x64_bf16 v[136:139], v[144:147], v[104:111], v148\n\t"
        "v_exp_f32 v117, v117\n\t"
        "s_waitcnt vmcnt(8)\n\t"
        "v_mfma_f32_32x32x16_bf16 v[96:111], v[200:203], v[64:67], 0\n\t"
        "v_exp_f32 v118, v118\n\t"
        "v_exp_f32 v119, v119\n\t"
        "v_exp_f32 v120, v120\n\t"
        "v_mfma_f32_32x32x16_bf16 v[96:111], v[204:207], v[68:71], v[96:111]\n\t"
        "v_exp_f32 v121, v121\n\t"
        "v_exp_f32 v122, v122\n\t"
        "v_exp_f32 v123, v123\n\t"
        "v_smfmac_f32_32x32x32_bf16 v[48:63], v[192:195], v[112:119], v148\n\t"
        "v_exp_f32 v124, v124\n\t"
        "v_exp_f32 v125, v125\n\t"
        "v_exp_f32 v126, v126\n\t"
        "v_smfmac_f32_16x16x64_bf16 v[140:143], v[144:147], v[112:119], v148\n\t"
        "buffer_load_dwordx4 v[184:187], v149, s[40:43], s44 offen offset:0 sc1\n\t"
        "v_exp_f32 v127, v127\n\t"
        "v_exp_f32 v96, v96\n\t"
        "v_exp_f32 v97, v97\n\t"
        "v_smfmac_f32_32x32x32_bf16 v[48:63], v[196:199], v[120:127], v148\n\t"
        "v_exp_f32 v98, v98\n\t"
        "v_exp_f32 v99, v99\n\t"
        "v_exp_f32 v100, v100\n\t"
        "v_smfmac_f32_16x16x64_bf16 v[140:143], v[144:147], v[120:127], v148\n\t"
        "v_exp_f32 v101, v101\n\t"
        "v_mfma_f32_32x32x16_bf16 v[112:127], v[200:203], v[72:75], 0\n\t"
        "v_exp_f32 v102, v102\n\t"
        "v_exp_f32 v103, v103\n\t"
        "v_exp_f32 v104, v104\n\t"
        "v_mfma_f32_32x32x16_bf16 v[112:127], v[204:207], v[76:79], v[112:127]\n\t"
        "v_exp_f32 v105, v105\n\t"
        "v_exp_f32 v106, v106\n\t"
        "v_exp_f32 v107, v107\n\t"
        "v_smfmac_f32_32x32x32_bf16 v[0:15], v[208:211], v[96:103], v148\n\t"
        "v_exp_f32 v108, v108\n\t"
        "v_exp_f32 v109, v109\n\t"
        "v_exp_f32 v110, v110\n\t"
        "v_smfmac_f32_16x16x64_bf16 v[128:131], v[144:147], v[96:103], v148\n\t"
        "buffer_load_dwordx4 v[188:191], v149, s[40:43], s44 offen offset:1024 sc1\n\t"
        "v_exp_f32 v111, v111\n\t"
        "v_exp_f32 v112, v112\n\t"
        "v_exp_f32 v113, v113\n\t"
        "v_smfmac_f32_32x32x32_bf16 v[0:15], v[212:215], v[104:111], v148\n\t"
        "v_exp_f32 v114, v114\n\t"
        "v_exp_f32 v115, v115\n\t"
        "v_exp_f32 v116, v116\n\t"
        "v_smfmac_f32_16x16x64_bf16 v[128:131], v[144:147], v[104:111], v148\n\t"
        "v_exp_f32 v117, v117\n\t"
        "v_mfma_f32_32x32x16_bf16 v[96:111], v[200:203], v[80:83], 0\n\t"
        "v_exp_f32 v118, v118\n\t"
        "v_exp_f32 v119, v119\n\t"
        "v_exp_f32 v120, v120\n\t"
        "v_mfma_f32_32x32x16_bf16 v[96:111], v[204:207], v[84:87], v[96:111]\n\t"
        "v_exp_f32 v121, v121\n\t"
        "v_exp_f32 v122, v122\n\t"
        "v_exp_f32 v123, v123\n\t"
        "v_smfmac_f32_32x32x32_bf16 v[16:31], v[208:211], v[112:119], v148\n\t"
        "v_exp_f32 v124, v124\n\t"
        "v_exp_f32 v125, v125\n\t"
        "v_exp_f32 v126, v126\n\t"
        "v_smfmac_f32_16x16x64_bf16 v[132:135], v[144:147], v[112:119], v148\n\t"
        "buffer_load_dwordx4 v[192:195], v149, s[40:43], s44 offen offset:2048 sc1\n\t"
        "v_exp_f32 v127, v127\n\t"
        "v_exp_f32 v96, v96\n\t"
        "v_exp_f32 v97, v97\n\t"
        "v_smfmac_f32_32x32x32_bf16 v[16:31], v[212:215], v[120:127], v148\n\t"
        "v_exp_f32 v98, v98\n\t"
        "v_exp_f32 v99, v99\n\t"
        "v_exp_f32 v100, v100\n\t"
        "v_smfmac_f32_16x16x64_bf16 v[132:135], v[144:147], v[120:127], v148\n\t"
        "v_exp_f32 v101, v101\n\t"
        "v_mfma_f32_32x32x16_bf16 v[112:127], v[200:203], v[88:91], 0\n\t"
        "v_exp_f32 v102, v102\n\t"
        "v_exp_f32 v103, v103\n\t"
        "v_exp_f32 v104, v104\n\t"
        "v_mfma_f32_32x32x16_bf16 v[112:127], v[204:207], v[92:95], v[112:127]\n\t"
        "v_exp_f32 v105, v105\n\t"
        "v_exp_f32 v106, v106\n\t"
        "v_exp_f32 v107, v107\n\t"
        "v_smfmac_f32_32x32x32_bf16 v[32:47], v[208:211], v[96:103], v148\n\t"
        "v_exp_f32 v108, v108\n\t"
        "v_exp_f32 v109, v109\n\t"
        "v_exp_f32 v110, v110\n\t"
        "v_smfmac_f32_16x16x64_bf16 v[136:139], v[144:147], v[96:103], v148\n\t"
        "buffer_load_dwordx4 v[196:199], v149, s[40:43], s44 offen offset:3072 sc1\n\t"
        "v_exp_f32 v111, v111\n\t"
        "v_exp_f32 v112, v112\n\t"
        "v_exp_f32 v113, v113\n\t"
        "v_smfmac_f32_32x32x32_bf16 v[32:47], v[212:215], v[104:111], v148\n\t"
        "v_exp_f32 v114, v114\n\t"
        "v_exp_f32 v115, v115\n\t"
        "v_exp_f32 v116, v116\n\t"
        "v_smfmac_f32_16x16x64_bf16 v[136:139], v[144:147], v[104:111], v148\n\t"
        "v_exp_f32 v117, v117\n\t"
        "s_waitcnt vmcnt(8)\n\t"
        "v_mfma_f32_32x32x16_bf16 v[96:111], v[152:155], v[64:67], 0\n\t"
        "v_exp_f32 v118, v118\n\t"
        "v_exp_f32 v119, v119\n\t"
        "v_exp_f32 v120, v120\n\t"
        "v_mfma_f32_32x32x16_bf16 v[96:111], v[156:159], v[68:71], v[96:111]\n\t"
        "v_exp_f32 v121, v121\n\t"
        "v_exp_f32 v122, v122\n\t"
        "v_exp_f32 v123, v123\n\t"
        "v_smfmac_f32_32x32x32_bf16 v[48:63], v[208:211], v[112:119], v148\n\t"
        "v_exp_f32 v124, v124\n\t"
        "v_exp_f32 v125, v125\n\t"
        "v_exp_f32 v126, v126\n\t"
        "v_smfmac_f32_16x16x64_bf16 v[140:143], v[144:147], v[112:119], v148\n\t"
        "buffer_load_dwordx4 v[200:203], v150, s[40:43], s44 offen offset:0 sc1\n\t"
        "v_exp_f32 v127, v127\n\t"
        "v_exp_f32 v96, v96\n\t"
        "v_exp_f32 v97, v97\n\t"
        "v_smfmac_f32_32x32x32_bf16 v[48:63], v[212:215], v[120:127], v148\n\t"
        "v_exp_f32 v98, v98\n\t"
        "v_exp_f32 v99, v99\n\t"
        "v_exp_f32 v100, v100\n\t"
        "v_smfmac_f32_16x16x64_bf16 v[140:143], v[144:147], v[120:127], v148\n\t"
        "v_exp_f32 v101, v101\n\t"
        "v_mfma_f32_32x32x16_bf16 v[112:127], v[152:155], v[72:75], 0\n\t"
        "v_exp_f32 v102, v102\n\t"
        "v_exp_f32 v103, v103\n\t"
        "v_exp_f32 v104, v104\n\t"
        "v_mfma_f32_32x32x16_bf16 v[112:127], v[156:159], v[76:79], v[112:127]\n\t"
        "v_exp_f32 v105, v105\n\t"
        "v_exp_f32 v106, v106\n\t"
        "v_exp_f32 v107, v107\n\t"
        "v_smfmac_f32_32x32x32_bf16 v[0:15], v[160:163], v[96:103], v148\n\t"
        "v_exp_f32 v108, v108\n\t"
        "v_exp_f32 v109, v109\n\t"
        "v_exp_f32 v110, v110\n\t"
        "v_smfmac_f32_16x16x64_bf16 v[128:131], v[144:147], v[96:103], v148\n\t"
        "buffer_load_dwordx4 v[204:207], v150, s[40:43], s44 offen offset:1024 sc1\n\t"
        "v_exp_f32 v111, v111\n\t"
        "v_exp_f32 v112, v112\n\t"
        "v_exp_f32 v113, v113\n\t"
        "v_smfmac_f32_32x32x32_bf16 v[0:15], v[164:167], v[104:111], v148\n\t"
        "v_exp_f32 v114, v114\n\t"
        "v_exp_f32 v115, v115\n\t"
        "v_exp_f32 v116, v116\n\t"
        "v_smfmac_f32_16x16x64_bf16 v[128:131], v[144:147], v[104:111], v148\n\t"
        "v_exp_f32 v117, v117\n\t"
        "v_mfma_f32_32x32x16_bf16 v[96:111], v[152:155], v[80:83], 0\n\t"
        "v_exp_f32 v118, v118\n\t"
        "v_exp_f32 v119, v119\n\t"
        "v_exp_f32 v120, v120\n\t"
        "v_mfma_f32_32x32x16_bf16 v[96:111], v[156:159], v[84:87], v[96:111]\n\t"
        "v_exp_f32 v121, v121\n\t"
        "v_exp_f32 v122, v122\n\t"
        "v_exp_f32 v123, v123\n\t"
        "v_smfmac_f32_32x32x32_bf16 v[16:31], v[160:163], v[112:119], v148\n\t"
        "v_exp_f32 v124, v124\n\t"
        "v_exp_f32 v125, v125\n\t"
        "v_exp_f32 v126, v126\n\t"
        "v_smfmac_f32_16x16x64_bf16 v[132:135], v[144:147], v[112:119], v148\n\t"
        "buffer_load_dwordx4 v[208:211], v150, s[40:43], s44 offen offset:2048 sc1\n\t"
        "v_exp_f32 v127, v127\n\t"
        "v_exp_f32 v96, v96\n\t"
        "v_exp_f32 v97, v97\n\t"
        "v_smfmac_f32_32x32x32_bf16 v[16:31], v[164:167], v[120:127], v148\n\t"
        "v_exp_f32 v98, v98\n\t"
        "v_exp_f32 v99, v99\n\t"
        "v_exp_f32 v100, v100\n\t"
        "v_smfmac_f32_16x16x64_bf16 v[132:135], v[144:147], v[120:127], v148\n\t"
        "v_exp_f32 v101, v101\n\t"
        "v_mfma_f32_32x32x16_bf16 v[112:127], v[152:155], v[88:91], 0\n\t"
        "v_exp_f32 v102, v102\n\t"
        "v_exp_f32 v103, v103\n\t"
        "v_exp_f32 v104, v104\n\t"
        "v_mfma_f32_32x32x16_bf16 v[112:127], v[156:159], v[92:95], v[112:127]\n\t"
        "v_exp_f32 v105, v105\n\t"
        "v_exp_f32 v106, v106\n\t"
        "v_exp_f32 v107, v107\n\t"
        "v_smfmac_f32_32x32x32_bf16 v[32:47], v[160:163], v[96:103], v148\n\t"
        "v_exp_f32 v108, v108\n\t"
        "v_exp_f32 v109, v109\n\t"
        "v_exp_f32 v110, v110\n\t"
        "v_smfmac_f32_16x16x64_bf16 v[136:139], v[144:147], v[96:103], v148\n\t"
        "buffer_load_dwordx4 v[212:215], v150, s[40:43], s44 offen offset:3072 sc1\n\t"
        "v_exp_f32 v111, v111\n\t"
        "v_exp_f32 v112, v112\n\t"
        "v_exp_f32 v113, v113\n\t"
        "v_smfmac_f32_32x32x32_bf16 v[32:47], v[164:167], v[104:111], v148\n\t"
        "s_add_u32 s44, s44, 0x4000\n\t"
        "s_and_b32 s44, s44, 0x3ffff\n\t"
        "v_exp_f32 v114, v114\n\t"
        "v_exp_f32 v115, v115\n\t"
        "v_exp_f32 v116, v116\n\t"
        "v_smfmac_f32_16x16x64_bf16 v[136:139], v[144:147], v[104:111], v148\n\t"
        "v_exp_f32 v117, v117\n\t"
        "s_waitcnt vmcnt(8)\n\t"
        "v_mfma_f32_32x32x16_bf16 v[96:111], v[168:171], v[64:67], 0\n\t"
        "v_exp_f32 v118, v118\n\t"
        "v_exp_f32 v119, v119\n\t"
        "v_exp_f32 v120, v120\n\t"
        "v_mfma_f32_32x32x16_bf16 v[96:111], v[172:175], v[68:71], v[96:111]\n\t"
        "v_exp_f32 v121, v121\n\t"
        "v_exp_f32 v122, v122\n\t"
        "v_exp_f32 v123, v123\n\t"
        "v_smfmac_f32_32x32x32_bf16 v[48:63], v[160:163], v[112:119], v148\n\t"
        "v_exp_f32 v124, v124\n\t"
        "v_exp_f32 v125, v125\n\t"
        "v_exp_f32 v126, v126\n\t"
        "v_smfmac_f32_16x16x64_bf16 v[140:143], v[144:147], v[112:119], v148\n\t"
        "buffer_load_dwordx4 v[152:155], v149, s[40:43], s44 offen offset:0 sc1\n\t"
        "v_exp_f32 v127, v127\n\t"
        "v_exp_f32 v96, v96\n\t"
        "v_exp_f32 v97, v97\n\t"
        "v_smfmac_f32_32x32x32_bf16 v[48:63], v[164:167], v[120:127], v148\n\t"
        "v_exp_f32 v98, v98\n\t"
        "v_exp_f32 v99, v99\n\t"
        "v_exp_f32 v100, v100\n\t"
        "v_smfmac_f32_16x16x64_bf16 v[140:143], v[144:147], v[120:127], v148\n\t"
        "v_exp_f32 v101, v101\n\t"
        "v_mfma_f32_32x32x16_bf16 v[112:127], v[168:171], v[72:75], 0\n\t"
        "v_exp_f32 v102, v102\n\t"
        "v_exp_f32 v103, v103\n\t"
        "v_exp_f32 v104, v104\n\t"
        "v_mfma_f32_32x32x16_bf16 v[112:127], v[172:175], v[76:79], v[112:127]\n\t"
        "v_exp_f32 v105, v105\n\t"
        "v_exp_f32 v106, v106\n\t"
        "v_exp_f32 v107, v107\n\t"
        "v_smfmac_f32_32x32x32_bf16 v[0:15], v[176:179], v[96:103], v148\n\t"
        "v_exp_f32 v108, v108\n\t"
        "v_exp_f32 v109, v109\n\t"
        "v_exp_f32 v110, v110\n\t"
        "v_smfmac_f32_16x16x64_bf16 v[128:131], v[144:147], v[96:103], v148\n\t"
        "buffer_load_dwordx4 v[156:159], v149, s[40:43], s44 offen offset:1024 sc1\n\t"
        "v_exp_f32 v111, v111\n\t"
        "v_exp_f32 v112, v112\n\t"
        "v_exp_f32 v113, v113\n\t"
        "v_smfmac_f32_32x32x32_bf16 v[0:15], v[180:183], v[104:111], v148\n\t"
        "v_exp_f32 v114, v114\n\t"
        "v_exp_f32 v115, v115\n\t"
        "v_exp_f32 v116, v116\n\t"
        "v_smfmac_f32_16x16x64_bf16 v[128:131], v[144:147], v[104:111], v148\n\t"
        "v_exp_f32 v117, v117\n\t"
        "v_mfma_f32_32x32x16_bf16 v[96:111], v[168:171], v[80:83], 0\n\t"
        "v_exp_f32 v118, v118\n\t"
        "v_exp_f32 v119, v119\n\t"
        "v_exp_f32 v120, v120\n\t"
        "v_mfma_f32_32x32x16_bf16 v[96:111], v[172:175], v[84:87], v[96:111]\n\t"
        "v_exp_f32 v121, v121\n\t"
        "v_exp_f32 v122, v122\n\t"
        "v_exp_f32 v123, v123\n\t"
        "v_smfmac_f32_32x32x32_bf16 v[16:31], v[176:179], v[112:119], v148\n\t"
        "v_exp_f32 v124, v124\n\t"
        "v_exp_f32 v125, v125\n\t"
        "v_exp_f32 v126, v126\n\t"
        "v_smfmac_f32_16x16x64_bf16 v[132:135], v[144:147], v[112:119], v148\n\t"
        "buffer_load_dwordx4 v[160:163], v149, s[40:43], s44 offen offset:2048 sc1\n\t"
        "v_exp_f32 v127, v127\n\t"
        "v_exp_f32 v96, v96\n\t"
        "v_exp_f32 v97, v97\n\t"
        "v_smfmac_f32_32x32x32_bf16 v[16:31], v[180:183], v[120:127], v148\n\t"
        "v_exp_f32 v98, v98\n\t"
        "v_exp_f32 v99, v99\n\t"
        "v_exp_f32 v100, v100\n\t"
        "v_smfmac_f32_16x16x64_bf16 v[132:135], v[144:147], v[120:127], v148\n\t"
        "v_exp_f32 v101, v101\n\t"
        "v_mfma_f32_32x32x16_bf16 v[112:127], v[168:171], v[88:91], 0\n\t"
        "v_exp_f32 v102, v102\n\t"
        "v_exp_f32 v103, v103\n\t"
        "v_exp_f32 v104, v104\n\t"
        "v_mfma_f32_32x32x16_bf16 v[112:127], v[172:175], v[92:95], v[112:127]\n\t"
        "v_exp_f32 v105, v105\n\t"
        "v_exp_f32 v106, v106\n\t"
        "v_exp_f32 v107, v107\n\t"
        "v_smfmac_f32_32x32x32_bf16 v[32:47], v[176:179], v[96:103], v148\n\t"
        "v_exp_f32 v108, v108\n\t"
        "v_exp_f32 v109, v109\n\t"
        "v_exp_f32 v110, v110\n\t"
        "v_smfmac_f32_16x16x64_bf16 v[136:139], v[144:147], v[96:103], v148\n\t"
        "buffer_load_dwordx4 v[164:167], v149, s[40:43], s44 offen offset:3072 sc1\n\t"
        "v_exp_f32 v111, v111\n\t"
        "s_sub_u32 s45, s45, 1\n\t"
        "s_cmp_lg_u32 s45, 0\n\t"
        "s_cbranch_scc1 1b\n\t"
        "v_exp_f32 v112, v112\n\t"
        "v_exp_f32 v113, v113\n\t"
        "v_smfmac_f32_32x32x32_bf16 v[32:47], v[180:183], v[104:111], v148\n\t"
        "v_exp_f32 v114, v114\n\t"
        "v_exp_f32 v115, v115\n\t"
        "v_exp_f32 v116, v116\n\t"
        "v_smfmac_f32_16x16x64_bf16 v[136:139], v[144:147], v[104:111], v148\n\t"
        "v_exp_f32 v117, v117\n\t"
        "s_waitcnt vmcnt(8)\n\t"
        "v_mfma_f32_32x32x16_bf16 v[96:111], v[184:187], v[64:67], 0\n\t"
        "v_exp_f32 v118, v118\n\t"
        "v_exp_f32 v119, v119\n\t"
        "v_exp_f32 v120, v120\n\t"
        "v_mfma_f32_32x32x16_bf16 v[96:111], v[188:191], v[68:71], v[96:111]\n\t"
        "v_exp_f32 v121, v121\n\t"
        "v_exp_f32 v122, v122\n\t"
        "v_exp_f32 v123, v123\n\t"
        "v_smfmac_f32_32x32x32_bf16 v[48:63], v[176:179], v[112:119], v148\n\t"
        "v_exp_f32 v124, v124\n\t"
        "v_exp_f32 v125, v125\n\t"
        "v_exp_f32 v126, v126\n\t"
        "v_smfmac_f32_16x16x64_bf16 v[140:143], v[144:147], v[112:119], v148\n\t"
        "v_exp_f32 v127, v127\n\t"
        "v_exp_f32 v96, v96\n\t"
        "v_exp_f32 v97, v97\n\t"
        "v_smfmac_f32_32x32x32_bf16 v[48:63], v[180:183], v[120:127], v148\n\t"
        "v_exp_f32 v98, v98\n\t"
        "v_exp_f32 v99, v99\n\t"
        "v_exp_f32 v100, v100\n\t"
        "v_smfmac_f32_16x16x64_bf16 v[140:143], v[144:147], v[120:127], v148\n\t"
        "v_exp_f32 v101, v101\n\t"
        "v_mfma_f32_32x32x16_bf16 v[112:127], v[184:187], v[72:75], 0\n\t"
        "v_exp_f32 v102, v102\n\t"
        "v_exp_f32 v103, v103\n\t"
        "v_exp_f32 v104, v104\n\t"
        "v_mfma_f32_32x32x16_bf16 v[112:127], v[188:191], v[76:79], v[112:127]\n\t"
        "v_exp_f32 v105, v105\n\t"
        "v_exp_f32 v106, v106\n\t"
        "v_exp_f32 v107, v107\n\t"
        "v_smfmac_f32_32x32x32_bf16 v[0:15], v[192:195], v[96:103], v148\n\t"
        "v_exp_f32 v108, v108\n\t"
        "v_exp_f32 v109, v109\n\t"
        "v_exp_f32 v110, v110\n\t"
        "v_smfmac_f32_16x16x64_bf16 v[128:131], v[144:147], v[96:103], v148\n\t"
        "v_exp_f32 v111, v111\n\t"
        "v_exp_f32 v112, v112\n\t"
        "v_exp_f32 v113, v113\n\t"
        "v_smfmac_f32_32x32x32_bf16 v[0:15], v[196:199], v[104:111], v148\n\t"
        "v_exp_f32 v114, v114\n\t"
        "v_exp_f32 v115, v115\n\t"
        "v_exp_f32 v116, v116\n\t"
        "v_smfmac_f32_16x16x64_bf16 v[128:131], v[144:147], v[104:111], v148\n\t"
        "v_exp_f32 v117, v117\n\t"
        "s_waitcnt vmcnt(4)\n\t"
        "v_mfma_f32_32x32x16_bf16 v[96:111], v[200:203], v[64:67], 0\n\t"
        "v_exp_f32 v118, v118\n\t"
        "v_exp_f32 v119, v119\n\t"
        "v_exp_f32 v120, v120\n\t"
        "v_mfma_f32_32x32x16_bf16 v[96:111], v[204:207], v[68:71], v[96:111]\n\t"
        "v_exp_f32 v121, v121\n\t"
        "v_exp_f32 v122, v122\n\t"
        "v_exp_f32 v123, v123\n\t"
        "v_smfmac_f32_32x32x32_bf16 v[16:31], v[192:195], v[112:119], v148\n\t"
        "v_exp_f32 v124, v124\n\t"
        "v_exp_f32 v125, v125\n\t"
        "v_exp_f32 v126, v126\n\t"
        "v_smfmac_f32_16x16x64_bf16 v[132:135], v[144:147], v[112:119], v148\n\t"
        "v_exp_f32 v127, v127\n\t"
        "s_cmp_lg_u32 s47, 0\n\t"
        "s_cbranch_scc1 4f\n\t"
        "global_load_dwordx4 %[r0], v223, %[xb] offset:0\n\t"
        "global_load_dwordx4 %[r1], v223, %[xb] offset:32\n\t"
        "global_load_dwordx4 %[r2], v223, %[xb] offset:64\n\t"
        "global_load_dwordx4 %[r3], v223, %[xb] offset:96\n\t"
        "4:\n\t"
        "v_exp_f32 v96, v96\n\t"
        "v_exp_f32 v97, v97\n\t"
        "v_smfmac_f32_32x32x32_bf16 v[16:31], v[196:199], v[120:127], v148\n\t"
        "v_exp_f32 v98, v98\n\t"
        "v_exp_f32 v99, v99\n\t"
        "v_exp_f32 v100, v100\n\t"
        "v_smfmac_f32_16x16x64_bf16 v[132:135], v[144:147], v[120:127], v148\n\t"
        "v_exp_f32 v101, v101\n\t"
        "v_mfma_f32_32x32x16_bf16 v[112:127], v[200:203], v[72:75], 0\n\t"
        "v_exp_f32 v102, v102\n\t"
        "v_exp_f32 v103, v103\n\t"
        "v_exp_f32 v104, v104\n\t"
        "v_mfma_f32_32x32x16_bf16 v[112:127], v[204:207], v[76:79], v[112:127]\n\t"
        "v_exp_f32 v105, v105\n\t"
        "v_exp_f32 v106, v106\n\t"
        "v_exp_f32 v107, v107\n\t"
        "v_smfmac_f32_32x32x32_bf16 v[0:15], v[208:211], v[96:103], v148\n\t"
        "v_exp_f32 v108, v108\n\t"
        "v_exp_f32 v109, v109\n\t"
        "v_exp_f32 v110, v110\n\t"
        "v_smfmac_f32_16x16x64_bf16 v[128:131], v[144:147], v[96:103], v148\n\t"
        "v_exp_f32 v111, v111\n\t"
        "v_exp_f32 v112, v112\n\t"
        "v_exp_f32 v113, v113\n\t"
        "v_smfmac_f32_32x32x32_bf16 v[0:15], v[212:215], v[104:111], v148\n\t"
        "v_exp_f32 v114, v114\n\t"
        "v_exp_f32 v115, v115\n\t"
        "v_exp_f32 v116, v116\n\t"
        "v_smfmac_f32_16x16x64_bf16 v[128:131], v[144:147], v[104:111], v148\n\t"
        "v_exp_f32 v117, v117\n\t"
        "v_mfma_f32_32x32x16_bf16 v[96:111], v[184:187], v[80:83], 0\n\t"
        "v_exp_f32 v118, v118\n\t"
        "v_exp_f32 v119, v119\n\t"
        "v_exp_f32 v120, v120\n\t"
        "ds_write_b128 v216, v[0:3] offset:0\n\t"
        "v_mfma_f32_32x32x16_bf16 v[96:111], v[188:191], v[84:87], v[96:111]\n\t"
        "v_exp_f32 v121, v121\n\t"
        "ds_write_b128 v216, v[4:7] offset:1024\n\t"
        "v_exp_f32 v122, v122\n\t"
        "ds_write_b128 v216, v[8:11] offset:2048\n\t"
        "v_exp_f32 v123, v123\n\t"
        "ds_write_b128 v216, v[12:15] offset:3072\n\t"
        "v_smfmac_f32_32x32x32_bf16 v[16:31], v[208:211], v[112:119], v148\n\t"
        "v_exp_f32 v124, v124\n\t"
        "ds_write_b32 v218, v128 offset:0\n\t"
        "v_exp_f32 v125, v125\n\t"
        "v_exp_f32 v126, v126\n\t"
        "v_smfmac_f32_16x16x64_bf16 v[132:135], v[144:147], v[112:119], v148\n\t"
        "v_exp_f32 v127, v127\n\t"
        "v_exp_f32 v96, v96\n\t"
        "v_exp_f32 v97, v97\n\t"
        "v_smfmac_f32_32x32x32_bf16 v[16:31], v[212:215], v[120:127], v148\n\t"
        "v_exp_f32 v98, v98\n\t"
        "v_exp_f32 v99, v99\n\t"
        "v_exp_f32 v100, v100\n\t"
        "v_smfmac_f32_16x16x64_bf16 v[132:135], v[144:147], v[120:127], v148\n\t"
        "v_exp_f32 v101, v101\n\t"
        "v_mfma_f32_32x32x16_bf16 v[112:127], v[184:187], v[88:91], 0\n\t"
        "v_exp_f32 v102, v102\n\t"
        "v_exp_f32 v103, v103\n\t"
        "v_exp_f32 v104, v104\n\t"
        "ds_write_b128 v216, v[16:19] offset:4096\n\t"
        "v_mfma_f32_32x32x16_bf16 v[112:127], v[188:191], v[92:95], v[112:127]\n\t"
        "v_exp_f32 v105, v105\n\t"
        "ds_write_b128 v216, v[20:23] offset:5120\n\t"
        "v_exp_f32 v106, v106\n\t"
        "ds_write_b128 v216, v[24:27] offset:6144\n\t"
        "v_exp_f32 v107, v107\n\t"
        "ds_write_b128 v216, v[28:31] offset:7168\n\t"
        "v_smfmac_f32_32x32x32_bf16 v[32:47], v[192:195], v[96:103], v148\n\t"
        "v_exp_f32 v108, v108\n\t"
        "ds_write_b32 v218, v132 offset:256\n\t"
        "v_exp_f32 v109, v109\n\t"
        "ds_write_b32 v220, v222\n\t"
        "v_exp_f32 v110, v110\n\t"
        "v_smfmac_f32_16x16x64_bf16 v[136:139], v[144:147], v[96:103], v148\n\t"
        "v_exp_f32 v111, v111\n\t"
        "v_exp_f32 v112, v112\n\t"
        "v_exp_f32 v113, v113\n\t"
        "v_smfmac_f32_32x32x32_bf16 v[32:47], v[196:199], v[104:111], v148\n\t"
        "v_exp_f32 v114, v114\n\t"
        "v_exp_f32 v115, v115\n\t"
        "v_exp_f32 v116, v116\n\t"
        "v_smfmac_f32_16x16x64_bf16 v[136:139], v[144:147], v[104:111], v148\n\t"
        "v_exp_f32 v117, v117\n\t"
        "v_mfma_f32_32x32x16_bf16 v[96:111], v[200:203], v[80:83], 0\n\t"
        "v_exp_f32 v118, v118\n\t"
        "v_exp_f32 v119, v119\n\t"
        "v_exp_f32 v120, v120\n\t"
        "v_mfma_f32_32x32x16_bf16 v[96:111], v[204:207], v[84:87], v[96:111]\n\t"
        "v_exp_f32 v121, v121\n\t"
        "v_exp_f32 v122, v122\n\t"
        "v_exp_f32 v123, v123\n\t"
        "v_smfmac_f32_32x32x32_bf16 v[48:63], v[192:195], v[112:119], v148\n\t"
        "v_exp_f32 v124, v124\n\t"
        "v_exp_f32 v125, v125\n\t"
        "v_exp_f32 v126, v126\n\t"
        "v_smfmac_f32_16x16x64_bf16 v[140:143], v[144:147], v[112:119], v148\n\t"
        "v_exp_f32 v127, v127\n\t"
        "v_exp_f32 v96, v96\n\t"
        "v_exp_f32 v97, v97\n\t"
        "v_smfmac_f32_32x32x32_bf16 v[48:63], v[196:199], v[120:127], v148\n\t"
        "v_exp_f32 v98, v98\n\t"
        "v_exp_f32 v99, v99\n\t"
        "v_exp_f32 v100, v100\n\t"
        "v_smfmac_f32_16x16x64_bf16 v[140:143], v[144:147], v[120:127], v148\n\t"
        "v_exp_f32 v101, v101\n\t"
        "v_mfma_f32_32x32x16_bf16 v[112:127], v[200:203], v[88:91], 0\n\t"
        "v_exp_f32 v102, v102\n\t"
        "v_exp_f32 v103, v103\n\t"
        "v_exp_f32 v104, v104\n\t"
        "ds_read_b32 v151, v221\n\t"
        "v_mfma_f32_32x32x16_bf16 v[112:127], v[204:207], v[92:95], v[112:127]\n\t"
        "v_exp_f32 v105, v105\n\t"
        "v_exp_f32 v106, v106\n\t"
        "v_exp_f32 v107, v107\n\t"
        "v_smfmac_f32_32x32x32_bf16 v[32:47], v[208:211], v[96:103], v148\n\t"
        "v_exp_f32 v108, v108\n\t"
        "v_exp_f32 v109, v109\n\t"
        "v_exp_f32 v110, v110\n\t"
        "v_smfmac_f32_16x16x64_bf16 v[136:139], v[144:147], v[96:103], v148\n\t"
        "v_exp_f32 v111, v111\n\t"
        "s_waitcnt lgkmcnt(0)\n\t"
        "v_cmp_ne_u32 vcc, v222, v151\n\t"
        "s_cbranch_vccnz 40f\n\t"
        "41:\n\t"
        "v_exp_f32 v112, v112\n\t"
        "ds_read_b128 v[0:3], v217 offset:0\n\t"
        "v_exp_f32 v113, v113\n\t"
        "ds_read_b128 v[4:7], v217 offset:1024\n\t"
        "v_smfmac_f32_32x32x32_bf16 v[32:47], v[212:215], v[104:111], v148\n\t"
        "v_exp_f32 v114, v114\n\t"
        "ds_read_b128 v[8:11], v217 offset:2048\n\t"
        "v_exp_f32 v115, v115\n\t"
        "ds_read_b128 v[12:15], v217 offset:3072\n\t"
        "v_exp_f32 v116, v116\n\t"
        "ds_read_b32 v128, v219 offset:0\n\t"
        "v_smfmac_f32_16x16x64_bf16 v[136:139], v[144:147], v[104:111], v148\n\t"
        "v_exp_f32 v117, v117\n\t"
        "ds_read_b128 v[16:19], v217 offset:4096\n\t"
        "v_exp_f32 v118, v118\n\t"
        "ds_read_b128 v[20:23], v217 offset:5120\n\t"
        "v_exp_f32 v119, v119\n\t"
        "ds_read_b128 v[24:27], v217 offset:6144\n\t"
        "v_exp_f32 v120, v120\n\t"
        "ds_read_b128 v[28:31], v217 offset:7168\n\t"
        "v_exp_f32 v121, v121\n\t"
        "ds_read_b32 v132, v219 offset:256\n\t"
        "v_exp_f32 v122, v122\n\t"
        "v_exp_f32 v123, v123\n\t"
        "v_smfmac_f32_32x32x32_bf16 v[48:63], v[208:211], v[112:119], v148\n\t"
        "v_exp_f32 v124, v124\n\t"
        "v_exp_f32 v125, v125\n\t"
        "v_exp_f32 v126, v126\n\t"
        "v_smfmac_f32_16x16x64_bf16 v[140:143], v[144:147], v[112:119], v148\n\t"
        "v_exp_f32 v127, v127\n\t"
        "s_nop 1\n\t"
        "v_smfmac_f32_32x32x32_bf16 v[48:63], v[212:215], v[120:127], v148\n\t"
        "v_smfmac_f32_16x16x64_bf16 v[140:143], v[144:147], v[120:127], v148\n\t"
        "s_waitcnt vmcnt(0) lgkmcnt(0)\n\t"
        "s_nop 6\n\t"
        "s_branch 9f\n\t"
        "24:\n\t"
        "s_add_u32 s44, s44, 0x4000\n\t"
        "s_and_b32 s44, s44, 0x3ffff\n\t"
        "buffer_load_dwordx4 v[184:187], v149, s[40:43], s44 offen offset:0 sc1\n\t"
        "buffer_load_dwordx4 v[188:191], v149, s[40:43], s44 offen offset:1024 sc1\n\t"
        "buffer_load_dwordx4 v[192:195], v149, s[40:43], s44 offen offset:2048 sc1\n\t"
        "buffer_load_dwordx4 v[196:199], v149, s[40:43], s44 offen offset:3072 sc1\n\t"
        "buffer_load_dwordx4 v[200:203], v150, s[40:43], s44 offen offset:0 sc1\n\t"
        "buffer_load_dwordx4 v[204:207], v150, s[40:43], s44 offen offset:1024 sc1\n\t"
        "buffer_load_dwordx4 v[208:211], v150, s[40:43], s44 offen offset:2048 sc1\n\t"
        "buffer_load_dwordx4 v[212:215], v150, s[40:43], s44 offen offset:3072 sc1\n\t"
        "s_mov_b32 s46, 1\n\t"
        "s_branch 25b\n\t"
        "26:\n\t"
        "s_add_u32 s44, s44, 0x4000\n\t"
        "s_and_b32 s44, s44, 0x3ffff\n\t"
        "buffer_load_dwordx4 v[184:187], v149, s[40:43], s44 offen offset:0 sc1\n\t"
        "buffer_load_dwordx4 v[188:191], v149, s[40:43], s44 offen offset:1024 sc1\n\t"
        "buffer_load_dwordx4 v[192:195], v149, s[40:43], s44 offen offset:2048 sc1\n\t"
        "buffer_load_dwordx4 v[196:199], v149, s[40:43], s44 offen offset:3072 sc1\n\t"
        "buffer_load_dwordx4 v[200:203], v150, s[40:43], s44 offen offset:0 sc1\n\t"
        "buffer_load_dwordx4 v[204:207], v150, s[40:43], s44 offen offset:1024 sc1\n\t"
        "buffer_load_dwordx4 v[208:211], v150, s[40:43], s44 offen offset:2048 sc1\n\t"
        "buffer_load_dwordx4 v[212:215], v150, s[40:43], s44 offen offset:3072 sc1\n\t"
        "s_mov_b32 s46, 1\n\t"
        "s_branch 27b\n\t"
        "28:\n\t"
        "s_add_u32 s44, s44, 0x4000\n\t"
        "s_and_b32 s44, s44, 0x3ffff\n\t"
        "buffer_load_dwordx4 v[184:187], v149, s[40:43], s44 offen offset:0 sc1\n\t"
        "buffer_load_dwordx4 v[188:191], v149, s[40:43], s44 offen offset:1024 sc1\n\t"
        "buffer_load_dwordx4 v[192:195], v149, s[40:43], s44 offen offset:2048 sc1\n\t"
        "buffer_load_dwordx4 v[196:199], v149, s[40:43], s44 offen offset:3072 sc1\n\t"
        "buffer_load_dwordx4 v[200:203], v150, s[40:43], s44 offen offset:0 sc1\n\t"
        "buffer_load_dwordx4 v[204:207], v150, s[40:43], s44 offen offset:1024 sc1\n\t"
        "buffer_load_dwordx4 v[208:211], v150, s[40:43], s44 offen offset:2048 sc1\n\t"
        "buffer_load_dwordx4 v[212:215], v150, s[40:43], s44 offen offset:3072 sc1\n\t"
        "s_mov_b32 s46, 1\n\t"
        "s_branch 29b\n\t"
        "30:\n\t"
        "s_add_u32 s44, s44, 0x4000\n\t"
        "s_and_b32 s44, s44, 0x3ffff\n\t"
        "buffer_load_dwordx4 v[184:187], v149, s[40:43], s44 offen offset:0 sc1\n\t"
        "buffer_load_dwordx4 v[188:191], v149, s[40:43], s44 offen offset:1024 sc1\n\t"
        "buffer_load_dwordx4 v[192:195], v149, s[40:43], s44 offen offset:2048 sc1\n\t"
        "buffer_load_dwordx4 v[196:199], v149, s[40:43], s44 offen offset:3072 sc1\n\t"
        "buffer_load_dwordx4 v[200:203], v150, s[40:43], s44 offen offset:0 sc1\n\t"
        "buffer_load_dwordx4 v[204:207], v150, s[40:43], s44 offen offset:1024 sc1\n\t"
        "buffer_load_dwordx4 v[208:211], v150, s[40:43], s44 offen offset:2048 sc1\n\t"
        "buffer_load_dwordx4 v[212:215], v150, s[40:43], s44 offen offset:3072 sc1\n\t"
        "s_mov_b32 s46, 1\n\t"
        "s_branch 31b\n\t"
        "32:\n\t"
        "s_add_u32 s44, s44, 0x4000\n\t"
        "s_and_b32 s44, s44, 0x3ffff\n\t"
        "buffer_load_dwordx4 v[184:187], v149, s[40:43], s44 offen offset:0 sc1\n\t"
        "buffer_load_dwordx4 v[188:191], v149, s[40:43], s44 offen offset:1024 sc1\n\t"
        "buffer_load_dwordx4 v[192:195], v149, s[40:43], s44 offen offset:2048 sc1\n\t"
        "buffer_load_dwordx4 v[196:199], v149, s[40:43], s44 offen offset:3072 sc1\n\t"
        "buffer_load_dwordx4 v[200:203], v150, s[40:43], s44 offen offset:0 sc1\n\t"
        "buffer_load_dwordx4 v[204:207], v150, s[40:43], s44 offen offset:1024 sc1\n\t"
        "buffer_load_dwordx4 v[208:211], v150, s[40:43], s44 offen offset:2048 sc1\n\t"
        "buffer_load_dwordx4 v[212:215], v150, s[40:43], s44 offen offset:3072 sc1\n\t"
        "s_mov_b32 s46, 1\n\t"
        "s_branch 33b\n\t"
        "40:\n\t"
        "s_mov_b32 s45, 0x100000\n\t"
        "42:\n\t"
        "ds_read_b32 v151, v221\n\t"
        "s_sub_u32 s45, s45, 1\n\t"
        "s_waitcnt lgkmcnt(0)\n\t"
        "s_cbranch_scc1 41b\n\t"
        "v_cmp_ne_u32 vcc, v222, v151\n\t"
        "s_cbranch_vccnz 42b\n\t"
        "s_branch 41b\n\t"
        "9:\n\t"
        : "+{v[0:15]}"(o0), "+{v[16:31]}"(o1), "+{v[32:47]}"(o2), "+{v[48:63]}"(o3), "+{v[128:143]}"(lv), "+{s44}"(soff), "+{v[152:167]}"(kva), "+{v[168:183]}"(kvb), "+{v[184:191]}"(fv),
          [r0] "+v"(x1p[0]), [r1] "+v"(x1p[1]), [r2] "+v"(x1p[2]), [r3] "+v"(x1p[3]) STAMP_OUTS
        : "{v[64:71]}"(q[0]), "{v[72:79]}"(q[1]), "{v[80:87]}"(q[2]), "{v[88:95]}"(q[3]), "{v[144:147]}"(ones), "{v148}"(0xddddu), "{v149}"(kvoff), "{v150}"(kvoff + 4096),
          "{v[216:223]}"(cv), "{v224}"(lbase + L_W + 16), "{s[40:43]}"(srd), "{s47}"(L), [xb] "s"(x0)
        : "v96", "v97", "v98", "v99", "v100", "v101", "v102", "v103", "v104", "v105", "v106", "v107", "v108", "v109", "v110", "v111", "v112", "v113", "v114", "v115", "v116", "v117", "v118", "v119", "v120", "v121", "v122", "v123", "v124", "v125", "v126", "v127", "v151", "v192", "v193", "v194", "v195", "v196", "v197", "v198", "v199", "v200", "v201", "v202", "v203", "v204", "v205", "v206", "v207", "v208", "v209", "v210", "v211", "v212", "v213", "v214", "v215", "s45", "s46", "vcc", "scc", "memory");
      STAMP_PUT(6 + 16 * L);
      STAMPV(9 + 16 * L);
      PHASE_VARS;
      const unsigned dst = OFF_OF + L * (5 * MBY) + (tm * 4 + 2 * kh) * TS + (tn * 2 + hh) * 2048 + lane * 16;
      auto put = [&](int u, const f32x16& o) {
#pragma unroll
        for (int c = 0; c < 2; ++c) {
          u32x4 w;
#pragma unroll
          for (int e = 0; e < 4; ++e) w[e] = pack2(o[8 * c + 2 * e], o[8 * c + 2 * e + 1]);
          stg(rs, dst + u * TS + c * 1024, w);
        }
      };
      const float la = lv[8] + lv[0], lb = lv[12] + lv[4];
      o2 = (o2 + o0) * RCP(la); put(0, o2);
      SBAR();
      o3 = (o3 + o1) * RCP(lb); put(1, o3);
      SBAR();
      {
        const f32x16 t = o2 + o3;
        float sum = 0.f;
#pragma unroll
        for (int e = 0; e < 16; e += 4) sum += (t[e] + t[e + 1]) + (t[e + 2] + t[e + 3]);
        if (__any(!(fabsf(sum) < 1e37f) | !(fmaxf(la, lb) < 1e27f) | !(fminf(la, lb) > 1e-9f) | !(la + lb == la + lb))) {
          DRAIN();
          attn_safe(rs, kvoff - kh * 8192, q[2], ones, o2); put(0, o2);
          attn_safe(rs, kvoff - kh * 8192, q[3], ones, o3); put(1, o3);
        }
      }
    }
    STAMPV(10 + 16 * L);
    {
      f32x16 acc[8];
      u32x2 wrc[8];
      STAMP_DECL;
      { PHASE_VARS;
      float r0 = 0.f, r1 = 1.f;
      if (L == 0) {
#pragma unroll
        for (int i = 0; i < 4; ++i) x1p[i] = fo_swap(x1p[i]);
      } else {
        r0 = reinterpret_cast<const float*>(smem + L_AB)[256 + wid * 64 + r32]; r1 = reinterpret_cast<const float*>(smem + L_AB)[256 + wid * 64 + 32 + r32];
      }
#pragma unroll
      for (int e = 0; e < 16; ++e) { acc[e >> 3][2 * (e & 7)] = lo16(x1p[e >> 2][e & 3]) * r1 + r0; acc[e >> 3][2 * (e & 7) + 1] = hi16(x1p[e >> 2][e & 3]) * r1 + r0; }
      PIN(acc[0]); PIN(acc[1]);
      asm volatile("s_waitcnt vmcnt(0)\n\tds_write_b32 %0, %1" :: "v"(lbase + L_W + 80 + 4 * wid), "v"(2 * L + 2) : "memory");
      STAMPV(11 + 16 * L);
      const unsigned a0 = lbase + wrow * 128 + ((wch ^ wsw) << 4) + 8 * (tp & 1), a1 = lbase + wrow * 128 + (((4 + wch) ^ wsw) << 4) + 8 * (tp & 1);
      asm volatile(
        "s_mov_b32 s46, 0\n\t"
        "ostart_%=:\n\t"
        "s_mov_b32 s48, 0x200000\n\t"
        "2:\n\t"
        "ds_read_b32 v233, v232\n\t"
        "s_sub_u32 s48, s48, 1\n\t"
        "s_waitcnt lgkmcnt(0)\n\t"
        "s_cselect_b32 s47, 15, 0\n\t"
        "v_readfirstlane_b32 s51, v233\n\t"
        "s_nop 3\n\t"
        "s_or_b32 s51, s51, s47\n\t"
        "s_andn2_b32 s47, s51, s46\n\t"
        "s_cbranch_scc0 2b\n\t"
        "s_andn2_b32 s51, s51, s46\n\t"
        "s_ff1_i32_b32 s47, s51\n\t"
        "s_add_u32 s45, s47, s49\n\t"
        "s_and_b32 s45, s45, 3\n\t"
        "s_lshl_b32 s45, s45, 14\n\t"
        "s_or_b32 s45, s45, s50\n\t"
        "s_and_b32 s51, s47, 0x80000000\n\t"
        "v_or_b32 v225, s51, v224\n\t"
        "s_and_b32 s51, s47, 3\n\t"
        "s_lshl_b32 s51, s51, 15\n\t"
        "v_add_u32 v230, s51, v226\n\t"
        "v_add_u32 v231, s51, v227\n\t"
        "buffer_load_dwordx4 v[160:163], v225, s[40:43], s45 offen offset:0 sc1\n\t"
        "buffer_load_dwordx4 v[164:167], v225, s[40:43], s45 offen offset:1024 sc1\n\t"
        "buffer_load_dwordx4 v[168:171], v225, s[40:43], s45 offen offset:2048 sc1\n\t"
        "buffer_load_dwordx4 v[172:175], v225, s[40:43], s45 offen offset:3072 sc1\n\t"
        "s_add_u32 s51, s45, 0x1000\n\t"
        "s_and_b32 s51, s51, 0x3fff\n\t"
        "s_andn2_b32 s45, s45, 0x3fff\n\t"
        "s_or_b32 s45, s45, s51\n\t"
        "buffer_load_dwordx4 v[176:179], v225, s[40:43], s45 offen offset:0 sc1\n\t"
        "buffer_load_dwordx4 v[180:183], v225, s[40:43], s45 offen offset:1024 sc1\n\t"
        "buffer_load_dwordx4 v[184:187], v225, s[40:43], s45 offen offset:2048 sc1\n\t"
        "buffer_load_dwordx4 v[188:191], v225, s[40:43], s45 offen offset:3072 sc1\n\t"
        "s_add_u32 s51, s45, 0x1000\n\t"
        "s_and_b32 s51, s51, 0x3fff\n\t"
        "s_andn2_b32 s45, s45, 0x3fff\n\t"
        "s_or_b32 s45, s45, s51\n\t"
        "buffer_load_dwordx4 v[192:195], v225, s[40:43], s45 offen offset:0 sc1\n\t"
        "buffer_load_dwordx4 v[196:199], v225, s[40:43], s45 offen offset:1024 sc1\n\t"
        "buffer_load_dwordx4 v[200:203], v225, s[40:43], s45 offen offset:2048 sc1\n\t"
        "buffer_load_dwordx4 v[204:207], v225, s[40:43], s45 offen offset:3072 sc1\n\t"
        "ds_read_b64_tr_b16 v[128:129], v230 offset:0\n\t"
        "ds_read_b64_tr_b16 v[130:131], v230 offset:1024\n\t"
        "ds_read_b64_tr_b16 v[132:133], v231 offset:0\n\t"
        "ds_read_b64_tr_b16 v[134:135], v231 offset:1024\n\t"
        "ds_read_b64_tr_b16 v[136:137], v230 offset:2048\n\t"
        "ds_read_b64_tr_b16 v[138:139], v230 offset:3072\n\t"
        "ds_read_b64_tr_b16 v[140:141], v231 offset:2048\n\t"
        "ds_read_b64_tr_b16 v[142:143], v231 offset:3072\n\t"
        "ds_read_b64_tr_b16 v[144:145], v230 offset:4096\n\t"
        "ds_read_b64_tr_b16 v[146:147], v230 offset:5120\n\t"
        "ds_read_b64_tr_b16 v[148:149], v231 offset:4096\n\t"
        "ds_read_b64_tr_b16 v[150:151], v231 offset:5120\n\t"
        "ds_read_b64_tr_b16 v[152:153], v230 offset:6144\n\t"
        "ds_read_b64_tr_b16 v[154:155], v230 offset:7168\n\t"
        "ds_read_b64_tr_b16 v[156:157], v231 offset:6144\n\t"
        "ds_read_b64_tr_b16 v[158:159], v231 offset:7168\n\t"
        "s_mov_b32 s44, s45\n\t"
        "s_cmp_eq_u32 s47, 0\n\t"
        "s_cbranch_scc1 oseg0_%=\n\t"
        "s_cmp_eq_u32 s47, 1\n\t"
        "s_cbranch_scc1 oseg1_%=\n\t"
        "s_cmp_eq_u32 s47, 2\n\t"
        "s_cbranch_scc1 oseg2_%=\n\t"
        "s_cmp_eq_u32 s47, 3\n\t"
        "s_cbranch_scc1 oseg3_%=\n\t"
        "oseg0_%=:\n\t"
        "s_or_b32 s46, s46, 1\n\t"
        "s_waitcnt vmcnt(11) lgkmcnt(14)\n\t"
        "v_mfma_f32_32x32x16_bf16 v[0:15], v[128:131], v[160:163], v[0:15]\n\t"
        "ds_read_b64_tr_b16 v[128:129], v226 offset:8192\n\t"
        "ds_read_b64_tr_b16 v[130:131], v226 offset:9216\n\t"
        "s_waitcnt lgkmcnt(14)\n\t"
        "v_mfma_f32_32x32x16_bf16 v[16:31], v[132:135], v[160:163], v[16:31]\n\t"
        "ds_read_b64_tr_b16 v[132:133], v227 offset:8192\n\t"
        "ds_read_b64_tr_b16 v[134:135], v227 offset:9216\n\t"
        "s_add_u32 s51, s44, 0x1000\n\t"
        "s_and_b32 s51, s51, 0x3fff\n\t"
        "s_andn2_b32 s44, s44, 0x3fff\n\t"
        "s_or_b32 s44, s44, s51\n\t"
        "buffer_load_dwordx4 v[208:211], v224, s[40:43], s44 offen offset:0 sc1\n\t"
        "s_waitcnt vmcnt(11) lgkmcnt(14)\n\t"
        "v_mfma_f32_32x32x16_bf16 v[0:15], v[136:139], v[164:167], v[0:15]\n\t"
        "ds_read_b32 v233, v232\n\t"
        "ds_read_b64_tr_b16 v[136:137], v226 offset:10240\n\t"
        "ds_read_b64_tr_b16 v[138:139], v226 offset:11264\n\t"
        "s_waitcnt lgkmcnt(14)\n\t"
        "v_mfma_f32_32x32x16_bf16 v[16:31], v[140:143], v[164:167], v[16:31]\n\t"
        "ds_read_b64_tr_b16 v[140:141], v227 offset:10240\n\t"
        "ds_read_b64_tr_b16 v[142:143], v227 offset:11264\n\t"
        "buffer_load_dwordx4 v[212:215], v224, s[40:43], s44 offen offset:1024 sc1\n\t"
        "s_waitcnt vmcnt(11) lgkmcnt(14)\n\t"
        "v_mfma_f32_32x32x16_bf16 v[0:15], v[144:147], v[168:171], v[0:15]\n\t"
        "ds_read_b64_tr_b16 v[144:145], v226 offset:12288\n\t"
        "ds_read_b64_tr_b16 v[146:147], v226 offset:13312\n\t"
        "s_waitcnt lgkmcnt(14)\n\t"
        "v_mfma_f32_32x32x16_bf16 v[16:31], v[148:151], v[168:171], v[16:31]\n\t"
        "ds_read_b64_tr_b16 v[148:149], v227 offset:12288\n\t"
        "ds_read_b64_tr_b16 v[150:151], v227 offset:13312\n\t"
        "buffer_load_dwordx4 v[216:219], v224, s[40:43], s44 offen offset:2048 sc1\n\t"
        "s_waitcnt vmcnt(11) lgkmcnt(8)\n\t"
        "v_mfma_f32_32x32x16_bf16 v[0:15], v[152:155], v[172:175], v[0:15]\n\t"
        "v_readfirstlane_b32 s51, v233\n\t"
        "ds_read_b64_tr_b16 v[152:153], v226 offset:14336\n\t"
        "ds_read_b64_tr_b16 v[154:155], v226 offset:15360\n\t"
        "s_waitcnt lgkmcnt(14)\n\t"
        "v_mfma_f32_32x32x16_bf16 v[16:31], v[156:159], v[172:175], v[16:31]\n\t"
        "s_andn2_b32 s51, s51, s46\n\t"
        "s_ff1_i32_b32 s47, s51\n\t"
        "s_add_u32 s45, s47, s49\n\t"
        "s_and_b32 s45, s45, 3\n\t"
        "s_lshl_b32 s45, s45, 14\n\t"
        "s_or_b32 s45, s45, s50\n\t"
        "ds_read_b64_tr_b16 v[156:157], v227 offset:14336\n\t"
        "ds_read_b64_tr_b16 v[158:159], v227 offset:15360\n\t"
        "buffer_load_dwordx4 v[220:223], v224, s[40:43], s44 offen offset:3072 sc1\n\t"
        "s_waitcnt vmcnt(11) lgkmcnt(14)\n\t"
        "v_mfma_f32_32x32x16_bf16 v[0:15], v[128:131], v[176:179], v[0:15]\n\t"
        "s_and_b32 s51, s47, 0x80000000\n\t"
        "v_or_b32 v225, s51, v224\n\t"
        "s_and_b32 s51, s47, 3\n\t"
        "s_lshl_b32 s51, s51, 15\n\t"
        "v_add_u32 v230, s51, v226\n\t"
        "v_add_u32 v231, s51, v227\n\t"
        "ds_read_b64_tr_b16 v[128:129], v226 offset:16384\n\t"
        "ds_read_b64_tr_b16 v[130:131], v226 offset:17408\n\t"
        "s_waitcnt lgkmcnt(14)\n\t"
        "v_mfma_f32_32x32x16_bf16 v[16:31], v[132:135], v[176:179], v[16:31]\n\t"
        "ds_read_b64_tr_b16 v[132:133], v227 offset:16384\n\t"
        "ds_read_b64_tr_b16 v[134:135], v227 offset:17408\n\t"
        "buffer_load_dwordx4 v[160:163], v225, s[40:43], s45 offen offset:0 sc1\n\t"
        "s_waitcnt vmcnt(11) lgkmcnt(14)\n\t"
        "v_mfma_f32_32x32x16_bf16 v[0:15], v[136:139], v[180:183], v[0:15]\n\t"
        "ds_read_b64_tr_b16 v[136:137], v226 offset:18432\n\t"
        "ds_read_b64_tr_b16 v[138:139], v226 offset:19456\n\t"
        "s_waitcnt lgkmcnt(14)\n\t"
        "v_mfma_f32_32x32x16_bf16 v[16:31], v[140:143], v[180:183], v[16:31]\n\t"
        "ds_read_b64_tr_b16 v[140:141], v227 offset:18432\n\t"
        "ds_read_b64_tr_b16 v[142:143], v227 offset:19456\n\t"
        "buffer_load_dwordx4 v[164:167], v225, s[40:43], s45 offen offset:1024 sc1\n\t"
        "s_waitcnt vmcnt(11) lgkmcnt(14)\n\t"
        "v_mfma_f32_32x32x16_bf16 v[0:15], v[144:147], v[184:187], v[0:15]\n\t"
        "ds_read_b64_tr_b16 v[144:145], v226 offset:20480\n\t"
        "ds_read_b64_tr_b16 v[146:147], v226 offset:21504\n\t"
        "s_waitcnt lgkmcnt(14)\n\t"
        "v_mfma_f32_32x32x16_bf16 v[16:31], v[148:151], v[184:187], v[16:31]\n\t"
        "ds_read_b64_tr_b16 v[148:149], v227 offset:20480\n\t"
        "ds_read_b64_tr_b16 v[150:151], v227 offset:21504\n\t"
        "buffer_load_dwordx4 v[168:171], v225, s[40:43], s45 offen offset:2048 sc1\n\t"
        "s_waitcnt vmcnt(11) lgkmcnt(14)\n\t"
        "v_mfma_f32_32x32x16_bf16 v[0:15], v[152:155], v[188:191], v[0:15]\n\t"
        "ds_read_b64_tr_b16 v[152:153], v226 offset:22528\n\t"
        "ds_read_b64_tr_b16 v[154:155], v226 offset:23552\n\t"
        "s_waitcnt lgkmcnt(14)\n\t"
        "v_mfma_f32_32x32x16_bf16 v[16:31], v[156:159], v[188:191], v[16:31]\n\t"
        "ds_read_b64_tr_b16 v[156:157], v227 offset:22528\n\t"
        "ds_read_b64_tr_b16 v[158:159], v227 offset:23552\n\t"
        "buffer_load_dwordx4 v[172:175], v225, s[40:43], s45 offen offset:3072 sc1\n\t"
        "s_waitcnt vmcnt(11) lgkmcnt(14)\n\t"
        "v_mfma_f32_32x32x16_bf16 v[0:15], v[128:131], v[192:195], v[0:15]\n\t"
        "ds_read_b64_tr_b16 v[128:129], v226 offset:24576\n\t"
        "ds_read_b64_tr_b16 v[130:131], v226 offset:25600\n\t"
        "s_waitcnt lgkmcnt(14)\n\t"
        "v_mfma_f32_32x32x16_bf16 v[16:31], v[132:135], v[192:195], v[16:31]\n\t"
        "ds_read_b64_tr_b16 v[132:133], v227 offset:24576\n\t"
        "ds_read_b64_tr_b16 v[134:135], v227 offset:25600\n\t"
        "s_add_u32 s51, s45, 0x1000\n\t"
        "s_and_b32 s51, s51, 0x3fff\n\t"
        "s_andn2_b32 s45, s45, 0x3fff\n\t"
        "s_or_b32 s45, s45, s51\n\t"
        "buffer_load_dwordx4 v[176:179], v225, s[40:43], s45 offen offset:0 sc1\n\t"
        "s_waitcnt vmcnt(11) lgkmcnt(14)\n\t"
        "v_mfma_f32_32x32x16_bf16 v[0:15], v[136:139], v[196:199], v[0:15]\n\t"
        "ds_read_b64_tr_b16 v[136:137], v226 offset:26624\n\t"
        "ds_read_b64_tr_b16 v[138:139], v226 offset:27648\n\t"
        "s_waitcnt lgkmcnt(14)\n\t"
        "v_mfma_f32_32x32x16_bf16 v[16:31], v[140:143], v[196:199], v[16:31]\n\t"
        "ds_read_b64_tr_b16 v[140:141], v227 offset:26624\n\t"
        "ds_read_b64_tr_b16 v[142:143], v227 offset:27648\n\t"
        "buffer_load_dwordx4 v[180:183], v225, s[40:43], s45 offen offset:1024 sc1\n\t"
        "s_waitcnt vmcnt(11) lgkmcnt(14)\n\t"
        "v_mfma_f32_32x32x16_bf16 v[0:15], v[144:147], v[200:203], v[0:15]\n\t"
        "ds_read_b64_tr_b16 v[144:145], v226 offset:28672\n\t"
        "ds_read_b64_tr_b16 v[146:147], v226 offset:29696\n\t"
        "s_waitcnt lgkmcnt(14)\n\t"
        "v_mfma_f32_32x32x16_bf16 v[16:31], v[148:151], v[200:203], v[16:31]\n\t"
        "ds_read_b64_tr_b16 v[148:149], v227 offset:28672\n\t"
        "ds_read_b64_tr_b16 v[150:151], v227 offset:29696\n\t"
        "buffer_load_dwordx4 v[184:187], v225, s[40:43], s45 offen offset:2048 sc1\n\t"
        "s_waitcnt vmcnt(11) lgkmcnt(14)\n\t"
        "v_mfma_f32_32x32x16_bf16 v[0:15], v[152:155], v[204:207], v[0:15]\n\t"
        "ds_read_b64_tr_b16 v[152:153], v226 offset:30720\n\t"
        "ds_read_b64_tr_b16 v[154:155], v226 offset:31744\n\t"
        "s_waitcnt lgkmcnt(14)\n\t"
        "v_mfma_f32_32x32x16_bf16 v[16:31], v[156:159], v[204:207], v[16:31]\n\t"
        "ds_read_b64_tr_b16 v[156:157], v227 offset:30720\n\t"
        "ds_read_b64_tr_b16 v[158:159], v227 offset:31744\n\t"
        "buffer_load_dwordx4 v[188:191], v225, s[40:43], s45 offen offset:3072 sc1\n\t"
        "s_waitcnt vmcnt(11) lgkmcnt(14)\n\t"
        "v_mfma_f32_32x32x16_bf16 v[0:15], v[128:131], v[208:211], v[0:15]\n\t"
        "ds_read_b64_tr_b16 v[128:129], v230 offset:0\n\t"
        "ds_read_b64_tr_b16 v[130:131], v230 offset:1024\n\t"
        "s_waitcnt lgkmcnt(14)\n\t"
        "v_mfma_f32_32x32x16_bf16 v[16:31], v[132:135], v[208:211], v[16:31]\n\t"
        "ds_read_b64_tr_b16 v[132:133], v231 offset:0\n\t"
        "ds_read_b64_tr_b16 v[134:135], v231 offset:1024\n\t"
        "s_add_u32 s51, s45, 0x1000\n\t"
        "s_and_b32 s51, s51, 0x3fff\n\t"
        "s_andn2_b32 s45, s45, 0x3fff\n\t"
        "s_or_b32 s45, s45, s51\n\t"
        "buffer_load_dwordx4 v[192:195], v225, s[40:43], s45 offen offset:0 sc1\n\t"
        "s_waitcnt vmcnt(11) lgkmcnt(14)\n\t"
        "v_mfma_f32_32x32x16_bf16 v[0:15], v[136:139], v[212:215], v[0:15]\n\t"
        "ds_read_b64_tr_b16 v[136:137], v230 offset:2048\n\t"
        "ds_read_b64_tr_b16 v[138:139], v230 offset:3072\n\t"
        "s_waitcnt lgkmcnt(14)\n\t"
        "v_mfma_f32_32x32x16_bf16 v[16:31], v[140:143], v[212:215], v[16:31]\n\t"
        "ds_read_b64_tr_b16 v[140:141], v231 offset:2048\n\t"
        "ds_read_b64_tr_b16 v[142:143], v231 offset:3072\n\t"
        "buffer_load_dwordx4 v[196:199], v225, s[40:43], s45 offen offset:1024 sc1\n\t"
        "s_waitcnt vmcnt(11) lgkmcnt(14)\n\t"
        "v_mfma_f32_32x32x16_bf16 v[0:15], v[144:147], v[216:219], v[0:15]\n\t"
        "ds_read_b64_tr_b16 v[144:145], v230 offset:4096\n\t"
        "ds_read_b64_tr_b16 v[146:147], v230 offset:5120\n\t"
        "s_waitcnt lgkmcnt(14)\n\t"
        "v_mfma_f32_32x32x16_bf16 v[16:31], v[148:151], v[216:219], v[16:31]\n\t"
        "ds_read_b64_tr_b16 v[148:149], v231 offset:4096\n\t"
        "ds_read_b64_tr_b16 v[150:151], v231 offset:5120\n\t"
        "buffer_load_dwordx4 v[200:203], v225, s[40:43], s45 offen offset:2048 sc1\n\t"
        "s_waitcnt vmcnt(11) lgkmcnt(14)\n\t"
        "v_mfma_f32_32x32x16_bf16 v[0:15], v[152:155], v[220:223], v[0:15]\n\t"
        "ds_read_b64_tr_b16 v[152:153], v230 offset:6144\n\t"
        "ds_read_b64_tr_b16 v[154:155], v230 offset:7168\n\t"
        "s_waitcnt lgkmcnt(14)\n\t"
        "v_mfma_f32_32x32x16_bf16 v[16:31], v[156:159], v[220:223], v[16:31]\n\t"
        "ds_read_b64_tr_b16 v[156:157], v231 offset:6144\n\t"
        "ds_read_b64_tr_b16 v[158:159], v231 offset:7168\n\t"
        "buffer_load_dwordx4 v[204:207], v225, s[40:43], s45 offen offset:3072 sc1\n\t"
        "s_mov_b32 s44, s45\n\t"
        "s_cmp_eq_u32 s47, 0\n\t"
        "s_cbranch_scc1 oseg0_%=\n\t"
        "s_cmp_eq_u32 s47, 1\n\t"
        "s_cbranch_scc1 oseg1_%=\n\t"
        "s_cmp_eq_u32 s47, 2\n\t"
        "s_cbranch_scc1 oseg2_%=\n\t"
        "s_cmp_eq_u32 s47, 3\n\t"
        "s_cbranch_scc1 oseg3_%=\n\t"
        "s_cmp_lg_u32 s46, 15\n\t"
        "s_cbranch_scc1 ostart_%=\n\t"
        "s_branch oend_%=\n\t"
        "oseg1_%=:\n\t"
        "s_or_b32 s46, s46, 2\n\t"
        "s_waitcnt vmcnt(11) lgkmcnt(14)\n\t"
        "v_mfma_f32_32x32x16_bf16 v[32:47], v[128:131], v[160:163], 0\n\t"
        "ds_read_b64_tr_b16 v[128:129], v226 offset:40960\n\t"
        "ds_read_b64_tr_b16 v[130:131], v226 offset:41984\n\t"
        "s_waitcnt lgkmcnt(14)\n\t"
        "v_mfma_f32_32x32x16_bf16 v[48:63], v[132:135], v[160:163], 0\n\t"
        "ds_read_b64_tr_b16 v[132:133], v227 offset:40960\n\t"
        "ds_read_b64_tr_b16 v[134:135], v227 offset:41984\n\t"
        "s_add_u32 s51, s44, 0x1000\n\t"
        "s_and_b32 s51, s51, 0x3fff\n\t"
        "s_andn2_b32 s44, s44, 0x3fff\n\t"
        "s_or_b32 s44, s44, s51\n\t"
        "buffer_load_dwordx4 v[208:211], v224, s[40:43], s44 offen offset:0 sc1\n\t"
        "s_waitcnt vmcnt(11) lgkmcnt(14)\n\t"
        "v_mfma_f32_32x32x16_bf16 v[32:47], v[136:139], v[164:167], v[32:47]\n\t"
        "ds_read_b32 v233, v232\n\t"
        "ds_read_b64_tr_b16 v[136:137], v226 offset:43008\n\t"
        "ds_read_b64_tr_b16 v[138:139], v226 offset:44032\n\t"
        "s_waitcnt lgkmcnt(14)\n\t"
        "v_mfma_f32_32x32x16_bf16 v[48:63], v[140:143], v[164:167], v[48:63]\n\t"
        "ds_read_b64_tr_b16 v[140:141], v227 offset:43008\n\t"
        "ds_read_b64_tr_b16 v[142:143], v227 offset:44032\n\t"
        "buffer_load_dwordx4 v[212:215], v224, s[40:43], s44 offen offset:1024 sc1\n\t"
        "s_waitcnt vmcnt(11) lgkmcnt(14)\n\t"
        "v_mfma_f32_32x32x16_bf16 v[32:47], v[144:147], v[168:171], v[32:47]\n\t"
        "ds_read_b64_tr_b16 v[144:145], v226 offset:45056\n\t"
        "ds_read_b64_tr_b16 v[146:147], v226 offset:46080\n\t"
        "s_waitcnt lgkmcnt(14)\n\t"
        "v_mfma_f32_32x32x16_bf16 v[48:63], v[148:151], v[168:171], v[48:63]\n\t"
        "ds_read_b64_tr_b16 v[148:149], v227 offset:45056\n\t"
        "ds_read_b64_tr_b16 v[150:151], v227 offset:46080\n\t"
        "buffer_load_dwordx4 v[216:219], v224, s[40:43], s44 offen offset:2048 sc1\n\t"
        "s_waitcnt vmcnt(11) lgkmcnt(8)\n\t"
        "v_mfma_f32_32x32x16_bf16 v[32:47], v[152:155], v[172:175], v[32:47]\n\t"
        "v_readfirstlane_b32 s51, v233\n\t"
        "ds_read_b64_tr_b16 v[152:153], v226 offset:47104\n\t"
        "ds_read_b64_tr_b16 v[154:155], v226 offset:48128\n\t"
        "s_waitcnt lgkmcnt(14)\n\t"
        "v_mfma_f32_32x32x16_bf16 v[48:63], v[156:159], v[172:175], v[48:63]\n\t"
        "s_andn2_b32 s51, s51, s46\n\t"
        "s_ff1_i32_b32 s47, s51\n\t"
        "s_add_u32 s45, s47, s49\n\t"
        "s_and_b32 s45, s45, 3\n\t"
        "s_lshl_b32 s45, s45, 14\n\t"
        "s_or_b32 s45, s45, s50\n\t"
        "ds_read_b64_tr_b16 v[156:157], v227 offset:47104\n\t"
        "ds_read_b64_tr_b16 v[158:159], v227 offset:48128\n\t"
        "buffer_load_dwordx4 v[220:223], v224, s[40:43], s44 offen offset:3072 sc1\n\t"
        "s_waitcnt vmcnt(11) lgkmcnt(14)\n\t"
        "v_mfma_f32_32x32x16_bf16 v[32:47], v[128:131], v[176:179], v[32:47]\n\t"
        "s_and_b32 s51, s47, 0x80000000\n\t"
        "v_or_b32 v225, s51, v224\n\t"
        "s_and_b32 s51, s47, 3\n\t"
        "s_lshl_b32 s51, s51, 15\n\t"
        "v_add_u32 v230, s51, v226\n\t"
        "v_add_u32 v231, s51, v227\n\t"
        "ds_read_b64_tr_b16 v[128:129], v226 offset:49152\n\t"
        "ds_read_b64_tr_b16 v[130:131], v226 offset:50176\n\t"
        "s_waitcnt lgkmcnt(14)\n\t"
        "v_mfma_f32_32x32x16_bf16 v[48:63], v[132:135], v[176:179], v[48:63]\n\t"
        "ds_read_b64_tr_b16 v[132:133], v227 offset:49152\n\t"
        "ds_read_b64_tr_b16 v[134:135], v227 offset:50176\n\t"
        "buffer_load_dwordx4 v[160:163], v225, s[40:43], s45 offen offset:0 sc1\n\t"
        "s_waitcnt vmcnt(11) lgkmcnt(14)\n\t"
        "v_mfma_f32_32x32x16_bf16 v[32:47], v[136:139], v[180:183], v[32:47]\n\t"
        "ds_read_b64_tr_b16 v[136:137], v226 offset:51200\n\t"
        "ds_read_b64_tr_b16 v[138:139], v226 offset:52224\n\t"
        "s_waitcnt lgkmcnt(14)\n\t"
        "v_mfma_f32_32x32x16_bf16 v[48:63], v[140:143], v[180:183], v[48:63]\n\t"
        "ds_read_b64_tr_b16 v[140:141], v227 offset:51200\n\t"
        "ds_read_b64_tr_b16 v[142:143], v227 offset:52224\n\t"
        "buffer_load_dwordx4 v[164:167], v225, s[40:43], s45 offen offset:1024 sc1\n\t"
        "s_waitcnt vmcnt(11) lgkmcnt(14)\n\t"
        "v_mfma_f32_32x32x16_bf16 v[32:47], v[144:147], v[184:187], v[32:47]\n\t"
        "ds_read_b64_tr_b16 v[144:145], v226 offset:53248\n\t"
        "ds_read_b64_tr_b16 v[146:147], v226 offset:54272\n\t"
        "s_waitcnt lgkmcnt(14)\n\t"
        "v_mfma_f32_32x32x16_bf16 v[48:63], v[148:151], v[184:187], v[48:63]\n\t"
        "ds_read_b64_tr_b16 v[148:149], v227 offset:53248\n\t"
        "ds_read_b64_tr_b16 v[150:151], v227 offset:54272\n\t"
        "buffer_load_dwordx4 v[168:171], v225, s[40:43], s45 offen offset:2048 sc1\n\t"
        "s_waitcnt vmcnt(11) lgkmcnt(14)\n\t"
        "v_mfma_f32_32x32x16_bf16 v[32:47], v[152:155], v[188:191], v[32:47]\n\t"
        "ds_read_b64_tr_b16 v[152:153], v226 offset:55296\n\t"
        "ds_read_b64_tr_b16 v[154:155], v226 offset:56320\n\t"
        "s_waitcnt lgkmcnt(14)\n\t"
        "v_mfma_f32_32x32x16_bf16 v[48:63], v[156:159], v[188:191], v[48:63]\n\t"
        "ds_read_b64_tr_b16 v[156:157], v227 offset:55296\n\t"
        "ds_read_b64_tr_b16 v[158:159], v227 offset:56320\n\t"
        "buffer_load_dwordx4 v[172:175], v225, s[40:43], s45 offen offset:3072 sc1\n\t"
        "s_waitcnt vmcnt(11) lgkmcnt(14)\n\t"
        "v_mfma_f32_32x32x16_bf16 v[32:47], v[128:131], v[192:195], v[32:47]\n\t"
        "ds_read_b64_tr_b16 v[128:129], v226 offset:57344\n\t"
        "ds_read_b64_tr_b16 v[130:131], v226 offset:58368\n\t"
        "s_waitcnt lgkmcnt(14)\n\t"
        "v_mfma_f32_32x32x16_bf16 v[48:63], v[132:135], v[192:195], v[48:63]\n\t"
        "ds_read_b64_tr_b16 v[132:133], v227 offset:57344\n\t"
        "ds_read_b64_tr_b16 v[134:135], v227 offset:58368\n\t"
        "s_add_u32 s51, s45, 0x1000\n\t"
        "s_and_b32 s51, s51, 0x3fff\n\t"
        "s_andn2_b32 s45, s45, 0x3fff\n\t"
        "s_or_b32 s45, s45, s51\n\t"
        "buffer_load_dwordx4 v[176:179], v225, s[40:43], s45 offen offset:0 sc1\n\t"
        "s_waitcnt vmcnt(11) lgkmcnt(14)\n\t"
        "v_mfma_f32_32x32x16_bf16 v[32:47], v[136:139], v[196:199], v[32:47]\n\t"
        "ds_read_b64_tr_b16 v[136:137], v226 offset:59392\n\t"
        "ds_read_b64_tr_b16 v[138:139], v226 offset:60416\n\t"
        "s_waitcnt lgkmcnt(14)\n\t"
        "v_mfma_f32_32x32x16_bf16 v[48:63], v[140:143], v[196:199], v[48:63]\n\t"
        "ds_read_b64_tr_b16 v[140:141], v227 offset:59392\n\t"
        "ds_read_b64_tr_b16 v[142:143], v227 offset:60416\n\t"
        "buffer_load_dwordx4 v[180:183], v225, s[40:43], s45 offen offset:1024 sc1\n\t"
        "s_waitcnt vmcnt(11) lgkmcnt(14)\n\t"
        "v_mfma_f32_32x32x16_bf16 v[32:47], v[144:147], v[200:203], v[32:47]\n\t"
        "ds_read_b64_tr_b16 v[144:145], v226 offset:61440\n\t"
        "ds_read_b64_tr_b16 v[146:147], v226 offset:62464\n\t"
        "s_waitcnt lgkmcnt(14)\n\t"
        "v_mfma_f32_32x32x16_bf16 v[48:63], v[148:151], v[200:203], v[48:63]\n\t"
        "ds_read_b64_tr_b16 v[148:149], v227 offset:61440\n\t"
        "ds_read_b64_tr_b16 v[150:151], v227 offset:62464\n\t"
        "buffer_load_dwordx4 v[184:187], v225, s[40:43], s45 offen offset:2048 sc1\n\t"
        "s_waitcnt vmcnt(11) lgkmcnt(14)\n\t"
        "v_mfma_f32_32x32x16_bf16 v[32:47], v[152:155], v[204:207], v[32:47]\n\t"
        "ds_read_b64_tr_b16 v[152:153], v226 offset:63488\n\t"
        "ds_read_b64_tr_b16 v[154:155], v226 offset:64512\n\t"
        "s_waitcnt lgkmcnt(14)\n\t"
        "v_mfma_f32_32x32x16_bf16 v[48:63], v[156:159], v[204:207], v[48:63]\n\t"
        "ds_read_b64_tr_b16 v[156:157], v227 offset:63488\n\t"
        "ds_read_b64_tr_b16 v[158:159], v227 offset:64512\n\t"
        "buffer_load_dwordx4 v[188:191], v225, s[40:43], s45 offen offset:3072 sc1\n\t"
        "s_waitcnt vmcnt(11) lgkmcnt(14)\n\t"
        "v_mfma_f32_32x32x16_bf16 v[32:47], v[128:131], v[208:211], v[32:47]\n\t"
        "ds_read_b64_tr_b16 v[128:129], v230 offset:0\n\t"
        "ds_read_b64_tr_b16 v[130:131], v230 offset:1024\n\t"
        "s_waitcnt lgkmcnt(14)\n\t"
        "v_mfma_f32_32x32x16_bf16 v[48:63], v[132:135], v[208:211], v[48:63]\n\t"
        "ds_read_b64_tr_b16 v[132:133], v231 offset:0\n\t"
        "ds_read_b64_tr_b16 v[134:135], v231 offset:1024\n\t"
        "s_add_u32 s51, s45, 0x1000\n\t"
        "s_and_b32 s51, s51, 0x3fff\n\t"
        "s_andn2_b32 s45, s45, 0x3fff\n\t"
        "s_or_b32 s45, s45, s51\n\t"
        "buffer_load_dwordx4 v[192:195], v225, s[40:43], s45 offen offset:0 sc1\n\t"
        "s_waitcnt vmcnt(11) lgkmcnt(14)\n\t"
        "v_mfma_f32_32x32x16_bf16 v[32:47], v[136:139], v[212:215], v[32:47]\n\t"
        "ds_read_b64_tr_b16 v[136:137], v230 offset:2048\n\t"
        "ds_read_b64_tr_b16 v[138:139], v230 offset:3072\n\t"
        "s_waitcnt lgkmcnt(14)\n\t"
        "v_mfma_f32_32x32x16_bf16 v[48:63], v[140:143], v[212:215], v[48:63]\n\t"
        "ds_read_b64_tr_b16 v[140:141], v231 offset:2048\n\t"
        "ds_read_b64_tr_b16 v[142:143], v231 offset:3072\n\t"
        "buffer_load_dwordx4 v[196:199], v225, s[40:43], s45 offen offset:1024 sc1\n\t"
        "s_waitcnt vmcnt(11) lgkmcnt(14)\n\t"
        "v_mfma_f32_32x32x16_bf16 v[32:47], v[144:147], v[216:219], v[32:47]\n\t"
        "ds_read_b64_tr_b16 v[144:145], v230 offset:4096\n\t"
        "ds_read_b64_tr_b16 v[146:147], v230 offset:5120\n\t"
        "s_waitcnt lgkmcnt(14)\n\t"
        "v_mfma_f32_32x32x16_bf16 v[48:63], v[148:151], v[216:219], v[48:63]\n\t"
        "ds_read_b64_tr_b16 v[148:149], v231 offset:4096\n\t"
        "ds_read_b64_tr_b16 v[150:151], v231 offset:5120\n\t"
        "buffer_load_dwordx4 v[200:203], v225, s[40:43], s45 offen offset:2048 sc1\n\t"
        "s_waitcnt vmcnt(11) lgkmcnt(14)\n\t"
        "v_mfma_f32_32x32x16_bf16 v[32:47], v[152:155], v[220:223], v[32:47]\n\t"
        "ds_read_b64_tr_b16 v[152:153], v230 offset:6144\n\t"
        "ds_read_b64_tr_b16 v[154:155], v230 offset:7168\n\t"
        "s_waitcnt lgkmcnt(14)\n\t"
        "v_mfma_f32_32x32x16_bf16 v[48:63], v[156:159], v[220:223], v[48:63]\n\t"
        "ds_read_b64_tr_b16 v[156:157], v231 offset:6144\n\t"
        "ds_read_b64_tr_b16 v[158:159], v231 offset:7168\n\t"
        "buffer_load_dwordx4 v[204:207], v225, s[40:43], s45 offen offset:3072 sc1\n\t"
        "s_mov_b32 s44, s45\n\t"
        "s_cmp_eq_u32 s47, 0\n\t"
        "s_cbranch_scc1 oseg0_%=\n\t"
        "s_cmp_eq_u32 s47, 1\n\t"
        "s_cbranch_scc1 oseg1_%=\n\t"
        "s_cmp_eq_u32 s47, 2\n\t"
        "s_cbranch_scc1 oseg2_%=\n\t"
        "s_cmp_eq_u32 s47, 3\n\t"
        "s_cbranch_scc1 oseg3_%=\n\t"
        "s_cmp_lg_u32 s46, 15\n\t"
        "s_cbranch_scc1 ostart_%=\n\t"
        "s_branch oend_%=\n\t"
        "oseg2_%=:\n\t"
        "s_or_b32 s46, s46, 4\n\t"
        "s_waitcnt vmcnt(11) lgkmcnt(14)\n\t"
        "v_mfma_f32_32x32x16_bf16 v[64:79], v[128:131], v[160:163], 0\n\t"
        "ds_read_b64_tr_b16 v[128:129], v228 offset:8192\n\t"
        "ds_read_b64_tr_b16 v[130:131], v228 offset:9216\n\t"
        "s_waitcnt lgkmcnt(14)\n\t"
        "v_mfma_f32_32x32x16_bf16 v[80:95], v[132:135], v[160:163], 0\n\t"
        "ds_read_b64_tr_b16 v[132:133], v229 offset:8192\n\t"
        "ds_read_b64_tr_b16 v[134:135], v229 offset:9216\n\t"
        "s_add_u32 s51, s44, 0x1000\n\t"
        "s_and_b32 s51, s51, 0x3fff\n\t"
        "s_andn2_b32 s44, s44, 0x3fff\n\t"
        "s_or_b32 s44, s44, s51\n\t"
        "buffer_load_dwordx4 v[208:211], v224, s[40:43], s44 offen offset:0 sc1\n\t"
        "s_waitcnt vmcnt(11) lgkmcnt(14)\n\t"
        "v_mfma_f32_32x32x16_bf16 v[64:79], v[136:139], v[164:167], v[64:79]\n\t"
        "ds_read_b32 v233, v232\n\t"
        "ds_read_b64_tr_b16 v[136:137], v228 offset:10240\n\t"
        "ds_read_b64_tr_b16 v[138:139], v228 offset:11264\n\t"
        "s_waitcnt lgkmcnt(14)\n\t"
        "v_mfma_f32_32x32x16_bf16 v[80:95], v[140:143], v[164:167], v[80:95]\n\t"
        "ds_read_b64_tr_b16 v[140:141], v229 offset:10240\n\t"
        "ds_read_b64_tr_b16 v[142:143], v229 offset:11264\n\t"
        "buffer_load_dwordx4 v[212:215], v224, s[40:43], s44 offen offset:1024 sc1\n\t"
        "s_waitcnt vmcnt(11) lgkmcnt(14)\n\t"
        "v_mfma_f32_32x32x16_bf16 v[64:79], v[144:147], v[168:171], v[64:79]\n\t"
        "ds_read_b64_tr_b16 v[144:145], v228 offset:12288\n\t"
        "ds_read_b64_tr_b16 v[146:147], v228 offset:13312\n\t"
        "s_waitcnt lgkmcnt(14)\n\t"
        "v_mfma_f32_32x32x16_bf16 v[80:95], v[148:151], v[168:171], v[80:95]\n\t"
        "ds_read_b64_tr_b16 v[148:149], v229 offset:12288\n\t"
        "ds_read_b64_tr_b16 v[150:151], v229 offset:13312\n\t"
        "buffer_load_dwordx4 v[216:219], v224, s[40:43], s44 offen offset:2048 sc1\n\t"
        "s_waitcnt vmcnt(11) lgkmcnt(8)\n\t"
        "v_mfma_f32_32x32x16_bf16 v[64:79], v[152:155], v[172:175], v[64:79]\n\t"
        "v_readfirstlane_b32 s51, v233\n\t"
        "ds_read_b64_tr_b16 v[152:153], v228 offset:14336\n\t"
        "ds_read_b64_tr_b16 v[154:155], v228 offset:15360\n\t"
        "s_waitcnt lgkmcnt(14)\n\t"
        "v_mfma_f32_32x32x16_bf16 v[80:95], v[156:159], v[172:175], v[80:95]\n\t"
        "s_andn2_b32 s51, s51, s46\n\t"
        "s_ff1_i32_b32 s47, s51\n\t"
        "s_add_u32 s45, s47, s49\n\t"
        "s_and_b32 s45, s45, 3\n\t"
        "s_lshl_b32 s45, s45, 14\n\t"
        "s_or_b32 s45, s45, s50\n\t"
        "ds_read_b64_tr_b16 v[156:157], v229 offset:14336\n\t"
        "ds_read_b64_tr_b16 v[158:159], v229 offset:15360\n\t"
        "buffer_load_dwordx4 v[220:223], v224, s[40:43], s44 offen offset:3072 sc1\n\t"
        "s_waitcnt vmcnt(11) lgkmcnt(14)\n\t"
        "v_mfma_f32_32x32x16_bf16 v[64:79], v[128:131], v[176:179], v[64:79]\n\t"
        "s_and_b32 s51, s47, 0x80000000\n\t"
        "v_or_b32 v225, s51, v224\n\t"
        "s_and_b32 s51, s47, 3\n\t"
        "s_lshl_b32 s51, s51, 15\n\t"
        "v_add_u32 v230, s51, v226\n\t"
        "v_add_u32 v231, s51, v227\n\t"
        "ds_read_b64_tr_b16 v[128:129], v228 offset:16384\n\t"
        "ds_read_b64_tr_b16 v[130:131], v228 offset:17408\n\t"
        "s_waitcnt lgkmcnt(14)\n\t"
        "v_mfma_f32_32x32x16_bf16 v[80:95], v[132:135], v[176:179], v[80:95]\n\t"
        "ds_read_b64_tr_b16 v[132:133], v229 offset:16384\n\t"
        "ds_read_b64_tr_b16 v[134:135], v229 offset:17408\n\t"
        "buffer_load_dwordx4 v[160:163], v225, s[40:43], s45 offen offset:0 sc1\n\t"
        "s_waitcnt vmcnt(11) lgkmcnt(14)\n\t"
        "v_mfma_f32_32x32x16_bf16 v[64:79], v[136:139], v[180:183], v[64:79]\n\t"
        "ds_read_b64_tr_b16 v[136:137], v228 offset:18432\n\t"
        "ds_read_b64_tr_b16 v[138:139], v228 offset:19456\n\t"
        "s_waitcnt lgkmcnt(14)\n\t"
        "v_mfma_f32_32x32x16_bf16 v[80:95], v[140:143], v[180:183], v[80:95]\n\t"
        "ds_read_b64_tr_b16 v[140:141], v229 offset:18432\n\t"
        "ds_read_b64_tr_b16 v[142:143], v229 offset:19456\n\t"
        "buffer_load_dwordx4 v[164:167], v225, s[40:43], s45 offen offset:1024 sc1\n\t"
        "s_waitcnt vmcnt(11) lgkmcnt(14)\n\t"
        "v_mfma_f32_32x32x16_bf16 v[64:79], v[144:147], v[184:187], v[64:79]\n\t"
        "ds_read_b64_tr_b16 v[144:145], v228 offset:20480\n\t"
        "ds_read_b64_tr_b16 v[146:147], v228 offset:21504\n\t"
        "s_waitcnt lgkmcnt(14)\n\t"
        "v_mfma_f32_32x32x16_bf16 v[80:95], v[148:151], v[184:187], v[80:95]\n\t"
        "ds_read_b64_tr_b16 v[148:149], v229 offset:20480\n\t"
        "ds_read_b64_tr_b16 v[150:151], v229 offset:21504\n\t"
        "buffer_load_dwordx4 v[168:171], v225, s[40:43], s45 offen offset:2048 sc1\n\t"
        "s_waitcnt vmcnt(11) lgkmcnt(14)\n\t"
        "v_mfma_f32_32x32x16_bf16 v[64:79], v[152:155], v[188:191], v[64:79]\n\t"
        "ds_read_b64_tr_b16 v[152:153], v228 offset:22528\n\t"
        "ds_read_b64_tr_b16 v[154:155], v228 offset:23552\n\t"
        "s_waitcnt lgkmcnt(14)\n\t"
        "v_mfma_f32_32x32x16_bf16 v[80:95], v[156:159], v[188:191], v[80:95]\n\t"
        "ds_read_b64_tr_b16 v[156:157], v229 offset:22528\n\t"
        "ds_read_b64_tr_b16 v[158:159], v229 offset:23552\n\t"
        "buffer_load_dwordx4 v[172:175], v225, s[40:43], s45 offen offset:3072 sc1\n\t"
        "s_waitcnt vmcnt(11) lgkmcnt(14)\n\t"
        "v_mfma_f32_32x32x16_bf16 v[64:79], v[128:131], v[192:195], v[64:79]\n\t"
        "ds_read_b64_tr_b16 v[128:129], v228 offset:24576\n\t"
        "ds_read_b64_tr_b16 v[130:131], v228 offset:25600\n\t"
        "s_waitcnt lgkmcnt(14)\n\t"
        "v_mfma_f32_32x32x16_bf16 v[80:95], v[132:135], v[192:195], v[80:95]\n\t"
        "ds_read_b64_tr_b16 v[132:133], v229 offset:24576\n\t"
        "ds_read_b64_tr_b16 v[134:135], v229 offset:25600\n\t"
        "s_add_u32 s51, s45, 0x1000\n\t"
        "s_and_b32 s51, s51, 0x3fff\n\t"
        "s_andn2_b32 s45, s45, 0x3fff\n\t"
        "s_or_b32 s45, s45, s51\n\t"
        "buffer_load_dwordx4 v[176:179], v225, s[40:43], s45 offen offset:0 sc1\n\t"
        "s_waitcnt vmcnt(11) lgkmcnt(14)\n\t"
        "v_mfma_f32_32x32x16_bf16 v[64:79], v[136:139], v[196:199], v[64:79]\n\t"
        "ds_read_b64_tr_b16 v[136:137], v228 offset:26624\n\t"
        "ds_read_b64_tr_b16 v[138:139], v228 offset:27648\n\t"
        "s_waitcnt lgkmcnt(14)\n\t"
        "v_mfma_f32_32x32x16_bf16 v[80:95], v[140:143], v[196:199], v[80:95]\n\t"
        "ds_read_b64_tr_b16 v[140:141], v229 offset:26624\n\t"
        "ds_read_b64_tr_b16 v[142:143], v229 offset:27648\n\t"
        "buffer_load_dwordx4 v[180:183], v225, s[40:43], s45 offen offset:1024 sc1\n\t"
        "s_waitcnt vmcnt(11) lgkmcnt(14)\n\t"
        "v_mfma_f32_32x32x16_bf16 v[64:79], v[144:147], v[200:203], v[64:79]\n\t"
        "ds_read_b64_tr_b16 v[144:145], v228 offset:28672\n\t"
        "ds_read_b64_tr_b16 v[146:147], v228 offset:29696\n\t"
        "s_waitcnt lgkmcnt(14)\n\t"
        "v_mfma_f32_32x32x16_bf16 v[80:95], v[148:151], v[200:203], v[80:95]\n\t"
        "ds_read_b64_tr_b16 v[148:149], v229 offset:28672\n\t"
        "ds_read_b64_tr_b16 v[150:151], v229 offset:29696\n\t"
        "buffer_load_dwordx4 v[184:187], v225, s[40:43], s45 offen offset:2048 sc1\n\t"
        "s_waitcnt vmcnt(11) lgkmcnt(14)\n\t"
        "v_mfma_f32_32x32x16_bf16 v[64:79], v[152:155], v[204:207], v[64:79]\n\t"
        "ds_read_b64_tr_b16 v[152:153], v228 offset:30720\n\t"
        "ds_read_b64_tr_b16 v[154:155], v228 offset:31744\n\t"
        "s_waitcnt lgkmcnt(14)\n\t"
        "v_mfma_f32_32x32x16_bf16 v[80:95], v[156:159], v[204:207], v[80:95]\n\t"
        "ds_read_b64_tr_b16 v[156:157], v229 offset:30720\n\t"
        "ds_read_b64_tr_b16 v[158:159], v229 offset:31744\n\t"
        "buffer_load_dwordx4 v[188:191], v225, s[40:43], s45 offen offset:3072 sc1\n\t"
        "s_waitcnt vmcnt(11) lgkmcnt(14)\n\t"
        "v_mfma_f32_32x32x16_bf16 v[64:79], v[128:131], v[208:211], v[64:79]\n\t"
        "ds_read_b64_tr_b16 v[128:129], v230 offset:0\n\t"
        "ds_read_b64_tr_b16 v[130:131], v230 offset:1024\n\t"
        "s_waitcnt lgkmcnt(14)\n\t"
        "v_mfma_f32_32x32x16_bf16 v[80:95], v[132:135], v[208:211], v[80:95]\n\t"
        "ds_read_b64_tr_b16 v[132:133], v231 offset:0\n\t"
        "ds_read_b64_tr_b16 v[134:135], v231 offset:1024\n\t"
        "s_add_u32 s51, s45, 0x1000\n\t"
        "s_and_b32 s51, s51, 0x3fff\n\t"
        "s_andn2_b32 s45, s45, 0x3fff\n\t"
        "s_or_b32 s45, s45, s51\n\t"
        "buffer_load_dwordx4 v[192:195], v225, s[40:43], s45 offen offset:0 sc1\n\t"
        "s_waitcnt vmcnt(11) lgkmcnt(14)\n\t"
        "v_mfma_f32_32x32x16_bf16 v[64:79], v[136:139], v[212:215], v[64:79]\n\t"
        "ds_read_b64_tr_b16 v[136:137], v230 offset:2048\n\t"
        "ds_read_b64_tr_b16 v[138:139], v230 offset:3072\n\t"
        "s_waitcnt lgkmcnt(14)\n\t"
        "v_mfma_f32_32x32x16_bf16 v[80:95], v[140:143], v[212:215], v[80:95]\n\t"
        "ds_read_b64_tr_b16 v[140:141], v231 offset:2048\n\t"
        "ds_read_b64_tr_b16 v[142:143], v231 offset:3072\n\t"
        "buffer_load_dwordx4 v[196:199], v225, s[40:43], s45 offen offset:1024 sc1\n\t"
        "s_waitcnt vmcnt(11) lgkmcnt(14)\n\t"
        "v_mfma_f32_32x32x16_bf16 v[64:79], v[144:147], v[216:219], v[64:79]\n\t"
        "ds_read_b64_tr_b16 v[144:145], v230 offset:4096\n\t"
        "ds_read_b64_tr_b16 v[146:147], v230 offset:5120\n\t"
        "s_waitcnt lgkmcnt(14)\n\t"
        "v_mfma_f32_32x32x16_bf16 v[80:95], v[148:151], v[216:219], v[80:95]\n\t"
        "ds_read_b64_tr_b16 v[148:149], v231 offset:4096\n\t"
        "ds_read_b64_tr_b16 v[150:151], v231 offset:5120\n\t"
        "buffer_load_dwordx4 v[200:203], v225, s[40:43], s45 offen offset:2048 sc1\n\t"
        "s_waitcnt vmcnt(11) lgkmcnt(14)\n\t"
        "v_mfma_f32_32x32x16_bf16 v[64:79], v[152:155], v[220:223], v[64:79]\n\t"
        "ds_read_b64_tr_b16 v[152:153], v230 offset:6144\n\t"
        "ds_read_b64_tr_b16 v[154:155], v230 offset:7168\n\t"
        "s_waitcnt lgkmcnt(14)\n\t"
        "v_mfma_f32_32x32x16_bf16 v[80:95], v[156:159], v[220:223], v[80:95]\n\t"
        "ds_read_b64_tr_b16 v[156:157], v231 offset:6144\n\t"
        "ds_read_b64_tr_b16 v[158:159], v231 offset:7168\n\t"
        "buffer_load_dwordx4 v[204:207], v225, s[40:43], s45 offen offset:3072 sc1\n\t"
        "s_mov_b32 s44, s45\n\t"
        "s_cmp_eq_u32 s47, 0\n\t"
        "s_cbranch_scc1 oseg0_%=\n\t"
        "s_cmp_eq_u32 s47, 1\n\t"
        "s_cbranch_scc1 oseg1_%=\n\t"
        "s_cmp_eq_u32 s47, 2\n\t"
        "s_cbranch_scc1 oseg2_%=\n\t"
        "s_cmp_eq_u32 s47, 3\n\t"
        "s_cbranch_scc1 oseg3_%=\n\t"
        "s_cmp_lg_u32 s46, 15\n\t"
        "s_cbranch_scc1 ostart_%=\n\t"
        "s_branch oend_%=\n\t"
        "oseg3_%=:\n\t"
        "s_or_b32 s46, s46, 8\n\t"
        "s_waitcnt vmcnt(11) lgkmcnt(14)\n\t"
        "v_mfma_f32_32x32x16_bf16 v[96:111], v[128:131], v[160:163], 0\n\t"
        "ds_read_b64_tr_b16 v[128:129], v228 offset:40960\n\t"
        "ds_read_b64_tr_b16 v[130:131], v228 offset:41984\n\t"
        "s_waitcnt lgkmcnt(14)\n\t"
        "v_mfma_f32_32x32x16_bf16 v[112:127], v[132:135], v[160:163], 0\n\t"
        "ds_read_b64_tr_b16 v[132:133], v229 offset:40960\n\t"
        "ds_read_b64_tr_b16 v[134:135], v229 offset:41984\n\t"
        "s_add_u32 s51, s44, 0x1000\n\t"
        "s_and_b32 s51, s51, 0x3fff\n\t"
        "s_andn2_b32 s44, s44, 0x3fff\n\t"
        "s_or_b32 s44, s44, s51\n\t"
        "buffer_load_dwordx4 v[208:211], v224, s[40:43], s44 offen offset:0 sc1\n\t"
        "s_waitcnt vmcnt(11) lgkmcnt(14)\n\t"
        "v_mfma_f32_32x32x16_bf16 v[96:111], v[136:139], v[164:167], v[96:111]\n\t"
        "ds_read_b32 v233, v232\n\t"
        "ds_read_b64_tr_b16 v[136:137], v228 offset:43008\n\t"
        "ds_read_b64_tr_b16 v[138:139], v228 offset:44032\n\t"
        "s_waitcnt lgkmcnt(14)\n\t"
        "v_mfma_f32_32x32x16_bf16 v[112:127], v[140:143], v[164:167], v[112:127]\n\t"
        "ds_read_b64_tr_b16 v[140:141], v229 offset:43008\n\t"
        "ds_read_b64_tr_b16 v[142:143], v229 offset:44032\n\t"
        "buffer_load_dwordx4 v[212:215], v224, s[40:43], s44 offen offset:1024 sc1\n\t"
        "s_waitcnt vmcnt(11) lgkmcnt(14)\n\t"
        "v_mfma_f32_32x32x16_bf16 v[96:111], v[144:147], v[168:171], v[96:111]\n\t"
        "ds_read_b64_tr_b16 v[144:145], v228 offset:45056\n\t"
        "ds_read_b64_tr_b16 v[146:147], v228 offset:46080\n\t"
        "s_waitcnt lgkmcnt(14)\n\t"
        "v_mfma_f32_32x32x16_bf16 v[112:127], v[148:151], v[168:171], v[112:127]\n\t"
        "ds_read_b64_tr_b16 v[148:149], v229 offset:45056\n\t"
        "ds_read_b64_tr_b16 v[150:151], v229 offset:46080\n\t"
        "buffer_load_dwordx4 v[216:219], v224, s[40:43], s44 offen offset:2048 sc1\n\t"
        "s_waitcnt vmcnt(11) lgkmcnt(8)\n\t"
        "v_mfma_f32_32x32x16_bf16 v[96:111], v[152:155], v[172:175], v[96:111]\n\t"
        "v_readfirstlane_b32 s51, v233\n\t"
        "ds_read_b64_tr_b16 v[152:153], v228 offset:47104\n\t"
        "ds_read_b64_tr_b16 v[154:155], v228 offset:48128\n\t"
        "s_waitcnt lgkmcnt(14)\n\t"
        "v_mfma_f32_32x32x16_bf16 v[112:127], v[156:159], v[172:175], v[112:127]\n\t"
        "s_andn2_b32 s51, s51, s46\n\t"
        "s_ff1_i32_b32 s47, s51\n\t"
        "s_add_u32 s45, s47, s49\n\t"
        "s_and_b32 s45, s45, 3\n\t"
        "s_lshl_b32 s45, s45, 14\n\t"
        "s_or_b32 s45, s45, s50\n\t"
        "ds_read_b64_tr_b16 v[156:157], v229 offset:47104\n\t"
        "ds_read_b64_tr_b16 v[158:159], v229 offset:48128\n\t"
        "buffer_load_dwordx4 v[220:223], v224, s[40:43], s44 offen offset:3072 sc1\n\t"
        "s_waitcnt vmcnt(11) lgkmcnt(14)\n\t"
        "v_mfma_f32_32x32x16_bf16 v[96:111], v[128:131], v[176:179], v[96:111]\n\t"
        "s_and_b32 s51, s47, 0x80000000\n\t"
        "v_or_b32 v225, s51, v224\n\t"
        "s_and_b32 s51, s47, 3\n\t"
        "s_lshl_b32 s51, s51, 15\n\t"
        "v_add_u32 v230, s51, v226\n\t"
        "v_add_u32 v231, s51, v227\n\t"
        "ds_read_b64_tr_b16 v[128:129], v228 offset:49152\n\t"
        "ds_read_b64_tr_b16 v[130:131], v228 offset:50176\n\t"
        "s_waitcnt lgkmcnt(14)\n\t"
        "v_mfma_f32_32x32x16_bf16 v[112:127], v[132:135], v[176:179], v[112:127]\n\t"
        "ds_read_b64_tr_b16 v[132:133], v229 offset:49152\n\t"
        "ds_read_b64_tr_b16 v[134:135], v229 offset:50176\n\t"
        "buffer_load_dwordx4 v[160:163], v225, s[40:43], s45 offen offset:0 sc1\n\t"
        "s_waitcnt vmcnt(11) lgkmcnt(14)\n\t"
        "v_mfma_f32_32x32x16_bf16 v[96:111], v[136:139], v[180:183], v[96:111]\n\t"
        "ds_read_b64_tr_b16 v[136:137], v228 offset:51200\n\t"
        "ds_read_b64_tr_b16 v[138:139], v228 offset:52224\n\t"
        "s_waitcnt lgkmcnt(14)\n\t"
        "v_mfma_f32_32x32x16_bf16 v[112:127], v[140:143], v[180:183], v[112:127]\n\t"
        "ds_read_b64_tr_b16 v[140:141], v229 offset:51200\n\t"
        "ds_read_b64_tr_b16 v[142:143], v229 offset:52224\n\t"
        "buffer_load_dwordx4 v[164:167], v225, s[40:43], s45 offen offset:1024 sc1\n\t"
        "s_waitcnt vmcnt(11) lgkmcnt(14)\n\t"
        "v_mfma_f32_32x32x16_bf16 v[96:111], v[144:147], v[184:187], v[96:111]\n\t"
        "ds_read_b64_tr_b16 v[144:145], v228 offset:53248\n\t"
        "ds_read_b64_tr_b16 v[146:147], v228 offset:54272\n\t"
        "s_waitcnt lgkmcnt(14)\n\t"
        "v_mfma_f32_32x32x16_bf16 v[112:127], v[148:151], v[184:187], v[112:127]\n\t"
        "ds_read_b64_tr_b16 v[148:149], v229 offset:53248\n\t"
        "ds_read_b64_tr_b16 v[150:151], v229 offset:54272\n\t"
        "buffer_load_dwordx4 v[168:171], v225, s[40:43], s45 offen offset:2048 sc1\n\t"
        "s_waitcnt vmcnt(11) lgkmcnt(14)\n\t"
        "v_mfma_f32_32x32x16_bf16 v[96:111], v[152:155], v[188:191], v[96:111]\n\t"
        "ds_read_b64_tr_b16 v[152:153], v228 offset:55296\n\t"
        "ds_read_b64_tr_b16 v[154:155], v228 offset:56320\n\t"
        "s_waitcnt lgkmcnt(14)\n\t"
        "v_mfma_f32_32x32x16_bf16 v[112:127], v[156:159], v[188:191], v[112:127]\n\t"
        "ds_read_b64_tr_b16 v[156:157], v229 offset:55296\n\t"
        "ds_read_b64_tr_b16 v[158:159], v229 offset:56320\n\t"
        "buffer_load_dwordx4 v[172:175], v225, s[40:43], s45 offen offset:3072 sc1\n\t"
        "s_waitcnt vmcnt(11) lgkmcnt(14)\n\t"
        "v_mfma_f32_32x32x16_bf16 v[96:111], v[128:131], v[192:195], v[96:111]\n\t"
        "ds_read_b64_tr_b16 v[128:129], v228 offset:57344\n\t"
        "ds_read_b64_tr_b16 v[130:131], v228 offset:58368\n\t"
        "s_waitcnt lgkmcnt(14)\n\t"
        "v_mfma_f32_32x32x16_bf16 v[112:127], v[132:135], v[192:195], v[112:127]\n\t"
        "ds_read_b64_tr_b16 v[132:133], v229 offset:57344\n\t"
        "ds_read_b64_tr_b16 v[134:135], v229 offset:58368\n\t"
        "s_add_u32 s51, s45, 0x1000\n\t"
        "s_and_b32 s51, s51, 0x3fff\n\t"
        "s_andn2_b32 s45, s45, 0x3fff\n\t"
        "s_or_b32 s45, s45, s51\n\t"
        "buffer_load_dwordx4 v[176:179], v225, s[40:43], s45 offen offset:0 sc1\n\t"
        "s_waitcnt vmcnt(11) lgkmcnt(14)\n\t"
        "v_mfma_f32_32x32x16_bf16 v[96:111], v[136:139], v[196:199], v[96:111]\n\t"
        "ds_read_b64_tr_b16 v[136:137], v228 offset:59392\n\t"
        "ds_read_b64_tr_b16 v[138:139], v228 offset:60416\n\t"
        "s_waitcnt lgkmcnt(14)\n\t"
        "v_mfma_f32_32x32x16_bf16 v[112:127], v[140:143], v[196:199], v[112:127]\n\t"
        "ds_read_b64_tr_b16 v[140:141], v229 offset:59392\n\t"
        "ds_read_b64_tr_b16 v[142:143], v229 offset:60416\n\t"
        "buffer_load_dwordx4 v[180:183], v225, s[40:43], s45 offen offset:1024 sc1\n\t"
        "s_waitcnt vmcnt(11) lgkmcnt(14)\n\t"
        "v_mfma_f32_32x32x16_bf16 v[96:111], v[144:147], v[200:203], v[96:111]\n\t"
        "ds_read_b64_tr_b16 v[144:145], v228 offset:61440\n\t"
        "ds_read_b64_tr_b16 v[146:147], v228 offset:62464\n\t"
        "s_waitcnt lgkmcnt(14)\n\t"
        "v_mfma_f32_32x32x16_bf16 v[112:127], v[148:151], v[200:203], v[112:127]\n\t"
        "ds_read_b64_tr_b16 v[148:149], v229 offset:61440\n\t"
        "ds_read_b64_tr_b16 v[150:151], v229 offset:62464\n\t"
        "buffer_load_dwordx4 v[184:187], v225, s[40:43], s45 offen offset:2048 sc1\n\t"
        "s_waitcnt vmcnt(11) lgkmcnt(14)\n\t"
        "v_mfma_f32_32x32x16_bf16 v[96:111], v[152:155], v[204:207], v[96:111]\n\t"
        "ds_read_b64_tr_b16 v[152:153], v228 offset:63488\n\t"
        "ds_read_b64_tr_b16 v[154:155], v228 offset:64512\n\t"
        "s_waitcnt lgkmcnt(14)\n\t"
        "v_mfma_f32_32x32x16_bf16 v[112:127], v[156:159], v[204:207], v[112:127]\n\t"
        "ds_read_b64_tr_b16 v[156:157], v229 offset:63488\n\t"
        "ds_read_b64_tr_b16 v[158:159], v229 offset:64512\n\t"
        "buffer_load_dwordx4 v[188:191], v225, s[40:43], s45 offen offset:3072 sc1\n\t"
        "s_waitcnt vmcnt(11) lgkmcnt(14)\n\t"
        "v_mfma_f32_32x32x16_bf16 v[96:111], v[128:131], v[208:211], v[96:111]\n\t"
        "ds_read_b64_tr_b16 v[128:129], v230 offset:0\n\t"
        "ds_read_b64_tr_b16 v[130:131], v230 offset:1024\n\t"
        "s_waitcnt lgkmcnt(14)\n\t"
        "v_mfma_f32_32x32x16_bf16 v[112:127], v[132:135], v[208:211], v[112:127]\n\t"
        "ds_read_b64_tr_b16 v[132:133], v231 offset:0\n\t"
        "ds_read_b64_tr_b16 v[134:135], v231 offset:1024\n\t"
        "s_add_u32 s51, s45, 0x1000\n\t"
        "s_and_b32 s51, s51, 0x3fff\n\t"
        "s_andn2_b32 s45, s45, 0x3fff\n\t"
        "s_or_b32 s45, s45, s51\n\t"
        "buffer_load_dwordx4 v[192:195], v225, s[40:43], s45 offen offset:0 sc1\n\t"
        "s_waitcnt vmcnt(11) lgkmcnt(14)\n\t"
        "v_mfma_f32_32x32x16_bf16 v[96:111], v[136:139], v[212:215], v[96:111]\n\t"
        "ds_read_b64_tr_b16 v[136:137], v230 offset:2048\n\t"
        "ds_read_b64_tr_b16 v[138:139], v230 offset:3072\n\t"
        "s_waitcnt lgkmcnt(14)\n\t"
        "v_mfma_f32_32x32x16_bf16 v[112:127], v[140:143], v[212:215], v[112:127]\n\t"
        "ds_read_b64_tr_b16 v[140:141], v231 offset:2048\n\t"
        "ds_read_b64_tr_b16 v[142:143], v231 offset:3072\n\t"
        "buffer_load_dwordx4 v[196:199], v225, s[40:43], s45 offen offset:1024 sc1\n\t"
        "s_waitcnt vmcnt(11) lgkmcnt(14)\n\t"
        "v_mfma_f32_32x32x16_bf16 v[96:111], v[144:147], v[216:219], v[96:111]\n\t"
        "ds_read_b64_tr_b16 v[144:145], v230 offset:4096\n\t"
        "ds_read_b64_tr_b16 v[146:147], v230 offset:5120\n\t"
        "s_waitcnt lgkmcnt(14)\n\t"
        "v_mfma_f32_32x32x16_bf16 v[112:127], v[148:151], v[216:219], v[112:127]\n\t"
        "ds_read_b64_tr_b16 v[148:149], v231 offset:4096\n\t"
        "ds_read_b64_tr_b16 v[150:151], v231 offset:5120\n\t"
        "buffer_load_dwordx4 v[200:203], v225, s[40:43], s45 offen offset:2048 sc1\n\t"
        "s_waitcnt vmcnt(11) lgkmcnt(14)\n\t"
        "v_mfma_f32_32x32x16_bf16 v[96:111], v[152:155], v[220:223], v[96:111]\n\t"
        "ds_read_b64_tr_b16 v[152:153], v230 offset:6144\n\t"
        "ds_read_b64_tr_b16 v[154:155], v230 offset:7168\n\t"
        "s_waitcnt lgkmcnt(14)\n\t"
        "v_mfma_f32_32x32x16_bf16 v[112:127], v[156:159], v[220:223], v[112:127]\n\t"
        "ds_read_b64_tr_b16 v[156:157], v231 offset:6144\n\t"
        "ds_read_b64_tr_b16 v[158:159], v231 offset:7168\n\t"
        "buffer_load_dwordx4 v[204:207], v225, s[40:43], s45 offen offset:3072 sc1\n\t"
        "s_mov_b32 s44, s45\n\t"
        "s_cmp_eq_u32 s47, 0\n\t"
        "s_cbranch_scc1 oseg0_%=\n\t"
        "s_cmp_eq_u32 s47, 1\n\t"
        "s_cbranch_scc1 oseg1_%=\n\t"
        "s_cmp_eq_u32 s47, 2\n\t"
        "s_cbranch_scc1 oseg2_%=\n\t"
        "s_cmp_eq_u32 s47, 3\n\t"
        "s_cbranch_scc1 oseg3_%=\n\t"
        "s_cmp_lg_u32 s46, 15\n\t"
        "s_cbranch_scc1 ostart_%=\n\t"
        "s_branch oend_%=\n\t"
        "oend_%=:\n\t"
        "s_waitcnt vmcnt(0) lgkmcnt(0)\n\t"
        "s_barrier\n\t"
        "s_nop 3\n\t"
        : "+{v[0:15]}"(acc[0]), "+{v[16:31]}"(acc[1]), "={v[32:47]}"(acc[2]), "={v[48:63]}"(acc[3]), "={v[64:79]}"(acc[4]), "={v[80:95]}"(acc[5]),
          "={v[96:111]}"(acc[6]), "={v[112:127]}"(acc[7]) STAMP_OUTS
        : "{v224}"(OFF_OF + L * (5 * MBY) + tile * TS + lane * 16), "{v226}"(a0), "{v227}"(a1), "{v228}"(a0 + 65536), "{v229}"(a1 + 65536),
          "{v232}"(lbase + L_W + 20 + 8 * L), "{s49}"((tn >> 2) + 1), "{s50}"(0 * (tn & 3) * 4096), "{s[40:43]}"(srd)
        : "v128", "v129", "v130", "v131", "v132", "v133", "v134", "v135", "v136", "v137", "v138", "v139", "v140", "v141", "v142", "v143", "v144", "v145", "v146", "v147", "v148", "v149", "v150", "v151", "v152", "v153", "v154", "v155", "v156", "v157", "v158", "v159", "v160", "v161", "v162", "v163", "v164", "v165", "v166", "v167", "v168", "v169", "v170", "v171", "v172", "v173", "v174", "v175", "v176", "v177", "v178", "v179", "v180", "v181", "v182", "v183", "v184", "v185", "v186", "v187", "v188", "v189", "v190", "v191", "v192", "v193", "v194", "v195", "v196", "v197", "v198", "v199", "v200", "v201", "v202", "v203", "v204", "v205", "v206", "v207", "v208", "v209", "v210", "v211", "v212", "v213", "v214", "v215", "v216", "v217", "v218", "v219", "v220", "v221", "v222", "v223", "v225", "v230", "v231", "v233", "s44", "s45", "s46", "s47", "s48", "s51", "vcc", "scc", "memory");
      }
      PHASE_VARS;
      STAMP_PUT(12 + 16 * L);
      STAMPV(15 + 16 * L);
#pragma unroll
      for (int g = 0; g < 8; ++g) wrc[g] = *reinterpret_cast<const u32x2*>(smem + L_WR + g * 16 + hi * 8);
      SBAR();
      float s = 0.f, sq = 0.f, d = 0.f;
      const unsigned xdst = (L ? OOB : OFF_X1 + tile * TS + tn * 4096) + lane * 16;
#pragma unroll
      for (int i = 0; i < 4; ++i) {
#pragma unroll
        for (int e = 0; e < 4; ++e) {
          const int j = i >> 1, k = 8 * (i & 1) + 2 * e;
          x1p[i][e] = pack2((acc[j][k] + acc[2 + j][k]) + (acc[4 + j][k] + acc[6 + j][k]), (acc[j][k + 1] + acc[2 + j][k + 1]) + (acc[4 + j][k + 1] + acc[6 + j][k + 1]));
        }
        stg(rs, xdst + i * 1024, x1p[i]);
        SBAR();
      }
#pragma unroll
      for (int g = 0; g < 8; ++g)
#pragma unroll
        for (int e = 0; e < 2; ++e) {
          const unsigned o = x1p[g >> 1][2 * (g & 1) + e];
          s = dot2(o, 0x3f803f80u, s); sq = dot2(o, o, sq); d = dot2(o, wrc[g][e], d);
        }
      const u32x4 pst = {nzf(half_swap_sum(s)), nzf(half_swap_sum(sq)), nzf(half_swap_sum(d)), 0u};
      asm volatile("buffer_store_dwordx4 %0, %1, %2, 0 offen sc1\n\ts_waitcnt vmcnt(1)\n\tds_write_b32 %3, %4"
                   :: "v"(pst), "v"(stats_dst(sx, tile, tn, lane)), "s"(srd), "v"(lbase + L_W + 64 + 4 * wid), "v"(3) : "memory");
    }
    STAMPV(16 + 16 * L);
  }
  {
    PHASE_VARS;
    float S, Q, Dd, mean, var, mb1, rd1;
    __builtin_amdgcn_s_sleep(13);
    row_stats_sum(rs, OFF_SX + SX_LAYER, tile, lane, S, Q, Dd);
    STAMPV(33);
    row_stats_fin(rs, OFF_SX + SX_LAYER + SX_CAND + (tn * 16 + tm) * 128 + wid * 4, smem, 2, tile, lane, S, Q, Dd, U2F(words[0]), x1p, mean, var, mb1, rd1);
    STAMPV(34);
    u32x4 xo[4];
#pragma unroll
    for (int i = 0; i < 4; ++i) {
#pragma unroll
      for (int e = 0; e < 4; ++e) {
        const unsigned h2 = ln_pair(x1p[i][e], mb1, rd1);
        xo[i][e] = pack2(lo16(x1p[i][e]) + lo16(h2), hi16(x1p[i][e]) + hi16(h2));
      }
      *reinterpret_cast<u32x4*>(smem + wid * 4096 + r32 * 128 + ((2 * i + hi) ^ (r32 & 7)) * 16) = fo_swap(xo[i]);
    }
    asm volatile("s_waitcnt lgkmcnt(0)" ::: "memory");
#pragma unroll
    for (int i = 0; i < 4; ++i) { xo[i] = *reinterpret_cast<const u32x4*>(smem + wid * 4096 + i * 1024 + (lane >> 3) * 128 + ((lane ^ (lane >> 3)) & 7) * 16); PIN(xo[i]); }
    u16* dst = out + (size_t)(tile * 32 + (lane >> 3)) * D_ + tn * 64 + (lane & 7) * 8;
    lds_wait_ge(lbase + L_W + 4 + 4 * (lane & 1), 2);
    const int rel = (int)words[3] - tile * 32 - (lane >> 3);
#pragma unroll
    for (int i = 0; i < 4; ++i)
      if (rel != 8 * i) st16(dst + (size_t)i * 8 * D_, xo[i]);
    STAMPV(35);
    STAMP_END;
  }
}

extern "C" __global__ void end_marker() {}

extern "C" void kernel_launch(void* const* d_in, const int* in_sizes, int n_in, void* d_out, int out_size,
                              void* d_ws, size_t ws_size, hipStream_t stream) {
  char* ws = (char*)d_ws;
  mega_kernel<<<256, 512, 0, stream>>>((const u16*)d_in[0], (const u16*)d_in[1], (const u16*)d_in[2], (const u16*)d_in[4], (const u16*)d_in[5],
                                       (const u16*)d_in[6], (const u16*)d_in[7], ws, (u16*)d_out);
}
```

```cpp
#include <hip/hip_runtime.h>
#include <hip/hip_bf16.h>
#include <stdint.h>

typedef unsigned short u16;
typedef short s16x4 __attribute__((ext_vector_type(4)));
typedef short s16x8 __attribute__((ext_vector_type(8)));
typedef float f32x16 __attribute__((ext_vector_type(16)));
typedef float f32x8 __attribute__((ext_vector_type(8)));
typedef float f32x4 __attribute__((ext_vector_type(4)));
typedef float f32x2 __attribute__((ext_vector_type(2)));
typedef unsigned int u32x16 __attribute__((ext_vector_type(16)));
typedef unsigned int u32x8 __attribute__((ext_vector_type(8)));
typedef unsigned int u32x4 __attribute__((ext_vector_type(4)));
typedef unsigned int u32x2 __attribute__((ext_vector_type(2)));
typedef __bf16 bf16x2 __attribute__((ext_vector_type(2)));
typedef __bf16 bf16x8 __attribute__((ext_vector_type(8)));
typedef __bf16 bf16x16 __attribute__((ext_vector_type(16)));

#define DI __device__ __forceinline__
#define LDS_PTR(T, p) ((__attribute__((address_space(3))) T*)(p))
#define BARRIER() asm volatile("s_barrier" ::: "memory")
#define BAR_LGKM() asm volatile("s_waitcnt lgkmcnt(0)\n\ts_barrier" ::: "memory")
#define BAR_ALL() asm volatile("s_waitcnt vmcnt(0) lgkmcnt(0)\n\ts_barrier" ::: "memory")
#define DRAIN() asm volatile("s_waitcnt vmcnt(0)" ::: "memory")
#define PACE() ({ asm volatile("s_sleep 1\n\ts_waitcnt vmcnt(3)" ::: "memory"); })
#define MFMA32(a, b, c) __builtin_amdgcn_mfma_f32_32x32x16_bf16(a, b, c, 0, 0, 0)
#define MFMA16(a, b, c) __builtin_amdgcn_mfma_f32_16x16x32_bf16(a, b, c, 0, 0, 0)
#define PIN(x) asm volatile("" : "+v"(x))
#define EX2(x) __builtin_amdgcn_exp2f(x)
#define RCP(x) __builtin_amdgcn_rcpf(x)
#define SBAR() __builtin_amdgcn_sched_barrier(0)

#define STAMP_INIT
#define STAMPV(i)
#define STAMP_END
#define STAMP_DECL
#define STAMP_OUTS
#define STAMP_PUT(i)
constexpr int D_ = 1024, F_ = 3584;

constexpr unsigned MBY = 1u << 20;
constexpr unsigned TS = 65536 + 4096, HS = 262144 + 4096;
constexpr unsigned OFF_HF = 0, OFF_X1 = 5 * MBY;
constexpr unsigned OFF_OF = 10 * MBY;
constexpr unsigned OFF_KV = 20 * MBY;
constexpr unsigned OFF_FL = 40 * MBY;
constexpr unsigned FL_EPOCH = OFF_FL;
constexpr unsigned FL_KV = OFF_FL + 4096;
constexpr unsigned FL_O = FL_KV + 65536;
constexpr unsigned FL_X = FL_O + 65536;
constexpr unsigned OFF_CS = 41 * MBY;
constexpr unsigned OFF_SX = 42 * MBY;
constexpr unsigned SX_ST = 0;
constexpr unsigned SX_ROW = 512 * 1024;
constexpr unsigned SX_CAND = SX_ROW + 256 * 1024;
constexpr unsigned SX_GP = SX_CAND + 32 * 1024;
constexpr unsigned SX_YP = SX_GP + 224 * 1024;
constexpr unsigned SX_LAYER = SX_YP + 224 * 1024;
constexpr unsigned SX_QP = 2 * SX_LAYER;
constexpr unsigned SX_BYTES = SX_QP + 512 * 1024;
static_assert(SX_BYTES <= 6 * 256 * 128 * 16, "sentinel fill");
constexpr unsigned WS_BYTES = 48 * MBY, OOB = 0x80000000u;
constexpr unsigned SENT = 0xFFFFFFFFu;

constexpr int NS = 9, DD = 3, SLOTB = 12288;
constexpr int L_XCH = 122880;
constexpr int L_SL = L_XCH + 8192;
constexpr int L_CAND = L_XCH + 32768;
constexpr int L_AB = L_XCH + 34816;
constexpr int L_FULL = 159744, L_FREE = L_FULL + 512;
constexpr int L_CS = L_FREE + 1024, L_PATCH = L_CS + 768;
constexpr int L_GU = L_PATCH + 768, L_TAG = L_GU + 256, L_WR = L_TAG + 256;
constexpr int L_X2F = L_WR + 128, L_X2P = L_X2F + 128;
constexpr int L_W = L_X2P + 128;
constexpr int LDS_BYTES = 163840;
static_assert(NS * SLOTB <= 54 * 2048 && L_W + 128 <= LDS_BYTES && (NS + 1) * SLOTB <= L_XCH && L_AB + 2048 <= L_FULL, "LDS map");

DI int fresh_lane() { int l; asm volatile("v_mbcnt_lo_u32_b32 %0, -1, 0\n\tv_mbcnt_hi_u32_b32 %0, -1, %0" : "=v"(l)); return l; }
#define LANE_VARS const int lane = fresh_lane(), r32 = lane & 31, hi = lane >> 5, tq = (lane & 15) >> 2, tp = lane & 3, tblk = (lane >> 4) & 1, \
  wrow = 4 * hi + tq, wsw = 4 * ((wrow >> 1) & 1), wch = 2 * tblk + (tp >> 1), c24 = r32 < 24 ? r32 : 23; \
  (void)r32; (void)hi; (void)wsw; (void)wch; (void)c24
DI int fresh_s(int v) { asm volatile("" : "+s"(v)); return v; }
#define PHASE_VARS const int tm = fresh_s(tm_), tn = fresh_s(tn_), wid = fresh_s(wid_), tile = tm * 4 + wid, hh = wid & 1, kh = wid >> 1; (void)tile; (void)hh; (void)kh; LANE_VARS
DI float bf2f(u16 v) { return __uint_as_float(((unsigned)v) << 16); }
DI unsigned pack2(float lo, float hi) { f32x2 v = {lo, hi}; return __builtin_bit_cast(unsigned, __builtin_convertvector(v, bf16x2)); }
DI float rbf(float f) { return __uint_as_float(pack2(f, 0.f) << 16); }
DI float lo16(unsigned u) { return __uint_as_float(u << 16); }
DI float hi16(unsigned u) { return __uint_as_float(u & 0xffff0000u); }
DI float opq(float r) { asm("" : "+v"(r)); return r; }
DI float fadd(float a, float b) { return opq(a + b); }
DI float fmul(float a, float b) { return opq(a * b); }
DI float ffma(float a, float b, float c) { return opq(__builtin_fmaf(a, b, c)); }
DI float U2F(unsigned u) { return __uint_as_float(u); }
DI float half_swap_sum(float v) {
  auto r = __builtin_amdgcn_permlane32_swap(__float_as_uint(v), __float_as_uint(v), false, false);
  return fadd(U2F(r[0]), U2F(r[1]));
}
DI float half_swap_max(float v) {
  auto r = __builtin_amdgcn_permlane32_swap(__float_as_uint(v), __float_as_uint(v), false, false);
  return fmaxf(__uint_as_float(r[0]), __uint_as_float(r[1]));
}
template <int CTRL> DI unsigned dpp(unsigned v) { return (unsigned)__builtin_amdgcn_update_dpp(0, (int)v, CTRL, 0xf, 0xf, false); }
DI float row_swap_sum(float v) {
  auto r = __builtin_amdgcn_permlane16_swap(__float_as_uint(v), __float_as_uint(v), false, false);
  return fadd(U2F(r[0]), U2F(r[1]));
}
DI float half_sum(float v) {
  v = fadd(v, U2F(dpp<0xB1>(__float_as_uint(v)))); v = fadd(v, U2F(dpp<0x4E>(__float_as_uint(v))));
  v = fadd(v, U2F(dpp<0x141>(__float_as_uint(v)))); v = fadd(v, U2F(dpp<0x140>(__float_as_uint(v))));
  return row_swap_sum(v);
}
DI float wave_sum(float v) { return half_swap_sum(half_sum(v)); }
DI unsigned half_max_u32(unsigned v) {
  v = max(v, dpp<0xB1>(v)); v = max(v, dpp<0x4E>(v)); v = max(v, dpp<0x141>(v)); v = max(v, dpp<0x140>(v));
  auto r = __builtin_amdgcn_permlane16_swap(v, v, false, false);
  return max(r[0], r[1]);
}
DI unsigned wave_max_u32(unsigned v) {
  v = half_max_u32(v);
  auto r = __builtin_amdgcn_permlane32_swap(v, v, false, false);
  return max(r[0], r[1]);
}
DI u32x4 fo_swap(u32x4 v) {
  auto a = __builtin_amdgcn_permlane32_swap(v[0], v[2], false, false);
  auto b = __builtin_amdgcn_permlane32_swap(v[1], v[3], false, false);
  u32x4 o; o[0] = a[0]; o[1] = b[0]; o[2] = a[1]; o[3] = b[1];
  return o;
}
DI void glds4(const void* sb, unsigned vo, unsigned lds_dst) {
  unsigned keep;
  asm volatile("s_mov_b32 %0, m0\n\ts_mov_b32 m0, %3\n\ts_nop 0\n\tglobal_load_lds_dword %1, %2\n\ts_mov_b32 m0, %0"
               : "=&s"(keep) : "v"(vo), "s"(sb), "s"(lds_dst) : "memory");
}
#define GL_ "global_load_lds_dwordx4 "
DI void glds_v4(const void* sb, const unsigned (&v)[4], unsigned m0v) {
  unsigned keep;
  asm volatile("s_mov_b32 %0, m0\n\ts_mov_b32 m0, %6\n\ts_nop 0\n\t" GL_ "%1, %5\n\t" GL_ "%2, %5 offset:1024\n\t" GL_ "%3, %5 offset:2048\n\t"
               GL_ "%4, %5 offset:3072\n\ts_mov_b32 m0, %0" : "=&s"(keep) : "v"(v[0]), "v"(v[1]), "v"(v[2]), "v"(v[3]), "s"(sb), "s"(m0v) : "memory");
}
#define GP_ "\n\ts_sleep %8\n\ts_waitcnt vmcnt(%7)\n\t"
template <int TH, int SL> DI void glds_v4t(const void* sb, const unsigned (&v)[4], unsigned m0v) {
  unsigned keep;
  asm volatile("s_mov_b32 %0, m0\n\ts_mov_b32 m0, %6\n\ts_nop 0\n\t" GL_ "%1, %5" GP_ GL_ "%2, %5 offset:1024" GP_ GL_ "%3, %5 offset:2048" GP_ GL_ "%4, %5 offset:3072" GP_ "s_mov_b32 m0, %0"
               : "=&s"(keep) : "v"(v[0]), "v"(v[1]), "v"(v[2]), "v"(v[3]), "s"(sb), "s"(m0v), "n"(TH), "n"(SL) : "memory");
}
DI void glds_v3(const void* sb, const unsigned (&v)[3], unsigned m0v) {
  unsigned keep;
  asm volatile("s_mov_b32 %0, m0\n\ts_mov_b32 m0, %5\n\ts_nop 0\n\t" GL_ "%1, %4\n\t" GL_ "%2, %4 offset:1024\n\t" GL_ "%3, %4 offset:2048\n\t"
               "s_mov_b32 m0, %0" : "=&s"(keep) : "v"(v[0]), "v"(v[1]), "v"(v[2]), "s"(sb), "s"(m0v) : "memory");
}
DI float dot2(unsigned a, unsigned b, float c) {
  return __builtin_amdgcn_fdot2_f32_bf16(__builtin_bit_cast(bf16x2, a), __builtin_bit_cast(bf16x2, b), c, false);
}
#define CODE_ADDR(sym, out) do { unsigned long long pc_; unsigned off_; \
  asm volatile("s_getpc_b64 %0\n1:\n\ts_mov_b32 %1, " #sym "-1b" : "=s"(pc_), "=s"(off_)); out = (const char*)(pc_ + (long long)(int)off_); } while (0)
DI void st16(void* p, u32x4 v) { asm volatile("global_store_dwordx4 %0, %1, off sc0 sc1\n\ts_nop 1" :: "v"(p), "v"(v) : "memory"); }
DI void ln_scalars(float mean, float var, float& mb, float& den) { mb = rbf(mean); den = rbf(__builtin_amdgcn_sqrtf(rbf(rbf(var) + 1e-5f))); }
DI unsigned ln_pair(unsigned v, float mb, float rd) {
  const unsigned c = pack2(lo16(v) - mb, hi16(v) - mb);
  return pack2(lo16(c) * rd, hi16(c) * rd);
}
DI unsigned ln_pair_s(unsigned v, float nmb, float rd) {
  const unsigned c = pack2(fadd(lo16(v), nmb), fadd(hi16(v), nmb));
  return pack2(fmul(lo16(c), rd), fmul(hi16(c), rd));
}
template <int N, int M> DI f32x4 sum4(const u32x4 (&v)[M]) {
  f32x4 t;
#pragma unroll
  for (int e = 0; e < 4; ++e) {
    t[e] = U2F(v[0][e]);
#pragma unroll
    for (int i = 1; i < N; ++i) t[e] = fadd(t[e], U2F(v[i][e]));
  }
  return t;
}
#define PERM_LO(x, y) __builtin_amdgcn_perm(y, x, 0x05040100u)
#define PERM_HI(x, y) __builtin_amdgcn_perm(y, x, 0x07060302u)
constexpr unsigned SPIN_CAP = 1u << 15;
DI unsigned nz(unsigned v) { return min(v, SENT - 1u); }
DI unsigned nzf(float v) { return nz(__float_as_uint(v)); }
DI unsigned mx4(unsigned m, const u32x4& v) { return max(max(max(m, v[0]), v[1]), max(v[2], v[3])); }
template <int N> DI bool none_sent(const u32x4 (&v)[N]) {
  unsigned m = 0u;
#pragma unroll
  for (int i = 0; i < N; ++i) m = mx4(m, v[i]);
  return m != SENT;
}
typedef __amdgpu_buffer_rsrc_t rsrc_t;
DI u32x4 ldg(rsrc_t rs, unsigned off) { return __builtin_amdgcn_raw_buffer_load_b128(rs, (int)off, 0, 16); }
DI void stg(rsrc_t rs, unsigned off, u32x4 v) { __builtin_amdgcn_raw_buffer_store_b128(v, rs, (int)off, 0, 16); }
DI void poll_pause(bool slow) { if (slow) __builtin_amdgcn_s_sleep(4); asm volatile("s_sleep 1" ::: "memory"); }
template <int N, class LD, class OKF> DI void poll_all(u32x4 (&out)[N], bool slow, LD ld, OKF okf) {
  for (unsigned spins = 0;; ++spins) {
    ld(out);
    SBAR();
    if (__all(okf(out)) || spins > SPIN_CAP) break;
    poll_pause(slow);
  }
}
DI void lds_set(unsigned addr, unsigned v) { asm volatile("s_waitcnt lgkmcnt(0)\n\tds_write_b32 %0, %1" :: "v"(addr), "v"(v) : "memory"); }
template <int SLEEP = 1> DI void lds_wait_ge(unsigned addr, unsigned need) {
  unsigned spin = 0x100000, v;
  asm volatile("1:\n\tds_read_b32 %0, %2\n\ts_sub_u32 %1, %1, 1\n\ts_waitcnt lgkmcnt(0)\n\ts_cbranch_scc1 2f\n\tv_cmp_gt_u32 vcc, %3, %0\n\ts_cbranch_vccz 2f\n\ts_sleep %4\n\ts_branch 1b\n2:"
               : "=&v"(v), "+s"(spin) : "v"(addr), "s"(need), "n"(SLEEP) : "vcc", "scc", "memory");
}
DI s16x8 tr2(const char* p, int second) {
  s16x4 lo = __builtin_amdgcn_ds_read_tr16_b64_v4i16(LDS_PTR(s16x4, p)), hi = __builtin_amdgcn_ds_read_tr16_b64_v4i16(LDS_PTR(s16x4, p + second));
  return __builtin_shufflevector(lo, hi, 0, 1, 2, 3, 4, 5, 6, 7);
}
DI u32x16 cat4(const u32x4& a, const u32x4& b, const u32x4& c, const u32x4& d) {
  const u32x8 x = __builtin_shufflevector(a, b, 0, 1, 2, 3, 4, 5, 6, 7), y = __builtin_shufflevector(c, d, 0, 1, 2, 3, 4, 5, 6, 7);
  return __builtin_shufflevector(x, y, 0, 1, 2, 3, 4, 5, 6, 7, 8, 9, 10, 11, 12, 13, 14, 15);
}

DI unsigned stats_dst(unsigned sx, int tile, int tn, int lane) { return lane < 32 ? sx + SX_ST + tile * 8192 + tn * 512 + lane * 16 : OOB; }
DI void row_stats_sum(rsrc_t rs, unsigned sx, int tile, int lane, float& S, float& Q, float& Dd) {
  const unsigned exb = sx + SX_ST + tile * 8192 + (lane & 31) * 16;
  u32x4 pv[8];
  poll_all<8>(pv, false, [&](u32x4 (&v)[8]) {
#pragma unroll
    for (int i = 0; i < 8; ++i) v[i] = ldg(rs, exb + ((lane >> 5) * 8 + i) * 512);
  }, [&](const u32x4 (&v)[8]) { return none_sent(v); });
  S = 0.f; Q = 0.f; Dd = 0.f;
#pragma unroll
  for (int i = 0; i < 8; ++i) { S += __uint_as_float(pv[i][0]); Q += __uint_as_float(pv[i][1]); Dd += __uint_as_float(pv[i][2]); }
}
DI void row_stats_fin(rsrc_t rs, unsigned kdst, char* smem, int epoch, int tile, int lane, float S, float Q, float Dd, float wsum, const u32x4 (&x1p)[4],
                      float& mean, float& var, float& mb, float& rd) {
  S = half_swap_sum(S); Q = half_swap_sum(Q); Dd = half_swap_sum(Dd);
  mean = S * (1.0f / 1024.0f); var = fmaxf(Q * (1.0f / 1024.0f) - mean * mean, 0.f);
  float den;
  ln_scalars(mean, var, mb, den);
  rd = RCP(den);
  const unsigned rb = pack2((Dd - mb * wsum) * rd, 0.f) & 0xffffu;
  const unsigned mine = (((rb & 0x8000u) ? (~rb & 0xffffu) : (rb | 0x8000u)) << 16) | (unsigned)(2047 - (tile * 32 + (lane & 31)));
  const bool best = half_max_u32(mine) == mine;
  __builtin_amdgcn_raw_buffer_store_b32(mine, rs, (best && lane < 32) ? kdst : OOB, 0, 16);
  if (best) {
    char* c = smem + L_CAND + (tile & 3) * 144;
#pragma unroll
    for (int i = 0; i < 4; ++i) *reinterpret_cast<u32x4*>(c + (2 * i + (lane >> 5)) * 16) = x1p[i];
    *reinterpret_cast<u32x4*>(c + 128) = u32x4{mine, __float_as_uint(mb), __float_as_uint(rd), 0u};
  }
  lds_set((unsigned)(uintptr_t)smem + L_W + 32 + (tile & 3) * 4, epoch);
}

DI void attn_safe(rsrc_t rs, unsigned kvoff, const u32x8& q, const u32x4& ones, f32x16& o) {
  const s16x8 q0 = __builtin_bit_cast(s16x8, __builtin_shufflevector(q, q, 0, 1, 2, 3)), q1 = __builtin_bit_cast(s16x8, __builtin_shufflevector(q, q, 4, 5, 6, 7));
  f32x4 l = {0.f, 0.f, 0.f, 0.f};
#pragma unroll
  for (int e = 0; e < 16; ++e) o[e] = 0.f;
  float m = -INFINITY;
#pragma unroll 1
  for (int kt = 0; kt < 64; ++kt) {
    const unsigned p = kvoff + kt * 4096;
    const u32x4 k0 = ldg(rs, p), k1 = ldg(rs, p + 1024), v0 = ldg(rs, p + 2048), v1 = ldg(rs, p + 3072);
    f32x16 s = {};
    s = MFMA32(__builtin_bit_cast(s16x8, k0), q0, s);
    s = MFMA32(__builtin_bit_cast(s16x8, k1), q1, s);
    float mt = fmaxf(s[0], s[1]);
#pragma unroll
    for (int e = 2; e < 16; ++e) mt = fmaxf(mt, s[e]);
    const float mn = fmaxf(m, half_swap_max(mt));
    const float alpha = EX2(m - mn);
    m = mn;
#pragma unroll
    for (int e = 0; e < 16; ++e) { s[e] = EX2(s[e] - mn); o[e] *= alpha; }
    l[0] *= alpha;
    const bf16x16 pa = __builtin_bit_cast(bf16x16, __builtin_shufflevector(s, s, 0, 1, 2, 3, 4, 5, 6, 7));
    const bf16x16 pb = __builtin_bit_cast(bf16x16, __builtin_shufflevector(s, s, 8, 9, 10, 11, 12, 13, 14, 15));
    o = __builtin_amdgcn_smfmac_f32_32x32x32_bf16(__builtin_bit_cast(bf16x8, v0), pa, o, 0xdddd, 0, 0);
    l = __builtin_amdgcn_smfmac_f32_16x16x64_bf16(__builtin_bit_cast(bf16x8, ones), pa, l, 0xdddd, 0, 0);
    o = __builtin_amdgcn_smfmac_f32_32x32x32_bf16(__builtin_bit_cast(bf16x8, v1), pb, o, 0xdddd, 0, 0);
    l = __builtin_amdgcn_smfmac_f32_16x16x64_bf16(__builtin_bit_cast(bf16x8, ones), pb, l, 0xdddd, 0, 0);
  }
  o *= 1.0f / l[0];
}

extern "C" __global__ __launch_bounds__(512) void mega_kernel(const u16* __restrict__ x0, const u16* __restrict__ wqkv, const u16* __restrict__ wo,
                                                              const u16* __restrict__ w_r, const u16* __restrict__ wg, const u16* __restrict__ wu,
                                                              const u16* __restrict__ wd, char* ws, u16* __restrict__ out) {
  __shared__ __attribute__((aligned(16))) char smem[LDS_BYTES];
  const int wid = __builtin_amdgcn_readfirstlane(threadIdx.x >> 6);
  STAMP_INIT;
  STAMPV(0);
  const int bid = blockIdx.x, xcd = bid & 7, loc = bid >> 3;
  const int tm = 8 * (xcd >> 2) + (loc >> 2), tn = 4 * (xcd & 3) + (loc & 3);
  const unsigned lbase = (unsigned)(uintptr_t)smem;
  const rsrc_t rs = __builtin_amdgcn_make_buffer_rsrc(ws, 0, (int)WS_BYTES, 0x00020000);
  volatile __attribute__((address_space(3))) unsigned* words = LDS_PTR(volatile unsigned, smem + L_W);
  u32x4 v[4];
  if (wid < 4)
    asm volatile("global_load_dwordx4 %0, %4, %5\n\tglobal_load_dwordx4 %1, %4, %5 offset:512\n\tglobal_load_dwordx4 %2, %4, %5 offset:1024\n\tglobal_load_dwordx4 %3, %4, %5 offset:1536"
                 : "=&v"(v[0]), "=&v"(v[1]), "=&v"(v[2]), "=&v"(v[3])
                 : "v"((threadIdx.x >> 5) * (D_ * 2) + (threadIdx.x & 31) * 16), "s"(x0 + (size_t)(tm * 128 + tn * 8) * D_) : "memory");
  else {
    asm volatile("" : "=v"(v[0]), "=v"(v[1]), "=v"(v[2]), "=v"(v[3]));
    if (wid == 6) {
      const char *c0, *c1;
      CODE_ADDR(mega_kernel, c0); CODE_ADDR(end_marker, c1);
      const unsigned o = (loc * 64 + (threadIdx.x & 63)) * 32;
      glds4(c0, (long long)o + 4 <= c1 - c0 ? o : 0u, lbase + NS * SLOTB);
    }
  }
  STAMPV(48);
  *reinterpret_cast<unsigned*>(smem + L_FULL + threadIdx.x * 4) = 0u;
  words[threadIdx.x & 31] = 0u;
  BAR_LGKM();
  if (wid < 4) {
    const int tid = threadIdx.x, lane = tid & 63, r32 = lane & 31, row = 2 * wid + (lane >> 5);
    char* sm = smem + L_XCH;
    unsigned dst = OFF_HF + (tm * 4 + (tn >> 2)) * TS + (tn & 3) * 128 + (tid >> 3) * 512 + (tid & 7) * 16;
    asm volatile("s_waitcnt vmcnt(0)" : "+v"(v[0]), "+v"(v[1]), "+v"(v[2]), "+v"(v[3]), "+v"(dst) :: "memory");
    STAMPV(49);
    float s = 0.f, s2 = 0.f;
#pragma unroll
    for (int i = 0; i < 4; ++i)
#pragma unroll
      for (int e = 0; e < 4; e += 2) { s = dot2(v[i][e], 0x3f803f80u, s); s2 = dot2(v[i][e + 1], 0x3f803f80u, s2); }
    const float mean = half_sum(s + s2) * (1.0f / 1024.0f);
    f32x2 m2 = {0.f, 0.f};
#pragma unroll
    for (int i = 0; i < 4; ++i)
#pragma unroll
      for (int e = 0; e < 4; ++e) { const f32x2 d = f32x2{lo16(v[i][e]), hi16(v[i][e])} - mean; m2 += d * d; }
    float mb, den;
    ln_scalars(mean, half_sum(m2[0] + m2[1]) * (1.0f / 1024.0f), mb, den);
    const float rd = 1.0f / den;
#pragma unroll
    for (int i = 0; i < 4; ++i) {
      char* p = sm + ((16 * i + (r32 >> 1)) * 2) * 144 + row * 16 + (r32 & 1) * 8;
      *reinterpret_cast<u32x2*>(p) = u32x2{ln_pair(v[i][0], mb, rd), ln_pair(v[i][1], mb, rd)};
      *reinterpret_cast<u32x2*>(p + 144) = u32x2{ln_pair(v[i][2], mb, rd), ln_pair(v[i][3], mb, rd)};
    }
    STAMPV(37);
    lds_set(lbase + L_W + 64 + 4 * wid, 1);
    lds_wait_ge<0>(lbase + L_W + 64 + 4 * (lane & 3), 1);
    STAMPV(50);
    u32x4 t[4];
#pragma unroll
    for (int i = 0; i < 4; ++i) t[i] = *reinterpret_cast<const u32x4*>(sm + (i * 32 + (tid >> 3)) * 144 + (tid & 7) * 16);
    SBAR();
#pragma unroll
    for (int i = 0; i < 4; ++i) stg(rs, dst + i * 16384, t[i]);
    STAMPV(51);
    DRAIN();
    lds_set(lbase + L_W + 64 + 4 * wid, 2);
    STAMPV(38);
  }
  const int lw = wid & 1;
  const char* wb = reinterpret_cast<const char*>(wqkv + (size_t)(16 * lw) * (3 * D_) + tn * 64);
  auto stage_lanes = [](unsigned (&wv)[3]) {
    const int lane = fresh_lane();
#pragma unroll
    for (int i = 0; i < 3; ++i) {
      const int li = lane + 64 * i, k = li / 24, cp = li % 24, c = cp ^ (4 * ((k >> 1) & 1));
      wv[i] = (unsigned)(k * (3 * D_ * 2) + (c >> 3) * (D_ * 2) + (c & 7) * 16 - i * 1024);
    }
  };
  auto stage = [&](const unsigned (&wv)[3], int s, int slot) {
    const char* p = wb + (size_t)((s + 2 * tn) & 31) * (32 * 3 * D_ * 2);
    glds_v3(p, wv, lbase + slot * SLOTB + lw * 6144);
    glds_v3(p + 8 * (3 * D_ * 2), wv, lbase + slot * SLOTB + lw * 6144 + 3072);
  };
  auto exchange = [&](const u32x2& tag, unsigned line0, int me, bool bar = false) {
    const int lane = fresh_lane();
    __builtin_amdgcn_raw_buffer_store_b64(tag, rs, lane == 0 ? line0 + me * 128 : OOB, 0, 16);
    if (bar) BARRIER();
    if (1) {
      unsigned po = lane < 16 ? line0 + lane * 128 : OOB;
      const u32x4 srd = {(unsigned)(uintptr_t)ws, (unsigned)((uintptr_t)ws >> 32) & 0xffffu, WS_BYTES, 0x00020000u};
      for (unsigned spins = 0;; ++spins) {
        u32x2 v = {0u, 0u};
        asm volatile("buffer_atomic_or_x2 %0, %1, %2, 0 offen sc0 sc1\n\ts_waitcnt vmcnt(0)" : "+v"(v) : "v"(po), "s"(srd) : "memory");
        po = ((v[0] == tag[0]) & (v[1] == tag[1])) ? OOB : po;
        if (__all(po == OOB) || spins > SPIN_CAP) break;
      }
      return;
    }
    __builtin_amdgcn_s_sleep(8);
    for (unsigned spins = 0;; ++spins) {
      const u32x2 v = __builtin_amdgcn_raw_buffer_load_b64(rs, line0 + (lane & 15) * 128, 0, 16);
      if (__all((v[0] == tag[0]) & (v[1] == tag[1])) || spins > SPIN_CAP) break;
      asm volatile("s_sleep 1" ::: "memory");
    }
  };
  if ((wid >> 1) == 2) {
    unsigned wv[3];
    stage_lanes(wv);
    __builtin_amdgcn_s_sleep(0);
    const int r = tm & 7;
#pragma unroll 1
    for (int i = 0; i < NS; ++i) {
      const int s = i == 0 ? r : i <= r ? i - 1 : i;
      stage(wv, s, s);
      const int j = i - 1, full = j < 1 ? (j == 0 && r == 0) : j < r ? j : j + 1;
      asm volatile("s_waitcnt vmcnt(%2)\n\tds_write_b32 %0, %1" :: "v"(lbase + L_FULL + lw * 256), "v"(full), "n"(6 * 1) : "memory");
    }
    asm volatile("s_waitcnt vmcnt(0)\n\tds_write_b32 %0, %1" :: "v"(lbase + L_FULL + lw * 256), "v"(NS) : "memory");
    STAMPV(1);
  }
  if (wid == 7) {
    u32x2 tag = __builtin_amdgcn_raw_buffer_load_b64(rs, FL_EPOCH, 0, 16);
    tag[0] += 1u; tag[1] += 0x9E3779B9u;
    *reinterpret_cast<u32x2*>(smem + L_TAG) = tag;
    lds_wait_ge<0>(lbase + L_W + 64 + 4 * (fresh_lane() & 3), 2);
    exchange(tag, FL_X + tm * 2048, tn);
    lds_set(lbase + L_W + 24, 1);
  }
  if (wid >= 4) {
    auto wo_rows = [&](int g0, int n, int gs) {
      const int lane = fresh_lane();
      unsigned wov[4];
#pragma unroll
      for (int i = 0; i < 4; ++i) wov[i] = (unsigned)((8 * i + (lane >> 3)) * (D_ * 2) + ((lane & 7) ^ (4 * ((lane >> 4) & 1))) * 16 - i * 1024);
#pragma unroll 1
      for (int i = 0, k = (2 * (tm & 7)) % n, g; i < n && !0; ++i, k = k + 1 == n ? 0 : k + 1) g = g0 + gs * k, glds_v4t<3, 1>(reinterpret_cast<const char*>(wo) + (size_t)((((tn >> 2) + 1 + (g >> 3)) & 3) * 256 + ((0 * 64 * tn + 32 * g) & 255)) * (D_ * 2) + tn * 128, wov, lbase + g * 4096);
    };
    if (wid < 6) {
      const int lane = fresh_lane();
      unsigned wv[3];
      stage_lanes(wv);
      __builtin_amdgcn_s_setprio(3);
      const unsigned fra = lbase + L_FREE + (lane & 3) * 256, fla = lbase + L_FULL + lw * 256 + lane * 4;
      const rsrc_t rw = __builtin_amdgcn_make_buffer_rsrc(const_cast<char*>(wb), 0, 0 ? 0 : 6 * MBY - (16 * lw * 3 * D_ + tn * 64) * 2, 0x00020000);
#pragma unroll 1
      for (int L = 0; L < 2; ++L) {
        const int s0 = NS, lead = L ? 8 : NS - 1;
        int slot = 0;
#pragma unroll 1
        for (int s = NS; s < 32; ++s) {
          lds_wait_ge(fra, 32 * L + s - lead);
          stage(wv, s, slot);
          slot = (slot == NS - 1) ? 0 : slot + 1;
          if (s >= s0 + DD) asm volatile("s_waitcnt vmcnt(%0)\n\tds_write_b32 %1, %2" :: "n"(DD * 6), "v"(fla), "v"(32 * L + s - DD + 1) : "memory");
        }
#pragma unroll
        for (int d = DD - 1; d >= 0; --d) asm volatile("s_waitcnt vmcnt(%0)\n\tds_write_b32 %1, %2" :: "n"(d * 6), "v"(fla), "v"(32 * L + 32 - d) : "memory");
        u32x2 tag = *reinterpret_cast<const u32x2*>(smem + L_TAG);
        PIN(tag);
        if (lw == 0) {
          lds_wait_ge(lbase + L_W + 96 + 4 * (lane & 1), 1);
          lds_wait_ge<0>(lbase + L_W + 80 + 4 * (lane & 3), 2 * L + 1);
          exchange(tag, FL_KV + (L * 16 + tn) * 2048, tm, true);
          lds_wait_ge(fra, 32 * L + 32);
          if (0) { const unsigned long long t0 = __builtin_amdgcn_s_memrealtime(); while (__builtin_amdgcn_s_memrealtime() < t0 + 0) __builtin_amdgcn_s_sleep(2); }
          lds_set(lbase + L_W + 16, L + 1);
        } else { BAR_LGKM(); lds_wait_ge<8>(lbase + L_W + 16, L + 1); }
        __builtin_amdgcn_s_sleep(24);
        wo_rows(lw, 15, 2);
        u32x4 pre[NS * 6];
        if (L == 0) {
#pragma unroll
          for (int s = 0; s < NS; ++s) {
#pragma unroll
            for (int i = 0; i < 6; ++i)
              pre[s * 6 + i] = __builtin_amdgcn_raw_buffer_load_b128(rw, wv[i % 3], ((s + 2 * tn) & 31) * (32 * 3 * D_ * 2) + (i / 3) * (8 * 3 * D_ * 2) + (i % 3) * 1024, 0), PACE();
          }
        } else {
#pragma unroll
          for (int i = 0; i < NS * 6; ++i) asm volatile("" : "=v"(pre[i]));
        }
        DRAIN();
        const unsigned fo = FL_O + (L * 16 + tm) * 2048;
        if (lw) {
          lds_set(lbase + L_W + 112, 2 * L + 1);
          lds_wait_ge<4>(lbase + L_W + 48 + 4 * (lane & 3), L + 1);
          lds_wait_ge<0>(lbase + L_W + 80 + 4 * (lane & 3), 2 * L + 2);
          __builtin_amdgcn_raw_buffer_store_b64(tag, rs, lane == 0 ? fo + tn * 128 : OOB, 0, 16);
          wo_rows(30, 2, 1);
          DRAIN();
          lds_set(lbase + L_W + 112, 2 * L + 2);
        } else {
          lds_wait_ge<4>(lbase + L_W + 112, 2 * L + 1);
          lds_wait_ge<4>(lbase + L_W + 48 + 4 * (lane & 3), L + 1);
          unsigned po = lane < 16 ? fo + ((lane + 4 + (tn & 12)) & 15) * 128 : OOB;
          const u32x4 srd = {(unsigned)(uintptr_t)ws, (unsigned)((uintptr_t)ws >> 32) & 0xffffu, WS_BYTES, 0x00020000u};
          for (unsigned spins = 0;; ++spins) {
            u32x2 v = {0u, 0u};
            asm volatile("buffer_atomic_or_x2 %0, %1, %2, 0 offen sc0 sc1\n\ts_waitcnt vmcnt(0)" : "+v"(v) : "v"(po), "s"(srd) : "memory");
            po = ((v[0] == tag[0]) & (v[1] == tag[1])) ? OOB : po;
            unsigned m = (unsigned)__ballot(po == OOB);
            m &= m >> 1; m &= m >> 2;
            m = (m & 1u) | ((m >> 3) & 2u) | ((m >> 6) & 4u) | ((m >> 9) & 8u);
            if (m == 15u || spins > SPIN_CAP) break;
            words[5 + 2 * L] = words[28] == 2u * L + 2u ? m : m & 7u;
          }
          words[5 + 2 * L] = words[28] == 2u * L + 2u ? 15u : 7u;
          lds_wait_ge<0>(lbase + L_W + 112, 2 * L + 2);
          words[5 + 2 * L] = 15u;
          __builtin_amdgcn_raw_buffer_store_b64(tag, rs, (bid == 0 && lane == 0) ? FL_EPOCH : OOB, 0, 16);
        }
        BAR_ALL();
        if (L == 0) {
          char* d = smem + lw * 6144 + fresh_lane() * 16;
#pragma unroll
          for (int s = 0; s < NS; ++s) {
#pragma unroll
            for (int i = 0; i < 6; ++i) *reinterpret_cast<u32x4*>(d + s * SLOTB + i * 1024) = pre[s * 6 + i];
            lds_set(fla, 32 + s + 1);
          }
          STAMPV(2);
          lds_wait_ge<2>(lbase + L_W + 24, 2);
        }
      }
      STAMP_END;
      return;
    }
    const int a = wid - 6;
    const bool ffn = loc < 28;
    const int grp = xcd * 7 + (ffn ? loc >> 2 : 0), qa = (loc & 3) * 2 + a;
    const int kr = loc, cg = xcd;
    const unsigned wa = lbase + L_W;
    lds_wait_ge<2>(wa + 24, 1);
    __builtin_amdgcn_s_sleep(16);
    auto transpose8 = [](u32x4* v) {
      u32x4 t[8];
#pragma unroll
      for (int j = 0; j < 8; ++j)
#pragma unroll
        for (int e = 0; e < 4; ++e) t[j][e] = (j & 1) ? PERM_HI(v[2 * e][j >> 1], v[2 * e + 1][j >> 1]) : PERM_LO(v[2 * e][j >> 1], v[2 * e + 1][j >> 1]);
#pragma unroll
      for (int j = 0; j < 8; ++j) { v[j] = t[j]; PIN(v[j]); }
    };
    u32x4 gf[32];
    u32x4 wdf[16];
    auto wait_go = [&](int L) {
      lds_wait_ge<8>(lbase + L_W + 16, L + 1);
      __builtin_amdgcn_s_sleep(24);
    };
    auto load_wd = [&](unsigned dv) {
      const rsrc_t rd = __builtin_amdgcn_make_buffer_rsrc(const_cast<u16*>(wd) + (size_t)grp * 64 * D_ + 128 * qa, 0, 0 ? 0 : 64 * D_ * 2 - 256 * qa, 0x00020000);
#pragma unroll
      for (int i = 0; i < 16; ++i) {
        wdf[i] = __builtin_amdgcn_raw_buffer_load_b128(rd, dv, (32 * (i >> 3) + (i & 7)) * (D_ * 2), 0);
        asm volatile("s_sleep 2\n\ts_waitcnt vmcnt(3)" ::: "memory");
      }
    };
    {
      LANE_VARS;
      const u32x4 sv = {SENT, SENT, SENT, SENT};
#pragma unroll
      for (int i = 0; i < 6; ++i) {
        const unsigned o = ((i * 256 + bid) * 128 + a * 64 + lane) * 16;
        stg(rs, o < SX_BYTES ? OFF_SX + o : OOB, sv);
      }
      if (a == 0) {
        const u32x4 w0 = *reinterpret_cast<const u32x4*>(w_r + lane * 8), w1 = *reinterpret_cast<const u32x4*>(w_r + 512 + lane * 8);
        float t = 0.f;
#pragma unroll
        for (int e = 0; e < 4; ++e) t = fadd(fadd(t, fadd(lo16(w0[e]), hi16(w0[e]))), fadd(lo16(w1[e]), hi16(w1[e])));
        words[0] = __float_as_uint(wave_sum(t));
        if ((lane >> 3) == (tn & 7)) *reinterpret_cast<u32x4*>(smem + L_WR + (lane & 7) * 16) = tn < 8 ? w0 : w1;
      }
      const u16* p = wqkv + (size_t)(64 * tm + 32 * a + hi) * (3 * D_) + (c24 >> 3) * D_ + tn * 64 + (c24 & 7) * 8;
      float cs[8] = {0.f, 0.f, 0.f, 0.f, 0.f, 0.f, 0.f, 0.f};
      u32x4 v[16];
#pragma unroll
      for (int i = 0; i < 16; ++i) v[i] = *reinterpret_cast<const u32x4*>(p + (size_t)i * (2 * 3 * D_));
      SBAR();
#pragma unroll
      for (int i = 0; i < 16; ++i)
#pragma unroll
        for (int e = 0; e < 4; ++e) { cs[2 * e] = fadd(cs[2 * e], lo16(v[i][e])); cs[2 * e + 1] = fadd(cs[2 * e + 1], hi16(v[i][e])); }
      u32x4 o;
#pragma unroll
      for (int e = 0; e < 4; ++e) { const float x = half_swap_sum(cs[e]), y = half_swap_sum(cs[4 + e]); o[e] = __float_as_uint(hi ? y : x); }
      stg(rs, r32 < 24 ? OFF_CS + (tn * 32 + tm * 2 + a) * 768 + r32 * 32 + hi * 16 : OOB, o);
    }
    {
      DRAIN();
      lds_set(wa + 96 + 4 * a, 1);
      BAR_LGKM();
      LANE_VARS;
      const char* gp = reinterpret_cast<const char*>(((lane & 8) ? wu : wg) + (size_t)(128 * qa + 16 * hi + 4 * tblk) * F_ + 64 * grp + (lane & 7) * 8);
      unsigned dv = (lane >> 4) * (8 * D_ * 2) + (lane & 15) * 16;
      PIN(gp); PIN(dv);
      unsigned sv[3];
#pragma unroll
      for (int i = 0; i < 3; ++i) { const int li = lane + 64 * i, k = li / 24, c = li % 24; sv[i] = (unsigned)(k * (3 * D_ * 2) + (c >> 3) * (D_ * 2) + (c & 7) * 16 - i * 1024); PIN(sv[i]); }
      wait_go(0);
#pragma unroll
      for (int i = 0; i < 32; ++i) {
        gf[i] = *reinterpret_cast<const u32x4*>(gp + (size_t)(0 ? 0 : 32 * (i >> 3) + 8 * ((i >> 2) & 1) + (i & 3)) * (F_ * 2));
        asm volatile("s_sleep 2\n\ts_waitcnt vmcnt(3)" ::: "memory");
      }
      load_wd(dv);
      STAMPV(7);
      lds_wait_ge<4>(wa + 80 + 4 * (lane & 3), 2);
#pragma unroll
      for (int g = 0; g < 4; ++g) glds_v3(wqkv + (size_t)(32 * kr + 8 * g) * (3 * D_) + (2 * cg + a) * 64, sv, lbase + L_SL + a * 12288 + g * 3072);
      transpose8(gf); transpose8(gf + 8); transpose8(gf + 16); transpose8(gf + 24);
      transpose8(wdf); transpose8(wdf + 8);
      {
        LANE_VARS;
        f32x4 t[2];
#pragma unroll
        for (int c = 0; c < 2; ++c) {
          u32x4 v[8];
#pragma unroll
          for (int i = 0; i < 8; ++i) v[i] = ldg(rs, OFF_CS + (tn * 32 + 16 * c + 2 * i + hi) * 768 + a * 384 + c24 * 16);
          SBAR();
          t[c] = sum4<8>(v);
          SBAR();
        }
#pragma unroll
        for (int e = 0; e < 4; ++e) t[0][e] = half_swap_sum(fadd(t[0][e], t[1][e]));
        if (lane < 24) *reinterpret_cast<f32x4*>(smem + L_CS + a * 384 + lane * 16) = t[0];
      }
      u32x2 tag = *reinterpret_cast<const u32x2*>(smem + L_TAG);
      PIN(tag);
      BAR_ALL();
      if (a) {
        lds_wait_ge<0>(wa + 64 + 4 * (lane & 3), 3);
        exchange(tag, FL_X + (16 + tm) * 2048, tn);
        lds_set(wa + 24, 2);
        STAMPV(12);
      }
    }
#pragma unroll 1
    for (int L = fresh_s(0);; ++L) {
      const unsigned sx = OFF_SX + L * SX_LAYER;
      STAMPV(1 + 16 * L);
      if (0 && L == 0) { words[3] = 5000u; lds_set(wa + 4 + 4 * a, 1); } else {
      u32x4 hf[4];
      unsigned key;
      { LANE_VARS;
      lds_wait_ge<0>(wa + 32 + 4 * tp, L + 1);
      if (a == 0) {
        const char* cp = smem + L_CAND + (r32 >> 3) * 144;
        const u32x4 c = *reinterpret_cast<const u32x4*>(cp + (lane & 7) * 16), rec = *reinterpret_cast<const u32x4*>(cp + 128);
        u32x4 o;
#pragma unroll
        for (int e = 0; e < 4; ++e) { unsigned h = ln_pair_s(c[e], -U2F(rec[1]), U2F(rec[2])); PIN(h); o[e] = nz(hi ? c[e] : h); }
        stg(rs, sx + SX_ROW + hi * 131072 + (tm * 4 + (r32 >> 3)) * 2048 + tn * 128 + (lane & 7) * 16, o);
        STAMPV(8 + 16 * L);
        lds_wait_ge<0>(wa + 104, L + 1);
        key = __builtin_amdgcn_readfirstlane(words[27]);
      } else {
        const u32x4 srd = {(unsigned)(uintptr_t)ws, (unsigned)((uintptr_t)ws >> 32) & 0xffffu, WS_BYTES, 0x00020000u};
        unsigned po = sx + SX_CAND + (tn * 16 + (lane >> 2)) * 128 + tp * 4;
        key = 0u;
        for (unsigned spins = 0;; ++spins) {
          unsigned v = 0u;
          asm volatile("buffer_atomic_or %0, %1, %2, 0 offen sc0 sc1\n\ts_waitcnt vmcnt(0)" : "+v"(v) : "v"(po), "s"(srd) : "memory");
          key = v == SENT ? key : max(key, v);
          po = v == SENT ? po : OOB;
          if (__all(po == OOB) || spins > SPIN_CAP) break;
        }
        key = __builtin_amdgcn_readfirstlane(wave_max_u32(key));
        words[27] = key;
        words[3] = 2047u - (key & 2047u);
        lds_set(wa + 104, L + 1);
        STAMPV(13 + 16 * L);
      }
      poll_all<4>(hf, false, [&](u32x4 (&t)[4]) {
#pragma unroll
        for (int i = 0; i < 4; ++i) t[i] = ldg(rs, sx + SX_ROW + ((2047 - (int)(key & 2047u)) >> 5) * 2048 + (16 * qa + 4 * i + (lane >> 4)) * 16);
      }, [&](const u32x4 (&t)[4]) { return none_sent(t); });
      if (L == 1) lds_set(wa + 4 + 4 * a, 2);
      }
      STAMPV(2 + 16 * L);
      f32x4 d[8];
      const f32x4 zero = {0.f, 0.f, 0.f, 0.f};
      SBAR();
      __builtin_amdgcn_s_setprio(3);
#pragma unroll
      for (int t = 0; t < 32; ++t) d[t & 7] = MFMA16(__builtin_bit_cast(s16x8, hf[t >> 3]), __builtin_bit_cast(s16x8, gf[t]), t < 8 ? zero : d[t & 7]);
      SBAR();
      __builtin_amdgcn_s_setprio(0);
      LANE_VARS;
      const int idx = 2047 - (int)(key & 2047u), win = idx >> 7;
      const bool owner = win == tm;
      auto pick2 = [&](const f32x4 (&d)[8]) {
        u32x2 o;
#pragma unroll
        for (int e = 0; e < 2; ++e) {
          const float x = tblk ? d[2 + e][0] : d[e][0], y = tblk ? d[6 + e][0] : d[4 + e][0];
          o[e] = nzf(hi ? y : x);
        }
        return o;
      };
      if (ffn) {
        const unsigned ppos = (lane & 15) * 32 + (lane >> 4) * 8;
        __builtin_amdgcn_raw_buffer_store_b64(pick2(d), rs, sx + SX_GP + (grp * 8 + qa) * 512 + ppos, 0, 16);
        STAMPV(9 + 16 * L);
        __builtin_amdgcn_s_sleep(13);
        if (L == 0) __builtin_amdgcn_s_sleep(0);
        u32x2 gu[8];
        for (unsigned spins = 0;; ++spins) {
#pragma unroll
          for (int i = 0; i < 8; ++i) gu[i] = __builtin_amdgcn_raw_buffer_load_b64(rs, sx + SX_GP + (grp * 8 + i) * 512 + lane * 8, 0, 16);
          SBAR();
          unsigned m = 0u;
#pragma unroll
          for (int i = 0; i < 8; ++i) m = max(max(m, gu[i][0]), gu[i][1]);
          if (__all(m != SENT) || spins > SPIN_CAP) break;
          poll_pause(L == 0);
        }
        STAMPV(10 + 16 * L);
        float t0 = opq(U2F(gu[0][0])), t1 = opq(U2F(gu[0][1]));
#pragma unroll
        for (int i = 1; i < 8; ++i) { t0 = fadd(t0, U2F(gu[i][0])); t1 = fadd(t1, U2F(gu[i][1])); }
        auto sw = __builtin_amdgcn_permlane32_swap(__float_as_uint(t0), __float_as_uint(t1), false, false);
        const float gv = rbf(U2F(sw[0])), uv = rbf(U2F(sw[1]));
        reinterpret_cast<u16*>(smem + L_GU + a * 128)[2 * r32 + hi] = (u16)pack2(fmul(rbf(fmul(gv, RCP(fadd(1.0f, __expf(-gv))))), uv), 0.f);
        asm volatile("s_waitcnt lgkmcnt(0)" ::: "memory");
        const s16x8 a0 = *reinterpret_cast<const s16x8*>(smem + L_GU + a * 128 + (lane >> 4) * 16), a1 = *reinterpret_cast<const s16x8*>(smem + L_GU + a * 128 + 64 + (lane >> 4) * 16);
        SBAR();
        __builtin_amdgcn_s_setprio(3);
#pragma unroll
        for (int t = 0; t < 16; ++t) d[t & 7] = MFMA16(t < 8 ? a0 : a1, __builtin_bit_cast(s16x8, wdf[t]), t < 8 ? zero : d[t & 7]);
        SBAR();
        __builtin_amdgcn_s_setprio(0);
        __builtin_amdgcn_raw_buffer_store_b64(pick2(d), rs, sx + SX_YP + grp * 4096 + qa * 512 + ppos, 0, 16);
      }
      STAMPV(3 + 16 * L);
      if (L == 1 && (cg != (kr & 7) || a)) { STAMP_END; __builtin_amdgcn_endpgm(); }
      s16x8 wpf[12];
#pragma unroll
      for (int t = 0; t < 12; ++t) wpf[t] = tr2(smem + L_SL + a * 12288 + (16 * hi + 4 * tblk + tq) * 384 + t * 32 + tp * 8, 8 * 384);
#pragma unroll
      for (int t = 0; t < 12; ++t) PIN(wpf[t]);
      if (L == 0 && !owner) lds_set(wa + 4 + 4 * a, 1);
      __builtin_amdgcn_s_sleep(13 - 4);
      if (L == 0) __builtin_amdgcn_s_sleep(0);
      if (L == 0 && !ffn) __builtin_amdgcn_s_sleep(64);
      const unsigned rk = key >> 16;
      float rsig = rbf(RCP(fadd(1.0f, __expf(-lo16((rk & 0x8000u) ? (rk & 0x7fffu) : (~rk & 0xffffu))))));
      PIN(rsig);
      const int n = 4 * (lane & 7) + ((lane >> 3) & 3);
      const int fo = ((n >> 4) * 2 + ((n >> 2) & 1)) * 8 + ((n >> 3) & 1) * 4 + (n & 3);
      u32x4 pv[8];
      poll_all<8>(pv, L == 0, [&](u32x4 (&t)[8]) {
#pragma unroll
        for (int i = 0; i < 7; ++i) t[i] = ldg(rs, sx + SX_YP + (8 * i + (lane >> 3)) * 4096 + kr * 128 + (lane & 7) * 16);
        t[7][0] = t[7][2] = __builtin_amdgcn_raw_buffer_load_b32(rs, sx + SX_ROW + (idx >> 5) * 2048 + kr * 64 + (fo >> 1) * 4, 0, 16);
        t[7][1] = t[7][3] = __builtin_amdgcn_raw_buffer_load_b32(rs, sx + SX_ROW + 131072 + (idx >> 5) * 2048 + kr * 64 + (fo >> 1) * 4, 0, 16);
      }, [&](const u32x4 (&t)[8]) { return none_sent(t); });
      const float hv = (fo & 1) ? hi16(pv[7][0]) : lo16(pv[7][0]), xv = (fo & 1) ? hi16(pv[7][1]) : lo16(pv[7][1]);
      STAMPV(4 + 16 * L);
      float ys;
      {
        f32x4 t = sum4<7>(pv);
#pragma unroll
        for (int e = 0; e < 4; ++e) {
          t[e] = fadd(t[e], U2F(dpp<0x128>(__float_as_uint(t[e]))));
          t[e] = half_swap_sum(row_swap_sum(t[e]));
        }
        const float x = (lane & 8) ? t[1] : t[0], y = (lane & 8) ? t[3] : t[2];
        ys = tblk ? y : x;
      }
      const unsigned ob = pack2(fadd(xv, rbf(fadd(hv, rbf(fmul(rbf(ys), rsig))))), 0.f) & 0xffffu;
      if (L == 1) {
        if (lane < 32) out[(size_t)idx * D_ + kr * 32 + n] = (u16)ob;
        STAMPV(5 + 16 * L);
        STAMP_END;
        __builtin_amdgcn_endpgm();
      }
      reinterpret_cast<u16*>(smem + L_X2F + a * 64)[fo] = (u16)ob;
      asm volatile("s_waitcnt lgkmcnt(0)" ::: "memory");
      {
        const s16x8 xa = *reinterpret_cast<const s16x8*>(smem + L_X2F + a * 64 + (lane >> 4) * 16);
        const u32x4 xf = *reinterpret_cast<const u32x4*>(smem + L_X2F + a * 64 + tp * 16);
        f32x4 z[12];
        SBAR();
        __builtin_amdgcn_s_setprio(3);
#pragma unroll
        for (int t = 0; t < 12; ++t) z[t] = MFMA16(xa, wpf[t], zero);
        SBAR();
        __builtin_amdgcn_s_setprio(0);
        const float x2 = lo16(ob), s1 = half_sum(x2), s2 = half_sum(fmul(x2, x2));
        u32x4 o;
#pragma unroll
        for (int e = 0; e < 4; ++e) {
          const unsigned sp = lane < 52 ? (e == 0 ? __float_as_uint(s1) : __float_as_uint(s2)) : xf[e];
          const unsigned x = __float_as_uint(tblk ? z[4 + e][0] : z[e][0]), y = tblk ? sp : __float_as_uint(z[8 + e][0]);
          o[e] = nz(hi ? y : x);
        }
        const unsigned pos = lane < 24 ? lane * 16 : lane < 48 ? lane * 16 + 128 : lane < 52 ? (lane & 1) * 512 + 384 : 400 + tp * 16;
        stg(rs, lane < 56 ? OFF_SX + SX_QP + ((2 * cg + a) * 32 + kr) * 1024 + pos : OOB, o);
      }
      STAMPV(5 + 16 * L);
      if (owner) {
        __builtin_amdgcn_s_sleep(13);
        const int c25 = r32 < 25 ? r32 : 24, pl = 24 * a + c24, col = 64 * (pl >> 4) + (pl & 15);
        float cs[4];
#pragma unroll
        for (int e = 0; e < 4; ++e) { cs[e] = reinterpret_cast<const float*>(smem + L_CS)[col + 16 * e]; PIN(cs[e]); }
        u32x4 qv[17];
        poll_all<17>(qv, true, [&](u32x4 (&t)[17]) {
#pragma unroll
          for (int i = 0; i < 16; ++i) t[i] = ldg(rs, OFF_SX + SX_QP + (tn * 32 + 2 * i + hi) * 1024 + a * 512 + c25 * 16);
          t[16] = ldg(rs, OFF_SX + SX_QP + (tn * 34 + hi) * 1024 + 400 + tp * 16);
        }, [&](const u32x4 (&t)[17]) { return none_sent(t); });
        *reinterpret_cast<u32x4*>(smem + L_X2P + hi * 64 + tp * 16) = qv[16];
        f32x4 t = sum4<16>(qv);
#pragma unroll
        for (int e = 0; e < 4; ++e) t[e] = half_swap_sum(t[e]);
        const float mean = fmul(U2F(__builtin_amdgcn_readlane(__float_as_uint(t[0]), 24)), 1.0f / 1024.0f);
        const float var = fmaxf(ffma(-mean, mean, fmul(U2F(__builtin_amdgcn_readlane(__float_as_uint(t[1]), 24)), 1.0f / 1024.0f)), 0.f);
        float mb, den;
        ln_scalars(mean, var, mb, den);
        const float rd = RCP(den);
        if (lane < 24) {
#pragma unroll
          for (int e = 0; e < 4; ++e) reinterpret_cast<float*>(smem + L_PATCH)[col + 16 * e] = fmul(ffma(-mb, cs[e], t[e]), rd);
        }
        lds_set(wa + 4 + 4 * a, L + 1);
      }
      STAMPV(6 + 16 * L);
      }
      BAR_ALL();
      unsigned dv = fresh_lane();
      dv = (dv >> 4) * (8 * D_ * 2) + (dv & 15) * 16;
      PIN(dv);
      wait_go(1);
      load_wd(dv);
      STAMPV(23);
      lds_wait_ge<4>(wa + 80 + 4 * (fresh_lane() & 3), 4);
      transpose8(wdf); transpose8(wdf + 8);
      BARRIER();
    }
  }
  const int tm_ = tm, tn_ = tn, wid_ = wid;
  const u32x4 srd = {(unsigned)(uintptr_t)ws, (unsigned)((uintptr_t)ws >> 32) & 0xffffu, WS_BYTES, 0x00020000u};
  u32x4 x1p[4];
  asm volatile("" : "=v"(x1p[0]), "=v"(x1p[1]), "=v"(x1p[2]), "=v"(x1p[3]));
#pragma unroll 1
  for (int L = 0; L < 2; ++L) {
    const unsigned sx = OFF_SX + L * SX_LAYER;
    u32x8 q[4];
    u32x16 kva, kvb;
    f32x16 oz[5];
    {
      f32x16 acc[6];
      u32x16 stA, stB, xr0, xr1, xr2, xr3;
      u32x4 stC;
      asm volatile("" : "={v[112:127]}"(stB), "={v[128:131]}"(stC));
      { PHASE_VARS;
      stB[8] = lane * 16 + (L ? OFF_X1 : OFF_HF) + tile * TS;
      stB[9] = lbase + wrow * 384 + (((4 * hh + wch) ^ wsw) << 4) + 8 * (tp & 1);
      stB[10] = lbase + wrow * 384 + (((4 * (hh ^ 1) + wch) ^ wsw) << 4) + 8 * (tp & 1);
      stB[13] = lbase + L_FULL + (lane & 1) * 256;
      stB[15] = lbase + L_FREE + wid * 256 + lane * 4;
      stC[0] = 32 * L; }
      unsigned soff, slot, need = 32 * L + 1;
      if (L == 0) {
        lds_wait_ge<0>(lbase + L_W + 24, 1);
        STAMPV(39);
        soff = tn * 4096;
        asm volatile(
          "buffer_load_dwordx4 v[132:135], v120, s[40:43], s44 offen offset:0 sc1\n\t"
          "buffer_load_dwordx4 v[136:139], v120, s[40:43], s44 offen offset:1024 sc1\n\t"
          "buffer_load_dwordx4 v[140:143], v120, s[40:43], s44 offen offset:2048 sc1\n\t"
          "buffer_load_dwordx4 v[144:147], v120, s[40:43], s44 offen offset:3072 sc1\n\t"
          "s_add_u32 s44, s44, 0x1000\n\t"
          "s_and_b32 s44, s44, 0xffff\n\t"
          "buffer_load_dwordx4 v[148:151], v120, s[40:43], s44 offen offset:0 sc1\n\t"
          "buffer_load_dwordx4 v[152:155], v120, s[40:43], s44 offen offset:1024 sc1\n\t"
          "buffer_load_dwordx4 v[156:159], v120, s[40:43], s44 offen offset:2048 sc1\n\t"
          "buffer_load_dwordx4 v[160:163], v120, s[40:43], s44 offen offset:3072 sc1\n\t"
          "s_add_u32 s44, s44, 0x1000\n\t"
          "s_and_b32 s44, s44, 0xffff\n\t"
          "buffer_load_dwordx4 v[164:167], v120, s[40:43], s44 offen offset:0 sc1\n\t"
          "buffer_load_dwordx4 v[168:171], v120, s[40:43], s44 offen offset:1024 sc1\n\t"
          "buffer_load_dwordx4 v[172:175], v120, s[40:43], s44 offen offset:2048 sc1\n\t"
          "buffer_load_dwordx4 v[176:179], v120, s[40:43], s44 offen offset:3072 sc1\n\t"
          "s_add_u32 s44, s44, 0x1000\n\t"
          "s_and_b32 s44, s44, 0xffff\n\t"
          "buffer_load_dwordx4 v[180:183], v120, s[40:43], s44 offen offset:0 sc1\n\t"
          "buffer_load_dwordx4 v[184:187], v120, s[40:43], s44 offen offset:1024 sc1\n\t"
          "buffer_load_dwordx4 v[188:191], v120, s[40:43], s44 offen offset:2048 sc1\n\t"
          "buffer_load_dwordx4 v[192:195], v120, s[40:43], s44 offen offset:3072 sc1\n\t"
          : "={v[132:147]}"(xr0), "={v[148:163]}"(xr1), "={v[164:179]}"(xr2), "={v[180:195]}"(xr3), "+{s44}"(soff) : "{v[112:127]}"(stB), "{s[40:43]}"(srd) : "scc", "memory");
      } else {
        xr0 = cat4(x1p[0], x1p[1], x1p[2], x1p[3]);
        asm volatile("" : "={v[148:163]}"(xr1), "={v[164:179]}"(xr2), "={v[180:195]}"(xr3));
      }
      STAMPV(1 + 16 * L);
      asm volatile(
        "v_mov_b32 v123, v121\n\t"
        "v_mov_b32 v124, v122\n\t"
        "s_mov_b64 vcc, -1\n\t"
        "s_cbranch_vccnz 10f\n\t"
        "11:\n\t"
        "s_mov_b32 s46, 12288\n\t"
        "s_add_u32 s47, s47, 1\n\t"
        "ds_read_b64_tr_b16 v[96:97], v123 offset:0\n\t"
        "ds_read_b64_tr_b16 v[98:99], v123 offset:3072\n\t"
        "ds_read_b64_tr_b16 v[100:101], v124 offset:0\n\t"
        "ds_read_b64_tr_b16 v[102:103], v124 offset:3072\n\t"
        "ds_read_b64_tr_b16 v[104:105], v123 offset:128\n\t"
        "ds_read_b64_tr_b16 v[106:107], v123 offset:3200\n\t"
        "ds_read_b64_tr_b16 v[108:109], v124 offset:128\n\t"
        "ds_read_b64_tr_b16 v[110:111], v124 offset:3200\n\t"
        "ds_read_b64_tr_b16 v[112:113], v123 offset:256\n\t"
        "ds_read_b64_tr_b16 v[114:115], v123 offset:3328\n\t"
        "ds_write_b32 v127, v128\n\t"
        "ds_read_b64_tr_b16 v[116:117], v124 offset:256\n\t"
        "ds_read_b64_tr_b16 v[118:119], v124 offset:3328\n\t"
        "s_waitcnt vmcnt(15) lgkmcnt(11)\n\t"
        "v_mfma_f32_32x32x16_bf16 v[0:15], v[96:99], v[132:135], 0\n\t"
        "ds_read_b32 v126, v125\n\t"
        "ds_read_b64_tr_b16 v[96:97], v123 offset:6144\n\t"
        "ds_read_b64_tr_b16 v[98:99], v123 offset:9216\n\t"
        "s_waitcnt lgkmcnt(12)\n\t"
        "v_mfma_f32_32x32x16_bf16 v[16:31], v[100:103], v[132:135], 0\n\t"
        "v_add_u32 v129, s46, v121\n\t"
        "ds_read_b64_tr_b16 v[100:101], v124 offset:6144\n\t"
        "ds_read_b64_tr_b16 v[102:103], v124 offset:9216\n\t"
        "s_waitcnt lgkmcnt(12)\n\t"
        "v_mfma_f32_32x32x16_bf16 v[32:47], v[104:107], v[132:135], 0\n\t"
        "ds_read_b64_tr_b16 v[104:105], v123 offset:6272\n\t"
        "ds_read_b64_tr_b16 v[106:107], v123 offset:9344\n\t"
        "s_waitcnt lgkmcnt(12)\n\t"
        "v_mfma_f32_32x32x16_bf16 v[48:63], v[108:111], v[132:135], 0\n\t"
        "v_add_u32 v130, s46, v122\n\t"
        "ds_read_b64_tr_b16 v[108:109], v124 offset:6272\n\t"
        "ds_read_b64_tr_b16 v[110:111], v124 offset:9344\n\t"
        "s_waitcnt lgkmcnt(12)\n\t"
        "v_mfma_f32_32x32x16_bf16 v[64:79], v[132:135], v[112:115], 0\n\t"
        "ds_read_b64_tr_b16 v[112:113], v123 offset:6400\n\t"
        "ds_read_b64_tr_b16 v[114:115], v123 offset:9472\n\t"
        "s_waitcnt lgkmcnt(10)\n\t"
        "v_mfma_f32_32x32x16_bf16 v[80:95], v[132:135], v[116:119], 0\n\t"
        "v_cmp_gt_u32 vcc, s47, v126\n\t"
        "ds_read_b64_tr_b16 v[116:117], v124 offset:6400\n\t"
        "ds_read_b64_tr_b16 v[118:119], v124 offset:9472\n\t"
        "s_waitcnt vmcnt(14) lgkmcnt(10)\n\t"
        "v_mfma_f32_32x32x16_bf16 v[0:15], v[96:99], v[136:139], v[0:15]\n\t"
        "s_cbranch_vccnz 12f\n\t"
        "13:\n\t"
        "ds_read_b64_tr_b16 v[96:97], v129 offset:0\n\t"
        "ds_read_b64_tr_b16 v[98:99], v129 offset:3072\n\t"
        "s_waitcnt lgkmcnt(10)\n\t"
        "v_mfma_f32_32x32x16_bf16 v[16:31], v[100:103], v[136:139], v[16:31]\n\t"
        "v_add_u32 v128, 1, v128\n\t"
        "ds_read_b64_tr_b16 v[100:101], v130 offset:0\n\t"
        "ds_read_b64_tr_b16 v[102:103], v130 offset:3072\n\t"
        "s_waitcnt lgkmcnt(10)\n\t"
        "v_mfma_f32_32x32x16_bf16 v[32:47], v[104:107], v[136:139], v[32:47]\n\t"
        "ds_read_b64_tr_b16 v[104:105], v129 offset:128\n\t"
        "ds_read_b64_tr_b16 v[106:107], v129 offset:3200\n\t"
        "s_waitcnt lgkmcnt(10)\n\t"
        "v_mfma_f32_32x32x16_bf16 v[48:63], v[108:111], v[136:139], v[48:63]\n\t"
        "s_add_u32 s46, s46, 12288\n\t"
        "s_add_u32 s47, s47, 1\n\t"
        "ds_read_b64_tr_b16 v[108:109], v130 offset:128\n\t"
        "ds_read_b64_tr_b16 v[110:111], v130 offset:3200\n\t"
        "s_waitcnt lgkmcnt(10)\n\t"
        "v_mfma_f32_32x32x16_bf16 v[64:79], v[136:139], v[112:115], v[64:79]\n\t"
        "s_cmp_eq_u32 s46, 110592\n\t"
        "s_cselect_b32 s46, 0, s46\n\t"
        "ds_read_b64_tr_b16 v[112:113], v129 offset:256\n\t"
        "ds_read_b64_tr_b16 v[114:115], v129 offset:3328\n\t"
        "s_waitcnt lgkmcnt(10)\n\t"
        "v_mfma_f32_32x32x16_bf16 v[80:95], v[136:139], v[116:119], v[80:95]\n\t"
        "ds_write_b32 v127, v128\n\t"
        "ds_read_b64_tr_b16 v[116:117], v130 offset:256\n\t"
        "ds_read_b64_tr_b16 v[118:119], v130 offset:3328\n\t"
        "s_waitcnt vmcnt(13) lgkmcnt(11)\n\t"
        "v_mfma_f32_32x32x16_bf16 v[0:15], v[96:99], v[140:143], v[0:15]\n\t"
        "ds_read_b32 v126, v125\n\t"
        "ds_read_b64_tr_b16 v[96:97], v129 offset:6144\n\t"
        "ds_read_b64_tr_b16 v[98:99], v129 offset:9216\n\t"
        "s_waitcnt lgkmcnt(12)\n\t"
        "v_mfma_f32_32x32x16_bf16 v[16:31], v[100:103], v[140:143], v[16:31]\n\t"
        "v_add_u32 v123, s46, v121\n\t"
        "ds_read_b64_tr_b16 v[100:101], v130 offset:6144\n\t"
        "ds_read_b64_tr_b16 v[102:103], v130 offset:9216\n\t"
        "s_waitcnt lgkmcnt(12)\n\t"
        "v_mfma_f32_32x32x16_bf16 v[32:47], v[104:107], v[140:143], v[32:47]\n\t"
        "ds_read_b64_tr_b16 v[104:105], v129 offset:6272\n\t"
        "ds_read_b64_tr_b16 v[106:107], v129 offset:9344\n\t"
        "s_waitcnt lgkmcnt(12)\n\t"
        "v_mfma_f32_32x32x16_bf16 v[48:63], v[108:111], v[140:143], v[48:63]\n\t"
        "v_add_u32 v124, s46, v122\n\t"
        "ds_read_b64_tr_b16 v[108:109], v130 offset:6272\n\t"
        "ds_read_b64_tr_b16 v[110:111], v130 offset:9344\n\t"
        "s_waitcnt lgkmcnt(12)\n\t"
        "v_mfma_f32_32x32x16_bf16 v[64:79], v[140:143], v[112:115], v[64:79]\n\t"
        "ds_read_b64_tr_b16 v[112:113], v129 offset:6400\n\t"
        "ds_read_b64_tr_b16 v[114:115], v129 offset:9472\n\t"
        "s_waitcnt lgkmcnt(10)\n\t"
        "v_mfma_f32_32x32x16_bf16 v[80:95], v[140:143], v[116:119], v[80:95]\n\t"
        "v_cmp_gt_u32 vcc, s47, v126\n\t"
        "ds_read_b64_tr_b16 v[116:117], v130 offset:6400\n\t"
        "ds_read_b64_tr_b16 v[118:119], v130 offset:9472\n\t"
        "s_waitcnt vmcnt(12) lgkmcnt(10)\n\t"
        "v_mfma_f32_32x32x16_bf16 v[0:15], v[96:99], v[144:147], v[0:15]\n\t"
        "s_cbranch_vccnz 14f\n\t"
        "15:\n\t"
        "ds_read_b64_tr_b16 v[96:97], v123 offset:0\n\t"
        "ds_read_b64_tr_b16 v[98:99], v123 offset:3072\n\t"
        "s_waitcnt lgkmcnt(10)\n\t"
        "v_mfma_f32_32x32x16_bf16 v[16:31], v[100:103], v[144:147], v[16:31]\n\t"
        "v_add_u32 v128, 1, v128\n\t"
        "ds_read_b64_tr_b16 v[100:101], v124 offset:0\n\t"
        "ds_read_b64_tr_b16 v[102:103], v124 offset:3072\n\t"
        "s_waitcnt lgkmcnt(10)\n\t"
        "v_mfma_f32_32x32x16_bf16 v[32:47], v[104:107], v[144:147], v[32:47]\n\t"
        "ds_read_b64_tr_b16 v[104:105], v123 offset:128\n\t"
        "ds_read_b64_tr_b16 v[106:107], v123 offset:3200\n\t"
        "s_waitcnt lgkmcnt(10)\n\t"
        "v_mfma_f32_32x32x16_bf16 v[48:63], v[108:111], v[144:147], v[48:63]\n\t"
        "s_add_u32 s46, s46, 12288\n\t"
        "s_add_u32 s47, s47, 1\n\t"
        "ds_read_b64_tr_b16 v[108:109], v124 offset:128\n\t"
        "ds_read_b64_tr_b16 v[110:111], v124 offset:3200\n\t"
        "s_waitcnt lgkmcnt(10)\n\t"
        "v_mfma_f32_32x32x16_bf16 v[64:79], v[144:147], v[112:115], v[64:79]\n\t"
        "s_cmp_eq_u32 s46, 110592\n\t"
        "s_cselect_b32 s46, 0, s46\n\t"
        "ds_read_b64_tr_b16 v[112:113], v123 offset:256\n\t"
        "ds_read_b64_tr_b16 v[114:115], v123 offset:3328\n\t"
        "s_waitcnt lgkmcnt(10)\n\t"
        "v_mfma_f32_32x32x16_bf16 v[80:95], v[144:147], v[116:119], v[80:95]\n\t"
        "ds_write_b32 v127, v128\n\t"
        "ds_read_b64_tr_b16 v[116:117], v124 offset:256\n\t"
        "ds_read_b64_tr_b16 v[118:119], v124 offset:3328\n\t"
        "s_waitcnt lgkmcnt(0)\n\t"
        "s_nop 7\n\t"
        "s_branch 9f\n\t"
        "10:\n\t"
        "s_mov_b32 s48, 0x10000\n\t"
        "8:\n\t"
        "ds_read_b32 v126, v125\n\t"
        "s_sub_u32 s48, s48, 1\n\t"
        "s_waitcnt lgkmcnt(0)\n\t"
        "s_cbranch_scc1 11b\n\t"
        "v_cmp_gt_u32 vcc, s47, v126\n\t"
        "s_cbranch_vccnz 8b\n\t"
        "s_branch 11b\n\t"
        "12:\n\t"
        "s_mov_b32 s48, 0x10000\n\t"
        "8:\n\t"
        "ds_read_b32 v126, v125\n\t"
        "s_sub_u32 s48, s48, 1\n\t"
        "s_waitcnt lgkmcnt(0)\n\t"
        "s_cbranch_scc1 13b\n\t"
        "v_cmp_gt_u32 vcc, s47, v126\n\t"
        "s_cbranch_vccnz 8b\n\t"
        "s_branch 13b\n\t"
        "14:\n\t"
        "s_mov_b32 s48, 0x10000\n\t"
        "8:\n\t"
        "ds_read_b32 v126, v125\n\t"
        "s_sub_u32 s48, s48, 1\n\t"
        "s_waitcnt lgkmcnt(0)\n\t"
        "s_cbranch_scc1 15b\n\t"
        "v_cmp_gt_u32 vcc, s47, v126\n\t"
        "s_cbranch_vccnz 8b\n\t"
        "s_branch 15b\n\t"
        "9:\n\t"
        : "={v[0:15]}"(acc[0]), "={v[16:31]}"(acc[1]), "={v[32:47]}"(acc[2]), "={v[48:63]}"(acc[3]), "={v[64:79]}"(acc[4]), "={v[80:95]}"(acc[5]),
          "={v[96:111]}"(stA), "+{v[112:127]}"(stB), "+{v[128:131]}"(stC), "={s46}"(slot), "+{s47}"(need)
        : "{v[132:147]}"(xr0) : "s48", "vcc", "scc", "memory");
      STAMPV(2 + 16 * L);
      float al = 1.f;
      if (L == 1) {
        PHASE_VARS;
        float S, Q, Dd;
        row_stats_sum(rs, OFF_SX, tile, lane, S, Q, Dd);
        STAMPV(36);
        lds_wait_ge<0>(lbase + L_W + 24, 2);
        soff = ((tn + 1) & 15) * 4096;
        asm volatile(
          "buffer_load_dwordx4 v[148:151], v120, s[40:43], s44 offen offset:0 sc1\n\t"
          "buffer_load_dwordx4 v[152:155], v120, s[40:43], s44 offen offset:1024 sc1\n\t"
          "buffer_load_dwordx4 v[156:159], v120, s[40:43], s44 offen offset:2048 sc1\n\t"
          "buffer_load_dwordx4 v[160:163], v120, s[40:43], s44 offen offset:3072 sc1\n\t"
          : "={v[148:163]}"(xr1), "={v[164:179]}"(xr2), "={v[180:195]}"(xr3), "+{s44}"(soff), "+v"(S), "+v"(Q), "+v"(Dd) : "{v[112:127]}"(stB), "{s[40:43]}"(srd) : "scc", "memory");
        float mean, var, mb1, rd1, mb2, den2;
        row_stats_fin(rs, OFF_SX + SX_CAND + (tn * 16 + tm) * 128 + wid * 4, smem, 1, tile, lane, S, Q, Dd, U2F(words[0]), x1p, mean, var, mb1, rd1);
        ln_scalars(mean + (mean - mb1) * rd1, var * (1.0f + rd1) * (1.0f + rd1), mb2, den2);
        const float rd2 = RCP(den2);
        al = (1.0f + rd1) * rd2;
        const float be = -(mb1 * rd1 + mb2) * RCP(1.0f + rd1);
        reinterpret_cast<float*>(smem + L_AB)[wid * 64 + lane] = hi ? be : al;
        reinterpret_cast<float*>(smem + L_AB)[256 + wid * 64 + lane] = hi ? 1.0f + rd1 : -mb1 * rd1;
        soff = ((tn + 1 + 0) & 15) * 4096;
        asm volatile(
          "s_nop 0\n\t"
          "s_add_u32 s44, s44, 0x1000\n\t"
          "s_and_b32 s44, s44, 0xffff\n\t"
          "buffer_load_dwordx4 v[164:167], v120, s[40:43], s44 offen offset:0 sc1\n\t"
          "buffer_load_dwordx4 v[168:171], v120, s[40:43], s44 offen offset:1024 sc1\n\t"
          "buffer_load_dwordx4 v[172:175], v120, s[40:43], s44 offen offset:2048 sc1\n\t"
          "buffer_load_dwordx4 v[176:179], v120, s[40:43], s44 offen offset:3072 sc1\n\t"
          "s_add_u32 s44, s44, 0x1000\n\t"
          "s_and_b32 s44, s44, 0xffff\n\t"
          "buffer_load_dwordx4 v[180:183], v120, s[40:43], s44 offen offset:0 sc1\n\t"
          "buffer_load_dwordx4 v[184:187], v120, s[40:43], s44 offen offset:1024 sc1\n\t"
          "buffer_load_dwordx4 v[188:191], v120, s[40:43], s44 offen offset:2048 sc1\n\t"
          "buffer_load_dwordx4 v[192:195], v120, s[40:43], s44 offen offset:3072 sc1\n\t"
          : "+{v[148:163]}"(xr1), "+{v[164:179]}"(xr2), "+{v[180:195]}"(xr3), "+{s44}"(soff) : "{v[112:127]}"(stB), "{s[40:43]}"(srd) : "scc", "memory");
#pragma unroll
        for (int jj = 0; jj < 3; ++jj) {
          f32x4 cs[2][4];
          float cv[2];
#pragma unroll
          for (int j = 0; j < 2; ++j) {
            const int cb = jj * 64 + (j ^ hh) * 32;
            if (jj == 2) cv[j] = *reinterpret_cast<const float*>(smem + L_CS + (cb + r32) * 4);
#pragma unroll
            for (int g = 0; g < 4; ++g)
              if (jj < 2 || j == 0) cs[j][g] = *reinterpret_cast<const f32x4*>(smem + (jj < 2 ? L_CS + cb * 4 : L_AB + wid * 256 + 128) + (8 * g + 4 * hi) * 4);
          }
          SBAR();
#pragma unroll
          for (int j = 0; j < 2; ++j)
#pragma unroll
            for (int e = 0; e < 16; ++e) acc[2 * jj + j][e] += jj < 2 ? be * cs[j][e >> 2][e & 3] : cs[0][e >> 2][e & 3] * cv[j];
          SBAR();
        }
      }
      u32x4 ep;
      { PHASE_VARS;
        if (L == 0) reinterpret_cast<float*>(smem + L_AB)[wid * 64 + lane] = 1.0f;
        const unsigned dst = OFF_KV + L * (10 * MBY) + tn * 2 * HS + tile * 4096 + lane * 16;
        ep = u32x4{__float_as_uint(al), dst + hh * HS, dst + (hh ^ 1) * HS, lbase + L_AB + wid * 256 + hi * 16}; }
      STAMPV(3 + 16 * L);
      soff = ((tn + 1 + 2) & 15) * 4096;
      asm volatile(
        "s_nop 0\n\t"
        "s_add_u32 s44, s44, 0x1000\n\t"
        "s_and_b32 s44, s44, 0xffff\n\t"
        "buffer_load_dwordx4 v[132:135], v120, s[40:43], s44 offen offset:0 sc1\n\t"
        "buffer_load_dwordx4 v[136:139], v120, s[40:43], s44 offen offset:1024 sc1\n\t"
        "buffer_load_dwordx4 v[140:143], v120, s[40:43], s44 offen offset:2048 sc1\n\t"
        "s_mov_b32 s45, 3\n\t"
        "1:\n\t"
        "s_waitcnt vmcnt(14) lgkmcnt(11)\n\t"
        "v_mfma_f32_32x32x16_bf16 v[0:15], v[96:99], v[148:151], v[0:15]\n\t"
        "ds_read_b32 v126, v125\n\t"
        "ds_read_b64_tr_b16 v[96:97], v123 offset:6144\n\t"
        "ds_read_b64_tr_b16 v[98:99], v123 offset:9216\n\t"
        "s_waitcnt lgkmcnt(12)\n\t"
        "v_mfma_f32_32x32x16_bf16 v[16:31], v[100:103], v[148:151], v[16:31]\n\t"
        "v_add_u32 v129, s46, v121\n\t"
        "ds_read_b64_tr_b16 v[100:101], v124 offset:6144\n\t"
        "ds_read_b64_tr_b16 v[102:103], v124 offset:9216\n\t"
        "s_waitcnt lgkmcnt(12)\n\t"
        "v_mfma_f32_32x32x16_bf16 v[32:47], v[104:107], v[148:151], v[32:47]\n\t"
        "ds_read_b64_tr_b16 v[104:105], v123 offset:6272\n\t"
        "ds_read_b64_tr_b16 v[106:107], v123 offset:9344\n\t"
        "buffer_load_dwordx4 v[144:147], v120, s[40:43], s44 offen offset:3072 sc1\n\t"
        "s_waitcnt lgkmcnt(12)\n\t"
        "v_mfma_f32_32x32x16_bf16 v[48:63], v[108:111], v[148:151], v[48:63]\n\t"
        "v_add_u32 v130, s46, v122\n\t"
        "ds_read_b64_tr_b16 v[108:109], v124 offset:6272\n\t"
        "ds_read_b64_tr_b16 v[110:111], v124 offset:9344\n\t"
        "s_waitcnt lgkmcnt(12)\n\t"
        "v_mfma_f32_32x32x16_bf16 v[64:79], v[148:151], v[112:115], v[64:79]\n\t"
        "ds_read_b64_tr_b16 v[112:113], v123 offset:6400\n\t"
        "ds_read_b64_tr_b16 v[114:115], v123 offset:9472\n\t"
        "s_waitcnt lgkmcnt(10)\n\t"
        "v_mfma_f32_32x32x16_bf16 v[80:95], v[148:151], v[116:119], v[80:95]\n\t"
        "v_cmp_gt_u32 vcc, s47, v126\n\t"
        "ds_read_b64_tr_b16 v[116:117], v124 offset:6400\n\t"
        "ds_read_b64_tr_b16 v[118:119], v124 offset:9472\n\t"
        "s_waitcnt vmcnt(14) lgkmcnt(10)\n\t"
        "v_mfma_f32_32x32x16_bf16 v[0:15], v[96:99], v[152:155], v[0:15]\n\t"
        "s_cbranch_vccnz 16f\n\t"
        "17:\n\t"
        "ds_read_b64_tr_b16 v[96:97], v129 offset:0\n\t"
        "ds_read_b64_tr_b16 v[98:99], v129 offset:3072\n\t"
        "s_waitcnt lgkmcnt(10)\n\t"
        "v_mfma_f32_32x32x16_bf16 v[16:31], v[100:103], v[152:155], v[16:31]\n\t"
        "v_add_u32 v128, 1, v128\n\t"
        "ds_read_b64_tr_b16 v[100:101], v130 offset:0\n\t"
        "ds_read_b64_tr_b16 v[102:103], v130 offset:3072\n\t"
        "s_waitcnt lgkmcnt(10)\n\t"
        "v_mfma_f32_32x32x16_bf16 v[32:47], v[104:107], v[152:155], v[32:47]\n\t"
        "ds_read_b64_tr_b16 v[104:105], v129 offset:128\n\t"
        "ds_read_b64_tr_b16 v[106:107], v129 offset:3200\n\t"
        "s_add_u32 s44, s44, 0x1000\n\t"
        "s_and_b32 s44, s44, 0xffff\n\t"
        "buffer_load_dwordx4 v[148:151], v120, s[40:43], s44 offen offset:0 sc1\n\t"
        "s_waitcnt lgkmcnt(10)\n\t"
        "v_mfma_f32_32x32x16_bf16 v[48:63], v[108:111], v[152:155], v[48:63]\n\t"
        "s_add_u32 s46, s46, 12288\n\t"
        "s_add_u32 s47, s47, 1\n\t"
        "ds_read_b64_tr_b16 v[108:109], v130 offset:128\n\t"
        "ds_read_b64_tr_b16 v[110:111], v130 offset:3200\n\t"
        "s_waitcnt lgkmcnt(10)\n\t"
        "v_mfma_f32_32x32x16_bf16 v[64:79], v[152:155], v[112:115], v[64:79]\n\t"
        "s_cmp_eq_u32 s46, 110592\n\t"
        "s_cselect_b32 s46, 0, s46\n\t"
        "ds_read_b64_tr_b16 v[112:113], v129 offset:256\n\t"
        "ds_read_b64_tr_b16 v[114:115], v129 offset:3328\n\t"
        "s_waitcnt lgkmcnt(10)\n\t"
        "v_mfma_f32_32x32x16_bf16 v[80:95], v[152:155], v[116:119], v[80:95]\n\t"
        "ds_write_b32 v127, v128\n\t"
        "ds_read_b64_tr_b16 v[116:117], v130 offset:256\n\t"
        "ds_read_b64_tr_b16 v[118:119], v130 offset:3328\n\t"
        "s_waitcnt vmcnt(14) lgkmcnt(11)\n\t"
        "v_mfma_f32_32x32x16_bf16 v[0:15], v[96:99], v[156:159], v[0:15]\n\t"
        "ds_read_b32 v126, v125\n\t"
        "ds_read_b64_tr_b16 v[96:97], v129 offset:6144\n\t"
        "ds_read_b64_tr_b16 v[98:99], v129 offset:9216\n\t"
        "s_waitcnt lgkmcnt(12)\n\t"
        "v_mfma_f32_32x32x16_bf16 v[16:31], v[100:103], v[156:159], v[16:31]\n\t"
        "v_add_u32 v123, s46, v121\n\t"
        "ds_read_b64_tr_b16 v[100:101], v130 offset:6144\n\t"
        "ds_read_b64_tr_b16 v[102:103], v130 offset:9216\n\t"
        "s_waitcnt lgkmcnt(12)\n\t"
        "v_mfma_f32_32x32x16_bf16 v[32:47], v[104:107], v[156:159], v[32:47]\n\t"
        "ds_read_b64_tr_b16 v[104:105], v129 offset:6272\n\t"
        "ds_read_b64_tr_b16 v[106:107], v129 offset:9344\n\t"
        "buffer_load_dwordx4 v[152:155], v120, s[40:43], s44 offen offset:1024 sc1\n\t"
        "s_waitcnt lgkmcnt(12)\n\t"
        "v_mfma_f32_32x32x16_bf16 v[48:63], v[108:111], v[156:159], v[48:63]\n\t"
        "v_add_u32 v124, s46, v122\n\t"
        "ds_read_b64_tr_b16 v[108:109], v130 offset:6272\n\t"
        "ds_read_b64_tr_b16 v[110:111], v130 offset:9344\n\t"
        "s_waitcnt lgkmcnt(12)\n\t"
        "v_mfma_f32_32x32x16_bf16 v[64:79], v[156:159], v[112:115], v[64:79]\n\t"
        "ds_read_b64_tr_b16 v[112:113], v129 offset:6400\n\t"
        "ds_read_b64_tr_b16 v[114:115], v129 offset:9472\n\t"
        "s_waitcnt lgkmcnt(10)\n\t"
        "v_mfma_f32_32x32x16_bf16 v[80:95], v[156:159], v[116:119], v[80:95]\n\t"
        "v_cmp_gt_u32 vcc, s47, v126\n\t"
        "ds_read_b64_tr_b16 v[116:117], v130 offset:6400\n\t"
        "ds_read_b64_tr_b16 v[118:119], v130 offset:9472\n\t"
        "s_waitcnt vmcnt(14) lgkmcnt(10)\n\t"
        "v_mfma_f32_32x32x16_bf16 v[0:15], v[96:99], v[160:163], v[0:15]\n\t"
        "s_cbranch_vccnz 18f\n\t"
        "19:\n\t"
        "ds_read_b64_tr_b16 v[96:97], v123 offset:0\n\t"
        "ds_read_b64_tr_b16 v[98:99], v123 offset:3072\n\t"
        "s_waitcnt lgkmcnt(10)\n\t"
        "v_mfma_f32_32x32x16_bf16 v[16:31], v[100:103], v[160:163], v[16:31]\n\t"
        "v_add_u32 v128, 1, v128\n\t"
        "ds_read_b64_tr_b16 v[100:101], v124 offset:0\n\t"
        "ds_read_b64_tr_b16 v[102:103], v124 offset:3072\n\t"
        "s_waitcnt lgkmcnt(10)\n\t"
        "v_mfma_f32_32x32x16_bf16 v[32:47], v[104:107], v[160:163], v[32:47]\n\t"
        "ds_read_b64_tr_b16 v[104:105], v123 offset:128\n\t"
        "ds_read_b64_tr_b16 v[106:107], v123 offset:3200\n\t"
        "buffer_load_dwordx4 v[156:159], v120, s[40:43], s44 offen offset:2048 sc1\n\t"
        "s_waitcnt lgkmcnt(10)\n\t"
        "v_mfma_f32_32x32x16_bf16 v[48:63], v[108:111], v[160:163], v[48:63]\n\t"
        "s_add_u32 s46, s46, 12288\n\t"
        "s_add_u32 s47, s47, 1\n\t"
        "ds_read_b64_tr_b16 v[108:109], v124 offset:128\n\t"
        "ds_read_b64_tr_b16 v[110:111], v124 offset:3200\n\t"
        "s_waitcnt lgkmcnt(10)\n\t"
        "v_mfma_f32_32x32x16_bf16 v[64:79], v[160:163], v[112:115], v[64:79]\n\t"
        "s_cmp_eq_u32 s46, 110592\n\t"
        "s_cselect_b32 s46, 0, s46\n\t"
        "ds_read_b64_tr_b16 v[112:113], v123 offset:256\n\t"
        "ds_read_b64_tr_b16 v[114:115], v123 offset:3328\n\t"
        "s_waitcnt lgkmcnt(10)\n\t"
        "v_mfma_f32_32x32x16_bf16 v[80:95], v[160:163], v[116:119], v[80:95]\n\t"
        "ds_write_b32 v127, v128\n\t"
        "ds_read_b64_tr_b16 v[116:117], v124 offset:256\n\t"
        "ds_read_b64_tr_b16 v[118:119], v124 offset:3328\n\t"
        "s_waitcnt vmcnt(14) lgkmcnt(11)\n\t"
        "v_mfma_f32_32x32x16_bf16 v[0:15], v[96:99], v[164:167], v[0:15]\n\t"
        "ds_read_b32 v126, v125\n\t"
        "ds_read_b64_tr_b16 v[96:97], v123 offset:6144\n\t"
        "ds_read_b64_tr_b16 v[98:99], v123 offset:9216\n\t"
        "s_waitcnt lgkmcnt(12)\n\t"
        "v_mfma_f32_32x32x16_bf16 v[16:31], v[100:103], v[164:167], v[16:31]\n\t"
        "v_add_u32 v129, s46, v121\n\t"
        "ds_read_b64_tr_b16 v[100:101], v124 offset:6144\n\t"
        "ds_read_b64_tr_b16 v[102:103], v124 offset:9216\n\t"
        "s_waitcnt lgkmcnt(12)\n\t"
        "v_mfma_f32_32x32x16_bf16 v[32:47], v[104:107], v[164:167], v[32:47]\n\t"
        "ds_read_b64_tr_b16 v[104:105], v123 offset:6272\n\t"
        "ds_read_b64_tr_b16 v[106:107], v123 offset:9344\n\t"
        "buffer_load_dwordx4 v[160:163], v120, s[40:43], s44 offen offset:3072 sc1\n\t"
        "s_waitcnt lgkmcnt(12)\n\t"
        "v_mfma_f32_32x32x16_bf16 v[48:63], v[108:111], v[164:167], v[48:63]\n\t"
        "v_add_u32 v130, s46, v122\n\t"
        "ds_read_b64_tr_b16 v[108:109], v124 offset:6272\n\t"
        "ds_read_b64_tr_b16 v[110:111], v124 offset:9344\n\t"
        "s_waitcnt lgkmcnt(12)\n\t"
        "v_mfma_f32_32x32x16_bf16 v[64:79], v[164:167], v[112:115], v[64:79]\n\t"
        "ds_read_b64_tr_b16 v[112:113], v123 offset:6400\n\t"
        "ds_read_b64_tr_b16 v[114:115], v123 offset:9472\n\t"
        "s_waitcnt lgkmcnt(10)\n\t"
        "v_mfma_f32_32x32x16_bf16 v[80:95], v[164:167], v[116:119], v[80:95]\n\t"
        "v_cmp_gt_u32 vcc, s47, v126\n\t"
        "ds_read_b64_tr_b16 v[116:117], v124 offset:6400\n\t"
        "ds_read_b64_tr_b16 v[118:119], v124 offset:9472\n\t"
        "s_waitcnt vmcnt(14) lgkmcnt(10)\n\t"
        "v_mfma_f32_32x32x16_bf16 v[0:15], v[96:99], v[168:171], v[0:15]\n\t"
        "s_cbranch_vccnz 20f\n\t"
        "21:\n\t"
        "ds_read_b64_tr_b16 v[96:97], v129 offset:0\n\t"
        "ds_read_b64_tr_b16 v[98:99], v129 offset:3072\n\t"
        "s_waitcnt lgkmcnt(10)\n\t"
        "v_mfma_f32_32x32x16_bf16 v[16:31], v[100:103], v[168:171], v[16:31]\n\t"
        "v_add_u32 v128, 1, v128\n\t"
        "ds_read_b64_tr_b16 v[100:101], v130 offset:0\n\t"
        "ds_read_b64_tr_b16 v[102:103], v130 offset:3072\n\t"
        "s_waitcnt lgkmcnt(10)\n\t"
        "v_mfma_f32_32x32x16_bf16 v[32:47], v[104:107], v[168:171], v[32:47]\n\t"
        "ds_read_b64_tr_b16 v[104:105], v129 offset:128\n\t"
        "ds_read_b64_tr_b16 v[106:107], v129 offset:3200\n\t"
        "s_add_u32 s44, s44, 0x1000\n\t"
        "s_and_b32 s44, s44, 0xffff\n\t"
        "buffer_load_dwordx4 v[164:167], v120, s[40:43], s44 offen offset:0 sc1\n\t"
        "s_waitcnt lgkmcnt(10)\n\t"
        "v_mfma_f32_32x32x16_bf16 v[48:63], v[108:111], v[168:171], v[48:63]\n\t"
        "s_add_u32 s46, s46, 12288\n\t"
        "s_add_u32 s47, s47, 1\n\t"
        "ds_read_b64_tr_b16 v[108:109], v130 offset:128\n\t"
        "ds_read_b64_tr_b16 v[110:111], v130 offset:3200\n\t"
        "s_waitcnt lgkmcnt(10)\n\t"
        "v_mfma_f32_32x32x16_bf16 v[64:79], v[168:171], v[112:115], v[64:79]\n\t"
        "s_cmp_eq_u32 s46, 110592\n\t"
        "s_cselect_b32 s46, 0, s46\n\t"
        "ds_read_b64_tr_b16 v[112:113], v129 offset:256\n\t"
        "ds_read_b64_tr_b16 v[114:115], v129 offset:3328\n\t"
        "s_waitcnt lgkmcnt(10)\n\t"
        "v_mfma_f32_32x32x16_bf16 v[80:95], v[168:171], v[116:119], v[80:95]\n\t"
        "ds_write_b32 v127, v128\n\t"
        "ds_read_b64_tr_b16 v[116:117], v130 offset:256\n\t"
        "ds_read_b64_tr_b16 v[118:119], v130 offset:3328\n\t"
        "s_waitcnt vmcnt(14) lgkmcnt(11)\n\t"
        "v_mfma_f32_32x32x16_bf16 v[0:15], v[96:99], v[172:175], v[0:15]\n\t"
        "ds_read_b32 v126, v125\n\t"
        "ds_read_b64_tr_b16 v[96:97], v129 offset:6144\n\t"
        "ds_read_b64_tr_b16 v[98:99], v129 offset:9216\n\t"
        "s_waitcnt lgkmcnt(12)\n\t"
        "v_mfma_f32_32x32x16_bf16 v[16:31], v[100:103], v[172:175], v[16:31]\n\t"
        "v_add_u32 v123, s46, v121\n\t"
        "ds_read_b64_tr_b16 v[100:101], v130 offset:6144\n\t"
        "ds_read_b64_tr_b16 v[102:103], v130 offset:9216\n\t"
        "s_waitcnt lgkmcnt(12)\n\t"
        "v_mfma_f32_32x32x16_bf16 v[32:47], v[104:107], v[172:175], v[32:47]\n\t"
        "ds_read_b64_tr_b16 v[104:105], v129 offset:6272\n\t"
        "ds_read_b64_tr_b16 v[106:107], v129 offset:9344\n\t"
        "buffer_load_dwordx4 v[168:171], v120, s[40:43], s44 offen offset:1024 sc1\n\t"
        "s_waitcnt lgkmcnt(12)\n\t"
        "v_mfma_f32_32x32x16_bf16 v[48:63], v[108:111], v[172:175], v[48:63]\n\t"
        "v_add_u32 v124, s46, v122\n\t"
        "ds_read_b64_tr_b16 v[108:109], v130 offset:6272\n\t"
        "ds_read_b64_tr_b16 v[110:111], v130 offset:9344\n\t"
        "s_waitcnt lgkmcnt(12)\n\t"
        "v_mfma_f32_32x32x16_bf16 v[64:79], v[172:175], v[112:115], v[64:79]\n\t"
        "ds_read_b64_tr_b16 v[112:113], v129 offset:6400\n\t"
        "ds_read_b64_tr_b16 v[114:115], v129 offset:9472\n\t"
        "s_waitcnt lgkmcnt(10)\n\t"
        "v_mfma_f32_32x32x16_bf16 v[80:95], v[172:175], v[116:119], v[80:95]\n\t"
        "v_cmp_gt_u32 vcc, s47, v126\n\t"
        "ds_read_b64_tr_b16 v[116:117], v130 offset:6400\n\t"
        "ds_read_b64_tr_b16 v[118:119], v130 offset:9472\n\t"
        "s_waitcnt vmcnt(14) lgkmcnt(10)\n\t"
        "v_mfma_f32_32x32x16_bf16 v[0:15], v[96:99], v[176:179], v[0:15]\n\t"
        "s_cbranch_vccnz 22f\n\t"
        "23:\n\t"
        "ds_read_b64_tr_b16 v[96:97], v123 offset:0\n\t"
        "ds_read_b64_tr_b16 v[98:99], v123 offset:3072\n\t"
        "s_waitcnt lgkmcnt(10)\n\t"
        "v_mfma_f32_32x32x16_bf16 v[16:31], v[100:103], v[176:179], v[16:31]\n\t"
        "v_add_u32 v128, 1, v128\n\t"
        "ds_read_b64_tr_b16 v[100:101], v124 offset:0\n\t"
        "ds_read_b64_tr_b16 v[102:103], v124 offset:3072\n\t"
        "s_waitcnt lgkmcnt(10)\n\t"
        "v_mfma_f32_32x32x16_bf16 v[32:47], v[104:107], v[176:179], v[32:47]\n\t"
        "ds_read_b64_tr_b16 v[104:105], v123 offset:128\n\t"
        "ds_read_b64_tr_b16 v[106:107], v123 offset:3200\n\t"
        "buffer_load_dwordx4 v[172:175], v120, s[40:43], s44 offen offset:2048 sc1\n\t"
        "s_waitcnt lgkmcnt(10)\n\t"
        "v_mfma_f32_32x32x16_bf16 v[48:63], v[108:111], v[176:179], v[48:63]\n\t"
        "s_add_u32 s46, s46, 12288\n\t"
        "s_add_u32 s47, s47, 1\n\t"
        "ds_read_b64_tr_b16 v[108:109], v124 offset:128\n\t"
        "ds_read_b64_tr_b16 v[110:111], v124 offset:3200\n\t"
        "s_waitcnt lgkmcnt(10)\n\t"
        "v_mfma_f32_32x32x16_bf16 v[64:79], v[176:179], v[112:115], v[64:79]\n\t"
        "s_cmp_eq_u32 s46, 110592\n\t"
        "s_cselect_b32 s46, 0, s46\n\t"
        "ds_read_b64_tr_b16 v[112:113], v123 offset:256\n\t"
        "ds_read_b64_tr_b16 v[114:115], v123 offset:3328\n\t"
        "s_waitcnt lgkmcnt(10)\n\t"
        "v_mfma_f32_32x32x16_bf16 v[80:95], v[176:179], v[116:119], v[80:95]\n\t"
        "ds_write_b32 v127, v128\n\t"
        "ds_read_b64_tr_b16 v[116:117], v124 offset:256\n\t"
        "ds_read_b64_tr_b16 v[118:119], v124 offset:3328\n\t"
        "s_waitcnt vmcnt(14) lgkmcnt(11)\n\t"
        "v_mfma_f32_32x32x16_bf16 v[0:15], v[96:99], v[180:183], v[0:15]\n\t"
        "ds_read_b32 v126, v125\n\t"
        "ds_read_b64_tr_b16 v[96:97], v123 offset:6144\n\t"
        "ds_read_b64_tr_b16 v[98:99], v123 offset:9216\n\t"
        "s_waitcnt lgkmcnt(12)\n\t"
        "v_mfma_f32_32x32x16_bf16 v[16:31], v[100:103], v[180:183], v[16:31]\n\t"
        "v_add_u32 v129, s46, v121\n\t"
        "ds_read_b64_tr_b16 v[100:101], v124 offset:6144\n\t"
        "ds_read_b64_tr_b16 v[102:103], v124 offset:9216\n\t"
        "s_waitcnt lgkmcnt(12)\n\t"
        "v_mfma_f32_32x32x16_bf16 v[32:47], v[104:107], v[180:183], v[32:47]\n\t"
        "ds_read_b64_tr_b16 v[104:105], v123 offset:6272\n\t"
        "ds_read_b64_tr_b16 v[106:107], v123 offset:9344\n\t"
        "buffer_load_dwordx4 v[176:179], v120, s[40:43], s44 offen offset:3072 sc1\n\t"
        "s_waitcnt lgkmcnt(12)\n\t"
        "v_mfma_f32_32x32x16_bf16 v[48:63], v[108:111], v[180:183], v[48:63]\n\t"
        "v_add_u32 v130, s46, v122\n\t"
        "ds_read_b64_tr_b16 v[108:109], v124 offset:6272\n\t"
        "ds_read_b64_tr_b16 v[110:111], v124 offset:9344\n\t"
        "s_waitcnt lgkmcnt(12)\n\t"
        "v_mfma_f32_32x32x16_bf16 v[64:79], v[180:183], v[112:115], v[64:79]\n\t"
        "ds_read_b64_tr_b16 v[112:113], v123 offset:6400\n\t"
        "ds_read_b64_tr_b16 v[114:115], v123 offset:9472\n\t"
        "s_waitcnt lgkmcnt(10)\n\t"
        "v_mfma_f32_32x32x16_bf16 v[80:95], v[180:183], v[116:119], v[80:95]\n\t"
        "v_cmp_gt_u32 vcc, s47, v126\n\t"
        "ds_read_b64_tr_b16 v[116:117], v124 offset:6400\n\t"
        "ds_read_b64_tr_b16 v[118:119], v124 offset:9472\n\t"
        "s_waitcnt vmcnt(14) lgkmcnt(10)\n\t"
        "v_mfma_f32_32x32x16_bf16 v[0:15], v[96:99], v[184:187], v[0:15]\n\t"
        "s_cbranch_vccnz 24f\n\t"
        "25:\n\t"
        "ds_read_b64_tr_b16 v[96:97], v129 offset:0\n\t"
        "ds_read_b64_tr_b16 v[98:99], v129 offset:3072\n\t"
        "s_waitcnt lgkmcnt(10)\n\t"
        "v_mfma_f32_32x32x16_bf16 v[16:31], v[100:103], v[184:187], v[16:31]\n\t"
        "v_add_u32 v128, 1, v128\n\t"
        "ds_read_b64_tr_b16 v[100:101], v130 offset:0\n\t"
        "ds_read_b64_tr_b16 v[102:103], v130 offset:3072\n\t"
        "s_waitcnt lgkmcnt(10)\n\t"
        "v_mfma_f32_32x32x16_bf16 v[32:47], v[104:107], v[184:187], v[32:47]\n\t"
        "ds_read_b64_tr_b16 v[104:105], v129 offset:128\n\t"
        "ds_read_b64_tr_b16 v[106:107], v129 offset:3200\n\t"
        "s_add_u32 s44, s44, 0x1000\n\t"
        "s_and_b32 s44, s44, 0xffff\n\t"
        "buffer_load_dwordx4 v[180:183], v120, s[40:43], s44 offen offset:0 sc1\n\t"
        "s_waitcnt lgkmcnt(10)\n\t"
        "v_mfma_f32_32x32x16_bf16 v[48:63], v[108:111], v[184:187], v[48:63]\n\t"
        "s_add_u32 s46, s46, 12288\n\t"
        "s_add_u32 s47, s47, 1\n\t"
        "ds_read_b64_tr_b16 v[108:109], v130 offset:128\n\t"
        "ds_read_b64_tr_b16 v[110:111], v130 offset:3200\n\t"
        "s_waitcnt lgkmcnt(10)\n\t"
        "v_mfma_f32_32x32x16_bf16 v[64:79], v[184:187], v[112:115], v[64:79]\n\t"
        "s_cmp_eq_u32 s46, 110592\n\t"
        "s_cselect_b32 s46, 0, s46\n\t"
        "ds_read_b64_tr_b16 v[112:113], v129 offset:256\n\t"
        "ds_read_b64_tr_b16 v[114:115], v129 offset:3328\n\t"
        "s_waitcnt lgkmcnt(10)\n\t"
        "v_mfma_f32_32x32x16_bf16 v[80:95], v[184:187], v[116:119], v[80:95]\n\t"
        "ds_write_b32 v127, v128\n\t"
        "ds_read_b64_tr_b16 v[116:117], v130 offset:256\n\t"
        "ds_read_b64_tr_b16 v[118:119], v130 offset:3328\n\t"
        "s_waitcnt vmcnt(14) lgkmcnt(11)\n\t"
        "v_mfma_f32_32x32x16_bf16 v[0:15], v[96:99], v[188:191], v[0:15]\n\t"
        "ds_read_b32 v126, v125\n\t"
        "ds_read_b64_tr_b16 v[96:97], v129 offset:6144\n\t"
        "ds_read_b64_tr_b16 v[98:99], v129 offset:9216\n\t"
        "s_waitcnt lgkmcnt(12)\n\t"
        "v_mfma_f32_32x32x16_bf16 v[16:31], v[100:103], v[188:191], v[16:31]\n\t"
        "v_add_u32 v123, s46, v121\n\t"
        "ds_read_b64_tr_b16 v[100:101], v130 offset:6144\n\t"
        "ds_read_b64_tr_b16 v[102:103], v130 offset:9216\n\t"
        "s_waitcnt lgkmcnt(12)\n\t"
        "v_mfma_f32_32x32x16_bf16 v[32:47], v[104:107], v[188:191], v[32:47]\n\t"
        "ds_read_b64_tr_b16 v[104:105], v129 offset:6272\n\t"
        "ds_read_b64_tr_b16 v[106:107], v129 offset:9344\n\t"
        "buffer_load_dwordx4 v[184:187], v120, s[40:43], s44 offen offset:1024 sc1\n\t"
        "s_waitcnt lgkmcnt(12)\n\t"
        "v_mfma_f32_32x32x16_bf16 v[48:63], v[108:111], v[188:191], v[48:63]\n\t"
        "v_add_u32 v124, s46, v122\n\t"
        "ds_read_b64_tr_b16 v[108:109], v130 offset:6272\n\t"
        "ds_read_b64_tr_b16 v[110:111], v130 offset:9344\n\t"
        "s_waitcnt lgkmcnt(12)\n\t"
        "v_mfma_f32_32x32x16_bf16 v[64:79], v[188:191], v[112:115], v[64:79]\n\t"
        "ds_read_b64_tr_b16 v[112:113], v129 offset:6400\n\t"
        "ds_read_b64_tr_b16 v[114:115], v129 offset:9472\n\t"
        "s_waitcnt lgkmcnt(10)\n\t"
        "v_mfma_f32_32x32x16_bf16 v[80:95], v[188:191], v[116:119], v[80:95]\n\t"
        "v_cmp_gt_u32 vcc, s47, v126\n\t"
        "ds_read_b64_tr_b16 v[116:117], v130 offset:6400\n\t"
        "ds_read_b64_tr_b16 v[118:119], v130 offset:9472\n\t"
        "s_waitcnt vmcnt(14) lgkmcnt(10)\n\t"
        "v_mfma_f32_32x32x16_bf16 v[0:15], v[96:99], v[192:195], v[0:15]\n\t"
        "s_cbranch_vccnz 26f\n\t"
        "27:\n\t"
        "ds_read_b64_tr_b16 v[96:97], v123 offset:0\n\t"
        "ds_read_b64_tr_b16 v[98:99], v123 offset:3072\n\t"
        "s_waitcnt lgkmcnt(10)\n\t"
        "v_mfma_f32_32x32x16_bf16 v[16:31], v[100:103], v[192:195], v[16:31]\n\t"
        "v_add_u32 v128, 1, v128\n\t"
        "ds_read_b64_tr_b16 v[100:101], v124 offset:0\n\t"
        "ds_read_b64_tr_b16 v[102:103], v124 offset:3072\n\t"
        "s_waitcnt lgkmcnt(10)\n\t"
        "v_mfma_f32_32x32x16_bf16 v[32:47], v[104:107], v[192:195], v[32:47]\n\t"
        "ds_read_b64_tr_b16 v[104:105], v123 offset:128\n\t"
        "ds_read_b64_tr_b16 v[106:107], v123 offset:3200\n\t"
        "buffer_load_dwordx4 v[188:191], v120, s[40:43], s44 offen offset:2048 sc1\n\t"
        "s_waitcnt lgkmcnt(10)\n\t"
        "v_mfma_f32_32x32x16_bf16 v[48:63], v[108:111], v[192:195], v[48:63]\n\t"
        "s_add_u32 s46, s46, 12288\n\t"
        "s_add_u32 s47, s47, 1\n\t"
        "ds_read_b64_tr_b16 v[108:109], v124 offset:128\n\t"
        "ds_read_b64_tr_b16 v[110:111], v124 offset:3200\n\t"
        "s_waitcnt lgkmcnt(10)\n\t"
        "v_mfma_f32_32x32x16_bf16 v[64:79], v[192:195], v[112:115], v[64:79]\n\t"
        "s_cmp_eq_u32 s46, 110592\n\t"
        "s_cselect_b32 s46, 0, s46\n\t"
        "ds_read_b64_tr_b16 v[112:113], v123 offset:256\n\t"
        "ds_read_b64_tr_b16 v[114:115], v123 offset:3328\n\t"
        "s_waitcnt lgkmcnt(10)\n\t"
        "v_mfma_f32_32x32x16_bf16 v[80:95], v[192:195], v[116:119], v[80:95]\n\t"
        "ds_write_b32 v127, v128\n\t"
        "ds_read_b64_tr_b16 v[116:117], v124 offset:256\n\t"
        "ds_read_b64_tr_b16 v[118:119], v124 offset:3328\n\t"
        "s_waitcnt vmcnt(14) lgkmcnt(11)\n\t"
        "v_mfma_f32_32x32x16_bf16 v[0:15], v[96:99], v[132:135], v[0:15]\n\t"
        "ds_read_b32 v126, v125\n\t"
        "ds_read_b64_tr_b16 v[96:97], v123 offset:6144\n\t"
        "ds_read_b64_tr_b16 v[98:99], v123 offset:9216\n\t"
        "s_waitcnt lgkmcnt(12)\n\t"
        "v_mfma_f32_32x32x16_bf16 v[16:31], v[100:103], v[132:135], v[16:31]\n\t"
        "v_add_u32 v129, s46, v121\n\t"
        "ds_read_b64_tr_b16 v[100:101], v124 offset:6144\n\t"
        "ds_read_b64_tr_b16 v[102:103], v124 offset:9216\n\t"
        "s_waitcnt lgkmcnt(12)\n\t"
        "v_mfma_f32_32x32x16_bf16 v[32:47], v[104:107], v[132:135], v[32:47]\n\t"
        "ds_read_b64_tr_b16 v[104:105], v123 offset:6272\n\t"
        "ds_read_b64_tr_b16 v[106:107], v123 offset:9344\n\t"
        "buffer_load_dwordx4 v[192:195], v120, s[40:43], s44 offen offset:3072 sc1\n\t"
        "s_waitcnt lgkmcnt(12)\n\t"
        "v_mfma_f32_32x32x16_bf16 v[48:63], v[108:111], v[132:135], v[48:63]\n\t"
        "v_add_u32 v130, s46, v122\n\t"
        "ds_read_b64_tr_b16 v[108:109], v124 offset:6272\n\t"
        "ds_read_b64_tr_b16 v[110:111], v124 offset:9344\n\t"
        "s_waitcnt lgkmcnt(12)\n\t"
        "v_mfma_f32_32x32x16_bf16 v[64:79], v[132:135], v[112:115], v[64:79]\n\t"
        "ds_read_b64_tr_b16 v[112:113], v123 offset:6400\n\t"
        "ds_read_b64_tr_b16 v[114:115], v123 offset:9472\n\t"
        "s_waitcnt lgkmcnt(10)\n\t"
        "v_mfma_f32_32x32x16_bf16 v[80:95], v[132:135], v[116:119], v[80:95]\n\t"
        "v_cmp_gt_u32 vcc, s47, v126\n\t"
        "ds_read_b64_tr_b16 v[116:117], v124 offset:6400\n\t"
        "ds_read_b64_tr_b16 v[118:119], v124 offset:9472\n\t"
        "s_waitcnt vmcnt(14) lgkmcnt(10)\n\t"
        "v_mfma_f32_32x32x16_bf16 v[0:15], v[96:99], v[136:139], v[0:15]\n\t"
        "s_cbranch_vccnz 28f\n\t"
        "29:\n\t"
        "ds_read_b64_tr_b16 v[96:97], v129 offset:0\n\t"
        "ds_read_b64_tr_b16 v[98:99], v129 offset:3072\n\t"
        "s_waitcnt lgkmcnt(10)\n\t"
        "v_mfma_f32_32x32x16_bf16 v[16:31], v[100:103], v[136:139], v[16:31]\n\t"
        "v_add_u32 v128, 1, v128\n\t"
        "ds_read_b64_tr_b16 v[100:101], v130 offset:0\n\t"
        "ds_read_b64_tr_b16 v[102:103], v130 offset:3072\n\t"
        "s_waitcnt lgkmcnt(10)\n\t"
        "v_mfma_f32_32x32x16_bf16 v[32:47], v[104:107], v[136:139], v[32:47]\n\t"
        "ds_read_b64_tr_b16 v[104:105], v129 offset:128\n\t"
        "ds_read_b64_tr_b16 v[106:107], v129 offset:3200\n\t"
        "s_add_u32 s44, s44, 0x1000\n\t"
        "s_and_b32 s44, s44, 0xffff\n\t"
        "buffer_load_dwordx4 v[132:135], v120, s[40:43], s44 offen offset:0 sc1\n\t"
        "s_waitcnt lgkmcnt(10)\n\t"
        "v_mfma_f32_32x32x16_bf16 v[48:63], v[108:111], v[136:139], v[48:63]\n\t"
        "s_add_u32 s46, s46, 12288\n\t"
        "s_add_u32 s47, s47, 1\n\t"
        "ds_read_b64_tr_b16 v[108:109], v130 offset:128\n\t"
        "ds_read_b64_tr_b16 v[110:111], v130 offset:3200\n\t"
        "s_waitcnt lgkmcnt(10)\n\t"
        "v_mfma_f32_32x32x16_bf16 v[64:79], v[136:139], v[112:115], v[64:79]\n\t"
        "s_cmp_eq_u32 s46, 110592\n\t"
        "s_cselect_b32 s46, 0, s46\n\t"
        "ds_read_b64_tr_b16 v[112:113], v129 offset:256\n\t"
        "ds_read_b64_tr_b16 v[114:115], v129 offset:3328\n\t"
        "s_waitcnt lgkmcnt(10)\n\t"
        "v_mfma_f32_32x32x16_bf16 v[80:95], v[136:139], v[116:119], v[80:95]\n\t"
        "ds_write_b32 v127, v128\n\t"
        "ds_read_b64_tr_b16 v[116:117], v130 offset:256\n\t"
        "ds_read_b64_tr_b16 v[118:119], v130 offset:3328\n\t"
        "s_waitcnt vmcnt(14) lgkmcnt(11)\n\t"
        "v_mfma_f32_32x32x16_bf16 v[0:15], v[96:99], v[140:143], v[0:15]\n\t"
        "ds_read_b32 v126, v125\n\t"
        "ds_read_b64_tr_b16 v[96:97], v129 offset:6144\n\t"
        "ds_read_b64_tr_b16 v[98:99], v129 offset:9216\n\t"
        "s_waitcnt lgkmcnt(12)\n\t"
        "v_mfma_f32_32x32x16_bf16 v[16:31], v[100:103], v[140:143], v[16:31]\n\t"
        "v_add_u32 v123, s46, v121\n\t"
        "ds_read_b64_tr_b16 v[100:101], v130 offset:6144\n\t"
        "ds_read_b64_tr_b16 v[102:103], v130 offset:9216\n\t"
        "s_waitcnt lgkmcnt(12)\n\t"
        "v_mfma_f32_32x32x16_bf16 v[32:47], v[104:107], v[140:143], v[32:47]\n\t"
        "ds_read_b64_tr_b16 v[104:105], v129 offset:6272\n\t"
        "ds_read_b64_tr_b16 v[106:107], v129 offset:9344\n\t"
        "buffer_load_dwordx4 v[136:139], v120, s[40:43], s44 offen offset:1024 sc1\n\t"
        "s_waitcnt lgkmcnt(12)\n\t"
        "v_mfma_f32_32x32x16_bf16 v[48:63], v[108:111], v[140:143], v[48:63]\n\t"
        "v_add_u32 v124, s46, v122\n\t"
        "ds_read_b64_tr_b16 v[108:109], v130 offset:6272\n\t"
        "ds_read_b64_tr_b16 v[110:111], v130 offset:9344\n\t"
        "s_waitcnt lgkmcnt(12)\n\t"
        "v_mfma_f32_32x32x16_bf16 v[64:79], v[140:143], v[112:115], v[64:79]\n\t"
        "ds_read_b64_tr_b16 v[112:113], v129 offset:6400\n\t"
        "ds_read_b64_tr_b16 v[114:115], v129 offset:9472\n\t"
        "s_waitcnt lgkmcnt(10)\n\t"
        "v_mfma_f32_32x32x16_bf16 v[80:95], v[140:143], v[116:119], v[80:95]\n\t"
        "v_cmp_gt_u32 vcc, s47, v126\n\t"
        "ds_read_b64_tr_b16 v[116:117], v130 offset:6400\n\t"
        "ds_read_b64_tr_b16 v[118:119], v130 offset:9472\n\t"
        "s_waitcnt vmcnt(14) lgkmcnt(10)\n\t"
        "v_mfma_f32_32x32x16_bf16 v[0:15], v[96:99], v[144:147], v[0:15]\n\t"
        "s_cbranch_vccnz 30f\n\t"
        "31:\n\t"
        "ds_read_b64_tr_b16 v[96:97], v123 offset:0\n\t"
        "ds_read_b64_tr_b16 v[98:99], v123 offset:3072\n\t"
        "s_waitcnt lgkmcnt(10)\n\t"
        "v_mfma_f32_32x32x16_bf16 v[16:31], v[100:103], v[144:147], v[16:31]\n\t"
        "v_add_u32 v128, 1, v128\n\t"
        "ds_read_b64_tr_b16 v[100:101], v124 offset:0\n\t"
        "ds_read_b64_tr_b16 v[102:103], v124 offset:3072\n\t"
        "s_waitcnt lgkmcnt(10)\n\t"
        "v_mfma_f32_32x32x16_bf16 v[32:47], v[104:107], v[144:147], v[32:47]\n\t"
        "ds_read_b64_tr_b16 v[104:105], v123 offset:128\n\t"
        "ds_read_b64_tr_b16 v[106:107], v123 offset:3200\n\t"
        "buffer_load_dwordx4 v[140:143], v120, s[40:43], s44 offen offset:2048 sc1\n\t"
        "s_waitcnt lgkmcnt(10)\n\t"
        "v_mfma_f32_32x32x16_bf16 v[48:63], v[108:111], v[144:147], v[48:63]\n\t"
        "s_add_u32 s46, s46, 12288\n\t"
        "s_add_u32 s47, s47, 1\n\t"
        "ds_read_b64_tr_b16 v[108:109], v124 offset:128\n\t"
        "ds_read_b64_tr_b16 v[110:111], v124 offset:3200\n\t"
        "s_waitcnt lgkmcnt(10)\n\t"
        "v_mfma_f32_32x32x16_bf16 v[64:79], v[144:147], v[112:115], v[64:79]\n\t"
        "s_cmp_eq_u32 s46, 110592\n\t"
        "s_cselect_b32 s46, 0, s46\n\t"
        "ds_read_b64_tr_b16 v[112:113], v123 offset:256\n\t"
        "ds_read_b64_tr_b16 v[114:115], v123 offset:3328\n\t"
        "s_waitcnt lgkmcnt(10)\n\t"
        "v_mfma_f32_32x32x16_bf16 v[80:95], v[144:147], v[116:119], v[80:95]\n\t"
        "ds_write_b32 v127, v128\n\t"
        "ds_read_b64_tr_b16 v[116:117], v124 offset:256\n\t"
        "ds_read_b64_tr_b16 v[118:119], v124 offset:3328\n\t"
        "s_sub_u32 s45, s45, 1\n\t"
        "s_cmp_lg_u32 s45, 0\n\t"
        "s_cbranch_scc1 1b\n\t"
        "s_waitcnt vmcnt(14) lgkmcnt(11)\n\t"
        "v_mfma_f32_32x32x16_bf16 v[0:15], v[96:99], v[148:151], v[0:15]\n\t"
        "ds_read_b32 v126, v125\n\t"
        "s_add_u32 s47, s47, 4\n\t"
        "ds_read_b64_tr_b16 v[96:97], v123 offset:6400\n\t"
        "ds_read_b64_tr_b16 v[98:99], v123 offset:9472\n\t"
        "s_waitcnt lgkmcnt(12)\n\t"
        "v_mfma_f32_32x32x16_bf16 v[16:31], v[100:103], v[148:151], v[16:31]\n\t"
        "ds_read_b64_tr_b16 v[100:101], v124 offset:6400\n\t"
        "ds_read_b64_tr_b16 v[102:103], v124 offset:9472\n\t"
        "s_waitcnt lgkmcnt(4)\n\t"
        "v_cmp_gt_u32 vcc, s47, v126\n\t"
        "v_mfma_f32_32x32x16_bf16 v[32:47], v[104:107], v[148:151], v[32:47]\n\t"
        "s_cbranch_vccnz 74f\n\t"
        "75:\n\t"
        "ds_read_b64_tr_b16 v[104:105], v121 offset:256\n\t"
        "ds_read_b64_tr_b16 v[106:107], v121 offset:3328\n\t"
        "s_waitcnt lgkmcnt(12)\n\t"
        "v_mfma_f32_32x32x16_bf16 v[48:63], v[108:111], v[148:151], v[48:63]\n\t"
        "ds_read_b64_tr_b16 v[108:109], v122 offset:256\n\t"
        "ds_read_b64_tr_b16 v[110:111], v122 offset:3328\n\t"
        "s_waitcnt lgkmcnt(12)\n\t"
        "v_mfma_f32_32x32x16_bf16 v[64:79], v[148:151], v[112:115], v[64:79]\n\t"
        "ds_read_b64_tr_b16 v[112:113], v121 offset:6400\n\t"
        "ds_read_b64_tr_b16 v[114:115], v121 offset:9472\n\t"
        "s_waitcnt lgkmcnt(11)\n\t"
        "v_mfma_f32_32x32x16_bf16 v[80:95], v[148:151], v[116:119], v[80:95]\n\t"
        "ds_read_b64_tr_b16 v[116:117], v122 offset:6400\n\t"
        "ds_read_b64_tr_b16 v[118:119], v122 offset:9472\n\t"
        "s_waitcnt vmcnt(13) lgkmcnt(10)\n\t"
        "v_mfma_f32_32x32x16_bf16 v[64:79], v[152:155], v[96:99], v[64:79]\n\t"
        "ds_read_b64_tr_b16 v[96:97], v121 offset:12544\n\t"
        "ds_read_b64_tr_b16 v[98:99], v121 offset:15616\n\t"
        "s_waitcnt lgkmcnt(10)\n\t"
        "v_mfma_f32_32x32x16_bf16 v[80:95], v[152:155], v[100:103], v[80:95]\n\t"
        "ds_read_b64_tr_b16 v[100:101], v122 offset:12544\n\t"
        "ds_read_b64_tr_b16 v[102:103], v122 offset:15616\n\t"
        "s_waitcnt vmcnt(12) lgkmcnt(10)\n\t"
        "v_mfma_f32_32x32x16_bf16 v[64:79], v[156:159], v[104:107], v[64:79]\n\t"
        "ds_read_b128 v[196:199], v215 offset:0\n\t"
        "ds_read_b64_tr_b16 v[104:105], v121 offset:18688\n\t"
        "ds_read_b64_tr_b16 v[106:107], v121 offset:21760\n\t"
        "s_waitcnt lgkmcnt(11)\n\t"
        "v_mfma_f32_32x32x16_bf16 v[80:95], v[156:159], v[108:111], v[80:95]\n\t"
        "ds_read_b128 v[200:203], v215 offset:32\n\t"
        "ds_read_b64_tr_b16 v[108:109], v122 offset:18688\n\t"
        "ds_read_b64_tr_b16 v[110:111], v122 offset:21760\n\t"
        "s_waitcnt vmcnt(11) lgkmcnt(12)\n\t"
        "v_mfma_f32_32x32x16_bf16 v[64:79], v[160:163], v[112:115], v[64:79]\n\t"
        "ds_read_b128 v[204:207], v215 offset:64\n\t"
        "ds_read_b64_tr_b16 v[112:113], v121 offset:24832\n\t"
        "ds_read_b64_tr_b16 v[114:115], v121 offset:27904\n\t"
        "s_waitcnt lgkmcnt(13)\n\t"
        "v_mfma_f32_32x32x16_bf16 v[80:95], v[160:163], v[116:119], v[80:95]\n\t"
        "ds_read_b128 v[208:211], v215 offset:96\n\t"
        "ds_read_b64_tr_b16 v[116:117], v122 offset:24832\n\t"
        "ds_read_b64_tr_b16 v[118:119], v122 offset:27904\n\t"
        "s_waitcnt vmcnt(10) lgkmcnt(14)\n\t"
        "v_mfma_f32_32x32x16_bf16 v[64:79], v[164:167], v[96:99], v[64:79]\n\t"
        "ds_read_b64_tr_b16 v[96:97], v121 offset:30976\n\t"
        "ds_read_b64_tr_b16 v[98:99], v121 offset:34048\n\t"
        "s_waitcnt lgkmcnt(14)\n\t"
        "v_mfma_f32_32x32x16_bf16 v[80:95], v[164:167], v[100:103], v[80:95]\n\t"
        "ds_read_b64_tr_b16 v[100:101], v122 offset:30976\n\t"
        "ds_read_b64_tr_b16 v[102:103], v122 offset:34048\n\t"
        "s_waitcnt vmcnt(9) lgkmcnt(13)\n\t"
        "v_mfma_f32_32x32x16_bf16 v[64:79], v[168:171], v[104:107], v[64:79]\n\t"
        "ds_read_b64_tr_b16 v[104:105], v121 offset:37120\n\t"
        "ds_read_b64_tr_b16 v[106:107], v121 offset:40192\n\t"
        "s_waitcnt lgkmcnt(12)\n\t"
        "v_mfma_f32_32x32x16_bf16 v[80:95], v[168:171], v[108:111], v[80:95]\n\t"
        "ds_read_b64_tr_b16 v[108:109], v122 offset:37120\n\t"
        "ds_read_b64_tr_b16 v[110:111], v122 offset:40192\n\t"
        "s_waitcnt vmcnt(8) lgkmcnt(11)\n\t"
        "v_mfma_f32_32x32x16_bf16 v[64:79], v[172:175], v[112:115], v[64:79]\n\t"
        "ds_read_b64_tr_b16 v[112:113], v121 offset:43264\n\t"
        "ds_read_b64_tr_b16 v[114:115], v121 offset:46336\n\t"
        "s_waitcnt lgkmcnt(10)\n\t"
        "v_mfma_f32_32x32x16_bf16 v[80:95], v[172:175], v[116:119], v[80:95]\n\t"
        "ds_read_b64_tr_b16 v[116:117], v122 offset:43264\n\t"
        "ds_read_b64_tr_b16 v[118:119], v122 offset:46336\n\t"
        "s_waitcnt vmcnt(7) lgkmcnt(10)\n\t"
        "v_mfma_f32_32x32x16_bf16 v[64:79], v[176:179], v[96:99], v[64:79]\n\t"
        "ds_read_b64_tr_b16 v[96:97], v121 offset:49408\n\t"
        "ds_read_b64_tr_b16 v[98:99], v121 offset:52480\n\t"
        "s_waitcnt lgkmcnt(10)\n\t"
        "v_mfma_f32_32x32x16_bf16 v[80:95], v[176:179], v[100:103], v[80:95]\n\t"
        "ds_read_b64_tr_b16 v[100:101], v122 offset:49408\n\t"
        "ds_read_b64_tr_b16 v[102:103], v122 offset:52480\n\t"
        "s_waitcnt vmcnt(6) lgkmcnt(10)\n\t"
        "v_mfma_f32_32x32x16_bf16 v[64:79], v[180:183], v[104:107], v[64:79]\n\t"
        "ds_read_b64_tr_b16 v[104:105], v121 offset:55552\n\t"
        "ds_read_b64_tr_b16 v[106:107], v121 offset:58624\n\t"
        "s_waitcnt lgkmcnt(10)\n\t"
        "v_mfma_f32_32x32x16_bf16 v[80:95], v[180:183], v[108:111], v[80:95]\n\t"
        "ds_read_b64_tr_b16 v[108:109], v122 offset:55552\n\t"
        "ds_read_b64_tr_b16 v[110:111], v122 offset:58624\n\t"
        "s_waitcnt vmcnt(5) lgkmcnt(10)\n\t"
        "v_mfma_f32_32x32x16_bf16 v[64:79], v[184:187], v[112:115], v[64:79]\n\t"
        "ds_read_b64_tr_b16 v[112:113], v123 offset:6272\n\t"
        "ds_read_b64_tr_b16 v[114:115], v123 offset:9344\n\t"
        "s_waitcnt lgkmcnt(10)\n\t"
        "v_mfma_f32_32x32x16_bf16 v[80:95], v[184:187], v[116:119], v[80:95]\n\t"
        "ds_read_b64_tr_b16 v[116:117], v124 offset:6272\n\t"
        "ds_read_b64_tr_b16 v[118:119], v124 offset:9344\n\t"
        "s_waitcnt vmcnt(4) lgkmcnt(10)\n\t"
        "v_mfma_f32_32x32x16_bf16 v[64:79], v[188:191], v[96:99], v[64:79]\n\t"
        "ds_read_b64_tr_b16 v[96:97], v121 offset:128\n\t"
        "ds_read_b64_tr_b16 v[98:99], v121 offset:3200\n\t"
        "s_waitcnt lgkmcnt(10)\n\t"
        "v_mfma_f32_32x32x16_bf16 v[80:95], v[188:191], v[100:103], v[80:95]\n\t"
        "ds_read_b64_tr_b16 v[100:101], v122 offset:128\n\t"
        "ds_read_b64_tr_b16 v[102:103], v122 offset:3200\n\t"
        "s_waitcnt vmcnt(0) lgkmcnt(10)\n\t"
        "v_mfma_f32_32x32x16_bf16 v[64:79], v[192:195], v[104:107], v[64:79]\n\t"
        "ds_read_b64_tr_b16 v[104:105], v121 offset:6272\n\t"
        "ds_read_b64_tr_b16 v[106:107], v121 offset:9344\n\t"
        "s_waitcnt lgkmcnt(10)\n\t"
        "v_mfma_f32_32x32x16_bf16 v[80:95], v[192:195], v[108:111], v[80:95]\n\t"
        "ds_read_b64_tr_b16 v[108:109], v122 offset:6272\n\t"
        "ds_read_b64_tr_b16 v[110:111], v122 offset:9344\n\t"
        "s_waitcnt lgkmcnt(10)\n\t"
        "v_mfma_f32_32x32x16_bf16 v[32:47], v[112:115], v[152:155], v[32:47]\n\t"
        "ds_read_b64_tr_b16 v[112:113], v121 offset:12416\n\t"
        "ds_read_b64_tr_b16 v[114:115], v121 offset:15488\n\t"
        "s_waitcnt lgkmcnt(10)\n\t"
        "v_mfma_f32_32x32x16_bf16 v[48:63], v[116:119], v[152:155], v[48:63]\n\t"
        "ds_read_b64_tr_b16 v[116:117], v122 offset:12416\n\t"
        "ds_read_b64_tr_b16 v[118:119], v122 offset:15488\n\t"
        "v_mul_f32 v64, v196, v64\n\t"
        "v_mul_f32 v65, v197, v65\n\t"
        "v_mul_f32 v66, v198, v66\n\t"
        "s_waitcnt lgkmcnt(10)\n\t"
        "v_mfma_f32_32x32x16_bf16 v[32:47], v[96:99], v[156:159], v[32:47]\n\t"
        "ds_read_b64_tr_b16 v[96:97], v121 offset:18560\n\t"
        "ds_read_b64_tr_b16 v[98:99], v121 offset:21632\n\t"
        "v_mul_f32 v67, v199, v67\n\t"
        "v_mul_f32 v68, v200, v68\n\t"
        "v_mul_f32 v69, v201, v69\n\t"
        "s_waitcnt lgkmcnt(10)\n\t"
        "v_mfma_f32_32x32x16_bf16 v[48:63], v[100:103], v[156:159], v[48:63]\n\t"
        "ds_read_b64_tr_b16 v[100:101], v122 offset:18560\n\t"
        "ds_read_b64_tr_b16 v[102:103], v122 offset:21632\n\t"
        "v_mul_f32 v70, v202, v70\n\t"
        "v_mul_f32 v71, v203, v71\n\t"
        "v_cvt_pk_bf16_f32 v64, v64, v65\n\t"
        "s_waitcnt lgkmcnt(10)\n\t"
        "v_mfma_f32_32x32x16_bf16 v[32:47], v[104:107], v[160:163], v[32:47]\n\t"
        "ds_read_b64_tr_b16 v[104:105], v121 offset:24704\n\t"
        "ds_read_b64_tr_b16 v[106:107], v121 offset:27776\n\t"
        "v_cvt_pk_bf16_f32 v65, v66, v67\n\t"
        "v_cvt_pk_bf16_f32 v66, v68, v69\n\t"
        "v_cvt_pk_bf16_f32 v67, v70, v71\n\t"
        "s_waitcnt lgkmcnt(10)\n\t"
        "v_mfma_f32_32x32x16_bf16 v[48:63], v[108:111], v[160:163], v[48:63]\n\t"
        "ds_read_b64_tr_b16 v[108:109], v122 offset:24704\n\t"
        "ds_read_b64_tr_b16 v[110:111], v122 offset:27776\n\t"
        "v_mul_f32 v72, v204, v72\n\t"
        "v_mul_f32 v73, v205, v73\n\t"
        "v_mul_f32 v74, v206, v74\n\t"
        "s_waitcnt lgkmcnt(10)\n\t"
        "v_mfma_f32_32x32x16_bf16 v[32:47], v[112:115], v[164:167], v[32:47]\n\t"
        "ds_read_b64_tr_b16 v[112:113], v121 offset:30848\n\t"
        "ds_read_b64_tr_b16 v[114:115], v121 offset:33920\n\t"
        "v_mul_f32 v75, v207, v75\n\t"
        "v_mul_f32 v76, v208, v76\n\t"
        "v_mul_f32 v77, v209, v77\n\t"
        "s_waitcnt lgkmcnt(10)\n\t"
        "v_mfma_f32_32x32x16_bf16 v[48:63], v[116:119], v[164:167], v[48:63]\n\t"
        "ds_read_b64_tr_b16 v[116:117], v122 offset:30848\n\t"
        "ds_read_b64_tr_b16 v[118:119], v122 offset:33920\n\t"
        "v_mul_f32 v78, v210, v78\n\t"
        "v_mul_f32 v79, v211, v79\n\t"
        "v_cvt_pk_bf16_f32 v68, v72, v73\n\t"
        "s_waitcnt lgkmcnt(10)\n\t"
        "v_mfma_f32_32x32x16_bf16 v[32:47], v[96:99], v[168:171], v[32:47]\n\t"
        "ds_read_b64_tr_b16 v[96:97], v121 offset:36992\n\t"
        "ds_read_b64_tr_b16 v[98:99], v121 offset:40064\n\t"
        "v_cvt_pk_bf16_f32 v69, v74, v75\n\t"
        "v_cvt_pk_bf16_f32 v70, v76, v77\n\t"
        "v_cvt_pk_bf16_f32 v71, v78, v79\n\t"
        "s_waitcnt lgkmcnt(10)\n\t"
        "v_mfma_f32_32x32x16_bf16 v[48:63], v[100:103], v[168:171], v[48:63]\n\t"
        "ds_read_b64_tr_b16 v[100:101], v122 offset:36992\n\t"
        "ds_read_b64_tr_b16 v[102:103], v122 offset:40064\n\t"
        "v_permlane32_swap_b32 v64, v66\n\t"
        "v_permlane32_swap_b32 v65, v67\n\t"
        "v_permlane32_swap_b32 v68, v70\n\t"
        "s_waitcnt lgkmcnt(10)\n\t"
        "v_mfma_f32_32x32x16_bf16 v[32:47], v[104:107], v[172:175], v[32:47]\n\t"
        "ds_read_b64_tr_b16 v[104:105], v121 offset:43136\n\t"
        "ds_read_b64_tr_b16 v[106:107], v121 offset:46208\n\t"
        "v_permlane32_swap_b32 v69, v71\n\t"
        "buffer_store_dwordx4 v[64:67], v213, s[40:43], 0 offen offset:2048 sc1\n\t"
        "buffer_store_dwordx4 v[68:71], v213, s[40:43], 0 offen offset:3072 sc1\n\t"
        "s_waitcnt lgkmcnt(10)\n\t"
        "v_mfma_f32_32x32x16_bf16 v[48:63], v[108:111], v[172:175], v[48:63]\n\t"
        "ds_read_b64_tr_b16 v[108:109], v122 offset:43136\n\t"
        "ds_read_b64_tr_b16 v[110:111], v122 offset:46208\n\t"
        "v_mul_f32 v80, v196, v80\n\t"
        "v_mul_f32 v81, v197, v81\n\t"
        "v_mul_f32 v82, v198, v82\n\t"
        "s_waitcnt lgkmcnt(10)\n\t"
        "v_mfma_f32_32x32x16_bf16 v[32:47], v[112:115], v[176:179], v[32:47]\n\t"
        "ds_read_b64_tr_b16 v[112:113], v121 offset:49280\n\t"
        "ds_read_b64_tr_b16 v[114:115], v121 offset:52352\n\t"
        "v_mul_f32 v83, v199, v83\n\t"
        "v_mul_f32 v84, v200, v84\n\t"
        "v_mul_f32 v85, v201, v85\n\t"
        "s_waitcnt lgkmcnt(10)\n\t"
        "v_mfma_f32_32x32x16_bf16 v[48:63], v[116:119], v[176:179], v[48:63]\n\t"
        "ds_read_b64_tr_b16 v[116:117], v122 offset:49280\n\t"
        "ds_read_b64_tr_b16 v[118:119], v122 offset:52352\n\t"
        "v_mul_f32 v86, v202, v86\n\t"
        "v_mul_f32 v87, v203, v87\n\t"
        "v_cvt_pk_bf16_f32 v80, v80, v81\n\t"
        "s_waitcnt lgkmcnt(10)\n\t"
        "v_mfma_f32_32x32x16_bf16 v[32:47], v[96:99], v[180:183], v[32:47]\n\t"
        "ds_read_b64_tr_b16 v[96:97], v121 offset:55424\n\t"
        "ds_read_b64_tr_b16 v[98:99], v121 offset:58496\n\t"
        "v_cvt_pk_bf16_f32 v81, v82, v83\n\t"
        "v_cvt_pk_bf16_f32 v82, v84, v85\n\t"
        "v_cvt_pk_bf16_f32 v83, v86, v87\n\t"
        "s_waitcnt lgkmcnt(10)\n\t"
        "v_mfma_f32_32x32x16_bf16 v[48:63], v[100:103], v[180:183], v[48:63]\n\t"
        "ds_read_b64_tr_b16 v[100:101], v122 offset:55424\n\t"
        "ds_read_b64_tr_b16 v[102:103], v122 offset:58496\n\t"
        "v_mul_f32 v88, v204, v88\n\t"
        "v_mul_f32 v89, v205, v89\n\t"
        "v_mul_f32 v90, v206, v90\n\t"
        "s_waitcnt lgkmcnt(10)\n\t"
        "v_mfma_f32_32x32x16_bf16 v[32:47], v[104:107], v[184:187], v[32:47]\n\t"
        "ds_read_b64_tr_b16 v[104:105], v123 offset:6144\n\t"
        "ds_read_b64_tr_b16 v[106:107], v123 offset:9216\n\t"
        "v_mul_f32 v91, v207, v91\n\t"
        "v_mul_f32 v92, v208, v92\n\t"
        "v_mul_f32 v93, v209, v93\n\t"
        "s_waitcnt lgkmcnt(10)\n\t"
        "v_mfma_f32_32x32x16_bf16 v[48:63], v[108:111], v[184:187], v[48:63]\n\t"
        "ds_read_b64_tr_b16 v[108:109], v124 offset:6144\n\t"
        "ds_read_b64_tr_b16 v[110:111], v124 offset:9216\n\t"
        "v_mul_f32 v94, v210, v94\n\t"
        "v_mul_f32 v95, v211, v95\n\t"
        "v_cvt_pk_bf16_f32 v84, v88, v89\n\t"
        "s_waitcnt lgkmcnt(10)\n\t"
        "v_mfma_f32_32x32x16_bf16 v[32:47], v[112:115], v[188:191], v[32:47]\n\t"
        "ds_read_b64_tr_b16 v[112:113], v121 offset:0\n\t"
        "ds_read_b64_tr_b16 v[114:115], v121 offset:3072\n\t"
        "v_cvt_pk_bf16_f32 v85, v90, v91\n\t"
        "v_cvt_pk_bf16_f32 v86, v92, v93\n\t"
        "v_cvt_pk_bf16_f32 v87, v94, v95\n\t"
        "s_waitcnt lgkmcnt(10)\n\t"
        "v_mfma_f32_32x32x16_bf16 v[48:63], v[116:119], v[188:191], v[48:63]\n\t"
        "ds_read_b64_tr_b16 v[116:117], v122 offset:0\n\t"
        "ds_read_b64_tr_b16 v[118:119], v122 offset:3072\n\t"
        "v_permlane32_swap_b32 v80, v82\n\t"
        "v_permlane32_swap_b32 v81, v83\n\t"
        "v_permlane32_swap_b32 v84, v86\n\t"
        "s_waitcnt lgkmcnt(10)\n\t"
        "v_mfma_f32_32x32x16_bf16 v[32:47], v[96:99], v[192:195], v[32:47]\n\t"
        "ds_read_b64_tr_b16 v[96:97], v121 offset:6144\n\t"
        "ds_read_b64_tr_b16 v[98:99], v121 offset:9216\n\t"
        "v_permlane32_swap_b32 v85, v87\n\t"
        "buffer_store_dwordx4 v[80:83], v214, s[40:43], 0 offen offset:2048 sc1\n\t"
        "buffer_store_dwordx4 v[84:87], v214, s[40:43], 0 offen offset:3072 sc1\n\t"
        "s_waitcnt lgkmcnt(10)\n\t"
        "v_mfma_f32_32x32x16_bf16 v[48:63], v[100:103], v[192:195], v[48:63]\n\t"
        "ds_read_b64_tr_b16 v[100:101], v122 offset:6144\n\t"
        "ds_read_b64_tr_b16 v[102:103], v122 offset:9216\n\t"
        "s_waitcnt lgkmcnt(10)\n\t"
        "v_mfma_f32_32x32x16_bf16 v[0:15], v[104:107], v[152:155], v[0:15]\n\t"
        "ds_read_b64_tr_b16 v[104:105], v121 offset:12288\n\t"
        "ds_read_b64_tr_b16 v[106:107], v121 offset:15360\n\t"
        "s_waitcnt lgkmcnt(10)\n\t"
        "v_mfma_f32_32x32x16_bf16 v[16:31], v[108:111], v[152:155], v[16:31]\n\t"
        "ds_read_b64_tr_b16 v[108:109], v122 offset:12288\n\t"
        "ds_read_b64_tr_b16 v[110:111], v122 offset:15360\n\t"
        "v_mul_f32 v32, v212, v32\n\t"
        "v_mul_f32 v33, v212, v33\n\t"
        "v_mul_f32 v34, v212, v34\n\t"
        "s_waitcnt lgkmcnt(10)\n\t"
        "v_mfma_f32_32x32x16_bf16 v[0:15], v[112:115], v[156:159], v[0:15]\n\t"
        "ds_read_b64_tr_b16 v[112:113], v121 offset:18432\n\t"
        "ds_read_b64_tr_b16 v[114:115], v121 offset:21504\n\t"
        "v_mul_f32 v35, v212, v35\n\t"
        "v_mul_f32 v36, v212, v36\n\t"
        "v_mul_f32 v37, v212, v37\n\t"
        "s_waitcnt lgkmcnt(10)\n\t"
        "v_mfma_f32_32x32x16_bf16 v[16:31], v[116:119], v[156:159], v[16:31]\n\t"
        "ds_read_b64_tr_b16 v[116:117], v122 offset:18432\n\t"
        "ds_read_b64_tr_b16 v[118:119], v122 offset:21504\n\t"
        "v_mul_f32 v38, v212, v38\n\t"
        "v_mul_f32 v39, v212, v39\n\t"
        "v_cvt_pk_bf16_f32 v32, v32, v33\n\t"
        "s_waitcnt lgkmcnt(10)\n\t"
        "v_mfma_f32_32x32x16_bf16 v[0:15], v[96:99], v[160:163], v[0:15]\n\t"
        "ds_read_b64_tr_b16 v[96:97], v121 offset:24576\n\t"
        "ds_read_b64_tr_b16 v[98:99], v121 offset:27648\n\t"
        "v_cvt_pk_bf16_f32 v33, v34, v35\n\t"
        "v_cvt_pk_bf16_f32 v34, v36, v37\n\t"
        "v_cvt_pk_bf16_f32 v35, v38, v39\n\t"
        "s_waitcnt lgkmcnt(10)\n\t"
        "v_mfma_f32_32x32x16_bf16 v[16:31], v[100:103], v[160:163], v[16:31]\n\t"
        "ds_read_b64_tr_b16 v[100:101], v122 offset:24576\n\t"
        "ds_read_b64_tr_b16 v[102:103], v122 offset:27648\n\t"
        "v_mul_f32 v40, v212, v40\n\t"
        "v_mul_f32 v41, v212, v41\n\t"
        "v_mul_f32 v42, v212, v42\n\t"
        "s_waitcnt lgkmcnt(10)\n\t"
        "v_mfma_f32_32x32x16_bf16 v[0:15], v[104:107], v[164:167], v[0:15]\n\t"
        "ds_read_b64_tr_b16 v[104:105], v121 offset:30720\n\t"
        "ds_read_b64_tr_b16 v[106:107], v121 offset:33792\n\t"
        "v_mul_f32 v43, v212, v43\n\t"
        "v_mul_f32 v44, v212, v44\n\t"
        "v_mul_f32 v45, v212, v45\n\t"
        "s_waitcnt lgkmcnt(10)\n\t"
        "v_mfma_f32_32x32x16_bf16 v[16:31], v[108:111], v[164:167], v[16:31]\n\t"
        "ds_read_b64_tr_b16 v[108:109], v122 offset:30720\n\t"
        "ds_read_b64_tr_b16 v[110:111], v122 offset:33792\n\t"
        "v_mul_f32 v46, v212, v46\n\t"
        "v_mul_f32 v47, v212, v47\n\t"
        "v_cvt_pk_bf16_f32 v36, v40, v41\n\t"
        "s_waitcnt lgkmcnt(10)\n\t"
        "v_mfma_f32_32x32x16_bf16 v[0:15], v[112:115], v[168:171], v[0:15]\n\t"
        "ds_read_b64_tr_b16 v[112:113], v121 offset:36864\n\t"
        "ds_read_b64_tr_b16 v[114:115], v121 offset:39936\n\t"
        "v_cvt_pk_bf16_f32 v37, v42, v43\n\t"
        "v_cvt_pk_bf16_f32 v38, v44, v45\n\t"
        "v_cvt_pk_bf16_f32 v39, v46, v47\n\t"
        "s_waitcnt lgkmcnt(10)\n\t"
        "v_mfma_f32_32x32x16_bf16 v[16:31], v[116:119], v[168:171], v[16:31]\n\t"
        "ds_read_b64_tr_b16 v[116:117], v122 offset:36864\n\t"
        "ds_read_b64_tr_b16 v[118:119], v122 offset:39936\n\t"
        "buffer_store_dwordx4 v[32:35], v213, s[40:43], 0 offen offset:0 sc1\n\t"
        "buffer_store_dwordx4 v[36:39], v213, s[40:43], 0 offen offset:1024 sc1\n\t"
        "v_mul_f32 v48, v212, v48\n\t"
        "s_waitcnt lgkmcnt(10)\n\t"
        "v_mfma_f32_32x32x16_bf16 v[0:15], v[96:99], v[172:175], v[0:15]\n\t"
        "ds_read_b64_tr_b16 v[96:97], v121 offset:43008\n\t"
        "ds_read_b64_tr_b16 v[98:99], v121 offset:46080\n\t"
        "v_mul_f32 v49, v212, v49\n\t"
        "v_mul_f32 v50, v212, v50\n\t"
        "v_mul_f32 v51, v212, v51\n\t"
        "s_waitcnt lgkmcnt(10)\n\t"
        "v_mfma_f32_32x32x16_bf16 v[16:31], v[100:103], v[172:175], v[16:31]\n\t"
        "ds_read_b64_tr_b16 v[100:101], v122 offset:43008\n\t"
        "ds_read_b64_tr_b16 v[102:103], v122 offset:46080\n\t"
        "v_mul_f32 v52, v212, v52\n\t"
        "v_mul_f32 v53, v212, v53\n\t"
        "v_mul_f32 v54, v212, v54\n\t"
        "s_waitcnt lgkmcnt(10)\n\t"
        "v_mfma_f32_32x32x16_bf16 v[0:15], v[104:107], v[176:179], v[0:15]\n\t"
        "ds_read_b64_tr_b16 v[104:105], v121 offset:49152\n\t"
        "ds_read_b64_tr_b16 v[106:107], v121 offset:52224\n\t"
        "v_mul_f32 v55, v212, v55\n\t"
        "v_cvt_pk_bf16_f32 v48, v48, v49\n\t"
        "v_cvt_pk_bf16_f32 v49, v50, v51\n\t"
        "s_waitcnt lgkmcnt(10)\n\t"
        "v_mfma_f32_32x32x16_bf16 v[16:31], v[108:111], v[176:179], v[16:31]\n\t"
        "ds_read_b64_tr_b16 v[108:109], v122 offset:49152\n\t"
        "ds_read_b64_tr_b16 v[110:111], v122 offset:52224\n\t"
        "v_cvt_pk_bf16_f32 v50, v52, v53\n\t"
        "v_cvt_pk_bf16_f32 v51, v54, v55\n\t"
        "v_mul_f32 v56, v212, v56\n\t"
        "s_waitcnt lgkmcnt(10)\n\t"
        "v_mfma_f32_32x32x16_bf16 v[0:15], v[112:115], v[180:183], v[0:15]\n\t"
        "ds_read_b64_tr_b16 v[112:113], v121 offset:55296\n\t"
        "ds_read_b64_tr_b16 v[114:115], v121 offset:58368\n\t"
        "v_mul_f32 v57, v212, v57\n\t"
        "v_mul_f32 v58, v212, v58\n\t"
        "v_mul_f32 v59, v212, v59\n\t"
        "s_waitcnt lgkmcnt(10)\n\t"
        "v_mfma_f32_32x32x16_bf16 v[16:31], v[116:119], v[180:183], v[16:31]\n\t"
        "ds_read_b64_tr_b16 v[116:117], v122 offset:55296\n\t"
        "ds_read_b64_tr_b16 v[118:119], v122 offset:58368\n\t"
        "v_mul_f32 v60, v212, v60\n\t"
        "v_mul_f32 v61, v212, v61\n\t"
        "v_mul_f32 v62, v212, v62\n\t"
        "s_waitcnt lgkmcnt(10)\n\t"
        "v_mfma_f32_32x32x16_bf16 v[0:15], v[96:99], v[184:187], v[0:15]\n\t"
        "v_mul_f32 v63, v212, v63\n\t"
        "v_cvt_pk_bf16_f32 v52, v56, v57\n\t"
        "v_cvt_pk_bf16_f32 v53, v58, v59\n\t"
        "v_cvt_pk_bf16_f32 v54, v60, v61\n\t"
        "v_cvt_pk_bf16_f32 v55, v62, v63\n\t"
        "s_waitcnt lgkmcnt(8)\n\t"
        "v_mfma_f32_32x32x16_bf16 v[16:31], v[100:103], v[184:187], v[16:31]\n\t"
        "buffer_store_dwordx4 v[48:51], v214, s[40:43], 0 offen offset:0 sc1\n\t"
        "buffer_store_dwordx4 v[52:55], v214, s[40:43], 0 offen offset:1024 sc1\n\t"
        "s_waitcnt lgkmcnt(6)\n\t"
        "v_mfma_f32_32x32x16_bf16 v[0:15], v[104:107], v[188:191], v[0:15]\n\t"
        "s_waitcnt lgkmcnt(4)\n\t"
        "v_mfma_f32_32x32x16_bf16 v[16:31], v[108:111], v[188:191], v[16:31]\n\t"
        "s_waitcnt lgkmcnt(2)\n\t"
        "v_mfma_f32_32x32x16_bf16 v[0:15], v[112:115], v[192:195], v[0:15]\n\t"
        "s_waitcnt lgkmcnt(0)\n\t"
        "v_mfma_f32_32x32x16_bf16 v[16:31], v[116:119], v[192:195], v[16:31]\n\t"
        "s_nop 11\n\t"
        "s_branch 9f\n\t"
        "16:\n\t"
        "s_mov_b32 s48, 0x10000\n\t"
        "8:\n\t"
        "ds_read_b32 v126, v125\n\t"
        "s_sub_u32 s48, s48, 1\n\t"
        "s_waitcnt lgkmcnt(0)\n\t"
        "s_cbranch_scc1 17b\n\t"
        "v_cmp_gt_u32 vcc, s47, v126\n\t"
        "s_cbranch_vccnz 8b\n\t"
        "s_branch 17b\n\t"
        "18:\n\t"
        "s_mov_b32 s48, 0x10000\n\t"
        "8:\n\t"
        "ds_read_b32 v126, v125\n\t"
        "s_sub_u32 s48, s48, 1\n\t"
        "s_waitcnt lgkmcnt(0)\n\t"
        "s_cbranch_scc1 19b\n\t"
        "v_cmp_gt_u32 vcc, s47, v126\n\t"
        "s_cbranch_vccnz 8b\n\t"
        "s_branch 19b\n\t"
        "20:\n\t"
        "s_mov_b32 s48, 0x10000\n\t"
        "8:\n\t"
        "ds_read_b32 v126, v125\n\t"
        "s_sub_u32 s48, s48, 1\n\t"
        "s_waitcnt lgkmcnt(0)\n\t"
        "s_cbranch_scc1 21b\n\t"
        "v_cmp_gt_u32 vcc, s47, v126\n\t"
        "s_cbranch_vccnz 8b\n\t"
        "s_branch 21b\n\t"
        "22:\n\t"
        "s_mov_b32 s48, 0x10000\n\t"
        "8:\n\t"
        "ds_read_b32 v126, v125\n\t"
        "s_sub_u32 s48, s48, 1\n\t"
        "s_waitcnt lgkmcnt(0)\n\t"
        "s_cbranch_scc1 23b\n\t"
        "v_cmp_gt_u32 vcc, s47, v126\n\t"
        "s_cbranch_vccnz 8b\n\t"
        "s_branch 23b\n\t"
        "24:\n\t"
        "s_mov_b32 s48, 0x10000\n\t"
        "8:\n\t"
        "ds_read_b32 v126, v125\n\t"
        "s_sub_u32 s48, s48, 1\n\t"
        "s_waitcnt lgkmcnt(0)\n\t"
        "s_cbranch_scc1 25b\n\t"
        "v_cmp_gt_u32 vcc, s47, v126\n\t"
        "s_cbranch_vccnz 8b\n\t"
        "s_branch 25b\n\t"
        "26:\n\t"
        "s_mov_b32 s48, 0x10000\n\t"
        "8:\n\t"
        "ds_read_b32 v126, v125\n\t"
        "s_sub_u32 s48, s48, 1\n\t"
        "s_waitcnt lgkmcnt(0)\n\t"
        "s_cbranch_scc1 27b\n\t"
        "v_cmp_gt_u32 vcc, s47, v126\n\t"
        "s_cbranch_vccnz 8b\n\t"
        "s_branch 27b\n\t"
        "28:\n\t"
        "s_mov_b32 s48, 0x10000\n\t"
        "8:\n\t"
        "ds_read_b32 v126, v125\n\t"
        "s_sub_u32 s48, s48, 1\n\t"
        "s_waitcnt lgkmcnt(0)\n\t"
        "s_cbranch_scc1 29b\n\t"
        "v_cmp_gt_u32 vcc, s47, v126\n\t"
        "s_cbranch_vccnz 8b\n\t"
        "s_branch 29b\n\t"
        "30:\n\t"
        "s_mov_b32 s48, 0x10000\n\t"
        "8:\n\t"
        "ds_read_b32 v126, v125\n\t"
        "s_sub_u32 s48, s48, 1\n\t"
        "s_waitcnt lgkmcnt(0)\n\t"
        "s_cbranch_scc1 31b\n\t"
        "v_cmp_gt_u32 vcc, s47, v126\n\t"
        "s_cbranch_vccnz 8b\n\t"
        "s_branch 31b\n\t"
        "74:\n\t"
        "s_mov_b32 s48, 0x10000\n\t"
        "8:\n\t"
        "ds_read_b32 v126, v125\n\t"
        "s_sub_u32 s48, s48, 1\n\t"
        "s_waitcnt lgkmcnt(0)\n\t"
        "s_cbranch_scc1 75b\n\t"
        "v_cmp_gt_u32 vcc, s47, v126\n\t"
        "s_cbranch_vccnz 8b\n\t"
        "s_branch 75b\n\t"
        "9:\n\t"
        : "+{v[0:15]}"(acc[0]), "+{v[16:31]}"(acc[1]), "+{v[32:47]}"(acc[2]), "+{v[48:63]}"(acc[3]), "+{v[64:79]}"(acc[4]), "+{v[80:95]}"(acc[5]),
          "+{v[96:111]}"(stA), "+{v[112:127]}"(stB), "+{v[128:131]}"(stC), "+{v[148:163]}"(xr1), "+{v[164:179]}"(xr2), "+{v[180:195]}"(xr3),
          "+{s44}"(soff), "+{s46}"(slot), "+{s47}"(need)
        : "{s[40:43]}"(srd), "{v[212:215]}"(ep) : "v196", "v197", "v198", "v199", "v200", "v201", "v202", "v203", "v204", "v205", "v206", "v207", "v208", "v209", "v210", "v211", "v132", "v133", "v134", "v135", "v136", "v137", "v138", "v139", "v140", "v141", "v142", "v143", "v144", "v145", "v146", "v147", "s45", "s48", "vcc", "scc", "memory");
      STAMPV(4 + 16 * L);
      PHASE_VARS;
      u32x4 kv[4][2];
#pragma unroll
      for (int j = 0; j < 4; ++j)
#pragma unroll
        for (int e = 0; e < 8; ++e) kv[j][e >> 2][e & 3] = __float_as_uint(acc[2 + j][e]);
      constexpr float QS = 0.17677669529663687f * 1.4426950408889634f;
      u32x4 qo[2][2];
      auto drained = [&]() { asm volatile("s_waitcnt vmcnt(0)\n\tds_write_b32 %0, %1" :: "v"(lbase + L_W + 80 + 4 * wid), "v"(2 * L + 1) : "memory"); };
      if (L == 0) {
#pragma unroll
        for (int e = 0; e < 16; ++e) {
          if (e == 8) { SBAR(); drained(); SBAR(); }
          const unsigned w = pack2(acc[e >> 3][2 * (e & 7)], acc[e >> 3][2 * (e & 7) + 1]);
          qo[e >> 3][(e >> 2) & 1][e & 3] = pack2(lo16(w) * QS, hi16(w) * QS);
        }
      } else {
        const float aq = U2F(ep[0]) * QS;
#pragma unroll
        for (int e = 0; e < 16; ++e) qo[e >> 3][(e >> 2) & 1][e & 3] = pack2(acc[e >> 3][2 * (e & 7)] * aq, acc[e >> 3][2 * (e & 7) + 1] * aq);
        SBAR();
        lds_wait_ge<0>(lbase + L_W + 4 + 4 * (lane & 1), 1);
        const int idx = (int)__builtin_amdgcn_readfirstlane(words[3]);
        if ((idx >> 5) == tile) {
          f32x4 cs[4][4];
          float cv[2];
#pragma unroll
          for (int j = 0; j < 6; ++j) {
            const int cb = (j >> 1) * 64 + ((j & 1) ^ hh) * 32;
            if (j >= 4) cv[j - 4] = *reinterpret_cast<const float*>(smem + L_PATCH + (cb + r32) * 4);
            else {
#pragma unroll
              for (int g = 0; g < 4; ++g) cs[j][g] = *reinterpret_cast<const f32x4*>(smem + L_PATCH + (cb + 8 * g + 4 * hi) * 4);
            }
          }
          SBAR();
          const bool me = r32 == (idx & 31);
          const int rel = (idx & 31) - 8 * hi;
#pragma unroll
          for (int j = 0; j < 2; ++j) {
            const unsigned pv = pack2(cv[j], cv[j]);
#pragma unroll
            for (int e = 0; e < 8; ++e) {
              unsigned a = pack2(cs[j][e >> 1][2 * (e & 1)] * QS, cs[j][e >> 1][2 * (e & 1) + 1] * QS), b = pack2(cs[2 + j][e >> 1][2 * (e & 1)], cs[2 + j][e >> 1][2 * (e & 1) + 1]);
              PIN(a); PIN(b);
              qo[j][e >> 2][e & 3] = me ? a : qo[j][e >> 2][e & 3];
              kv[j][e >> 2][e & 3] = me ? b : kv[j][e >> 2][e & 3];
              const int t0 = 16 * (e >> 2) + 2 * (e & 3);
              const unsigned mask = rel == t0 ? 0xffffu : rel == t0 + 1 ? 0xffff0000u : 0u;
              kv[2 + j][e >> 2][e & 3] = (kv[2 + j][e >> 2][e & 3] & ~mask) | (pv & mask);
            }
          }
          if (me) {
#pragma unroll
            for (int i = 0; i < 4; ++i) x1p[i] = *reinterpret_cast<const u32x4*>(smem + L_X2P + (i * 2 + hi) * 16);
            reinterpret_cast<float*>(smem + L_AB)[256 + wid * 64 + lane] = hi ? 1.0f : 0.0f;
          }
          DRAIN();
#pragma unroll
          for (int j = 0; j < 4; ++j) { stg(rs, ep[1 + (j & 1)] + (j >> 1) * 2048, kv[j][0]); stg(rs, ep[1 + (j & 1)] + (j >> 1) * 2048 + 1024, kv[j][1]); }
        }
        drained();
      }
      SBAR();
#pragma unroll
      for (int j = 0; j < 2; ++j) {
        char* p = smem + L_XCH + ((j ^ hh) * 4 + wid) * 2048 + lane * 16;
        *reinterpret_cast<u32x4*>(p) = qo[j][0]; *reinterpret_cast<u32x4*>(p + 1024) = qo[j][1];
        p = smem + L_XCH + 16384 + wid * 4096 + j * 2048 + lane * 16;
        *reinterpret_cast<u32x4*>(p) = kv[2 * j + 1][0]; *reinterpret_cast<u32x4*>(p + 1024) = kv[2 * j + 1][1];
      }
      STAMPV(41 + 4 * L);
      BAR_LGKM();
      STAMPV(43 + 4 * L);
      kva = cat4(kv[0][0], kv[0][1], kv[2][0], kv[2][1]);
      const u32x4* pk = reinterpret_cast<const u32x4*>(smem + L_XCH + 16384 + (wid ^ 1) * 4096 + lane * 16);
#pragma unroll
      for (int u = 0; u < 4; ++u) {
        const u32x4* p = reinterpret_cast<const u32x4*>(smem + L_XCH + (hh * 4 + ((2 * kh + 2 + u) & 3)) * 2048 + lane * 16);
        q[u] = __builtin_shufflevector(p[0], p[64], 0, 1, 2, 3, 4, 5, 6, 7);
      }
      kvb = cat4(pk[0], pk[64], pk[128], pk[192]);
      SBAR();
      asm volatile(
        "v_mov_b64 v[0:1], 0\n\t"
        "v_mov_b64 v[2:3], 0\n\t"
        "v_mov_b64 v[4:5], 0\n\t"
        "v_mov_b64 v[6:7], 0\n\t"
        "v_mov_b64 v[8:9], 0\n\t"
        "v_mov_b64 v[10:11], 0\n\t"
        "v_mov_b64 v[12:13], 0\n\t"
        "v_mov_b64 v[14:15], 0\n\t"
        "v_mov_b64 v[16:17], 0\n\t"
        "v_mov_b64 v[18:19], 0\n\t"
        "v_mov_b64 v[20:21], 0\n\t"
        "v_mov_b64 v[22:23], 0\n\t"
        "v_mov_b64 v[24:25], 0\n\t"
        "v_mov_b64 v[26:27], 0\n\t"
        "v_mov_b64 v[28:29], 0\n\t"
        "v_mov_b64 v[30:31], 0\n\t"
        "v_mov_b64 v[32:33], 0\n\t"
        "v_mov_b64 v[34:35], 0\n\t"
        "v_mov_b64 v[36:37], 0\n\t"
        "v_mov_b64 v[38:39], 0\n\t"
        "v_mov_b64 v[40:41], 0\n\t"
        "v_mov_b64 v[42:43], 0\n\t"
        "v_mov_b64 v[44:45], 0\n\t"
        "v_mov_b64 v[46:47], 0\n\t"
        "v_mov_b64 v[48:49], 0\n\t"
        "v_mov_b64 v[50:51], 0\n\t"
        "v_mov_b64 v[52:53], 0\n\t"
        "v_mov_b64 v[54:55], 0\n\t"
        "v_mov_b64 v[56:57], 0\n\t"
        "v_mov_b64 v[58:59], 0\n\t"
        "v_mov_b64 v[60:61], 0\n\t"
        "v_mov_b64 v[62:63], 0\n\t"
        "v_mov_b64 v[128:129], 0\n\t"
        "v_mov_b64 v[130:131], 0\n\t"
        "v_mov_b64 v[132:133], 0\n\t"
        "v_mov_b64 v[134:135], 0\n\t"
        "v_mov_b64 v[136:137], 0\n\t"
        "v_mov_b64 v[138:139], 0\n\t"
        "v_mov_b64 v[140:141], 0\n\t"
        "v_mov_b64 v[142:143], 0\n\t"
        : "={v[0:15]}"(oz[0]), "={v[16:31]}"(oz[1]), "={v[32:47]}"(oz[2]), "={v[48:63]}"(oz[3]), "={v[128:143]}"(oz[4]));
    }
    STAMPV(5 + 16 * L);
    {
      u32x4 ones;
      unsigned kvoff;
      u32x8 fv, cv;
      asm volatile("" : "={v[184:191]}"(fv), "={v[216:223]}"(cv));
      { PHASE_VARS;
        const unsigned v = (lane & 3) == 0 ? 0x3F803F80u : 0u; const bool up = (lane & 4) != 0; ones[0] = ones[1] = up ? 0u : v; ones[2] = ones[3] = up ? v : 0u;
        kvoff = OFF_KV + L * (10 * MBY) + (tn * 2 + hh) * HS + kh * 8192 + lane * 16;
        fv[1] = lbase + L_FREE + wid * 256 + lane * 4; fv[2] = 32 * L + 32;
        cv[0] = lbase + L_XCH + wid * 8192 + lane * 16; cv[1] = lbase + L_XCH + (wid ^ 2) * 8192 + lane * 16;
        cv[2] = lbase + L_XCH + 32768 + wid * 512 + lane * 4; cv[3] = lbase + L_XCH + 32768 + (wid ^ 2) * 512 + lane * 4;
        cv[4] = lbase + L_W + 48 + wid * 4; cv[5] = lbase + L_W + 48 + (wid ^ 2) * 4; cv[6] = L + 1;
        cv[7] = ((tile * 32 + r32) * D_ + tn * 64 + hi * 8) * 2; }
      f32x16 o0 = oz[0], o1 = oz[1], o2 = oz[2], o3 = oz[3], lv = oz[4];
      unsigned soff = tm * 16384;
      STAMP_DECL;
      asm volatile(
        "s_nop 1\n\t"
        "v_mfma_f32_32x32x16_bf16 v[96:111], v[152:155], v[64:67], 0\n\t"
        "v_mfma_f32_32x32x16_bf16 v[96:111], v[156:159], v[68:71], v[96:111]\n\t"
        "s_mov_b32 s46, 0\n\t"
        "ds_write_b32 v185, v186\n\t"
        "s_nop 9\n\t"
        "v_exp_f32 v96, v96\n\t"
        "v_exp_f32 v97, v97\n\t"
        "v_exp_f32 v98, v98\n\t"
        "v_exp_f32 v99, v99\n\t"
        "v_exp_f32 v100, v100\n\t"
        "v_exp_f32 v101, v101\n\t"
        "v_mfma_f32_32x32x16_bf16 v[112:127], v[152:155], v[72:75], 0\n\t"
        "v_exp_f32 v102, v102\n\t"
        "v_exp_f32 v103, v103\n\t"
        "v_exp_f32 v104, v104\n\t"
        "v_mfma_f32_32x32x16_bf16 v[112:127], v[156:159], v[76:79], v[112:127]\n\t"
        "v_exp_f32 v105, v105\n\t"
        "v_exp_f32 v106, v106\n\t"
        "v_exp_f32 v107, v107\n\t"
        "v_smfmac_f32_32x32x32_bf16 v[0:15], v[160:163], v[96:103], v148\n\t"
        "v_exp_f32 v108, v108\n\t"
        "v_exp_f32 v109, v109\n\t"
        "v_exp_f32 v110, v110\n\t"
        "v_smfmac_f32_16x16x64_bf16 v[128:131], v[144:147], v[96:103], v148\n\t"
        "v_exp_f32 v111, v111\n\t"
        "v_exp_f32 v112, v112\n\t"
        "v_exp_f32 v113, v113\n\t"
        "v_smfmac_f32_32x32x32_bf16 v[0:15], v[164:167], v[104:111], v148\n\t"
        "v_exp_f32 v114, v114\n\t"
        "v_exp_f32 v115, v115\n\t"
        "v_exp_f32 v116, v116\n\t"
        "v_smfmac_f32_16x16x64_bf16 v[128:131], v[144:147], v[104:111], v148\n\t"
        "v_exp_f32 v117, v117\n\t"
        "v_mfma_f32_32x32x16_bf16 v[96:111], v[152:155], v[80:83], 0\n\t"
        "v_exp_f32 v118, v118\n\t"
        "v_exp_f32 v119, v119\n\t"
        "v_exp_f32 v120, v120\n\t"
        "ds_read_b32 v151, v224\n\t"
        "v_mfma_f32_32x32x16_bf16 v[96:111], v[156:159], v[84:87], v[96:111]\n\t"
        "v_exp_f32 v121, v121\n\t"
        "v_exp_f32 v122, v122\n\t"
        "v_exp_f32 v123, v123\n\t"
        "v_smfmac_f32_32x32x32_bf16 v[16:31], v[160:163], v[112:119], v148\n\t"
        "v_exp_f32 v124, v124\n\t"
        "v_exp_f32 v125, v125\n\t"
        "v_exp_f32 v126, v126\n\t"
        "v_smfmac_f32_16x16x64_bf16 v[132:135], v[144:147], v[112:119], v148\n\t"
        "v_exp_f32 v127, v127\n\t"
        "s_cmp_lg_u32 s46, 0\n\t"
        "s_cbranch_scc1 25f\n\t"
        "s_waitcnt lgkmcnt(0)\n\t"
        "v_cmp_ne_u32 vcc, v222, v151\n\t"
        "s_cbranch_vccz 24f\n\t"
        "25:\n\t"
        "v_exp_f32 v96, v96\n\t"
        "v_exp_f32 v97, v97\n\t"
        "v_smfmac_f32_32x32x32_bf16 v[16:31], v[164:167], v[120:127], v148\n\t"
        "v_exp_f32 v98, v98\n\t"
        "v_exp_f32 v99, v99\n\t"
        "v_exp_f32 v100, v100\n\t"
        "v_smfmac_f32_16x16x64_bf16 v[132:135], v[144:147], v[120:127], v148\n\t"
        "v_exp_f32 v101, v101\n\t"
        "v_mfma_f32_32x32x16_bf16 v[112:127], v[152:155], v[88:91], 0\n\t"
        "v_exp_f32 v102, v102\n\t"
        "v_exp_f32 v103, v103\n\t"
        "v_exp_f32 v104, v104\n\t"
        "ds_read_b32 v151, v224\n\t"
        "v_mfma_f32_32x32x16_bf16 v[112:127], v[156:159], v[92:95], v[112:127]\n\t"
        "v_exp_f32 v105, v105\n\t"
        "v_exp_f32 v106, v106\n\t"
        "v_exp_f32 v107, v107\n\t"
        "v_smfmac_f32_32x32x32_bf16 v[32:47], v[160:163], v[96:103], v148\n\t"
        "v_exp_f32 v108, v108\n\t"
        "v_exp_f32 v109, v109\n\t"
        "v_exp_f32 v110, v110\n\t"
        "v_smfmac_f32_16x16x64_bf16 v[136:139], v[144:147], v[96:103], v148\n\t"
        "v_exp_f32 v111, v111\n\t"
        "s_cmp_lg_u32 s46, 0\n\t"
        "s_cbranch_scc1 27f\n\t"
        "s_waitcnt lgkmcnt(0)\n\t"
        "v_cmp_ne_u32 vcc, v222, v151\n\t"
        "s_cbranch_vccz 26f\n\t"
        "27:\n\t"
        "v_exp_f32 v112, v112\n\t"
        "v_exp_f32 v113, v113\n\t"
        "v_smfmac_f32_32x32x32_bf16 v[32:47], v[164:167], v[104:111], v148\n\t"
        "v_exp_f32 v114, v114\n\t"
        "v_exp_f32 v115, v115\n\t"
        "v_exp_f32 v116, v116\n\t"
        "v_smfmac_f32_16x16x64_bf16 v[136:139], v[144:147], v[104:111], v148\n\t"
        "v_exp_f32 v117, v117\n\t"
        "v_mfma_f32_32x32x16_bf16 v[96:111], v[168:171], v[64:67], 0\n\t"
        "v_exp_f32 v118, v118\n\t"
        "v_exp_f32 v119, v119\n\t"
        "v_exp_f32 v120, v120\n\t"
        "ds_read_b32 v151, v224\n\t"
        "v_mfma_f32_32x32x16_bf16 v[96:111], v[172:175], v[68:71], v[96:111]\n\t"
        "v_exp_f32 v121, v121\n\t"
        "v_exp_f32 v122, v122\n\t"
        "v_exp_f32 v123, v123\n\t"
        "v_smfmac_f32_32x32x32_bf16 v[48:63], v[160:163], v[112:119], v148\n\t"
        "v_exp_f32 v124, v124\n\t"
        "v_exp_f32 v125, v125\n\t"
        "v_exp_f32 v126, v126\n\t"
        "v_smfmac_f32_16x16x64_bf16 v[140:143], v[144:147], v[112:119], v148\n\t"
        "v_exp_f32 v127, v127\n\t"
        "s_cmp_lg_u32 s46, 0\n\t"
        "s_cbranch_scc1 29f\n\t"
        "s_waitcnt lgkmcnt(0)\n\t"
        "v_cmp_ne_u32 vcc, v222, v151\n\t"
        "s_cbranch_vccz 28f\n\t"
        "29:\n\t"
        "v_exp_f32 v96, v96\n\t"
        "v_exp_f32 v97, v97\n\t"
        "v_smfmac_f32_32x32x32_bf16 v[48:63], v[164:167], v[120:127], v148\n\t"
        "v_exp_f32 v98, v98\n\t"
        "v_exp_f32 v99, v99\n\t"
        "v_exp_f32 v100, v100\n\t"
        "v_smfmac_f32_16x16x64_bf16 v[140:143], v[144:147], v[120:127], v148\n\t"
        "v_exp_f32 v101, v101\n\t"
        "v_mfma_f32_32x32x16_bf16 v[112:127], v[168:171], v[72:75], 0\n\t"
        "v_exp_f32 v102, v102\n\t"
        "v_exp_f32 v103, v103\n\t"
        "v_exp_f32 v104, v104\n\t"
        "ds_read_b32 v151, v224\n\t"
        "v_mfma_f32_32x32x16_bf16 v[112:127], v[172:175], v[76:79], v[112:127]\n\t"
        "v_exp_f32 v105, v105\n\t"
        "v_exp_f32 v106, v106\n\t"
        "v_exp_f32 v107, v107\n\t"
        "v_smfmac_f32_32x32x32_bf16 v[0:15], v[176:179], v[96:103], v148\n\t"
        "v_exp_f32 v108, v108\n\t"
        "v_exp_f32 v109, v109\n\t"
        "v_exp_f32 v110, v110\n\t"
        "v_smfmac_f32_16x16x64_bf16 v[128:131], v[144:147], v[96:103], v148\n\t"
        "v_exp_f32 v111, v111\n\t"
        "s_cmp_lg_u32 s46, 0\n\t"
        "s_cbranch_scc1 31f\n\t"
        "s_waitcnt lgkmcnt(0)\n\t"
        "v_cmp_ne_u32 vcc, v222, v151\n\t"
        "s_cbranch_vccz 30f\n\t"
        "31:\n\t"
        "v_exp_f32 v112, v112\n\t"
        "v_exp_f32 v113, v113\n\t"
        "v_smfmac_f32_32x32x32_bf16 v[0:15], v[180:183], v[104:111], v148\n\t"
        "v_exp_f32 v114, v114\n\t"
        "v_exp_f32 v115, v115\n\t"
        "v_exp_f32 v116, v116\n\t"
        "v_smfmac_f32_16x16x64_bf16 v[128:131], v[144:147], v[104:111], v148\n\t"
        "v_exp_f32 v117, v117\n\t"
        "v_mfma_f32_32x32x16_bf16 v[96:111], v[168:171], v[80:83], 0\n\t"
        "v_exp_f32 v118, v118\n\t"
        "v_exp_f32 v119, v119\n\t"
        "v_exp_f32 v120, v120\n\t"
        "ds_read_b32 v151, v224\n\t"
        "v_mfma_f32_32x32x16_bf16 v[96:111], v[172:175], v[84:87], v[96:111]\n\t"
        "v_exp_f32 v121, v121\n\t"
        "v_exp_f32 v122, v122\n\t"
        "v_exp_f32 v123, v123\n\t"
        "v_smfmac_f32_32x32x32_bf16 v[16:31], v[176:179], v[112:119], v148\n\t"
        "v_exp_f32 v124, v124\n\t"
        "v_exp_f32 v125, v125\n\t"
        "v_exp_f32 v126, v126\n\t"
        "v_smfmac_f32_16x16x64_bf16 v[132:135], v[144:147], v[112:119], v148\n\t"
        "v_exp_f32 v127, v127\n\t"
        "s_cmp_lg_u32 s46, 0\n\t"
        "s_cbranch_scc1 33f\n\t"
        "s_waitcnt lgkmcnt(0)\n\t"
        "v_cmp_ne_u32 vcc, v222, v151\n\t"
        "s_cbranch_vccz 32f\n\t"
        "33:\n\t"
        "v_exp_f32 v96, v96\n\t"
        "v_exp_f32 v97, v97\n\t"
        "v_smfmac_f32_32x32x32_bf16 v[16:31], v[180:183], v[120:127], v148\n\t"
        "v_exp_f32 v98, v98\n\t"
        "v_exp_f32 v99, v99\n\t"
        "v_exp_f32 v100, v100\n\t"
        "v_smfmac_f32_16x16x64_bf16 v[132:135], v[144:147], v[120:127], v148\n\t"
        "v_exp_f32 v101, v101\n\t"
        "v_mfma_f32_32x32x16_bf16 v[112:127], v[168:171], v[88:91], 0\n\t"
        "v_exp_f32 v102, v102\n\t"
        "v_exp_f32 v103, v103\n\t"
        "v_exp_f32 v104, v104\n\t"
        "v_mfma_f32_32x32x16_bf16 v[112:127], v[172:175], v[92:95], v[112:127]\n\t"
        "v_exp_f32 v105, v105\n\t"
        "v_exp_f32 v106, v106\n\t"
        "v_exp_f32 v107, v107\n\t"
        "v_smfmac_f32_32x32x32_bf16 v[32:47], v[176:179], v[96:103], v148\n\t"
        "v_exp_f32 v108, v108\n\t"
        "v_exp_f32 v109, v109\n\t"
        "v_exp_f32 v110, v110\n\t"
        "v_smfmac_f32_16x16x64_bf16 v[136:139], v[144:147], v[96:103], v148\n\t"
        "v_exp_f32 v111, v111\n\t"
        "s_cmp_lg_u32 s46, 0\n\t"
        "s_cbranch_scc1 5f\n\t"
        "s_mov_b32 s45, 0x200000\n\t"
        "2:\n\t"
        "ds_read_b32 v151, v224\n\t"
        "s_sub_u32 s45, s45, 1\n\t"
        "s_waitcnt lgkmcnt(0)\n\t"
        "s_cbranch_scc1 3f\n\t"
        "v_cmp_ne_u32 vcc, v222, v151\n\t"
        "s_cbranch_vccnz 2b\n\t"
        "3:\n\t"
        "s_add_u32 s44, s44, 0x4000\n\t"
        "s_and_b32 s44, s44, 0x3ffff\n\t"
        "buffer_load_dwordx4 v[184:187], v149, s[40:43], s44 offen offset:0 sc1\n\t"
        "buffer_load_dwordx4 v[188:191], v149, s[40:43], s44 offen offset:1024 sc1\n\t"
        "buffer_load_dwordx4 v[192:195], v149, s[40:43], s44 offen offset:2048 sc1\n\t"
        "buffer_load_dwordx4 v[196:199], v149, s[40:43], s44 offen offset:3072 sc1\n\t"
        "buffer_load_dwordx4 v[200:203], v150, s[40:43], s44 offen offset:0 sc1\n\t"
        "buffer_load_dwordx4 v[204:207], v150, s[40:43], s44 offen offset:1024 sc1\n\t"
        "buffer_load_dwordx4 v[208:211], v150, s[40:43], s44 offen offset:2048 sc1\n\t"
        "buffer_load_dwordx4 v[212:215], v150, s[40:43], s44 offen offset:3072 sc1\n\t"
        "5:\n\t"
        "s_add_u32 s44, s44, 0x4000\n\t"
        "s_and_b32 s44, s44, 0x3ffff\n\t"
        "buffer_load_dwordx4 v[152:155], v149, s[40:43], s44 offen offset:0 sc1\n\t"
        "buffer_load_dwordx4 v[156:159], v149, s[40:43], s44 offen offset:1024 sc1\n\t"
        "buffer_load_dwordx4 v[160:163], v149, s[40:43], s44 offen offset:2048 sc1\n\t"
        "buffer_load_dwordx4 v[164:167], v149, s[40:43], s44 offen offset:3072 sc1\n\t"
        "s_mov_b32 s45, 7\n\t"
        "1:\n\t"
        "v_exp_f32 v112, v112\n\t"
        "v_exp_f32 v113, v113\n\t"
        "v_smfmac_f32_32x32x32_bf16 v[32:47], v[180:183], v[104:111], v148\n\t"
        "v_exp_f32 v114, v114\n\t"
        "v_exp_f32 v115, v115\n\t"
        "v_exp_f32 v116, v116\n\t"
        "v_smfmac_f32_16x16x64_bf16 v[136:139], v[144:147], v[104:111], v148\n\t"
        "v_exp_f32 v117, v117\n\t"
        "s_waitcnt vmcnt(8)\n\t"
        "v_mfma_f32_32x32x16_bf16 v[96:111], v[184:187], v[64:67], 0\n\t"
        "v_exp_f32 v118, v118\n\t"
        "v_exp_f32 v119, v119\n\t"
        "v_exp_f32 v120, v120\n\t"
        "v_mfma_f32_32x32x16_bf16 v[96:111], v[188:191], v[68:71], v[96:111]\n\t"
        "v_exp_f32 v121, v121\n\t"
        "v_exp_f32 v122, v122\n\t"
        "v_exp_f32 v123, v123\n\t"
        "v_smfmac_f32_32x32x32_bf16 v[48:63], v[176:179], v[112:119], v148\n\t"
        "v_exp_f32 v124, v124\n\t"
        "v_exp_f32 v125, v125\n\t"
        "v_exp_f32 v126, v126\n\t"
        "v_smfmac_f32_16x16x64_bf16 v[140:143], v[144:147], v[112:119], v148\n\t"
        "buffer_load_dwordx4 v[168:171], v150, s[40:43], s44 offen offset:0 sc1\n\t"
        "v_exp_f32 v127, v127\n\t"
        "v_exp_f32 v96, v96\n\t"
        "v_exp_f32 v97, v97\n\t"
        "v_smfmac_f32_32x32x32_bf16 v[48:63], v[180:183], v[120:127], v148\n\t"
        "v_exp_f32 v98, v98\n\t"
        "v_exp_f32 v99, v99\n\t"
        "v_exp_f32 v100, v100\n\t"
        "v_smfmac_f32_16x16x64_bf16 v[140:143], v[144:147], v[120:127], v148\n\t"
        "v_exp_f32 v101, v101\n\t"
        "v_mfma_f32_32x32x16_bf16 v[112:127], v[184:187], v[72:75], 0\n\t"
        "v_exp_f32 v102, v102\n\t"
        "v_exp_f32 v103, v103\n\t"
        "v_exp_f32 v104, v104\n\t"
        "v_mfma_f32_32x32x16_bf16 v[112:127], v[188:191], v[76:79], v[112:127]\n\t"
        "v_exp_f32 v105, v105\n\t"
        "v_exp_f32 v106, v106\n\t"
        "v_exp_f32 v107, v107\n\t"
        "v_smfmac_f32_32x32x32_bf16 v[0:15], v[192:195], v[96:103], v148\n\t"
        "v_exp_f32 v108, v108\n\t"
        "v_exp_f32 v109, v109\n\t"
        "v_exp_f32 v110, v110\n\t"
        "v_smfmac_f32_16x16x64_bf16 v[128:131], v[144:147], v[96:103], v148\n\t"
        "buffer_load_dwordx4 v[172:175], v150, s[40:43], s44 offen offset:1024 sc1\n\t"
        "v_exp_f32 v111, v111\n\t"
        "v_exp_f32 v112, v112\n\t"
        "v_exp_f32 v113, v113\n\t"
        "v_smfmac_f32_32x32x32_bf16 v[0:15], v[196:199], v[104:111], v148\n\t"
        "v_exp_f32 v114, v114\n\t"
        "v_exp_f32 v115, v115\n\t"
        "v_exp_f32 v116, v116\n\t"
        "v_smfmac_f32_16x16x64_bf16 v[128:131], v[144:147], v[104:111], v148\n\t"
        "v_exp_f32 v117, v117\n\t"
        "v_mfma_f32_32x32x16_bf16 v[96:111], v[184:187], v[80:83], 0\n\t"
        "v_exp_f32 v118, v118\n\t"
        "v_exp_f32 v119, v119\n\t"
        "v_exp_f32 v120, v120\n\t"
        "v_mfma_f32_32x32x16_bf16 v[96:111], v[188:191], v[84:87], v[96:111]\n\t"
        "v_exp_f32 v121, v121\n\t"
        "v_exp_f32 v122, v122\n\t"
        "v_exp_f32 v123, v123\n\t"
        "v_smfmac_f32_32x32x32_bf16 v[16:31], v[192:195], v[112:119], v148\n\t"
        "v_exp_f32 v124, v124\n\t"
        "v_exp_f32 v125, v125\n\t"
        "v_exp_f32 v126, v126\n\t"
        "v_smfmac_f32_16x16x64_bf16 v[132:135], v[144:147], v[112:119], v148\n\t"
        "buffer_load_dwordx4 v[176:179], v150, s[40:43], s44 offen offset:2048 sc1\n\t"
        "v_exp_f32 v127, v127\n\t"
        "v_exp_f32 v96, v96\n\t"
        "v_exp_f32 v97, v97\n\t"
        "v_smfmac_f32_32x32x32_bf16 v[16:31], v[196:199], v[120:127], v148\n\t"
        "v_exp_f32 v98, v98\n\t"
        "v_exp_f32 v99, v99\n\t"
        "v_exp_f32 v100, v100\n\t"
        "v_smfmac_f32_16x16x64_bf16 v[132:135], v[144:147], v[120:127], v148\n\t"
        "v_exp_f32 v101, v101\n\t"
        "v_mfma_f32_32x32x16_bf16 v[112:127], v[184:187], v[88:91], 0\n\t"
        "v_exp_f32 v102, v102\n\t"
        "v_exp_f32 v103, v103\n\t"
        "v_exp_f32 v104, v104\n\t"
        "v_mfma_f32_32x32x16_bf16 v[112:127], v[188:191], v[92:95], v[112:127]\n\t"
        "v_exp_f32 v105, v105\n\t"
        "v_exp_f32 v106, v106\n\t"
        "v_exp_f32 v107, v107\n\t"
        "v_smfmac_f32_32x32x32_bf16 v[32:47], v[192:195], v[96:103], v148\n\t"
        "v_exp_f32 v108, v108\n\t"
        "v_exp_f32 v109, v109\n\t"
        "v_exp_f32 v110, v110\n\t"
        "v_smfmac_f32_16x16x64_bf16 v[136:139], v[144:147], v[96:103], v148\n\t"
        "buffer_load_dwordx4 v[180:183], v150, s[40:43], s44 offen offset:3072 sc1\n\t"
        "v_exp_f32 v111, v111\n\t"
        "v_exp_f32 v112, v112\n\t"
        "v_exp_f32 v113, v113\n\t"
        "v_smfmac_f32_32x32x32_bf16 v[32:47], v[196:199], v[104:111], v148\n\t"
        "s_add_u32 s44, s44, 0x4000\n\t"
        "s_and_b32 s44, s44, 0x3ffff\n\t"
        "v_exp_f32 v114, v114\n\t"
        "v_exp_f32 v115, v115\n\t"
        "v_exp_f32 v116, v116\n\t"
        "v_smfmac_f32_16x16x64_bf16 v[136:139], v[144:147], v[104:111], v148\n\t"
        "v_exp_f32 v117, v117\n\t"
        "s_waitcnt vmcnt(8)\n\t"
        "v_mfma_f32_32x32x16_bf16 v[96:111], v[200:203], v[64:67], 0\n\t"
        "v_exp_f32 v118, v118\n\t"
        "v_exp_f32 v119, v119\n\t"
        "v_exp_f32 v120, v120\n\t"
        "v_mfma_f32_32x32x16_bf16 v[96:111], v[204:207], v[68:71], v[96:111]\n\t"
        "v_exp_f32 v121, v121\n\t"
        "v_exp_f32 v122, v122\n\t"
        "v_exp_f32 v123, v123\n\t"
        "v_smfmac_f32_32x32x32_bf16 v[48:63], v[192:195], v[112:119], v148\n\t"
        "v_exp_f32 v124, v124\n\t"
        "v_exp_f32 v125, v125\n\t"
        "v_exp_f32 v126, v126\n\t"
        "v_smfmac_f32_16x16x64_bf16 v[140:143], v[144:147], v[112:119], v148\n\t"
        "buffer_load_dwordx4 v[184:187], v149, s[40:43], s44 offen offset:0 sc1\n\t"
        "v_exp_f32 v127, v127\n\t"
        "v_exp_f32 v96, v96\n\t"
        "v_exp_f32 v97, v97\n\t"
        "v_smfmac_f32_32x32x32_bf16 v[48:63], v[196:199], v[120:127], v148\n\t"
        "v_exp_f32 v98, v98\n\t"
        "v_exp_f32 v99, v99\n\t"
        "v_exp_f32 v100, v100\n\t"
        "v_smfmac_f32_16x16x64_bf16 v[140:143], v[144:147], v[120:127], v148\n\t"
        "v_exp_f32 v101, v101\n\t"
        "v_mfma_f32_32x32x16_bf16 v[112:127], v[200:203], v[72:75], 0\n\t"
        "v_exp_f32 v102, v102\n\t"
        "v_exp_f32 v103, v103\n\t"
        "v_exp_f32 v104, v104\n\t"
        "v_mfma_f32_32x32x16_bf16 v[112:127], v[204:207], v[76:79], v[112:127]\n\t"
        "v_exp_f32 v105, v105\n\t"
        "v_exp_f32 v106, v106\n\t"
        "v_exp_f32 v107, v107\n\t"
        "v_smfmac_f32_32x32x32_bf16 v[0:15], v[208:211], v[96:103], v148\n\t"
        "v_exp_f32 v108, v108\n\t"
        "v_exp_f32 v109, v109\n\t"
        "v_exp_f32 v110, v110\n\t"
        "v_smfmac_f32_16x16x64_bf16 v[128:131], v[144:147], v[96:103], v148\n\t"
        "buffer_load_dwordx4 v[188:191], v149, s[40:43], s44 offen offset:1024 sc1\n\t"
        "v_exp_f32 v111, v111\n\t"
        "v_exp_f32 v112, v112\n\t"
        "v_exp_f32 v113, v113\n\t"
        "v_smfmac_f32_32x32x32_bf16 v[0:15], v[212:215], v[104:111], v148\n\t"
        "v_exp_f32 v114, v114\n\t"
        "v_exp_f32 v115, v115\n\t"
        "v_exp_f32 v116, v116\n\t"
        "v_smfmac_f32_16x16x64_bf16 v[128:131], v[144:147], v[104:111], v148\n\t"
        "v_exp_f32 v117, v117\n\t"
        "v_mfma_f32_32x32x16_bf16 v[96:111], v[200:203], v[80:83], 0\n\t"
        "v_exp_f32 v118, v118\n\t"
        "v_exp_f32 v119, v119\n\t"
        "v_exp_f32 v120, v120\n\t"
        "v_mfma_f32_32x32x16_bf16 v[96:111], v[204:207], v[84:87], v[96:111]\n\t"
        "v_exp_f32 v121, v121\n\t"
        "v_exp_f32 v122, v122\n\t"
        "v_exp_f32 v123, v123\n\t"
        "v_smfmac_f32_32x32x32_bf16 v[16:31], v[208:211], v[112:119], v148\n\t"
        "v_exp_f32 v124, v124\n\t"
        "v_exp_f32 v125, v125\n\t"
        "v_exp_f32 v126, v126\n\t"
        "v_smfmac_f32_16x16x64_bf16 v[132:135], v[144:147], v[112:119], v148\n\t"
        "buffer_load_dwordx4 v[192:195], v149, s[40:43], s44 offen offset:2048 sc1\n\t"
        "v_exp_f32 v127, v127\n\t"
        "v_exp_f32 v96, v96\n\t"
        "v_exp_f32 v97, v97\n\t"
        "v_smfmac_f32_32x32x32_bf16 v[16:31], v[212:215], v[120:127], v148\n\t"
        "v_exp_f32 v98, v98\n\t"
        "v_exp_f32 v99, v99\n\t"
        "v_exp_f32 v100, v100\n\t"
        "v_smfmac_f32_16x16x64_bf16 v[132:135], v[144:147], v[120:127], v148\n\t"
        "v_exp_f32 v101, v101\n\t"
        "v_mfma_f32_32x32x16_bf16 v[112:127], v[200:203], v[88:91], 0\n\t"
        "v_exp_f32 v102, v102\n\t"
        "v_exp_f32 v103, v103\n\t"
        "v_exp_f32 v104, v104\n\t"
        "v_mfma_f32_32x32x16_bf16 v[112:127], v[204:207], v[92:95], v[112:127]\n\t"
        "v_exp_f32 v105, v105\n\t"
        "v_exp_f32 v106, v106\n\t"
        "v_exp_f32 v107, v107\n\t"
        "v_smfmac_f32_32x32x32_bf16 v[32:47], v[208:211], v[96:103], v148\n\t"
        "v_exp_f32 v108, v108\n\t"
        "v_exp_f32 v109, v109\n\t"
        "v_exp_f32 v110, v110\n\t"
        "v_smfmac_f32_16x16x64_bf16 v[136:139], v[144:147], v[96:103], v148\n\t"
        "buffer_load_dwordx4 v[196:199], v149, s[40:43], s44 offen offset:3072 sc1\n\t"
        "v_exp_f32 v111, v111\n\t"
        "v_exp_f32 v112, v112\n\t"
        "v_exp_f32 v113, v113\n\t"
        "v_smfmac_f32_32x32x32_bf16 v[32:47], v[212:215], v[104:111], v148\n\t"
        "v_exp_f32 v114, v114\n\t"
        "v_exp_f32 v115, v115\n\t"
        "v_exp_f32 v116, v116\n\t"
        "v_smfmac_f32_16x16x64_bf16 v[136:139], v[144:147], v[104:111], v148\n\t"
        "v_exp_f32 v117, v117\n\t"
        "s_waitcnt vmcnt(8)\n\t"
        "v_mfma_f32_32x32x16_bf16 v[96:111], v[152:155], v[64:67], 0\n\t"
        "v_exp_f32 v118, v118\n\t"
        "v_exp_f32 v119, v119\n\t"
        "v_exp_f32 v120, v120\n\t"
        "v_mfma_f32_32x32x16_bf16 v[96:111], v[156:159], v[68:71], v[96:111]\n\t"
        "v_exp_f32 v121, v121\n\t"
        "v_exp_f32 v122, v122\n\t"
        "v_exp_f32 v123, v123\n\t"
        "v_smfmac_f32_32x32x32_bf16 v[48:63], v[208:211], v[112:119], v148\n\t"
        "v_exp_f32 v124, v124\n\t"
        "v_exp_f32 v125, v125\n\t"
        "v_exp_f32 v126, v126\n\t"
        "v_smfmac_f32_16x16x64_bf16 v[140:143], v[144:147], v[112:119], v148\n\t"
        "buffer_load_dwordx4 v[200:203], v150, s[40:43], s44 offen offset:0 sc1\n\t"
        "v_exp_f32 v127, v127\n\t"
        "v_exp_f32 v96, v96\n\t"
        "v_exp_f32 v97, v97\n\t"
        "v_smfmac_f32_32x32x32_bf16 v[48:63], v[212:215], v[120:127], v148\n\t"
        "v_exp_f32 v98, v98\n\t"
        "v_exp_f32 v99, v99\n\t"
        "v_exp_f32 v100, v100\n\t"
        "v_smfmac_f32_16x16x64_bf16 v[140:143], v[144:147], v[120:127], v148\n\t"
        "v_exp_f32 v101, v101\n\t"
        "v_mfma_f32_32x32x16_bf16 v[112:127], v[152:155], v[72:75], 0\n\t"
        "v_exp_f32 v102, v102\n\t"
        "v_exp_f32 v103, v103\n\t"
        "v_exp_f32 v104, v104\n\t"
        "v_mfma_f32_32x32x16_bf16 v[112:127], v[156:159], v[76:79], v[112:127]\n\t"
        "v_exp_f32 v105, v105\n\t"
        "v_exp_f32 v106, v106\n\t"
        "v_exp_f32 v107, v107\n\t"
        "v_smfmac_f32_32x32x32_bf16 v[0:15], v[160:163], v[96:103], v148\n\t"
        "v_exp_f32 v108, v108\n\t"
        "v_exp_f32 v109, v109\n\t"
        "v_exp_f32 v110, v110\n\t"
        "v_smfmac_f32_16x16x64_bf16 v[128:131], v[144:147], v[96:103], v148\n\t"
        "buffer_load_dwordx4 v[204:207], v150, s[40:43], s44 offen offset:1024 sc1\n\t"
        "v_exp_f32 v111, v111\n\t"
        "v_exp_f32 v112, v112\n\t"
        "v_exp_f32 v113, v113\n\t"
        "v_smfmac_f32_32x32x32_bf16 v[0:15], v[164:167], v[104:111], v148\n\t"
        "v_exp_f32 v114, v114\n\t"
        "v_exp_f32 v115, v115\n\t"
        "v_exp_f32 v116, v116\n\t"
        "v_smfmac_f32_16x16x64_bf16 v[128:131], v[144:147], v[104:111], v148\n\t"
        "v_exp_f32 v117, v117\n\t"
        "v_mfma_f32_32x32x16_bf16 v[96:111], v[152:155], v[80:83], 0\n\t"
        "v_exp_f32 v118, v118\n\t"
        "v_exp_f32 v119, v119\n\t"
        "v_exp_f32 v120, v120\n\t"
        "v_mfma_f32_32x32x16_bf16 v[96:111], v[156:159], v[84:87], v[96:111]\n\t"
        "v_exp_f32 v121, v121\n\t"
        "v_exp_f32 v122, v122\n\t"
        "v_exp_f32 v123, v123\n\t"
        "v_smfmac_f32_32x32x32_bf16 v[16:31], v[160:163], v[112:119], v148\n\t"
        "v_exp_f32 v124, v124\n\t"
        "v_exp_f32 v125, v125\n\t"
        "v_exp_f32 v126, v126\n\t"
        "v_smfmac_f32_16x16x64_bf16 v[132:135], v[144:147], v[112:119], v148\n\t"
        "buffer_load_dwordx4 v[208:211], v150, s[40:43], s44 offen offset:2048 sc1\n\t"
        "v_exp_f32 v127, v127\n\t"
        "v_exp_f32 v96, v96\n\t"
        "v_exp_f32 v97, v97\n\t"
        "v_smfmac_f32_32x32x32_bf16 v[16:31], v[164:167], v[120:127], v148\n\t"
        "v_exp_f32 v98, v98\n\t"
        "v_exp_f32 v99, v99\n\t"
        "v_exp_f32 v100, v100\n\t"
        "v_smfmac_f32_16x16x64_bf16 v[132:135], v[144:147], v[120:127], v148\n\t"
        "v_exp_f32 v101, v101\n\t"
        "v_mfma_f32_32x32x16_bf16 v[112:127], v[152:155], v[88:91], 0\n\t"
        "v_exp_f32 v102, v102\n\t"
        "v_exp_f32 v103, v103\n\t"
        "v_exp_f32 v104, v104\n\t"
        "v_mfma_f32_32x32x16_bf16 v[112:127], v[156:159], v[92:95], v[112:127]\n\t"
        "v_exp_f32 v105, v105\n\t"
        "v_exp_f32 v106, v106\n\t"
        "v_exp_f32 v107, v107\n\t"
        "v_smfmac_f32_32x32x32_bf16 v[32:47], v[160:163], v[96:103], v148\n\t"
        "v_exp_f32 v108, v108\n\t"
        "v_exp_f32 v109, v109\n\t"
        "v_exp_f32 v110, v110\n\t"
        "v_smfmac_f32_16x16x64_bf16 v[136:139], v[144:147], v[96:103], v148\n\t"
        "buffer_load_dwordx4 v[212:215], v150, s[40:43], s44 offen offset:3072 sc1\n\t"
        "v_exp_f32 v111, v111\n\t"
        "v_exp_f32 v112, v112\n\t"
        "v_exp_f32 v113, v113\n\t"
        "v_smfmac_f32_32x32x32_bf16 v[32:47], v[164:167], v[104:111], v148\n\t"
        "s_add_u32 s44, s44, 0x4000\n\t"
        "s_and_b32 s44, s44, 0x3ffff\n\t"
        "v_exp_f32 v114, v114\n\t"
        "v_exp_f32 v115, v115\n\t"
        "v_exp_f32 v116, v116\n\t"
        "v_smfmac_f32_16x16x64_bf16 v[136:139], v[144:147], v[104:111], v148\n\t"
        "v_exp_f32 v117, v117\n\t"
        "s_waitcnt vmcnt(8)\n\t"
        "v_mfma_f32_32x32x16_bf16 v[96:111], v[168:171], v[64:67], 0\n\t"
        "v_exp_f32 v118, v118\n\t"
        "v_exp_f32 v119, v119\n\t"
        "v_exp_f32 v120, v120\n\t"
        "v_mfma_f32_32x32x16_bf16 v[96:111], v[172:175], v[68:71], v[96:111]\n\t"
        "v_exp_f32 v121, v121\n\t"
        "v_exp_f32 v122, v122\n\t"
        "v_exp_f32 v123, v123\n\t"
        "v_smfmac_f32_32x32x32_bf16 v[48:63], v[160:163], v[112:119], v148\n\t"
        "v_exp_f32 v124, v124\n\t"
        "v_exp_f32 v125, v125\n\t"
        "v_exp_f32 v126, v126\n\t"
        "v_smfmac_f32_16x16x64_bf16 v[140:143], v[144:147], v[112:119], v148\n\t"
        "buffer_load_dwordx4 v[152:155], v149, s[40:43], s44 offen offset:0 sc1\n\t"
        "v_exp_f32 v127, v127\n\t"
        "v_exp_f32 v96, v96\n\t"
        "v_exp_f32 v97, v97\n\t"
        "v_smfmac_f32_32x32x32_bf16 v[48:63], v[164:167], v[120:127], v148\n\t"
        "v_exp_f32 v98, v98\n\t"
        "v_exp_f32 v99, v99\n\t"
        "v_exp_f32 v100, v100\n\t"
        "v_smfmac_f32_16x16x64_bf16 v[140:143], v[144:147], v[120:127], v148\n\t"
        "v_exp_f32 v101, v101\n\t"
        "v_mfma_f32_32x32x16_bf16 v[112:127], v[168:171], v[72:75], 0\n\t"
        "v_exp_f32 v102, v102\n\t"
        "v_exp_f32 v103, v103\n\t"
        "v_exp_f32 v104, v104\n\t"
        "v_mfma_f32_32x32x16_bf16 v[112:127], v[172:175], v[76:79], v[112:127]\n\t"
        "v_exp_f32 v105, v105\n\t"
        "v_exp_f32 v106, v106\n\t"
        "v_exp_f32 v107, v107\n\t"
        "v_smfmac_f32_32x32x32_bf16 v[0:15], v[176:179], v[96:103], v148\n\t"
        "v_exp_f32 v108, v108\n\t"
        "v_exp_f32 v109, v109\n\t"
        "v_exp_f32 v110, v110\n\t"
        "v_smfmac_f32_16x16x64_bf16 v[128:131], v[144:147], v[96:103], v148\n\t"
        "buffer_load_dwordx4 v[156:159], v149, s[40:43], s44 offen offset:1024 sc1\n\t"
        "v_exp_f32 v111, v111\n\t"
        "v_exp_f32 v112, v112\n\t"
        "v_exp_f32 v113, v113\n\t"
        "v_smfmac_f32_32x32x32_bf16 v[0:15], v[180:183], v[104:111], v148\n\t"
        "v_exp_f32 v114, v114\n\t"
        "v_exp_f32 v115, v115\n\t"
        "v_exp_f32 v116, v116\n\t"
        "v_smfmac_f32_16x16x64_bf16 v[128:131], v[144:147], v[104:111], v148\n\t"
        "v_exp_f32 v117, v117\n\t"
        "v_mfma_f32_32x32x16_bf16 v[96:111], v[168:171], v[80:83], 0\n\t"
        "v_exp_f32 v118, v118\n\t"
        "v_exp_f32 v119, v119\n\t"
        "v_exp_f32 v120, v120\n\t"
        "v_mfma_f32_32x32x16_bf16 v[96:111], v[172:175], v[84:87], v[96:111]\n\t"
        "v_exp_f32 v121, v121\n\t"
        "v_exp_f32 v122, v122\n\t"
        "v_exp_f32 v123, v123\n\t"
        "v_smfmac_f32_32x32x32_bf16 v[16:31], v[176:179], v[112:119], v148\n\t"
        "v_exp_f32 v124, v124\n\t"
        "v_exp_f32 v125, v125\n\t"
        "v_exp_f32 v126, v126\n\t"
        "v_smfmac_f32_16x16x64_bf16 v[132:135], v[144:147], v[112:119], v148\n\t"
        "buffer_load_dwordx4 v[160:163], v149, s[40:43], s44 offen offset:2048 sc1\n\t"
        "v_exp_f32 v127, v127\n\t"
        "v_exp_f32 v96, v96\n\t"
        "v_exp_f32 v97, v97\n\t"
        "v_smfmac_f32_32x32x32_bf16 v[16:31], v[180:183], v[120:127], v148\n\t"
        "v_exp_f32 v98, v98\n\t"
        "v_exp_f32 v99, v99\n\t"
        "v_exp_f32 v100, v100\n\t"
        "v_smfmac_f32_16x16x64_bf16 v[132:135], v[144:147], v[120:127], v148\n\t"
        "v_exp_f32 v101, v101\n\t"
        "v_mfma_f32_32x32x16_bf16 v[112:127], v[168:171], v[88:91], 0\n\t"
        "v_exp_f32 v102, v102\n\t"
        "v_exp_f32 v103, v103\n\t"
        "v_exp_f32 v104, v104\n\t"
        "v_mfma_f32_32x32x16_bf16 v[112:127], v[172:175], v[92:95], v[112:127]\n\t"
        "v_exp_f32 v105, v105\n\t"
        "v_exp_f32 v106, v106\n\t"
        "v_exp_f32 v107, v107\n\t"
        "v_smfmac_f32_32x32x32_bf16 v[32:47], v[176:179], v[96:103], v148\n\t"
        "v_exp_f32 v108, v108\n\t"
        "v_exp_f32 v109, v109\n\t"
        "v_exp_f32 v110, v110\n\t"
        "v_smfmac_f32_16x16x64_bf16 v[136:139], v[144:147], v[96:103], v148\n\t"
        "buffer_load_dwordx4 v[164:167], v149, s[40:43], s44 offen offset:3072 sc1\n\t"
        "v_exp_f32 v111, v111\n\t"
        "s_sub_u32 s45, s45, 1\n\t"
        "s_cmp_lg_u32 s45, 0\n\t"
        "s_cbranch_scc1 1b\n\t"
        "v_exp_f32 v112, v112\n\t"
        "v_exp_f32 v113, v113\n\t"
        "v_smfmac_f32_32x32x32_bf16 v[32:47], v[180:183], v[104:111], v148\n\t"
        "v_exp_f32 v114, v114\n\t"
        "v_exp_f32 v115, v115\n\t"
        "v_exp_f32 v116, v116\n\t"
        "v_smfmac_f32_16x16x64_bf16 v[136:139], v[144:147], v[104:111], v148\n\t"
        "v_exp_f32 v117, v117\n\t"
        "s_waitcnt vmcnt(8)\n\t"
        "v_mfma_f32_32x32x16_bf16 v[96:111], v[184:187], v[64:67], 0\n\t"
        "v_exp_f32 v118, v118\n\t"
        "v_exp_f32 v119, v119\n\t"
        "v_exp_f32 v120, v120\n\t"
        "v_mfma_f32_32x32x16_bf16 v[96:111], v[188:191], v[68:71], v[96:111]\n\t"
        "v_exp_f32 v121, v121\n\t"
        "v_exp_f32 v122, v122\n\t"
        "v_exp_f32 v123, v123\n\t"
        "v_smfmac_f32_32x32x32_bf16 v[48:63], v[176:179], v[112:119], v148\n\t"
        "v_exp_f32 v124, v124\n\t"
        "v_exp_f32 v125, v125\n\t"
        "v_exp_f32 v126, v126\n\t"
        "v_smfmac_f32_16x16x64_bf16 v[140:143], v[144:147], v[112:119], v148\n\t"
        "v_exp_f32 v127, v127\n\t"
        "v_exp_f32 v96, v96\n\t"
        "v_exp_f32 v97, v97\n\t"
        "v_smfmac_f32_32x32x32_bf16 v[48:63], v[180:183], v[120:127], v148\n\t"
        "v_exp_f32 v98, v98\n\t"
        "v_exp_f32 v99, v99\n\t"
        "v_exp_f32 v100, v100\n\t"
        "v_smfmac_f32_16x16x64_bf16 v[140:143], v[144:147], v[120:127], v148\n\t"
        "v_exp_f32 v101, v101\n\t"
        "v_mfma_f32_32x32x16_bf16 v[112:127], v[184:187], v[72:75], 0\n\t"
        "v_exp_f32 v102, v102\n\t"
        "v_exp_f32 v103, v103\n\t"
        "v_exp_f32 v104, v104\n\t"
        "v_mfma_f32_32x32x16_bf16 v[112:127], v[188:191], v[76:79], v[112:127]\n\t"
        "v_exp_f32 v105, v105\n\t"
        "v_exp_f32 v106, v106\n\t"
        "v_exp_f32 v107, v107\n\t"
        "v_smfmac_f32_32x32x32_bf16 v[0:15], v[192:195], v[96:103], v148\n\t"
        "v_exp_f32 v108, v108\n\t"
        "v_exp_f32 v109, v109\n\t"
        "v_exp_f32 v110, v110\n\t"
        "v_smfmac_f32_16x16x64_bf16 v[128:131], v[144:147], v[96:103], v148\n\t"
        "v_exp_f32 v111, v111\n\t"
        "v_exp_f32 v112, v112\n\t"
        "v_exp_f32 v113, v113\n\t"
        "v_smfmac_f32_32x32x32_bf16 v[0:15], v[196:199], v[104:111], v148\n\t"
        "v_exp_f32 v114, v114\n\t"
        "v_exp_f32 v115, v115\n\t"
        "v_exp_f32 v116, v116\n\t"
        "v_smfmac_f32_16x16x64_bf16 v[128:131], v[144:147], v[104:111], v148\n\t"
        "v_exp_f32 v117, v117\n\t"
        "s_waitcnt vmcnt(4)\n\t"
        "v_mfma_f32_32x32x16_bf16 v[96:111], v[200:203], v[64:67], 0\n\t"
        "v_exp_f32 v118, v118\n\t"
        "v_exp_f32 v119, v119\n\t"
        "v_exp_f32 v120, v120\n\t"
        "v_mfma_f32_32x32x16_bf16 v[96:111], v[204:207], v[68:71], v[96:111]\n\t"
        "v_exp_f32 v121, v121\n\t"
        "v_exp_f32 v122, v122\n\t"
        "v_exp_f32 v123, v123\n\t"
        "v_smfmac_f32_32x32x32_bf16 v[16:31], v[192:195], v[112:119], v148\n\t"
        "v_exp_f32 v124, v124\n\t"
        "v_exp_f32 v125, v125\n\t"
        "v_exp_f32 v126, v126\n\t"
        "v_smfmac_f32_16x16x64_bf16 v[132:135], v[144:147], v[112:119], v148\n\t"
        "v_exp_f32 v127, v127\n\t"
        "s_cmp_lg_u32 s47, 0\n\t"
        "s_cbranch_scc1 4f\n\t"
        "global_load_dwordx4 %[r0], v223, %[xb] offset:0\n\t"
        "global_load_dwordx4 %[r1], v223, %[xb] offset:32\n\t"
        "global_load_dwordx4 %[r2], v223, %[xb] offset:64\n\t"
        "global_load_dwordx4 %[r3], v223, %[xb] offset:96\n\t"
        "4:\n\t"
        "v_exp_f32 v96, v96\n\t"
        "v_exp_f32 v97, v97\n\t"
        "v_smfmac_f32_32x32x32_bf16 v[16:31], v[196:199], v[120:127], v148\n\t"
        "v_exp_f32 v98, v98\n\t"
        "v_exp_f32 v99, v99\n\t"
        "v_exp_f32 v100, v100\n\t"
        "v_smfmac_f32_16x16x64_bf16 v[132:135], v[144:147], v[120:127], v148\n\t"
        "v_exp_f32 v101, v101\n\t"
        "v_mfma_f32_32x32x16_bf16 v[112:127], v[200:203], v[72:75], 0\n\t"
        "v_exp_f32 v102, v102\n\t"
        "v_exp_f32 v103, v103\n\t"
        "v_exp_f32 v104, v104\n\t"
        "v_mfma_f32_32x32x16_bf16 v[112:127], v[204:207], v[76:79], v[112:127]\n\t"
        "v_exp_f32 v105, v105\n\t"
        "v_exp_f32 v106, v106\n\t"
        "v_exp_f32 v107, v107\n\t"
        "v_smfmac_f32_32x32x32_bf16 v[0:15], v[208:211], v[96:103], v148\n\t"
        "v_exp_f32 v108, v108\n\t"
        "v_exp_f32 v109, v109\n\t"
        "v_exp_f32 v110, v110\n\t"
        "v_smfmac_f32_16x16x64_bf16 v[128:131], v[144:147], v[96:103], v148\n\t"
        "v_exp_f32 v111, v111\n\t"
        "v_exp_f32 v112, v112\n\t"
        "v_exp_f32 v113, v113\n\t"
        "v_smfmac_f32_32x32x32_bf16 v[0:15], v[212:215], v[104:111], v148\n\t"
        "v_exp_f32 v114, v114\n\t"
        "v_exp_f32 v115, v115\n\t"
        "v_exp_f32 v116, v116\n\t"
        "v_smfmac_f32_16x16x64_bf16 v[128:131], v[144:147], v[104:111], v148\n\t"
        "v_exp_f32 v117, v117\n\t"
        "v_mfma_f32_32x32x16_bf16 v[96:111], v[184:187], v[80:83], 0\n\t"
        "v_exp_f32 v118, v118\n\t"
        "v_exp_f32 v119, v119\n\t"
        "v_exp_f32 v120, v120\n\t"
        "ds_write_b128 v216, v[0:3] offset:0\n\t"
        "v_mfma_f32_32x32x16_bf16 v[96:111], v[188:191], v[84:87], v[96:111]\n\t"
        "v_exp_f32 v121, v121\n\t"
        "ds_write_b128 v216, v[4:7] offset:1024\n\t"
        "v_exp_f32 v122, v122\n\t"
        "ds_write_b128 v216, v[8:11] offset:2048\n\t"
        "v_exp_f32 v123, v123\n\t"
        "ds_write_b128 v216, v[12:15] offset:3072\n\t"
        "v_smfmac_f32_32x32x32_bf16 v[16:31], v[208:211], v[112:119], v148\n\t"
        "v_exp_f32 v124, v124\n\t"
        "ds_write_b32 v218, v128 offset:0\n\t"
        "v_exp_f32 v125, v125\n\t"
        "v_exp_f32 v126, v126\n\t"
        "v_smfmac_f32_16x16x64_bf16 v[132:135], v[144:147], v[112:119], v148\n\t"
        "v_exp_f32 v127, v127\n\t"
        "v_exp_f32 v96, v96\n\t"
        "v_exp_f32 v97, v97\n\t"
        "v_smfmac_f32_32x32x32_bf16 v[16:31], v[212:215], v[120:127], v148\n\t"
        "v_exp_f32 v98, v98\n\t"
        "v_exp_f32 v99, v99\n\t"
        "v_exp_f32 v100, v100\n\t"
        "v_smfmac_f32_16x16x64_bf16 v[132:135], v[144:147], v[120:127], v148\n\t"
        "v_exp_f32 v101, v101\n\t"
        "v_mfma_f32_32x32x16_bf16 v[112:127], v[184:187], v[88:91], 0\n\t"
        "v_exp_f32 v102, v102\n\t"
        "v_exp_f32 v103, v103\n\t"
        "v_exp_f32 v104, v104\n\t"
        "ds_write_b128 v216, v[16:19] offset:4096\n\t"
        "v_mfma_f32_32x32x16_bf16 v[112:127], v[188:191], v[92:95], v[112:127]\n\t"
        "v_exp_f32 v105, v105\n\t"
        "ds_write_b128 v216, v[20:23] offset:5120\n\t"
        "v_exp_f32 v106, v106\n\t"
        "ds_write_b128 v216, v[24:27] offset:6144\n\t"
        "v_exp_f32 v107, v107\n\t"
        "ds_write_b128 v216, v[28:31] offset:7168\n\t"
        "v_smfmac_f32_32x32x32_bf16 v[32:47], v[192:195], v[96:103], v148\n\t"
        "v_exp_f32 v108, v108\n\t"
        "ds_write_b32 v218, v132 offset:256\n\t"
        "v_exp_f32 v109, v109\n\t"
        "ds_write_b32 v220, v222\n\t"
        "v_exp_f32 v110, v110\n\t"
        "v_smfmac_f32_16x16x64_bf16 v[136:139], v[144:147], v[96:103], v148\n\t"
        "v_exp_f32 v111, v111\n\t"
        "v_exp_f32 v112, v112\n\t"
        "v_exp_f32 v113, v113\n\t"
        "v_smfmac_f32_32x32x32_bf16 v[32:47], v[196:199], v[104:111], v148\n\t"
        "v_exp_f32 v114, v114\n\t"
        "v_exp_f32 v115, v115\n\t"
        "v_exp_f32 v116, v116\n\t"
        "v_smfmac_f32_16x16x64_bf16 v[136:139], v[144:147], v[104:111], v148\n\t"
        "v_exp_f32 v117, v117\n\t"
        "v_mfma_f32_32x32x16_bf16 v[96:111], v[200:203], v[80:83], 0\n\t"
        "v_exp_f32 v118, v118\n\t"
        "v_exp_f32 v119, v119\n\t"
        "v_exp_f32 v120, v120\n\t"
        "v_mfma_f32_32x32x16_bf16 v[96:111], v[204:207], v[84:87], v[96:111]\n\t"
        "v_exp_f32 v121, v121\n\t"
        "v_exp_f32 v122, v122\n\t"
        "v_exp_f32 v123, v123\n\t"
        "v_smfmac_f32_32x32x32_bf16 v[48:63], v[192:195], v[112:119], v148\n\t"
        "v_exp_f32 v124, v124\n\t"
        "v_exp_f32 v125, v125\n\t"
        "v_exp_f32 v126, v126\n\t"
        "v_smfmac_f32_16x16x64_bf16 v[140:143], v[144:147], v[112:119], v148\n\t"
        "v_exp_f32 v127, v127\n\t"
        "v_exp_f32 v96, v96\n\t"
        "v_exp_f32 v97, v97\n\t"
        "v_smfmac_f32_32x32x32_bf16 v[48:63], v[196:199], v[120:127], v148\n\t"
        "v_exp_f32 v98, v98\n\t"
        "v_exp_f32 v99, v99\n\t"
        "v_exp_f32 v100, v100\n\t"
        "v_smfmac_f32_16x16x64_bf16 v[140:143], v[144:147], v[120:127], v148\n\t"
        "v_exp_f32 v101, v101\n\t"
        "v_mfma_f32_32x32x16_bf16 v[112:127], v[200:203], v[88:91], 0\n\t"
        "v_exp_f32 v102, v102\n\t"
        "v_exp_f32 v103, v103\n\t"
        "v_exp_f32 v104, v104\n\t"
        "ds_read_b32 v151, v221\n\t"
        "v_mfma_f32_32x32x16_bf16 v[112:127], v[204:207], v[92:95], v[112:127]\n\t"
        "v_exp_f32 v105, v105\n\t"
        "v_exp_f32 v106, v106\n\t"
        "v_exp_f32 v107, v107\n\t"
        "v_smfmac_f32_32x32x32_bf16 v[32:47], v[208:211], v[96:103], v148\n\t"
        "v_exp_f32 v108, v108\n\t"
        "v_exp_f32 v109, v109\n\t"
        "v_exp_f32 v110, v110\n\t"
        "v_smfmac_f32_16x16x64_bf16 v[136:139], v[144:147], v[96:103], v148\n\t"
        "v_exp_f32 v111, v111\n\t"
        "s_waitcnt lgkmcnt(0)\n\t"
        "v_cmp_ne_u32 vcc, v222, v151\n\t"
        "s_cbranch_vccnz 40f\n\t"
        "41:\n\t"
        "v_exp_f32 v112, v112\n\t"
        "ds_read_b128 v[0:3], v217 offset:0\n\t"
        "v_exp_f32 v113, v113\n\t"
        "ds_read_b128 v[4:7], v217 offset:1024\n\t"
        "v_smfmac_f32_32x32x32_bf16 v[32:47], v[212:215], v[104:111], v148\n\t"
        "v_exp_f32 v114, v114\n\t"
        "ds_read_b128 v[8:11], v217 offset:2048\n\t"
        "v_exp_f32 v115, v115\n\t"
        "ds_read_b128 v[12:15], v217 offset:3072\n\t"
        "v_exp_f32 v116, v116\n\t"
        "ds_read_b32 v128, v219 offset:0\n\t"
        "v_smfmac_f32_16x16x64_bf16 v[136:139], v[144:147], v[104:111], v148\n\t"
        "v_exp_f32 v117, v117\n\t"
        "ds_read_b128 v[16:19], v217 offset:4096\n\t"
        "v_exp_f32 v118, v118\n\t"
        "ds_read_b128 v[20:23], v217 offset:5120\n\t"
        "v_exp_f32 v119, v119\n\t"
        "ds_read_b128 v[24:27], v217 offset:6144\n\t"
        "v_exp_f32 v120, v120\n\t"
        "ds_read_b128 v[28:31], v217 offset:7168\n\t"
        "v_exp_f32 v121, v121\n\t"
        "ds_read_b32 v132, v219 offset:256\n\t"
        "v_exp_f32 v122, v122\n\t"
        "v_exp_f32 v123, v123\n\t"
        "v_smfmac_f32_32x32x32_bf16 v[48:63], v[208:211], v[112:119], v148\n\t"
        "v_exp_f32 v124, v124\n\t"
        "v_exp_f32 v125, v125\n\t"
        "v_exp_f32 v126, v126\n\t"
        "v_smfmac_f32_16x16x64_bf16 v[140:143], v[144:147], v[112:119], v148\n\t"
        "v_exp_f32 v127, v127\n\t"
        "s_nop 1\n\t"
        "v_smfmac_f32_32x32x32_bf16 v[48:63], v[212:215], v[120:127], v148\n\t"
        "v_smfmac_f32_16x16x64_bf16 v[140:143], v[144:147], v[120:127], v148\n\t"
        "s_waitcnt vmcnt(0) lgkmcnt(0)\n\t"
        "s_nop 6\n\t"
        "s_branch 9f\n\t"
        "24:\n\t"
        "s_add_u32 s44, s44, 0x4000\n\t"
        "s_and_b32 s44, s44, 0x3ffff\n\t"
        "buffer_load_dwordx4 v[184:187], v149, s[40:43], s44 offen offset:0 sc1\n\t"
        "buffer_load_dwordx4 v[188:191], v149, s[40:43], s44 offen offset:1024 sc1\n\t"
        "buffer_load_dwordx4 v[192:195], v149, s[40:43], s44 offen offset:2048 sc1\n\t"
        "buffer_load_dwordx4 v[196:199], v149, s[40:43], s44 offen offset:3072 sc1\n\t"
        "buffer_load_dwordx4 v[200:203], v150, s[40:43], s44 offen offset:0 sc1\n\t"
        "buffer_load_dwordx4 v[204:207], v150, s[40:43], s44 offen offset:1024 sc1\n\t"
        "buffer_load_dwordx4 v[208:211], v150, s[40:43], s44 offen offset:2048 sc1\n\t"
        "buffer_load_dwordx4 v[212:215], v150, s[40:43], s44 offen offset:3072 sc1\n\t"
        "s_mov_b32 s46, 1\n\t"
        "s_branch 25b\n\t"
        "26:\n\t"
        "s_add_u32 s44, s44, 0x4000\n\t"
        "s_and_b32 s44, s44, 0x3ffff\n\t"
        "buffer_load_dwordx4 v[184:187], v149, s[40:43], s44 offen offset:0 sc1\n\t"
        "buffer_load_dwordx4 v[188:191], v149, s[40:43], s44 offen offset:1024 sc1\n\t"
        "buffer_load_dwordx4 v[192:195], v149, s[40:43], s44 offen offset:2048 sc1\n\t"
        "buffer_load_dwordx4 v[196:199], v149, s[40:43], s44 offen offset:3072 sc1\n\t"
        "buffer_load_dwordx4 v[200:203], v150, s[40:43], s44 offen offset:0 sc1\n\t"
        "buffer_load_dwordx4 v[204:207], v150, s[40:43], s44 offen offset:1024 sc1\n\t"
        "buffer_load_dwordx4 v[208:211], v150, s[40:43], s44 offen offset:2048 sc1\n\t"
        "buffer_load_dwordx4 v[212:215], v150, s[40:43], s44 offen offset:3072 sc1\n\t"
        "s_mov_b32 s46, 1\n\t"
        "s_branch 27b\n\t"
        "28:\n\t"
        "s_add_u32 s44, s44, 0x4000\n\t"
        "s_and_b32 s44, s44, 0x3ffff\n\t"
        "buffer_load_dwordx4 v[184:187], v149, s[40:43], s44 offen offset:0 sc1\n\t"
        "buffer_load_dwordx4 v[188:191], v149, s[40:43], s44 offen offset:1024 sc1\n\t"
        "buffer_load_dwordx4 v[192:195], v149, s[40:43], s44 offen offset:2048 sc1\n\t"
        "buffer_load_dwordx4 v[196:199], v149, s[40:43], s44 offen offset:3072 sc1\n\t"
        "buffer_load_dwordx4 v[200:203], v150, s[40:43], s44 offen offset:0 sc1\n\t"
        "buffer_load_dwordx4 v[204:207], v150, s[40:43], s44 offen offset:1024 sc1\n\t"
        "buffer_load_dwordx4 v[208:211], v150, s[40:43], s44 offen offset:2048 sc1\n\t"
        "buffer_load_dwordx4 v[212:215], v150, s[40:43], s44 offen offset:3072 sc1\n\t"
        "s_mov_b32 s46, 1\n\t"
        "s_branch 29b\n\t"
        "30:\n\t"
        "s_add_u32 s44, s44, 0x4000\n\t"
        "s_and_b32 s44, s44, 0x3ffff\n\t"
        "buffer_load_dwordx4 v[184:187], v149, s[40:43], s44 offen offset:0 sc1\n\t"
        "buffer_load_dwordx4 v[188:191], v149, s[40:43], s44 offen offset:1024 sc1\n\t"
        "buffer_load_dwordx4 v[192:195], v149, s[40:43], s44 offen offset:2048 sc1\n\t"
        "buffer_load_dwordx4 v[196:199], v149, s[40:43], s44 offen offset:3072 sc1\n\t"
        "buffer_load_dwordx4 v[200:203], v150, s[40:43], s44 offen offset:0 sc1\n\t"
        "buffer_load_dwordx4 v[204:207], v150, s[40:43], s44 offen offset:1024 sc1\n\t"
        "buffer_load_dwordx4 v[208:211], v150, s[40:43], s44 offen offset:2048 sc1\n\t"
        "buffer_load_dwordx4 v[212:215], v150, s[40:43], s44 offen offset:3072 sc1\n\t"
        "s_mov_b32 s46, 1\n\t"
        "s_branch 31b\n\t"
        "32:\n\t"
        "s_add_u32 s44, s44, 0x4000\n\t"
        "s_and_b32 s44, s44, 0x3ffff\n\t"
        "buffer_load_dwordx4 v[184:187], v149, s[40:43], s44 offen offset:0 sc1\n\t"
        "buffer_load_dwordx4 v[188:191], v149, s[40:43], s44 offen offset:1024 sc1\n\t"
        "buffer_load_dwordx4 v[192:195], v149, s[40:43], s44 offen offset:2048 sc1\n\t"
        "buffer_load_dwordx4 v[196:199], v149, s[40:43], s44 offen offset:3072 sc1\n\t"
        "buffer_load_dwordx4 v[200:203], v150, s[40:43], s44 offen offset:0 sc1\n\t"
        "buffer_load_dwordx4 v[204:207], v150, s[40:43], s44 offen offset:1024 sc1\n\t"
        "buffer_load_dwordx4 v[208:211], v150, s[40:43], s44 offen offset:2048 sc1\n\t"
        "buffer_load_dwordx4 v[212:215], v150, s[40:43], s44 offen offset:3072 sc1\n\t"
        "s_mov_b32 s46, 1\n\t"
        "s_branch 33b\n\t"
        "40:\n\t"
        "s_mov_b32 s45, 0x100000\n\t"
        "42:\n\t"
        "ds_read_b32 v151, v221\n\t"
        "s_sub_u32 s45, s45, 1\n\t"
        "s_waitcnt lgkmcnt(0)\n\t"
        "s_cbranch_scc1 41b\n\t"
        "v_cmp_ne_u32 vcc, v222, v151\n\t"
        "s_cbranch_vccnz 42b\n\t"
        "s_branch 41b\n\t"
        "9:\n\t"
        : "+{v[0:15]}"(o0), "+{v[16:31]}"(o1), "+{v[32:47]}"(o2), "+{v[48:63]}"(o3), "+{v[128:143]}"(lv), "+{s44}"(soff), "+{v[152:167]}"(kva), "+{v[168:183]}"(kvb), "+{v[184:191]}"(fv),
          [r0] "+v"(x1p[0]), [r1] "+v"(x1p[1]), [r2] "+v"(x1p[2]), [r3] "+v"(x1p[3]) STAMP_OUTS
        : "{v[64:71]}"(q[0]), "{v[72:79]}"(q[1]), "{v[80:87]}"(q[2]), "{v[88:95]}"(q[3]), "{v[144:147]}"(ones), "{v148}"(0xddddu), "{v149}"(kvoff), "{v150}"(kvoff + 4096),
          "{v[216:223]}"(cv), "{v224}"(lbase + L_W + 16), "{s[40:43]}"(srd), "{s47}"(L), [xb] "s"(x0)
        : "v96", "v97", "v98", "v99", "v100", "v101", "v102", "v103", "v104", "v105", "v106", "v107", "v108", "v109", "v110", "v111", "v112", "v113", "v114", "v115", "v116", "v117", "v118", "v119", "v120", "v121", "v122", "v123", "v124", "v125", "v126", "v127", "v151", "v192", "v193", "v194", "v195", "v196", "v197", "v198", "v199", "v200", "v201", "v202", "v203", "v204", "v205", "v206", "v207", "v208", "v209", "v210", "v211", "v212", "v213", "v214", "v215", "s45", "s46", "vcc", "scc", "memory");
      STAMP_PUT(6 + 16 * L);
      STAMPV(9 + 16 * L);
      PHASE_VARS;
      const unsigned dst = OFF_OF + L * (5 * MBY) + (tm * 4 + 2 * kh) * TS + (tn * 2 + hh) * 2048 + lane * 16;
      auto put = [&](int u, const f32x16& o) {
#pragma unroll
        for (int c = 0; c < 2; ++c) {
          u32x4 w;
#pragma unroll
          for (int e = 0; e < 4; ++e) w[e] = pack2(o[8 * c + 2 * e], o[8 * c + 2 * e + 1]);
          stg(rs, dst + u * TS + c * 1024, w);
        }
      };
      const float la = lv[8] + lv[0], lb = lv[12] + lv[4];
      o2 = (o2 + o0) * RCP(la); put(0, o2);
      SBAR();
      o3 = (o3 + o1) * RCP(lb); put(1, o3);
      SBAR();
      {
        const f32x16 t = o2 + o3;
        float sum = 0.f;
#pragma unroll
        for (int e = 0; e < 16; e += 4) sum += (t[e] + t[e + 1]) + (t[e + 2] + t[e + 3]);
        if (__any(!(fabsf(sum) < 1e37f) | !(fmaxf(la, lb) < 1e27f) | !(fminf(la, lb) > 1e-9f) | !(la + lb == la + lb))) {
          DRAIN();
          attn_safe(rs, kvoff - kh * 8192, q[2], ones, o2); put(0, o2);
          attn_safe(rs, kvoff - kh * 8192, q[3], ones, o3); put(1, o3);
        }
      }
    }
    STAMPV(10 + 16 * L);
    {
      f32x16 acc[8];
      u32x2 wrc[8];
      STAMP_DECL;
      { PHASE_VARS;
      float r0 = 0.f, r1 = 1.f;
      if (L == 0) {
#pragma unroll
        for (int i = 0; i < 4; ++i) x1p[i] = fo_swap(x1p[i]);
      } else {
        r0 = reinterpret_cast<const float*>(smem + L_AB)[256 + wid * 64 + r32]; r1 = reinterpret_cast<const float*>(smem + L_AB)[256 + wid * 64 + 32 + r32];
      }
#pragma unroll
      for (int e = 0; e < 16; ++e) { acc[e >> 3][2 * (e & 7)] = lo16(x1p[e >> 2][e & 3]) * r1 + r0; acc[e >> 3][2 * (e & 7) + 1] = hi16(x1p[e >> 2][e & 3]) * r1 + r0; }
      PIN(acc[0]); PIN(acc[1]);
      asm volatile("s_waitcnt vmcnt(0)\n\tds_write_b32 %0, %1" :: "v"(lbase + L_W + 80 + 4 * wid), "v"(2 * L + 2) : "memory");
      STAMPV(11 + 16 * L);
      const unsigned a0 = lbase + wrow * 128 + ((wch ^ wsw) << 4) + 8 * (tp & 1), a1 = lbase + wrow * 128 + (((4 + wch) ^ wsw) << 4) + 8 * (tp & 1);
      asm volatile(
        "s_mov_b32 s46, 0\n\t"
        "ostart_%=:\n\t"
        "s_mov_b32 s48, 0x200000\n\t"
        "2:\n\t"
        "ds_read_b32 v233, v232\n\t"
        "s_sub_u32 s48, s48, 1\n\t"
        "s_waitcnt lgkmcnt(0)\n\t"
        "s_cselect_b32 s47, 15, 0\n\t"
        "v_readfirstlane_b32 s51, v233\n\t"
        "s_nop 3\n\t"
        "s_or_b32 s51, s51, s47\n\t"
        "s_andn2_b32 s47, s51, s46\n\t"
        "s_cbranch_scc0 2b\n\t"
        "s_andn2_b32 s51, s51, s46\n\t"
        "s_ff1_i32_b32 s47, s51\n\t"
        "s_add_u32 s45, s47, s49\n\t"
        "s_and_b32 s45, s45, 3\n\t"
        "s_lshl_b32 s45, s45, 14\n\t"
        "s_or_b32 s45, s45, s50\n\t"
        "s_and_b32 s51, s47, 0x80000000\n\t"
        "v_or_b32 v225, s51, v224\n\t"
        "s_and_b32 s51, s47, 3\n\t"
        "s_lshl_b32 s51, s51, 15\n\t"
        "v_add_u32 v230, s51, v226\n\t"
        "v_add_u32 v231, s51, v227\n\t"
        "buffer_load_dwordx4 v[160:163], v225, s[40:43], s45 offen offset:0 sc1\n\t"
        "buffer_load_dwordx4 v[164:167], v225, s[40:43], s45 offen offset:1024 sc1\n\t"
        "buffer_load_dwordx4 v[168:171], v225, s[40:43], s45 offen offset:2048 sc1\n\t"
        "buffer_load_dwordx4 v[172:175], v225, s[40:43], s45 offen offset:3072 sc1\n\t"
        "s_add_u32 s51, s45, 0x1000\n\t"
        "s_and_b32 s51, s51, 0x3fff\n\t"
        "s_andn2_b32 s45, s45, 0x3fff\n\t"
        "s_or_b32 s45, s45, s51\n\t"
        "buffer_load_dwordx4 v[176:179], v225, s[40:43], s45 offen offset:0 sc1\n\t"
        "buffer_load_dwordx4 v[180:183], v225, s[40:43], s45 offen offset:1024 sc1\n\t"
        "buffer_load_dwordx4 v[184:187], v225, s[40:43], s45 offen offset:2048 sc1\n\t"
        "buffer_load_dwordx4 v[188:191], v225, s[40:43], s45 offen offset:3072 sc1\n\t"
        "s_add_u32 s51, s45, 0x1000\n\t"
        "s_and_b32 s51, s51, 0x3fff\n\t"
        "s_andn2_b32 s45, s45, 0x3fff\n\t"
        "s_or_b32 s45, s45, s51\n\t"
        "buffer_load_dwordx4 v[192:195], v225, s[40:43], s45 offen offset:0 sc1\n\t"
        "buffer_load_dwordx4 v[196:199], v225, s[40:43], s45 offen offset:1024 sc1\n\t"
        "buffer_load_dwordx4 v[200:203], v225, s[40:43], s45 offen offset:2048 sc1\n\t"
        "buffer_load_dwordx4 v[204:207], v225, s[40:43], s45 offen offset:3072 sc1\n\t"
        "ds_read_b64_tr_b16 v[128:129], v230 offset:0\n\t"
        "ds_read_b64_tr_b16 v[130:131], v230 offset:1024\n\t"
        "ds_read_b64_tr_b16 v[132:133], v231 offset:0\n\t"
        "ds_read_b64_tr_b16 v[134:135], v231 offset:1024\n\t"
        "ds_read_b64_tr_b16 v[136:137], v230 offset:2048\n\t"
        "ds_read_b64_tr_b16 v[138:139], v230 offset:3072\n\t"
        "ds_read_b64_tr_b16 v[140:141], v231 offset:2048\n\t"
        "ds_read_b64_tr_b16 v[142:143], v231 offset:3072\n\t"
        "ds_read_b64_tr_b16 v[144:145], v230 offset:4096\n\t"
        "ds_read_b64_tr_b16 v[146:147], v230 offset:5120\n\t"
        "ds_read_b64_tr_b16 v[148:149], v231 offset:4096\n\t"
        "ds_read_b64_tr_b16 v[150:151], v231 offset:5120\n\t"
        "ds_read_b64_tr_b16 v[152:153], v230 offset:6144\n\t"
        "ds_read_b64_tr_b16 v[154:155], v230 offset:7168\n\t"
        "ds_read_b64_tr_b16 v[156:157], v231 offset:6144\n\t"
        "ds_read_b64_tr_b16 v[158:159], v231 offset:7168\n\t"
        "s_mov_b32 s44, s45\n\t"
        "s_cmp_eq_u32 s47, 0\n\t"
        "s_cbranch_scc1 oseg0_%=\n\t"
        "s_cmp_eq_u32 s47, 1\n\t"
        "s_cbranch_scc1 oseg1_%=\n\t"
        "s_cmp_eq_u32 s47, 2\n\t"
        "s_cbranch_scc1 oseg2_%=\n\t"
        "s_cmp_eq_u32 s47, 3\n\t"
        "s_cbranch_scc1 oseg3_%=\n\t"
        "oseg0_%=:\n\t"
        "s_or_b32 s46, s46, 1\n\t"
        "s_waitcnt vmcnt(11) lgkmcnt(14)\n\t"
        "v_mfma_f32_32x32x16_bf16 v[0:15], v[128:131], v[160:163], v[0:15]\n\t"
        "ds_read_b64_tr_b16 v[128:129], v226 offset:8192\n\t"
        "ds_read_b64_tr_b16 v[130:131], v226 offset:9216\n\t"
        "s_waitcnt lgkmcnt(14)\n\t"
        "v_mfma_f32_32x32x16_bf16 v[16:31], v[132:135], v[160:163], v[16:31]\n\t"
        "ds_read_b64_tr_b16 v[132:133], v227 offset:8192\n\t"
        "ds_read_b64_tr_b16 v[134:135], v227 offset:9216\n\t"
        "s_add_u32 s51, s44, 0x1000\n\t"
        "s_and_b32 s51, s51, 0x3fff\n\t"
        "s_andn2_b32 s44, s44, 0x3fff\n\t"
        "s_or_b32 s44, s44, s51\n\t"
        "buffer_load_dwordx4 v[208:211], v224, s[40:43], s44 offen offset:0 sc1\n\t"
        "s_waitcnt vmcnt(11) lgkmcnt(14)\n\t"
        "v_mfma_f32_32x32x16_bf16 v[0:15], v[136:139], v[164:167], v[0:15]\n\t"
        "ds_read_b32 v233, v232\n\t"
        "ds_read_b64_tr_b16 v[136:137], v226 offset:10240\n\t"
        "ds_read_b64_tr_b16 v[138:139], v226 offset:11264\n\t"
        "s_waitcnt lgkmcnt(14)\n\t"
        "v_mfma_f32_32x32x16_bf16 v[16:31], v[140:143], v[164:167], v[16:31]\n\t"
        "ds_read_b64_tr_b16 v[140:141], v227 offset:10240\n\t"
        "ds_read_b64_tr_b16 v[142:143], v227 offset:11264\n\t"
        "buffer_load_dwordx4 v[212:215], v224, s[40:43], s44 offen offset:1024 sc1\n\t"
        "s_waitcnt vmcnt(11) lgkmcnt(14)\n\t"
        "v_mfma_f32_32x32x16_bf16 v[0:15], v[144:147], v[168:171], v[0:15]\n\t"
        "ds_read_b64_tr_b16 v[144:145], v226 offset:12288\n\t"
        "ds_read_b64_tr_b16 v[146:147], v226 offset:13312\n\t"
        "s_waitcnt lgkmcnt(14)\n\t"
        "v_mfma_f32_32x32x16_bf16 v[16:31], v[148:151], v[168:171], v[16:31]\n\t"
        "ds_read_b64_tr_b16 v[148:149], v227 offset:12288\n\t"
        "ds_read_b64_tr_b16 v[150:151], v227 offset:13312\n\t"
        "buffer_load_dwordx4 v[216:219], v224, s[40:43], s44 offen offset:2048 sc1\n\t"
        "s_waitcnt vmcnt(11) lgkmcnt(8)\n\t"
        "v_mfma_f32_32x32x16_bf16 v[0:15], v[152:155], v[172:175], v[0:15]\n\t"
        "v_readfirstlane_b32 s51, v233\n\t"
        "ds_read_b64_tr_b16 v[152:153], v226 offset:14336\n\t"
        "ds_read_b64_tr_b16 v[154:155], v226 offset:15360\n\t"
        "s_waitcnt lgkmcnt(14)\n\t"
        "v_mfma_f32_32x32x16_bf16 v[16:31], v[156:159], v[172:175], v[16:31]\n\t"
        "s_andn2_b32 s51, s51, s46\n\t"
        "s_ff1_i32_b32 s47, s51\n\t"
        "s_add_u32 s45, s47, s49\n\t"
        "s_and_b32 s45, s45, 3\n\t"
        "s_lshl_b32 s45, s45, 14\n\t"
        "s_or_b32 s45, s45, s50\n\t"
        "ds_read_b64_tr_b16 v[156:157], v227 offset:14336\n\t"
        "ds_read_b64_tr_b16 v[158:159], v227 offset:15360\n\t"
        "buffer_load_dwordx4 v[220:223], v224, s[40:43], s44 offen offset:3072 sc1\n\t"
        "s_waitcnt vmcnt(11) lgkmcnt(14)\n\t"
        "v_mfma_f32_32x32x16_bf16 v[0:15], v[128:131], v[176:179], v[0:15]\n\t"
        "s_and_b32 s51, s47, 0x80000000\n\t"
        "v_or_b32 v225, s51, v224\n\t"
        "s_and_b32 s51, s47, 3\n\t"
        "s_lshl_b32 s51, s51, 15\n\t"
        "v_add_u32 v230, s51, v226\n\t"
        "v_add_u32 v231, s51, v227\n\t"
        "ds_read_b64_tr_b16 v[128:129], v226 offset:16384\n\t"
        "ds_read_b64_tr_b16 v[130:131], v226 offset:17408\n\t"
        "s_waitcnt lgkmcnt(14)\n\t"
        "v_mfma_f32_32x32x16_bf16 v[16:31], v[132:135], v[176:179], v[16:31]\n\t"
        "ds_read_b64_tr_b16 v[132:133], v227 offset:16384\n\t"
        "ds_read_b64_tr_b16 v[134:135], v227 offset:17408\n\t"
        "buffer_load_dwordx4 v[160:163], v225, s[40:43], s45 offen offset:0 sc1\n\t"
        "s_waitcnt vmcnt(11) lgkmcnt(14)\n\t"
        "v_mfma_f32_32x32x16_bf16 v[0:15], v[136:139], v[180:183], v[0:15]\n\t"
        "ds_read_b64_tr_b16 v[136:137], v226 offset:18432\n\t"
        "ds_read_b64_tr_b16 v[138:139], v226 offset:19456\n\t"
        "s_waitcnt lgkmcnt(14)\n\t"
        "v_mfma_f32_32x32x16_bf16 v[16:31], v[140:143], v[180:183], v[16:31]\n\t"
        "ds_read_b64_tr_b16 v[140:141], v227 offset:18432\n\t"
        "ds_read_b64_tr_b16 v[142:143], v227 offset:19456\n\t"
        "buffer_load_dwordx4 v[164:167], v225, s[40:43], s45 offen offset:1024 sc1\n\t"
        "s_waitcnt vmcnt(11) lgkmcnt(14)\n\t"
        "v_mfma_f32_32x32x16_bf16 v[0:15], v[144:147], v[184:187], v[0:15]\n\t"
        "ds_read_b64_tr_b16 v[144:145], v226 offset:20480\n\t"
        "ds_read_b64_tr_b16 v[146:147], v226 offset:21504\n\t"
        "s_waitcnt lgkmcnt(14)\n\t"
        "v_mfma_f32_32x32x16_bf16 v[16:31], v[148:151], v[184:187], v[16:31]\n\t"
        "ds_read_b64_tr_b16 v[148:149], v227 offset:20480\n\t"
        "ds_read_b64_tr_b16 v[150:151], v227 offset:21504\n\t"
        "buffer_load_dwordx4 v[168:171], v225, s[40:43], s45 offen offset:2048 sc1\n\t"
        "s_waitcnt vmcnt(11) lgkmcnt(14)\n\t"
        "v_mfma_f32_32x32x16_bf16 v[0:15], v[152:155], v[188:191], v[0:15]\n\t"
        "ds_read_b64_tr_b16 v[152:153], v226 offset:22528\n\t"
        "ds_read_b64_tr_b16 v[154:155], v226 offset:23552\n\t"
        "s_waitcnt lgkmcnt(14)\n\t"
        "v_mfma_f32_32x32x16_bf16 v[16:31], v[156:159], v[188:191], v[16:31]\n\t"
        "ds_read_b64_tr_b16 v[156:157], v227 offset:22528\n\t"
        "ds_read_b64_tr_b16 v[158:159], v227 offset:23552\n\t"
        "buffer_load_dwordx4 v[172:175], v225, s[40:43], s45 offen offset:3072 sc1\n\t"
        "s_waitcnt vmcnt(11) lgkmcnt(14)\n\t"
        "v_mfma_f32_32x32x16_bf16 v[0:15], v[128:131], v[192:195], v[0:15]\n\t"
        "ds_read_b64_tr_b16 v[128:129], v226 offset:24576\n\t"
        "ds_read_b64_tr_b16 v[130:131], v226 offset:25600\n\t"
        "s_waitcnt lgkmcnt(14)\n\t"
        "v_mfma_f32_32x32x16_bf16 v[16:31], v[132:135], v[192:195], v[16:31]\n\t"
        "ds_read_b64_tr_b16 v[132:133], v227 offset:24576\n\t"
        "ds_read_b64_tr_b16 v[134:135], v227 offset:25600\n\t"
        "s_add_u32 s51, s45, 0x1000\n\t"
        "s_and_b32 s51, s51, 0x3fff\n\t"
        "s_andn2_b32 s45, s45, 0x3fff\n\t"
        "s_or_b32 s45, s45, s51\n\t"
        "buffer_load_dwordx4 v[176:179], v225, s[40:43], s45 offen offset:0 sc1\n\t"
        "s_waitcnt vmcnt(11) lgkmcnt(14)\n\t"
        "v_mfma_f32_32x32x16_bf16 v[0:15], v[136:139], v[196:199], v[0:15]\n\t"
        "ds_read_b64_tr_b16 v[136:137], v226 offset:26624\n\t"
        "ds_read_b64_tr_b16 v[138:139], v226 offset:27648\n\t"
        "s_waitcnt lgkmcnt(14)\n\t"
        "v_mfma_f32_32x32x16_bf16 v[16:31], v[140:143], v[196:199], v[16:31]\n\t"
        "ds_read_b64_tr_b16 v[140:141], v227 offset:26624\n\t"
        "ds_read_b64_tr_b16 v[142:143], v227 offset:27648\n\t"
        "buffer_load_dwordx4 v[180:183], v225, s[40:43], s45 offen offset:1024 sc1\n\t"
        "s_waitcnt vmcnt(11) lgkmcnt(14)\n\t"
        "v_mfma_f32_32x32x16_bf16 v[0:15], v[144:147], v[200:203], v[0:15]\n\t"
        "ds_read_b64_tr_b16 v[144:145], v226 offset:28672\n\t"
        "ds_read_b64_tr_b16 v[146:147], v226 offset:29696\n\t"
        "s_waitcnt lgkmcnt(14)\n\t"
        "v_mfma_f32_32x32x16_bf16 v[16:31], v[148:151], v[200:203], v[16:31]\n\t"
        "ds_read_b64_tr_b16 v[148:149], v227 offset:28672\n\t"
        "ds_read_b64_tr_b16 v[150:151], v227 offset:29696\n\t"
        "buffer_load_dwordx4 v[184:187], v225, s[40:43], s45 offen offset:2048 sc1\n\t"
        "s_waitcnt vmcnt(11) lgkmcnt(14)\n\t"
        "v_mfma_f32_32x32x16_bf16 v[0:15], v[152:155], v[204:207], v[0:15]\n\t"
        "ds_read_b64_tr_b16 v[152:153], v226 offset:30720\n\t"
        "ds_read_b64_tr_b16 v[154:155], v226 offset:31744\n\t"
        "s_waitcnt lgkmcnt(14)\n\t"
        "v_mfma_f32_32x32x16_bf16 v[16:31], v[156:159], v[204:207], v[16:31]\n\t"
        "ds_read_b64_tr_b16 v[156:157], v227 offset:30720\n\t"
        "ds_read_b64_tr_b16 v[158:159], v227 offset:31744\n\t"
        "buffer_load_dwordx4 v[188:191], v225, s[40:43], s45 offen offset:3072 sc1\n\t"
        "s_waitcnt vmcnt(11) lgkmcnt(14)\n\t"
        "v_mfma_f32_32x32x16_bf16 v[0:15], v[128:131], v[208:211], v[0:15]\n\t"
        "ds_read_b64_tr_b16 v[128:129], v230 offset:0\n\t"
        "ds_read_b64_tr_b16 v[130:131], v230 offset:1024\n\t"
        "s_waitcnt lgkmcnt(14)\n\t"
        "v_mfma_f32_32x32x16_bf16 v[16:31], v[132:135], v[208:211], v[16:31]\n\t"
        "ds_read_b64_tr_b16 v[132:133], v231 offset:0\n\t"
        "ds_read_b64_tr_b16 v[134:135], v231 offset:1024\n\t"
        "s_add_u32 s51, s45, 0x1000\n\t"
        "s_and_b32 s51, s51, 0x3fff\n\t"
        "s_andn2_b32 s45, s45, 0x3fff\n\t"
        "s_or_b32 s45, s45, s51\n\t"
        "buffer_load_dwordx4 v[192:195], v225, s[40:43], s45 offen offset:0 sc1\n\t"
        "s_waitcnt vmcnt(11) lgkmcnt(14)\n\t"
        "v_mfma_f32_32x32x16_bf16 v[0:15], v[136:139], v[212:215], v[0:15]\n\t"
        "ds_read_b64_tr_b16 v[136:137], v230 offset:2048\n\t"
        "ds_read_b64_tr_b16 v[138:139], v230 offset:3072\n\t"
        "s_waitcnt lgkmcnt(14)\n\t"
        "v_mfma_f32_32x32x16_bf16 v[16:31], v[140:143], v[212:215], v[16:31]\n\t"
        "ds_read_b64_tr_b16 v[140:141], v231 offset:2048\n\t"
        "ds_read_b64_tr_b16 v[142:143], v231 offset:3072\n\t"
        "buffer_load_dwordx4 v[196:199], v225, s[40:43], s45 offen offset:1024 sc1\n\t"
        "s_waitcnt vmcnt(11) lgkmcnt(14)\n\t"
        "v_mfma_f32_32x32x16_bf16 v[0:15], v[144:147], v[216:219], v[0:15]\n\t"
        "ds_read_b64_tr_b16 v[144:145], v230 offset:4096\n\t"
        "ds_read_b64_tr_b16 v[146:147], v230 offset:5120\n\t"
        "s_waitcnt lgkmcnt(14)\n\t"
        "v_mfma_f32_32x32x16_bf16 v[16:31], v[148:151], v[216:219], v[16:31]\n\t"
        "ds_read_b64_tr_b16 v[148:149], v231 offset:4096\n\t"
        "ds_read_b64_tr_b16 v[150:151], v231 offset:5120\n\t"
        "buffer_load_dwordx4 v[200:203], v225, s[40:43], s45 offen offset:2048 sc1\n\t"
        "s_waitcnt vmcnt(11) lgkmcnt(14)\n\t"
        "v_mfma_f32_32x32x16_bf16 v[0:15], v[152:155], v[220:223], v[0:15]\n\t"
        "ds_read_b64_tr_b16 v[152:153], v230 offset:6144\n\t"
        "ds_read_b64_tr_b16 v[154:155], v230 offset:7168\n\t"
        "s_waitcnt lgkmcnt(14)\n\t"
        "v_mfma_f32_32x32x16_bf16 v[16:31], v[156:159], v[220:223], v[16:31]\n\t"
        "ds_read_b64_tr_b16 v[156:157], v231 offset:6144\n\t"
        "ds_read_b64_tr_b16 v[158:159], v231 offset:7168\n\t"
        "buffer_load_dwordx4 v[204:207], v225, s[40:43], s45 offen offset:3072 sc1\n\t"
        "s_mov_b32 s44, s45\n\t"
        "s_cmp_eq_u32 s47, 0\n\t"
        "s_cbranch_scc1 oseg0_%=\n\t"
        "s_cmp_eq_u32 s47, 1\n\t"
        "s_cbranch_scc1 oseg1_%=\n\t"
        "s_cmp_eq_u32 s47, 2\n\t"
        "s_cbranch_scc1 oseg2_%=\n\t"
        "s_cmp_eq_u32 s47, 3\n\t"
        "s_cbranch_scc1 oseg3_%=\n\t"
        "s_cmp_lg_u32 s46, 15\n\t"
        "s_cbranch_scc1 ostart_%=\n\t"
        "s_branch oend_%=\n\t"
        "oseg1_%=:\n\t"
        "s_or_b32 s46, s46, 2\n\t"
        "s_waitcnt vmcnt(11) lgkmcnt(14)\n\t"
        "v_mfma_f32_32x32x16_bf16 v[32:47], v[128:131], v[160:163], 0\n\t"
        "ds_read_b64_tr_b16 v[128:129], v226 offset:40960\n\t"
        "ds_read_b64_tr_b16 v[130:131], v226 offset:41984\n\t"
        "s_waitcnt lgkmcnt(14)\n\t"
        "v_mfma_f32_32x32x16_bf16 v[48:63], v[132:135], v[160:163], 0\n\t"
        "ds_read_b64_tr_b16 v[132:133], v227 offset:40960\n\t"
        "ds_read_b64_tr_b16 v[134:135], v227 offset:41984\n\t"
        "s_add_u32 s51, s44, 0x1000\n\t"
        "s_and_b32 s51, s51, 0x3fff\n\t"
        "s_andn2_b32 s44, s44, 0x3fff\n\t"
        "s_or_b32 s44, s44, s51\n\t"
        "buffer_load_dwordx4 v[208:211], v224, s[40:43], s44 offen offset:0 sc1\n\t"
        "s_waitcnt vmcnt(11) lgkmcnt(14)\n\t"
        "v_mfma_f32_32x32x16_bf16 v[32:47], v[136:139], v[164:167], v[32:47]\n\t"
        "ds_read_b32 v233, v232\n\t"
        "ds_read_b64_tr_b16 v[136:137], v226 offset:43008\n\t"
        "ds_read_b64_tr_b16 v[138:139], v226 offset:44032\n\t"
        "s_waitcnt lgkmcnt(14)\n\t"
        "v_mfma_f32_32x32x16_bf16 v[48:63], v[140:143], v[164:167], v[48:63]\n\t"
        "ds_read_b64_tr_b16 v[140:141], v227 offset:43008\n\t"
        "ds_read_b64_tr_b16 v[142:143], v227 offset:44032\n\t"
        "buffer_load_dwordx4 v[212:215], v224, s[40:43], s44 offen offset:1024 sc1\n\t"
        "s_waitcnt vmcnt(11) lgkmcnt(14)\n\t"
        "v_mfma_f32_32x32x16_bf16 v[32:47], v[144:147], v[168:171], v[32:47]\n\t"
        "ds_read_b64_tr_b16 v[144:145], v226 offset:45056\n\t"
        "ds_read_b64_tr_b16 v[146:147], v226 offset:46080\n\t"
        "s_waitcnt lgkmcnt(14)\n\t"
        "v_mfma_f32_32x32x16_bf16 v[48:63], v[148:151], v[168:171], v[48:63]\n\t"
        "ds_read_b64_tr_b16 v[148:149], v227 offset:45056\n\t"
        "ds_read_b64_tr_b16 v[150:151], v227 offset:46080\n\t"
        "buffer_load_dwordx4 v[216:219], v224, s[40:43], s44 offen offset:2048 sc1\n\t"
        "s_waitcnt vmcnt(11) lgkmcnt(8)\n\t"
        "v_mfma_f32_32x32x16_bf16 v[32:47], v[152:155], v[172:175], v[32:47]\n\t"
        "v_readfirstlane_b32 s51, v233\n\t"
        "ds_read_b64_tr_b16 v[152:153], v226 offset:47104\n\t"
        "ds_read_b64_tr_b16 v[154:155], v226 offset:48128\n\t"
        "s_waitcnt lgkmcnt(14)\n\t"
        "v_mfma_f32_32x32x16_bf16 v[48:63], v[156:159], v[172:175], v[48:63]\n\t"
        "s_andn2_b32 s51, s51, s46\n\t"
        "s_ff1_i32_b32 s47, s51\n\t"
        "s_add_u32 s45, s47, s49\n\t"
        "s_and_b32 s45, s45, 3\n\t"
        "s_lshl_b32 s45, s45, 14\n\t"
        "s_or_b32 s45, s45, s50\n\t"
        "ds_read_b64_tr_b16 v[156:157], v227 offset:47104\n\t"
        "ds_read_b64_tr_b16 v[158:159], v227 offset:48128\n\t"
        "buffer_load_dwordx4 v[220:223], v224, s[40:43], s44 offen offset:3072 sc1\n\t"
        "s_waitcnt vmcnt(11) lgkmcnt(14)\n\t"
        "v_mfma_f32_32x32x16_bf16 v[32:47], v[128:131], v[176:179], v[32:47]\n\t"
        "s_and_b32 s51, s47, 0x80000000\n\t"
        "v_or_b32 v225, s51, v224\n\t"
        "s_and_b32 s51, s47, 3\n\t"
        "s_lshl_b32 s51, s51, 15\n\t"
        "v_add_u32 v230, s51, v226\n\t"
        "v_add_u32 v231, s51, v227\n\t"
        "ds_read_b64_tr_b16 v[128:129], v226 offset:49152\n\t"
        "ds_read_b64_tr_b16 v[130:131], v226 offset:50176\n\t"
        "s_waitcnt lgkmcnt(14)\n\t"
        "v_mfma_f32_32x32x16_bf16 v[48:63], v[132:135], v[176:179], v[48:63]\n\t"
        "ds_read_b64_tr_b16 v[132:133], v227 offset:49152\n\t"
        "ds_read_b64_tr_b16 v[134:135], v227 offset:50176\n\t"
        "buffer_load_dwordx4 v[160:163], v225, s[40:43], s45 offen offset:0 sc1\n\t"
        "s_waitcnt vmcnt(11) lgkmcnt(14)\n\t"
        "v_mfma_f32_32x32x16_bf16 v[32:47], v[136:139], v[180:183], v[32:47]\n\t"
        "ds_read_b64_tr_b16 v[136:137], v226 offset:51200\n\t"
        "ds_read_b64_tr_b16 v[138:139], v226 offset:52224\n\t"
        "s_waitcnt lgkmcnt(14)\n\t"
        "v_mfma_f32_32x32x16_bf16 v[48:63], v[140:143], v[180:183], v[48:63]\n\t"
        "ds_read_b64_tr_b16 v[140:141], v227 offset:51200\n\t"
        "ds_read_b64_tr_b16 v[142:143], v227 offset:52224\n\t"
        "buffer_load_dwordx4 v[164:167], v225, s[40:43], s45 offen offset:1024 sc1\n\t"
        "s_waitcnt vmcnt(11) lgkmcnt(14)\n\t"
        "v_mfma_f32_32x32x16_bf16 v[32:47], v[144:147], v[184:187], v[32:47]\n\t"
        "ds_read_b64_tr_b16 v[144:145], v226 offset:53248\n\t"
        "ds_read_b64_tr_b16 v[146:147], v226 offset:54272\n\t"
        "s_waitcnt lgkmcnt(14)\n\t"
        "v_mfma_f32_32x32x16_bf16 v[48:63], v[148:151], v[184:187], v[48:63]\n\t"
        "ds_read_b64_tr_b16 v[148:149], v227 offset:53248\n\t"
        "ds_read_b64_tr_b16 v[150:151], v227 offset:54272\n\t"
        "buffer_load_dwordx4 v[168:171], v225, s[40:43], s45 offen offset:2048 sc1\n\t"
        "s_waitcnt vmcnt(11) lgkmcnt(14)\n\t"
        "v_mfma_f32_32x32x16_bf16 v[32:47], v[152:155], v[188:191], v[32:47]\n\t"
        "ds_read_b64_tr_b16 v[152:153], v226 offset:55296\n\t"
        "ds_read_b64_tr_b16 v[154:155], v226 offset:56320\n\t"
        "s_waitcnt lgkmcnt(14)\n\t"
        "v_mfma_f32_32x32x16_bf16 v[48:63], v[156:159], v[188:191], v[48:63]\n\t"
        "ds_read_b64_tr_b16 v[156:157], v227 offset:55296\n\t"
        "ds_read_b64_tr_b16 v[158:159], v227 offset:56320\n\t"
        "buffer_load_dwordx4 v[172:175], v225, s[40:43], s45 offen offset:3072 sc1\n\t"
        "s_waitcnt vmcnt(11) lgkmcnt(14)\n\t"
        "v_mfma_f32_32x32x16_bf16 v[32:47], v[128:131], v[192:195], v[32:47]\n\t"
        "ds_read_b64_tr_b16 v[128:129], v226 offset:57344\n\t"
        "ds_read_b64_tr_b16 v[130:131], v226 offset:58368\n\t"
        "s_waitcnt lgkmcnt(14)\n\t"
        "v_mfma_f32_32x32x16_bf16 v[48:63], v[132:135], v[192:195], v[48:63]\n\t"
        "ds_read_b64_tr_b16 v[132:133], v227 offset:57344\n\t"
        "ds_read_b64_tr_b16 v[134:135], v227 offset:58368\n\t"
        "s_add_u32 s51, s45, 0x1000\n\t"
        "s_and_b32 s51, s51, 0x3fff\n\t"
        "s_andn2_b32 s45, s45, 0x3fff\n\t"
        "s_or_b32 s45, s45, s51\n\t"
        "buffer_load_dwordx4 v[176:179], v225, s[40:43], s45 offen offset:0 sc1\n\t"
        "s_waitcnt vmcnt(11) lgkmcnt(14)\n\t"
        "v_mfma_f32_32x32x16_bf16 v[32:47], v[136:139], v[196:199], v[32:47]\n\t"
        "ds_read_b64_tr_b16 v[136:137], v226 offset:59392\n\t"
        "ds_read_b64_tr_b16 v[138:139], v226 offset:60416\n\t"
        "s_waitcnt lgkmcnt(14)\n\t"
        "v_mfma_f32_32x32x16_bf16 v[48:63], v[140:143], v[196:199], v[48:63]\n\t"
        "ds_read_b64_tr_b16 v[140:141], v227 offset:59392\n\t"
        "ds_read_b64_tr_b16 v[142:143], v227 offset:60416\n\t"
        "buffer_load_dwordx4 v[180:183], v225, s[40:43], s45 offen offset:1024 sc1\n\t"
        "s_waitcnt vmcnt(11) lgkmcnt(14)\n\t"
        "v_mfma_f32_32x32x16_bf16 v[32:47], v[144:147], v[200:203], v[32:47]\n\t"
        "ds_read_b64_tr_b16 v[144:145], v226 offset:61440\n\t"
        "ds_read_b64_tr_b16 v[146:147], v226 offset:62464\n\t"
        "s_waitcnt lgkmcnt(14)\n\t"
        "v_mfma_f32_32x32x16_bf16 v[48:63], v[148:151], v[200:203], v[48:63]\n\t"
        "ds_read_b64_tr_b16 v[148:149], v227 offset:61440\n\t"
        "ds_read_b64_tr_b16 v[150:151], v227 offset:62464\n\t"
        "buffer_load_dwordx4 v[184:187], v225, s[40:43], s45 offen offset:2048 sc1\n\t"
        "s_waitcnt vmcnt(11) lgkmcnt(14)\n\t"
        "v_mfma_f32_32x32x16_bf16 v[32:47], v[152:155], v[204:207], v[32:47]\n\t"
        "ds_read_b64_tr_b16 v[152:153], v226 offset:63488\n\t"
        "ds_read_b64_tr_b16 v[154:155], v226 offset:64512\n\t"
        "s_waitcnt lgkmcnt(14)\n\t"
        "v_mfma_f32_32x32x16_bf16 v[48:63], v[156:159], v[204:207], v[48:63]\n\t"
        "ds_read_b64_tr_b16 v[156:157], v227 offset:63488\n\t"
        "ds_read_b64_tr_b16 v[158:159], v227 offset:64512\n\t"
        "buffer_load_dwordx4 v[188:191], v225, s[40:43], s45 offen offset:3072 sc1\n\t"
        "s_waitcnt vmcnt(11) lgkmcnt(14)\n\t"
        "v_mfma_f32_32x32x16_bf16 v[32:47], v[128:131], v[208:211], v[32:47]\n\t"
        "ds_read_b64_tr_b16 v[128:129], v230 offset:0\n\t"
        "ds_read_b64_tr_b16 v[130:131], v230 offset:1024\n\t"
        "s_waitcnt lgkmcnt(14)\n\t"
        "v_mfma_f32_32x32x16_bf16 v[48:63], v[132:135], v[208:211], v[48:63]\n\t"
        "ds_read_b64_tr_b16 v[132:133], v231 offset:0\n\t"
        "ds_read_b64_tr_b16 v[134:135], v231 offset:1024\n\t"
        "s_add_u32 s51, s45, 0x1000\n\t"
        "s_and_b32 s51, s51, 0x3fff\n\t"
        "s_andn2_b32 s45, s45, 0x3fff\n\t"
        "s_or_b32 s45, s45, s51\n\t"
        "buffer_load_dwordx4 v[192:195], v225, s[40:43], s45 offen offset:0 sc1\n\t"
        "s_waitcnt vmcnt(11) lgkmcnt(14)\n\t"
        "v_mfma_f32_32x32x16_bf16 v[32:47], v[136:139], v[212:215], v[32:47]\n\t"
        "ds_read_b64_tr_b16 v[136:137], v230 offset:2048\n\t"
        "ds_read_b64_tr_b16 v[138:139], v230 offset:3072\n\t"
        "s_waitcnt lgkmcnt(14)\n\t"
        "v_mfma_f32_32x32x16_bf16 v[48:63], v[140:143], v[212:215], v[48:63]\n\t"
        "ds_read_b64_tr_b16 v[140:141], v231 offset:2048\n\t"
        "ds_read_b64_tr_b16 v[142:143], v231 offset:3072\n\t"
        "buffer_load_dwordx4 v[196:199], v225, s[40:43], s45 offen offset:1024 sc1\n\t"
        "s_waitcnt vmcnt(11) lgkmcnt(14)\n\t"
        "v_mfma_f32_32x32x16_bf16 v[32:47], v[144:147], v[216:219], v[32:47]\n\t"
        "ds_read_b64_tr_b16 v[144:145], v230 offset:4096\n\t"
        "ds_read_b64_tr_b16 v[146:147], v230 offset:5120\n\t"
        "s_waitcnt lgkmcnt(14)\n\t"
        "v_mfma_f32_32x32x16_bf16 v[48:63], v[148:151], v[216:219], v[48:63]\n\t"
        "ds_read_b64_tr_b16 v[148:149], v231 offset:4096\n\t"
        "ds_read_b64_tr_b16 v[150:151], v231 offset:5120\n\t"
        "buffer_load_dwordx4 v[200:203], v225, s[40:43], s45 offen offset:2048 sc1\n\t"
        "s_waitcnt vmcnt(11) lgkmcnt(14)\n\t"
        "v_mfma_f32_32x32x16_bf16 v[32:47], v[152:155], v[220:223], v[32:47]\n\t"
        "ds_read_b64_tr_b16 v[152:153], v230 offset:6144\n\t"
        "ds_read_b64_tr_b16 v[154:155], v230 offset:7168\n\t"
        "s_waitcnt lgkmcnt(14)\n\t"
        "v_mfma_f32_32x32x16_bf16 v[48:63], v[156:159], v[220:223], v[48:63]\n\t"
        "ds_read_b64_tr_b16 v[156:157], v231 offset:6144\n\t"
        "ds_read_b64_tr_b16 v[158:159], v231 offset:7168\n\t"
        "buffer_load_dwordx4 v[204:207], v225, s[40:43], s45 offen offset:3072 sc1\n\t"
        "s_mov_b32 s44, s45\n\t"
        "s_cmp_eq_u32 s47, 0\n\t"
        "s_cbranch_scc1 oseg0_%=\n\t"
        "s_cmp_eq_u32 s47, 1\n\t"
        "s_cbranch_scc1 oseg1_%=\n\t"
        "s_cmp_eq_u32 s47, 2\n\t"
        "s_cbranch_scc1 oseg2_%=\n\t"
        "s_cmp_eq_u32 s47, 3\n\t"
        "s_cbranch_scc1 oseg3_%=\n\t"
        "s_cmp_lg_u32 s46, 15\n\t"
        "s_cbranch_scc1 ostart_%=\n\t"
        "s_branch oend_%=\n\t"
        "oseg2_%=:\n\t"
        "s_or_b32 s46, s46, 4\n\t"
        "s_waitcnt vmcnt(11) lgkmcnt(14)\n\t"
        "v_mfma_f32_32x32x16_bf16 v[64:79], v[128:131], v[160:163], 0\n\t"
        "ds_read_b64_tr_b16 v[128:129], v228 offset:8192\n\t"
        "ds_read_b64_tr_b16 v[130:131], v228 offset:9216\n\t"
        "s_waitcnt lgkmcnt(14)\n\t"
        "v_mfma_f32_32x32x16_bf16 v[80:95], v[132:135], v[160:163], 0\n\t"
        "ds_read_b64_tr_b16 v[132:133], v229 offset:8192\n\t"
        "ds_read_b64_tr_b16 v[134:135], v229 offset:9216\n\t"
        "s_add_u32 s51, s44, 0x1000\n\t"
        "s_and_b32 s51, s51, 0x3fff\n\t"
        "s_andn2_b32 s44, s44, 0x3fff\n\t"
        "s_or_b32 s44, s44, s51\n\t"
        "buffer_load_dwordx4 v[208:211], v224, s[40:43], s44 offen offset:0 sc1\n\t"
        "s_waitcnt vmcnt(11) lgkmcnt(14)\n\t"
        "v_mfma_f32_32x32x16_bf16 v[64:79], v[136:139], v[164:167], v[64:79]\n\t"
        "ds_read_b32 v233, v232\n\t"
        "ds_read_b64_tr_b16 v[136:137], v228 offset:10240\n\t"
        "ds_read_b64_tr_b16 v[138:139], v228 offset:11264\n\t"
        "s_waitcnt lgkmcnt(14)\n\t"
        "v_mfma_f32_32x32x16_bf16 v[80:95], v[140:143], v[164:167], v[80:95]\n\t"
        "ds_read_b64_tr_b16 v[140:141], v229 offset:10240\n\t"
        "ds_read_b64_tr_b16 v[142:143], v229 offset:11264\n\t"
        "buffer_load_dwordx4 v[212:215], v224, s[40:43], s44 offen offset:1024 sc1\n\t"
        "s_waitcnt vmcnt(11) lgkmcnt(14)\n\t"
        "v_mfma_f32_32x32x16_bf16 v[64:79], v[144:147], v[168:171], v[64:79]\n\t"
        "ds_read_b64_tr_b16 v[144:145], v228 offset:12288\n\t"
        "ds_read_b64_tr_b16 v[146:147], v228 offset:13312\n\t"
        "s_waitcnt lgkmcnt(14)\n\t"
        "v_mfma_f32_32x32x16_bf16 v[80:95], v[148:151], v[168:171], v[80:95]\n\t"
        "ds_read_b64_tr_b16 v[148:149], v229 offset:12288\n\t"
        "ds_read_b64_tr_b16 v[150:151], v229 offset:13312\n\t"
        "buffer_load_dwordx4 v[216:219], v224, s[40:43], s44 offen offset:2048 sc1\n\t"
        "s_waitcnt vmcnt(11) lgkmcnt(8)\n\t"
        "v_mfma_f32_32x32x16_bf16 v[64:79], v[152:155], v[172:175], v[64:79]\n\t"
        "v_readfirstlane_b32 s51, v233\n\t"
        "ds_read_b64_tr_b16 v[152:153], v228 offset:14336\n\t"
        "ds_read_b64_tr_b16 v[154:155], v228 offset:15360\n\t"
        "s_waitcnt lgkmcnt(14)\n\t"
        "v_mfma_f32_32x32x16_bf16 v[80:95], v[156:159], v[172:175], v[80:95]\n\t"
        "s_andn2_b32 s51, s51, s46\n\t"
        "s_ff1_i32_b32 s47, s51\n\t"
        "s_add_u32 s45, s47, s49\n\t"
        "s_and_b32 s45, s45, 3\n\t"
        "s_lshl_b32 s45, s45, 14\n\t"
        "s_or_b32 s45, s45, s50\n\t"
        "ds_read_b64_tr_b16 v[156:157], v229 offset:14336\n\t"
        "ds_read_b64_tr_b16 v[158:159], v229 offset:15360\n\t"
        "buffer_load_dwordx4 v[220:223], v224, s[40:43], s44 offen offset:3072 sc1\n\t"
        "s_waitcnt vmcnt(11) lgkmcnt(14)\n\t"
        "v_mfma_f32_32x32x16_bf16 v[64:79], v[128:131], v[176:179], v[64:79]\n\t"
        "s_and_b32 s51, s47, 0x80000000\n\t"
        "v_or_b32 v225, s51, v224\n\t"
        "s_and_b32 s51, s47, 3\n\t"
        "s_lshl_b32 s51, s51, 15\n\t"
        "v_add_u32 v230, s51, v226\n\t"
        "v_add_u32 v231, s51, v227\n\t"
        "ds_read_b64_tr_b16 v[128:129], v228 offset:16384\n\t"
        "ds_read_b64_tr_b16 v[130:131], v228 offset:17408\n\t"
        "s_waitcnt lgkmcnt(14)\n\t"
        "v_mfma_f32_32x32x16_bf16 v[80:95], v[132:135], v[176:179], v[80:95]\n\t"
        "ds_read_b64_tr_b16 v[132:133], v229 offset:16384\n\t"
        "ds_read_b64_tr_b16 v[134:135], v229 offset:17408\n\t"
        "buffer_load_dwordx4 v[160:163], v225, s[40:43], s45 offen offset:0 sc1\n\t"
        "s_waitcnt vmcnt(11) lgkmcnt(14)\n\t"
        "v_mfma_f32_32x32x16_bf16 v[64:79], v[136:139], v[180:183], v[64:79]\n\t"
        "ds_read_b64_tr_b16 v[136:137], v228 offset:18432\n\t"
        "ds_read_b64_tr_b16 v[138:139], v228 offset:19456\n\t"
        "s_waitcnt lgkmcnt(14)\n\t"
        "v_mfma_f32_32x32x16_bf16 v[80:95], v[140:143], v[180:183], v[80:95]\n\t"
        "ds_read_b64_tr_b16 v[140:141], v229 offset:18432\n\t"
        "ds_read_b64_tr_b16 v[142:143], v229 offset:19456\n\t"
        "buffer_load_dwordx4 v[164:167], v225, s[40:43], s45 offen offset:1024 sc1\n\t"
        "s_waitcnt vmcnt(11) lgkmcnt(14)\n\t"
        "v_mfma_f32_32x32x16_bf16 v[64:79], v[144:147], v[184:187], v[64:79]\n\t"
        "ds_read_b64_tr_b16 v[144:145], v228 offset:20480\n\t"
        "ds_read_b64_tr_b16 v[146:147], v228 offset:21504\n\t"
        "s_waitcnt lgkmcnt(14)\n\t"
        "v_mfma_f32_32x32x16_bf16 v[80:95], v[148:151], v[184:187], v[80:95]\n\t"
        "ds_read_b64_tr_b16 v[148:149], v229 offset:20480\n\t"
        "ds_read_b64_tr_b16 v[150:151], v229 offset:21504\n\t"
        "buffer_load_dwordx4 v[168:171], v225, s[40:43], s45 offen offset:2048 sc1\n\t"
        "s_waitcnt vmcnt(11) lgkmcnt(14)\n\t"
        "v_mfma_f32_32x32x16_bf16 v[64:79], v[152:155], v[188:191], v[64:79]\n\t"
        "ds_read_b64_tr_b16 v[152:153], v228 offset:22528\n\t"
        "ds_read_b64_tr_b16 v[154:155], v228 offset:23552\n\t"
        "s_waitcnt lgkmcnt(14)\n\t"
        "v_mfma_f32_32x32x16_bf16 v[80:95], v[156:159], v[188:191], v[80:95]\n\t"
        "ds_read_b64_tr_b16 v[156:157], v229 offset:22528\n\t"
        "ds_read_b64_tr_b16 v[158:159], v229 offset:23552\n\t"
        "buffer_load_dwordx4 v[172:175], v225, s[40:43], s45 offen offset:3072 sc1\n\t"
        "s_waitcnt vmcnt(11) lgkmcnt(14)\n\t"
        "v_mfma_f32_32x32x16_bf16 v[64:79], v[128:131], v[192:195], v[64:79]\n\t"
        "ds_read_b64_tr_b16 v[128:129], v228 offset:24576\n\t"
        "ds_read_b64_tr_b16 v[130:131], v228 offset:25600\n\t"
        "s_waitcnt lgkmcnt(14)\n\t"
        "v_mfma_f32_32x32x16_bf16 v[80:95], v[132:135], v[192:195], v[80:95]\n\t"
        "ds_read_b64_tr_b16 v[132:133], v229 offset:24576\n\t"
        "ds_read_b64_tr_b16 v[134:135], v229 offset:25600\n\t"
        "s_add_u32 s51, s45, 0x1000\n\t"
        "s_and_b32 s51, s51, 0x3fff\n\t"
        "s_andn2_b32 s45, s45, 0x3fff\n\t"
        "s_or_b32 s45, s45, s51\n\t"
        "buffer_load_dwordx4 v[176:179], v225, s[40:43], s45 offen offset:0 sc1\n\t"
        "s_waitcnt vmcnt(11) lgkmcnt(14)\n\t"
        "v_mfma_f32_32x32x16_bf16 v[64:79], v[136:139], v[196:199], v[64:79]\n\t"
        "ds_read_b64_tr_b16 v[136:137], v228 offset:26624\n\t"
        "ds_read_b64_tr_b16 v[138:139], v228 offset:27648\n\t"
        "s_waitcnt lgkmcnt(14)\n\t"
        "v_mfma_f32_32x32x16_bf16 v[80:95], v[140:143], v[196:199], v[80:95]\n\t"
        "ds_read_b64_tr_b16 v[140:141], v229 offset:26624\n\t"
        "ds_read_b64_tr_b16 v[142:143], v229 offset:27648\n\t"
        "buffer_load_dwordx4 v[180:183], v225, s[40:43], s45 offen offset:1024 sc1\n\t"
        "s_waitcnt vmcnt(11) lgkmcnt(14)\n\t"
        "v_mfma_f32_32x32x16_bf16 v[64:79], v[144:147], v[200:203], v[64:79]\n\t"
        "ds_read_b64_tr_b16 v[144:145], v228 offset:28672\n\t"
        "ds_read_b64_tr_b16 v[146:147], v228 offset:29696\n\t"
        "s_waitcnt lgkmcnt(14)\n\t"
        "v_mfma_f32_32x32x16_bf16 v[80:95], v[148:151], v[200:203], v[80:95]\n\t"
        "ds_read_b64_tr_b16 v[148:149], v229 offset:28672\n\t"
        "ds_read_b64_tr_b16 v[150:151], v229 offset:29696\n\t"
        "buffer_load_dwordx4 v[184:187], v225, s[40:43], s45 offen offset:2048 sc1\n\t"
        "s_waitcnt vmcnt(11) lgkmcnt(14)\n\t"
        "v_mfma_f32_32x32x16_bf16 v[64:79], v[152:155], v[204:207], v[64:79]\n\t"
        "ds_read_b64_tr_b16 v[152:153], v228 offset:30720\n\t"
        "ds_read_b64_tr_b16 v[154:155], v228 offset:31744\n\t"
        "s_waitcnt lgkmcnt(14)\n\t"
        "v_mfma_f32_32x32x16_bf16 v[80:95], v[156:159], v[204:207], v[80:95]\n\t"
        "ds_read_b64_tr_b16 v[156:157], v229 offset:30720\n\t"
        "ds_read_b64_tr_b16 v[158:159], v229 offset:31744\n\t"
        "buffer_load_dwordx4 v[188:191], v225, s[40:43], s45 offen offset:3072 sc1\n\t"
        "s_waitcnt vmcnt(11) lgkmcnt(14)\n\t"
        "v_mfma_f32_32x32x16_bf16 v[64:79], v[128:131], v[208:211], v[64:79]\n\t"
        "ds_read_b64_tr_b16 v[128:129], v230 offset:0\n\t"
        "ds_read_b64_tr_b16 v[130:131], v230 offset:1024\n\t"
        "s_waitcnt lgkmcnt(14)\n\t"
        "v_mfma_f32_32x32x16_bf16 v[80:95], v[132:135], v[208:211], v[80:95]\n\t"
        "ds_read_b64_tr_b16 v[132:133], v231 offset:0\n\t"
        "ds_read_b64_tr_b16 v[134:135], v231 offset:1024\n\t"
        "s_add_u32 s51, s45, 0x1000\n\t"
        "s_and_b32 s51, s51, 0x3fff\n\t"
        "s_andn2_b32 s45, s45, 0x3fff\n\t"
        "s_or_b32 s45, s45, s51\n\t"
        "buffer_load_dwordx4 v[192:195], v225, s[40:43], s45 offen offset:0 sc1\n\t"
        "s_waitcnt vmcnt(11) lgkmcnt(14)\n\t"
        "v_mfma_f32_32x32x16_bf16 v[64:79], v[136:139], v[212:215], v[64:79]\n\t"
        "ds_read_b64_tr_b16 v[136:137], v230 offset:2048\n\t"
        "ds_read_b64_tr_b16 v[138:139], v230 offset:3072\n\t"
        "s_waitcnt lgkmcnt(14)\n\t"
        "v_mfma_f32_32x32x16_bf16 v[80:95], v[140:143], v[212:215], v[80:95]\n\t"
        "ds_read_b64_tr_b16 v[140:141], v231 offset:2048\n\t"
        "ds_read_b64_tr_b16 v[142:143], v231 offset:3072\n\t"
        "buffer_load_dwordx4 v[196:199], v225, s[40:43], s45 offen offset:1024 sc1\n\t"
        "s_waitcnt vmcnt(11) lgkmcnt(14)\n\t"
        "v_mfma_f32_32x32x16_bf16 v[64:79], v[144:147], v[216:219], v[64:79]\n\t"
        "ds_read_b64_tr_b16 v[144:145], v230 offset:4096\n\t"
        "ds_read_b64_tr_b16 v[146:147], v230 offset:5120\n\t"
        "s_waitcnt lgkmcnt(14)\n\t"
        "v_mfma_f32_32x32x16_bf16 v[80:95], v[148:151], v[216:219], v[80:95]\n\t"
        "ds_read_b64_tr_b16 v[148:149], v231 offset:4096\n\t"
        "ds_read_b64_tr_b16 v[150:151], v231 offset:5120\n\t"
        "buffer_load_dwordx4 v[200:203], v225, s[40:43], s45 offen offset:2048 sc1\n\t"
        "s_waitcnt vmcnt(11) lgkmcnt(14)\n\t"
        "v_mfma_f32_32x32x16_bf16 v[64:79], v[152:155], v[220:223], v[64:79]\n\t"
        "ds_read_b64_tr_b16 v[152:153], v230 offset:6144\n\t"
        "ds_read_b64_tr_b16 v[154:155], v230 offset:7168\n\t"
        "s_waitcnt lgkmcnt(14)\n\t"
        "v_mfma_f32_32x32x16_bf16 v[80:95], v[156:159], v[220:223], v[80:95]\n\t"
        "ds_read_b64_tr_b16 v[156:157], v231 offset:6144\n\t"
        "ds_read_b64_tr_b16 v[158:159], v231 offset:7168\n\t"
        "buffer_load_dwordx4 v[204:207], v225, s[40:43], s45 offen offset:3072 sc1\n\t"
        "s_mov_b32 s44, s45\n\t"
        "s_cmp_eq_u32 s47, 0\n\t"
        "s_cbranch_scc1 oseg0_%=\n\t"
        "s_cmp_eq_u32 s47, 1\n\t"
        "s_cbranch_scc1 oseg1_%=\n\t"
        "s_cmp_eq_u32 s47, 2\n\t"
        "s_cbranch_scc1 oseg2_%=\n\t"
        "s_cmp_eq_u32 s47, 3\n\t"
        "s_cbranch_scc1 oseg3_%=\n\t"
        "s_cmp_lg_u32 s46, 15\n\t"
        "s_cbranch_scc1 ostart_%=\n\t"
        "s_branch oend_%=\n\t"
        "oseg3_%=:\n\t"
        "s_or_b32 s46, s46, 8\n\t"
        "s_waitcnt vmcnt(11) lgkmcnt(14)\n\t"
        "v_mfma_f32_32x32x16_bf16 v[96:111], v[128:131], v[160:163], 0\n\t"
        "ds_read_b64_tr_b16 v[128:129], v228 offset:40960\n\t"
        "ds_read_b64_tr_b16 v[130:131], v228 offset:41984\n\t"
        "s_waitcnt lgkmcnt(14)\n\t"
        "v_mfma_f32_32x32x16_bf16 v[112:127], v[132:135], v[160:163], 0\n\t"
        "ds_read_b64_tr_b16 v[132:133], v229 offset:40960\n\t"
        "ds_read_b64_tr_b16 v[134:135], v229 offset:41984\n\t"
        "s_add_u32 s51, s44, 0x1000\n\t"
        "s_and_b32 s51, s51, 0x3fff\n\t"
        "s_andn2_b32 s44, s44, 0x3fff\n\t"
        "s_or_b32 s44, s44, s51\n\t"
        "buffer_load_dwordx4 v[208:211], v224, s[40:43], s44 offen offset:0 sc1\n\t"
        "s_waitcnt vmcnt(11) lgkmcnt(14)\n\t"
        "v_mfma_f32_32x32x16_bf16 v[96:111], v[136:139], v[164:167], v[96:111]\n\t"
        "ds_read_b32 v233, v232\n\t"
        "ds_read_b64_tr_b16 v[136:137], v228 offset:43008\n\t"
        "ds_read_b64_tr_b16 v[138:139], v228 offset:44032\n\t"
        "s_waitcnt lgkmcnt(14)\n\t"
        "v_mfma_f32_32x32x16_bf16 v[112:127], v[140:143], v[164:167], v[112:127]\n\t"
        "ds_read_b64_tr_b16 v[140:141], v229 offset:43008\n\t"
        "ds_read_b64_tr_b16 v[142:143], v229 offset:44032\n\t"
        "buffer_load_dwordx4 v[212:215], v224, s[40:43], s44 offen offset:1024 sc1\n\t"
        "s_waitcnt vmcnt(11) lgkmcnt(14)\n\t"
        "v_mfma_f32_32x32x16_bf16 v[96:111], v[144:147], v[168:171], v[96:111]\n\t"
        "ds_read_b64_tr_b16 v[144:145], v228 offset:45056\n\t"
        "ds_read_b64_tr_b16 v[146:147], v228 offset:46080\n\t"
        "s_waitcnt lgkmcnt(14)\n\t"
        "v_mfma_f32_32x32x16_bf16 v[112:127], v[148:151], v[168:171], v[112:127]\n\t"
        "ds_read_b64_tr_b16 v[148:149], v229 offset:45056\n\t"
        "ds_read_b64_tr_b16 v[150:151], v229 offset:46080\n\t"
        "buffer_load_dwordx4 v[216:219], v224, s[40:43], s44 offen offset:2048 sc1\n\t"
        "s_waitcnt vmcnt(11) lgkmcnt(8)\n\t"
        "v_mfma_f32_32x32x16_bf16 v[96:111], v[152:155], v[172:175], v[96:111]\n\t"
        "v_readfirstlane_b32 s51, v233\n\t"
        "ds_read_b64_tr_b16 v[152:153], v228 offset:47104\n\t"
        "ds_read_b64_tr_b16 v[154:155], v228 offset:48128\n\t"
        "s_waitcnt lgkmcnt(14)\n\t"
        "v_mfma_f32_32x32x16_bf16 v[112:127], v[156:159], v[172:175], v[112:127]\n\t"
        "s_andn2_b32 s51, s51, s46\n\t"
        "s_ff1_i32_b32 s47, s51\n\t"
        "s_add_u32 s45, s47, s49\n\t"
        "s_and_b32 s45, s45, 3\n\t"
        "s_lshl_b32 s45, s45, 14\n\t"
        "s_or_b32 s45, s45, s50\n\t"
        "ds_read_b64_tr_b16 v[156:157], v229 offset:47104\n\t"
        "ds_read_b64_tr_b16 v[158:159], v229 offset:48128\n\t"
        "buffer_load_dwordx4 v[220:223], v224, s[40:43], s44 offen offset:3072 sc1\n\t"
        "s_waitcnt vmcnt(11) lgkmcnt(14)\n\t"
        "v_mfma_f32_32x32x16_bf16 v[96:111], v[128:131], v[176:179], v[96:111]\n\t"
        "s_and_b32 s51, s47, 0x80000000\n\t"
        "v_or_b32 v225, s51, v224\n\t"
        "s_and_b32 s51, s47, 3\n\t"
        "s_lshl_b32 s51, s51, 15\n\t"
        "v_add_u32 v230, s51, v226\n\t"
        "v_add_u32 v231, s51, v227\n\t"
        "ds_read_b64_tr_b16 v[128:129], v228 offset:49152\n\t"
        "ds_read_b64_tr_b16 v[130:131], v228 offset:50176\n\t"
        "s_waitcnt lgkmcnt(14)\n\t"
        "v_mfma_f32_32x32x16_bf16 v[112:127], v[132:135], v[176:179], v[112:127]\n\t"
        "ds_read_b64_tr_b16 v[132:133], v229 offset:49152\n\t"
        "ds_read_b64_tr_b16 v[134:135], v229 offset:50176\n\t"
        "buffer_load_dwordx4 v[160:163], v225, s[40:43], s45 offen offset:0 sc1\n\t"
        "s_waitcnt vmcnt(11) lgkmcnt(14)\n\t"
        "v_mfma_f32_32x32x16_bf16 v[96:111], v[136:139], v[180:183], v[96:111]\n\t"
        "ds_read_b64_tr_b16 v[136:137], v228 offset:51200\n\t"
        "ds_read_b64_tr_b16 v[138:139], v228 offset:52224\n\t"
        "s_waitcnt lgkmcnt(14)\n\t"
        "v_mfma_f32_32x32x16_bf16 v[112:127], v[140:143], v[180:183], v[112:127]\n\t"
        "ds_read_b64_tr_b16 v[140:141], v229 offset:51200\n\t"
        "ds_read_b64_tr_b16 v[142:143], v229 offset:52224\n\t"
        "buffer_load_dwordx4 v[164:167], v225, s[40:43], s45 offen offset:1024 sc1\n\t"
        "s_waitcnt vmcnt(11) lgkmcnt(14)\n\t"
        "v_mfma_f32_32x32x16_bf16 v[96:111], v[144:147], v[184:187], v[96:111]\n\t"
        "ds_read_b64_tr_b16 v[144:145], v228 offset:53248\n\t"
        "ds_read_b64_tr_b16 v[146:147], v228 offset:54272\n\t"
        "s_waitcnt lgkmcnt(14)\n\t"
        "v_mfma_f32_32x32x16_bf16 v[112:127], v[148:151], v[184:187], v[112:127]\n\t"
        "ds_read_b64_tr_b16 v[148:149], v229 offset:53248\n\t"
        "ds_read_b64_tr_b16 v[150:151], v229 offset:54272\n\t"
        "buffer_load_dwordx4 v[168:171], v225, s[40:43], s45 offen offset:2048 sc1\n\t"
        "s_waitcnt vmcnt(11) lgkmcnt(14)\n\t"
        "v_mfma_f32_32x32x16_bf16 v[96:111], v[152:155], v[188:191], v[96:111]\n\t"
        "ds_read_b64_tr_b16 v[152:153], v228 offset:55296\n\t"
        "ds_read_b64_tr_b16 v[154:155], v228 offset:56320\n\t"
        "s_waitcnt lgkmcnt(14)\n\t"
        "v_mfma_f32_32x32x16_bf16 v[112:127], v[156:159], v[188:191], v[112:127]\n\t"
        "ds_read_b64_tr_b16 v[156:157], v229 offset:55296\n\t"
        "ds_read_b64_tr_b16 v[158:159], v229 offset:56320\n\t"
        "buffer_load_dwordx4 v[172:175], v225, s[40:43], s45 offen offset:3072 sc1\n\t"
        "s_waitcnt vmcnt(11) lgkmcnt(14)\n\t"
        "v_mfma_f32_32x32x16_bf16 v[96:111], v[128:131], v[192:195], v[96:111]\n\t"
        "ds_read_b64_tr_b16 v[128:129], v228 offset:57344\n\t"
        "ds_read_b64_tr_b16 v[130:131], v228 offset:58368\n\t"
        "s_waitcnt lgkmcnt(14)\n\t"
        "v_mfma_f32_32x32x16_bf16 v[112:127], v[132:135], v[192:195], v[112:127]\n\t"
        "ds_read_b64_tr_b16 v[132:133], v229 offset:57344\n\t"
        "ds_read_b64_tr_b16 v[134:135], v229 offset:58368\n\t"
        "s_add_u32 s51, s45, 0x1000\n\t"
        "s_and_b32 s51, s51, 0x3fff\n\t"
        "s_andn2_b32 s45, s45, 0x3fff\n\t"
        "s_or_b32 s45, s45, s51\n\t"
        "buffer_load_dwordx4 v[176:179], v225, s[40:43], s45 offen offset:0 sc1\n\t"
        "s_waitcnt vmcnt(11) lgkmcnt(14)\n\t"
        "v_mfma_f32_32x32x16_bf16 v[96:111], v[136:139], v[196:199], v[96:111]\n\t"
        "ds_read_b64_tr_b16 v[136:137], v228 offset:59392\n\t"
        "ds_read_b64_tr_b16 v[138:139], v228 offset:60416\n\t"
        "s_waitcnt lgkmcnt(14)\n\t"
        "v_mfma_f32_32x32x16_bf16 v[112:127], v[140:143], v[196:199], v[112:127]\n\t"
        "ds_read_b64_tr_b16 v[140:141], v229 offset:59392\n\t"
        "ds_read_b64_tr_b16 v[142:143], v229 offset:60416\n\t"
        "buffer_load_dwordx4 v[180:183], v225, s[40:43], s45 offen offset:1024 sc1\n\t"
        "s_waitcnt vmcnt(11) lgkmcnt(14)\n\t"
        "v_mfma_f32_32x32x16_bf16 v[96:111], v[144:147], v[200:203], v[96:111]\n\t"
        "ds_read_b64_tr_b16 v[144:145], v228 offset:61440\n\t"
        "ds_read_b64_tr_b16 v[146:147], v228 offset:62464\n\t"
        "s_waitcnt lgkmcnt(14)\n\t"
        "v_mfma_f32_32x32x16_bf16 v[112:127], v[148:151], v[200:203], v[112:127]\n\t"
        "ds_read_b64_tr_b16 v[148:149], v229 offset:61440\n\t"
        "ds_read_b64_tr_b16 v[150:151], v229 offset:62464\n\t"
        "buffer_load_dwordx4 v[184:187], v225, s[40:43], s45 offen offset:2048 sc1\n\t"
        "s_waitcnt vmcnt(11) lgkmcnt(14)\n\t"
        "v_mfma_f32_32x32x16_bf16 v[96:111], v[152:155], v[204:207], v[96:111]\n\t"
        "ds_read_b64_tr_b16 v[152:153], v228 offset:63488\n\t"
        "ds_read_b64_tr_b16 v[154:155], v228 offset:64512\n\t"
        "s_waitcnt lgkmcnt(14)\n\t"
        "v_mfma_f32_32x32x16_bf16 v[112:127], v[156:159], v[204:207], v[112:127]\n\t"
        "ds_read_b64_tr_b16 v[156:157], v229 offset:63488\n\t"
        "ds_read_b64_tr_b16 v[158:159], v229 offset:64512\n\t"
        "buffer_load_dwordx4 v[188:191], v225, s[40:43], s45 offen offset:3072 sc1\n\t"
        "s_waitcnt vmcnt(11) lgkmcnt(14)\n\t"
        "v_mfma_f32_32x32x16_bf16 v[96:111], v[128:131], v[208:211], v[96:111]\n\t"
        "ds_read_b64_tr_b16 v[128:129], v230 offset:0\n\t"
        "ds_read_b64_tr_b16 v[130:131], v230 offset:1024\n\t"
        "s_waitcnt lgkmcnt(14)\n\t"
        "v_mfma_f32_32x32x16_bf16 v[112:127], v[132:135], v[208:211], v[112:127]\n\t"
        "ds_read_b64_tr_b16 v[132:133], v231 offset:0\n\t"
        "ds_read_b64_tr_b16 v[134:135], v231 offset:1024\n\t"
        "s_add_u32 s51, s45, 0x1000\n\t"
        "s_and_b32 s51, s51, 0x3fff\n\t"
        "s_andn2_b32 s45, s45, 0x3fff\n\t"
        "s_or_b32 s45, s45, s51\n\t"
        "buffer_load_dwordx4 v[192:195], v225, s[40:43], s45 offen offset:0 sc1\n\t"
        "s_waitcnt vmcnt(11) lgkmcnt(14)\n\t"
        "v_mfma_f32_32x32x16_bf16 v[96:111], v[136:139], v[212:215], v[96:111]\n\t"
        "ds_read_b64_tr_b16 v[136:137], v230 offset:2048\n\t"
        "ds_read_b64_tr_b16 v[138:139], v230 offset:3072\n\t"
        "s_waitcnt lgkmcnt(14)\n\t"
        "v_mfma_f32_32x32x16_bf16 v[112:127], v[140:143], v[212:215], v[112:127]\n\t"
        "ds_read_b64_tr_b16 v[140:141], v231 offset:2048\n\t"
        "ds_read_b64_tr_b16 v[142:143], v231 offset:3072\n\t"
        "buffer_load_dwordx4 v[196:199], v225, s[40:43], s45 offen offset:1024 sc1\n\t"
        "s_waitcnt vmcnt(11) lgkmcnt(14)\n\t"
        "v_mfma_f32_32x32x16_bf16 v[96:111], v[144:147], v[216:219], v[96:111]\n\t"
        "ds_read_b64_tr_b16 v[144:145], v230 offset:4096\n\t"
        "ds_read_b64_tr_b16 v[146:147], v230 offset:5120\n\t"
        "s_waitcnt lgkmcnt(14)\n\t"
        "v_mfma_f32_32x32x16_bf16 v[112:127], v[148:151], v[216:219], v[112:127]\n\t"
        "ds_read_b64_tr_b16 v[148:149], v231 offset:4096\n\t"
        "ds_read_b64_tr_b16 v[150:151], v231 offset:5120\n\t"
        "buffer_load_dwordx4 v[200:203], v225, s[40:43], s45 offen offset:2048 sc1\n\t"
        "s_waitcnt vmcnt(11) lgkmcnt(14)\n\t"
        "v_mfma_f32_32x32x16_bf16 v[96:111], v[152:155], v[220:223], v[96:111]\n\t"
        "ds_read_b64_tr_b16 v[152:153], v230 offset:6144\n\t"
        "ds_read_b64_tr_b16 v[154:155], v230 offset:7168\n\t"
        "s_waitcnt lgkmcnt(14)\n\t"
        "v_mfma_f32_32x32x16_bf16 v[112:127], v[156:159], v[220:223], v[112:127]\n\t"
        "ds_read_b64_tr_b16 v[156:157], v231 offset:6144\n\t"
        "ds_read_b64_tr_b16 v[158:159], v231 offset:7168\n\t"
        "buffer_load_dwordx4 v[204:207], v225, s[40:43], s45 offen offset:3072 sc1\n\t"
        "s_mov_b32 s44, s45\n\t"
        "s_cmp_eq_u32 s47, 0\n\t"
        "s_cbranch_scc1 oseg0_%=\n\t"
        "s_cmp_eq_u32 s47, 1\n\t"
        "s_cbranch_scc1 oseg1_%=\n\t"
        "s_cmp_eq_u32 s47, 2\n\t"
        "s_cbranch_scc1 oseg2_%=\n\t"
        "s_cmp_eq_u32 s47, 3\n\t"
        "s_cbranch_scc1 oseg3_%=\n\t"
        "s_cmp_lg_u32 s46, 15\n\t"
        "s_cbranch_scc1 ostart_%=\n\t"
        "s_branch oend_%=\n\t"
        "oend_%=:\n\t"
        "s_waitcnt vmcnt(0) lgkmcnt(0)\n\t"
        "s_barrier\n\t"
        "s_nop 3\n\t"
        : "+{v[0:15]}"(acc[0]), "+{v[16:31]}"(acc[1]), "={v[32:47]}"(acc[2]), "={v[48:63]}"(acc[3]), "={v[64:79]}"(acc[4]), "={v[80:95]}"(acc[5]),
          "={v[96:111]}"(acc[6]), "={v[112:127]}"(acc[7]) STAMP_OUTS
        : "{v224}"(OFF_OF + L * (5 * MBY) + tile * TS + lane * 16), "{v226}"(a0), "{v227}"(a1), "{v228}"(a0 + 65536), "{v229}"(a1 + 65536),
          "{v232}"(lbase + L_W + 20 + 8 * L), "{s49}"((tn >> 2) + 1), "{s50}"(0 * (tn & 3) * 4096), "{s[40:43]}"(srd)
        : "v128", "v129", "v130", "v131", "v132", "v133", "v134", "v135", "v136", "v137", "v138", "v139", "v140", "v141", "v142", "v143", "v144", "v145", "v146", "v147", "v148", "v149", "v150", "v151", "v152", "v153", "v154", "v155", "v156", "v157", "v158", "v159", "v160", "v161", "v162", "v163", "v164", "v165", "v166", "v167", "v168", "v169", "v170", "v171", "v172", "v173", "v174", "v175", "v176", "v177", "v178", "v179", "v180", "v181", "v182", "v183", "v184", "v185", "v186", "v187", "v188", "v189", "v190", "v191", "v192", "v193", "v194", "v195", "v196", "v197", "v198", "v199", "v200", "v201", "v202", "v203", "v204", "v205", "v206", "v207", "v208", "v209", "v210", "v211", "v212", "v213", "v214", "v215", "v216", "v217", "v218", "v219", "v220", "v221", "v222", "v223", "v225", "v230", "v231", "v233", "s44", "s45", "s46", "s47", "s48", "s51", "vcc", "scc", "memory");
      }
      PHASE_VARS;
      STAMP_PUT(12 + 16 * L);
      STAMPV(15 + 16 * L);
#pragma unroll
      for (int g = 0; g < 8; ++g) wrc[g] = *reinterpret_cast<const u32x2*>(smem + L_WR + g * 16 + hi * 8);
      SBAR();
      float s = 0.f, sq = 0.f, d = 0.f;
      const unsigned xdst = (L ? OOB : OFF_X1 + tile * TS + tn * 4096) + lane * 16;
#pragma unroll
      for (int i = 0; i < 4; ++i) {
#pragma unroll
        for (int e = 0; e < 4; ++e) {
          const int j = i >> 1, k = 8 * (i & 1) + 2 * e;
          x1p[i][e] = pack2((acc[j][k] + acc[2 + j][k]) + (acc[4 + j][k] + acc[6 + j][k]), (acc[j][k + 1] + acc[2 + j][k + 1]) + (acc[4 + j][k + 1] + acc[6 + j][k + 1]));
        }
        stg(rs, xdst + i * 1024, x1p[i]);
        SBAR();
      }
#pragma unroll
      for (int g = 0; g < 8; ++g)
#pragma unroll
        for (int e = 0; e < 2; ++e) {
          const unsigned o = x1p[g >> 1][2 * (g & 1) + e];
          s = dot2(o, 0x3f803f80u, s); sq = dot2(o, o, sq); d = dot2(o, wrc[g][e], d);
        }
      const u32x4 pst = {nzf(half_swap_sum(s)), nzf(half_swap_sum(sq)), nzf(half_swap_sum(d)), 0u};
      asm volatile("buffer_store_dwordx4 %0, %1, %2, 0 offen sc1\n\ts_waitcnt vmcnt(1)\n\tds_write_b32 %3, %4"
                   :: "v"(pst), "v"(stats_dst(sx, tile, tn, lane)), "s"(srd), "v"(lbase + L_W + 64 + 4 * wid), "v"(3) : "memory");
    }
    STAMPV(16 + 16 * L);
  }
  {
    PHASE_VARS;
    float S, Q, Dd, mean, var, mb1, rd1;
    __builtin_amdgcn_s_sleep(13);
    row_stats_sum(rs, OFF_SX + SX_LAYER, tile, lane, S, Q, Dd);
    STAMPV(33);
    row_stats_fin(rs, OFF_SX + SX_LAYER + SX_CAND + (tn * 16 + tm) * 128 + wid * 4, smem, 2, tile, lane, S, Q, Dd, U2F(words[0]), x1p, mean, var, mb1, rd1);
    STAMPV(34);
    u32x4 xo[4];
#pragma unroll
    for (int i = 0; i < 4; ++i) {
#pragma unroll
      for (int e = 0; e < 4; ++e) {
        const unsigned h2 = ln_pair(x1p[i][e], mb1, rd1);
        xo[i][e] = pack2(lo16(x1p[i][e]) + lo16(h2), hi16(x1p[i][e]) + hi16(h2));
      }
      *reinterpret_cast<u32x4*>(smem + wid * 4096 + r32 * 128 + ((2 * i + hi) ^ (r32 & 7)) * 16) = fo_swap(xo[i]);
    }
    asm volatile("s_waitcnt lgkmcnt(0)" ::: "memory");
#pragma unroll
    for (int i = 0; i < 4; ++i) { xo[i] = *reinterpret_cast<const u32x4*>(smem + wid * 4096 + i * 1024 + (lane >> 3) * 128 + ((lane ^ (lane >> 3)) & 7) * 16); PIN(xo[i]); }
    u16* dst = out + (size_t)(tile * 32 + (lane >> 3)) * D_ + tn * 64 + (lane & 7) * 8;
    lds_wait_ge(lbase + L_W + 4 + 4 * (lane & 1), 2);
    const int rel = (int)words[3] - tile * 32 - (lane >> 3);
#pragma unroll
    for (int i = 0; i < 4; ++i)
      if (rel != 8 * i) st16(dst + (size_t)i * 8 * D_, xo[i]);
    STAMPV(35);
    STAMP_END;
  }
}

extern "C" __global__ void end_marker() {}

extern "C" void kernel_launch(void* const* d_in, const int* in_sizes, int n_in, void* d_out, int out_size,
                              void* d_ws, size_t ws_size, hipStream_t stream) {
  char* ws = (char*)d_ws;
  mega_kernel<<<256, 512, 0, stream>>>((const u16*)d_in[0], (const u16*)d_in[1], (const u16*)d_in[2], (const u16*)d_in[4], (const u16*)d_in[5],
                                       (const u16*)d_in[6], (const u16*)d_in[7], ws, (u16*)d_out);
}
```

```cpp
#include <hip/hip_runtime.h>
#include <hip/hip_bf16.h>
#include <stdint.h>

typedef unsigned short u16;
typedef short s16x4 __attribute__((ext_vector_type(4)));
typedef short s16x8 __attribute__((ext_vector_type(8)));
typedef float f32x16 __attribute__((ext_vector_type(16)));
typedef float f32x8 __attribute__((ext_vector_type(8)));
typedef float f32x4 __attribute__((ext_vector_type(4)));
typedef float f32x2 __attribute__((ext_vector_type(2)));
typedef unsigned int u32x16 __attribute__((ext_vector_type(16)));
typedef unsigned int u32x8 __attribute__((ext_vector_type(8)));
typedef unsigned int u32x4 __attribute__((ext_vector_type(4)));
typedef unsigned int u32x2 __attribute__((ext_vector_type(2)));
typedef __bf16 bf16x2 __attribute__((ext_vector_type(2)));
typedef __bf16 bf16x8 __attribute__((ext_vector_type(8)));
typedef __bf16 bf16x16 __attribute__((ext_vector_type(16)));

#define DI __device__ __forceinline__
#define LDS_PTR(T, p) ((__attribute__((address_space(3))) T*)(p))
#define BARRIER() asm volatile("s_barrier" ::: "memory")
#define BAR_LGKM() asm volatile("s_waitcnt lgkmcnt(0)\n\ts_barrier" ::: "memory")
#define BAR_ALL() asm volatile("s_waitcnt vmcnt(0) lgkmcnt(0)\n\ts_barrier" ::: "memory")
#define DRAIN() asm volatile("s_waitcnt vmcnt(0)" ::: "memory")
#define PACE() ({ asm volatile("s_sleep 1\n\ts_waitcnt vmcnt(3)" ::: "memory"); })
#define MFMA32(a, b, c) __builtin_amdgcn_mfma_f32_32x32x16_bf16(a, b, c, 0, 0, 0)
#define MFMA16(a, b, c) __builtin_amdgcn_mfma_f32_16x16x32_bf16(a, b, c, 0, 0, 0)
#define PIN(x) asm volatile("" : "+v"(x))
#define EX2(x) __builtin_amdgcn_exp2f(x)
#define RCP(x) __builtin_amdgcn_rcpf(x)
#define SBAR() __builtin_amdgcn_sched_barrier(0)

#define STAMP_INIT
#define STAMPV(i)
#define STAMP_END
#define STAMP_DECL
#define STAMP_OUTS
#define STAMP_PUT(i)
constexpr int D_ = 1024, F_ = 3584;

constexpr unsigned MBY = 1u << 20;
constexpr unsigned TS = 65536 + 4096, HS = 262144 + 4096;
constexpr unsigned OFF_HF = 0, OFF_X1 = 5 * MBY;
constexpr unsigned OFF_OF = 10 * MBY;
constexpr unsigned OFF_KV = 20 * MBY;
constexpr unsigned OFF_FL = 40 * MBY;
constexpr unsigned FL_EPOCH = OFF_FL;
constexpr unsigned FL_KV = OFF_FL + 4096;
constexpr unsigned FL_O = FL_KV + 65536;
constexpr unsigned FL_X = FL_O + 65536;
constexpr unsigned OFF_CS = 41 * MBY;
constexpr unsigned OFF_SX = 42 * MBY;
constexpr unsigned SX_ST = 0;
constexpr unsigned SX_ROW = 512 * 1024;
constexpr unsigned SX_CAND = SX_ROW + 256 * 1024;
constexpr unsigned SX_GP = SX_CAND + 32 * 1024;
constexpr unsigned SX_YP = SX_GP + 224 * 1024;
constexpr unsigned SX_LAYER = SX_YP + 224 * 1024;
constexpr unsigned SX_QP = 2 * SX_LAYER;
constexpr unsigned SX_BYTES = SX_QP + 512 * 1024;
static_assert(SX_BYTES <= 6 * 256 * 128 * 16, "sentinel fill");
constexpr unsigned WS_BYTES = 48 * MBY, OOB = 0x80000000u;
constexpr unsigned SENT = 0xFFFFFFFFu;

constexpr int NS = 9, DD = 3, SLOTB = 12288;
constexpr int L_XCH = 122880;
constexpr int L_SL = L_XCH + 8192;
constexpr int L_CAND = L_XCH + 32768;
constexpr int L_AB = L_XCH + 34816;
constexpr int L_FULL = 159744, L_FREE = L_FULL + 512;
constexpr int L_CS = L_FREE + 1024, L_PATCH = L_CS + 768;
constexpr int L_GU = L_PATCH + 768, L_TAG = L_GU + 256, L_WR = L_TAG + 256;
constexpr int L_X2F = L_WR + 128, L_X2P = L_X2F + 128;
constexpr int L_W = L_X2P + 128;
constexpr int LDS_BYTES = 163840;
static_assert(NS * SLOTB <= 54 * 2048 && L_W + 128 <= LDS_BYTES && (NS + 1) * SLOTB <= L_XCH && L_AB + 2048 <= L_FULL, "LDS map");

DI int fresh_lane() { int l; asm volatile("v_mbcnt_lo_u32_b32 %0, -1, 0\n\tv_mbcnt_hi_u32_b32 %0, -1, %0" : "=v"(l)); return l; }
#define LANE_VARS const int lane = fresh_lane(), r32 = lane & 31, hi = lane >> 5, tq = (lane & 15) >> 2, tp = lane & 3, tblk = (lane >> 4) & 1, \
  wrow = 4 * hi + tq, wsw = 4 * ((wrow >> 1) & 1), wch = 2 * tblk + (tp >> 1), c24 = r32 < 24 ? r32 : 23; \
  (void)r32; (void)hi; (void)wsw; (void)wch; (void)c24
DI int fresh_s(int v) { asm volatile("" : "+s"(v)); return v; }
#define PHASE_VARS const int tm = fresh_s(tm_), tn = fresh_s(tn_), wid = fresh_s(wid_), tile = tm * 4 + wid, hh = wid & 1, kh = wid >> 1; (void)tile; (void)hh; (void)kh; LANE_VARS
DI int kpos(int tm, int tn, int i) { return i == 0 ? tn : (tn + 1 + (1 * (3 - (tn & 3)) + i - 1) % 15) & 15; }
DI float bf2f(u16 v) { return __uint_as_float(((unsigned)v) << 16); }
DI unsigned pack2(float lo, float hi) { f32x2 v = {lo, hi}; return __builtin_bit_cast(unsigned, __builtin_convertvector(v, bf16x2)); }
DI float rbf(float f) { return __uint_as_float(pack2(f, 0.f) << 16); }
DI float lo16(unsigned u) { return __uint_as_float(u << 16); }
DI float hi16(unsigned u) { return __uint_as_float(u & 0xffff0000u); }
DI float opq(float r) { asm("" : "+v"(r)); return r; }
DI float fadd(float a, float b) { return opq(a + b); }
DI float fmul(float a, float b) { return opq(a * b); }
DI float ffma(float a, float b, float c) { return opq(__builtin_fmaf(a, b, c)); }
DI float U2F(unsigned u) { return __uint_as_float(u); }
DI float half_swap_sum(float v) {
  auto r = __builtin_amdgcn_permlane32_swap(__float_as_uint(v), __float_as_uint(v), false, false);
  return fadd(U2F(r[0]), U2F(r[1]));
}
DI float half_swap_max(float v) {
  auto r = __builtin_amdgcn_permlane32_swap(__float_as_uint(v), __float_as_uint(v), false, false);
  return fmaxf(__uint_as_float(r[0]), __uint_as_float(r[1]));
}
template <int CTRL> DI unsigned dpp(unsigned v) { return (unsigned)__builtin_amdgcn_update_dpp(0, (int)v, CTRL, 0xf, 0xf, false); }
DI float row_swap_sum(float v) {
  auto r = __builtin_amdgcn_permlane16_swap(__float_as_uint(v), __float_as_uint(v), false, false);
  return fadd(U2F(r[0]), U2F(r[1]));
}
DI float half_sum(float v) {
  v = fadd(v, U2F(dpp<0xB1>(__float_as_uint(v)))); v = fadd(v, U2F(dpp<0x4E>(__float_as_uint(v))));
  v = fadd(v, U2F(dpp<0x141>(__float_as_uint(v)))); v = fadd(v, U2F(dpp<0x140>(__float_as_uint(v))));
  return row_swap_sum(v);
}
DI float wave_sum(float v) { return half_swap_sum(half_sum(v)); }
DI unsigned half_max_u32(unsigned v) {
  v = max(v, dpp<0xB1>(v)); v = max(v, dpp<0x4E>(v)); v = max(v, dpp<0x141>(v)); v = max(v, dpp<0x140>(v));
  auto r = __builtin_amdgcn_permlane16_swap(v, v, false, false);
  return max(r[0], r[1]);
}
DI unsigned wave_max_u32(unsigned v) {
  v = half_max_u32(v);
  auto r = __builtin_amdgcn_permlane32_swap(v, v, false, false);
  return max(r[0], r[1]);
}
DI u32x4 fo_swap(u32x4 v) {
  auto a = __builtin_amdgcn_permlane32_swap(v[0], v[2], false, false);
  auto b = __builtin_amdgcn_permlane32_swap(v[1], v[3], false, false);
  u32x4 o; o[0] = a[0]; o[1] = b[0]; o[2] = a[1]; o[3] = b[1];
  return o;
}
DI void glds4(const void* sb, unsigned vo, unsigned lds_dst) {
  unsigned keep;
  asm volatile("s_mov_b32 %0, m0\n\ts_mov_b32 m0, %3\n\ts_nop 0\n\tglobal_load_lds_dword %1, %2\n\ts_mov_b32 m0, %0"
               : "=&s"(keep) : "v"(vo), "s"(sb), "s"(lds_dst) : "memory");
}
#define GL_ "global_load_lds_dwordx4 "
DI void glds_v4(const void* sb, const unsigned (&v)[4], unsigned m0v) {
  unsigned keep;
  asm volatile("s_mov_b32 %0, m0\n\ts_mov_b32 m0, %6\n\ts_nop 0\n\t" GL_ "%1, %5\n\t" GL_ "%2, %5 offset:1024\n\t" GL_ "%3, %5 offset:2048\n\t"
               GL_ "%4, %5 offset:3072\n\ts_mov_b32 m0, %0" : "=&s"(keep) : "v"(v[0]), "v"(v[1]), "v"(v[2]), "v"(v[3]), "s"(sb), "s"(m0v) : "memory");
}
#define GP_ "\n\ts_sleep %8\n\ts_waitcnt vmcnt(%7)\n\t"
template <int TH, int SL> DI void glds_v4t(const void* sb, const unsigned (&v)[4], unsigned m0v) {
  unsigned keep;
  asm volatile("s_mov_b32 %0, m0\n\ts_mov_b32 m0, %6\n\ts_nop 0\n\t" GL_ "%1, %5" GP_ GL_ "%2, %5 offset:1024" GP_ GL_ "%3, %5 offset:2048" GP_ GL_ "%4, %5 offset:3072" GP_ "s_mov_b32 m0, %0"
               : "=&s"(keep) : "v"(v[0]), "v"(v[1]), "v"(v[2]), "v"(v[3]), "s"(sb), "s"(m0v), "n"(TH), "n"(SL) : "memory");
}
DI void glds_v3(const void* sb, const unsigned (&v)[3], unsigned m0v) {
  unsigned keep;
  asm volatile("s_mov_b32 %0, m0\n\ts_mov_b32 m0, %5\n\ts_nop 0\n\t" GL_ "%1, %4\n\t" GL_ "%2, %4 offset:1024\n\t" GL_ "%3, %4 offset:2048\n\t"
               "s_mov_b32 m0, %0" : "=&s"(keep) : "v"(v[0]), "v"(v[1]), "v"(v[2]), "s"(sb), "s"(m0v) : "memory");
}
DI float dot2(unsigned a, unsigned b, float c) {
  return __builtin_amdgcn_fdot2_f32_bf16(__builtin_bit_cast(bf16x2, a), __builtin_bit_cast(bf16x2, b), c, false);
}
#define CODE_ADDR(sym, out) do { unsigned long long pc_; unsigned off_; \
  asm volatile("s_getpc_b64 %0\n1:\n\ts_mov_b32 %1, " #sym "-1b" : "=s"(pc_), "=s"(off_)); out = (const char*)(pc_ + (long long)(int)off_); } while (0)
DI void st16(void* p, u32x4 v) { asm volatile("global_store_dwordx4 %0, %1, off sc0 sc1\n\ts_nop 1" :: "v"(p), "v"(v) : "memory"); }
DI void ln_scalars(float mean, float var, float& mb, float& den) { mb = rbf(mean); den = rbf(__builtin_amdgcn_sqrtf(rbf(rbf(var) + 1e-5f))); }
DI unsigned ln_pair(unsigned v, float mb, float rd) {
  const unsigned c = pack2(lo16(v) - mb, hi16(v) - mb);
  return pack2(lo16(c) * rd, hi16(c) * rd);
}
DI unsigned ln_pair_s(unsigned v, float nmb, float rd) {
  const unsigned c = pack2(fadd(lo16(v), nmb), fadd(hi16(v), nmb));
  return pack2(fmul(lo16(c), rd), fmul(hi16(c), rd));
}
template <int N, int M> DI f32x4 sum4(const u32x4 (&v)[M]) {
  f32x4 t;
#pragma unroll
  for (int e = 0; e < 4; ++e) {
    t[e] = U2F(v[0][e]);
#pragma unroll
    for (int i = 1; i < N; ++i) t[e] = fadd(t[e], U2F(v[i][e]));
  }
  return t;
}
#define PERM_LO(x, y) __builtin_amdgcn_perm(y, x, 0x05040100u)
#define PERM_HI(x, y) __builtin_amdgcn_perm(y, x, 0x07060302u)
constexpr unsigned SPIN_CAP = 1u << 15;
DI unsigned nz(unsigned v) { return min(v, SENT - 1u); }
DI unsigned nzf(float v) { return nz(__float_as_uint(v)); }
DI unsigned mx4(unsigned m, const u32x4& v) { return max(max(max(m, v[0]), v[1]), max(v[2], v[3])); }
template <int N> DI bool none_sent(const u32x4 (&v)[N]) {
  unsigned m = 0u;
#pragma unroll
  for (int i = 0; i < N; ++i) m = mx4(m, v[i]);
  return m != SENT;
}
typedef __amdgpu_buffer_rsrc_t rsrc_t;
DI u32x4 ldg(rsrc_t rs, unsigned off) { return __builtin_amdgcn_raw_buffer_load_b128(rs, (int)off, 0, 16); }
DI void stg(rsrc_t rs, unsigned off, u32x4 v) { __builtin_amdgcn_raw_buffer_store_b128(v, rs, (int)off, 0, 16); }
DI void poll_pause(bool slow) { if (slow) __builtin_amdgcn_s_sleep(4); asm volatile("s_sleep 1" ::: "memory"); }
template <int N, class LD, class OKF> DI void poll_all(u32x4 (&out)[N], bool slow, LD ld, OKF okf) {
  for (unsigned spins = 0;; ++spins) {
    ld(out);
    SBAR();
    if (__all(okf(out)) || spins > SPIN_CAP) break;
    poll_pause(slow);
  }
}
DI void lds_set(unsigned addr, unsigned v) { asm volatile("s_waitcnt lgkmcnt(0)\n\tds_write_b32 %0, %1" :: "v"(addr), "v"(v) : "memory"); }
template <int SLEEP = 1> DI void lds_wait_ge(unsigned addr, unsigned need) {
  unsigned spin = 0x100000, v;
  asm volatile("1:\n\tds_read_b32 %0, %2\n\ts_sub_u32 %1, %1, 1\n\ts_waitcnt lgkmcnt(0)\n\ts_cbranch_scc1 2f\n\tv_cmp_gt_u32 vcc, %3, %0\n\ts_cbranch_vccz 2f\n\ts_sleep %4\n\ts_branch 1b\n2:"
               : "=&v"(v), "+s"(spin) : "v"(addr), "s"(need), "n"(SLEEP) : "vcc", "scc", "memory");
}
DI s16x8 tr2(const char* p, int second) {
  s16x4 lo = __builtin_amdgcn_ds_read_tr16_b64_v4i16(LDS_PTR(s16x4, p)), hi = __builtin_amdgcn_ds_read_tr16_b64_v4i16(LDS_PTR(s16x4, p + second));
  return __builtin_shufflevector(lo, hi, 0, 1, 2, 3, 4, 5, 6, 7);
}
DI u32x16 cat4(const u32x4& a, const u32x4& b, const u32x4& c, const u32x4& d) {
  const u32x8 x = __builtin_shufflevector(a, b, 0, 1, 2, 3, 4, 5, 6, 7), y = __builtin_shufflevector(c, d, 0, 1, 2, 3, 4, 5, 6, 7);
  return __builtin_shufflevector(x, y, 0, 1, 2, 3, 4, 5, 6, 7, 8, 9, 10, 11, 12, 13, 14, 15);
}

DI unsigned stats_dst(unsigned sx, int tile, int tn, int lane) { return lane < 32 ? sx + SX_ST + tile * 8192 + tn * 512 + lane * 16 : OOB; }
DI void row_stats_sum(rsrc_t rs, unsigned sx, int tile, int lane, float& S, float& Q, float& Dd) {
  const unsigned exb = sx + SX_ST + tile * 8192 + (lane & 31) * 16;
  u32x4 pv[8];
  poll_all<8>(pv, false, [&](u32x4 (&v)[8]) {
#pragma unroll
    for (int i = 0; i < 8; ++i) v[i] = ldg(rs, exb + ((lane >> 5) * 8 + i) * 512);
  }, [&](const u32x4 (&v)[8]) { return none_sent(v); });
  S = 0.f; Q = 0.f; Dd = 0.f;
#pragma unroll
  for (int i = 0; i < 8; ++i) { S += __uint_as_float(pv[i][0]); Q += __uint_as_float(pv[i][1]); Dd += __uint_as_float(pv[i][2]); }
}
DI void row_stats_fin(rsrc_t rs, unsigned kdst, char* smem, int epoch, int tile, int lane, float S, float Q, float Dd, float wsum, const u32x4 (&x1p)[4],
                      float& mean, float& var, float& mb, float& rd) {
  S = half_swap_sum(S); Q = half_swap_sum(Q); Dd = half_swap_sum(Dd);
  mean = S * (1.0f / 1024.0f); var = fmaxf(Q * (1.0f / 1024.0f) - mean * mean, 0.f);
  float den;
  ln_scalars(mean, var, mb, den);
  rd = RCP(den);
  const unsigned rb = pack2((Dd - mb * wsum) * rd, 0.f) & 0xffffu;
  const unsigned mine = (((rb & 0x8000u) ? (~rb & 0xffffu) : (rb | 0x8000u)) << 16) | (unsigned)(2047 - (tile * 32 + (lane & 31)));
  const bool best = half_max_u32(mine) == mine;
  __builtin_amdgcn_raw_buffer_store_b32(mine, rs, (best && lane < 32) ? kdst : OOB, 0, 16);
  if (best) {
    char* c = smem + L_CAND + (tile & 3) * 144;
#pragma unroll
    for (int i = 0; i < 4; ++i) *reinterpret_cast<u32x4*>(c + (2 * i + (lane >> 5)) * 16) = x1p[i];
    *reinterpret_cast<u32x4*>(c + 128) = u32x4{mine, __float_as_uint(mb), __float_as_uint(rd), 0u};
  }
  lds_set((unsigned)(uintptr_t)smem + L_W + 32 + (tile & 3) * 4, epoch);
}

DI void attn_safe(rsrc_t rs, unsigned kvoff, const u32x8& q, const u32x4& ones, f32x16& o) {
  const s16x8 q0 = __builtin_bit_cast(s16x8, __builtin_shufflevector(q, q, 0, 1, 2, 3)), q1 = __builtin_bit_cast(s16x8, __builtin_shufflevector(q, q, 4, 5, 6, 7));
  f32x4 l = {0.f, 0.f, 0.f, 0.f};
#pragma unroll
  for (int e = 0; e < 16; ++e) o[e] = 0.f;
  float m = -INFINITY;
#pragma unroll 1
  for (int kt = 0; kt < 64; ++kt) {
    const unsigned p = kvoff + kt * 4096;
    const u32x4 k0 = ldg(rs, p), k1 = ldg(rs, p + 1024), v0 = ldg(rs, p + 2048), v1 = ldg(rs, p + 3072);
    f32x16 s = {};
    s = MFMA32(__builtin_bit_cast(s16x8, k0), q0, s);
    s = MFMA32(__builtin_bit_cast(s16x8, k1), q1, s);
    float mt = fmaxf(s[0], s[1]);
#pragma unroll
    for (int e = 2; e < 16; ++e) mt = fmaxf(mt, s[e]);
    const float mn = fmaxf(m, half_swap_max(mt));
    const float alpha = EX2(m - mn);
    m = mn;
#pragma unroll
    for (int e = 0; e < 16; ++e) { s[e] = EX2(s[e] - mn); o[e] *= alpha; }
    l[0] *= alpha;
    const bf16x16 pa = __builtin_bit_cast(bf16x16, __builtin_shufflevector(s, s, 0, 1, 2, 3, 4, 5, 6, 7));
    const bf16x16 pb = __builtin_bit_cast(bf16x16, __builtin_shufflevector(s, s, 8, 9, 10, 11, 12, 13, 14, 15));
    o = __builtin_amdgcn_smfmac_f32_32x32x32_bf16(__builtin_bit_cast(bf16x8, v0), pa, o, 0xdddd, 0, 0);
    l = __builtin_amdgcn_smfmac_f32_16x16x64_bf16(__builtin_bit_cast(bf16x8, ones), pa, l, 0xdddd, 0, 0);
    o = __builtin_amdgcn_smfmac_f32_32x32x32_bf16(__builtin_bit_cast(bf16x8, v1), pb, o, 0xdddd, 0, 0);
    l = __builtin_amdgcn_smfmac_f32_16x16x64_bf16(__builtin_bit_cast(bf16x8, ones), pb, l, 0xdddd, 0, 0);
  }
  o *= 1.0f / l[0];
}

extern "C" __global__ __launch_bounds__(512) void mega_kernel(const u16* __restrict__ x0, const u16* __restrict__ wqkv, const u16* __restrict__ wo,
                                                              const u16* __restrict__ w_r, const u16* __restrict__ wg, const u16* __restrict__ wu,
                                                              const u16* __restrict__ wd, char* ws, u16* __restrict__ out) {
  __shared__ __attribute__((aligned(16))) char smem[LDS_BYTES];
  const int wid = __builtin_amdgcn_readfirstlane(threadIdx.x >> 6);
  STAMP_INIT;
  STAMPV(0);
  const int bid = blockIdx.x, xcd = bid & 7, loc = bid >> 3;
  const int tm = 8 * (xcd >> 2) + (loc >> 2), tn = 4 * (xcd & 3) + (loc & 3);
  const unsigned lbase = (unsigned)(uintptr_t)smem;
  const rsrc_t rs = __builtin_amdgcn_make_buffer_rsrc(ws, 0, (int)WS_BYTES, 0x00020000);
  volatile __attribute__((address_space(3))) unsigned* words = LDS_PTR(volatile unsigned, smem + L_W);
  u32x4 v[4];
  if (wid < 4)
    asm volatile("global_load_dwordx4 %0, %4, %5\n\tglobal_load_dwordx4 %1, %4, %5 offset:512\n\tglobal_load_dwordx4 %2, %4, %5 offset:1024\n\tglobal_load_dwordx4 %3, %4, %5 offset:1536"
                 : "=&v"(v[0]), "=&v"(v[1]), "=&v"(v[2]), "=&v"(v[3])
                 : "v"((threadIdx.x >> 5) * (D_ * 2) + (threadIdx.x & 31) * 16), "s"(x0 + (size_t)(tm * 128 + tn * 8) * D_) : "memory");
  else {
    asm volatile("" : "=v"(v[0]), "=v"(v[1]), "=v"(v[2]), "=v"(v[3]));
    if (wid == 6) {
      const char *c0, *c1;
      CODE_ADDR(mega_kernel, c0); CODE_ADDR(end_marker, c1);
      const unsigned o = (loc * 64 + (threadIdx.x & 63)) * 32;
      glds4(c0, (long long)o + 4 <= c1 - c0 ? o : 0u, lbase + NS * SLOTB);
    }
  }
  STAMPV(48);
  *reinterpret_cast<unsigned*>(smem + L_FULL + threadIdx.x * 4) = 0u;
  words[threadIdx.x & 31] = 0u;
  BAR_LGKM();
  if (wid < 4) {
    const int tid = threadIdx.x, lane = tid & 63, r32 = lane & 31, row = 2 * wid + (lane >> 5);
    char* sm = smem + L_XCH;
    unsigned dst = OFF_HF + (tm * 4 + (tn >> 2)) * TS + (tn & 3) * 128 + (tid >> 3) * 512 + (tid & 7) * 16;
    asm volatile("s_waitcnt vmcnt(0)" : "+v"(v[0]), "+v"(v[1]), "+v"(v[2]), "+v"(v[3]), "+v"(dst) :: "memory");
    STAMPV(49);
    float s = 0.f, s2 = 0.f;
#pragma unroll
    for (int i = 0; i < 4; ++i)
#pragma unroll
      for (int e = 0; e < 4; e += 2) { s = dot2(v[i][e], 0x3f803f80u, s); s2 = dot2(v[i][e + 1], 0x3f803f80u, s2); }
    const float mean = half_sum(s + s2) * (1.0f / 1024.0f);
    f32x2 m2 = {0.f, 0.f};
#pragma unroll
    for (int i = 0; i < 4; ++i)
#pragma unroll
      for (int e = 0; e < 4; ++e) { const f32x2 d = f32x2{lo16(v[i][e]), hi16(v[i][e])} - mean; m2 += d * d; }
    float mb, den;
    ln_scalars(mean, half_sum(m2[0] + m2[1]) * (1.0f / 1024.0f), mb, den);
    const float rd = 1.0f / den;
#pragma unroll
    for (int i = 0; i < 4; ++i) {
      char* p = sm + ((16 * i + (r32 >> 1)) * 2) * 144 + row * 16 + (r32 & 1) * 8;
      *reinterpret_cast<u32x2*>(p) = u32x2{ln_pair(v[i][0], mb, rd), ln_pair(v[i][1], mb, rd)};
      *reinterpret_cast<u32x2*>(p + 144) = u32x2{ln_pair(v[i][2], mb, rd), ln_pair(v[i][3], mb, rd)};
    }
    STAMPV(37);
    lds_set(lbase + L_W + 64 + 4 * wid, 1);
    lds_wait_ge<0>(lbase + L_W + 64 + 4 * (lane & 3), 1);
    STAMPV(50);
    u32x4 t[4];
#pragma unroll
    for (int i = 0; i < 4; ++i) t[i] = *reinterpret_cast<const u32x4*>(sm + (i * 32 + (tid >> 3)) * 144 + (tid & 7) * 16);
    SBAR();
#pragma unroll
    for (int i = 0; i < 4; ++i) stg(rs, dst + i * 16384, t[i]);
    STAMPV(51);
    DRAIN();
    lds_set(lbase + L_W + 64 + 4 * wid, 2);
    STAMPV(38);
  }
  const int lw = wid & 1;
  const char* wb = reinterpret_cast<const char*>(wqkv + (size_t)(16 * lw) * (3 * D_) + tn * 64);
  auto stage_lanes = [](unsigned (&wv)[3]) {
    const int lane = fresh_lane();
#pragma unroll
    for (int i = 0; i < 3; ++i) {
      const int li = lane + 64 * i, k = li / 24, cp = li % 24, c = cp ^ (4 * ((k >> 1) & 1));
      wv[i] = (unsigned)(k * (3 * D_ * 2) + (c >> 3) * (D_ * 2) + (c & 7) * 16 - i * 1024);
    }
  };
  auto stage = [&](const unsigned (&wv)[3], int s, int slot) {
    const char* p = wb + (size_t)(2 * kpos(tm, tn, s >> 1) + (s & 1)) * (32 * 3 * D_ * 2);
    glds_v3(p, wv, lbase + slot * SLOTB + lw * 6144);
    glds_v3(p + 8 * (3 * D_ * 2), wv, lbase + slot * SLOTB + lw * 6144 + 3072);
  };
  auto exchange = [&](const u32x2& tag, unsigned line0, int me, bool bar = false) {
    const int lane = fresh_lane();
    __builtin_amdgcn_raw_buffer_store_b64(tag, rs, lane == 0 ? line0 + me * 128 : OOB, 0, 16);
    if (bar) BARRIER();
    unsigned po = lane < 16 ? line0 + lane * 128 : OOB;
    const u32x4 srd = {(unsigned)(uintptr_t)ws, (unsigned)((uintptr_t)ws >> 32) & 0xffffu, WS_BYTES, 0x00020000u};
    for (unsigned spins = 0;; ++spins) {
      u32x2 v = {0u, 0u};
      asm volatile("buffer_atomic_or_x2 %0, %1, %2, 0 offen sc0 sc1\n\ts_waitcnt vmcnt(0)" : "+v"(v) : "v"(po), "s"(srd) : "memory");
      po = ((v[0] == tag[0]) & (v[1] == tag[1])) ? OOB : po;
      if (__all(po == OOB) || spins > SPIN_CAP) break;
    }
  };
  if ((wid >> 1) == 2) {
    unsigned wv[3];
    stage_lanes(wv);
    __builtin_amdgcn_s_sleep(0);
    const int r = tm & 7;
#pragma unroll 1
    for (int i = 0; i < NS; ++i) {
      const int s = i == 0 ? r : i <= r ? i - 1 : i;
      stage(wv, s, s);
      const int j = i - 1, full = j < 1 ? (j == 0 && r == 0) : j < r ? j : j + 1;
      asm volatile("s_waitcnt vmcnt(%2)\n\tds_write_b32 %0, %1" :: "v"(lbase + L_FULL + lw * 256), "v"(full), "n"(6 * 1) : "memory");
    }
    asm volatile("s_waitcnt vmcnt(0)\n\tds_write_b32 %0, %1" :: "v"(lbase + L_FULL + lw * 256), "v"(NS) : "memory");
    STAMPV(1);
  }
  if (wid == 7) {
    u32x2 tag = __builtin_amdgcn_raw_buffer_load_b64(rs, FL_EPOCH, 0, 16);
    tag[0] += 1u; tag[1] += 0x9E3779B9u;
    *reinterpret_cast<u32x2*>(smem + L_TAG) = tag;
    lds_wait_ge<0>(lbase + L_W + 64 + 4 * (fresh_lane() & 3), 2);
    exchange(tag, FL_X + tm * 2048, tn);
    lds_set(lbase + L_W + 24, 1);
  }
  if (wid >= 4) {
    auto wo_rows = [&](int g0, int n, int gs) {
      const int lane = fresh_lane();
      unsigned wov[4];
#pragma unroll
      for (int i = 0; i < 4; ++i) wov[i] = (unsigned)((8 * i + (lane >> 3)) * (D_ * 2) + ((lane & 7) ^ (4 * ((lane >> 4) & 1))) * 16 - i * 1024);
#pragma unroll 1
      for (int i = 0, k = (2 * (tm & 7)) % n, g; i < n && !0; ++i, k = k + 1 == n ? 0 : k + 1) g = g0 + gs * k, glds_v4t<3, 1>(reinterpret_cast<const char*>(wo) + (size_t)((((tn >> 2) + 1 + (g >> 3)) & 3) * 256 + ((0 * 64 * tn + 32 * g) & 255)) * (D_ * 2) + tn * 128, wov, lbase + g * 4096);
    };
    if (wid < 6) {
      const int lane = fresh_lane();
      unsigned wv[3];
      stage_lanes(wv);
      __builtin_amdgcn_s_setprio(3);
      const unsigned fra = lbase + L_FREE + (lane & 3) * 256, fla = lbase + L_FULL + lw * 256 + lane * 4;
      const rsrc_t rw = __builtin_amdgcn_make_buffer_rsrc(const_cast<char*>(wb), 0, 0 ? 0 : 6 * MBY - (16 * lw * 3 * D_ + tn * 64) * 2, 0x00020000);
#pragma unroll 1
      for (int L = 0; L < 2; ++L) {
        const int s0 = NS, lead = L ? 8 : NS - 1;
        int slot = 0;
#pragma unroll 1
        for (int s = NS; s < 32; ++s) {
          lds_wait_ge(fra, 32 * L + s - lead);
          stage(wv, s, slot);
          slot = (slot == NS - 1) ? 0 : slot + 1;
          if (s >= s0 + DD) asm volatile("s_waitcnt vmcnt(%0)\n\tds_write_b32 %1, %2" :: "n"(DD * 6), "v"(fla), "v"(32 * L + s - DD + 1) : "memory");
        }
#pragma unroll
        for (int d = DD - 1; d >= 0; --d) asm volatile("s_waitcnt vmcnt(%0)\n\tds_write_b32 %1, %2" :: "n"(d * 6), "v"(fla), "v"(32 * L + 32 - d) : "memory");
        u32x2 tag = *reinterpret_cast<const u32x2*>(smem + L_TAG);
        PIN(tag);
        if (lw == 0) {
          lds_wait_ge(lbase + L_W + 96 + 4 * (lane & 1), 1);
          lds_wait_ge<0>(lbase + L_W + 80 + 4 * (lane & 3), 2 * L + 1);
          exchange(tag, FL_KV + (L * 16 + tn) * 2048, tm, true);
          lds_wait_ge(fra, 32 * L + 32);
          lds_set(lbase + L_W + 16, L + 1);
        } else { BAR_LGKM(); lds_wait_ge<8>(lbase + L_W + 16, L + 1); }
        __builtin_amdgcn_s_sleep(24);
        wo_rows(lw, 15, 2);
        u32x4 pre[NS * 6];
        if (L == 0) {
#pragma unroll
          for (int s = 0; s < NS; ++s) {
#pragma unroll
            for (int i = 0; i < 6; ++i)
              pre[s * 6 + i] = __builtin_amdgcn_raw_buffer_load_b128(rw, wv[i % 3], (2 * kpos(tm, tn, s >> 1) + (s & 1)) * (32 * 3 * D_ * 2) + (i / 3) * (8 * 3 * D_ * 2) + (i % 3) * 1024, 0), PACE();
          }
        } else {
#pragma unroll
          for (int i = 0; i < NS * 6; ++i) asm volatile("" : "=v"(pre[i]));
        }
        DRAIN();
        const unsigned fo = FL_O + (L * 16 + tm) * 2048;
        if (lw) {
          lds_set(lbase + L_W + 112, 2 * L + 1);
          lds_wait_ge<4>(lbase + L_W + 48 + 4 * (lane & 3), L + 1);
          lds_wait_ge<0>(lbase + L_W + 80 + 4 * (lane & 3), 2 * L + 2);
          __builtin_amdgcn_raw_buffer_store_b64(tag, rs, lane == 0 ? fo + tn * 128 : OOB, 0, 16);
          wo_rows(30, 2, 1);
          DRAIN();
          lds_set(lbase + L_W + 112, 2 * L + 2);
        } else {
          lds_wait_ge<4>(lbase + L_W + 112, 2 * L + 1);
          lds_wait_ge<4>(lbase + L_W + 48 + 4 * (lane & 3), L + 1);
          unsigned po = lane < 16 ? fo + ((lane + 4 + (tn & 12)) & 15) * 128 : OOB;
          const u32x4 srd = {(unsigned)(uintptr_t)ws, (unsigned)((uintptr_t)ws >> 32) & 0xffffu, WS_BYTES, 0x00020000u};
          for (unsigned spins = 0;; ++spins) {
            u32x2 v = {0u, 0u};
            asm volatile("buffer_atomic_or_x2 %0, %1, %2, 0 offen sc0 sc1\n\ts_waitcnt vmcnt(0)" : "+v"(v) : "v"(po), "s"(srd) : "memory");
            po = ((v[0] == tag[0]) & (v[1] == tag[1])) ? OOB : po;
            unsigned m = (unsigned)__ballot(po == OOB);
            m &= m >> 1; m &= m >> 2;
            m = (m & 1u) | ((m >> 3) & 2u) | ((m >> 6) & 4u) | ((m >> 9) & 8u);
            if (m == 15u || spins > SPIN_CAP) break;
            words[5 + 2 * L] = words[28] == 2u * L + 2u ? m : m & 7u;
          }
          words[5 + 2 * L] = words[28] == 2u * L + 2u ? 15u : 7u;
          lds_wait_ge<0>(lbase + L_W + 112, 2 * L + 2);
          words[5 + 2 * L] = 15u;
          __builtin_amdgcn_raw_buffer_store_b64(tag, rs, (bid == 0 && lane == 0) ? FL_EPOCH : OOB, 0, 16);
        }
        BAR_ALL();
        if (L == 0) {
          char* d = smem + lw * 6144 + fresh_lane() * 16;
#pragma unroll
          for (int s = 0; s < NS; ++s) {
#pragma unroll
            for (int i = 0; i < 6; ++i) *reinterpret_cast<u32x4*>(d + s * SLOTB + i * 1024) = pre[s * 6 + i];
            lds_set(fla, 32 + s + 1);
          }
          STAMPV(2);
          lds_wait_ge<2>(lbase + L_W + 24, 2);
        }
      }
      STAMP_END;
      return;
    }
    const int a = wid - 6;
    const bool ffn = loc < 28;
    const int grp = xcd * 7 + (ffn ? loc >> 2 : 0), qa = (loc & 3) * 2 + a;
    const int kr = loc, cg = xcd;
    const unsigned wa = lbase + L_W;
    lds_wait_ge<2>(wa + 24, 1);
    __builtin_amdgcn_s_sleep(16);
    auto transpose8 = [](u32x4* v) {
      u32x4 t[8];
#pragma unroll
      for (int j = 0; j < 8; ++j)
#pragma unroll
        for (int e = 0; e < 4; ++e) t[j][e] = (j & 1) ? PERM_HI(v[2 * e][j >> 1], v[2 * e + 1][j >> 1]) : PERM_LO(v[2 * e][j >> 1], v[2 * e + 1][j >> 1]);
#pragma unroll
      for (int j = 0; j < 8; ++j) { v[j] = t[j]; PIN(v[j]); }
    };
    u32x4 gf[32];
    u32x4 wdf[16];
    auto wait_go = [&](int L) {
      lds_wait_ge<8>(lbase + L_W + 16, L + 1);
      __builtin_amdgcn_s_sleep(24);
    };
    auto load_wd = [&](unsigned dv) {
      const rsrc_t rd = __builtin_amdgcn_make_buffer_rsrc(const_cast<u16*>(wd) + (size_t)grp * 64 * D_ + 128 * qa, 0, 0 ? 0 : 64 * D_ * 2 - 256 * qa, 0x00020000);
#pragma unroll
      for (int i = 0; i < 16; ++i) {
        wdf[i] = __builtin_amdgcn_raw_buffer_load_b128(rd, dv, (32 * (i >> 3) + (i & 7)) * (D_ * 2), 0);
        asm volatile("s_sleep 2\n\ts_waitcnt vmcnt(3)" ::: "memory");
      }
    };
    {
      LANE_VARS;
      const u32x4 sv = {SENT, SENT, SENT, SENT};
#pragma unroll
      for (int i = 0; i < 6; ++i) {
        const unsigned o = ((i * 256 + bid) * 128 + a * 64 + lane) * 16;
        stg(rs, o < SX_BYTES ? OFF_SX + o : OOB, sv);
      }
      if (a == 0) {
        const u32x4 w0 = *reinterpret_cast<const u32x4*>(w_r + lane * 8), w1 = *reinterpret_cast<const u32x4*>(w_r + 512 + lane * 8);
        float t = 0.f;
#pragma unroll
        for (int e = 0; e < 4; ++e) t = fadd(fadd(t, fadd(lo16(w0[e]), hi16(w0[e]))), fadd(lo16(w1[e]), hi16(w1[e])));
        words[0] = __float_as_uint(wave_sum(t));
        if ((lane >> 3) == (tn & 7)) *reinterpret_cast<u32x4*>(smem + L_WR + (lane & 7) * 16) = tn < 8 ? w0 : w1;
      }
      const u16* p = wqkv + (size_t)(64 * tm + 32 * a + hi) * (3 * D_) + (c24 >> 3) * D_ + tn * 64 + (c24 & 7) * 8;
      float cs[8] = {0.f, 0.f, 0.f, 0.f, 0.f, 0.f, 0.f, 0.f};
      u32x4 v[16];
#pragma unroll
      for (int i = 0; i < 16; ++i) v[i] = *reinterpret_cast<const u32x4*>(p + (size_t)i * (2 * 3 * D_));
      SBAR();
#pragma unroll
      for (int i = 0; i < 16; ++i)
#pragma unroll
        for (int e = 0; e < 4; ++e) { cs[2 * e] = fadd(cs[2 * e], lo16(v[i][e])); cs[2 * e + 1] = fadd(cs[2 * e + 1], hi16(v[i][e])); }
      u32x4 o;
#pragma unroll
      for (int e = 0; e < 4; ++e) { const float x = half_swap_sum(cs[e]), y = half_swap_sum(cs[4 + e]); o[e] = __float_as_uint(hi ? y : x); }
      stg(rs, r32 < 24 ? OFF_CS + (tn * 32 + tm * 2 + a) * 768 + r32 * 32 + hi * 16 : OOB, o);
    }
    {
      DRAIN();
      lds_set(wa + 96 + 4 * a, 1);
      BAR_LGKM();
      LANE_VARS;
      const char* gp = reinterpret_cast<const char*>(((lane & 8) ? wu : wg) + (size_t)(128 * qa + 16 * hi + 4 * tblk) * F_ + 64 * grp + (lane & 7) * 8);
      unsigned dv = (lane >> 4) * (8 * D_ * 2) + (lane & 15) * 16;
      PIN(gp); PIN(dv);
      unsigned sv[3];
#pragma unroll
      for (int i = 0; i < 3; ++i) { const int li = lane + 64 * i, k = li / 24, c = li % 24; sv[i] = (unsigned)(k * (3 * D_ * 2) + (c >> 3) * (D_ * 2) + (c & 7) * 16 - i * 1024); PIN(sv[i]); }
      wait_go(0);
#pragma unroll
      for (int i = 0; i < 32; ++i) {
        gf[i] = *reinterpret_cast<const u32x4*>(gp + (size_t)(0 ? 0 : 32 * (i >> 3) + 8 * ((i >> 2) & 1) + (i & 3)) * (F_ * 2));
        asm volatile("s_sleep 2\n\ts_waitcnt vmcnt(3)" ::: "memory");
      }
      load_wd(dv);
      STAMPV(7);
      lds_wait_ge<4>(wa + 80 + 4 * (lane & 3), 2);
#pragma unroll
      for (int g = 0; g < 4; ++g) glds_v3(wqkv + (size_t)(32 * kr + 8 * g) * (3 * D_) + (2 * cg + a) * 64, sv, lbase + L_SL + a * 12288 + g * 3072);
      transpose8(gf); transpose8(gf + 8); transpose8(gf + 16); transpose8(gf + 24);
      transpose8(wdf); transpose8(wdf + 8);
      {
        LANE_VARS;
        f32x4 t[2];
#pragma unroll
        for (int c = 0; c < 2; ++c) {
          u32x4 v[8];
#pragma unroll
          for (int i = 0; i < 8; ++i) v[i] = ldg(rs, OFF_CS + (tn * 32 + 16 * c + 2 * i + hi) * 768 + a * 384 + c24 * 16);
          SBAR();
          t[c] = sum4<8>(v);
          SBAR();
        }
#pragma unroll
        for (int e = 0; e < 4; ++e) t[0][e] = half_swap_sum(fadd(t[0][e], t[1][e]));
        if (lane < 24) *reinterpret_cast<f32x4*>(smem + L_CS + a * 384 + lane * 16) = t[0];
      }
      u32x2 tag = *reinterpret_cast<const u32x2*>(smem + L_TAG);
      PIN(tag);
      BAR_ALL();
      if (a) {
        lds_wait_ge<0>(wa + 64 + 4 * (lane & 3), 3);
        exchange(tag, FL_X + (16 + tm) * 2048, tn);
        lds_set(wa + 24, 2);
        STAMPV(12);
      }
    }
#pragma unroll 1
    for (int L = fresh_s(0);; ++L) {
      const unsigned sx = OFF_SX + L * SX_LAYER;
      STAMPV(1 + 16 * L);
      if (0 && L == 0) { words[3] = 5000u; lds_set(wa + 4 + 4 * a, 1); } else {
      u32x4 hf[4];
      unsigned key;
      { LANE_VARS;
      lds_wait_ge<0>(wa + 32 + 4 * tp, L + 1);
      if (a == 0) {
        const char* cp = smem + L_CAND + (r32 >> 3) * 144;
        const u32x4 c = *reinterpret_cast<const u32x4*>(cp + (lane & 7) * 16), rec = *reinterpret_cast<const u32x4*>(cp + 128);
        u32x4 o;
#pragma unroll
        for (int e = 0; e < 4; ++e) { unsigned h = ln_pair_s(c[e], -U2F(rec[1]), U2F(rec[2])); PIN(h); o[e] = nz(hi ? c[e] : h); }
        stg(rs, sx + SX_ROW + hi * 131072 + (tm * 4 + (r32 >> 3)) * 2048 + tn * 128 + (lane & 7) * 16, o);
        STAMPV(8 + 16 * L);
        lds_wait_ge<0>(wa + 104, L + 1);
        key = __builtin_amdgcn_readfirstlane(words[27]);
      } else {
        const u32x4 srd = {(unsigned)(uintptr_t)ws, (unsigned)((uintptr_t)ws >> 32) & 0xffffu, WS_BYTES, 0x00020000u};
        unsigned po = sx + SX_CAND + (tn * 16 + (lane >> 2)) * 128 + tp * 4;
        key = 0u;
        for (unsigned spins = 0;; ++spins) {
          unsigned v = 0u;
          asm volatile("buffer_atomic_or %0, %1, %2, 0 offen sc0 sc1\n\ts_waitcnt vmcnt(0)" : "+v"(v) : "v"(po), "s"(srd) : "memory");
          key = v == SENT ? key : max(key, v);
          po = v == SENT ? po : OOB;
          if (__all(po == OOB) || spins > SPIN_CAP) break;
        }
        key = __builtin_amdgcn_readfirstlane(wave_max_u32(key));
        words[27] = key;
        words[3] = 2047u - (key & 2047u);
        lds_set(wa + 104, L + 1);
        STAMPV(13 + 16 * L);
      }
      poll_all<4>(hf, false, [&](u32x4 (&t)[4]) {
#pragma unroll
        for (int i = 0; i < 4; ++i) t[i] = ldg(rs, sx + SX_ROW + ((2047 - (int)(key & 2047u)) >> 5) * 2048 + (16 * qa + 4 * i + (lane >> 4)) * 16);
      }, [&](const u32x4 (&t)[4]) { return none_sent(t); });
      if (L == 1) lds_set(wa + 4 + 4 * a, 2);
      }
      STAMPV(2 + 16 * L);
      f32x4 d[8];
      const f32x4 zero = {0.f, 0.f, 0.f, 0.f};
      SBAR();
      __builtin_amdgcn_s_setprio(3);
#pragma unroll
      for (int t = 0; t < 32; ++t) d[t & 7] = MFMA16(__builtin_bit_cast(s16x8, hf[t >> 3]), __builtin_bit_cast(s16x8, gf[t]), t < 8 ? zero : d[t & 7]);
      SBAR();
      __builtin_amdgcn_s_setprio(0);
      LANE_VARS;
      const int idx = 2047 - (int)(key & 2047u), win = idx >> 7;
      const bool owner = win == tm;
      auto pick2 = [&](const f32x4 (&d)[8]) {
        u32x2 o;
#pragma unroll
        for (int e = 0; e < 2; ++e) {
          const float x = tblk ? d[2 + e][0] : d[e][0], y = tblk ? d[6 + e][0] : d[4 + e][0];
          o[e] = nzf(hi ? y : x);
        }
        return o;
      };
      if (ffn) {
        const unsigned ppos = (lane & 15) * 32 + (lane >> 4) * 8;
        __builtin_amdgcn_raw_buffer_store_b64(pick2(d), rs, sx + SX_GP + (grp * 8 + qa) * 512 + ppos, 0, 16);
        STAMPV(9 + 16 * L);
        __builtin_amdgcn_s_sleep(13);
        if (L == 0) __builtin_amdgcn_s_sleep(0);
        u32x2 gu[8];
        for (unsigned spins = 0;; ++spins) {
#pragma unroll
          for (int i = 0; i < 8; ++i) gu[i] = __builtin_amdgcn_raw_buffer_load_b64(rs, sx + SX_GP + (grp * 8 + i) * 512 + lane * 8, 0, 16);
          SBAR();
          unsigned m = 0u;
#pragma unroll
          for (int i = 0; i < 8; ++i) m = max(max(m, gu[i][0]), gu[i][1]);
          if (__all(m != SENT) || spins > SPIN_CAP) break;
          poll_pause(L == 0);
        }
        STAMPV(10 + 16 * L);
        float t0 = opq(U2F(gu[0][0])), t1 = opq(U2F(gu[0][1]));
#pragma unroll
        for (int i = 1; i < 8; ++i) { t0 = fadd(t0, U2F(gu[i][0])); t1 = fadd(t1, U2F(gu[i][1])); }
        auto sw = __builtin_amdgcn_permlane32_swap(__float_as_uint(t0), __float_as_uint(t1), false, false);
        const float gv = rbf(U2F(sw[0])), uv = rbf(U2F(sw[1]));
        reinterpret_cast<u16*>(smem + L_GU + a * 128)[2 * r32 + hi] = (u16)pack2(fmul(rbf(fmul(gv, RCP(fadd(1.0f, __expf(-gv))))), uv), 0.f);
        asm volatile("s_waitcnt lgkmcnt(0)" ::: "memory");
        const s16x8 a0 = *reinterpret_cast<const s16x8*>(smem + L_GU + a * 128 + (lane >> 4) * 16), a1 = *reinterpret_cast<const s16x8*>(smem + L_GU + a * 128 + 64 + (lane >> 4) * 16);
        SBAR();
        __builtin_amdgcn_s_setprio(3);
#pragma unroll
        for (int t = 0; t < 16; ++t) d[t & 7] = MFMA16(t < 8 ? a0 : a1, __builtin_bit_cast(s16x8, wdf[t]), t < 8 ? zero : d[t & 7]);
        SBAR();
        __builtin_amdgcn_s_setprio(0);
        __builtin_amdgcn_raw_buffer_store_b64(pick2(d), rs, sx + SX_YP + grp * 4096 + qa * 512 + ppos, 0, 16);
      }
      STAMPV(3 + 16 * L);
      if (L == 1 && (cg != (kr & 7) || a)) { STAMP_END; __builtin_amdgcn_endpgm(); }
      s16x8 wpf[12];
#pragma unroll
      for (int t = 0; t < 12; ++t) wpf[t] = tr2(smem + L_SL + a * 12288 + (16 * hi + 4 * tblk + tq) * 384 + t * 32 + tp * 8, 8 * 384);
#pragma unroll
      for (int t = 0; t < 12; ++t) PIN(wpf[t]);
      if (L == 0 && !owner) lds_set(wa + 4 + 4 * a, 1);
      __builtin_amdgcn_s_sleep(13 - 4);
      if (L == 0) __builtin_amdgcn_s_sleep(0);
      if (L == 0 && !ffn) __builtin_amdgcn_s_sleep(64);
      const unsigned rk = key >> 16;
      float rsig = rbf(RCP(fadd(1.0f, __expf(-lo16((rk & 0x8000u) ? (rk & 0x7fffu) : (~rk & 0xffffu))))));
      PIN(rsig);
      const int n = 4 * (lane & 7) + ((lane >> 3) & 3);
      const int fo = ((n >> 4) * 2 + ((n >> 2) & 1)) * 8 + ((n >> 3) & 1) * 4 + (n & 3);
      u32x4 pv[8];
      poll_all<8>(pv, L == 0, [&](u32x4 (&t)[8]) {
#pragma unroll
        for (int i = 0; i < 7; ++i) t[i] = ldg(rs, sx + SX_YP + (8 * i + (lane >> 3)) * 4096 + kr * 128 + (lane & 7) * 16);
        t[7][0] = t[7][2] = __builtin_amdgcn_raw_buffer_load_b32(rs, sx + SX_ROW + (idx >> 5) * 2048 + kr * 64 + (fo >> 1) * 4, 0, 16);
        t[7][1] = t[7][3] = __builtin_amdgcn_raw_buffer_load_b32(rs, sx + SX_ROW + 131072 + (idx >> 5) * 2048 + kr * 64 + (fo >> 1) * 4, 0, 16);
      }, [&](const u32x4 (&t)[8]) { return none_sent(t); });
      const float hv = (fo & 1) ? hi16(pv[7][0]) : lo16(pv[7][0]), xv = (fo & 1) ? hi16(pv[7][1]) : lo16(pv[7][1]);
      STAMPV(4 + 16 * L);
      float ys;
      {
        f32x4 t = sum4<7>(pv);
#pragma unroll
        for (int e = 0; e < 4; ++e) {
          t[e] = fadd(t[e], U2F(dpp<0x128>(__float_as_uint(t[e]))));
          t[e] = half_swap_sum(row_swap_sum(t[e]));
        }
        const float x = (lane & 8) ? t[1] : t[0], y = (lane & 8) ? t[3] : t[2];
        ys = tblk ? y : x;
      }
      const unsigned ob = pack2(fadd(xv, rbf(fadd(hv, rbf(fmul(rbf(ys), rsig))))), 0.f) & 0xffffu;
      if (L == 1) {
        if (lane < 32) out[(size_t)idx * D_ + kr * 32 + n] = (u16)ob;
        STAMPV(5 + 16 * L);
        STAMP_END;
        __builtin_amdgcn_endpgm();
      }
      reinterpret_cast<u16*>(smem + L_X2F + a * 64)[fo] = (u16)ob;
      asm volatile("s_waitcnt lgkmcnt(0)" ::: "memory");
      {
        const s16x8 xa = *reinterpret_cast<const s16x8*>(smem + L_X2F + a * 64 + (lane >> 4) * 16);
        const u32x4 xf = *reinterpret_cast<const u32x4*>(smem + L_X2F + a * 64 + tp * 16);
        f32x4 z[12];
        SBAR();
        __builtin_amdgcn_s_setprio(3);
#pragma unroll
        for (int t = 0; t < 12; ++t) z[t] = MFMA16(xa, wpf[t], zero);
        SBAR();
        __builtin_amdgcn_s_setprio(0);
        const float x2 = lo16(ob), s1 = half_sum(x2), s2 = half_sum(fmul(x2, x2));
        u32x4 o;
#pragma unroll
        for (int e = 0; e < 4; ++e) {
          const unsigned sp = lane < 52 ? (e == 0 ? __float_as_uint(s1) : __float_as_uint(s2)) : xf[e];
          const unsigned x = __float_as_uint(tblk ? z[4 + e][0] : z[e][0]), y = tblk ? sp : __float_as_uint(z[8 + e][0]);
          o[e] = nz(hi ? y : x);
        }
        const unsigned pos = lane < 24 ? lane * 16 : lane < 48 ? lane * 16 + 128 : lane < 52 ? (lane & 1) * 512 + 384 : 400 + tp * 16;
        stg(rs, lane < 56 ? OFF_SX + SX_QP + ((2 * cg + a) * 32 + kr) * 1024 + pos : OOB, o);
      }
      STAMPV(5 + 16 * L);
      if (owner) {
        __builtin_amdgcn_s_sleep(13);
        const int c25 = r32 < 25 ? r32 : 24, pl = 24 * a + c24, col = 64 * (pl >> 4) + (pl & 15);
        float cs[4];
#pragma unroll
        for (int e = 0; e < 4; ++e) { cs[e] = reinterpret_cast<const float*>(smem + L_CS)[col + 16 * e]; PIN(cs[e]); }
        u32x4 qv[17];
        poll_all<17>(qv, true, [&](u32x4 (&t)[17]) {
#pragma unroll
          for (int i = 0; i < 16; ++i) t[i] = ldg(rs, OFF_SX + SX_QP + (tn * 32 + 2 * i + hi) * 1024 + a * 512 + c25 * 16);
          t[16] = ldg(rs, OFF_SX + SX_QP + (tn * 34 + hi) * 1024 + 400 + tp * 16);
        }, [&](const u32x4 (&t)[17]) { return none_sent(t); });
        *reinterpret_cast<u32x4*>(smem + L_X2P + hi * 64 + tp * 16) = qv[16];
        f32x4 t = sum4<16>(qv);
#pragma unroll
        for (int e = 0; e < 4; ++e) t[e] = half_swap_sum(t[e]);
        const float mean = fmul(U2F(__builtin_amdgcn_readlane(__float_as_uint(t[0]), 24)), 1.0f / 1024.0f);
        const float var = fmaxf(ffma(-mean, mean, fmul(U2F(__builtin_amdgcn_readlane(__float_as_uint(t[1]), 24)), 1.0f / 1024.0f)), 0.f);
        float mb, den;
        ln_scalars(mean, var, mb, den);
        const float rd = RCP(den);
        if (lane < 24) {
#pragma unroll
          for (int e = 0; e < 4; ++e) reinterpret_cast<float*>(smem + L_PATCH)[col + 16 * e] = fmul(ffma(-mb, cs[e], t[e]), rd);
        }
        lds_set(wa + 4 + 4 * a, L + 1);
      }
      STAMPV(6 + 16 * L);
      }
      BAR_ALL();
      unsigned dv = fresh_lane();
      dv = (dv >> 4) * (8 * D_ * 2) + (dv & 15) * 16;
      PIN(dv);
      wait_go(1);
      load_wd(dv);
      STAMPV(23);
      lds_wait_ge<4>(wa + 80 + 4 * (fresh_lane() & 3), 4);
      transpose8(wdf); transpose8(wdf + 8);
      BARRIER();
    }
  }
  const int tm_ = tm, tn_ = tn, wid_ = wid;
#define KSKIP "{s49}"(tn * 4096), "{s50}"(((tn + 1) & 15) * 4096)
  const u32x4 srd = {(unsigned)(uintptr_t)ws, (unsigned)((uintptr_t)ws >> 32) & 0xffffu, WS_BYTES, 0x00020000u};
  u32x4 x1p[4];
  asm volatile("" : "=v"(x1p[0]), "=v"(x1p[1]), "=v"(x1p[2]), "=v"(x1p[3]));
#pragma unroll 1
  for (int L = 0; L < 2; ++L) {
    const unsigned sx = OFF_SX + L * SX_LAYER;
    u32x8 q[4];
    u32x16 kva, kvb;
    f32x16 oz[5];
    {
      f32x16 acc[6];
      u32x16 stA, stB, xr0, xr1, xr2, xr3;
      u32x4 stC;
      asm volatile("" : "={v[112:127]}"(stB), "={v[128:131]}"(stC));
      { PHASE_VARS;
      stB[8] = lane * 16 + (L ? OFF_X1 : OFF_HF) + tile * TS;
      stB[9] = lbase + wrow * 384 + (((4 * hh + wch) ^ wsw) << 4) + 8 * (tp & 1);
      stB[10] = lbase + wrow * 384 + (((4 * (hh ^ 1) + wch) ^ wsw) << 4) + 8 * (tp & 1);
      stB[13] = lbase + L_FULL + (lane & 1) * 256;
      stB[15] = lbase + L_FREE + wid * 256 + lane * 4;
      stC[0] = 32 * L; }
      unsigned soff, slot, need = 32 * L + 1;
      if (L == 0) {
        lds_wait_ge<0>(lbase + L_W + 24, 1);
        STAMPV(39);
        soff = tn * 4096;
        asm volatile(
          "buffer_load_dwordx4 v[132:135], v120, s[40:43], s44 offen offset:0 sc1\n\t"
          "buffer_load_dwordx4 v[136:139], v120, s[40:43], s44 offen offset:1024 sc1\n\t"
          "buffer_load_dwordx4 v[140:143], v120, s[40:43], s44 offen offset:2048 sc1\n\t"
          "buffer_load_dwordx4 v[144:147], v120, s[40:43], s44 offen offset:3072 sc1\n\t"
          "s_add_u32 s44, s51, 0\n\t"
          "buffer_load_dwordx4 v[148:151], v120, s[40:43], s44 offen offset:0 sc1\n\t"
          "buffer_load_dwordx4 v[152:155], v120, s[40:43], s44 offen offset:1024 sc1\n\t"
          "buffer_load_dwordx4 v[156:159], v120, s[40:43], s44 offen offset:2048 sc1\n\t"
          "buffer_load_dwordx4 v[160:163], v120, s[40:43], s44 offen offset:3072 sc1\n\t"
          "s_add_u32 s44, s44, 0x1000\n\t"
          "s_and_b32 s44, s44, 0xffff\n\t"
          "s_cmp_eq_u32 s44, s49\n\t"
          "s_cselect_b32 s44, s50, s44\n\t"
          "buffer_load_dwordx4 v[164:167], v120, s[40:43], s44 offen offset:0 sc1\n\t"
          "buffer_load_dwordx4 v[168:171], v120, s[40:43], s44 offen offset:1024 sc1\n\t"
          "buffer_load_dwordx4 v[172:175], v120, s[40:43], s44 offen offset:2048 sc1\n\t"
          "buffer_load_dwordx4 v[176:179], v120, s[40:43], s44 offen offset:3072 sc1\n\t"
          "s_add_u32 s44, s44, 0x1000\n\t"
          "s_and_b32 s44, s44, 0xffff\n\t"
          "s_cmp_eq_u32 s44, s49\n\t"
          "s_cselect_b32 s44, s50, s44\n\t"
          "buffer_load_dwordx4 v[180:183], v120, s[40:43], s44 offen offset:0 sc1\n\t"
          "buffer_load_dwordx4 v[184:187], v120, s[40:43], s44 offen offset:1024 sc1\n\t"
          "buffer_load_dwordx4 v[188:191], v120, s[40:43], s44 offen offset:2048 sc1\n\t"
          "buffer_load_dwordx4 v[192:195], v120, s[40:43], s44 offen offset:3072 sc1\n\t"
          : "={v[132:147]}"(xr0), "={v[148:163]}"(xr1), "={v[164:179]}"(xr2), "={v[180:195]}"(xr3), "+{s44}"(soff) : "{v[112:127]}"(stB), "{s[40:43]}"(srd), KSKIP, "{s51}"(kpos(tm, tn, 1) * 4096) : "scc", "memory");
      } else {
        xr0 = cat4(x1p[0], x1p[1], x1p[2], x1p[3]);
        asm volatile("" : "={v[148:163]}"(xr1), "={v[164:179]}"(xr2), "={v[180:195]}"(xr3));
      }
      STAMPV(1 + 16 * L);
      asm volatile(
        "v_mov_b32 v123, v121\n\t"
        "v_mov_b32 v124, v122\n\t"
        "s_mov_b64 vcc, -1\n\t"
        "s_cbranch_vccnz 10f\n\t"
        "11:\n\t"
        "s_mov_b32 s46, 12288\n\t"
        "s_add_u32 s47, s47, 1\n\t"
        "ds_read_b64_tr_b16 v[96:97], v123 offset:0\n\t"
        "ds_read_b64_tr_b16 v[98:99], v123 offset:3072\n\t"
        "ds_read_b64_tr_b16 v[100:101], v124 offset:0\n\t"
        "ds_read_b64_tr_b16 v[102:103], v124 offset:3072\n\t"
        "ds_read_b64_tr_b16 v[104:105], v123 offset:128\n\t"
        "ds_read_b64_tr_b16 v[106:107], v123 offset:3200\n\t"
        "ds_read_b64_tr_b16 v[108:109], v124 offset:128\n\t"
        "ds_read_b64_tr_b16 v[110:111], v124 offset:3200\n\t"
        "ds_read_b64_tr_b16 v[112:113], v123 offset:256\n\t"
        "ds_read_b64_tr_b16 v[114:115], v123 offset:3328\n\t"
        "ds_write_b32 v127, v128\n\t"
        "ds_read_b64_tr_b16 v[116:117], v124 offset:256\n\t"
        "ds_read_b64_tr_b16 v[118:119], v124 offset:3328\n\t"
        "s_waitcnt vmcnt(15) lgkmcnt(11)\n\t"
        "v_mfma_f32_32x32x16_bf16 v[0:15], v[96:99], v[132:135], 0\n\t"
        "ds_read_b32 v126, v125\n\t"
        "ds_read_b64_tr_b16 v[96:97], v123 offset:6144\n\t"
        "ds_read_b64_tr_b16 v[98:99], v123 offset:9216\n\t"
        "s_waitcnt lgkmcnt(12)\n\t"
        "v_mfma_f32_32x32x16_bf16 v[16:31], v[100:103], v[132:135], 0\n\t"
        "v_add_u32 v129, s46, v121\n\t"
        "ds_read_b64_tr_b16 v[100:101], v124 offset:6144\n\t"
        "ds_read_b64_tr_b16 v[102:103], v124 offset:9216\n\t"
        "s_waitcnt lgkmcnt(12)\n\t"
        "v_mfma_f32_32x32x16_bf16 v[32:47], v[104:107], v[132:135], 0\n\t"
        "ds_read_b64_tr_b16 v[104:105], v123 offset:6272\n\t"
        "ds_read_b64_tr_b16 v[106:107], v123 offset:9344\n\t"
        "s_waitcnt lgkmcnt(12)\n\t"
        "v_mfma_f32_32x32x16_bf16 v[48:63], v[108:111], v[132:135], 0\n\t"
        "v_add_u32 v130, s46, v122\n\t"
        "ds_read_b64_tr_b16 v[108:109], v124 offset:6272\n\t"
        "ds_read_b64_tr_b16 v[110:111], v124 offset:9344\n\t"
        "s_waitcnt lgkmcnt(12)\n\t"
        "v_mfma_f32_32x32x16_bf16 v[64:79], v[132:135], v[112:115], 0\n\t"
        "ds_read_b64_tr_b16 v[112:113], v123 offset:6400\n\t"
        "ds_read_b64_tr_b16 v[114:115], v123 offset:9472\n\t"
        "s_waitcnt lgkmcnt(10)\n\t"
        "v_mfma_f32_32x32x16_bf16 v[80:95], v[132:135], v[116:119], 0\n\t"
        "v_cmp_gt_u32 vcc, s47, v126\n\t"
        "ds_read_b64_tr_b16 v[116:117], v124 offset:6400\n\t"
        "ds_read_b64_tr_b16 v[118:119], v124 offset:9472\n\t"
        "s_waitcnt vmcnt(14) lgkmcnt(10)\n\t"
        "v_mfma_f32_32x32x16_bf16 v[0:15], v[96:99], v[136:139], v[0:15]\n\t"
        "s_cbranch_vccnz 12f\n\t"
        "13:\n\t"
        "ds_read_b64_tr_b16 v[96:97], v129 offset:0\n\t"
        "ds_read_b64_tr_b16 v[98:99], v129 offset:3072\n\t"
        "s_waitcnt lgkmcnt(10)\n\t"
        "v_mfma_f32_32x32x16_bf16 v[16:31], v[100:103], v[136:139], v[16:31]\n\t"
        "v_add_u32 v128, 1, v128\n\t"
        "ds_read_b64_tr_b16 v[100:101], v130 offset:0\n\t"
        "ds_read_b64_tr_b16 v[102:103], v130 offset:3072\n\t"
        "s_waitcnt lgkmcnt(10)\n\t"
        "v_mfma_f32_32x32x16_bf16 v[32:47], v[104:107], v[136:139], v[32:47]\n\t"
        "ds_read_b64_tr_b16 v[104:105], v129 offset:128\n\t"
        "ds_read_b64_tr_b16 v[106:107], v129 offset:3200\n\t"
        "s_waitcnt lgkmcnt(10)\n\t"
        "v_mfma_f32_32x32x16_bf16 v[48:63], v[108:111], v[136:139], v[48:63]\n\t"
        "s_add_u32 s46, s46, 12288\n\t"
        "s_add_u32 s47, s47, 1\n\t"
        "ds_read_b64_tr_b16 v[108:109], v130 offset:128\n\t"
        "ds_read_b64_tr_b16 v[110:111], v130 offset:3200\n\t"
        "s_waitcnt lgkmcnt(10)\n\t"
        "v_mfma_f32_32x32x16_bf16 v[64:79], v[136:139], v[112:115], v[64:79]\n\t"
        "s_cmp_eq_u32 s46, 110592\n\t"
        "s_cselect_b32 s46, 0, s46\n\t"
        "ds_read_b64_tr_b16 v[112:113], v129 offset:256\n\t"
        "ds_read_b64_tr_b16 v[114:115], v129 offset:3328\n\t"
        "s_waitcnt lgkmcnt(10)\n\t"
        "v_mfma_f32_32x32x16_bf16 v[80:95], v[136:139], v[116:119], v[80:95]\n\t"
        "ds_write_b32 v127, v128\n\t"
        "ds_read_b64_tr_b16 v[116:117], v130 offset:256\n\t"
        "ds_read_b64_tr_b16 v[118:119], v130 offset:3328\n\t"
        "s_waitcnt vmcnt(13) lgkmcnt(11)\n\t"
        "v_mfma_f32_32x32x16_bf16 v[0:15], v[96:99], v[140:143], v[0:15]\n\t"
        "ds_read_b32 v126, v125\n\t"
        "ds_read_b64_tr_b16 v[96:97], v129 offset:6144\n\t"
        "ds_read_b64_tr_b16 v[98:99], v129 offset:9216\n\t"
        "s_waitcnt lgkmcnt(12)\n\t"
        "v_mfma_f32_32x32x16_bf16 v[16:31], v[100:103], v[140:143], v[16:31]\n\t"
        "v_add_u32 v123, s46, v121\n\t"
        "ds_read_b64_tr_b16 v[100:101], v130 offset:6144\n\t"
        "ds_read_b64_tr_b16 v[102:103], v130 offset:9216\n\t"
        "s_waitcnt lgkmcnt(12)\n\t"
        "v_mfma_f32_32x32x16_bf16 v[32:47], v[104:107], v[140:143], v[32:47]\n\t"
        "ds_read_b64_tr_b16 v[104:105], v129 offset:6272\n\t"
        "ds_read_b64_tr_b16 v[106:107], v129 offset:9344\n\t"
        "s_waitcnt lgkmcnt(12)\n\t"
        "v_mfma_f32_32x32x16_bf16 v[48:63], v[108:111], v[140:143], v[48:63]\n\t"
        "v_add_u32 v124, s46, v122\n\t"
        "ds_read_b64_tr_b16 v[108:109], v130 offset:6272\n\t"
        "ds_read_b64_tr_b16 v[110:111], v130 offset:9344\n\t"
        "s_waitcnt lgkmcnt(12)\n\t"
        "v_mfma_f32_32x32x16_bf16 v[64:79], v[140:143], v[112:115], v[64:79]\n\t"
        "ds_read_b64_tr_b16 v[112:113], v129 offset:6400\n\t"
        "ds_read_b64_tr_b16 v[114:115], v129 offset:9472\n\t"
        "s_waitcnt lgkmcnt(10)\n\t"
        "v_mfma_f32_32x32x16_bf16 v[80:95], v[140:143], v[116:119], v[80:95]\n\t"
        "v_cmp_gt_u32 vcc, s47, v126\n\t"
        "ds_read_b64_tr_b16 v[116:117], v130 offset:6400\n\t"
        "ds_read_b64_tr_b16 v[118:119], v130 offset:9472\n\t"
        "s_waitcnt vmcnt(12) lgkmcnt(10)\n\t"
        "v_mfma_f32_32x32x16_bf16 v[0:15], v[96:99], v[144:147], v[0:15]\n\t"
        "s_cbranch_vccnz 14f\n\t"
        "15:\n\t"
        "ds_read_b64_tr_b16 v[96:97], v123 offset:0\n\t"
        "ds_read_b64_tr_b16 v[98:99], v123 offset:3072\n\t"
        "s_waitcnt lgkmcnt(10)\n\t"
        "v_mfma_f32_32x32x16_bf16 v[16:31], v[100:103], v[144:147], v[16:31]\n\t"
        "v_add_u32 v128, 1, v128\n\t"
        "ds_read_b64_tr_b16 v[100:101], v124 offset:0\n\t"
        "ds_read_b64_tr_b16 v[102:103], v124 offset:3072\n\t"
        "s_waitcnt lgkmcnt(10)\n\t"
        "v_mfma_f32_32x32x16_bf16 v[32:47], v[104:107], v[144:147], v[32:47]\n\t"
        "ds_read_b64_tr_b16 v[104:105], v123 offset:128\n\t"
        "ds_read_b64_tr_b16 v[106:107], v123 offset:3200\n\t"
        "s_waitcnt lgkmcnt(10)\n\t"
        "v_mfma_f32_32x32x16_bf16 v[48:63], v[108:111], v[144:147], v[48:63]\n\t"
        "s_add_u32 s46, s46, 12288\n\t"
        "s_add_u32 s47, s47, 1\n\t"
        "ds_read_b64_tr_b16 v[108:109], v124 offset:128\n\t"
        "ds_read_b64_tr_b16 v[110:111], v124 offset:3200\n\t"
        "s_waitcnt lgkmcnt(10)\n\t"
        "v_mfma_f32_32x32x16_bf16 v[64:79], v[144:147], v[112:115], v[64:79]\n\t"
        "s_cmp_eq_u32 s46, 110592\n\t"
        "s_cselect_b32 s46, 0, s46\n\t"
        "ds_read_b64_tr_b16 v[112:113], v123 offset:256\n\t"
        "ds_read_b64_tr_b16 v[114:115], v123 offset:3328\n\t"
        "s_waitcnt lgkmcnt(10)\n\t"
        "v_mfma_f32_32x32x16_bf16 v[80:95], v[144:147], v[116:119], v[80:95]\n\t"
        "ds_write_b32 v127, v128\n\t"
        "ds_read_b64_tr_b16 v[116:117], v124 offset:256\n\t"
        "ds_read_b64_tr_b16 v[118:119], v124 offset:3328\n\t"
        "s_waitcnt lgkmcnt(0)\n\t"
        "s_nop 7\n\t"
        "s_branch 9f\n\t"
        "10:\n\t"
        "s_mov_b32 s48, 0x10000\n\t"
        "8:\n\t"
        "ds_read_b32 v126, v125\n\t"
        "s_sub_u32 s48, s48, 1\n\t"
        "s_waitcnt lgkmcnt(0)\n\t"
        "s_cbranch_scc1 11b\n\t"
        "v_cmp_gt_u32 vcc, s47, v126\n\t"
        "s_cbranch_vccnz 8b\n\t"
        "s_branch 11b\n\t"
        "12:\n\t"
        "s_mov_b32 s48, 0x10000\n\t"
        "8:\n\t"
        "ds_read_b32 v126, v125\n\t"
        "s_sub_u32 s48, s48, 1\n\t"
        "s_waitcnt lgkmcnt(0)\n\t"
        "s_cbranch_scc1 13b\n\t"
        "v_cmp_gt_u32 vcc, s47, v126\n\t"
        "s_cbranch_vccnz 8b\n\t"
        "s_branch 13b\n\t"
        "14:\n\t"
        "s_mov_b32 s48, 0x10000\n\t"
        "8:\n\t"
        "ds_read_b32 v126, v125\n\t"
        "s_sub_u32 s48, s48, 1\n\t"
        "s_waitcnt lgkmcnt(0)\n\t"
        "s_cbranch_scc1 15b\n\t"
        "v_cmp_gt_u32 vcc, s47, v126\n\t"
        "s_cbranch_vccnz 8b\n\t"
        "s_branch 15b\n\t"
        "9:\n\t"
        : "={v[0:15]}"(acc[0]), "={v[16:31]}"(acc[1]), "={v[32:47]}"(acc[2]), "={v[48:63]}"(acc[3]), "={v[64:79]}"(acc[4]), "={v[80:95]}"(acc[5]),
          "={v[96:111]}"(stA), "+{v[112:127]}"(stB), "+{v[128:131]}"(stC), "={s46}"(slot), "+{s47}"(need)
        : "{v[132:147]}"(xr0) : "s48", "vcc", "scc", "memory");
      STAMPV(2 + 16 * L);
      float al = 1.f;
      if (L == 1) {
        PHASE_VARS;
        float S, Q, Dd;
        row_stats_sum(rs, OFF_SX, tile, lane, S, Q, Dd);
        STAMPV(36);
        lds_wait_ge<0>(lbase + L_W + 24, 2);
        soff = kpos(tm, tn, 1) * 4096;
        asm volatile(
          "buffer_load_dwordx4 v[148:151], v120, s[40:43], s44 offen offset:0 sc1\n\t"
          "buffer_load_dwordx4 v[152:155], v120, s[40:43], s44 offen offset:1024 sc1\n\t"
          "buffer_load_dwordx4 v[156:159], v120, s[40:43], s44 offen offset:2048 sc1\n\t"
          "buffer_load_dwordx4 v[160:163], v120, s[40:43], s44 offen offset:3072 sc1\n\t"
          : "={v[148:163]}"(xr1), "={v[164:179]}"(xr2), "={v[180:195]}"(xr3), "+{s44}"(soff), "+v"(S), "+v"(Q), "+v"(Dd) : "{v[112:127]}"(stB), "{s[40:43]}"(srd), KSKIP, "{s51}"(kpos(tm, tn, 1) * 4096) : "scc", "memory");
        float mean, var, mb1, rd1, mb2, den2;
        row_stats_fin(rs, OFF_SX + SX_CAND + (tn * 16 + tm) * 128 + wid * 4, smem, 1, tile, lane, S, Q, Dd, U2F(words[0]), x1p, mean, var, mb1, rd1);
        ln_scalars(mean + (mean - mb1) * rd1, var * (1.0f + rd1) * (1.0f + rd1), mb2, den2);
        const float rd2 = RCP(den2);
        al = (1.0f + rd1) * rd2;
        const float be = -(mb1 * rd1 + mb2) * RCP(1.0f + rd1);
        reinterpret_cast<float*>(smem + L_AB)[wid * 64 + lane] = hi ? be : al;
        reinterpret_cast<float*>(smem + L_AB)[256 + wid * 64 + lane] = hi ? 1.0f + rd1 : -mb1 * rd1;
        soff = kpos(tm, tn, 1 + 0) * 4096;
        asm volatile(
          "s_nop 0\n\t"
          "s_add_u32 s44, s44, 0x1000\n\t"
          "s_and_b32 s44, s44, 0xffff\n\t"
          "s_cmp_eq_u32 s44, s49\n\t"
          "s_cselect_b32 s44, s50, s44\n\t"
          "buffer_load_dwordx4 v[164:167], v120, s[40:43], s44 offen offset:0 sc1\n\t"
          "buffer_load_dwordx4 v[168:171], v120, s[40:43], s44 offen offset:1024 sc1\n\t"
          "buffer_load_dwordx4 v[172:175], v120, s[40:43], s44 offen offset:2048 sc1\n\t"
          "buffer_load_dwordx4 v[176:179], v120, s[40:43], s44 offen offset:3072 sc1\n\t"
          "s_add_u32 s44, s44, 0x1000\n\t"
          "s_and_b32 s44, s44, 0xffff\n\t"
          "s_cmp_eq_u32 s44, s49\n\t"
          "s_cselect_b32 s44, s50, s44\n\t"
          "buffer_load_dwordx4 v[180:183], v120, s[40:43], s44 offen offset:0 sc1\n\t"
          "buffer_load_dwordx4 v[184:187], v120, s[40:43], s44 offen offset:1024 sc1\n\t"
          "buffer_load_dwordx4 v[188:191], v120, s[40:43], s44 offen offset:2048 sc1\n\t"
          "buffer_load_dwordx4 v[192:195], v120, s[40:43], s44 offen offset:3072 sc1\n\t"
          : "+{v[148:163]}"(xr1), "+{v[164:179]}"(xr2), "+{v[180:195]}"(xr3), "+{s44}"(soff) : "{v[112:127]}"(stB), "{s[40:43]}"(srd), KSKIP, "{s51}"(kpos(tm, tn, 1) * 4096) : "scc", "memory");
#pragma unroll
        for (int jj = 0; jj < 3; ++jj) {
          f32x4 cs[2][4];
          float cv[2];
#pragma unroll
          for (int j = 0; j < 2; ++j) {
            const int cb = jj * 64 + (j ^ hh) * 32;
            if (jj == 2) cv[j] = *reinterpret_cast<const float*>(smem + L_CS + (cb + r32) * 4);
#pragma unroll
            for (int g = 0; g < 4; ++g)
              if (jj < 2 || j == 0) cs[j][g] = *reinterpret_cast<const f32x4*>(smem + (jj < 2 ? L_CS + cb * 4 : L_AB + wid * 256 + 128) + (8 * g + 4 * hi) * 4);
          }
          SBAR();
#pragma unroll
          for (int j = 0; j < 2; ++j)
#pragma unroll
            for (int e = 0; e < 16; ++e) acc[2 * jj + j][e] += jj < 2 ? be * cs[j][e >> 2][e & 3] : cs[0][e >> 2][e & 3] * cv[j];
          SBAR();
        }
      }
      u32x4 ep;
      { PHASE_VARS;
        if (L == 0) reinterpret_cast<float*>(smem + L_AB)[wid * 64 + lane] = 1.0f;
        const unsigned dst = OFF_KV + L * (10 * MBY) + tn * 2 * HS + tile * 4096 + lane * 16;
        ep = u32x4{__float_as_uint(al), dst + hh * HS, dst + (hh ^ 1) * HS, lbase + L_AB + wid * 256 + hi * 16}; }
      STAMPV(3 + 16 * L);
      soff = kpos(tm, tn, 1 + 2) * 4096;
      asm volatile(
        "s_nop 0\n\t"
        "s_add_u32 s44, s44, 0x1000\n\t"
        "s_and_b32 s44, s44, 0xffff\n\t"
        "s_cmp_eq_u32 s44, s49\n\t"
        "s_cselect_b32 s44, s50, s44\n\t"
        "buffer_load_dwordx4 v[132:135], v120, s[40:43], s44 offen offset:0 sc1\n\t"
        "buffer_load_dwordx4 v[136:139], v120, s[40:43], s44 offen offset:1024 sc1\n\t"
        "buffer_load_dwordx4 v[140:143], v120, s[40:43], s44 offen offset:2048 sc1\n\t"
        "s_mov_b32 s45, 3\n\t"
        "1:\n\t"
        "s_waitcnt vmcnt(14) lgkmcnt(11)\n\t"
        "v_mfma_f32_32x32x16_bf16 v[0:15], v[96:99], v[148:151], v[0:15]\n\t"
        "ds_read_b32 v126, v125\n\t"
        "ds_read_b64_tr_b16 v[96:97], v123 offset:6144\n\t"
        "ds_read_b64_tr_b16 v[98:99], v123 offset:9216\n\t"
        "s_waitcnt lgkmcnt(12)\n\t"
        "v_mfma_f32_32x32x16_bf16 v[16:31], v[100:103], v[148:151], v[16:31]\n\t"
        "v_add_u32 v129, s46, v121\n\t"
        "ds_read_b64_tr_b16 v[100:101], v124 offset:6144\n\t"
        "ds_read_b64_tr_b16 v[102:103], v124 offset:9216\n\t"
        "s_waitcnt lgkmcnt(12)\n\t"
        "v_mfma_f32_32x32x16_bf16 v[32:47], v[104:107], v[148:151], v[32:47]\n\t"
        "ds_read_b64_tr_b16 v[104:105], v123 offset:6272\n\t"
        "ds_read_b64_tr_b16 v[106:107], v123 offset:9344\n\t"
        "buffer_load_dwordx4 v[144:147], v120, s[40:43], s44 offen offset:3072 sc1\n\t"
        "s_waitcnt lgkmcnt(12)\n\t"
        "v_mfma_f32_32x32x16_bf16 v[48:63], v[108:111], v[148:151], v[48:63]\n\t"
        "v_add_u32 v130, s46, v122\n\t"
        "ds_read_b64_tr_b16 v[108:109], v124 offset:6272\n\t"
        "ds_read_b64_tr_b16 v[110:111], v124 offset:9344\n\t"
        "s_waitcnt lgkmcnt(12)\n\t"
        "v_mfma_f32_32x32x16_bf16 v[64:79], v[148:151], v[112:115], v[64:79]\n\t"
        "ds_read_b64_tr_b16 v[112:113], v123 offset:6400\n\t"
        "ds_read_b64_tr_b16 v[114:115], v123 offset:9472\n\t"
        "s_waitcnt lgkmcnt(10)\n\t"
        "v_mfma_f32_32x32x16_bf16 v[80:95], v[148:151], v[116:119], v[80:95]\n\t"
        "v_cmp_gt_u32 vcc, s47, v126\n\t"
        "ds_read_b64_tr_b16 v[116:117], v124 offset:6400\n\t"
        "ds_read_b64_tr_b16 v[118:119], v124 offset:9472\n\t"
        "s_waitcnt vmcnt(14) lgkmcnt(10)\n\t"
        "v_mfma_f32_32x32x16_bf16 v[0:15], v[96:99], v[152:155], v[0:15]\n\t"
        "s_cbranch_vccnz 16f\n\t"
        "17:\n\t"
        "ds_read_b64_tr_b16 v[96:97], v129 offset:0\n\t"
        "ds_read_b64_tr_b16 v[98:99], v129 offset:3072\n\t"
        "s_waitcnt lgkmcnt(10)\n\t"
        "v_mfma_f32_32x32x16_bf16 v[16:31], v[100:103], v[152:155], v[16:31]\n\t"
        "v_add_u32 v128, 1, v128\n\t"
        "ds_read_b64_tr_b16 v[100:101], v130 offset:0\n\t"
        "ds_read_b64_tr_b16 v[102:103], v130 offset:3072\n\t"
        "s_add_u32 s44, s44, 0x1000\n\t"
        "s_and_b32 s44, s44, 0xffff\n\t"
        "s_waitcnt lgkmcnt(10)\n\t"
        "v_mfma_f32_32x32x16_bf16 v[32:47], v[104:107], v[152:155], v[32:47]\n\t"
        "ds_read_b64_tr_b16 v[104:105], v129 offset:128\n\t"
        "ds_read_b64_tr_b16 v[106:107], v129 offset:3200\n\t"
        "s_cmp_eq_u32 s44, s49\n\t"
        "s_cselect_b32 s44, s50, s44\n\t"
        "buffer_load_dwordx4 v[148:151], v120, s[40:43], s44 offen offset:0 sc1\n\t"
        "s_waitcnt lgkmcnt(10)\n\t"
        "v_mfma_f32_32x32x16_bf16 v[48:63], v[108:111], v[152:155], v[48:63]\n\t"
        "s_add_u32 s46, s46, 12288\n\t"
        "s_add_u32 s47, s47, 1\n\t"
        "ds_read_b64_tr_b16 v[108:109], v130 offset:128\n\t"
        "ds_read_b64_tr_b16 v[110:111], v130 offset:3200\n\t"
        "s_waitcnt lgkmcnt(10)\n\t"
        "v_mfma_f32_32x32x16_bf16 v[64:79], v[152:155], v[112:115], v[64:79]\n\t"
        "s_cmp_eq_u32 s46, 110592\n\t"
        "s_cselect_b32 s46, 0, s46\n\t"
        "ds_read_b64_tr_b16 v[112:113], v129 offset:256\n\t"
        "ds_read_b64_tr_b16 v[114:115], v129 offset:3328\n\t"
        "s_waitcnt lgkmcnt(10)\n\t"
        "v_mfma_f32_32x32x16_bf16 v[80:95], v[152:155], v[116:119], v[80:95]\n\t"
        "ds_write_b32 v127, v128\n\t"
        "ds_read_b64_tr_b16 v[116:117], v130 offset:256\n\t"
        "ds_read_b64_tr_b16 v[118:119], v130 offset:3328\n\t"
        "s_waitcnt vmcnt(14) lgkmcnt(11)\n\t"
        "v_mfma_f32_32x32x16_bf16 v[0:15], v[96:99], v[156:159], v[0:15]\n\t"
        "ds_read_b32 v126, v125\n\t"
        "ds_read_b64_tr_b16 v[96:97], v129 offset:6144\n\t"
        "ds_read_b64_tr_b16 v[98:99], v129 offset:9216\n\t"
        "s_waitcnt lgkmcnt(12)\n\t"
        "v_mfma_f32_32x32x16_bf16 v[16:31], v[100:103], v[156:159], v[16:31]\n\t"
        "v_add_u32 v123, s46, v121\n\t"
        "ds_read_b64_tr_b16 v[100:101], v130 offset:6144\n\t"
        "ds_read_b64_tr_b16 v[102:103], v130 offset:9216\n\t"
        "s_waitcnt lgkmcnt(12)\n\t"
        "v_mfma_f32_32x32x16_bf16 v[32:47], v[104:107], v[156:159], v[32:47]\n\t"
        "ds_read_b64_tr_b16 v[104:105], v129 offset:6272\n\t"
        "ds_read_b64_tr_b16 v[106:107], v129 offset:9344\n\t"
        "buffer_load_dwordx4 v[152:155], v120, s[40:43], s44 offen offset:1024 sc1\n\t"
        "s_waitcnt lgkmcnt(12)\n\t"
        "v_mfma_f32_32x32x16_bf16 v[48:63], v[108:111], v[156:159], v[48:63]\n\t"
        "v_add_u32 v124, s46, v122\n\t"
        "ds_read_b64_tr_b16 v[108:109], v130 offset:6272\n\t"
        "ds_read_b64_tr_b16 v[110:111], v130 offset:9344\n\t"
        "s_waitcnt lgkmcnt(12)\n\t"
        "v_mfma_f32_32x32x16_bf16 v[64:79], v[156:159], v[112:115], v[64:79]\n\t"
        "ds_read_b64_tr_b16 v[112:113], v129 offset:6400\n\t"
        "ds_read_b64_tr_b16 v[114:115], v129 offset:9472\n\t"
        "s_waitcnt lgkmcnt(10)\n\t"
        "v_mfma_f32_32x32x16_bf16 v[80:95], v[156:159], v[116:119], v[80:95]\n\t"
        "v_cmp_gt_u32 vcc, s47, v126\n\t"
        "ds_read_b64_tr_b16 v[116:117], v130 offset:6400\n\t"
        "ds_read_b64_tr_b16 v[118:119], v130 offset:9472\n\t"
        "s_waitcnt vmcnt(14) lgkmcnt(10)\n\t"
        "v_mfma_f32_32x32x16_bf16 v[0:15], v[96:99], v[160:163], v[0:15]\n\t"
        "s_cbranch_vccnz 18f\n\t"
        "19:\n\t"
        "ds_read_b64_tr_b16 v[96:97], v123 offset:0\n\t"
        "ds_read_b64_tr_b16 v[98:99], v123 offset:3072\n\t"
        "s_waitcnt lgkmcnt(10)\n\t"
        "v_mfma_f32_32x32x16_bf16 v[16:31], v[100:103], v[160:163], v[16:31]\n\t"
        "v_add_u32 v128, 1, v128\n\t"
        "ds_read_b64_tr_b16 v[100:101], v124 offset:0\n\t"
        "ds_read_b64_tr_b16 v[102:103], v124 offset:3072\n\t"
        "s_waitcnt lgkmcnt(10)\n\t"
        "v_mfma_f32_32x32x16_bf16 v[32:47], v[104:107], v[160:163], v[32:47]\n\t"
        "ds_read_b64_tr_b16 v[104:105], v123 offset:128\n\t"
        "ds_read_b64_tr_b16 v[106:107], v123 offset:3200\n\t"
        "buffer_load_dwordx4 v[156:159], v120, s[40:43], s44 offen offset:2048 sc1\n\t"
        "s_waitcnt lgkmcnt(10)\n\t"
        "v_mfma_f32_32x32x16_bf16 v[48:63], v[108:111], v[160:163], v[48:63]\n\t"
        "s_add_u32 s46, s46, 12288\n\t"
        "s_add_u32 s47, s47, 1\n\t"
        "ds_read_b64_tr_b16 v[108:109], v124 offset:128\n\t"
        "ds_read_b64_tr_b16 v[110:111], v124 offset:3200\n\t"
        "s_waitcnt lgkmcnt(10)\n\t"
        "v_mfma_f32_32x32x16_bf16 v[64:79], v[160:163], v[112:115], v[64:79]\n\t"
        "s_cmp_eq_u32 s46, 110592\n\t"
        "s_cselect_b32 s46, 0, s46\n\t"
        "ds_read_b64_tr_b16 v[112:113], v123 offset:256\n\t"
        "ds_read_b64_tr_b16 v[114:115], v123 offset:3328\n\t"
        "s_waitcnt lgkmcnt(10)\n\t"
        "v_mfma_f32_32x32x16_bf16 v[80:95], v[160:163], v[116:119], v[80:95]\n\t"
        "ds_write_b32 v127, v128\n\t"
        "ds_read_b64_tr_b16 v[116:117], v124 offset:256\n\t"
        "ds_read_b64_tr_b16 v[118:119], v124 offset:3328\n\t"
        "s_waitcnt vmcnt(14) lgkmcnt(11)\n\t"
        "v_mfma_f32_32x32x16_bf16 v[0:15], v[96:99], v[164:167], v[0:15]\n\t"
        "ds_read_b32 v126, v125\n\t"
        "ds_read_b64_tr_b16 v[96:97], v123 offset:6144\n\t"
        "ds_read_b64_tr_b16 v[98:99], v123 offset:9216\n\t"
        "s_waitcnt lgkmcnt(12)\n\t"
        "v_mfma_f32_32x32x16_bf16 v[16:31], v[100:103], v[164:167], v[16:31]\n\t"
        "v_add_u32 v129, s46, v121\n\t"
        "ds_read_b64_tr_b16 v[100:101], v124 offset:6144\n\t"
        "ds_read_b64_tr_b16 v[102:103], v124 offset:9216\n\t"
        "s_waitcnt lgkmcnt(12)\n\t"
        "v_mfma_f32_32x32x16_bf16 v[32:47], v[104:107], v[164:167], v[32:47]\n\t"
        "ds_read_b64_tr_b16 v[104:105], v123 offset:6272\n\t"
        "ds_read_b64_tr_b16 v[106:107], v123 offset:9344\n\t"
        "buffer_load_dwordx4 v[160:163], v120, s[40:43], s44 offen offset:3072 sc1\n\t"
        "s_waitcnt lgkmcnt(12)\n\t"
        "v_mfma_f32_32x32x16_bf16 v[48:63], v[108:111], v[164:167], v[48:63]\n\t"
        "v_add_u32 v130, s46, v122\n\t"
        "ds_read_b64_tr_b16 v[108:109], v124 offset:6272\n\t"
        "ds_read_b64_tr_b16 v[110:111], v124 offset:9344\n\t"
        "s_waitcnt lgkmcnt(12)\n\t"
        "v_mfma_f32_32x32x16_bf16 v[64:79], v[164:167], v[112:115], v[64:79]\n\t"
        "ds_read_b64_tr_b16 v[112:113], v123 offset:6400\n\t"
        "ds_read_b64_tr_b16 v[114:115], v123 offset:9472\n\t"
        "s_waitcnt lgkmcnt(10)\n\t"
        "v_mfma_f32_32x32x16_bf16 v[80:95], v[164:167], v[116:119], v[80:95]\n\t"
        "v_cmp_gt_u32 vcc, s47, v126\n\t"
        "ds_read_b64_tr_b16 v[116:117], v124 offset:6400\n\t"
        "ds_read_b64_tr_b16 v[118:119], v124 offset:9472\n\t"
        "s_waitcnt vmcnt(14) lgkmcnt(10)\n\t"
        "v_mfma_f32_32x32x16_bf16 v[0:15], v[96:99], v[168:171], v[0:15]\n\t"
        "s_cbranch_vccnz 20f\n\t"
        "21:\n\t"
        "ds_read_b64_tr_b16 v[96:97], v129 offset:0\n\t"
        "ds_read_b64_tr_b16 v[98:99], v129 offset:3072\n\t"
        "s_waitcnt lgkmcnt(10)\n\t"
        "v_mfma_f32_32x32x16_bf16 v[16:31], v[100:103], v[168:171], v[16:31]\n\t"
        "v_add_u32 v128, 1, v128\n\t"
        "ds_read_b64_tr_b16 v[100:101], v130 offset:0\n\t"
        "ds_read_b64_tr_b16 v[102:103], v130 offset:3072\n\t"
        "s_add_u32 s44, s44, 0x1000\n\t"
        "s_and_b32 s44, s44, 0xffff\n\t"
        "s_waitcnt lgkmcnt(10)\n\t"
        "v_mfma_f32_32x32x16_bf16 v[32:47], v[104:107], v[168:171], v[32:47]\n\t"
        "ds_read_b64_tr_b16 v[104:105], v129 offset:128\n\t"
        "ds_read_b64_tr_b16 v[106:107], v129 offset:3200\n\t"
        "s_cmp_eq_u32 s44, s49\n\t"
        "s_cselect_b32 s44, s50, s44\n\t"
        "buffer_load_dwordx4 v[164:167], v120, s[40:43], s44 offen offset:0 sc1\n\t"
        "s_waitcnt lgkmcnt(10)\n\t"
        "v_mfma_f32_32x32x16_bf16 v[48:63], v[108:111], v[168:171], v[48:63]\n\t"
        "s_add_u32 s46, s46, 12288\n\t"
        "s_add_u32 s47, s47, 1\n\t"
        "ds_read_b64_tr_b16 v[108:109], v130 offset:128\n\t"
        "ds_read_b64_tr_b16 v[110:111], v130 offset:3200\n\t"
        "s_waitcnt lgkmcnt(10)\n\t"
        "v_mfma_f32_32x32x16_bf16 v[64:79], v[168:171], v[112:115], v[64:79]\n\t"
        "s_cmp_eq_u32 s46, 110592\n\t"
        "s_cselect_b32 s46, 0, s46\n\t"
        "ds_read_b64_tr_b16 v[112:113], v129 offset:256\n\t"
        "ds_read_b64_tr_b16 v[114:115], v129 offset:3328\n\t"
        "s_waitcnt lgkmcnt(10)\n\t"
        "v_mfma_f32_32x32x16_bf16 v[80:95], v[168:171], v[116:119], v[80:95]\n\t"
        "ds_write_b32 v127, v128\n\t"
        "ds_read_b64_tr_b16 v[116:117], v130 offset:256\n\t"
        "ds_read_b64_tr_b16 v[118:119], v130 offset:3328\n\t"
        "s_waitcnt vmcnt(14) lgkmcnt(11)\n\t"
        "v_mfma_f32_32x32x16_bf16 v[0:15], v[96:99], v[172:175], v[0:15]\n\t"
        "ds_read_b32 v126, v125\n\t"
        "ds_read_b64_tr_b16 v[96:97], v129 offset:6144\n\t"
        "ds_read_b64_tr_b16 v[98:99], v129 offset:9216\n\t"
        "s_waitcnt lgkmcnt(12)\n\t"
        "v_mfma_f32_32x32x16_bf16 v[16:31], v[100:103], v[172:175], v[16:31]\n\t"
        "v_add_u32 v123, s46, v121\n\t"
        "ds_read_b64_tr_b16 v[100:101], v130 offset:6144\n\t"
        "ds_read_b64_tr_b16 v[102:103], v130 offset:9216\n\t"
        "s_waitcnt lgkmcnt(12)\n\t"
        "v_mfma_f32_32x32x16_bf16 v[32:47], v[104:107], v[172:175], v[32:47]\n\t"
        "ds_read_b64_tr_b16 v[104:105], v129 offset:6272\n\t"
        "ds_read_b64_tr_b16 v[106:107], v129 offset:9344\n\t"
        "buffer_load_dwordx4 v[168:171], v120, s[40:43], s44 offen offset:1024 sc1\n\t"
        "s_waitcnt lgkmcnt(12)\n\t"
        "v_mfma_f32_32x32x16_bf16 v[48:63], v[108:111], v[172:175], v[48:63]\n\t"
        "v_add_u32 v124, s46, v122\n\t"
        "ds_read_b64_tr_b16 v[108:109], v130 offset:6272\n\t"
        "ds_read_b64_tr_b16 v[110:111], v130 offset:9344\n\t"
        "s_waitcnt lgkmcnt(12)\n\t"
        "v_mfma_f32_32x32x16_bf16 v[64:79], v[172:175], v[112:115], v[64:79]\n\t"
        "ds_read_b64_tr_b16 v[112:113], v129 offset:6400\n\t"
        "ds_read_b64_tr_b16 v[114:115], v129 offset:9472\n\t"
        "s_waitcnt lgkmcnt(10)\n\t"
        "v_mfma_f32_32x32x16_bf16 v[80:95], v[172:175], v[116:119], v[80:95]\n\t"
        "v_cmp_gt_u32 vcc, s47, v126\n\t"
        "ds_read_b64_tr_b16 v[116:117], v130 offset:6400\n\t"
        "ds_read_b64_tr_b16 v[118:119], v130 offset:9472\n\t"
        "s_waitcnt vmcnt(14) lgkmcnt(10)\n\t"
        "v_mfma_f32_32x32x16_bf16 v[0:15], v[96:99], v[176:179], v[0:15]\n\t"
        "s_cbranch_vccnz 22f\n\t"
        "23:\n\t"
        "ds_read_b64_tr_b16 v[96:97], v123 offset:0\n\t"
        "ds_read_b64_tr_b16 v[98:99], v123 offset:3072\n\t"
        "s_waitcnt lgkmcnt(10)\n\t"
        "v_mfma_f32_32x32x16_bf16 v[16:31], v[100:103], v[176:179], v[16:31]\n\t"
        "v_add_u32 v128, 1, v128\n\t"
        "ds_read_b64_tr_b16 v[100:101], v124 offset:0\n\t"
        "ds_read_b64_tr_b16 v[102:103], v124 offset:3072\n\t"
        "s_waitcnt lgkmcnt(10)\n\t"
        "v_mfma_f32_32x32x16_bf16 v[32:47], v[104:107], v[176:179], v[32:47]\n\t"
        "ds_read_b64_tr_b16 v[104:105], v123 offset:128\n\t"
        "ds_read_b64_tr_b16 v[106:107], v123 offset:3200\n\t"
        "buffer_load_dwordx4 v[172:175], v120, s[40:43], s44 offen offset:2048 sc1\n\t"
        "s_waitcnt lgkmcnt(10)\n\t"
        "v_mfma_f32_32x32x16_bf16 v[48:63], v[108:111], v[176:179], v[48:63]\n\t"
        "s_add_u32 s46, s46, 12288\n\t"
        "s_add_u32 s47, s47, 1\n\t"
        "ds_read_b64_tr_b16 v[108:109], v124 offset:128\n\t"
        "ds_read_b64_tr_b16 v[110:111], v124 offset:3200\n\t"
        "s_waitcnt lgkmcnt(10)\n\t"
        "v_mfma_f32_32x32x16_bf16 v[64:79], v[176:179], v[112:115], v[64:79]\n\t"
        "s_cmp_eq_u32 s46, 110592\n\t"
        "s_cselect_b32 s46, 0, s46\n\t"
        "ds_read_b64_tr_b16 v[112:113], v123 offset:256\n\t"
        "ds_read_b64_tr_b16 v[114:115], v123 offset:3328\n\t"
        "s_waitcnt lgkmcnt(10)\n\t"
        "v_mfma_f32_32x32x16_bf16 v[80:95], v[176:179], v[116:119], v[80:95]\n\t"
        "ds_write_b32 v127, v128\n\t"
        "ds_read_b64_tr_b16 v[116:117], v124 offset:256\n\t"
        "ds_read_b64_tr_b16 v[118:119], v124 offset:3328\n\t"
        "s_waitcnt vmcnt(14) lgkmcnt(11)\n\t"
        "v_mfma_f32_32x32x16_bf16 v[0:15], v[96:99], v[180:183], v[0:15]\n\t"
        "ds_read_b32 v126, v125\n\t"
        "ds_read_b64_tr_b16 v[96:97], v123 offset:6144\n\t"
        "ds_read_b64_tr_b16 v[98:99], v123 offset:9216\n\t"
        "s_waitcnt lgkmcnt(12)\n\t"
        "v_mfma_f32_32x32x16_bf16 v[16:31], v[100:103], v[180:183], v[16:31]\n\t"
        "v_add_u32 v129, s46, v121\n\t"
        "ds_read_b64_tr_b16 v[100:101], v124 offset:6144\n\t"
        "ds_read_b64_tr_b16 v[102:103], v124 offset:9216\n\t"
        "s_waitcnt lgkmcnt(12)\n\t"
        "v_mfma_f32_32x32x16_bf16 v[32:47], v[104:107], v[180:183], v[32:47]\n\t"
        "ds_read_b64_tr_b16 v[104:105], v123 offset:6272\n\t"
        "ds_read_b64_tr_b16 v[106:107], v123 offset:9344\n\t"
        "buffer_load_dwordx4 v[176:179], v120, s[40:43], s44 offen offset:3072 sc1\n\t"
        "s_waitcnt lgkmcnt(12)\n\t"
        "v_mfma_f32_32x32x16_bf16 v[48:63], v[108:111], v[180:183], v[48:63]\n\t"
        "v_add_u32 v130, s46, v122\n\t"
        "ds_read_b64_tr_b16 v[108:109], v124 offset:6272\n\t"
        "ds_read_b64_tr_b16 v[110:111], v124 offset:9344\n\t"
        "s_waitcnt lgkmcnt(12)\n\t"
        "v_mfma_f32_32x32x16_bf16 v[64:79], v[180:183], v[112:115], v[64:79]\n\t"
        "ds_read_b64_tr_b16 v[112:113], v123 offset:6400\n\t"
        "ds_read_b64_tr_b16 v[114:115], v123 offset:9472\n\t"
        "s_waitcnt lgkmcnt(10)\n\t"
        "v_mfma_f32_32x32x16_bf16 v[80:95], v[180:183], v[116:119], v[80:95]\n\t"
        "v_cmp_gt_u32 vcc, s47, v126\n\t"
        "ds_read_b64_tr_b16 v[116:117], v124 offset:6400\n\t"
        "ds_read_b64_tr_b16 v[118:119], v124 offset:9472\n\t"
        "s_waitcnt vmcnt(14) lgkmcnt(10)\n\t"
        "v_mfma_f32_32x32x16_bf16 v[0:15], v[96:99], v[184:187], v[0:15]\n\t"
        "s_cbranch_vccnz 24f\n\t"
        "25:\n\t"
        "ds_read_b64_tr_b16 v[96:97], v129 offset:0\n\t"
        "ds_read_b64_tr_b16 v[98:99], v129 offset:3072\n\t"
        "s_waitcnt lgkmcnt(10)\n\t"
        "v_mfma_f32_32x32x16_bf16 v[16:31], v[100:103], v[184:187], v[16:31]\n\t"
        "v_add_u32 v128, 1, v128\n\t"
        "ds_read_b64_tr_b16 v[100:101], v130 offset:0\n\t"
        "ds_read_b64_tr_b16 v[102:103], v130 offset:3072\n\t"
        "s_add_u32 s44, s44, 0x1000\n\t"
        "s_and_b32 s44, s44, 0xffff\n\t"
        "s_waitcnt lgkmcnt(10)\n\t"
        "v_mfma_f32_32x32x16_bf16 v[32:47], v[104:107], v[184:187], v[32:47]\n\t"
        "ds_read_b64_tr_b16 v[104:105], v129 offset:128\n\t"
        "ds_read_b64_tr_b16 v[106:107], v129 offset:3200\n\t"
        "s_cmp_eq_u32 s44, s49\n\t"
        "s_cselect_b32 s44, s50, s44\n\t"
        "buffer_load_dwordx4 v[180:183], v120, s[40:43], s44 offen offset:0 sc1\n\t"
        "s_waitcnt lgkmcnt(10)\n\t"
        "v_mfma_f32_32x32x16_bf16 v[48:63], v[108:111], v[184:187], v[48:63]\n\t"
        "s_add_u32 s46, s46, 12288\n\t"
        "s_add_u32 s47, s47, 1\n\t"
        "ds_read_b64_tr_b16 v[108:109], v130 offset:128\n\t"
        "ds_read_b64_tr_b16 v[110:111], v130 offset:3200\n\t"
        "s_waitcnt lgkmcnt(10)\n\t"
        "v_mfma_f32_32x32x16_bf16 v[64:79], v[184:187], v[112:115], v[64:79]\n\t"
        "s_cmp_eq_u32 s46, 110592\n\t"
        "s_cselect_b32 s46, 0, s46\n\t"
        "ds_read_b64_tr_b16 v[112:113], v129 offset:256\n\t"
        "ds_read_b64_tr_b16 v[114:115], v129 offset:3328\n\t"
        "s_waitcnt lgkmcnt(10)\n\t"
        "v_mfma_f32_32x32x16_bf16 v[80:95], v[184:187], v[116:119], v[80:95]\n\t"
        "ds_write_b32 v127, v128\n\t"
        "ds_read_b64_tr_b16 v[116:117], v130 offset:256\n\t"
        "ds_read_b64_tr_b16 v[118:119], v130 offset:3328\n\t"
        "s_waitcnt vmcnt(14) lgkmcnt(11)\n\t"
        "v_mfma_f32_32x32x16_bf16 v[0:15], v[96:99], v[188:191], v[0:15]\n\t"
        "ds_read_b32 v126, v125\n\t"
        "ds_read_b64_tr_b16 v[96:97], v129 offset:6144\n\t"
        "ds_read_b64_tr_b16 v[98:99], v129 offset:9216\n\t"
        "s_waitcnt lgkmcnt(12)\n\t"
        "v_mfma_f32_32x32x16_bf16 v[16:31], v[100:103], v[188:191], v[16:31]\n\t"
        "v_add_u32 v123, s46, v121\n\t"
        "ds_read_b64_tr_b16 v[100:101], v130 offset:6144\n\t"
        "ds_read_b64_tr_b16 v[102:103], v130 offset:9216\n\t"
        "s_waitcnt lgkmcnt(12)\n\t"
        "v_mfma_f32_32x32x16_bf16 v[32:47], v[104:107], v[188:191], v[32:47]\n\t"
        "ds_read_b64_tr_b16 v[104:105], v129 offset:6272\n\t"
        "ds_read_b64_tr_b16 v[106:107], v129 offset:9344\n\t"
        "buffer_load_dwordx4 v[184:187], v120, s[40:43], s44 offen offset:1024 sc1\n\t"
        "s_waitcnt lgkmcnt(12)\n\t"
        "v_mfma_f32_32x32x16_bf16 v[48:63], v[108:111], v[188:191], v[48:63]\n\t"
        "v_add_u32 v124, s46, v122\n\t"
        "ds_read_b64_tr_b16 v[108:109], v130 offset:6272\n\t"
        "ds_read_b64_tr_b16 v[110:111], v130 offset:9344\n\t"
        "s_waitcnt lgkmcnt(12)\n\t"
        "v_mfma_f32_32x32x16_bf16 v[64:79], v[188:191], v[112:115], v[64:79]\n\t"
        "ds_read_b64_tr_b16 v[112:113], v129 offset:6400\n\t"
        "ds_read_b64_tr_b16 v[114:115], v129 offset:9472\n\t"
        "s_waitcnt lgkmcnt(10)\n\t"
        "v_mfma_f32_32x32x16_bf16 v[80:95], v[188:191], v[116:119], v[80:95]\n\t"
        "v_cmp_gt_u32 vcc, s47, v126\n\t"
        "ds_read_b64_tr_b16 v[116:117], v130 offset:6400\n\t"
        "ds_read_b64_tr_b16 v[118:119], v130 offset:9472\n\t"
        "s_waitcnt vmcnt(14) lgkmcnt(10)\n\t"
        "v_mfma_f32_32x32x16_bf16 v[0:15], v[96:99], v[192:195], v[0:15]\n\t"
        "s_cbranch_vccnz 26f\n\t"
        "27:\n\t"
        "ds_read_b64_tr_b16 v[96:97], v123 offset:0\n\t"
        "ds_read_b64_tr_b16 v[98:99], v123 offset:3072\n\t"
        "s_waitcnt lgkmcnt(10)\n\t"
        "v_mfma_f32_32x32x16_bf16 v[16:31], v[100:103], v[192:195], v[16:31]\n\t"
        "v_add_u32 v128, 1, v128\n\t"
        "ds_read_b64_tr_b16 v[100:101], v124 offset:0\n\t"
        "ds_read_b64_tr_b16 v[102:103], v124 offset:3072\n\t"
        "s_waitcnt lgkmcnt(10)\n\t"
        "v_mfma_f32_32x32x16_bf16 v[32:47], v[104:107], v[192:195], v[32:47]\n\t"
        "ds_read_b64_tr_b16 v[104:105], v123 offset:128\n\t"
        "ds_read_b64_tr_b16 v[106:107], v123 offset:3200\n\t"
        "buffer_load_dwordx4 v[188:191], v120, s[40:43], s44 offen offset:2048 sc1\n\t"
        "s_waitcnt lgkmcnt(10)\n\t"
        "v_mfma_f32_32x32x16_bf16 v[48:63], v[108:111], v[192:195], v[48:63]\n\t"
        "s_add_u32 s46, s46, 12288\n\t"
        "s_add_u32 s47, s47, 1\n\t"
        "ds_read_b64_tr_b16 v[108:109], v124 offset:128\n\t"
        "ds_read_b64_tr_b16 v[110:111], v124 offset:3200\n\t"
        "s_waitcnt lgkmcnt(10)\n\t"
        "v_mfma_f32_32x32x16_bf16 v[64:79], v[192:195], v[112:115], v[64:79]\n\t"
        "s_cmp_eq_u32 s46, 110592\n\t"
        "s_cselect_b32 s46, 0, s46\n\t"
        "ds_read_b64_tr_b16 v[112:113], v123 offset:256\n\t"
        "ds_read_b64_tr_b16 v[114:115], v123 offset:3328\n\t"
        "s_waitcnt lgkmcnt(10)\n\t"
        "v_mfma_f32_32x32x16_bf16 v[80:95], v[192:195], v[116:119], v[80:95]\n\t"
        "ds_write_b32 v127, v128\n\t"
        "ds_read_b64_tr_b16 v[116:117], v124 offset:256\n\t"
        "ds_read_b64_tr_b16 v[118:119], v124 offset:3328\n\t"
        "s_waitcnt vmcnt(14) lgkmcnt(11)\n\t"
        "v_mfma_f32_32x32x16_bf16 v[0:15], v[96:99], v[132:135], v[0:15]\n\t"
        "ds_read_b32 v126, v125\n\t"
        "ds_read_b64_tr_b16 v[96:97], v123 offset:6144\n\t"
        "ds_read_b64_tr_b16 v[98:99], v123 offset:9216\n\t"
        "s_waitcnt lgkmcnt(12)\n\t"
        "v_mfma_f32_32x32x16_bf16 v[16:31], v[100:103], v[132:135], v[16:31]\n\t"
        "v_add_u32 v129, s46, v121\n\t"
        "ds_read_b64_tr_b16 v[100:101], v124 offset:6144\n\t"
        "ds_read_b64_tr_b16 v[102:103], v124 offset:9216\n\t"
        "s_waitcnt lgkmcnt(12)\n\t"
        "v_mfma_f32_32x32x16_bf16 v[32:47], v[104:107], v[132:135], v[32:47]\n\t"
        "ds_read_b64_tr_b16 v[104:105], v123 offset:6272\n\t"
        "ds_read_b64_tr_b16 v[106:107], v123 offset:9344\n\t"
        "buffer_load_dwordx4 v[192:195], v120, s[40:43], s44 offen offset:3072 sc1\n\t"
        "s_waitcnt lgkmcnt(12)\n\t"
        "v_mfma_f32_32x32x16_bf16 v[48:63], v[108:111], v[132:135], v[48:63]\n\t"
        "v_add_u32 v130, s46, v122\n\t"
        "ds_read_b64_tr_b16 v[108:109], v124 offset:6272\n\t"
        "ds_read_b64_tr_b16 v[110:111], v124 offset:9344\n\t"
        "s_waitcnt lgkmcnt(12)\n\t"
        "v_mfma_f32_32x32x16_bf16 v[64:79], v[132:135], v[112:115], v[64:79]\n\t"
        "ds_read_b64_tr_b16 v[112:113], v123 offset:6400\n\t"
        "ds_read_b64_tr_b16 v[114:115], v123 offset:9472\n\t"
        "s_waitcnt lgkmcnt(10)\n\t"
        "v_mfma_f32_32x32x16_bf16 v[80:95], v[132:135], v[116:119], v[80:95]\n\t"
        "v_cmp_gt_u32 vcc, s47, v126\n\t"
        "ds_read_b64_tr_b16 v[116:117], v124 offset:6400\n\t"
        "ds_read_b64_tr_b16 v[118:119], v124 offset:9472\n\t"
        "s_waitcnt vmcnt(14) lgkmcnt(10)\n\t"
        "v_mfma_f32_32x32x16_bf16 v[0:15], v[96:99], v[136:139], v[0:15]\n\t"
        "s_cbranch_vccnz 28f\n\t"
        "29:\n\t"
        "ds_read_b64_tr_b16 v[96:97], v129 offset:0\n\t"
        "ds_read_b64_tr_b16 v[98:99], v129 offset:3072\n\t"
        "s_waitcnt lgkmcnt(10)\n\t"
        "v_mfma_f32_32x32x16_bf16 v[16:31], v[100:103], v[136:139], v[16:31]\n\t"
        "v_add_u32 v128, 1, v128\n\t"
        "ds_read_b64_tr_b16 v[100:101], v130 offset:0\n\t"
        "ds_read_b64_tr_b16 v[102:103], v130 offset:3072\n\t"
        "s_add_u32 s44, s44, 0x1000\n\t"
        "s_and_b32 s44, s44, 0xffff\n\t"
        "s_waitcnt lgkmcnt(10)\n\t"
        "v_mfma_f32_32x32x16_bf16 v[32:47], v[104:107], v[136:139], v[32:47]\n\t"
        "ds_read_b64_tr_b16 v[104:105], v129 offset:128\n\t"
        "ds_read_b64_tr_b16 v[106:107], v129 offset:3200\n\t"
        "s_cmp_eq_u32 s44, s49\n\t"
        "s_cselect_b32 s44, s50, s44\n\t"
        "buffer_load_dwordx4 v[132:135], v120, s[40:43], s44 offen offset:0 sc1\n\t"
        "s_waitcnt lgkmcnt(10)\n\t"
        "v_mfma_f32_32x32x16_bf16 v[48:63], v[108:111], v[136:139], v[48:63]\n\t"
        "s_add_u32 s46, s46, 12288\n\t"
        "s_add_u32 s47, s47, 1\n\t"
        "ds_read_b64_tr_b16 v[108:109], v130 offset:128\n\t"
        "ds_read_b64_tr_b16 v[110:111], v130 offset:3200\n\t"
        "s_waitcnt lgkmcnt(10)\n\t"
        "v_mfma_f32_32x32x16_bf16 v[64:79], v[136:139], v[112:115], v[64:79]\n\t"
        "s_cmp_eq_u32 s46, 110592\n\t"
        "s_cselect_b32 s46, 0, s46\n\t"
        "ds_read_b64_tr_b16 v[112:113], v129 offset:256\n\t"
        "ds_read_b64_tr_b16 v[114:115], v129 offset:3328\n\t"
        "s_waitcnt lgkmcnt(10)\n\t"
        "v_mfma_f32_32x32x16_bf16 v[80:95], v[136:139], v[116:119], v[80:95]\n\t"
        "ds_write_b32 v127, v128\n\t"
        "ds_read_b64_tr_b16 v[116:117], v130 offset:256\n\t"
        "ds_read_b64_tr_b16 v[118:119], v130 offset:3328\n\t"
        "s_waitcnt vmcnt(14) lgkmcnt(11)\n\t"
        "v_mfma_f32_32x32x16_bf16 v[0:15], v[96:99], v[140:143], v[0:15]\n\t"
        "ds_read_b32 v126, v125\n\t"
        "ds_read_b64_tr_b16 v[96:97], v129 offset:6144\n\t"
        "ds_read_b64_tr_b16 v[98:99], v129 offset:9216\n\t"
        "s_waitcnt lgkmcnt(12)\n\t"
        "v_mfma_f32_32x32x16_bf16 v[16:31], v[100:103], v[140:143], v[16:31]\n\t"
        "v_add_u32 v123, s46, v121\n\t"
        "ds_read_b64_tr_b16 v[100:101], v130 offset:6144\n\t"
        "ds_read_b64_tr_b16 v[102:103], v130 offset:9216\n\t"
        "s_waitcnt lgkmcnt(12)\n\t"
        "v_mfma_f32_32x32x16_bf16 v[32:47], v[104:107], v[140:143], v[32:47]\n\t"
        "ds_read_b64_tr_b16 v[104:105], v129 offset:6272\n\t"
        "ds_read_b64_tr_b16 v[106:107], v129 offset:9344\n\t"
        "buffer_load_dwordx4 v[136:139], v120, s[40:43], s44 offen offset:1024 sc1\n\t"
        "s_waitcnt lgkmcnt(12)\n\t"
        "v_mfma_f32_32x32x16_bf16 v[48:63], v[108:111], v[140:143], v[48:63]\n\t"
        "v_add_u32 v124, s46, v122\n\t"
        "ds_read_b64_tr_b16 v[108:109], v130 offset:6272\n\t"
        "ds_read_b64_tr_b16 v[110:111], v130 offset:9344\n\t"
        "s_waitcnt lgkmcnt(12)\n\t"
        "v_mfma_f32_32x32x16_bf16 v[64:79], v[140:143], v[112:115], v[64:79]\n\t"
        "ds_read_b64_tr_b16 v[112:113], v129 offset:6400\n\t"
        "ds_read_b64_tr_b16 v[114:115], v129 offset:9472\n\t"
        "s_waitcnt lgkmcnt(10)\n\t"
        "v_mfma_f32_32x32x16_bf16 v[80:95], v[140:143], v[116:119], v[80:95]\n\t"
        "v_cmp_gt_u32 vcc, s47, v126\n\t"
        "ds_read_b64_tr_b16 v[116:117], v130 offset:6400\n\t"
        "ds_read_b64_tr_b16 v[118:119], v130 offset:9472\n\t"
        "s_waitcnt vmcnt(14) lgkmcnt(10)\n\t"
        "v_mfma_f32_32x32x16_bf16 v[0:15], v[96:99], v[144:147], v[0:15]\n\t"
        "s_cbranch_vccnz 30f\n\t"
        "31:\n\t"
        "ds_read_b64_tr_b16 v[96:97], v123 offset:0\n\t"
        "ds_read_b64_tr_b16 v[98:99], v123 offset:3072\n\t"
        "s_waitcnt lgkmcnt(10)\n\t"
        "v_mfma_f32_32x32x16_bf16 v[16:31], v[100:103], v[144:147], v[16:31]\n\t"
        "v_add_u32 v128, 1, v128\n\t"
        "ds_read_b64_tr_b16 v[100:101], v124 offset:0\n\t"
        "ds_read_b64_tr_b16 v[102:103], v124 offset:3072\n\t"
        "s_waitcnt lgkmcnt(10)\n\t"
        "v_mfma_f32_32x32x16_bf16 v[32:47], v[104:107], v[144:147], v[32:47]\n\t"
        "ds_read_b64_tr_b16 v[104:105], v123 offset:128\n\t"
        "ds_read_b64_tr_b16 v[106:107], v123 offset:3200\n\t"
        "buffer_load_dwordx4 v[140:143], v120, s[40:43], s44 offen offset:2048 sc1\n\t"
        "s_waitcnt lgkmcnt(10)\n\t"
        "v_mfma_f32_32x32x16_bf16 v[48:63], v[108:111], v[144:147], v[48:63]\n\t"
        "s_add_u32 s46, s46, 12288\n\t"
        "s_add_u32 s47, s47, 1\n\t"
        "ds_read_b64_tr_b16 v[108:109], v124 offset:128\n\t"
        "ds_read_b64_tr_b16 v[110:111], v124 offset:3200\n\t"
        "s_waitcnt lgkmcnt(10)\n\t"
        "v_mfma_f32_32x32x16_bf16 v[64:79], v[144:147], v[112:115], v[64:79]\n\t"
        "s_cmp_eq_u32 s46, 110592\n\t"
        "s_cselect_b32 s46, 0, s46\n\t"
        "ds_read_b64_tr_b16 v[112:113], v123 offset:256\n\t"
        "ds_read_b64_tr_b16 v[114:115], v123 offset:3328\n\t"
        "s_waitcnt lgkmcnt(10)\n\t"
        "v_mfma_f32_32x32x16_bf16 v[80:95], v[144:147], v[116:119], v[80:95]\n\t"
        "ds_write_b32 v127, v128\n\t"
        "ds_read_b64_tr_b16 v[116:117], v124 offset:256\n\t"
        "ds_read_b64_tr_b16 v[118:119], v124 offset:3328\n\t"
        "s_sub_u32 s45, s45, 1\n\t"
        "s_cmp_lg_u32 s45, 0\n\t"
        "s_cbranch_scc1 1b\n\t"
        "s_waitcnt vmcnt(14) lgkmcnt(11)\n\t"
        "v_mfma_f32_32x32x16_bf16 v[0:15], v[96:99], v[148:151], v[0:15]\n\t"
        "ds_read_b32 v126, v125\n\t"
        "s_add_u32 s47, s47, 4\n\t"
        "ds_read_b64_tr_b16 v[96:97], v123 offset:6400\n\t"
        "ds_read_b64_tr_b16 v[98:99], v123 offset:9472\n\t"
        "s_waitcnt lgkmcnt(12)\n\t"
        "v_mfma_f32_32x32x16_bf16 v[16:31], v[100:103], v[148:151], v[16:31]\n\t"
        "ds_read_b64_tr_b16 v[100:101], v124 offset:6400\n\t"
        "ds_read_b64_tr_b16 v[102:103], v124 offset:9472\n\t"
        "s_waitcnt lgkmcnt(4)\n\t"
        "v_cmp_gt_u32 vcc, s47, v126\n\t"
        "v_mfma_f32_32x32x16_bf16 v[32:47], v[104:107], v[148:151], v[32:47]\n\t"
        "s_cbranch_vccnz 74f\n\t"
        "75:\n\t"
        "ds_read_b64_tr_b16 v[104:105], v121 offset:256\n\t"
        "ds_read_b64_tr_b16 v[106:107], v121 offset:3328\n\t"
        "s_waitcnt lgkmcnt(12)\n\t"
        "v_mfma_f32_32x32x16_bf16 v[48:63], v[108:111], v[148:151], v[48:63]\n\t"
        "ds_read_b64_tr_b16 v[108:109], v122 offset:256\n\t"
        "ds_read_b64_tr_b16 v[110:111], v122 offset:3328\n\t"
        "s_waitcnt lgkmcnt(12)\n\t"
        "v_mfma_f32_32x32x16_bf16 v[64:79], v[148:151], v[112:115], v[64:79]\n\t"
        "ds_read_b64_tr_b16 v[112:113], v121 offset:6400\n\t"
        "ds_read_b64_tr_b16 v[114:115], v121 offset:9472\n\t"
        "s_waitcnt lgkmcnt(11)\n\t"
        "v_mfma_f32_32x32x16_bf16 v[80:95], v[148:151], v[116:119], v[80:95]\n\t"
        "ds_read_b64_tr_b16 v[116:117], v122 offset:6400\n\t"
        "ds_read_b64_tr_b16 v[118:119], v122 offset:9472\n\t"
        "s_waitcnt vmcnt(13) lgkmcnt(10)\n\t"
        "v_mfma_f32_32x32x16_bf16 v[64:79], v[152:155], v[96:99], v[64:79]\n\t"
        "ds_read_b64_tr_b16 v[96:97], v121 offset:12544\n\t"
        "ds_read_b64_tr_b16 v[98:99], v121 offset:15616\n\t"
        "s_waitcnt lgkmcnt(10)\n\t"
        "v_mfma_f32_32x32x16_bf16 v[80:95], v[152:155], v[100:103], v[80:95]\n\t"
        "ds_read_b64_tr_b16 v[100:101], v122 offset:12544\n\t"
        "ds_read_b64_tr_b16 v[102:103], v122 offset:15616\n\t"
        "s_waitcnt vmcnt(12) lgkmcnt(10)\n\t"
        "v_mfma_f32_32x32x16_bf16 v[64:79], v[156:159], v[104:107], v[64:79]\n\t"
        "ds_read_b128 v[196:199], v215 offset:0\n\t"
        "ds_read_b64_tr_b16 v[104:105], v121 offset:18688\n\t"
        "ds_read_b64_tr_b16 v[106:107], v121 offset:21760\n\t"
        "s_waitcnt lgkmcnt(11)\n\t"
        "v_mfma_f32_32x32x16_bf16 v[80:95], v[156:159], v[108:111], v[80:95]\n\t"
        "ds_read_b128 v[200:203], v215 offset:32\n\t"
        "ds_read_b64_tr_b16 v[108:109], v122 offset:18688\n\t"
        "ds_read_b64_tr_b16 v[110:111], v122 offset:21760\n\t"
        "s_waitcnt vmcnt(11) lgkmcnt(12)\n\t"
        "v_mfma_f32_32x32x16_bf16 v[64:79], v[160:163], v[112:115], v[64:79]\n\t"
        "ds_read_b128 v[204:207], v215 offset:64\n\t"
        "ds_read_b64_tr_b16 v[112:113], v121 offset:24832\n\t"
        "ds_read_b64_tr_b16 v[114:115], v121 offset:27904\n\t"
        "s_waitcnt lgkmcnt(13)\n\t"
        "v_mfma_f32_32x32x16_bf16 v[80:95], v[160:163], v[116:119], v[80:95]\n\t"
        "ds_read_b128 v[208:211], v215 offset:96\n\t"
        "ds_read_b64_tr_b16 v[116:117], v122 offset:24832\n\t"
        "ds_read_b64_tr_b16 v[118:119], v122 offset:27904\n\t"
        "s_waitcnt vmcnt(10) lgkmcnt(14)\n\t"
        "v_mfma_f32_32x32x16_bf16 v[64:79], v[164:167], v[96:99], v[64:79]\n\t"
        "ds_read_b64_tr_b16 v[96:97], v121 offset:30976\n\t"
        "ds_read_b64_tr_b16 v[98:99], v121 offset:34048\n\t"
        "s_waitcnt lgkmcnt(14)\n\t"
        "v_mfma_f32_32x32x16_bf16 v[80:95], v[164:167], v[100:103], v[80:95]\n\t"
        "ds_read_b64_tr_b16 v[100:101], v122 offset:30976\n\t"
        "ds_read_b64_tr_b16 v[102:103], v122 offset:34048\n\t"
        "s_waitcnt vmcnt(9) lgkmcnt(13)\n\t"
        "v_mfma_f32_32x32x16_bf16 v[64:79], v[168:171], v[104:107], v[64:79]\n\t"
        "ds_read_b64_tr_b16 v[104:105], v121 offset:37120\n\t"
        "ds_read_b64_tr_b16 v[106:107], v121 offset:40192\n\t"
        "s_waitcnt lgkmcnt(12)\n\t"
        "v_mfma_f32_32x32x16_bf16 v[80:95], v[168:171], v[108:111], v[80:95]\n\t"
        "ds_read_b64_tr_b16 v[108:109], v122 offset:37120\n\t"
        "ds_read_b64_tr_b16 v[110:111], v122 offset:40192\n\t"
        "s_waitcnt vmcnt(8) lgkmcnt(11)\n\t"
        "v_mfma_f32_32x32x16_bf16 v[64:79], v[172:175], v[112:115], v[64:79]\n\t"
        "ds_read_b64_tr_b16 v[112:113], v121 offset:43264\n\t"
        "ds_read_b64_tr_b16 v[114:115], v121 offset:46336\n\t"
        "s_waitcnt lgkmcnt(10)\n\t"
        "v_mfma_f32_32x32x16_bf16 v[80:95], v[172:175], v[116:119], v[80:95]\n\t"
        "ds_read_b64_tr_b16 v[116:117], v122 offset:43264\n\t"
        "ds_read_b64_tr_b16 v[118:119], v122 offset:46336\n\t"
        "s_waitcnt vmcnt(7) lgkmcnt(10)\n\t"
        "v_mfma_f32_32x32x16_bf16 v[64:79], v[176:179], v[96:99], v[64:79]\n\t"
        "ds_read_b64_tr_b16 v[96:97], v121 offset:49408\n\t"
        "ds_read_b64_tr_b16 v[98:99], v121 offset:52480\n\t"
        "s_waitcnt lgkmcnt(10)\n\t"
        "v_mfma_f32_32x32x16_bf16 v[80:95], v[176:179], v[100:103], v[80:95]\n\t"
        "ds_read_b64_tr_b16 v[100:101], v122 offset:49408\n\t"
        "ds_read_b64_tr_b16 v[102:103], v122 offset:52480\n\t"
        "s_waitcnt vmcnt(6) lgkmcnt(10)\n\t"
        "v_mfma_f32_32x32x16_bf16 v[64:79], v[180:183], v[104:107], v[64:79]\n\t"
        "ds_read_b64_tr_b16 v[104:105], v121 offset:55552\n\t"
        "ds_read_b64_tr_b16 v[106:107], v121 offset:58624\n\t"
        "s_waitcnt lgkmcnt(10)\n\t"
        "v_mfma_f32_32x32x16_bf16 v[80:95], v[180:183], v[108:111], v[80:95]\n\t"
        "ds_read_b64_tr_b16 v[108:109], v122 offset:55552\n\t"
        "ds_read_b64_tr_b16 v[110:111], v122 offset:58624\n\t"
        "s_waitcnt vmcnt(5) lgkmcnt(10)\n\t"
        "v_mfma_f32_32x32x16_bf16 v[64:79], v[184:187], v[112:115], v[64:79]\n\t"
        "ds_read_b64_tr_b16 v[112:113], v123 offset:6272\n\t"
        "ds_read_b64_tr_b16 v[114:115], v123 offset:9344\n\t"
        "s_waitcnt lgkmcnt(10)\n\t"
        "v_mfma_f32_32x32x16_bf16 v[80:95], v[184:187], v[116:119], v[80:95]\n\t"
        "ds_read_b64_tr_b16 v[116:117], v124 offset:6272\n\t"
        "ds_read_b64_tr_b16 v[118:119], v124 offset:9344\n\t"
        "s_waitcnt vmcnt(4) lgkmcnt(10)\n\t"
        "v_mfma_f32_32x32x16_bf16 v[64:79], v[188:191], v[96:99], v[64:79]\n\t"
        "ds_read_b64_tr_b16 v[96:97], v121 offset:128\n\t"
        "ds_read_b64_tr_b16 v[98:99], v121 offset:3200\n\t"
        "s_waitcnt lgkmcnt(10)\n\t"
        "v_mfma_f32_32x32x16_bf16 v[80:95], v[188:191], v[100:103], v[80:95]\n\t"
        "ds_read_b64_tr_b16 v[100:101], v122 offset:128\n\t"
        "ds_read_b64_tr_b16 v[102:103], v122 offset:3200\n\t"
        "s_waitcnt vmcnt(0) lgkmcnt(10)\n\t"
        "v_mfma_f32_32x32x16_bf16 v[64:79], v[192:195], v[104:107], v[64:79]\n\t"
        "ds_read_b64_tr_b16 v[104:105], v121 offset:6272\n\t"
        "ds_read_b64_tr_b16 v[106:107], v121 offset:9344\n\t"
        "s_waitcnt lgkmcnt(10)\n\t"
        "v_mfma_f32_32x32x16_bf16 v[80:95], v[192:195], v[108:111], v[80:95]\n\t"
        "ds_read_b64_tr_b16 v[108:109], v122 offset:6272\n\t"
        "ds_read_b64_tr_b16 v[110:111], v122 offset:9344\n\t"
        "s_waitcnt lgkmcnt(10)\n\t"
        "v_mfma_f32_32x32x16_bf16 v[32:47], v[112:115], v[152:155], v[32:47]\n\t"
        "ds_read_b64_tr_b16 v[112:113], v121 offset:12416\n\t"
        "ds_read_b64_tr_b16 v[114:115], v121 offset:15488\n\t"
        "s_waitcnt lgkmcnt(10)\n\t"
        "v_mfma_f32_32x32x16_bf16 v[48:63], v[116:119], v[152:155], v[48:63]\n\t"
        "ds_read_b64_tr_b16 v[116:117], v122 offset:12416\n\t"
        "ds_read_b64_tr_b16 v[118:119], v122 offset:15488\n\t"
        "v_mul_f32 v64, v196, v64\n\t"
        "v_mul_f32 v65, v197, v65\n\t"
        "v_mul_f32 v66, v198, v66\n\t"
        "s_waitcnt lgkmcnt(10)\n\t"
        "v_mfma_f32_32x32x16_bf16 v[32:47], v[96:99], v[156:159], v[32:47]\n\t"
        "ds_read_b64_tr_b16 v[96:97], v121 offset:18560\n\t"
        "ds_read_b64_tr_b16 v[98:99], v121 offset:21632\n\t"
        "v_mul_f32 v67, v199, v67\n\t"
        "v_mul_f32 v68, v200, v68\n\t"
        "v_mul_f32 v69, v201, v69\n\t"
        "s_waitcnt lgkmcnt(10)\n\t"
        "v_mfma_f32_32x32x16_bf16 v[48:63], v[100:103], v[156:159], v[48:63]\n\t"
        "ds_read_b64_tr_b16 v[100:101], v122 offset:18560\n\t"
        "ds_read_b64_tr_b16 v[102:103], v122 offset:21632\n\t"
        "v_mul_f32 v70, v202, v70\n\t"
        "v_mul_f32 v71, v203, v71\n\t"
        "v_cvt_pk_bf16_f32 v64, v64, v65\n\t"
        "s_waitcnt lgkmcnt(10)\n\t"
        "v_mfma_f32_32x32x16_bf16 v[32:47], v[104:107], v[160:163], v[32:47]\n\t"
        "ds_read_b64_tr_b16 v[104:105], v121 offset:24704\n\t"
        "ds_read_b64_tr_b16 v[106:107], v121 offset:27776\n\t"
        "v_cvt_pk_bf16_f32 v65, v66, v67\n\t"
        "v_cvt_pk_bf16_f32 v66, v68, v69\n\t"
        "v_cvt_pk_bf16_f32 v67, v70, v71\n\t"
        "s_waitcnt lgkmcnt(10)\n\t"
        "v_mfma_f32_32x32x16_bf16 v[48:63], v[108:111], v[160:163], v[48:63]\n\t"
        "ds_read_b64_tr_b16 v[108:109], v122 offset:24704\n\t"
        "ds_read_b64_tr_b16 v[110:111], v122 offset:27776\n\t"
        "v_mul_f32 v72, v204, v72\n\t"
        "v_mul_f32 v73, v205, v73\n\t"
        "v_mul_f32 v74, v206, v74\n\t"
        "s_waitcnt lgkmcnt(10)\n\t"
        "v_mfma_f32_32x32x16_bf16 v[32:47], v[112:115], v[164:167], v[32:47]\n\t"
        "ds_read_b64_tr_b16 v[112:113], v121 offset:30848\n\t"
        "ds_read_b64_tr_b16 v[114:115], v121 offset:33920\n\t"
        "v_mul_f32 v75, v207, v75\n\t"
        "v_mul_f32 v76, v208, v76\n\t"
        "v_mul_f32 v77, v209, v77\n\t"
        "s_waitcnt lgkmcnt(10)\n\t"
        "v_mfma_f32_32x32x16_bf16 v[48:63], v[116:119], v[164:167], v[48:63]\n\t"
        "ds_read_b64_tr_b16 v[116:117], v122 offset:30848\n\t"
        "ds_read_b64_tr_b16 v[118:119], v122 offset:33920\n\t"
        "v_mul_f32 v78, v210, v78\n\t"
        "v_mul_f32 v79, v211, v79\n\t"
        "v_cvt_pk_bf16_f32 v68, v72, v73\n\t"
        "s_waitcnt lgkmcnt(10)\n\t"
        "v_mfma_f32_32x32x16_bf16 v[32:47], v[96:99], v[168:171], v[32:47]\n\t"
        "ds_read_b64_tr_b16 v[96:97], v121 offset:36992\n\t"
        "ds_read_b64_tr_b16 v[98:99], v121 offset:40064\n\t"
        "v_cvt_pk_bf16_f32 v69, v74, v75\n\t"
        "v_cvt_pk_bf16_f32 v70, v76, v77\n\t"
        "v_cvt_pk_bf16_f32 v71, v78, v79\n\t"
        "s_waitcnt lgkmcnt(10)\n\t"
        "v_mfma_f32_32x32x16_bf16 v[48:63], v[100:103], v[168:171], v[48:63]\n\t"
        "ds_read_b64_tr_b16 v[100:101], v122 offset:36992\n\t"
        "ds_read_b64_tr_b16 v[102:103], v122 offset:40064\n\t"
        "v_permlane32_swap_b32 v64, v66\n\t"
        "v_permlane32_swap_b32 v65, v67\n\t"
        "v_permlane32_swap_b32 v68, v70\n\t"
        "s_waitcnt lgkmcnt(10)\n\t"
        "v_mfma_f32_32x32x16_bf16 v[32:47], v[104:107], v[172:175], v[32:47]\n\t"
        "ds_read_b64_tr_b16 v[104:105], v121 offset:43136\n\t"
        "ds_read_b64_tr_b16 v[106:107], v121 offset:46208\n\t"
        "v_permlane32_swap_b32 v69, v71\n\t"
        "buffer_store_dwordx4 v[64:67], v213, s[40:43], 0 offen offset:2048 sc1\n\t"
        "buffer_store_dwordx4 v[68:71], v213, s[40:43], 0 offen offset:3072 sc1\n\t"
        "s_waitcnt lgkmcnt(10)\n\t"
        "v_mfma_f32_32x32x16_bf16 v[48:63], v[108:111], v[172:175], v[48:63]\n\t"
        "ds_read_b64_tr_b16 v[108:109], v122 offset:43136\n\t"
        "ds_read_b64_tr_b16 v[110:111], v122 offset:46208\n\t"
        "v_mul_f32 v80, v196, v80\n\t"
        "v_mul_f32 v81, v197, v81\n\t"
        "v_mul_f32 v82, v198, v82\n\t"
        "s_waitcnt lgkmcnt(10)\n\t"
        "v_mfma_f32_32x32x16_bf16 v[32:47], v[112:115], v[176:179], v[32:47]\n\t"
        "ds_read_b64_tr_b16 v[112:113], v121 offset:49280\n\t"
        "ds_read_b64_tr_b16 v[114:115], v121 offset:52352\n\t"
        "v_mul_f32 v83, v199, v83\n\t"
        "v_mul_f32 v84, v200, v84\n\t"
        "v_mul_f32 v85, v201, v85\n\t"
        "s_waitcnt lgkmcnt(10)\n\t"
        "v_mfma_f32_32x32x16_bf16 v[48:63], v[116:119], v[176:179], v[48:63]\n\t"
        "ds_read_b64_tr_b16 v[116:117], v122 offset:49280\n\t"
        "ds_read_b64_tr_b16 v[118:119], v122 offset:52352\n\t"
        "v_mul_f32 v86, v202, v86\n\t"
        "v_mul_f32 v87, v203, v87\n\t"
        "v_cvt_pk_bf16_f32 v80, v80, v81\n\t"
        "s_waitcnt lgkmcnt(10)\n\t"
        "v_mfma_f32_32x32x16_bf16 v[32:47], v[96:99], v[180:183], v[32:47]\n\t"
        "ds_read_b64_tr_b16 v[96:97], v121 offset:55424\n\t"
        "ds_read_b64_tr_b16 v[98:99], v121 offset:58496\n\t"
        "v_cvt_pk_bf16_f32 v81, v82, v83\n\t"
        "v_cvt_pk_bf16_f32 v82, v84, v85\n\t"
        "v_cvt_pk_bf16_f32 v83, v86, v87\n\t"
        "s_waitcnt lgkmcnt(10)\n\t"
        "v_mfma_f32_32x32x16_bf16 v[48:63], v[100:103], v[180:183], v[48:63]\n\t"
        "ds_read_b64_tr_b16 v[100:101], v122 offset:55424\n\t"
        "ds_read_b64_tr_b16 v[102:103], v122 offset:58496\n\t"
        "v_mul_f32 v88, v204, v88\n\t"
        "v_mul_f32 v89, v205, v89\n\t"
        "v_mul_f32 v90, v206, v90\n\t"
        "s_waitcnt lgkmcnt(10)\n\t"
        "v_mfma_f32_32x32x16_bf16 v[32:47], v[104:107], v[184:187], v[32:47]\n\t"
        "ds_read_b64_tr_b16 v[104:105], v123 offset:6144\n\t"
        "ds_read_b64_tr_b16 v[106:107], v123 offset:9216\n\t"
        "v_mul_f32 v91, v207, v91\n\t"
        "v_mul_f32 v92, v208, v92\n\t"
        "v_mul_f32 v93, v209, v93\n\t"
        "s_waitcnt lgkmcnt(10)\n\t"
        "v_mfma_f32_32x32x16_bf16 v[48:63], v[108:111], v[184:187], v[48:63]\n\t"
        "ds_read_b64_tr_b16 v[108:109], v124 offset:6144\n\t"
        "ds_read_b64_tr_b16 v[110:111], v124 offset:9216\n\t"
        "v_mul_f32 v94, v210, v94\n\t"
        "v_mul_f32 v95, v211, v95\n\t"
        "v_cvt_pk_bf16_f32 v84, v88, v89\n\t"
        "s_waitcnt lgkmcnt(10)\n\t"
        "v_mfma_f32_32x32x16_bf16 v[32:47], v[112:115], v[188:191], v[32:47]\n\t"
        "ds_read_b64_tr_b16 v[112:113], v121 offset:0\n\t"
        "ds_read_b64_tr_b16 v[114:115], v121 offset:3072\n\t"
        "v_cvt_pk_bf16_f32 v85, v90, v91\n\t"
        "v_cvt_pk_bf16_f32 v86, v92, v93\n\t"
        "v_cvt_pk_bf16_f32 v87, v94, v95\n\t"
        "s_waitcnt lgkmcnt(10)\n\t"
        "v_mfma_f32_32x32x16_bf16 v[48:63], v[116:119], v[188:191], v[48:63]\n\t"
        "ds_read_b64_tr_b16 v[116:117], v122 offset:0\n\t"
        "ds_read_b64_tr_b16 v[118:119], v122 offset:3072\n\t"
        "v_permlane32_swap_b32 v80, v82\n\t"
        "v_permlane32_swap_b32 v81, v83\n\t"
        "v_permlane32_swap_b32 v84, v86\n\t"
        "s_waitcnt lgkmcnt(10)\n\t"
        "v_mfma_f32_32x32x16_bf16 v[32:47], v[96:99], v[192:195], v[32:47]\n\t"
        "ds_read_b64_tr_b16 v[96:97], v121 offset:6144\n\t"
        "ds_read_b64_tr_b16 v[98:99], v121 offset:9216\n\t"
        "v_permlane32_swap_b32 v85, v87\n\t"
        "buffer_store_dwordx4 v[80:83], v214, s[40:43], 0 offen offset:2048 sc1\n\t"
        "buffer_store_dwordx4 v[84:87], v214, s[40:43], 0 offen offset:3072 sc1\n\t"
        "s_waitcnt lgkmcnt(10)\n\t"
        "v_mfma_f32_32x32x16_bf16 v[48:63], v[100:103], v[192:195], v[48:63]\n\t"
        "ds_read_b64_tr_b16 v[100:101], v122 offset:6144\n\t"
        "ds_read_b64_tr_b16 v[102:103], v122 offset:9216\n\t"
        "s_waitcnt lgkmcnt(10)\n\t"
        "v_mfma_f32_32x32x16_bf16 v[0:15], v[104:107], v[152:155], v[0:15]\n\t"
        "ds_read_b64_tr_b16 v[104:105], v121 offset:12288\n\t"
        "ds_read_b64_tr_b16 v[106:107], v121 offset:15360\n\t"
        "s_waitcnt lgkmcnt(10)\n\t"
        "v_mfma_f32_32x32x16_bf16 v[16:31], v[108:111], v[152:155], v[16:31]\n\t"
        "ds_read_b64_tr_b16 v[108:109], v122 offset:12288\n\t"
        "ds_read_b64_tr_b16 v[110:111], v122 offset:15360\n\t"
        "v_mul_f32 v32, v212, v32\n\t"
        "v_mul_f32 v33, v212, v33\n\t"
        "v_mul_f32 v34, v212, v34\n\t"
        "s_waitcnt lgkmcnt(10)\n\t"
        "v_mfma_f32_32x32x16_bf16 v[0:15], v[112:115], v[156:159], v[0:15]\n\t"
        "ds_read_b64_tr_b16 v[112:113], v121 offset:18432\n\t"
        "ds_read_b64_tr_b16 v[114:115], v121 offset:21504\n\t"
        "v_mul_f32 v35, v212, v35\n\t"
        "v_mul_f32 v36, v212, v36\n\t"
        "v_mul_f32 v37, v212, v37\n\t"
        "s_waitcnt lgkmcnt(10)\n\t"
        "v_mfma_f32_32x32x16_bf16 v[16:31], v[116:119], v[156:159], v[16:31]\n\t"
        "ds_read_b64_tr_b16 v[116:117], v122 offset:18432\n\t"
        "ds_read_b64_tr_b16 v[118:119], v122 offset:21504\n\t"
        "v_mul_f32 v38, v212, v38\n\t"
        "v_mul_f32 v39, v212, v39\n\t"
        "v_cvt_pk_bf16_f32 v32, v32, v33\n\t"
        "s_waitcnt lgkmcnt(10)\n\t"
        "v_mfma_f32_32x32x16_bf16 v[0:15], v[96:99], v[160:163], v[0:15]\n\t"
        "ds_read_b64_tr_b16 v[96:97], v121 offset:24576\n\t"
        "ds_read_b64_tr_b16 v[98:99], v121 offset:27648\n\t"
        "v_cvt_pk_bf16_f32 v33, v34, v35\n\t"
        "v_cvt_pk_bf16_f32 v34, v36, v37\n\t"
        "v_cvt_pk_bf16_f32 v35, v38, v39\n\t"
        "s_waitcnt lgkmcnt(10)\n\t"
        "v_mfma_f32_32x32x16_bf16 v[16:31], v[100:103], v[160:163], v[16:31]\n\t"
        "ds_read_b64_tr_b16 v[100:101], v122 offset:24576\n\t"
        "ds_read_b64_tr_b16 v[102:103], v122 offset:27648\n\t"
        "v_mul_f32 v40, v212, v40\n\t"
        "v_mul_f32 v41, v212, v41\n\t"
        "v_mul_f32 v42, v212, v42\n\t"
        "s_waitcnt lgkmcnt(10)\n\t"
        "v_mfma_f32_32x32x16_bf16 v[0:15], v[104:107], v[164:167], v[0:15]\n\t"
        "ds_read_b64_tr_b16 v[104:105], v121 offset:30720\n\t"
        "ds_read_b64_tr_b16 v[106:107], v121 offset:33792\n\t"
        "v_mul_f32 v43, v212, v43\n\t"
        "v_mul_f32 v44, v212, v44\n\t"
        "v_mul_f32 v45, v212, v45\n\t"
        "s_waitcnt lgkmcnt(10)\n\t"
        "v_mfma_f32_32x32x16_bf16 v[16:31], v[108:111], v[164:167], v[16:31]\n\t"
        "ds_read_b64_tr_b16 v[108:109], v122 offset:30720\n\t"
        "ds_read_b64_tr_b16 v[110:111], v122 offset:33792\n\t"
        "v_mul_f32 v46, v212, v46\n\t"
        "v_mul_f32 v47, v212, v47\n\t"
        "v_cvt_pk_bf16_f32 v36, v40, v41\n\t"
        "s_waitcnt lgkmcnt(10)\n\t"
        "v_mfma_f32_32x32x16_bf16 v[0:15], v[112:115], v[168:171], v[0:15]\n\t"
        "ds_read_b64_tr_b16 v[112:113], v121 offset:36864\n\t"
        "ds_read_b64_tr_b16 v[114:115], v121 offset:39936\n\t"
        "v_cvt_pk_bf16_f32 v37, v42, v43\n\t"
        "v_cvt_pk_bf16_f32 v38, v44, v45\n\t"
        "v_cvt_pk_bf16_f32 v39, v46, v47\n\t"
        "s_waitcnt lgkmcnt(10)\n\t"
        "v_mfma_f32_32x32x16_bf16 v[16:31], v[116:119], v[168:171], v[16:31]\n\t"
        "ds_read_b64_tr_b16 v[116:117], v122 offset:36864\n\t"
        "ds_read_b64_tr_b16 v[118:119], v122 offset:39936\n\t"
        "buffer_store_dwordx4 v[32:35], v213, s[40:43], 0 offen offset:0 sc1\n\t"
        "buffer_store_dwordx4 v[36:39], v213, s[40:43], 0 offen offset:1024 sc1\n\t"
        "v_mul_f32 v48, v212, v48\n\t"
        "s_waitcnt lgkmcnt(10)\n\t"
        "v_mfma_f32_32x32x16_bf16 v[0:15], v[96:99], v[172:175], v[0:15]\n\t"
        "ds_read_b64_tr_b16 v[96:97], v121 offset:43008\n\t"
        "ds_read_b64_tr_b16 v[98:99], v121 offset:46080\n\t"
        "v_mul_f32 v49, v212, v49\n\t"
        "v_mul_f32 v50, v212, v50\n\t"
        "v_mul_f32 v51, v212, v51\n\t"
        "s_waitcnt lgkmcnt(10)\n\t"
        "v_mfma_f32_32x32x16_bf16 v[16:31], v[100:103], v[172:175], v[16:31]\n\t"
        "ds_read_b64_tr_b16 v[100:101], v122 offset:43008\n\t"
        "ds_read_b64_tr_b16 v[102:103], v122 offset:46080\n\t"
        "v_mul_f32 v52, v212, v52\n\t"
        "v_mul_f32 v53, v212, v53\n\t"
        "v_mul_f32 v54, v212, v54\n\t"
        "s_waitcnt lgkmcnt(10)\n\t"
        "v_mfma_f32_32x32x16_bf16 v[0:15], v[104:107], v[176:179], v[0:15]\n\t"
        "ds_read_b64_tr_b16 v[104:105], v121 offset:49152\n\t"
        "ds_read_b64_tr_b16 v[106:107], v121 offset:52224\n\t"
        "v_mul_f32 v55, v212, v55\n\t"
        "v_cvt_pk_bf16_f32 v48, v48, v49\n\t"
        "v_cvt_pk_bf16_f32 v49, v50, v51\n\t"
        "s_waitcnt lgkmcnt(10)\n\t"
        "v_mfma_f32_32x32x16_bf16 v[16:31], v[108:111], v[176:179], v[16:31]\n\t"
        "ds_read_b64_tr_b16 v[108:109], v122 offset:49152\n\t"
        "ds_read_b64_tr_b16 v[110:111], v122 offset:52224\n\t"
        "v_cvt_pk_bf16_f32 v50, v52, v53\n\t"
        "v_cvt_pk_bf16_f32 v51, v54, v55\n\t"
        "v_mul_f32 v56, v212, v56\n\t"
        "s_waitcnt lgkmcnt(10)\n\t"
        "v_mfma_f32_32x32x16_bf16 v[0:15], v[112:115], v[180:183], v[0:15]\n\t"
        "ds_read_b64_tr_b16 v[112:113], v121 offset:55296\n\t"
        "ds_read_b64_tr_b16 v[114:115], v121 offset:58368\n\t"
        "v_mul_f32 v57, v212, v57\n\t"
        "v_mul_f32 v58, v212, v58\n\t"
        "v_mul_f32 v59, v212, v59\n\t"
        "s_waitcnt lgkmcnt(10)\n\t"
        "v_mfma_f32_32x32x16_bf16 v[16:31], v[116:119], v[180:183], v[16:31]\n\t"
        "ds_read_b64_tr_b16 v[116:117], v122 offset:55296\n\t"
        "ds_read_b64_tr_b16 v[118:119], v122 offset:58368\n\t"
        "v_mul_f32 v60, v212, v60\n\t"
        "v_mul_f32 v61, v212, v61\n\t"
        "v_mul_f32 v62, v212, v62\n\t"
        "s_waitcnt lgkmcnt(10)\n\t"
        "v_mfma_f32_32x32x16_bf16 v[0:15], v[96:99], v[184:187], v[0:15]\n\t"
        "v_mul_f32 v63, v212, v63\n\t"
        "v_cvt_pk_bf16_f32 v52, v56, v57\n\t"
        "v_cvt_pk_bf16_f32 v53, v58, v59\n\t"
        "v_cvt_pk_bf16_f32 v54, v60, v61\n\t"
        "v_cvt_pk_bf16_f32 v55, v62, v63\n\t"
        "s_waitcnt lgkmcnt(8)\n\t"
        "v_mfma_f32_32x32x16_bf16 v[16:31], v[100:103], v[184:187], v[16:31]\n\t"
        "buffer_store_dwordx4 v[48:51], v214, s[40:43], 0 offen offset:0 sc1\n\t"
        "buffer_store_dwordx4 v[52:55], v214, s[40:43], 0 offen offset:1024 sc1\n\t"
        "s_waitcnt lgkmcnt(6)\n\t"
        "v_mfma_f32_32x32x16_bf16 v[0:15], v[104:107], v[188:191], v[0:15]\n\t"
        "s_waitcnt lgkmcnt(4)\n\t"
        "v_mfma_f32_32x32x16_bf16 v[16:31], v[108:111], v[188:191], v[16:31]\n\t"
        "s_waitcnt lgkmcnt(2)\n\t"
        "v_mfma_f32_32x32x16_bf16 v[0:15], v[112:115], v[192:195], v[0:15]\n\t"
        "s_waitcnt lgkmcnt(0)\n\t"
        "v_mfma_f32_32x32x16_bf16 v[16:31], v[116:119], v[192:195], v[16:31]\n\t"
        "s_nop 11\n\t"
        "s_branch 9f\n\t"
        "16:\n\t"
        "s_mov_b32 s48, 0x10000\n\t"
        "8:\n\t"
        "ds_read_b32 v126, v125\n\t"
        "s_sub_u32 s48, s48, 1\n\t"
        "s_waitcnt lgkmcnt(0)\n\t"
        "s_cbranch_scc1 17b\n\t"
        "v_cmp_gt_u32 vcc, s47, v126\n\t"
        "s_cbranch_vccnz 8b\n\t"
        "s_branch 17b\n\t"
        "18:\n\t"
        "s_mov_b32 s48, 0x10000\n\t"
        "8:\n\t"
        "ds_read_b32 v126, v125\n\t"
        "s_sub_u32 s48, s48, 1\n\t"
        "s_waitcnt lgkmcnt(0)\n\t"
        "s_cbranch_scc1 19b\n\t"
        "v_cmp_gt_u32 vcc, s47, v126\n\t"
        "s_cbranch_vccnz 8b\n\t"
        "s_branch 19b\n\t"
        "20:\n\t"
        "s_mov_b32 s48, 0x10000\n\t"
        "8:\n\t"
        "ds_read_b32 v126, v125\n\t"
        "s_sub_u32 s48, s48, 1\n\t"
        "s_waitcnt lgkmcnt(0)\n\t"
        "s_cbranch_scc1 21b\n\t"
        "v_cmp_gt_u32 vcc, s47, v126\n\t"
        "s_cbranch_vccnz 8b\n\t"
        "s_branch 21b\n\t"
        "22:\n\t"
        "s_mov_b32 s48, 0x10000\n\t"
        "8:\n\t"
        "ds_read_b32 v126, v125\n\t"
        "s_sub_u32 s48, s48, 1\n\t"
        "s_waitcnt lgkmcnt(0)\n\t"
        "s_cbranch_scc1 23b\n\t"
        "v_cmp_gt_u32 vcc, s47, v126\n\t"
        "s_cbranch_vccnz 8b\n\t"
        "s_branch 23b\n\t"
        "24:\n\t"
        "s_mov_b32 s48, 0x10000\n\t"
        "8:\n\t"
        "ds_read_b32 v126, v125\n\t"
        "s_sub_u32 s48, s48, 1\n\t"
        "s_waitcnt lgkmcnt(0)\n\t"
        "s_cbranch_scc1 25b\n\t"
        "v_cmp_gt_u32 vcc, s47, v126\n\t"
        "s_cbranch_vccnz 8b\n\t"
        "s_branch 25b\n\t"
        "26:\n\t"
        "s_mov_b32 s48, 0x10000\n\t"
        "8:\n\t"
        "ds_read_b32 v126, v125\n\t"
        "s_sub_u32 s48, s48, 1\n\t"
        "s_waitcnt lgkmcnt(0)\n\t"
        "s_cbranch_scc1 27b\n\t"
        "v_cmp_gt_u32 vcc, s47, v126\n\t"
        "s_cbranch_vccnz 8b\n\t"
        "s_branch 27b\n\t"
        "28:\n\t"
        "s_mov_b32 s48, 0x10000\n\t"
        "8:\n\t"
        "ds_read_b32 v126, v125\n\t"
        "s_sub_u32 s48, s48, 1\n\t"
        "s_waitcnt lgkmcnt(0)\n\t"
        "s_cbranch_scc1 29b\n\t"
        "v_cmp_gt_u32 vcc, s47, v126\n\t"
        "s_cbranch_vccnz 8b\n\t"
        "s_branch 29b\n\t"
        "30:\n\t"
        "s_mov_b32 s48, 0x10000\n\t"
        "8:\n\t"
        "ds_read_b32 v126, v125\n\t"
        "s_sub_u32 s48, s48, 1\n\t"
        "s_waitcnt lgkmcnt(0)\n\t"
        "s_cbranch_scc1 31b\n\t"
        "v_cmp_gt_u32 vcc, s47, v126\n\t"
        "s_cbranch_vccnz 8b\n\t"
        "s_branch 31b\n\t"
        "74:\n\t"
        "s_mov_b32 s48, 0x10000\n\t"
        "8:\n\t"
        "ds_read_b32 v126, v125\n\t"
        "s_sub_u32 s48, s48, 1\n\t"
        "s_waitcnt lgkmcnt(0)\n\t"
        "s_cbranch_scc1 75b\n\t"
        "v_cmp_gt_u32 vcc, s47, v126\n\t"
        "s_cbranch_vccnz 8b\n\t"
        "s_branch 75b\n\t"
        "9:\n\t"
        : "+{v[0:15]}"(acc[0]), "+{v[16:31]}"(acc[1]), "+{v[32:47]}"(acc[2]), "+{v[48:63]}"(acc[3]), "+{v[64:79]}"(acc[4]), "+{v[80:95]}"(acc[5]),
          "+{v[96:111]}"(stA), "+{v[112:127]}"(stB), "+{v[128:131]}"(stC), "+{v[148:163]}"(xr1), "+{v[164:179]}"(xr2), "+{v[180:195]}"(xr3),
          "+{s44}"(soff), "+{s46}"(slot), "+{s47}"(need)
        : "{s[40:43]}"(srd), KSKIP, "{v[212:215]}"(ep) : "v196", "v197", "v198", "v199", "v200", "v201", "v202", "v203", "v204", "v205", "v206", "v207", "v208", "v209", "v210", "v211", "v132", "v133", "v134", "v135", "v136", "v137", "v138", "v139", "v140", "v141", "v142", "v143", "v144", "v145", "v146", "v147", "s45", "s48", "vcc", "scc", "memory");
      STAMPV(4 + 16 * L);
      PHASE_VARS;
      u32x4 kv[4][2];
#pragma unroll
      for (int j = 0; j < 4; ++j)
#pragma unroll
        for (int e = 0; e < 8; ++e) kv[j][e >> 2][e & 3] = __float_as_uint(acc[2 + j][e]);
      constexpr float QS = 0.17677669529663687f * 1.4426950408889634f;
      u32x4 qo[2][2];
      auto drained = [&]() { asm volatile("s_waitcnt vmcnt(0)\n\tds_write_b32 %0, %1" :: "v"(lbase + L_W + 80 + 4 * wid), "v"(2 * L + 1) : "memory"); };
      if (L == 0) {
#pragma unroll
        for (int e = 0; e < 16; ++e) {
          if (e == 8) { SBAR(); drained(); SBAR(); }
          const unsigned w = pack2(acc[e >> 3][2 * (e & 7)], acc[e >> 3][2 * (e & 7) + 1]);
          qo[e >> 3][(e >> 2) & 1][e & 3] = pack2(lo16(w) * QS, hi16(w) * QS);
        }
      } else {
        const float aq = U2F(ep[0]) * QS;
#pragma unroll
        for (int e = 0; e < 16; ++e) qo[e >> 3][(e >> 2) & 1][e & 3] = pack2(acc[e >> 3][2 * (e & 7)] * aq, acc[e >> 3][2 * (e & 7) + 1] * aq);
        SBAR();
        lds_wait_ge<0>(lbase + L_W + 4 + 4 * (lane & 1), 1);
        const int idx = (int)__builtin_amdgcn_readfirstlane(words[3]);
        if ((idx >> 5) == tile) {
          f32x4 cs[4][4];
          float cv[2];
#pragma unroll
          for (int j = 0; j < 6; ++j) {
            const int cb = (j >> 1) * 64 + ((j & 1) ^ hh) * 32;
            if (j >= 4) cv[j - 4] = *reinterpret_cast<const float*>(smem + L_PATCH + (cb + r32) * 4);
            else {
#pragma unroll
              for (int g = 0; g < 4; ++g) cs[j][g] = *reinterpret_cast<const f32x4*>(smem + L_PATCH + (cb + 8 * g + 4 * hi) * 4);
            }
          }
          SBAR();
          const bool me = r32 == (idx & 31);
          const int rel = (idx & 31) - 8 * hi;
#pragma unroll
          for (int j = 0; j < 2; ++j) {
            const unsigned pv = pack2(cv[j], cv[j]);
#pragma unroll
            for (int e = 0; e < 8; ++e) {
              unsigned a = pack2(cs[j][e >> 1][2 * (e & 1)] * QS, cs[j][e >> 1][2 * (e & 1) + 1] * QS), b = pack2(cs[2 + j][e >> 1][2 * (e & 1)], cs[2 + j][e >> 1][2 * (e & 1) + 1]);
              PIN(a); PIN(b);
              qo[j][e >> 2][e & 3] = me ? a : qo[j][e >> 2][e & 3];
              kv[j][e >> 2][e & 3] = me ? b : kv[j][e >> 2][e & 3];
              const int t0 = 16 * (e >> 2) + 2 * (e & 3);
              const unsigned mask = rel == t0 ? 0xffffu : rel == t0 + 1 ? 0xffff0000u : 0u;
              kv[2 + j][e >> 2][e & 3] = (kv[2 + j][e >> 2][e & 3] & ~mask) | (pv & mask);
            }
          }
          if (me) {
#pragma unroll
            for (int i = 0; i < 4; ++i) x1p[i] = *reinterpret_cast<const u32x4*>(smem + L_X2P + (i * 2 + hi) * 16);
            reinterpret_cast<float*>(smem + L_AB)[256 + wid * 64 + lane] = hi ? 1.0f : 0.0f;
          }
          DRAIN();
#pragma unroll
          for (int j = 0; j < 4; ++j) { stg(rs, ep[1 + (j & 1)] + (j >> 1) * 2048, kv[j][0]); stg(rs, ep[1 + (j & 1)] + (j >> 1) * 2048 + 1024, kv[j][1]); }
        }
        drained();
      }
      SBAR();
#pragma unroll
      for (int j = 0; j < 2; ++j) {
        char* p = smem + L_XCH + ((j ^ hh) * 4 + wid) * 2048 + lane * 16;
        *reinterpret_cast<u32x4*>(p) = qo[j][0]; *reinterpret_cast<u32x4*>(p + 1024) = qo[j][1];
        p = smem + L_XCH + 16384 + wid * 4096 + j * 2048 + lane * 16;
        *reinterpret_cast<u32x4*>(p) = kv[2 * j + 1][0]; *reinterpret_cast<u32x4*>(p + 1024) = kv[2 * j + 1][1];
      }
      STAMPV(41 + 4 * L);
      BAR_LGKM();
      STAMPV(43 + 4 * L);
      kva = cat4(kv[0][0], kv[0][1], kv[2][0], kv[2][1]);
      const u32x4* pk = reinterpret_cast<const u32x4*>(smem + L_XCH + 16384 + (wid ^ 1) * 4096 + lane * 16);
#pragma unroll
      for (int u = 0; u < 4; ++u) {
        const u32x4* p = reinterpret_cast<const u32x4*>(smem + L_XCH + (hh * 4 + ((2 * kh + 2 + u) & 3)) * 2048 + lane * 16);
        q[u] = __builtin_shufflevector(p[0], p[64], 0, 1, 2, 3, 4, 5, 6, 7);
      }
      kvb = cat4(pk[0], pk[64], pk[128], pk[192]);
      SBAR();
      asm volatile(
        "v_mov_b64 v[0:1], 0\n\t"
        "v_mov_b64 v[2:3], 0\n\t"
        "v_mov_b64 v[4:5], 0\n\t"
        "v_mov_b64 v[6:7], 0\n\t"
        "v_mov_b64 v[8:9], 0\n\t"
        "v_mov_b64 v[10:11], 0\n\t"
        "v_mov_b64 v[12:13], 0\n\t"
        "v_mov_b64 v[14:15], 0\n\t"
        "v_mov_b64 v[16:17], 0\n\t"
        "v_mov_b64 v[18:19], 0\n\t"
        "v_mov_b64 v[20:21], 0\n\t"
        "v_mov_b64 v[22:23], 0\n\t"
        "v_mov_b64 v[24:25], 0\n\t"
        "v_mov_b64 v[26:27], 0\n\t"
        "v_mov_b64 v[28:29], 0\n\t"
        "v_mov_b64 v[30:31], 0\n\t"
        "v_mov_b64 v[32:33], 0\n\t"
        "v_mov_b64 v[34:35], 0\n\t"
        "v_mov_b64 v[36:37], 0\n\t"
        "v_mov_b64 v[38:39], 0\n\t"
        "v_mov_b64 v[40:41], 0\n\t"
        "v_mov_b64 v[42:43], 0\n\t"
        "v_mov_b64 v[44:45], 0\n\t"
        "v_mov_b64 v[46:47], 0\n\t"
        "v_mov_b64 v[48:49], 0\n\t"
        "v_mov_b64 v[50:51], 0\n\t"
        "v_mov_b64 v[52:53], 0\n\t"
        "v_mov_b64 v[54:55], 0\n\t"
        "v_mov_b64 v[56:57], 0\n\t"
        "v_mov_b64 v[58:59], 0\n\t"
        "v_mov_b64 v[60:61], 0\n\t"
        "v_mov_b64 v[62:63], 0\n\t"
        "v_mov_b64 v[128:129], 0\n\t"
        "v_mov_b64 v[130:131], 0\n\t"
        "v_mov_b64 v[132:133], 0\n\t"
        "v_mov_b64 v[134:135], 0\n\t"
        "v_mov_b64 v[136:137], 0\n\t"
        "v_mov_b64 v[138:139], 0\n\t"
        "v_mov_b64 v[140:141], 0\n\t"
        "v_mov_b64 v[142:143], 0\n\t"
        : "={v[0:15]}"(oz[0]), "={v[16:31]}"(oz[1]), "={v[32:47]}"(oz[2]), "={v[48:63]}"(oz[3]), "={v[128:143]}"(oz[4]));
    }
    STAMPV(5 + 16 * L);
    {
      u32x4 ones;
      unsigned kvoff;
      u32x8 fv, cv;
      asm volatile("" : "={v[184:191]}"(fv), "={v[216:223]}"(cv));
      { PHASE_VARS;
        const unsigned v = (lane & 3) == 0 ? 0x3F803F80u : 0u; const bool up = (lane & 4) != 0; ones[0] = ones[1] = up ? 0u : v; ones[2] = ones[3] = up ? v : 0u;
        kvoff = OFF_KV + L * (10 * MBY) + (tn * 2 + hh) * HS + kh * 8192 + lane * 16;
        fv[1] = lbase + L_FREE + wid * 256 + lane * 4; fv[2] = 32 * L + 32;
        cv[0] = lbase + L_XCH + wid * 8192 + lane * 16; cv[1] = lbase + L_XCH + (wid ^ 2) * 8192 + lane * 16;
        cv[2] = lbase + L_XCH + 32768 + wid * 512 + lane * 4; cv[3] = lbase + L_XCH + 32768 + (wid ^ 2) * 512 + lane * 4;
        cv[4] = lbase + L_W + 48 + wid * 4; cv[5] = lbase + L_W + 48 + (wid ^ 2) * 4; cv[6] = L + 1;
        cv[7] = ((tile * 32 + r32) * D_ + tn * 64 + hi * 8) * 2; }
      f32x16 o0 = oz[0], o1 = oz[1], o2 = oz[2], o3 = oz[3], lv = oz[4];
      unsigned soff = (tm | 7) * 16384;
      STAMP_DECL;
      asm volatile(
        "s_nop 1\n\t"
        "v_mfma_f32_32x32x16_bf16 v[96:111], v[152:155], v[64:67], 0\n\t"
        "v_mfma_f32_32x32x16_bf16 v[96:111], v[156:159], v[68:71], v[96:111]\n\t"
        "s_mov_b32 s46, 0\n\t"
        "ds_write_b32 v185, v186\n\t"
        "s_nop 9\n\t"
        "v_exp_f32 v96, v96\n\t"
        "v_exp_f32 v97, v97\n\t"
        "v_exp_f32 v98, v98\n\t"
        "v_exp_f32 v99, v99\n\t"
        "v_exp_f32 v100, v100\n\t"
        "v_exp_f32 v101, v101\n\t"
        "v_mfma_f32_32x32x16_bf16 v[112:127], v[152:155], v[72:75], 0\n\t"
        "v_exp_f32 v102, v102\n\t"
        "v_exp_f32 v103, v103\n\t"
        "v_exp_f32 v104, v104\n\t"
        "v_mfma_f32_32x32x16_bf16 v[112:127], v[156:159], v[76:79], v[112:127]\n\t"
        "v_exp_f32 v105, v105\n\t"
        "v_exp_f32 v106, v106\n\t"
        "v_exp_f32 v107, v107\n\t"
        "v_smfmac_f32_32x32x32_bf16 v[0:15], v[160:163], v[96:103], v148\n\t"
        "v_exp_f32 v108, v108\n\t"
        "v_exp_f32 v109, v109\n\t"
        "v_exp_f32 v110, v110\n\t"
        "v_smfmac_f32_16x16x64_bf16 v[128:131], v[144:147], v[96:103], v148\n\t"
        "v_exp_f32 v111, v111\n\t"
        "v_exp_f32 v112, v112\n\t"
        "v_exp_f32 v113, v113\n\t"
        "v_smfmac_f32_32x32x32_bf16 v[0:15], v[164:167], v[104:111], v148\n\t"
        "v_exp_f32 v114, v114\n\t"
        "v_exp_f32 v115, v115\n\t"
        "v_exp_f32 v116, v116\n\t"
        "v_smfmac_f32_16x16x64_bf16 v[128:131], v[144:147], v[104:111], v148\n\t"
        "v_exp_f32 v117, v117\n\t"
        "v_mfma_f32_32x32x16_bf16 v[96:111], v[152:155], v[80:83], 0\n\t"
        "v_exp_f32 v118, v118\n\t"
        "v_exp_f32 v119, v119\n\t"
        "v_exp_f32 v120, v120\n\t"
        "ds_read_b32 v151, v224\n\t"
        "v_mfma_f32_32x32x16_bf16 v[96:111], v[156:159], v[84:87], v[96:111]\n\t"
        "v_exp_f32 v121, v121\n\t"
        "v_exp_f32 v122, v122\n\t"
        "v_exp_f32 v123, v123\n\t"
        "v_smfmac_f32_32x32x32_bf16 v[16:31], v[160:163], v[112:119], v148\n\t"
        "v_exp_f32 v124, v124\n\t"
        "v_exp_f32 v125, v125\n\t"
        "v_exp_f32 v126, v126\n\t"
        "v_smfmac_f32_16x16x64_bf16 v[132:135], v[144:147], v[112:119], v148\n\t"
        "v_exp_f32 v127, v127\n\t"
        "s_cmp_lg_u32 s46, 0\n\t"
        "s_cbranch_scc1 25f\n\t"
        "s_waitcnt lgkmcnt(0)\n\t"
        "v_cmp_ne_u32 vcc, v222, v151\n\t"
        "s_cbranch_vccz 24f\n\t"
        "25:\n\t"
        "v_exp_f32 v96, v96\n\t"
        "v_exp_f32 v97, v97\n\t"
        "v_smfmac_f32_32x32x32_bf16 v[16:31], v[164:167], v[120:127], v148\n\t"
        "v_exp_f32 v98, v98\n\t"
        "v_exp_f32 v99, v99\n\t"
        "v_exp_f32 v100, v100\n\t"
        "v_smfmac_f32_16x16x64_bf16 v[132:135], v[144:147], v[120:127], v148\n\t"
        "v_exp_f32 v101, v101\n\t"
        "v_mfma_f32_32x32x16_bf16 v[112:127], v[152:155], v[88:91], 0\n\t"
        "v_exp_f32 v102, v102\n\t"
        "v_exp_f32 v103, v103\n\t"
        "v_exp_f32 v104, v104\n\t"
        "ds_read_b32 v151, v224\n\t"
        "v_mfma_f32_32x32x16_bf16 v[112:127], v[156:159], v[92:95], v[112:127]\n\t"
        "v_exp_f32 v105, v105\n\t"
        "v_exp_f32 v106, v106\n\t"
        "v_exp_f32 v107, v107\n\t"
        "v_smfmac_f32_32x32x32_bf16 v[32:47], v[160:163], v[96:103], v148\n\t"
        "v_exp_f32 v108, v108\n\t"
        "v_exp_f32 v109, v109\n\t"
        "v_exp_f32 v110, v110\n\t"
        "v_smfmac_f32_16x16x64_bf16 v[136:139], v[144:147], v[96:103], v148\n\t"
        "v_exp_f32 v111, v111\n\t"
        "s_cmp_lg_u32 s46, 0\n\t"
        "s_cbranch_scc1 27f\n\t"
        "s_waitcnt lgkmcnt(0)\n\t"
        "v_cmp_ne_u32 vcc, v222, v151\n\t"
        "s_cbranch_vccz 26f\n\t"
        "27:\n\t"
        "v_exp_f32 v112, v112\n\t"
        "v_exp_f32 v113, v113\n\t"
        "v_smfmac_f32_32x32x32_bf16 v[32:47], v[164:167], v[104:111], v148\n\t"
        "v_exp_f32 v114, v114\n\t"
        "v_exp_f32 v115, v115\n\t"
        "v_exp_f32 v116, v116\n\t"
        "v_smfmac_f32_16x16x64_bf16 v[136:139], v[144:147], v[104:111], v148\n\t"
        "v_exp_f32 v117, v117\n\t"
        "v_mfma_f32_32x32x16_bf16 v[96:111], v[168:171], v[64:67], 0\n\t"
        "v_exp_f32 v118, v118\n\t"
        "v_exp_f32 v119, v119\n\t"
        "v_exp_f32 v120, v120\n\t"
        "ds_read_b32 v151, v224\n\t"
        "v_mfma_f32_32x32x16_bf16 v[96:111], v[172:175], v[68:71], v[96:111]\n\t"
        "v_exp_f32 v121, v121\n\t"
        "v_exp_f32 v122, v122\n\t"
        "v_exp_f32 v123, v123\n\t"
        "v_smfmac_f32_32x32x32_bf16 v[48:63], v[160:163], v[112:119], v148\n\t"
        "v_exp_f32 v124, v124\n\t"
        "v_exp_f32 v125, v125\n\t"
        "v_exp_f32 v126, v126\n\t"
        "v_smfmac_f32_16x16x64_bf16 v[140:143], v[144:147], v[112:119], v148\n\t"
        "v_exp_f32 v127, v127\n\t"
        "s_cmp_lg_u32 s46, 0\n\t"
        "s_cbranch_scc1 29f\n\t"
        "s_waitcnt lgkmcnt(0)\n\t"
        "v_cmp_ne_u32 vcc, v222, v151\n\t"
        "s_cbranch_vccz 28f\n\t"
        "29:\n\t"
        "v_exp_f32 v96, v96\n\t"
        "v_exp_f32 v97, v97\n\t"
        "v_smfmac_f32_32x32x32_bf16 v[48:63], v[164:167], v[120:127], v148\n\t"
        "v_exp_f32 v98, v98\n\t"
        "v_exp_f32 v99, v99\n\t"
        "v_exp_f32 v100, v100\n\t"
        "v_smfmac_f32_16x16x64_bf16 v[140:143], v[144:147], v[120:127], v148\n\t"
        "v_exp_f32 v101, v101\n\t"
        "v_mfma_f32_32x32x16_bf16 v[112:127], v[168:171], v[72:75], 0\n\t"
        "v_exp_f32 v102, v102\n\t"
        "v_exp_f32 v103, v103\n\t"
        "v_exp_f32 v104, v104\n\t"
        "ds_read_b32 v151, v224\n\t"
        "v_mfma_f32_32x32x16_bf16 v[112:127], v[172:175], v[76:79], v[112:127]\n\t"
        "v_exp_f32 v105, v105\n\t"
        "v_exp_f32 v106, v106\n\t"
        "v_exp_f32 v107, v107\n\t"
        "v_smfmac_f32_32x32x32_bf16 v[0:15], v[176:179], v[96:103], v148\n\t"
        "v_exp_f32 v108, v108\n\t"
        "v_exp_f32 v109, v109\n\t"
        "v_exp_f32 v110, v110\n\t"
        "v_smfmac_f32_16x16x64_bf16 v[128:131], v[144:147], v[96:103], v148\n\t"
        "v_exp_f32 v111, v111\n\t"
        "s_cmp_lg_u32 s46, 0\n\t"
        "s_cbranch_scc1 31f\n\t"
        "s_waitcnt lgkmcnt(0)\n\t"
        "v_cmp_ne_u32 vcc, v222, v151\n\t"
        "s_cbranch_vccz 30f\n\t"
        "31:\n\t"
        "v_exp_f32 v112, v112\n\t"
        "v_exp_f32 v113, v113\n\t"
        "v_smfmac_f32_32x32x32_bf16 v[0:15], v[180:183], v[104:111], v148\n\t"
        "v_exp_f32 v114, v114\n\t"
        "v_exp_f32 v115, v115\n\t"
        "v_exp_f32 v116, v116\n\t"
        "v_smfmac_f32_16x16x64_bf16 v[128:131], v[144:147], v[104:111], v148\n\t"
        "v_exp_f32 v117, v117\n\t"
        "v_mfma_f32_32x32x16_bf16 v[96:111], v[168:171], v[80:83], 0\n\t"
        "v_exp_f32 v118, v118\n\t"
        "v_exp_f32 v119, v119\n\t"
        "v_exp_f32 v120, v120\n\t"
        "ds_read_b32 v151, v224\n\t"
        "v_mfma_f32_32x32x16_bf16 v[96:111], v[172:175], v[84:87], v[96:111]\n\t"
        "v_exp_f32 v121, v121\n\t"
        "v_exp_f32 v122, v122\n\t"
        "v_exp_f32 v123, v123\n\t"
        "v_smfmac_f32_32x32x32_bf16 v[16:31], v[176:179], v[112:119], v148\n\t"
        "v_exp_f32 v124, v124\n\t"
        "v_exp_f32 v125, v125\n\t"
        "v_exp_f32 v126, v126\n\t"
        "v_smfmac_f32_16x16x64_bf16 v[132:135], v[144:147], v[112:119], v148\n\t"
        "v_exp_f32 v127, v127\n\t"
        "s_cmp_lg_u32 s46, 0\n\t"
        "s_cbranch_scc1 33f\n\t"
        "s_waitcnt lgkmcnt(0)\n\t"
        "v_cmp_ne_u32 vcc, v222, v151\n\t"
        "s_cbranch_vccz 32f\n\t"
        "33:\n\t"
        "v_exp_f32 v96, v96\n\t"
        "v_exp_f32 v97, v97\n\t"
        "v_smfmac_f32_32x32x32_bf16 v[16:31], v[180:183], v[120:127], v148\n\t"
        "v_exp_f32 v98, v98\n\t"
        "v_exp_f32 v99, v99\n\t"
        "v_exp_f32 v100, v100\n\t"
        "v_smfmac_f32_16x16x64_bf16 v[132:135], v[144:147], v[120:127], v148\n\t"
        "v_exp_f32 v101, v101\n\t"
        "v_mfma_f32_32x32x16_bf16 v[112:127], v[168:171], v[88:91], 0\n\t"
        "v_exp_f32 v102, v102\n\t"
        "v_exp_f32 v103, v103\n\t"
        "v_exp_f32 v104, v104\n\t"
        "v_mfma_f32_32x32x16_bf16 v[112:127], v[172:175], v[92:95], v[112:127]\n\t"
        "v_exp_f32 v105, v105\n\t"
        "v_exp_f32 v106, v106\n\t"
        "v_exp_f32 v107, v107\n\t"
        "v_smfmac_f32_32x32x32_bf16 v[32:47], v[176:179], v[96:103], v148\n\t"
        "v_exp_f32 v108, v108\n\t"
        "v_exp_f32 v109, v109\n\t"
        "v_exp_f32 v110, v110\n\t"
        "v_smfmac_f32_16x16x64_bf16 v[136:139], v[144:147], v[96:103], v148\n\t"
        "v_exp_f32 v111, v111\n\t"
        "s_cmp_lg_u32 s46, 0\n\t"
        "s_cbranch_scc1 5f\n\t"
        "s_mov_b32 s45, 0x200000\n\t"
        "2:\n\t"
        "ds_read_b32 v151, v224\n\t"
        "s_sub_u32 s45, s45, 1\n\t"
        "s_waitcnt lgkmcnt(0)\n\t"
        "s_cbranch_scc1 3f\n\t"
        "v_cmp_ne_u32 vcc, v222, v151\n\t"
        "s_cbranch_vccnz 2b\n\t"
        "3:\n\t"
        "s_add_u32 s44, s44, 0x4000\n\t"
        "s_and_b32 s44, s44, 0x3ffff\n\t"
        "s_cmp_eq_u32 s44, s49\n\t"
        "s_cselect_b32 s44, s50, s44\n\t"
        "buffer_load_dwordx4 v[184:187], v149, s[40:43], s44 offen offset:0 sc1\n\t"
        "buffer_load_dwordx4 v[188:191], v149, s[40:43], s44 offen offset:1024 sc1\n\t"
        "buffer_load_dwordx4 v[192:195], v149, s[40:43], s44 offen offset:2048 sc1\n\t"
        "buffer_load_dwordx4 v[196:199], v149, s[40:43], s44 offen offset:3072 sc1\n\t"
        "buffer_load_dwordx4 v[200:203], v150, s[40:43], s44 offen offset:0 sc1\n\t"
        "buffer_load_dwordx4 v[204:207], v150, s[40:43], s44 offen offset:1024 sc1\n\t"
        "buffer_load_dwordx4 v[208:211], v150, s[40:43], s44 offen offset:2048 sc1\n\t"
        "buffer_load_dwordx4 v[212:215], v150, s[40:43], s44 offen offset:3072 sc1\n\t"
        "5:\n\t"
        "s_add_u32 s44, s44, 0x4000\n\t"
        "s_and_b32 s44, s44, 0x3ffff\n\t"
        "s_cmp_eq_u32 s44, s49\n\t"
        "s_cselect_b32 s44, s50, s44\n\t"
        "buffer_load_dwordx4 v[152:155], v149, s[40:43], s44 offen offset:0 sc1\n\t"
        "buffer_load_dwordx4 v[156:159], v149, s[40:43], s44 offen offset:1024 sc1\n\t"
        "buffer_load_dwordx4 v[160:163], v149, s[40:43], s44 offen offset:2048 sc1\n\t"
        "buffer_load_dwordx4 v[164:167], v149, s[40:43], s44 offen offset:3072 sc1\n\t"
        "s_mov_b32 s45, 7\n\t"
        "1:\n\t"
        "v_exp_f32 v112, v112\n\t"
        "v_exp_f32 v113, v113\n\t"
        "v_smfmac_f32_32x32x32_bf16 v[32:47], v[180:183], v[104:111], v148\n\t"
        "v_exp_f32 v114, v114\n\t"
        "v_exp_f32 v115, v115\n\t"
        "v_exp_f32 v116, v116\n\t"
        "v_smfmac_f32_16x16x64_bf16 v[136:139], v[144:147], v[104:111], v148\n\t"
        "v_exp_f32 v117, v117\n\t"
        "s_waitcnt vmcnt(8)\n\t"
        "v_mfma_f32_32x32x16_bf16 v[96:111], v[184:187], v[64:67], 0\n\t"
        "v_exp_f32 v118, v118\n\t"
        "v_exp_f32 v119, v119\n\t"
        "v_exp_f32 v120, v120\n\t"
        "v_mfma_f32_32x32x16_bf16 v[96:111], v[188:191], v[68:71], v[96:111]\n\t"
        "v_exp_f32 v121, v121\n\t"
        "v_exp_f32 v122, v122\n\t"
        "v_exp_f32 v123, v123\n\t"
        "v_smfmac_f32_32x32x32_bf16 v[48:63], v[176:179], v[112:119], v148\n\t"
        "v_exp_f32 v124, v124\n\t"
        "v_exp_f32 v125, v125\n\t"
        "v_exp_f32 v126, v126\n\t"
        "v_smfmac_f32_16x16x64_bf16 v[140:143], v[144:147], v[112:119], v148\n\t"
        "buffer_load_dwordx4 v[168:171], v150, s[40:43], s44 offen offset:0 sc1\n\t"
        "v_exp_f32 v127, v127\n\t"
        "v_exp_f32 v96, v96\n\t"
        "v_exp_f32 v97, v97\n\t"
        "v_smfmac_f32_32x32x32_bf16 v[48:63], v[180:183], v[120:127], v148\n\t"
        "v_exp_f32 v98, v98\n\t"
        "v_exp_f32 v99, v99\n\t"
        "v_exp_f32 v100, v100\n\t"
        "v_smfmac_f32_16x16x64_bf16 v[140:143], v[144:147], v[120:127], v148\n\t"
        "v_exp_f32 v101, v101\n\t"
        "v_mfma_f32_32x32x16_bf16 v[112:127], v[184:187], v[72:75], 0\n\t"
        "v_exp_f32 v102, v102\n\t"
        "v_exp_f32 v103, v103\n\t"
        "v_exp_f32 v104, v104\n\t"
        "v_mfma_f32_32x32x16_bf16 v[112:127], v[188:191], v[76:79], v[112:127]\n\t"
        "v_exp_f32 v105, v105\n\t"
        "v_exp_f32 v106, v106\n\t"
        "v_exp_f32 v107, v107\n\t"
        "v_smfmac_f32_32x32x32_bf16 v[0:15], v[192:195], v[96:103], v148\n\t"
        "v_exp_f32 v108, v108\n\t"
        "v_exp_f32 v109, v109\n\t"
        "v_exp_f32 v110, v110\n\t"
        "v_smfmac_f32_16x16x64_bf16 v[128:131], v[144:147], v[96:103], v148\n\t"
        "buffer_load_dwordx4 v[172:175], v150, s[40:43], s44 offen offset:1024 sc1\n\t"
        "v_exp_f32 v111, v111\n\t"
        "v_exp_f32 v112, v112\n\t"
        "v_exp_f32 v113, v113\n\t"
        "v_smfmac_f32_32x32x32_bf16 v[0:15], v[196:199], v[104:111], v148\n\t"
        "v_exp_f32 v114, v114\n\t"
        "v_exp_f32 v115, v115\n\t"
        "v_exp_f32 v116, v116\n\t"
        "v_smfmac_f32_16x16x64_bf16 v[128:131], v[144:147], v[104:111], v148\n\t"
        "v_exp_f32 v117, v117\n\t"
        "v_mfma_f32_32x32x16_bf16 v[96:111], v[184:187], v[80:83], 0\n\t"
        "v_exp_f32 v118, v118\n\t"
        "v_exp_f32 v119, v119\n\t"
        "v_exp_f32 v120, v120\n\t"
        "v_mfma_f32_32x32x16_bf16 v[96:111], v[188:191], v[84:87], v[96:111]\n\t"
        "v_exp_f32 v121, v121\n\t"
        "v_exp_f32 v122, v122\n\t"
        "v_exp_f32 v123, v123\n\t"
        "v_smfmac_f32_32x32x32_bf16 v[16:31], v[192:195], v[112:119], v148\n\t"
        "v_exp_f32 v124, v124\n\t"
        "v_exp_f32 v125, v125\n\t"
        "v_exp_f32 v126, v126\n\t"
        "v_smfmac_f32_16x16x64_bf16 v[132:135], v[144:147], v[112:119], v148\n\t"
        "buffer_load_dwordx4 v[176:179], v150, s[40:43], s44 offen offset:2048 sc1\n\t"
        "v_exp_f32 v127, v127\n\t"
        "v_exp_f32 v96, v96\n\t"
        "v_exp_f32 v97, v97\n\t"
        "v_smfmac_f32_32x32x32_bf16 v[16:31], v[196:199], v[120:127], v148\n\t"
        "v_exp_f32 v98, v98\n\t"
        "v_exp_f32 v99, v99\n\t"
        "v_exp_f32 v100, v100\n\t"
        "v_smfmac_f32_16x16x64_bf16 v[132:135], v[144:147], v[120:127], v148\n\t"
        "v_exp_f32 v101, v101\n\t"
        "v_mfma_f32_32x32x16_bf16 v[112:127], v[184:187], v[88:91], 0\n\t"
        "v_exp_f32 v102, v102\n\t"
        "v_exp_f32 v103, v103\n\t"
        "v_exp_f32 v104, v104\n\t"
        "v_mfma_f32_32x32x16_bf16 v[112:127], v[188:191], v[92:95], v[112:127]\n\t"
        "v_exp_f32 v105, v105\n\t"
        "v_exp_f32 v106, v106\n\t"
        "v_exp_f32 v107, v107\n\t"
        "v_smfmac_f32_32x32x32_bf16 v[32:47], v[192:195], v[96:103], v148\n\t"
        "v_exp_f32 v108, v108\n\t"
        "v_exp_f32 v109, v109\n\t"
        "v_exp_f32 v110, v110\n\t"
        "v_smfmac_f32_16x16x64_bf16 v[136:139], v[144:147], v[96:103], v148\n\t"
        "buffer_load_dwordx4 v[180:183], v150, s[40:43], s44 offen offset:3072 sc1\n\t"
        "v_exp_f32 v111, v111\n\t"
        "v_exp_f32 v112, v112\n\t"
        "v_exp_f32 v113, v113\n\t"
        "v_smfmac_f32_32x32x32_bf16 v[32:47], v[196:199], v[104:111], v148\n\t"
        "s_add_u32 s44, s44, 0x4000\n\t"
        "s_and_b32 s44, s44, 0x3ffff\n\t"
        "s_cmp_eq_u32 s44, s49\n\t"
        "s_cselect_b32 s44, s50, s44\n\t"
        "v_exp_f32 v114, v114\n\t"
        "v_exp_f32 v115, v115\n\t"
        "v_exp_f32 v116, v116\n\t"
        "v_smfmac_f32_16x16x64_bf16 v[136:139], v[144:147], v[104:111], v148\n\t"
        "v_exp_f32 v117, v117\n\t"
        "s_waitcnt vmcnt(8)\n\t"
        "v_mfma_f32_32x32x16_bf16 v[96:111], v[200:203], v[64:67], 0\n\t"
        "v_exp_f32 v118, v118\n\t"
        "v_exp_f32 v119, v119\n\t"
        "v_exp_f32 v120, v120\n\t"
        "v_mfma_f32_32x32x16_bf16 v[96:111], v[204:207], v[68:71], v[96:111]\n\t"
        "v_exp_f32 v121, v121\n\t"
        "v_exp_f32 v122, v122\n\t"
        "v_exp_f32 v123, v123\n\t"
        "v_smfmac_f32_32x32x32_bf16 v[48:63], v[192:195], v[112:119], v148\n\t"
        "v_exp_f32 v124, v124\n\t"
        "v_exp_f32 v125, v125\n\t"
        "v_exp_f32 v126, v126\n\t"
        "v_smfmac_f32_16x16x64_bf16 v[140:143], v[144:147], v[112:119], v148\n\t"
        "buffer_load_dwordx4 v[184:187], v149, s[40:43], s44 offen offset:0 sc1\n\t"
        "v_exp_f32 v127, v127\n\t"
        "v_exp_f32 v96, v96\n\t"
        "v_exp_f32 v97, v97\n\t"
        "v_smfmac_f32_32x32x32_bf16 v[48:63], v[196:199], v[120:127], v148\n\t"
        "v_exp_f32 v98, v98\n\t"
        "v_exp_f32 v99, v99\n\t"
        "v_exp_f32 v100, v100\n\t"
        "v_smfmac_f32_16x16x64_bf16 v[140:143], v[144:147], v[120:127], v148\n\t"
        "v_exp_f32 v101, v101\n\t"
        "v_mfma_f32_32x32x16_bf16 v[112:127], v[200:203], v[72:75], 0\n\t"
        "v_exp_f32 v102, v102\n\t"
        "v_exp_f32 v103, v103\n\t"
        "v_exp_f32 v104, v104\n\t"
        "v_mfma_f32_32x32x16_bf16 v[112:127], v[204:207], v[76:79], v[112:127]\n\t"
        "v_exp_f32 v105, v105\n\t"
        "v_exp_f32 v106, v106\n\t"
        "v_exp_f32 v107, v107\n\t"
        "v_smfmac_f32_32x32x32_bf16 v[0:15], v[208:211], v[96:103], v148\n\t"
        "v_exp_f32 v108, v108\n\t"
        "v_exp_f32 v109, v109\n\t"
        "v_exp_f32 v110, v110\n\t"
        "v_smfmac_f32_16x16x64_bf16 v[128:131], v[144:147], v[96:103], v148\n\t"
        "buffer_load_dwordx4 v[188:191], v149, s[40:43], s44 offen offset:1024 sc1\n\t"
        "v_exp_f32 v111, v111\n\t"
        "v_exp_f32 v112, v112\n\t"
        "v_exp_f32 v113, v113\n\t"
        "v_smfmac_f32_32x32x32_bf16 v[0:15], v[212:215], v[104:111], v148\n\t"
        "v_exp_f32 v114, v114\n\t"
        "v_exp_f32 v115, v115\n\t"
        "v_exp_f32 v116, v116\n\t"
        "v_smfmac_f32_16x16x64_bf16 v[128:131], v[144:147], v[104:111], v148\n\t"
        "v_exp_f32 v117, v117\n\t"
        "v_mfma_f32_32x32x16_bf16 v[96:111], v[200:203], v[80:83], 0\n\t"
        "v_exp_f32 v118, v118\n\t"
        "v_exp_f32 v119, v119\n\t"
        "v_exp_f32 v120, v120\n\t"
        "v_mfma_f32_32x32x16_bf16 v[96:111], v[204:207], v[84:87], v[96:111]\n\t"
        "v_exp_f32 v121, v121\n\t"
        "v_exp_f32 v122, v122\n\t"
        "v_exp_f32 v123, v123\n\t"
        "v_smfmac_f32_32x32x32_bf16 v[16:31], v[208:211], v[112:119], v148\n\t"
        "v_exp_f32 v124, v124\n\t"
        "v_exp_f32 v125, v125\n\t"
        "v_exp_f32 v126, v126\n\t"
        "v_smfmac_f32_16x16x64_bf16 v[132:135], v[144:147], v[112:119], v148\n\t"
        "buffer_load_dwordx4 v[192:195], v149, s[40:43], s44 offen offset:2048 sc1\n\t"
        "v_exp_f32 v127, v127\n\t"
        "v_exp_f32 v96, v96\n\t"
        "v_exp_f32 v97, v97\n\t"
        "v_smfmac_f32_32x32x32_bf16 v[16:31], v[212:215], v[120:127], v148\n\t"
        "v_exp_f32 v98, v98\n\t"
        "v_exp_f32 v99, v99\n\t"
        "v_exp_f32 v100, v100\n\t"
        "v_smfmac_f32_16x16x64_bf16 v[132:135], v[144:147], v[120:127], v148\n\t"
        "v_exp_f32 v101, v101\n\t"
        "v_mfma_f32_32x32x16_bf16 v[112:127], v[200:203], v[88:91], 0\n\t"
        "v_exp_f32 v102, v102\n\t"
        "v_exp_f32 v103, v103\n\t"
        "v_exp_f32 v104, v104\n\t"
        "v_mfma_f32_32x32x16_bf16 v[112:127], v[204:207], v[92:95], v[112:127]\n\t"
        "v_exp_f32 v105, v105\n\t"
        "v_exp_f32 v106, v106\n\t"
        "v_exp_f32 v107, v107\n\t"
        "v_smfmac_f32_32x32x32_bf16 v[32:47], v[208:211], v[96:103], v148\n\t"
        "v_exp_f32 v108, v108\n\t"
        "v_exp_f32 v109, v109\n\t"
        "v_exp_f32 v110, v110\n\t"
        "v_smfmac_f32_16x16x64_bf16 v[136:139], v[144:147], v[96:103], v148\n\t"
        "buffer_load_dwordx4 v[196:199], v149, s[40:43], s44 offen offset:3072 sc1\n\t"
        "v_exp_f32 v111, v111\n\t"
        "v_exp_f32 v112, v112\n\t"
        "v_exp_f32 v113, v113\n\t"
        "v_smfmac_f32_32x32x32_bf16 v[32:47], v[212:215], v[104:111], v148\n\t"
        "v_exp_f32 v114, v114\n\t"
        "v_exp_f32 v115, v115\n\t"
        "v_exp_f32 v116, v116\n\t"
        "v_smfmac_f32_16x16x64_bf16 v[136:139], v[144:147], v[104:111], v148\n\t"
        "v_exp_f32 v117, v117\n\t"
        "s_waitcnt vmcnt(8)\n\t"
        "v_mfma_f32_32x32x16_bf16 v[96:111], v[152:155], v[64:67], 0\n\t"
        "v_exp_f32 v118, v118\n\t"
        "v_exp_f32 v119, v119\n\t"
        "v_exp_f32 v120, v120\n\t"
        "v_mfma_f32_32x32x16_bf16 v[96:111], v[156:159], v[68:71], v[96:111]\n\t"
        "v_exp_f32 v121, v121\n\t"
        "v_exp_f32 v122, v122\n\t"
        "v_exp_f32 v123, v123\n\t"
        "v_smfmac_f32_32x32x32_bf16 v[48:63], v[208:211], v[112:119], v148\n\t"
        "v_exp_f32 v124, v124\n\t"
        "v_exp_f32 v125, v125\n\t"
        "v_exp_f32 v126, v126\n\t"
        "v_smfmac_f32_16x16x64_bf16 v[140:143], v[144:147], v[112:119], v148\n\t"
        "buffer_load_dwordx4 v[200:203], v150, s[40:43], s44 offen offset:0 sc1\n\t"
        "v_exp_f32 v127, v127\n\t"
        "v_exp_f32 v96, v96\n\t"
        "v_exp_f32 v97, v97\n\t"
        "v_smfmac_f32_32x32x32_bf16 v[48:63], v[212:215], v[120:127], v148\n\t"
        "v_exp_f32 v98, v98\n\t"
        "v_exp_f32 v99, v99\n\t"
        "v_exp_f32 v100, v100\n\t"
        "v_smfmac_f32_16x16x64_bf16 v[140:143], v[144:147], v[120:127], v148\n\t"
        "v_exp_f32 v101, v101\n\t"
        "v_mfma_f32_32x32x16_bf16 v[112:127], v[152:155], v[72:75], 0\n\t"
        "v_exp_f32 v102, v102\n\t"
        "v_exp_f32 v103, v103\n\t"
        "v_exp_f32 v104, v104\n\t"
        "v_mfma_f32_32x32x16_bf16 v[112:127], v[156:159], v[76:79], v[112:127]\n\t"
        "v_exp_f32 v105, v105\n\t"
        "v_exp_f32 v106, v106\n\t"
        "v_exp_f32 v107, v107\n\t"
        "v_smfmac_f32_32x32x32_bf16 v[0:15], v[160:163], v[96:103], v148\n\t"
        "v_exp_f32 v108, v108\n\t"
        "v_exp_f32 v109, v109\n\t"
        "v_exp_f32 v110, v110\n\t"
        "v_smfmac_f32_16x16x64_bf16 v[128:131], v[144:147], v[96:103], v148\n\t"
        "buffer_load_dwordx4 v[204:207], v150, s[40:43], s44 offen offset:1024 sc1\n\t"
        "v_exp_f32 v111, v111\n\t"
        "v_exp_f32 v112, v112\n\t"
        "v_exp_f32 v113, v113\n\t"
        "v_smfmac_f32_32x32x32_bf16 v[0:15], v[164:167], v[104:111], v148\n\t"
        "v_exp_f32 v114, v114\n\t"
        "v_exp_f32 v115, v115\n\t"
        "v_exp_f32 v116, v116\n\t"
        "v_smfmac_f32_16x16x64_bf16 v[128:131], v[144:147], v[104:111], v148\n\t"
        "v_exp_f32 v117, v117\n\t"
        "v_mfma_f32_32x32x16_bf16 v[96:111], v[152:155], v[80:83], 0\n\t"
        "v_exp_f32 v118, v118\n\t"
        "v_exp_f32 v119, v119\n\t"
        "v_exp_f32 v120, v120\n\t"
        "v_mfma_f32_32x32x16_bf16 v[96:111], v[156:159], v[84:87], v[96:111]\n\t"
        "v_exp_f32 v121, v121\n\t"
        "v_exp_f32 v122, v122\n\t"
        "v_exp_f32 v123, v123\n\t"
        "v_smfmac_f32_32x32x32_bf16 v[16:31], v[160:163], v[112:119], v148\n\t"
        "v_exp_f32 v124, v124\n\t"
        "v_exp_f32 v125, v125\n\t"
        "v_exp_f32 v126, v126\n\t"
        "v_smfmac_f32_16x16x64_bf16 v[132:135], v[144:147], v[112:119], v148\n\t"
        "buffer_load_dwordx4 v[208:211], v150, s[40:43], s44 offen offset:2048 sc1\n\t"
        "v_exp_f32 v127, v127\n\t"
        "v_exp_f32 v96, v96\n\t"
        "v_exp_f32 v97, v97\n\t"
        "v_smfmac_f32_32x32x32_bf16 v[16:31], v[164:167], v[120:127], v148\n\t"
        "v_exp_f32 v98, v98\n\t"
        "v_exp_f32 v99, v99\n\t"
        "v_exp_f32 v100, v100\n\t"
        "v_smfmac_f32_16x16x64_bf16 v[132:135], v[144:147], v[120:127], v148\n\t"
        "v_exp_f32 v101, v101\n\t"
        "v_mfma_f32_32x32x16_bf16 v[112:127], v[152:155], v[88:91], 0\n\t"
        "v_exp_f32 v102, v102\n\t"
        "v_exp_f32 v103, v103\n\t"
        "v_exp_f32 v104, v104\n\t"
        "v_mfma_f32_32x32x16_bf16 v[112:127], v[156:159], v[92:95], v[112:127]\n\t"
        "v_exp_f32 v105, v105\n\t"
        "v_exp_f32 v106, v106\n\t"
        "v_exp_f32 v107, v107\n\t"
        "v_smfmac_f32_32x32x32_bf16 v[32:47], v[160:163], v[96:103], v148\n\t"
        "v_exp_f32 v108, v108\n\t"
        "v_exp_f32 v109, v109\n\t"
        "v_exp_f32 v110, v110\n\t"
        "v_smfmac_f32_16x16x64_bf16 v[136:139], v[144:147], v[96:103], v148\n\t"
        "buffer_load_dwordx4 v[212:215], v150, s[40:43], s44 offen offset:3072 sc1\n\t"
        "v_exp_f32 v111, v111\n\t"
        "v_exp_f32 v112, v112\n\t"
        "v_exp_f32 v113, v113\n\t"
        "v_smfmac_f32_32x32x32_bf16 v[32:47], v[164:167], v[104:111], v148\n\t"
        "s_add_u32 s44, s44, 0x4000\n\t"
        "s_and_b32 s44, s44, 0x3ffff\n\t"
        "s_cmp_eq_u32 s44, s49\n\t"
        "s_cselect_b32 s44, s50, s44\n\t"
        "v_exp_f32 v114, v114\n\t"
        "v_exp_f32 v115, v115\n\t"
        "v_exp_f32 v116, v116\n\t"
        "v_smfmac_f32_16x16x64_bf16 v[136:139], v[144:147], v[104:111], v148\n\t"
        "v_exp_f32 v117, v117\n\t"
        "s_waitcnt vmcnt(8)\n\t"
        "v_mfma_f32_32x32x16_bf16 v[96:111], v[168:171], v[64:67], 0\n\t"
        "v_exp_f32 v118, v118\n\t"
        "v_exp_f32 v119, v119\n\t"
        "v_exp_f32 v120, v120\n\t"
        "v_mfma_f32_32x32x16_bf16 v[96:111], v[172:175], v[68:71], v[96:111]\n\t"
        "v_exp_f32 v121, v121\n\t"
        "v_exp_f32 v122, v122\n\t"
        "v_exp_f32 v123, v123\n\t"
        "v_smfmac_f32_32x32x32_bf16 v[48:63], v[160:163], v[112:119], v148\n\t"
        "v_exp_f32 v124, v124\n\t"
        "v_exp_f32 v125, v125\n\t"
        "v_exp_f32 v126, v126\n\t"
        "v_smfmac_f32_16x16x64_bf16 v[140:143], v[144:147], v[112:119], v148\n\t"
        "buffer_load_dwordx4 v[152:155], v149, s[40:43], s44 offen offset:0 sc1\n\t"
        "v_exp_f32 v127, v127\n\t"
        "v_exp_f32 v96, v96\n\t"
        "v_exp_f32 v97, v97\n\t"
        "v_smfmac_f32_32x32x32_bf16 v[48:63], v[164:167], v[120:127], v148\n\t"
        "v_exp_f32 v98, v98\n\t"
        "v_exp_f32 v99, v99\n\t"
        "v_exp_f32 v100, v100\n\t"
        "v_smfmac_f32_16x16x64_bf16 v[140:143], v[144:147], v[120:127], v148\n\t"
        "v_exp_f32 v101, v101\n\t"
        "v_mfma_f32_32x32x16_bf16 v[112:127], v[168:171], v[72:75], 0\n\t"
        "v_exp_f32 v102, v102\n\t"
        "v_exp_f32 v103, v103\n\t"
        "v_exp_f32 v104, v104\n\t"
        "v_mfma_f32_32x32x16_bf16 v[112:127], v[172:175], v[76:79], v[112:127]\n\t"
        "v_exp_f32 v105, v105\n\t"
        "v_exp_f32 v106, v106\n\t"
        "v_exp_f32 v107, v107\n\t"
        "v_smfmac_f32_32x32x32_bf16 v[0:15], v[176:179], v[96:103], v148\n\t"
        "v_exp_f32 v108, v108\n\t"
        "v_exp_f32 v109, v109\n\t"
        "v_exp_f32 v110, v110\n\t"
        "v_smfmac_f32_16x16x64_bf16 v[128:131], v[144:147], v[96:103], v148\n\t"
        "buffer_load_dwordx4 v[156:159], v149, s[40:43], s44 offen offset:1024 sc1\n\t"
        "v_exp_f32 v111, v111\n\t"
        "v_exp_f32 v112, v112\n\t"
        "v_exp_f32 v113, v113\n\t"
        "v_smfmac_f32_32x32x32_bf16 v[0:15], v[180:183], v[104:111], v148\n\t"
        "v_exp_f32 v114, v114\n\t"
        "v_exp_f32 v115, v115\n\t"
        "v_exp_f32 v116, v116\n\t"
        "v_smfmac_f32_16x16x64_bf16 v[128:131], v[144:147], v[104:111], v148\n\t"
        "v_exp_f32 v117, v117\n\t"
        "v_mfma_f32_32x32x16_bf16 v[96:111], v[168:171], v[80:83], 0\n\t"
        "v_exp_f32 v118, v118\n\t"
        "v_exp_f32 v119, v119\n\t"
        "v_exp_f32 v120, v120\n\t"
        "v_mfma_f32_32x32x16_bf16 v[96:111], v[172:175], v[84:87], v[96:111]\n\t"
        "v_exp_f32 v121, v121\n\t"
        "v_exp_f32 v122, v122\n\t"
        "v_exp_f32 v123, v123\n\t"
        "v_smfmac_f32_32x32x32_bf16 v[16:31], v[176:179], v[112:119], v148\n\t"
        "v_exp_f32 v124, v124\n\t"
        "v_exp_f32 v125, v125\n\t"
        "v_exp_f32 v126, v126\n\t"
        "v_smfmac_f32_16x16x64_bf16 v[132:135], v[144:147], v[112:119], v148\n\t"
        "buffer_load_dwordx4 v[160:163], v149, s[40:43], s44 offen offset:2048 sc1\n\t"
        "v_exp_f32 v127, v127\n\t"
        "v_exp_f32 v96, v96\n\t"
        "v_exp_f32 v97, v97\n\t"
        "v_smfmac_f32_32x32x32_bf16 v[16:31], v[180:183], v[120:127], v148\n\t"
        "v_exp_f32 v98, v98\n\t"
        "v_exp_f32 v99, v99\n\t"
        "v_exp_f32 v100, v100\n\t"
        "v_smfmac_f32_16x16x64_bf16 v[132:135], v[144:147], v[120:127], v148\n\t"
        "v_exp_f32 v101, v101\n\t"
        "v_mfma_f32_32x32x16_bf16 v[112:127], v[168:171], v[88:91], 0\n\t"
        "v_exp_f32 v102, v102\n\t"
        "v_exp_f32 v103, v103\n\t"
        "v_exp_f32 v104, v104\n\t"
        "v_mfma_f32_32x32x16_bf16 v[112:127], v[172:175], v[92:95], v[112:127]\n\t"
        "v_exp_f32 v105, v105\n\t"
        "v_exp_f32 v106, v106\n\t"
        "v_exp_f32 v107, v107\n\t"
        "v_smfmac_f32_32x32x32_bf16 v[32:47], v[176:179], v[96:103], v148\n\t"
        "v_exp_f32 v108, v108\n\t"
        "v_exp_f32 v109, v109\n\t"
        "v_exp_f32 v110, v110\n\t"
        "v_smfmac_f32_16x16x64_bf16 v[136:139], v[144:147], v[96:103], v148\n\t"
        "buffer_load_dwordx4 v[164:167], v149, s[40:43], s44 offen offset:3072 sc1\n\t"
        "v_exp_f32 v111, v111\n\t"
        "s_sub_u32 s45, s45, 1\n\t"
        "s_cmp_lg_u32 s45, 0\n\t"
        "s_cbranch_scc1 1b\n\t"
        "v_exp_f32 v112, v112\n\t"
        "v_exp_f32 v113, v113\n\t"
        "v_smfmac_f32_32x32x32_bf16 v[32:47], v[180:183], v[104:111], v148\n\t"
        "v_exp_f32 v114, v114\n\t"
        "v_exp_f32 v115, v115\n\t"
        "v_exp_f32 v116, v116\n\t"
        "v_smfmac_f32_16x16x64_bf16 v[136:139], v[144:147], v[104:111], v148\n\t"
        "v_exp_f32 v117, v117\n\t"
        "s_waitcnt vmcnt(8)\n\t"
        "v_mfma_f32_32x32x16_bf16 v[96:111], v[184:187], v[64:67], 0\n\t"
        "v_exp_f32 v118, v118\n\t"
        "v_exp_f32 v119, v119\n\t"
        "v_exp_f32 v120, v120\n\t"
        "v_mfma_f32_32x32x16_bf16 v[96:111], v[188:191], v[68:71], v[96:111]\n\t"
        "v_exp_f32 v121, v121\n\t"
        "v_exp_f32 v122, v122\n\t"
        "v_exp_f32 v123, v123\n\t"
        "v_smfmac_f32_32x32x32_bf16 v[48:63], v[176:179], v[112:119], v148\n\t"
        "v_exp_f32 v124, v124\n\t"
        "v_exp_f32 v125, v125\n\t"
        "v_exp_f32 v126, v126\n\t"
        "v_smfmac_f32_16x16x64_bf16 v[140:143], v[144:147], v[112:119], v148\n\t"
        "v_exp_f32 v127, v127\n\t"
        "v_exp_f32 v96, v96\n\t"
        "v_exp_f32 v97, v97\n\t"
        "v_smfmac_f32_32x32x32_bf16 v[48:63], v[180:183], v[120:127], v148\n\t"
        "v_exp_f32 v98, v98\n\t"
        "v_exp_f32 v99, v99\n\t"
        "v_exp_f32 v100, v100\n\t"
        "v_smfmac_f32_16x16x64_bf16 v[140:143], v[144:147], v[120:127], v148\n\t"
        "v_exp_f32 v101, v101\n\t"
        "v_mfma_f32_32x32x16_bf16 v[112:127], v[184:187], v[72:75], 0\n\t"
        "v_exp_f32 v102, v102\n\t"
        "v_exp_f32 v103, v103\n\t"
        "v_exp_f32 v104, v104\n\t"
        "v_mfma_f32_32x32x16_bf16 v[112:127], v[188:191], v[76:79], v[112:127]\n\t"
        "v_exp_f32 v105, v105\n\t"
        "v_exp_f32 v106, v106\n\t"
        "v_exp_f32 v107, v107\n\t"
        "v_smfmac_f32_32x32x32_bf16 v[0:15], v[192:195], v[96:103], v148\n\t"
        "v_exp_f32 v108, v108\n\t"
        "v_exp_f32 v109, v109\n\t"
        "v_exp_f32 v110, v110\n\t"
        "v_smfmac_f32_16x16x64_bf16 v[128:131], v[144:147], v[96:103], v148\n\t"
        "v_exp_f32 v111, v111\n\t"
        "v_exp_f32 v112, v112\n\t"
        "v_exp_f32 v113, v113\n\t"
        "v_smfmac_f32_32x32x32_bf16 v[0:15], v[196:199], v[104:111], v148\n\t"
        "v_exp_f32 v114, v114\n\t"
        "v_exp_f32 v115, v115\n\t"
        "v_exp_f32 v116, v116\n\t"
        "v_smfmac_f32_16x16x64_bf16 v[128:131], v[144:147], v[104:111], v148\n\t"
        "v_exp_f32 v117, v117\n\t"
        "s_waitcnt vmcnt(4)\n\t"
        "v_mfma_f32_32x32x16_bf16 v[96:111], v[200:203], v[64:67], 0\n\t"
        "v_exp_f32 v118, v118\n\t"
        "v_exp_f32 v119, v119\n\t"
        "v_exp_f32 v120, v120\n\t"
        "v_mfma_f32_32x32x16_bf16 v[96:111], v[204:207], v[68:71], v[96:111]\n\t"
        "v_exp_f32 v121, v121\n\t"
        "v_exp_f32 v122, v122\n\t"
        "v_exp_f32 v123, v123\n\t"
        "v_smfmac_f32_32x32x32_bf16 v[16:31], v[192:195], v[112:119], v148\n\t"
        "v_exp_f32 v124, v124\n\t"
        "v_exp_f32 v125, v125\n\t"
        "v_exp_f32 v126, v126\n\t"
        "v_smfmac_f32_16x16x64_bf16 v[132:135], v[144:147], v[112:119], v148\n\t"
        "v_exp_f32 v127, v127\n\t"
        "s_cmp_lg_u32 s47, 0\n\t"
        "s_cbranch_scc1 4f\n\t"
        "global_load_dwordx4 %[r0], v223, %[xb] offset:0\n\t"
        "global_load_dwordx4 %[r1], v223, %[xb] offset:32\n\t"
        "global_load_dwordx4 %[r2], v223, %[xb] offset:64\n\t"
        "global_load_dwordx4 %[r3], v223, %[xb] offset:96\n\t"
        "4:\n\t"
        "v_exp_f32 v96, v96\n\t"
        "v_exp_f32 v97, v97\n\t"
        "v_smfmac_f32_32x32x32_bf16 v[16:31], v[196:199], v[120:127], v148\n\t"
        "v_exp_f32 v98, v98\n\t"
        "v_exp_f32 v99, v99\n\t"
        "v_exp_f32 v100, v100\n\t"
        "v_smfmac_f32_16x16x64_bf16 v[132:135], v[144:147], v[120:127], v148\n\t"
        "v_exp_f32 v101, v101\n\t"
        "v_mfma_f32_32x32x16_bf16 v[112:127], v[200:203], v[72:75], 0\n\t"
        "v_exp_f32 v102, v102\n\t"
        "v_exp_f32 v103, v103\n\t"
        "v_exp_f32 v104, v104\n\t"
        "v_mfma_f32_32x32x16_bf16 v[112:127], v[204:207], v[76:79], v[112:127]\n\t"
        "v_exp_f32 v105, v105\n\t"
        "v_exp_f32 v106, v106\n\t"
        "v_exp_f32 v107, v107\n\t"
        "v_smfmac_f32_32x32x32_bf16 v[0:15], v[208:211], v[96:103], v148\n\t"
        "v_exp_f32 v108, v108\n\t"
        "v_exp_f32 v109, v109\n\t"
        "v_exp_f32 v110, v110\n\t"
        "v_smfmac_f32_16x16x64_bf16 v[128:131], v[144:147], v[96:103], v148\n\t"
        "v_exp_f32 v111, v111\n\t"
        "v_exp_f32 v112, v112\n\t"
        "v_exp_f32 v113, v113\n\t"
        "v_smfmac_f32_32x32x32_bf16 v[0:15], v[212:215], v[104:111], v148\n\t"
        "v_exp_f32 v114, v114\n\t"
        "v_exp_f32 v115, v115\n\t"
        "v_exp_f32 v116, v116\n\t"
        "v_smfmac_f32_16x16x64_bf16 v[128:131], v[144:147], v[104:111], v148\n\t"
        "v_exp_f32 v117, v117\n\t"
        "v_mfma_f32_32x32x16_bf16 v[96:111], v[184:187], v[80:83], 0\n\t"
        "v_exp_f32 v118, v118\n\t"
        "v_exp_f32 v119, v119\n\t"
        "v_exp_f32 v120, v120\n\t"
        "ds_write_b128 v216, v[0:3] offset:0\n\t"
        "v_mfma_f32_32x32x16_bf16 v[96:111], v[188:191], v[84:87], v[96:111]\n\t"
        "v_exp_f32 v121, v121\n\t"
        "ds_write_b128 v216, v[4:7] offset:1024\n\t"
        "v_exp_f32 v122, v122\n\t"
        "ds_write_b128 v216, v[8:11] offset:2048\n\t"
        "v_exp_f32 v123, v123\n\t"
        "ds_write_b128 v216, v[12:15] offset:3072\n\t"
        "v_smfmac_f32_32x32x32_bf16 v[16:31], v[208:211], v[112:119], v148\n\t"
        "v_exp_f32 v124, v124\n\t"
        "ds_write_b32 v218, v128 offset:0\n\t"
        "v_exp_f32 v125, v125\n\t"
        "v_exp_f32 v126, v126\n\t"
        "v_smfmac_f32_16x16x64_bf16 v[132:135], v[144:147], v[112:119], v148\n\t"
        "v_exp_f32 v127, v127\n\t"
        "v_exp_f32 v96, v96\n\t"
        "v_exp_f32 v97, v97\n\t"
        "v_smfmac_f32_32x32x32_bf16 v[16:31], v[212:215], v[120:127], v148\n\t"
        "v_exp_f32 v98, v98\n\t"
        "v_exp_f32 v99, v99\n\t"
        "v_exp_f32 v100, v100\n\t"
        "v_smfmac_f32_16x16x64_bf16 v[132:135], v[144:147], v[120:127], v148\n\t"
        "v_exp_f32 v101, v101\n\t"
        "v_mfma_f32_32x32x16_bf16 v[112:127], v[184:187], v[88:91], 0\n\t"
        "v_exp_f32 v102, v102\n\t"
        "v_exp_f32 v103, v103\n\t"
        "v_exp_f32 v104, v104\n\t"
        "ds_write_b128 v216, v[16:19] offset:4096\n\t"
        "v_mfma_f32_32x32x16_bf16 v[112:127], v[188:191], v[92:95], v[112:127]\n\t"
        "v_exp_f32 v105, v105\n\t"
        "ds_write_b128 v216, v[20:23] offset:5120\n\t"
        "v_exp_f32 v106, v106\n\t"
        "ds_write_b128 v216, v[24:27] offset:6144\n\t"
        "v_exp_f32 v107, v107\n\t"
        "ds_write_b128 v216, v[28:31] offset:7168\n\t"
        "v_smfmac_f32_32x32x32_bf16 v[32:47], v[192:195], v[96:103], v148\n\t"
        "v_exp_f32 v108, v108\n\t"
        "ds_write_b32 v218, v132 offset:256\n\t"
        "v_exp_f32 v109, v109\n\t"
        "ds_write_b32 v220, v222\n\t"
        "v_exp_f32 v110, v110\n\t"
        "v_smfmac_f32_16x16x64_bf16 v[136:139], v[144:147], v[96:103], v148\n\t"
        "v_exp_f32 v111, v111\n\t"
        "v_exp_f32 v112, v112\n\t"
        "v_exp_f32 v113, v113\n\t"
        "v_smfmac_f32_32x32x32_bf16 v[32:47], v[196:199], v[104:111], v148\n\t"
        "v_exp_f32 v114, v114\n\t"
        "v_exp_f32 v115, v115\n\t"
        "v_exp_f32 v116, v116\n\t"
        "v_smfmac_f32_16x16x64_bf16 v[136:139], v[144:147], v[104:111], v148\n\t"
        "v_exp_f32 v117, v117\n\t"
        "v_mfma_f32_32x32x16_bf16 v[96:111], v[200:203], v[80:83], 0\n\t"
        "v_exp_f32 v118, v118\n\t"
        "v_exp_f32 v119, v119\n\t"
        "v_exp_f32 v120, v120\n\t"
        "v_mfma_f32_32x32x16_bf16 v[96:111], v[204:207], v[84:87], v[96:111]\n\t"
        "v_exp_f32 v121, v121\n\t"
        "v_exp_f32 v122, v122\n\t"
        "v_exp_f32 v123, v123\n\t"
        "v_smfmac_f32_32x32x32_bf16 v[48:63], v[192:195], v[112:119], v148\n\t"
        "v_exp_f32 v124, v124\n\t"
        "v_exp_f32 v125, v125\n\t"
        "v_exp_f32 v126, v126\n\t"
        "v_smfmac_f32_16x16x64_bf16 v[140:143], v[144:147], v[112:119], v148\n\t"
        "v_exp_f32 v127, v127\n\t"
        "v_exp_f32 v96, v96\n\t"
        "v_exp_f32 v97, v97\n\t"
        "v_smfmac_f32_32x32x32_bf16 v[48:63], v[196:199], v[120:127], v148\n\t"
        "v_exp_f32 v98, v98\n\t"
        "v_exp_f32 v99, v99\n\t"
        "v_exp_f32 v100, v100\n\t"
        "v_smfmac_f32_16x16x64_bf16 v[140:143], v[144:147], v[120:127], v148\n\t"
        "v_exp_f32 v101, v101\n\t"
        "v_mfma_f32_32x32x16_bf16 v[112:127], v[200:203], v[88:91], 0\n\t"
        "v_exp_f32 v102, v102\n\t"
        "v_exp_f32 v103, v103\n\t"
        "v_exp_f32 v104, v104\n\t"
        "ds_read_b32 v151, v221\n\t"
        "v_mfma_f32_32x32x16_bf16 v[112:127], v[204:207], v[92:95], v[112:127]\n\t"
        "v_exp_f32 v105, v105\n\t"
        "v_exp_f32 v106, v106\n\t"
        "v_exp_f32 v107, v107\n\t"
        "v_smfmac_f32_32x32x32_bf16 v[32:47], v[208:211], v[96:103], v148\n\t"
        "v_exp_f32 v108, v108\n\t"
        "v_exp_f32 v109, v109\n\t"
        "v_exp_f32 v110, v110\n\t"
        "v_smfmac_f32_16x16x64_bf16 v[136:139], v[144:147], v[96:103], v148\n\t"
        "v_exp_f32 v111, v111\n\t"
        "s_waitcnt lgkmcnt(0)\n\t"
        "v_cmp_ne_u32 vcc, v222, v151\n\t"
        "s_cbranch_vccnz 40f\n\t"
        "41:\n\t"
        "v_exp_f32 v112, v112\n\t"
        "ds_read_b128 v[0:3], v217 offset:0\n\t"
        "v_exp_f32 v113, v113\n\t"
        "ds_read_b128 v[4:7], v217 offset:1024\n\t"
        "v_smfmac_f32_32x32x32_bf16 v[32:47], v[212:215], v[104:111], v148\n\t"
        "v_exp_f32 v114, v114\n\t"
        "ds_read_b128 v[8:11], v217 offset:2048\n\t"
        "v_exp_f32 v115, v115\n\t"
        "ds_read_b128 v[12:15], v217 offset:3072\n\t"
        "v_exp_f32 v116, v116\n\t"
        "ds_read_b32 v128, v219 offset:0\n\t"
        "v_smfmac_f32_16x16x64_bf16 v[136:139], v[144:147], v[104:111], v148\n\t"
        "v_exp_f32 v117, v117\n\t"
        "ds_read_b128 v[16:19], v217 offset:4096\n\t"
        "v_exp_f32 v118, v118\n\t"
        "ds_read_b128 v[20:23], v217 offset:5120\n\t"
        "v_exp_f32 v119, v119\n\t"
        "ds_read_b128 v[24:27], v217 offset:6144\n\t"
        "v_exp_f32 v120, v120\n\t"
        "ds_read_b128 v[28:31], v217 offset:7168\n\t"
        "v_exp_f32 v121, v121\n\t"
        "ds_read_b32 v132, v219 offset:256\n\t"
        "v_exp_f32 v122, v122\n\t"
        "v_exp_f32 v123, v123\n\t"
        "v_smfmac_f32_32x32x32_bf16 v[48:63], v[208:211], v[112:119], v148\n\t"
        "v_exp_f32 v124, v124\n\t"
        "v_exp_f32 v125, v125\n\t"
        "v_exp_f32 v126, v126\n\t"
        "v_smfmac_f32_16x16x64_bf16 v[140:143], v[144:147], v[112:119], v148\n\t"
        "v_exp_f32 v127, v127\n\t"
        "s_nop 1\n\t"
        "v_smfmac_f32_32x32x32_bf16 v[48:63], v[212:215], v[120:127], v148\n\t"
        "v_smfmac_f32_16x16x64_bf16 v[140:143], v[144:147], v[120:127], v148\n\t"
        "s_waitcnt vmcnt(0) lgkmcnt(0)\n\t"
        "s_nop 6\n\t"
        "s_branch 9f\n\t"
        "24:\n\t"
        "s_add_u32 s44, s44, 0x4000\n\t"
        "s_and_b32 s44, s44, 0x3ffff\n\t"
        "s_cmp_eq_u32 s44, s49\n\t"
        "s_cselect_b32 s44, s50, s44\n\t"
        "buffer_load_dwordx4 v[184:187], v149, s[40:43], s44 offen offset:0 sc1\n\t"
        "buffer_load_dwordx4 v[188:191], v149, s[40:43], s44 offen offset:1024 sc1\n\t"
        "buffer_load_dwordx4 v[192:195], v149, s[40:43], s44 offen offset:2048 sc1\n\t"
        "buffer_load_dwordx4 v[196:199], v149, s[40:43], s44 offen offset:3072 sc1\n\t"
        "buffer_load_dwordx4 v[200:203], v150, s[40:43], s44 offen offset:0 sc1\n\t"
        "buffer_load_dwordx4 v[204:207], v150, s[40:43], s44 offen offset:1024 sc1\n\t"
        "buffer_load_dwordx4 v[208:211], v150, s[40:43], s44 offen offset:2048 sc1\n\t"
        "buffer_load_dwordx4 v[212:215], v150, s[40:43], s44 offen offset:3072 sc1\n\t"
        "s_mov_b32 s46, 1\n\t"
        "s_branch 25b\n\t"
        "26:\n\t"
        "s_add_u32 s44, s44, 0x4000\n\t"
        "s_and_b32 s44, s44, 0x3ffff\n\t"
        "s_cmp_eq_u32 s44, s49\n\t"
        "s_cselect_b32 s44, s50, s44\n\t"
        "buffer_load_dwordx4 v[184:187], v149, s[40:43], s44 offen offset:0 sc1\n\t"
        "buffer_load_dwordx4 v[188:191], v149, s[40:43], s44 offen offset:1024 sc1\n\t"
        "buffer_load_dwordx4 v[192:195], v149, s[40:43], s44 offen offset:2048 sc1\n\t"
        "buffer_load_dwordx4 v[196:199], v149, s[40:43], s44 offen offset:3072 sc1\n\t"
        "buffer_load_dwordx4 v[200:203], v150, s[40:43], s44 offen offset:0 sc1\n\t"
        "buffer_load_dwordx4 v[204:207], v150, s[40:43], s44 offen offset:1024 sc1\n\t"
        "buffer_load_dwordx4 v[208:211], v150, s[40:43], s44 offen offset:2048 sc1\n\t"
        "buffer_load_dwordx4 v[212:215], v150, s[40:43], s44 offen offset:3072 sc1\n\t"
        "s_mov_b32 s46, 1\n\t"
        "s_branch 27b\n\t"
        "28:\n\t"
        "s_add_u32 s44, s44, 0x4000\n\t"
        "s_and_b32 s44, s44, 0x3ffff\n\t"
        "s_cmp_eq_u32 s44, s49\n\t"
        "s_cselect_b32 s44, s50, s44\n\t"
        "buffer_load_dwordx4 v[184:187], v149, s[40:43], s44 offen offset:0 sc1\n\t"
        "buffer_load_dwordx4 v[188:191], v149, s[40:43], s44 offen offset:1024 sc1\n\t"
        "buffer_load_dwordx4 v[192:195], v149, s[40:43], s44 offen offset:2048 sc1\n\t"
        "buffer_load_dwordx4 v[196:199], v149, s[40:43], s44 offen offset:3072 sc1\n\t"
        "buffer_load_dwordx4 v[200:203], v150, s[40:43], s44 offen offset:0 sc1\n\t"
        "buffer_load_dwordx4 v[204:207], v150, s[40:43], s44 offen offset:1024 sc1\n\t"
        "buffer_load_dwordx4 v[208:211], v150, s[40:43], s44 offen offset:2048 sc1\n\t"
        "buffer_load_dwordx4 v[212:215], v150, s[40:43], s44 offen offset:3072 sc1\n\t"
        "s_mov_b32 s46, 1\n\t"
        "s_branch 29b\n\t"
        "30:\n\t"
        "s_add_u32 s44, s44, 0x4000\n\t"
        "s_and_b32 s44, s44, 0x3ffff\n\t"
        "s_cmp_eq_u32 s44, s49\n\t"
        "s_cselect_b32 s44, s50, s44\n\t"
        "buffer_load_dwordx4 v[184:187], v149, s[40:43], s44 offen offset:0 sc1\n\t"
        "buffer_load_dwordx4 v[188:191], v149, s[40:43], s44 offen offset:1024 sc1\n\t"
        "buffer_load_dwordx4 v[192:195], v149, s[40:43], s44 offen offset:2048 sc1\n\t"
        "buffer_load_dwordx4 v[196:199], v149, s[40:43], s44 offen offset:3072 sc1\n\t"
        "buffer_load_dwordx4 v[200:203], v150, s[40:43], s44 offen offset:0 sc1\n\t"
        "buffer_load_dwordx4 v[204:207], v150, s[40:43], s44 offen offset:1024 sc1\n\t"
        "buffer_load_dwordx4 v[208:211], v150, s[40:43], s44 offen offset:2048 sc1\n\t"
        "buffer_load_dwordx4 v[212:215], v150, s[40:43], s44 offen offset:3072 sc1\n\t"
        "s_mov_b32 s46, 1\n\t"
        "s_branch 31b\n\t"
        "32:\n\t"
        "s_add_u32 s44, s44, 0x4000\n\t"
        "s_and_b32 s44, s44, 0x3ffff\n\t"
        "s_cmp_eq_u32 s44, s49\n\t"
        "s_cselect_b32 s44, s50, s44\n\t"
        "buffer_load_dwordx4 v[184:187], v149, s[40:43], s44 offen offset:0 sc1\n\t"
        "buffer_load_dwordx4 v[188:191], v149, s[40:43], s44 offen offset:1024 sc1\n\t"
        "buffer_load_dwordx4 v[192:195], v149, s[40:43], s44 offen offset:2048 sc1\n\t"
        "buffer_load_dwordx4 v[196:199], v149, s[40:43], s44 offen offset:3072 sc1\n\t"
        "buffer_load_dwordx4 v[200:203], v150, s[40:43], s44 offen offset:0 sc1\n\t"
        "buffer_load_dwordx4 v[204:207], v150, s[40:43], s44 offen offset:1024 sc1\n\t"
        "buffer_load_dwordx4 v[208:211], v150, s[40:43], s44 offen offset:2048 sc1\n\t"
        "buffer_load_dwordx4 v[212:215], v150, s[40:43], s44 offen offset:3072 sc1\n\t"
        "s_mov_b32 s46, 1\n\t"
        "s_branch 33b\n\t"
        "40:\n\t"
        "s_mov_b32 s45, 0x100000\n\t"
        "42:\n\t"
        "ds_read_b32 v151, v221\n\t"
        "s_sub_u32 s45, s45, 1\n\t"
        "s_waitcnt lgkmcnt(0)\n\t"
        "s_cbranch_scc1 41b\n\t"
        "v_cmp_ne_u32 vcc, v222, v151\n\t"
        "s_cbranch_vccnz 42b\n\t"
        "s_branch 41b\n\t"
        "9:\n\t"
        : "+{v[0:15]}"(o0), "+{v[16:31]}"(o1), "+{v[32:47]}"(o2), "+{v[48:63]}"(o3), "+{v[128:143]}"(lv), "+{s44}"(soff), "+{v[152:167]}"(kva), "+{v[168:183]}"(kvb), "+{v[184:191]}"(fv),
          [r0] "+v"(x1p[0]), [r1] "+v"(x1p[1]), [r2] "+v"(x1p[2]), [r3] "+v"(x1p[3]) STAMP_OUTS
        : "{v[64:71]}"(q[0]), "{v[72:79]}"(q[1]), "{v[80:87]}"(q[2]), "{v[88:95]}"(q[3]), "{v[144:147]}"(ones), "{v148}"(0xddddu), "{v149}"(kvoff), "{v150}"(kvoff + 4096),
          "{v[216:223]}"(cv), "{v224}"(lbase + L_W + 16), "{s[40:43]}"(srd), "{s47}"(L), "{s49}"(tm * 16384), "{s50}"(((tm + 1) & 15) * 16384), [xb] "s"(x0)
        : "v96", "v97", "v98", "v99", "v100", "v101", "v102", "v103", "v104", "v105", "v106", "v107", "v108", "v109", "v110", "v111", "v112", "v113", "v114", "v115", "v116", "v117", "v118", "v119", "v120", "v121", "v122", "v123", "v124", "v125", "v126", "v127", "v151", "v192", "v193", "v194", "v195", "v196", "v197", "v198", "v199", "v200", "v201", "v202", "v203", "v204", "v205", "v206", "v207", "v208", "v209", "v210", "v211", "v212", "v213", "v214", "v215", "s45", "s46", "vcc", "scc", "memory");
      STAMP_PUT(6 + 16 * L);
      STAMPV(9 + 16 * L);
      PHASE_VARS;
      const unsigned dst = OFF_OF + L * (5 * MBY) + (tm * 4 + 2 * kh) * TS + (tn * 2 + hh) * 2048 + lane * 16;
      auto put = [&](int u, const f32x16& o) {
#pragma unroll
        for (int c = 0; c < 2; ++c) {
          u32x4 w;
#pragma unroll
          for (int e = 0; e < 4; ++e) w[e] = pack2(o[8 * c + 2 * e], o[8 * c + 2 * e + 1]);
          stg(rs, dst + u * TS + c * 1024, w);
        }
      };
      const float la = lv[8] + lv[0], lb = lv[12] + lv[4];
      o2 = (o2 + o0) * RCP(la); put(0, o2);
      SBAR();
      o3 = (o3 + o1) * RCP(lb); put(1, o3);
      SBAR();
      {
        const f32x16 t = o2 + o3;
        float sum = 0.f;
#pragma unroll
        for (int e = 0; e < 16; e += 4) sum += (t[e] + t[e + 1]) + (t[e + 2] + t[e + 3]);
        if (__any(!(fabsf(sum) < 1e37f) | !(fmaxf(la, lb) < 1e27f) | !(fminf(la, lb) > 1e-9f) | !(la + lb == la + lb))) {
          DRAIN();
          attn_safe(rs, kvoff - kh * 8192, q[2], ones, o2); put(0, o2);
          attn_safe(rs, kvoff - kh * 8192, q[3], ones, o3); put(1, o3);
        }
      }
    }
    STAMPV(10 + 16 * L);
    {
      f32x16 acc[8];
      u32x2 wrc[8];
      STAMP_DECL;
      { PHASE_VARS;
      float r0 = 0.f, r1 = 1.f;
      if (L == 0) {
#pragma unroll
        for (int i = 0; i < 4; ++i) x1p[i] = fo_swap(x1p[i]);
      } else {
        r0 = reinterpret_cast<const float*>(smem + L_AB)[256 + wid * 64 + r32]; r1 = reinterpret_cast<const float*>(smem + L_AB)[256 + wid * 64 + 32 + r32];
      }
#pragma unroll
      for (int e = 0; e < 16; ++e) { acc[e >> 3][2 * (e & 7)] = lo16(x1p[e >> 2][e & 3]) * r1 + r0; acc[e >> 3][2 * (e & 7) + 1] = hi16(x1p[e >> 2][e & 3]) * r1 + r0; }
      PIN(acc[0]); PIN(acc[1]);
      asm volatile("s_waitcnt vmcnt(0)\n\tds_write_b32 %0, %1" :: "v"(lbase + L_W + 80 + 4 * wid), "v"(2 * L + 2) : "memory");
      STAMPV(11 + 16 * L);
      const unsigned a0 = lbase + wrow * 128 + ((wch ^ wsw) << 4) + 8 * (tp & 1), a1 = lbase + wrow * 128 + (((4 + wch) ^ wsw) << 4) + 8 * (tp & 1);
      asm volatile(
        "s_mov_b32 s46, 0\n\t"
        "ostart_%=:\n\t"
        "s_mov_b32 s48, 0x200000\n\t"
        "2:\n\t"
        "ds_read_b32 v233, v232\n\t"
        "s_sub_u32 s48, s48, 1\n\t"
        "s_waitcnt lgkmcnt(0)\n\t"
        "s_cselect_b32 s47, 15, 0\n\t"
        "v_readfirstlane_b32 s51, v233\n\t"
        "s_nop 3\n\t"
        "s_or_b32 s51, s51, s47\n\t"
        "s_andn2_b32 s47, s51, s46\n\t"
        "s_cbranch_scc0 2b\n\t"
        "s_andn2_b32 s51, s51, s46\n\t"
        "s_ff1_i32_b32 s47, s51\n\t"
        "s_add_u32 s45, s47, s49\n\t"
        "s_and_b32 s45, s45, 3\n\t"
        "s_lshl_b32 s45, s45, 14\n\t"
        "s_or_b32 s45, s45, s50\n\t"
        "s_and_b32 s51, s47, 0x80000000\n\t"
        "v_or_b32 v225, s51, v224\n\t"
        "s_and_b32 s51, s47, 3\n\t"
        "s_lshl_b32 s51, s51, 15\n\t"
        "v_add_u32 v230, s51, v226\n\t"
        "v_add_u32 v231, s51, v227\n\t"
        "buffer_load_dwordx4 v[160:163], v225, s[40:43], s45 offen offset:0 sc1\n\t"
        "buffer_load_dwordx4 v[164:167], v225, s[40:43], s45 offen offset:1024 sc1\n\t"
        "buffer_load_dwordx4 v[168:171], v225, s[40:43], s45 offen offset:2048 sc1\n\t"
        "buffer_load_dwordx4 v[172:175], v225, s[40:43], s45 offen offset:3072 sc1\n\t"
        "s_add_u32 s51, s45, 0x1000\n\t"
        "s_and_b32 s51, s51, 0x3fff\n\t"
        "s_andn2_b32 s45, s45, 0x3fff\n\t"
        "s_or_b32 s45, s45, s51\n\t"
        "buffer_load_dwordx4 v[176:179], v225, s[40:43], s45 offen offset:0 sc1\n\t"
        "buffer_load_dwordx4 v[180:183], v225, s[40:43], s45 offen offset:1024 sc1\n\t"
        "buffer_load_dwordx4 v[184:187], v225, s[40:43], s45 offen offset:2048 sc1\n\t"
        "buffer_load_dwordx4 v[188:191], v225, s[40:43], s45 offen offset:3072 sc1\n\t"
        "s_add_u32 s51, s45, 0x1000\n\t"
        "s_and_b32 s51, s51, 0x3fff\n\t"
        "s_andn2_b32 s45, s45, 0x3fff\n\t"
        "s_or_b32 s45, s45, s51\n\t"
        "buffer_load_dwordx4 v[192:195], v225, s[40:43], s45 offen offset:0 sc1\n\t"
        "buffer_load_dwordx4 v[196:199], v225, s[40:43], s45 offen offset:1024 sc1\n\t"
        "buffer_load_dwordx4 v[200:203], v225, s[40:43], s45 offen offset:2048 sc1\n\t"
        "buffer_load_dwordx4 v[204:207], v225, s[40:43], s45 offen offset:3072 sc1\n\t"
        "ds_read_b64_tr_b16 v[128:129], v230 offset:0\n\t"
        "ds_read_b64_tr_b16 v[130:131], v230 offset:1024\n\t"
        "ds_read_b64_tr_b16 v[132:133], v231 offset:0\n\t"
        "ds_read_b64_tr_b16 v[134:135], v231 offset:1024\n\t"
        "ds_read_b64_tr_b16 v[136:137], v230 offset:2048\n\t"
        "ds_read_b64_tr_b16 v[138:139], v230 offset:3072\n\t"
        "ds_read_b64_tr_b16 v[140:141], v231 offset:2048\n\t"
        "ds_read_b64_tr_b16 v[142:143], v231 offset:3072\n\t"
        "ds_read_b64_tr_b16 v[144:145], v230 offset:4096\n\t"
        "ds_read_b64_tr_b16 v[146:147], v230 offset:5120\n\t"
        "ds_read_b64_tr_b16 v[148:149], v231 offset:4096\n\t"
        "ds_read_b64_tr_b16 v[150:151], v231 offset:5120\n\t"
        "ds_read_b64_tr_b16 v[152:153], v230 offset:6144\n\t"
        "ds_read_b64_tr_b16 v[154:155], v230 offset:7168\n\t"
        "ds_read_b64_tr_b16 v[156:157], v231 offset:6144\n\t"
        "ds_read_b64_tr_b16 v[158:159], v231 offset:7168\n\t"
        "s_mov_b32 s44, s45\n\t"
        "s_cmp_eq_u32 s47, 0\n\t"
        "s_cbranch_scc1 oseg0_%=\n\t"
        "s_cmp_eq_u32 s47, 1\n\t"
        "s_cbranch_scc1 oseg1_%=\n\t"
        "s_cmp_eq_u32 s47, 2\n\t"
        "s_cbranch_scc1 oseg2_%=\n\t"
        "s_cmp_eq_u32 s47, 3\n\t"
        "s_cbranch_scc1 oseg3_%=\n\t"
        "oseg0_%=:\n\t"
        "s_or_b32 s46, s46, 1\n\t"
        "s_waitcnt vmcnt(11) lgkmcnt(14)\n\t"
        "v_mfma_f32_32x32x16_bf16 v[0:15], v[128:131], v[160:163], v[0:15]\n\t"
        "ds_read_b64_tr_b16 v[128:129], v226 offset:8192\n\t"
        "ds_read_b64_tr_b16 v[130:131], v226 offset:9216\n\t"
        "s_waitcnt lgkmcnt(14)\n\t"
        "v_mfma_f32_32x32x16_bf16 v[16:31], v[132:135], v[160:163], v[16:31]\n\t"
        "ds_read_b64_tr_b16 v[132:133], v227 offset:8192\n\t"
        "ds_read_b64_tr_b16 v[134:135], v227 offset:9216\n\t"
        "s_add_u32 s51, s44, 0x1000\n\t"
        "s_and_b32 s51, s51, 0x3fff\n\t"
        "s_andn2_b32 s44, s44, 0x3fff\n\t"
        "s_or_b32 s44, s44, s51\n\t"
        "buffer_load_dwordx4 v[208:211], v224, s[40:43], s44 offen offset:0 sc1\n\t"
        "s_waitcnt vmcnt(11) lgkmcnt(14)\n\t"
        "v_mfma_f32_32x32x16_bf16 v[0:15], v[136:139], v[164:167], v[0:15]\n\t"
        "ds_read_b32 v233, v232\n\t"
        "ds_read_b64_tr_b16 v[136:137], v226 offset:10240\n\t"
        "ds_read_b64_tr_b16 v[138:139], v226 offset:11264\n\t"
        "s_waitcnt lgkmcnt(14)\n\t"
        "v_mfma_f32_32x32x16_bf16 v[16:31], v[140:143], v[164:167], v[16:31]\n\t"
        "ds_read_b64_tr_b16 v[140:141], v227 offset:10240\n\t"
        "ds_read_b64_tr_b16 v[142:143], v227 offset:11264\n\t"
        "buffer_load_dwordx4 v[212:215], v224, s[40:43], s44 offen offset:1024 sc1\n\t"
        "s_waitcnt vmcnt(11) lgkmcnt(14)\n\t"
        "v_mfma_f32_32x32x16_bf16 v[0:15], v[144:147], v[168:171], v[0:15]\n\t"
        "ds_read_b64_tr_b16 v[144:145], v226 offset:12288\n\t"
        "ds_read_b64_tr_b16 v[146:147], v226 offset:13312\n\t"
        "s_waitcnt lgkmcnt(14)\n\t"
        "v_mfma_f32_32x32x16_bf16 v[16:31], v[148:151], v[168:171], v[16:31]\n\t"
        "ds_read_b64_tr_b16 v[148:149], v227 offset:12288\n\t"
        "ds_read_b64_tr_b16 v[150:151], v227 offset:13312\n\t"
        "buffer_load_dwordx4 v[216:219], v224, s[40:43], s44 offen offset:2048 sc1\n\t"
        "s_waitcnt vmcnt(11) lgkmcnt(8)\n\t"
        "v_mfma_f32_32x32x16_bf16 v[0:15], v[152:155], v[172:175], v[0:15]\n\t"
        "v_readfirstlane_b32 s51, v233\n\t"
        "ds_read_b64_tr_b16 v[152:153], v226 offset:14336\n\t"
        "ds_read_b64_tr_b16 v[154:155], v226 offset:15360\n\t"
        "s_waitcnt lgkmcnt(14)\n\t"
        "v_mfma_f32_32x32x16_bf16 v[16:31], v[156:159], v[172:175], v[16:31]\n\t"
        "s_andn2_b32 s51, s51, s46\n\t"
        "s_ff1_i32_b32 s47, s51\n\t"
        "s_add_u32 s45, s47, s49\n\t"
        "s_and_b32 s45, s45, 3\n\t"
        "s_lshl_b32 s45, s45, 14\n\t"
        "s_or_b32 s45, s45, s50\n\t"
        "ds_read_b64_tr_b16 v[156:157], v227 offset:14336\n\t"
        "ds_read_b64_tr_b16 v[158:159], v227 offset:15360\n\t"
        "buffer_load_dwordx4 v[220:223], v224, s[40:43], s44 offen offset:3072 sc1\n\t"
        "s_waitcnt vmcnt(11) lgkmcnt(14)\n\t"
        "v_mfma_f32_32x32x16_bf16 v[0:15], v[128:131], v[176:179], v[0:15]\n\t"
        "s_and_b32 s51, s47, 0x80000000\n\t"
        "v_or_b32 v225, s51, v224\n\t"
        "s_and_b32 s51, s47, 3\n\t"
        "s_lshl_b32 s51, s51, 15\n\t"
        "v_add_u32 v230, s51, v226\n\t"
        "v_add_u32 v231, s51, v227\n\t"
        "ds_read_b64_tr_b16 v[128:129], v226 offset:16384\n\t"
        "ds_read_b64_tr_b16 v[130:131], v226 offset:17408\n\t"
        "s_waitcnt lgkmcnt(14)\n\t"
        "v_mfma_f32_32x32x16_bf16 v[16:31], v[132:135], v[176:179], v[16:31]\n\t"
        "ds_read_b64_tr_b16 v[132:133], v227 offset:16384\n\t"
        "ds_read_b64_tr_b16 v[134:135], v227 offset:17408\n\t"
        "buffer_load_dwordx4 v[160:163], v225, s[40:43], s45 offen offset:0 sc1\n\t"
        "s_waitcnt vmcnt(11) lgkmcnt(14)\n\t"
        "v_mfma_f32_32x32x16_bf16 v[0:15], v[136:139], v[180:183], v[0:15]\n\t"
        "ds_read_b64_tr_b16 v[136:137], v226 offset:18432\n\t"
        "ds_read_b64_tr_b16 v[138:139], v226 offset:19456\n\t"
        "s_waitcnt lgkmcnt(14)\n\t"
        "v_mfma_f32_32x32x16_bf16 v[16:31], v[140:143], v[180:183], v[16:31]\n\t"
        "ds_read_b64_tr_b16 v[140:141], v227 offset:18432\n\t"
        "ds_read_b64_tr_b16 v[142:143], v227 offset:19456\n\t"
        "buffer_load_dwordx4 v[164:167], v225, s[40:43], s45 offen offset:1024 sc1\n\t"
        "s_waitcnt vmcnt(11) lgkmcnt(14)\n\t"
        "v_mfma_f32_32x32x16_bf16 v[0:15], v[144:147], v[184:187], v[0:15]\n\t"
        "ds_read_b64_tr_b16 v[144:145], v226 offset:20480\n\t"
        "ds_read_b64_tr_b16 v[146:147], v226 offset:21504\n\t"
        "s_waitcnt lgkmcnt(14)\n\t"
        "v_mfma_f32_32x32x16_bf16 v[16:31], v[148:151], v[184:187], v[16:31]\n\t"
        "ds_read_b64_tr_b16 v[148:149], v227 offset:20480\n\t"
        "ds_read_b64_tr_b16 v[150:151], v227 offset:21504\n\t"
        "buffer_load_dwordx4 v[168:171], v225, s[40:43], s45 offen offset:2048 sc1\n\t"
        "s_waitcnt vmcnt(11) lgkmcnt(14)\n\t"
        "v_mfma_f32_32x32x16_bf16 v[0:15], v[152:155], v[188:191], v[0:15]\n\t"
        "ds_read_b64_tr_b16 v[152:153], v226 offset:22528\n\t"
        "ds_read_b64_tr_b16 v[154:155], v226 offset:23552\n\t"
        "s_waitcnt lgkmcnt(14)\n\t"
        "v_mfma_f32_32x32x16_bf16 v[16:31], v[156:159], v[188:191], v[16:31]\n\t"
        "ds_read_b64_tr_b16 v[156:157], v227 offset:22528\n\t"
        "ds_read_b64_tr_b16 v[158:159], v227 offset:23552\n\t"
        "buffer_load_dwordx4 v[172:175], v225, s[40:43], s45 offen offset:3072 sc1\n\t"
        "s_waitcnt vmcnt(11) lgkmcnt(14)\n\t"
        "v_mfma_f32_32x32x16_bf16 v[0:15], v[128:131], v[192:195], v[0:15]\n\t"
        "ds_read_b64_tr_b16 v[128:129], v226 offset:24576\n\t"
        "ds_read_b64_tr_b16 v[130:131], v226 offset:25600\n\t"
        "s_waitcnt lgkmcnt(14)\n\t"
        "v_mfma_f32_32x32x16_bf16 v[16:31], v[132:135], v[192:195], v[16:31]\n\t"
        "ds_read_b64_tr_b16 v[132:133], v227 offset:24576\n\t"
        "ds_read_b64_tr_b16 v[134:135], v227 offset:25600\n\t"
        "s_add_u32 s51, s45, 0x1000\n\t"
        "s_and_b32 s51, s51, 0x3fff\n\t"
        "s_andn2_b32 s45, s45, 0x3fff\n\t"
        "s_or_b32 s45, s45, s51\n\t"
        "buffer_load_dwordx4 v[176:179], v225, s[40:43], s45 offen offset:0 sc1\n\t"
        "s_waitcnt vmcnt(11) lgkmcnt(14)\n\t"
        "v_mfma_f32_32x32x16_bf16 v[0:15], v[136:139], v[196:199], v[0:15]\n\t"
        "ds_read_b64_tr_b16 v[136:137], v226 offset:26624\n\t"
        "ds_read_b64_tr_b16 v[138:139], v226 offset:27648\n\t"
        "s_waitcnt lgkmcnt(14)\n\t"
        "v_mfma_f32_32x32x16_bf16 v[16:31], v[140:143], v[196:199], v[16:31]\n\t"
        "ds_read_b64_tr_b16 v[140:141], v227 offset:26624\n\t"
        "ds_read_b64_tr_b16 v[142:143], v227 offset:27648\n\t"
        "buffer_load_dwordx4 v[180:183], v225, s[40:43], s45 offen offset:1024 sc1\n\t"
        "s_waitcnt vmcnt(11) lgkmcnt(14)\n\t"
        "v_mfma_f32_32x32x16_bf16 v[0:15], v[144:147], v[200:203], v[0:15]\n\t"
        "ds_read_b64_tr_b16 v[144:145], v226 offset:28672\n\t"
        "ds_read_b64_tr_b16 v[146:147], v226 offset:29696\n\t"
        "s_waitcnt lgkmcnt(14)\n\t"
        "v_mfma_f32_32x32x16_bf16 v[16:31], v[148:151], v[200:203], v[16:31]\n\t"
        "ds_read_b64_tr_b16 v[148:149], v227 offset:28672\n\t"
        "ds_read_b64_tr_b16 v[150:151], v227 offset:29696\n\t"
        "buffer_load_dwordx4 v[184:187], v225, s[40:43], s45 offen offset:2048 sc1\n\t"
        "s_waitcnt vmcnt(11) lgkmcnt(14)\n\t"
        "v_mfma_f32_32x32x16_bf16 v[0:15], v[152:155], v[204:207], v[0:15]\n\t"
        "ds_read_b64_tr_b16 v[152:153], v226 offset:30720\n\t"
        "ds_read_b64_tr_b16 v[154:155], v226 offset:31744\n\t"
        "s_waitcnt lgkmcnt(14)\n\t"
        "v_mfma_f32_32x32x16_bf16 v[16:31], v[156:159], v[204:207], v[16:31]\n\t"
        "ds_read_b64_tr_b16 v[156:157], v227 offset:30720\n\t"
        "ds_read_b64_tr_b16 v[158:159], v227 offset:31744\n\t"
        "buffer_load_dwordx4 v[188:191], v225, s[40:43], s45 offen offset:3072 sc1\n\t"
        "s_waitcnt vmcnt(11) lgkmcnt(14)\n\t"
        "v_mfma_f32_32x32x16_bf16 v[0:15], v[128:131], v[208:211], v[0:15]\n\t"
        "ds_read_b64_tr_b16 v[128:129], v230 offset:0\n\t"
        "ds_read_b64_tr_b16 v[130:131], v230 offset:1024\n\t"
        "s_waitcnt lgkmcnt(14)\n\t"
        "v_mfma_f32_32x32x16_bf16 v[16:31], v[132:135], v[208:211], v[16:31]\n\t"
        "ds_read_b64_tr_b16 v[132:133], v231 offset:0\n\t"
        "ds_read_b64_tr_b16 v[134:135], v231 offset:1024\n\t"
        "s_add_u32 s51, s45, 0x1000\n\t"
        "s_and_b32 s51, s51, 0x3fff\n\t"
        "s_andn2_b32 s45, s45, 0x3fff\n\t"
        "s_or_b32 s45, s45, s51\n\t"
        "buffer_load_dwordx4 v[192:195], v225, s[40:43], s45 offen offset:0 sc1\n\t"
        "s_waitcnt vmcnt(11) lgkmcnt(14)\n\t"
        "v_mfma_f32_32x32x16_bf16 v[0:15], v[136:139], v[212:215], v[0:15]\n\t"
        "ds_read_b64_tr_b16 v[136:137], v230 offset:2048\n\t"
        "ds_read_b64_tr_b16 v[138:139], v230 offset:3072\n\t"
        "s_waitcnt lgkmcnt(14)\n\t"
        "v_mfma_f32_32x32x16_bf16 v[16:31], v[140:143], v[212:215], v[16:31]\n\t"
        "ds_read_b64_tr_b16 v[140:141], v231 offset:2048\n\t"
        "ds_read_b64_tr_b16 v[142:143], v231 offset:3072\n\t"
        "buffer_load_dwordx4 v[196:199], v225, s[40:43], s45 offen offset:1024 sc1\n\t"
        "s_waitcnt vmcnt(11) lgkmcnt(14)\n\t"
        "v_mfma_f32_32x32x16_bf16 v[0:15], v[144:147], v[216:219], v[0:15]\n\t"
        "ds_read_b64_tr_b16 v[144:145], v230 offset:4096\n\t"
        "ds_read_b64_tr_b16 v[146:147], v230 offset:5120\n\t"
        "s_waitcnt lgkmcnt(14)\n\t"
        "v_mfma_f32_32x32x16_bf16 v[16:31], v[148:151], v[216:219], v[16:31]\n\t"
        "ds_read_b64_tr_b16 v[148:149], v231 offset:4096\n\t"
        "ds_read_b64_tr_b16 v[150:151], v231 offset:5120\n\t"
        "buffer_load_dwordx4 v[200:203], v225, s[40:43], s45 offen offset:2048 sc1\n\t"
        "s_waitcnt vmcnt(11) lgkmcnt(14)\n\t"
        "v_mfma_f32_32x32x16_bf16 v[0:15], v[152:155], v[220:223], v[0:15]\n\t"
        "ds_read_b64_tr_b16 v[152:153], v230 offset:6144\n\t"
        "ds_read_b64_tr_b16 v[154:155], v230 offset:7168\n\t"
        "s_waitcnt lgkmcnt(14)\n\t"
        "v_mfma_f32_32x32x16_bf16 v[16:31], v[156:159], v[220:223], v[16:31]\n\t"
        "ds_read_b64_tr_b16 v[156:157], v231 offset:6144\n\t"
        "ds_read_b64_tr_b16 v[158:159], v231 offset:7168\n\t"
        "buffer_load_dwordx4 v[204:207], v225, s[40:43], s45 offen offset:3072 sc1\n\t"
        "s_mov_b32 s44, s45\n\t"
        "s_cmp_eq_u32 s47, 0\n\t"
        "s_cbranch_scc1 oseg0_%=\n\t"
        "s_cmp_eq_u32 s47, 1\n\t"
        "s_cbranch_scc1 oseg1_%=\n\t"
        "s_cmp_eq_u32 s47, 2\n\t"
        "s_cbranch_scc1 oseg2_%=\n\t"
        "s_cmp_eq_u32 s47, 3\n\t"
        "s_cbranch_scc1 oseg3_%=\n\t"
        "s_cmp_lg_u32 s46, 15\n\t"
        "s_cbranch_scc1 ostart_%=\n\t"
        "s_branch oend_%=\n\t"
        "oseg1_%=:\n\t"
        "s_or_b32 s46, s46, 2\n\t"
        "s_waitcnt vmcnt(11) lgkmcnt(14)\n\t"
        "v_mfma_f32_32x32x16_bf16 v[32:47], v[128:131], v[160:163], 0\n\t"
        "ds_read_b64_tr_b16 v[128:129], v226 offset:40960\n\t"
        "ds_read_b64_tr_b16 v[130:131], v226 offset:41984\n\t"
        "s_waitcnt lgkmcnt(14)\n\t"
        "v_mfma_f32_32x32x16_bf16 v[48:63], v[132:135], v[160:163], 0\n\t"
        "ds_read_b64_tr_b16 v[132:133], v227 offset:40960\n\t"
        "ds_read_b64_tr_b16 v[134:135], v227 offset:41984\n\t"
        "s_add_u32 s51, s44, 0x1000\n\t"
        "s_and_b32 s51, s51, 0x3fff\n\t"
        "s_andn2_b32 s44, s44, 0x3fff\n\t"
        "s_or_b32 s44, s44, s51\n\t"
        "buffer_load_dwordx4 v[208:211], v224, s[40:43], s44 offen offset:0 sc1\n\t"
        "s_waitcnt vmcnt(11) lgkmcnt(14)\n\t"
        "v_mfma_f32_32x32x16_bf16 v[32:47], v[136:139], v[164:167], v[32:47]\n\t"
        "ds_read_b32 v233, v232\n\t"
        "ds_read_b64_tr_b16 v[136:137], v226 offset:43008\n\t"
        "ds_read_b64_tr_b16 v[138:139], v226 offset:44032\n\t"
        "s_waitcnt lgkmcnt(14)\n\t"
        "v_mfma_f32_32x32x16_bf16 v[48:63], v[140:143], v[164:167], v[48:63]\n\t"
        "ds_read_b64_tr_b16 v[140:141], v227 offset:43008\n\t"
        "ds_read_b64_tr_b16 v[142:143], v227 offset:44032\n\t"
        "buffer_load_dwordx4 v[212:215], v224, s[40:43], s44 offen offset:1024 sc1\n\t"
        "s_waitcnt vmcnt(11) lgkmcnt(14)\n\t"
        "v_mfma_f32_32x32x16_bf16 v[32:47], v[144:147], v[168:171], v[32:47]\n\t"
        "ds_read_b64_tr_b16 v[144:145], v226 offset:45056\n\t"
        "ds_read_b64_tr_b16 v[146:147], v226 offset:46080\n\t"
        "s_waitcnt lgkmcnt(14)\n\t"
        "v_mfma_f32_32x32x16_bf16 v[48:63], v[148:151], v[168:171], v[48:63]\n\t"
        "ds_read_b64_tr_b16 v[148:149], v227 offset:45056\n\t"
        "ds_read_b64_tr_b16 v[150:151], v227 offset:46080\n\t"
        "buffer_load_dwordx4 v[216:219], v224, s[40:43], s44 offen offset:2048 sc1\n\t"
        "s_waitcnt vmcnt(11) lgkmcnt(8)\n\t"
        "v_mfma_f32_32x32x16_bf16 v[32:47], v[152:155], v[172:175], v[32:47]\n\t"
        "v_readfirstlane_b32 s51, v233\n\t"
        "ds_read_b64_tr_b16 v[152:153], v226 offset:47104\n\t"
        "ds_read_b64_tr_b16 v[154:155], v226 offset:48128\n\t"
        "s_waitcnt lgkmcnt(14)\n\t"
        "v_mfma_f32_32x32x16_bf16 v[48:63], v[156:159], v[172:175], v[48:63]\n\t"
        "s_andn2_b32 s51, s51, s46\n\t"
        "s_ff1_i32_b32 s47, s51\n\t"
        "s_add_u32 s45, s47, s49\n\t"
        "s_and_b32 s45, s45, 3\n\t"
        "s_lshl_b32 s45, s45, 14\n\t"
        "s_or_b32 s45, s45, s50\n\t"
        "ds_read_b64_tr_b16 v[156:157], v227 offset:47104\n\t"
        "ds_read_b64_tr_b16 v[158:159], v227 offset:48128\n\t"
        "buffer_load_dwordx4 v[220:223], v224, s[40:43], s44 offen offset:3072 sc1\n\t"
        "s_waitcnt vmcnt(11) lgkmcnt(14)\n\t"
        "v_mfma_f32_32x32x16_bf16 v[32:47], v[128:131], v[176:179], v[32:47]\n\t"
        "s_and_b32 s51, s47, 0x80000000\n\t"
        "v_or_b32 v225, s51, v224\n\t"
        "s_and_b32 s51, s47, 3\n\t"
        "s_lshl_b32 s51, s51, 15\n\t"
        "v_add_u32 v230, s51, v226\n\t"
        "v_add_u32 v231, s51, v227\n\t"
        "ds_read_b64_tr_b16 v[128:129], v226 offset:49152\n\t"
        "ds_read_b64_tr_b16 v[130:131], v226 offset:50176\n\t"
        "s_waitcnt lgkmcnt(14)\n\t"
        "v_mfma_f32_32x32x16_bf16 v[48:63], v[132:135], v[176:179], v[48:63]\n\t"
        "ds_read_b64_tr_b16 v[132:133], v227 offset:49152\n\t"
        "ds_read_b64_tr_b16 v[134:135], v227 offset:50176\n\t"
        "buffer_load_dwordx4 v[160:163], v225, s[40:43], s45 offen offset:0 sc1\n\t"
        "s_waitcnt vmcnt(11) lgkmcnt(14)\n\t"
        "v_mfma_f32_32x32x16_bf16 v[32:47], v[136:139], v[180:183], v[32:47]\n\t"
        "ds_read_b64_tr_b16 v[136:137], v226 offset:51200\n\t"
        "ds_read_b64_tr_b16 v[138:139], v226 offset:52224\n\t"
        "s_waitcnt lgkmcnt(14)\n\t"
        "v_mfma_f32_32x32x16_bf16 v[48:63], v[140:143], v[180:183], v[48:63]\n\t"
        "ds_read_b64_tr_b16 v[140:141], v227 offset:51200\n\t"
        "ds_read_b64_tr_b16 v[142:143], v227 offset:52224\n\t"
        "buffer_load_dwordx4 v[164:167], v225, s[40:43], s45 offen offset:1024 sc1\n\t"
        "s_waitcnt vmcnt(11) lgkmcnt(14)\n\t"
        "v_mfma_f32_32x32x16_bf16 v[32:47], v[144:147], v[184:187], v[32:47]\n\t"
        "ds_read_b64_tr_b16 v[144:145], v226 offset:53248\n\t"
        "ds_read_b64_tr_b16 v[146:147], v226 offset:54272\n\t"
        "s_waitcnt lgkmcnt(14)\n\t"
        "v_mfma_f32_32x32x16_bf16 v[48:63], v[148:151], v[184:187], v[48:63]\n\t"
        "ds_read_b64_tr_b16 v[148:149], v227 offset:53248\n\t"
        "ds_read_b64_tr_b16 v[150:151], v227 offset:54272\n\t"
        "buffer_load_dwordx4 v[168:171], v225, s[40:43], s45 offen offset:2048 sc1\n\t"
        "s_waitcnt vmcnt(11) lgkmcnt(14)\n\t"
        "v_mfma_f32_32x32x16_bf16 v[32:47], v[152:155], v[188:191], v[32:47]\n\t"
        "ds_read_b64_tr_b16 v[152:153], v226 offset:55296\n\t"
        "ds_read_b64_tr_b16 v[154:155], v226 offset:56320\n\t"
        "s_waitcnt lgkmcnt(14)\n\t"
        "v_mfma_f32_32x32x16_bf16 v[48:63], v[156:159], v[188:191], v[48:63]\n\t"
        "ds_read_b64_tr_b16 v[156:157], v227 offset:55296\n\t"
        "ds_read_b64_tr_b16 v[158:159], v227 offset:56320\n\t"
        "buffer_load_dwordx4 v[172:175], v225, s[40:43], s45 offen offset:3072 sc1\n\t"
        "s_waitcnt vmcnt(11) lgkmcnt(14)\n\t"
        "v_mfma_f32_32x32x16_bf16 v[32:47], v[128:131], v[192:195], v[32:47]\n\t"
        "ds_read_b64_tr_b16 v[128:129], v226 offset:57344\n\t"
        "ds_read_b64_tr_b16 v[130:131], v226 offset:58368\n\t"
        "s_waitcnt lgkmcnt(14)\n\t"
        "v_mfma_f32_32x32x16_bf16 v[48:63], v[132:135], v[192:195], v[48:63]\n\t"
        "ds_read_b64_tr_b16 v[132:133], v227 offset:57344\n\t"
        "ds_read_b64_tr_b16 v[134:135], v227 offset:58368\n\t"
        "s_add_u32 s51, s45, 0x1000\n\t"
        "s_and_b32 s51, s51, 0x3fff\n\t"
        "s_andn2_b32 s45, s45, 0x3fff\n\t"
        "s_or_b32 s45, s45, s51\n\t"
        "buffer_load_dwordx4 v[176:179], v225, s[40:43], s45 offen offset:0 sc1\n\t"
        "s_waitcnt vmcnt(11) lgkmcnt(14)\n\t"
        "v_mfma_f32_32x32x16_bf16 v[32:47], v[136:139], v[196:199], v[32:47]\n\t"
        "ds_read_b64_tr_b16 v[136:137], v226 offset:59392\n\t"
        "ds_read_b64_tr_b16 v[138:139], v226 offset:60416\n\t"
        "s_waitcnt lgkmcnt(14)\n\t"
        "v_mfma_f32_32x32x16_bf16 v[48:63], v[140:143], v[196:199], v[48:63]\n\t"
        "ds_read_b64_tr_b16 v[140:141], v227 offset:59392\n\t"
        "ds_read_b64_tr_b16 v[142:143], v227 offset:60416\n\t"
        "buffer_load_dwordx4 v[180:183], v225, s[40:43], s45 offen offset:1024 sc1\n\t"
        "s_waitcnt vmcnt(11) lgkmcnt(14)\n\t"
        "v_mfma_f32_32x32x16_bf16 v[32:47], v[144:147], v[200:203], v[32:47]\n\t"
        "ds_read_b64_tr_b16 v[144:145], v226 offset:61440\n\t"
        "ds_read_b64_tr_b16 v[146:147], v226 offset:62464\n\t"
        "s_waitcnt lgkmcnt(14)\n\t"
        "v_mfma_f32_32x32x16_bf16 v[48:63], v[148:151], v[200:203], v[48:63]\n\t"
        "ds_read_b64_tr_b16 v[148:149], v227 offset:61440\n\t"
        "ds_read_b64_tr_b16 v[150:151], v227 offset:62464\n\t"
        "buffer_load_dwordx4 v[184:187], v225, s[40:43], s45 offen offset:2048 sc1\n\t"
        "s_waitcnt vmcnt(11) lgkmcnt(14)\n\t"
        "v_mfma_f32_32x32x16_bf16 v[32:47], v[152:155], v[204:207], v[32:47]\n\t"
        "ds_read_b64_tr_b16 v[152:153], v226 offset:63488\n\t"
        "ds_read_b64_tr_b16 v[154:155], v226 offset:64512\n\t"
        "s_waitcnt lgkmcnt(14)\n\t"
        "v_mfma_f32_32x32x16_bf16 v[48:63], v[156:159], v[204:207], v[48:63]\n\t"
        "ds_read_b64_tr_b16 v[156:157], v227 offset:63488\n\t"
        "ds_read_b64_tr_b16 v[158:159], v227 offset:64512\n\t"
        "buffer_load_dwordx4 v[188:191], v225, s[40:43], s45 offen offset:3072 sc1\n\t"
        "s_waitcnt vmcnt(11) lgkmcnt(14)\n\t"
        "v_mfma_f32_32x32x16_bf16 v[32:47], v[128:131], v[208:211], v[32:47]\n\t"
        "ds_read_b64_tr_b16 v[128:129], v230 offset:0\n\t"
        "ds_read_b64_tr_b16 v[130:131], v230 offset:1024\n\t"
        "s_waitcnt lgkmcnt(14)\n\t"
        "v_mfma_f32_32x32x16_bf16 v[48:63], v[132:135], v[208:211], v[48:63]\n\t"
        "ds_read_b64_tr_b16 v[132:133], v231 offset:0\n\t"
        "ds_read_b64_tr_b16 v[134:135], v231 offset:1024\n\t"
        "s_add_u32 s51, s45, 0x1000\n\t"
        "s_and_b32 s51, s51, 0x3fff\n\t"
        "s_andn2_b32 s45, s45, 0x3fff\n\t"
        "s_or_b32 s45, s45, s51\n\t"
        "buffer_load_dwordx4 v[192:195], v225, s[40:43], s45 offen offset:0 sc1\n\t"
        "s_waitcnt vmcnt(11) lgkmcnt(14)\n\t"
        "v_mfma_f32_32x32x16_bf16 v[32:47], v[136:139], v[212:215], v[32:47]\n\t"
        "ds_read_b64_tr_b16 v[136:137], v230 offset:2048\n\t"
        "ds_read_b64_tr_b16 v[138:139], v230 offset:3072\n\t"
        "s_waitcnt lgkmcnt(14)\n\t"
        "v_mfma_f32_32x32x16_bf16 v[48:63], v[140:143], v[212:215], v[48:63]\n\t"
        "ds_read_b64_tr_b16 v[140:141], v231 offset:2048\n\t"
        "ds_read_b64_tr_b16 v[142:143], v231 offset:3072\n\t"
        "buffer_load_dwordx4 v[196:199], v225, s[40:43], s45 offen offset:1024 sc1\n\t"
        "s_waitcnt vmcnt(11) lgkmcnt(14)\n\t"
        "v_mfma_f32_32x32x16_bf16 v[32:47], v[144:147], v[216:219], v[32:47]\n\t"
        "ds_read_b64_tr_b16 v[144:145], v230 offset:4096\n\t"
        "ds_read_b64_tr_b16 v[146:147], v230 offset:5120\n\t"
        "s_waitcnt lgkmcnt(14)\n\t"
        "v_mfma_f32_32x32x16_bf16 v[48:63], v[148:151], v[216:219], v[48:63]\n\t"
        "ds_read_b64_tr_b16 v[148:149], v231 offset:4096\n\t"
        "ds_read_b64_tr_b16 v[150:151], v231 offset:5120\n\t"
        "buffer_load_dwordx4 v[200:203], v225, s[40:43], s45 offen offset:2048 sc1\n\t"
        "s_waitcnt vmcnt(11) lgkmcnt(14)\n\t"
        "v_mfma_f32_32x32x16_bf16 v[32:47], v[152:155], v[220:223], v[32:47]\n\t"
        "ds_read_b64_tr_b16 v[152:153], v230 offset:6144\n\t"
        "ds_read_b64_tr_b16 v[154:155], v230 offset:7168\n\t"
        "s_waitcnt lgkmcnt(14)\n\t"
        "v_mfma_f32_32x32x16_bf16 v[48:63], v[156:159], v[220:223], v[48:63]\n\t"
        "ds_read_b64_tr_b16 v[156:157], v231 offset:6144\n\t"
        "ds_read_b64_tr_b16 v[158:159], v231 offset:7168\n\t"
        "buffer_load_dwordx4 v[204:207], v225, s[40:43], s45 offen offset:3072 sc1\n\t"
        "s_mov_b32 s44, s45\n\t"
        "s_cmp_eq_u32 s47, 0\n\t"
        "s_cbranch_scc1 oseg0_%=\n\t"
        "s_cmp_eq_u32 s47, 1\n\t"
        "s_cbranch_scc1 oseg1_%=\n\t"
        "s_cmp_eq_u32 s47, 2\n\t"
        "s_cbranch_scc1 oseg2_%=\n\t"
        "s_cmp_eq_u32 s47, 3\n\t"
        "s_cbranch_scc1 oseg3_%=\n\t"
        "s_cmp_lg_u32 s46, 15\n\t"
        "s_cbranch_scc1 ostart_%=\n\t"
        "s_branch oend_%=\n\t"
        "oseg2_%=:\n\t"
        "s_or_b32 s46, s46, 4\n\t"
        "s_waitcnt vmcnt(11) lgkmcnt(14)\n\t"
        "v_mfma_f32_32x32x16_bf16 v[64:79], v[128:131], v[160:163], 0\n\t"
        "ds_read_b64_tr_b16 v[128:129], v228 offset:8192\n\t"
        "ds_read_b64_tr_b16 v[130:131], v228 offset:9216\n\t"
        "s_waitcnt lgkmcnt(14)\n\t"
        "v_mfma_f32_32x32x16_bf16 v[80:95], v[132:135], v[160:163], 0\n\t"
        "ds_read_b64_tr_b16 v[132:133], v229 offset:8192\n\t"
        "ds_read_b64_tr_b16 v[134:135], v229 offset:9216\n\t"
        "s_add_u32 s51, s44, 0x1000\n\t"
        "s_and_b32 s51, s51, 0x3fff\n\t"
        "s_andn2_b32 s44, s44, 0x3fff\n\t"
        "s_or_b32 s44, s44, s51\n\t"
        "buffer_load_dwordx4 v[208:211], v224, s[40:43], s44 offen offset:0 sc1\n\t"
        "s_waitcnt vmcnt(11) lgkmcnt(14)\n\t"
        "v_mfma_f32_32x32x16_bf16 v[64:79], v[136:139], v[164:167], v[64:79]\n\t"
        "ds_read_b32 v233, v232\n\t"
        "ds_read_b64_tr_b16 v[136:137], v228 offset:10240\n\t"
        "ds_read_b64_tr_b16 v[138:139], v228 offset:11264\n\t"
        "s_waitcnt lgkmcnt(14)\n\t"
        "v_mfma_f32_32x32x16_bf16 v[80:95], v[140:143], v[164:167], v[80:95]\n\t"
        "ds_read_b64_tr_b16 v[140:141], v229 offset:10240\n\t"
        "ds_read_b64_tr_b16 v[142:143], v229 offset:11264\n\t"
        "buffer_load_dwordx4 v[212:215], v224, s[40:43], s44 offen offset:1024 sc1\n\t"
        "s_waitcnt vmcnt(11) lgkmcnt(14)\n\t"
        "v_mfma_f32_32x32x16_bf16 v[64:79], v[144:147], v[168:171], v[64:79]\n\t"
        "ds_read_b64_tr_b16 v[144:145], v228 offset:12288\n\t"
        "ds_read_b64_tr_b16 v[146:147], v228 offset:13312\n\t"
        "s_waitcnt lgkmcnt(14)\n\t"
        "v_mfma_f32_32x32x16_bf16 v[80:95], v[148:151], v[168:171], v[80:95]\n\t"
        "ds_read_b64_tr_b16 v[148:149], v229 offset:12288\n\t"
        "ds_read_b64_tr_b16 v[150:151], v229 offset:13312\n\t"
        "buffer_load_dwordx4 v[216:219], v224, s[40:43], s44 offen offset:2048 sc1\n\t"
        "s_waitcnt vmcnt(11) lgkmcnt(8)\n\t"
        "v_mfma_f32_32x32x16_bf16 v[64:79], v[152:155], v[172:175], v[64:79]\n\t"
        "v_readfirstlane_b32 s51, v233\n\t"
        "ds_read_b64_tr_b16 v[152:153], v228 offset:14336\n\t"
        "ds_read_b64_tr_b16 v[154:155], v228 offset:15360\n\t"
        "s_waitcnt lgkmcnt(14)\n\t"
        "v_mfma_f32_32x32x16_bf16 v[80:95], v[156:159], v[172:175], v[80:95]\n\t"
        "s_andn2_b32 s51, s51, s46\n\t"
        "s_ff1_i32_b32 s47, s51\n\t"
        "s_add_u32 s45, s47, s49\n\t"
        "s_and_b32 s45, s45, 3\n\t"
        "s_lshl_b32 s45, s45, 14\n\t"
        "s_or_b32 s45, s45, s50\n\t"
        "ds_read_b64_tr_b16 v[156:157], v229 offset:14336\n\t"
        "ds_read_b64_tr_b16 v[158:159], v229 offset:15360\n\t"
        "buffer_load_dwordx4 v[220:223], v224, s[40:43], s44 offen offset:3072 sc1\n\t"
        "s_waitcnt vmcnt(11) lgkmcnt(14)\n\t"
        "v_mfma_f32_32x32x16_bf16 v[64:79], v[128:131], v[176:179], v[64:79]\n\t"
        "s_and_b32 s51, s47, 0x80000000\n\t"
        "v_or_b32 v225, s51, v224\n\t"
        "s_and_b32 s51, s47, 3\n\t"
        "s_lshl_b32 s51, s51, 15\n\t"
        "v_add_u32 v230, s51, v226\n\t"
        "v_add_u32 v231, s51, v227\n\t"
        "ds_read_b64_tr_b16 v[128:129], v228 offset:16384\n\t"
        "ds_read_b64_tr_b16 v[130:131], v228 offset:17408\n\t"
        "s_waitcnt lgkmcnt(14)\n\t"
        "v_mfma_f32_32x32x16_bf16 v[80:95], v[132:135], v[176:179], v[80:95]\n\t"
        "ds_read_b64_tr_b16 v[132:133], v229 offset:16384\n\t"
        "ds_read_b64_tr_b16 v[134:135], v229 offset:17408\n\t"
        "buffer_load_dwordx4 v[160:163], v225, s[40:43], s45 offen offset:0 sc1\n\t"
        "s_waitcnt vmcnt(11) lgkmcnt(14)\n\t"
        "v_mfma_f32_32x32x16_bf16 v[64:79], v[136:139], v[180:183], v[64:79]\n\t"
        "ds_read_b64_tr_b16 v[136:137], v228 offset:18432\n\t"
        "ds_read_b64_tr_b16 v[138:139], v228 offset:19456\n\t"
        "s_waitcnt lgkmcnt(14)\n\t"
        "v_mfma_f32_32x32x16_bf16 v[80:95], v[140:143], v[180:183], v[80:95]\n\t"
        "ds_read_b64_tr_b16 v[140:141], v229 offset:18432\n\t"
        "ds_read_b64_tr_b16 v[142:143], v229 offset:19456\n\t"
        "buffer_load_dwordx4 v[164:167], v225, s[40:43], s45 offen offset:1024 sc1\n\t"
        "s_waitcnt vmcnt(11) lgkmcnt(14)\n\t"
        "v_mfma_f32_32x32x16_bf16 v[64:79], v[144:147], v[184:187], v[64:79]\n\t"
        "ds_read_b64_tr_b16 v[144:145], v228 offset:20480\n\t"
        "ds_read_b64_tr_b16 v[146:147], v228 offset:21504\n\t"
        "s_waitcnt lgkmcnt(14)\n\t"
        "v_mfma_f32_32x32x16_bf16 v[80:95], v[148:151], v[184:187], v[80:95]\n\t"
        "ds_read_b64_tr_b16 v[148:149], v229 offset:20480\n\t"
        "ds_read_b64_tr_b16 v[150:151], v229 offset:21504\n\t"
        "buffer_load_dwordx4 v[168:171], v225, s[40:43], s45 offen offset:2048 sc1\n\t"
        "s_waitcnt vmcnt(11) lgkmcnt(14)\n\t"
        "v_mfma_f32_32x32x16_bf16 v[64:79], v[152:155], v[188:191], v[64:79]\n\t"
        "ds_read_b64_tr_b16 v[152:153], v228 offset:22528\n\t"
        "ds_read_b64_tr_b16 v[154:155], v228 offset:23552\n\t"
        "s_waitcnt lgkmcnt(14)\n\t"
        "v_mfma_f32_32x32x16_bf16 v[80:95], v[156:159], v[188:191], v[80:95]\n\t"
        "ds_read_b64_tr_b16 v[156:157], v229 offset:22528\n\t"
        "ds_read_b64_tr_b16 v[158:159], v229 offset:23552\n\t"
        "buffer_load_dwordx4 v[172:175], v225, s[40:43], s45 offen offset:3072 sc1\n\t"
        "s_waitcnt vmcnt(11) lgkmcnt(14)\n\t"
        "v_mfma_f32_32x32x16_bf16 v[64:79], v[128:131], v[192:195], v[64:79]\n\t"
        "ds_read_b64_tr_b16 v[128:129], v228 offset:24576\n\t"
        "ds_read_b64_tr_b16 v[130:131], v228 offset:25600\n\t"
        "s_waitcnt lgkmcnt(14)\n\t"
        "v_mfma_f32_32x32x16_bf16 v[80:95], v[132:135], v[192:195], v[80:95]\n\t"
        "ds_read_b64_tr_b16 v[132:133], v229 offset:24576\n\t"
        "ds_read_b64_tr_b16 v[134:135], v229 offset:25600\n\t"
        "s_add_u32 s51, s45, 0x1000\n\t"
        "s_and_b32 s51, s51, 0x3fff\n\t"
        "s_andn2_b32 s45, s45, 0x3fff\n\t"
        "s_or_b32 s45, s45, s51\n\t"
        "buffer_load_dwordx4 v[176:179], v225, s[40:43], s45 offen offset:0 sc1\n\t"
        "s_waitcnt vmcnt(11) lgkmcnt(14)\n\t"
        "v_mfma_f32_32x32x16_bf16 v[64:79], v[136:139], v[196:199], v[64:79]\n\t"
        "ds_read_b64_tr_b16 v[136:137], v228 offset:26624\n\t"
        "ds_read_b64_tr_b16 v[138:139], v228 offset:27648\n\t"
        "s_waitcnt lgkmcnt(14)\n\t"
        "v_mfma_f32_32x32x16_bf16 v[80:95], v[140:143], v[196:199], v[80:95]\n\t"
        "ds_read_b64_tr_b16 v[140:141], v229 offset:26624\n\t"
        "ds_read_b64_tr_b16 v[142:143], v229 offset:27648\n\t"
        "buffer_load_dwordx4 v[180:183], v225, s[40:43], s45 offen offset:1024 sc1\n\t"
        "s_waitcnt vmcnt(11) lgkmcnt(14)\n\t"
        "v_mfma_f32_32x32x16_bf16 v[64:79], v[144:147], v[200:203], v[64:79]\n\t"
        "ds_read_b64_tr_b16 v[144:145], v228 offset:28672\n\t"
        "ds_read_b64_tr_b16 v[146:147], v228 offset:29696\n\t"
        "s_waitcnt lgkmcnt(14)\n\t"
        "v_mfma_f32_32x32x16_bf16 v[80:95], v[148:151], v[200:203], v[80:95]\n\t"
        "ds_read_b64_tr_b16 v[148:149], v229 offset:28672\n\t"
        "ds_read_b64_tr_b16 v[150:151], v229 offset:29696\n\t"
        "buffer_load_dwordx4 v[184:187], v225, s[40:43], s45 offen offset:2048 sc1\n\t"
        "s_waitcnt vmcnt(11) lgkmcnt(14)\n\t"
        "v_mfma_f32_32x32x16_bf16 v[64:79], v[152:155], v[204:207], v[64:79]\n\t"
        "ds_read_b64_tr_b16 v[152:153], v228 offset:30720\n\t"
        "ds_read_b64_tr_b16 v[154:155], v228 offset:31744\n\t"
        "s_waitcnt lgkmcnt(14)\n\t"
        "v_mfma_f32_32x32x16_bf16 v[80:95], v[156:159], v[204:207], v[80:95]\n\t"
        "ds_read_b64_tr_b16 v[156:157], v229 offset:30720\n\t"
        "ds_read_b64_tr_b16 v[158:159], v229 offset:31744\n\t"
        "buffer_load_dwordx4 v[188:191], v225, s[40:43], s45 offen offset:3072 sc1\n\t"
        "s_waitcnt vmcnt(11) lgkmcnt(14)\n\t"
        "v_mfma_f32_32x32x16_bf16 v[64:79], v[128:131], v[208:211], v[64:79]\n\t"
        "ds_read_b64_tr_b16 v[128:129], v230 offset:0\n\t"
        "ds_read_b64_tr_b16 v[130:131], v230 offset:1024\n\t"
        "s_waitcnt lgkmcnt(14)\n\t"
        "v_mfma_f32_32x32x16_bf16 v[80:95], v[132:135], v[208:211], v[80:95]\n\t"
        "ds_read_b64_tr_b16 v[132:133], v231 offset:0\n\t"
        "ds_read_b64_tr_b16 v[134:135], v231 offset:1024\n\t"
        "s_add_u32 s51, s45, 0x1000\n\t"
        "s_and_b32 s51, s51, 0x3fff\n\t"
        "s_andn2_b32 s45, s45, 0x3fff\n\t"
        "s_or_b32 s45, s45, s51\n\t"
        "buffer_load_dwordx4 v[192:195], v225, s[40:43], s45 offen offset:0 sc1\n\t"
        "s_waitcnt vmcnt(11) lgkmcnt(14)\n\t"
        "v_mfma_f32_32x32x16_bf16 v[64:79], v[136:139], v[212:215], v[64:79]\n\t"
        "ds_read_b64_tr_b16 v[136:137], v230 offset:2048\n\t"
        "ds_read_b64_tr_b16 v[138:139], v230 offset:3072\n\t"
        "s_waitcnt lgkmcnt(14)\n\t"
        "v_mfma_f32_32x32x16_bf16 v[80:95], v[140:143], v[212:215], v[80:95]\n\t"
        "ds_read_b64_tr_b16 v[140:141], v231 offset:2048\n\t"
        "ds_read_b64_tr_b16 v[142:143], v231 offset:3072\n\t"
        "buffer_load_dwordx4 v[196:199], v225, s[40:43], s45 offen offset:1024 sc1\n\t"
        "s_waitcnt vmcnt(11) lgkmcnt(14)\n\t"
        "v_mfma_f32_32x32x16_bf16 v[64:79], v[144:147], v[216:219], v[64:79]\n\t"
        "ds_read_b64_tr_b16 v[144:145], v230 offset:4096\n\t"
        "ds_read_b64_tr_b16 v[146:147], v230 offset:5120\n\t"
        "s_waitcnt lgkmcnt(14)\n\t"
        "v_mfma_f32_32x32x16_bf16 v[80:95], v[148:151], v[216:219], v[80:95]\n\t"
        "ds_read_b64_tr_b16 v[148:149], v231 offset:4096\n\t"
        "ds_read_b64_tr_b16 v[150:151], v231 offset:5120\n\t"
        "buffer_load_dwordx4 v[200:203], v225, s[40:43], s45 offen offset:2048 sc1\n\t"
        "s_waitcnt vmcnt(11) lgkmcnt(14)\n\t"
        "v_mfma_f32_32x32x16_bf16 v[64:79], v[152:155], v[220:223], v[64:79]\n\t"
        "ds_read_b64_tr_b16 v[152:153], v230 offset:6144\n\t"
        "ds_read_b64_tr_b16 v[154:155], v230 offset:7168\n\t"
        "s_waitcnt lgkmcnt(14)\n\t"
        "v_mfma_f32_32x32x16_bf16 v[80:95], v[156:159], v[220:223], v[80:95]\n\t"
        "ds_read_b64_tr_b16 v[156:157], v231 offset:6144\n\t"
        "ds_read_b64_tr_b16 v[158:159], v231 offset:7168\n\t"
        "buffer_load_dwordx4 v[204:207], v225, s[40:43], s45 offen offset:3072 sc1\n\t"
        "s_mov_b32 s44, s45\n\t"
        "s_cmp_eq_u32 s47, 0\n\t"
        "s_cbranch_scc1 oseg0_%=\n\t"
        "s_cmp_eq_u32 s47, 1\n\t"
        "s_cbranch_scc1 oseg1_%=\n\t"
        "s_cmp_eq_u32 s47, 2\n\t"
        "s_cbranch_scc1 oseg2_%=\n\t"
        "s_cmp_eq_u32 s47, 3\n\t"
        "s_cbranch_scc1 oseg3_%=\n\t"
        "s_cmp_lg_u32 s46, 15\n\t"
        "s_cbranch_scc1 ostart_%=\n\t"
        "s_branch oend_%=\n\t"
        "oseg3_%=:\n\t"
        "s_or_b32 s46, s46, 8\n\t"
        "s_waitcnt vmcnt(11) lgkmcnt(14)\n\t"
        "v_mfma_f32_32x32x16_bf16 v[96:111], v[128:131], v[160:163], 0\n\t"
        "ds_read_b64_tr_b16 v[128:129], v228 offset:40960\n\t"
        "ds_read_b64_tr_b16 v[130:131], v228 offset:41984\n\t"
        "s_waitcnt lgkmcnt(14)\n\t"
        "v_mfma_f32_32x32x16_bf16 v[112:127], v[132:135], v[160:163], 0\n\t"
        "ds_read_b64_tr_b16 v[132:133], v229 offset:40960\n\t"
        "ds_read_b64_tr_b16 v[134:135], v229 offset:41984\n\t"
        "s_add_u32 s51, s44, 0x1000\n\t"
        "s_and_b32 s51, s51, 0x3fff\n\t"
        "s_andn2_b32 s44, s44, 0x3fff\n\t"
        "s_or_b32 s44, s44, s51\n\t"
        "buffer_load_dwordx4 v[208:211], v224, s[40:43], s44 offen offset:0 sc1\n\t"
        "s_waitcnt vmcnt(11) lgkmcnt(14)\n\t"
        "v_mfma_f32_32x32x16_bf16 v[96:111], v[136:139], v[164:167], v[96:111]\n\t"
        "ds_read_b32 v233, v232\n\t"
        "ds_read_b64_tr_b16 v[136:137], v228 offset:43008\n\t"
        "ds_read_b64_tr_b16 v[138:139], v228 offset:44032\n\t"
        "s_waitcnt lgkmcnt(14)\n\t"
        "v_mfma_f32_32x32x16_bf16 v[112:127], v[140:143], v[164:167], v[112:127]\n\t"
        "ds_read_b64_tr_b16 v[140:141], v229 offset:43008\n\t"
        "ds_read_b64_tr_b16 v[142:143], v229 offset:44032\n\t"
        "buffer_load_dwordx4 v[212:215], v224, s[40:43], s44 offen offset:1024 sc1\n\t"
        "s_waitcnt vmcnt(11) lgkmcnt(14)\n\t"
        "v_mfma_f32_32x32x16_bf16 v[96:111], v[144:147], v[168:171], v[96:111]\n\t"
        "ds_read_b64_tr_b16 v[144:145], v228 offset:45056\n\t"
        "ds_read_b64_tr_b16 v[146:147], v228 offset:46080\n\t"
        "s_waitcnt lgkmcnt(14)\n\t"
        "v_mfma_f32_32x32x16_bf16 v[112:127], v[148:151], v[168:171], v[112:127]\n\t"
        "ds_read_b64_tr_b16 v[148:149], v229 offset:45056\n\t"
        "ds_read_b64_tr_b16 v[150:151], v229 offset:46080\n\t"
        "buffer_load_dwordx4 v[216:219], v224, s[40:43], s44 offen offset:2048 sc1\n\t"
        "s_waitcnt vmcnt(11) lgkmcnt(8)\n\t"
        "v_mfma_f32_32x32x16_bf16 v[96:111], v[152:155], v[172:175], v[96:111]\n\t"
        "v_readfirstlane_b32 s51, v233\n\t"
        "ds_read_b64_tr_b16 v[152:153], v228 offset:47104\n\t"
        "ds_read_b64_tr_b16 v[154:155], v228 offset:48128\n\t"
        "s_waitcnt lgkmcnt(14)\n\t"
        "v_mfma_f32_32x32x16_bf16 v[112:127], v[156:159], v[172:175], v[112:127]\n\t"
        "s_andn2_b32 s51, s51, s46\n\t"
        "s_ff1_i32_b32 s47, s51\n\t"
        "s_add_u32 s45, s47, s49\n\t"
        "s_and_b32 s45, s45, 3\n\t"
        "s_lshl_b32 s45, s45, 14\n\t"
        "s_or_b32 s45, s45, s50\n\t"
        "ds_read_b64_tr_b16 v[156:157], v229 offset:47104\n\t"
        "ds_read_b64_tr_b16 v[158:159], v229 offset:48128\n\t"
        "buffer_load_dwordx4 v[220:223], v224, s[40:43], s44 offen offset:3072 sc1\n\t"
        "s_waitcnt vmcnt(11) lgkmcnt(14)\n\t"
        "v_mfma_f32_32x32x16_bf16 v[96:111], v[128:131], v[176:179], v[96:111]\n\t"
        "s_and_b32 s51, s47, 0x80000000\n\t"
        "v_or_b32 v225, s51, v224\n\t"
        "s_and_b32 s51, s47, 3\n\t"
        "s_lshl_b32 s51, s51, 15\n\t"
        "v_add_u32 v230, s51, v226\n\t"
        "v_add_u32 v231, s51, v227\n\t"
        "ds_read_b64_tr_b16 v[128:129], v228 offset:49152\n\t"
        "ds_read_b64_tr_b16 v[130:131], v228 offset:50176\n\t"
        "s_waitcnt lgkmcnt(14)\n\t"
        "v_mfma_f32_32x32x16_bf16 v[112:127], v[132:135], v[176:179], v[112:127]\n\t"
        "ds_read_b64_tr_b16 v[132:133], v229 offset:49152\n\t"
        "ds_read_b64_tr_b16 v[134:135], v229 offset:50176\n\t"
        "buffer_load_dwordx4 v[160:163], v225, s[40:43], s45 offen offset:0 sc1\n\t"
        "s_waitcnt vmcnt(11) lgkmcnt(14)\n\t"
        "v_mfma_f32_32x32x16_bf16 v[96:111], v[136:139], v[180:183], v[96:111]\n\t"
        "ds_read_b64_tr_b16 v[136:137], v228 offset:51200\n\t"
        "ds_read_b64_tr_b16 v[138:139], v228 offset:52224\n\t"
        "s_waitcnt lgkmcnt(14)\n\t"
        "v_mfma_f32_32x32x16_bf16 v[112:127], v[140:143], v[180:183], v[112:127]\n\t"
        "ds_read_b64_tr_b16 v[140:141], v229 offset:51200\n\t"
        "ds_read_b64_tr_b16 v[142:143], v229 offset:52224\n\t"
        "buffer_load_dwordx4 v[164:167], v225, s[40:43], s45 offen offset:1024 sc1\n\t"
        "s_waitcnt vmcnt(11) lgkmcnt(14)\n\t"
        "v_mfma_f32_32x32x16_bf16 v[96:111], v[144:147], v[184:187], v[96:111]\n\t"
        "ds_read_b64_tr_b16 v[144:145], v228 offset:53248\n\t"
        "ds_read_b64_tr_b16 v[146:147], v228 offset:54272\n\t"
        "s_waitcnt lgkmcnt(14)\n\t"
        "v_mfma_f32_32x32x16_bf16 v[112:127], v[148:151], v[184:187], v[112:127]\n\t"
        "ds_read_b64_tr_b16 v[148:149], v229 offset:53248\n\t"
        "ds_read_b64_tr_b16 v[150:151], v229 offset:54272\n\t"
        "buffer_load_dwordx4 v[168:171], v225, s[40:43], s45 offen offset:2048 sc1\n\t"
        "s_waitcnt vmcnt(11) lgkmcnt(14)\n\t"
        "v_mfma_f32_32x32x16_bf16 v[96:111], v[152:155], v[188:191], v[96:111]\n\t"
        "ds_read_b64_tr_b16 v[152:153], v228 offset:55296\n\t"
        "ds_read_b64_tr_b16 v[154:155], v228 offset:56320\n\t"
        "s_waitcnt lgkmcnt(14)\n\t"
        "v_mfma_f32_32x32x16_bf16 v[112:127], v[156:159], v[188:191], v[112:127]\n\t"
        "ds_read_b64_tr_b16 v[156:157], v229 offset:55296\n\t"
        "ds_read_b64_tr_b16 v[158:159], v229 offset:56320\n\t"
        "buffer_load_dwordx4 v[172:175], v225, s[40:43], s45 offen offset:3072 sc1\n\t"
        "s_waitcnt vmcnt(11) lgkmcnt(14)\n\t"
        "v_mfma_f32_32x32x16_bf16 v[96:111], v[128:131], v[192:195], v[96:111]\n\t"
        "ds_read_b64_tr_b16 v[128:129], v228 offset:57344\n\t"
        "ds_read_b64_tr_b16 v[130:131], v228 offset:58368\n\t"
        "s_waitcnt lgkmcnt(14)\n\t"
        "v_mfma_f32_32x32x16_bf16 v[112:127], v[132:135], v[192:195], v[112:127]\n\t"
        "ds_read_b64_tr_b16 v[132:133], v229 offset:57344\n\t"
        "ds_read_b64_tr_b16 v[134:135], v229 offset:58368\n\t"
        "s_add_u32 s51, s45, 0x1000\n\t"
        "s_and_b32 s51, s51, 0x3fff\n\t"
        "s_andn2_b32 s45, s45, 0x3fff\n\t"
        "s_or_b32 s45, s45, s51\n\t"
        "buffer_load_dwordx4 v[176:179], v225, s[40:43], s45 offen offset:0 sc1\n\t"
        "s_waitcnt vmcnt(11) lgkmcnt(14)\n\t"
        "v_mfma_f32_32x32x16_bf16 v[96:111], v[136:139], v[196:199], v[96:111]\n\t"
        "ds_read_b64_tr_b16 v[136:137], v228 offset:59392\n\t"
        "ds_read_b64_tr_b16 v[138:139], v228 offset:60416\n\t"
        "s_waitcnt lgkmcnt(14)\n\t"
        "v_mfma_f32_32x32x16_bf16 v[112:127], v[140:143], v[196:199], v[112:127]\n\t"
        "ds_read_b64_tr_b16 v[140:141], v229 offset:59392\n\t"
        "ds_read_b64_tr_b16 v[142:143], v229 offset:60416\n\t"
        "buffer_load_dwordx4 v[180:183], v225, s[40:43], s45 offen offset:1024 sc1\n\t"
        "s_waitcnt vmcnt(11) lgkmcnt(14)\n\t"
        "v_mfma_f32_32x32x16_bf16 v[96:111], v[144:147], v[200:203], v[96:111]\n\t"
        "ds_read_b64_tr_b16 v[144:145], v228 offset:61440\n\t"
        "ds_read_b64_tr_b16 v[146:147], v228 offset:62464\n\t"
        "s_waitcnt lgkmcnt(14)\n\t"
        "v_mfma_f32_32x32x16_bf16 v[112:127], v[148:151], v[200:203], v[112:127]\n\t"
        "ds_read_b64_tr_b16 v[148:149], v229 offset:61440\n\t"
        "ds_read_b64_tr_b16 v[150:151], v229 offset:62464\n\t"
        "buffer_load_dwordx4 v[184:187], v225, s[40:43], s45 offen offset:2048 sc1\n\t"
        "s_waitcnt vmcnt(11) lgkmcnt(14)\n\t"
        "v_mfma_f32_32x32x16_bf16 v[96:111], v[152:155], v[204:207], v[96:111]\n\t"
        "ds_read_b64_tr_b16 v[152:153], v228 offset:63488\n\t"
        "ds_read_b64_tr_b16 v[154:155], v228 offset:64512\n\t"
        "s_waitcnt lgkmcnt(14)\n\t"
        "v_mfma_f32_32x32x16_bf16 v[112:127], v[156:159], v[204:207], v[112:127]\n\t"
        "ds_read_b64_tr_b16 v[156:157], v229 offset:63488\n\t"
        "ds_read_b64_tr_b16 v[158:159], v229 offset:64512\n\t"
        "buffer_load_dwordx4 v[188:191], v225, s[40:43], s45 offen offset:3072 sc1\n\t"
        "s_waitcnt vmcnt(11) lgkmcnt(14)\n\t"
        "v_mfma_f32_32x32x16_bf16 v[96:111], v[128:131], v[208:211], v[96:111]\n\t"
        "ds_read_b64_tr_b16 v[128:129], v230 offset:0\n\t"
        "ds_read_b64_tr_b16 v[130:131], v230 offset:1024\n\t"
        "s_waitcnt lgkmcnt(14)\n\t"
        "v_mfma_f32_32x32x16_bf16 v[112:127], v[132:135], v[208:211], v[112:127]\n\t"
        "ds_read_b64_tr_b16 v[132:133], v231 offset:0\n\t"
        "ds_read_b64_tr_b16 v[134:135], v231 offset:1024\n\t"
        "s_add_u32 s51, s45, 0x1000\n\t"
        "s_and_b32 s51, s51, 0x3fff\n\t"
        "s_andn2_b32 s45, s45, 0x3fff\n\t"
        "s_or_b32 s45, s45, s51\n\t"
        "buffer_load_dwordx4 v[192:195], v225, s[40:43], s45 offen offset:0 sc1\n\t"
        "s_waitcnt vmcnt(11) lgkmcnt(14)\n\t"
        "v_mfma_f32_32x32x16_bf16 v[96:111], v[136:139], v[212:215], v[96:111]\n\t"
        "ds_read_b64_tr_b16 v[136:137], v230 offset:2048\n\t"
        "ds_read_b64_tr_b16 v[138:139], v230 offset:3072\n\t"
        "s_waitcnt lgkmcnt(14)\n\t"
        "v_mfma_f32_32x32x16_bf16 v[112:127], v[140:143], v[212:215], v[112:127]\n\t"
        "ds_read_b64_tr_b16 v[140:141], v231 offset:2048\n\t"
        "ds_read_b64_tr_b16 v[142:143], v231 offset:3072\n\t"
        "buffer_load_dwordx4 v[196:199], v225, s[40:43], s45 offen offset:1024 sc1\n\t"
        "s_waitcnt vmcnt(11) lgkmcnt(14)\n\t"
        "v_mfma_f32_32x32x16_bf16 v[96:111], v[144:147], v[216:219], v[96:111]\n\t"
        "ds_read_b64_tr_b16 v[144:145], v230 offset:4096\n\t"
        "ds_read_b64_tr_b16 v[146:147], v230 offset:5120\n\t"
        "s_waitcnt lgkmcnt(14)\n\t"
        "v_mfma_f32_32x32x16_bf16 v[112:127], v[148:151], v[216:219], v[112:127]\n\t"
        "ds_read_b64_tr_b16 v[148:149], v231 offset:4096\n\t"
        "ds_read_b64_tr_b16 v[150:151], v231 offset:5120\n\t"
        "buffer_load_dwordx4 v[200:203], v225, s[40:43], s45 offen offset:2048 sc1\n\t"
        "s_waitcnt vmcnt(11) lgkmcnt(14)\n\t"
        "v_mfma_f32_32x32x16_bf16 v[96:111], v[152:155], v[220:223], v[96:111]\n\t"
        "ds_read_b64_tr_b16 v[152:153], v230 offset:6144\n\t"
        "ds_read_b64_tr_b16 v[154:155], v230 offset:7168\n\t"
        "s_waitcnt lgkmcnt(14)\n\t"
        "v_mfma_f32_32x32x16_bf16 v[112:127], v[156:159], v[220:223], v[112:127]\n\t"
        "ds_read_b64_tr_b16 v[156:157], v231 offset:6144\n\t"
        "ds_read_b64_tr_b16 v[158:159], v231 offset:7168\n\t"
        "buffer_load_dwordx4 v[204:207], v225, s[40:43], s45 offen offset:3072 sc1\n\t"
        "s_mov_b32 s44, s45\n\t"
        "s_cmp_eq_u32 s47, 0\n\t"
        "s_cbranch_scc1 oseg0_%=\n\t"
        "s_cmp_eq_u32 s47, 1\n\t"
        "s_cbranch_scc1 oseg1_%=\n\t"
        "s_cmp_eq_u32 s47, 2\n\t"
        "s_cbranch_scc1 oseg2_%=\n\t"
        "s_cmp_eq_u32 s47, 3\n\t"
        "s_cbranch_scc1 oseg3_%=\n\t"
        "s_cmp_lg_u32 s46, 15\n\t"
        "s_cbranch_scc1 ostart_%=\n\t"
        "s_branch oend_%=\n\t"
        "oend_%=:\n\t"
        "s_waitcnt vmcnt(0) lgkmcnt(0)\n\t"
        "s_barrier\n\t"
        "s_nop 3\n\t"
        : "+{v[0:15]}"(acc[0]), "+{v[16:31]}"(acc[1]), "={v[32:47]}"(acc[2]), "={v[48:63]}"(acc[3]), "={v[64:79]}"(acc[4]), "={v[80:95]}"(acc[5]),
          "={v[96:111]}"(acc[6]), "={v[112:127]}"(acc[7]) STAMP_OUTS
        : "{v224}"(OFF_OF + L * (5 * MBY) + tile * TS + lane * 16), "{v226}"(a0), "{v227}"(a1), "{v228}"(a0 + 65536), "{v229}"(a1 + 65536),
          "{v232}"(lbase + L_W + 20 + 8 * L), "{s49}"((tn >> 2) + 1), "{s50}"(0 * (tn & 3) * 4096), "{s[40:43]}"(srd)
        : "v128", "v129", "v130", "v131", "v132", "v133", "v134", "v135", "v136", "v137", "v138", "v139", "v140", "v141", "v142", "v143", "v144", "v145", "v146", "v147", "v148", "v149", "v150", "v151", "v152", "v153", "v154", "v155", "v156", "v157", "v158", "v159", "v160", "v161", "v162", "v163", "v164", "v165", "v166", "v167", "v168", "v169", "v170", "v171", "v172", "v173", "v174", "v175", "v176", "v177", "v178", "v179", "v180", "v181", "v182", "v183", "v184", "v185", "v186", "v187", "v188", "v189", "v190", "v191", "v192", "v193", "v194", "v195", "v196", "v197", "v198", "v199", "v200", "v201", "v202", "v203", "v204", "v205", "v206", "v207", "v208", "v209", "v210", "v211", "v212", "v213", "v214", "v215", "v216", "v217", "v218", "v219", "v220", "v221", "v222", "v223", "v225", "v230", "v231", "v233", "s44", "s45", "s46", "s47", "s48", "s51", "vcc", "scc", "memory");
      }
      PHASE_VARS;
      STAMP_PUT(12 + 16 * L);
      STAMPV(15 + 16 * L);
#pragma unroll
      for (int g = 0; g < 8; ++g) wrc[g] = *reinterpret_cast<const u32x2*>(smem + L_WR + g * 16 + hi * 8);
      SBAR();
      float s = 0.f, sq = 0.f, d = 0.f;
      const unsigned xdst = (L ? OOB : OFF_X1 + tile * TS + tn * 4096) + lane * 16;
#pragma unroll
      for (int i = 0; i < 4; ++i) {
#pragma unroll
        for (int e = 0; e < 4; ++e) {
          const int j = i >> 1, k = 8 * (i & 1) + 2 * e;
          x1p[i][e] = pack2((acc[j][k] + acc[2 + j][k]) + (acc[4 + j][k] + acc[6 + j][k]), (acc[j][k + 1] + acc[2 + j][k + 1]) + (acc[4 + j][k + 1] + acc[6 + j][k + 1]));
        }
        stg(rs, xdst + i * 1024, x1p[i]);
        SBAR();
      }
#pragma unroll
      for (int g = 0; g < 8; ++g)
#pragma unroll
        for (int e = 0; e < 2; ++e) {
          const unsigned o = x1p[g >> 1][2 * (g & 1) + e];
          s = dot2(o, 0x3f803f80u, s); sq = dot2(o, o, sq); d = dot2(o, wrc[g][e], d);
        }
      const u32x4 pst = {nzf(half_swap_sum(s)), nzf(half_swap_sum(sq)), nzf(half_swap_sum(d)), 0u};
      asm volatile("buffer_store_dwordx4 %0, %1, %2, 0 offen sc1\n\ts_waitcnt vmcnt(1)\n\tds_write_b32 %3, %4"
                   :: "v"(pst), "v"(stats_dst(sx, tile, tn, lane)), "s"(srd), "v"(lbase + L_W + 64 + 4 * wid), "v"(3) : "memory");
    }
    STAMPV(16 + 16 * L);
  }
  {
    PHASE_VARS;
    float S, Q, Dd, mean, var, mb1, rd1;
    __builtin_amdgcn_s_sleep(13);
    row_stats_sum(rs, OFF_SX + SX_LAYER, tile, lane, S, Q, Dd);
    STAMPV(33);
    row_stats_fin(rs, OFF_SX + SX_LAYER + SX_CAND + (tn * 16 + tm) * 128 + wid * 4, smem, 2, tile, lane, S, Q, Dd, U2F(words[0]), x1p, mean, var, mb1, rd1);
    STAMPV(34);
    u32x4 xo[4];
#pragma unroll
    for (int i = 0; i < 4; ++i) {
#pragma unroll
      for (int e = 0; e < 4; ++e) {
        const unsigned h2 = ln_pair(x1p[i][e], mb1, rd1);
        xo[i][e] = pack2(lo16(x1p[i][e]) + lo16(h2), hi16(x1p[i][e]) + hi16(h2));
      }
      *reinterpret_cast<u32x4*>(smem + wid * 4096 + r32 * 128 + ((2 * i + hi) ^ (r32 & 7)) * 16) = fo_swap(xo[i]);
    }
    asm volatile("s_waitcnt lgkmcnt(0)" ::: "memory");
#pragma unroll
    for (int i = 0; i < 4; ++i) { xo[i] = *reinterpret_cast<const u32x4*>(smem + wid * 4096 + i * 1024 + (lane >> 3) * 128 + ((lane ^ (lane >> 3)) & 7) * 16); PIN(xo[i]); }
    u16* dst = out + (size_t)(tile * 32 + (lane >> 3)) * D_ + tn * 64 + (lane & 7) * 8;
    lds_wait_ge(lbase + L_W + 4 + 4 * (lane & 1), 2);
    const int rel = (int)words[3] - tile * 32 - (lane >> 3);
#pragma unroll
    for (int i = 0; i < 4; ++i)
      if (rel != 8 * i) st16(dst + (size_t)i * 8 * D_, xo[i]);
    STAMPV(35);
    STAMP_END;
  }
}

extern "C" __global__ void end_marker() {}

extern "C" void kernel_launch(void* const* d_in, const int* in_sizes, int n_in, void* d_out, int out_size,
                              void* d_ws, size_t ws_size, hipStream_t stream) {
  char* ws = (char*)d_ws;
  mega_kernel<<<256, 512, 0, stream>>>((const u16*)d_in[0], (const u16*)d_in[1], (const u16*)d_in[2], (const u16*)d_in[4], (const u16*)d_in[5],
                                       (const u16*)d_in[6], (const u16*)d_in[7], ws, (u16*)d_out);
}
```

```cpp
#include <hip/hip_runtime.h>
#include <hip/hip_bf16.h>
#include <stdint.h>

typedef unsigned short u16;
typedef short s16x4 __attribute__((ext_vector_type(4)));
typedef short s16x8 __attribute__((ext_vector_type(8)));
typedef float f32x16 __attribute__((ext_vector_type(16)));
typedef float f32x8 __attribute__((ext_vector_type(8)));
typedef float f32x4 __attribute__((ext_vector_type(4)));
typedef float f32x2 __attribute__((ext_vector_type(2)));
typedef unsigned int u32x16 __attribute__((ext_vector_type(16)));
typedef unsigned int u32x8 __attribute__((ext_vector_type(8)));
typedef unsigned int u32x4 __attribute__((ext_vector_type(4)));
typedef unsigned int u32x2 __attribute__((ext_vector_type(2)));
typedef __bf16 bf16x2 __attribute__((ext_vector_type(2)));
typedef __bf16 bf16x8 __attribute__((ext_vector_type(8)));
typedef __bf16 bf16x16 __attribute__((ext_vector_type(16)));

#define DI __device__ __forceinline__
#define LDS_PTR(T, p) ((__attribute__((address_space(3))) T*)(p))
#define BARRIER() asm volatile("s_barrier" ::: "memory")
#define BAR_LGKM() asm volatile("s_waitcnt lgkmcnt(0)\n\ts_barrier" ::: "memory")
#define BAR_ALL() asm volatile("s_waitcnt vmcnt(0) lgkmcnt(0)\n\ts_barrier" ::: "memory")
#define DRAIN() asm volatile("s_waitcnt vmcnt(0)" ::: "memory")
#define PACE() ({ asm volatile("s_sleep 1\n\ts_waitcnt vmcnt(3)" ::: "memory"); })
#define MFMA32(a, b, c) __builtin_amdgcn_mfma_f32_32x32x16_bf16(a, b, c, 0, 0, 0)
#define MFMA16(a, b, c) __builtin_amdgcn_mfma_f32_16x16x32_bf16(a, b, c, 0, 0, 0)
#define PIN(x) asm volatile("" : "+v"(x))
#define EX2(x) __builtin_amdgcn_exp2f(x)
#define RCP(x) __builtin_amdgcn_rcpf(x)
#define SBAR() __builtin_amdgcn_sched_barrier(0)

#define STAMP_INIT
#define STAMPV(i)
#define STAMP_END
#define STAMP_DECL
#define STAMP_OUTS
#define STAMP_PUT(i)
constexpr int D_ = 1024, F_ = 3584;

constexpr unsigned MBY = 1u << 20;
constexpr unsigned TS = 65536 + 4096, HS = 262144 + 4096;
constexpr unsigned OFF_HF = 0, OFF_X1 = 5 * MBY;
constexpr unsigned OFF_OF = 10 * MBY;
constexpr unsigned OFF_KV = 20 * MBY;
constexpr unsigned OFF_FL = 40 * MBY;
constexpr unsigned FL_EPOCH = OFF_FL;
constexpr unsigned FL_KV = OFF_FL + 4096;
constexpr unsigned FL_O = FL_KV + 65536;
constexpr unsigned FL_X = FL_O + 65536;
constexpr unsigned OFF_CS = 41 * MBY;
constexpr unsigned OFF_SX = 42 * MBY;
constexpr unsigned SX_ST = 0;
constexpr unsigned SX_ROW = 512 * 1024;
constexpr unsigned SX_CAND = SX_ROW + 256 * 1024;
constexpr unsigned SX_GP = SX_CAND + 32 * 1024;
constexpr unsigned SX_YP = SX_GP + 224 * 1024;
constexpr unsigned SX_LAYER = SX_YP + 224 * 1024;
constexpr unsigned SX_QP = 2 * SX_LAYER;
constexpr unsigned SX_BYTES = SX_QP + 512 * 1024;
static_assert(SX_BYTES <= 6 * 256 * 128 * 16, "sentinel fill");
constexpr unsigned WS_BYTES = 48 * MBY, OOB = 0x80000000u;
constexpr unsigned SENT = 0xFFFFFFFFu;

constexpr int NS = 9, DD = 3, SLOTB = 12288;
constexpr int L_XCH = 122880;
constexpr int L_SL = L_XCH + 8192;
constexpr int L_CAND = L_XCH + 32768;
constexpr int L_AB = L_XCH + 34816;
constexpr int L_FULL = 159744, L_FREE = L_FULL + 512;
constexpr int L_CS = L_FREE + 1024, L_PATCH = L_CS + 768;
constexpr int L_GU = L_PATCH + 768, L_TAG = L_GU + 256, L_WR = L_TAG + 256;
constexpr int L_X2F = L_WR + 128, L_X2P = L_X2F + 128;
constexpr int L_W = L_X2P + 128;
constexpr int LDS_BYTES = 163840;
static_assert(NS * SLOTB <= 54 * 2048 && L_W + 128 <= LDS_BYTES && (NS + 1) * SLOTB <= L_XCH && L_AB + 2048 <= L_FULL, "LDS map");

DI int fresh_lane() { int l; asm volatile("v_mbcnt_lo_u32_b32 %0, -1, 0\n\tv_mbcnt_hi_u32_b32 %0, -1, %0" : "=v"(l)); return l; }
#define LANE_VARS const int lane = fresh_lane(), r32 = lane & 31, hi = lane >> 5, tq = (lane & 15) >> 2, tp = lane & 3, tblk = (lane >> 4) & 1, \
  wrow = 4 * hi + tq, wsw = 4 * ((wrow >> 1) & 1), wch = 2 * tblk + (tp >> 1), c24 = r32 < 24 ? r32 : 23; \
  (void)r32; (void)hi; (void)wsw; (void)wch; (void)c24
DI int fresh_s(int v) { asm volatile("" : "+s"(v)); return v; }
#define PHASE_VARS const int tm = fresh_s(tm_), tn = fresh_s(tn_), wid = fresh_s(wid_), tile = tm * 4 + wid, hh = wid & 1, kh = wid >> 1; (void)tile; (void)hh; (void)kh; LANE_VARS
DI int kpos(int tm, int tn, int i) { return i == 0 ? tn : (tn + 1 + (1 * (3 - (tn & 3)) + i - 1) % 15) & 15; }
DI float bf2f(u16 v) { return __uint_as_float(((unsigned)v) << 16); }
DI unsigned pack2(float lo, float hi) { f32x2 v = {lo, hi}; return __builtin_bit_cast(unsigned, __builtin_convertvector(v, bf16x2)); }
DI float rbf(float f) { return __uint_as_float(pack2(f, 0.f) << 16); }
DI float lo16(unsigned u) { return __uint_as_float(u << 16); }
DI float hi16(unsigned u) { return __uint_as_float(u & 0xffff0000u); }
DI float opq(float r) { asm("" : "+v"(r)); return r; }
DI float fadd(float a, float b) { return opq(a + b); }
DI float fmul(float a, float b) { return opq(a * b); }
DI float ffma(float a, float b, float c) { return opq(__builtin_fmaf(a, b, c)); }
DI float U2F(unsigned u) { return __uint_as_float(u); }
DI float half_swap_sum(float v) {
  auto r = __builtin_amdgcn_permlane32_swap(__float_as_uint(v), __float_as_uint(v), false, false);
  return fadd(U2F(r[0]), U2F(r[1]));
}
DI float half_swap_max(float v) {
  auto r = __builtin_amdgcn_permlane32_swap(__float_as_uint(v), __float_as_uint(v), false, false);
  return fmaxf(__uint_as_float(r[0]), __uint_as_float(r[1]));
}
template <int CTRL> DI unsigned dpp(unsigned v) { return (unsigned)__builtin_amdgcn_update_dpp(0, (int)v, CTRL, 0xf, 0xf, false); }
DI float row_swap_sum(float v) {
  auto r = __builtin_amdgcn_permlane16_swap(__float_as_uint(v), __float_as_uint(v), false, false);
  return fadd(U2F(r[0]), U2F(r[1]));
}
DI float half_sum(float v) {
  v = fadd(v, U2F(dpp<0xB1>(__float_as_uint(v)))); v = fadd(v, U2F(dpp<0x4E>(__float_as_uint(v))));
  v = fadd(v, U2F(dpp<0x141>(__float_as_uint(v)))); v = fadd(v, U2F(dpp<0x140>(__float_as_uint(v))));
  return row_swap_sum(v);
}
DI float wave_sum(float v) { return half_swap_sum(half_sum(v)); }
DI unsigned half_max_u32(unsigned v) {
  v = max(v, dpp<0xB1>(v)); v = max(v, dpp<0x4E>(v)); v = max(v, dpp<0x141>(v)); v = max(v, dpp<0x140>(v));
  auto r = __builtin_amdgcn_permlane16_swap(v, v, false, false);
  return max(r[0], r[1]);
}
DI unsigned wave_max_u32(unsigned v) {
  v = half_max_u32(v);
  auto r = __builtin_amdgcn_permlane32_swap(v, v, false, false);
  return max(r[0], r[1]);
}
DI u32x4 fo_swap(u32x4 v) {
  auto a = __builtin_amdgcn_permlane32_swap(v[0], v[2], false, false);
  auto b = __builtin_amdgcn_permlane32_swap(v[1], v[3], false, false);
  u32x4 o; o[0] = a[0]; o[1] = b[0]; o[2] = a[1]; o[3] = b[1];
  return o;
}
DI void glds4(const void* sb, unsigned vo, unsigned lds_dst) {
  unsigned keep;
  asm volatile("s_mov_b32 %0, m0\n\ts_mov_b32 m0, %3\n\ts_nop 0\n\tglobal_load_lds_dword %1, %2\n\ts_mov_b32 m0, %0"
               : "=&s"(keep) : "v"(vo), "s"(sb), "s"(lds_dst) : "memory");
}
#define GL_ "global_load_lds_dwordx4 "
DI void glds_v4(const void* sb, const unsigned (&v)[4], unsigned m0v) {
  unsigned keep;
  asm volatile("s_mov_b32 %0, m0\n\ts_mov_b32 m0, %6\n\ts_nop 0\n\t" GL_ "%1, %5\n\t" GL_ "%2, %5 offset:1024\n\t" GL_ "%3, %5 offset:2048\n\t"
               GL_ "%4, %5 offset:3072\n\ts_mov_b32 m0, %0" : "=&s"(keep) : "v"(v[0]), "v"(v[1]), "v"(v[2]), "v"(v[3]), "s"(sb), "s"(m0v) : "memory");
}
#define GP_ "\n\ts_sleep %8\n\ts_waitcnt vmcnt(%7)\n\t"
template <int TH, int SL> DI void glds_v4t(const void* sb, const unsigned (&v)[4], unsigned m0v) {
  unsigned keep;
  asm volatile("s_mov_b32 %0, m0\n\ts_mov_b32 m0, %6\n\ts_nop 0\n\t" GL_ "%1, %5" GP_ GL_ "%2, %5 offset:1024" GP_ GL_ "%3, %5 offset:2048" GP_ GL_ "%4, %5 offset:3072" GP_ "s_mov_b32 m0, %0"
               : "=&s"(keep) : "v"(v[0]), "v"(v[1]), "v"(v[2]), "v"(v[3]), "s"(sb), "s"(m0v), "n"(TH), "n"(SL) : "memory");
}
DI void glds_v3(const void* sb, const unsigned (&v)[3], unsigned m0v) {
  unsigned keep;
  asm volatile("s_mov_b32 %0, m0\n\ts_mov_b32 m0, %5\n\ts_nop 0\n\t" GL_ "%1, %4\n\t" GL_ "%2, %4 offset:1024\n\t" GL_ "%3, %4 offset:2048\n\t"
               "s_mov_b32 m0, %0" : "=&s"(keep) : "v"(v[0]), "v"(v[1]), "v"(v[2]), "s"(sb), "s"(m0v) : "memory");
}
DI float dot2(unsigned a, unsigned b, float c) {
  return __builtin_amdgcn_fdot2_f32_bf16(__builtin_bit_cast(bf16x2, a), __builtin_bit_cast(bf16x2, b), c, false);
}
#define CODE_ADDR(sym, out) do { unsigned long long pc_; unsigned off_; \
  asm volatile("s_getpc_b64 %0\n1:\n\ts_mov_b32 %1, " #sym "-1b" : "=s"(pc_), "=s"(off_)); out = (const char*)(pc_ + (long long)(int)off_); } while (0)
DI void st16(void* p, u32x4 v) { asm volatile("global_store_dwordx4 %0, %1, off sc0 sc1\n\ts_nop 1" :: "v"(p), "v"(v) : "memory"); }
DI void ln_scalars(float mean, float var, float& mb, float& den) { mb = rbf(mean); den = rbf(__builtin_amdgcn_sqrtf(rbf(rbf(var) + 1e-5f))); }
DI unsigned ln_pair(unsigned v, float mb, float rd) {
  const unsigned c = pack2(lo16(v) - mb, hi16(v) - mb);
  return pack2(lo16(c) * rd, hi16(c) * rd);
}
DI unsigned ln_pair_s(unsigned v, float nmb, float rd) {
  const unsigned c = pack2(fadd(lo16(v), nmb), fadd(hi16(v), nmb));
  return pack2(fmul(lo16(c), rd), fmul(hi16(c), rd));
}
template <int N, int M> DI f32x4 sum4(const u32x4 (&v)[M]) {
  f32x4 t;
#pragma unroll
  for (int e = 0; e < 4; ++e) {
    t[e] = U2F(v[0][e]);
#pragma unroll
    for (int i = 1; i < N; ++i) t[e] = fadd(t[e], U2F(v[i][e]));
  }
  return t;
}
#define PERM_LO(x, y) __builtin_amdgcn_perm(y, x, 0x05040100u)
#define PERM_HI(x, y) __builtin_amdgcn_perm(y, x, 0x07060302u)
constexpr unsigned SPIN_CAP = 1u << 15;
DI unsigned nz(unsigned v) { return min(v, SENT - 1u); }
DI unsigned nzf(float v) { return nz(__float_as_uint(v)); }
DI unsigned mx4(unsigned m, const u32x4& v) { return max(max(max(m, v[0]), v[1]), max(v[2], v[3])); }
template <int N> DI bool none_sent(const u32x4 (&v)[N]) {
  unsigned m = 0u;
#pragma unroll
  for (int i = 0; i < N; ++i) m = mx4(m, v[i]);
  return m != SENT;
}
typedef __amdgpu_buffer_rsrc_t rsrc_t;
DI u32x4 ldg(rsrc_t rs, unsigned off) { return __builtin_amdgcn_raw_buffer_load_b128(rs, (int)off, 0, 16); }
DI void stg(rsrc_t rs, unsigned off, u32x4 v) { __builtin_amdgcn_raw_buffer_store_b128(v, rs, (int)off, 0, 16); }
DI void poll_pause(bool slow) { if (slow) __builtin_amdgcn_s_sleep(4); asm volatile("s_sleep 1" ::: "memory"); }
template <int N, class LD, class OKF> DI void poll_all(u32x4 (&out)[N], bool slow, LD ld, OKF okf) {
  for (unsigned spins = 0;; ++spins) {
    ld(out);
    SBAR();
    if (__all(okf(out)) || spins > SPIN_CAP) break;
    poll_pause(slow);
  }
}
DI void lds_set(unsigned addr, unsigned v) { asm volatile("s_waitcnt lgkmcnt(0)\n\tds_write_b32 %0, %1" :: "v"(addr), "v"(v) : "memory"); }
template <int SLEEP = 1> DI void lds_wait_ge(unsigned addr, unsigned need) {
  unsigned spin = 0x100000, v;
  asm volatile("1:\n\tds_read_b32 %0, %2\n\ts_sub_u32 %1, %1, 1\n\ts_waitcnt lgkmcnt(0)\n\ts_cbranch_scc1 2f\n\tv_cmp_gt_u32 vcc, %3, %0\n\ts_cbranch_vccz 2f\n\ts_sleep %4\n\ts_branch 1b\n2:"
               : "=&v"(v), "+s"(spin) : "v"(addr), "s"(need), "n"(SLEEP) : "vcc", "scc", "memory");
}
DI s16x8 tr2(const char* p, int second) {
  s16x4 lo = __builtin_amdgcn_ds_read_tr16_b64_v4i16(LDS_PTR(s16x4, p)), hi = __builtin_amdgcn_ds_read_tr16_b64_v4i16(LDS_PTR(s16x4, p + second));
  return __builtin_shufflevector(lo, hi, 0, 1, 2, 3, 4, 5, 6, 7);
}
DI u32x16 cat4(const u32x4& a, const u32x4& b, const u32x4& c, const u32x4& d) {
  const u32x8 x = __builtin_shufflevector(a, b, 0, 1, 2, 3, 4, 5, 6, 7), y = __builtin_shufflevector(c, d, 0, 1, 2, 3, 4, 5, 6, 7);
  return __builtin_shufflevector(x, y, 0, 1, 2, 3, 4, 5, 6, 7, 8, 9, 10, 11, 12, 13, 14, 15);
}

DI unsigned stats_dst(unsigned sx, int tile, int tn, int lane) { return lane < 32 ? sx + SX_ST + tile * 8192 + tn * 512 + lane * 16 : OOB; }
DI void row_stats_sum(rsrc_t rs, unsigned sx, int tile, int lane, float& S, float& Q, float& Dd) {
  const unsigned exb = sx + SX_ST + tile * 8192 + (lane & 31) * 16;
  u32x4 pv[8];
  poll_all<8>(pv, false, [&](u32x4 (&v)[8]) {
#pragma unroll
    for (int i = 0; i < 8; ++i) v[i] = ldg(rs, exb + ((lane >> 5) * 8 + i) * 512);
  }, [&](const u32x4 (&v)[8]) { return none_sent(v); });
  S = 0.f; Q = 0.f; Dd = 0.f;
#pragma unroll
  for (int i = 0; i < 8; ++i) { S += __uint_as_float(pv[i][0]); Q += __uint_as_float(pv[i][1]); Dd += __uint_as_float(pv[i][2]); }
}
DI void row_stats_fin(rsrc_t rs, unsigned kdst, char* smem, int epoch, int tile, int lane, float S, float Q, float Dd, float wsum, const u32x4 (&x1p)[4],
                      float& mean, float& var, float& mb, float& rd) {
  S = half_swap_sum(S); Q = half_swap_sum(Q); Dd = half_swap_sum(Dd);
  mean = S * (1.0f / 1024.0f); var = fmaxf(Q * (1.0f / 1024.0f) - mean * mean, 0.f);
  float den;
  ln_scalars(mean, var, mb, den);
  rd = RCP(den);
  const unsigned rb = pack2((Dd - mb * wsum) * rd, 0.f) & 0xffffu;
  const unsigned mine = (((rb & 0x8000u) ? (~rb & 0xffffu) : (rb | 0x8000u)) << 16) | (unsigned)(2047 - (tile * 32 + (lane & 31)));
  const bool best = half_max_u32(mine) == mine;
  __builtin_amdgcn_raw_buffer_store_b32(mine, rs, (best && lane < 32) ? kdst : OOB, 0, 16);
  if (best) {
    char* c = smem + L_CAND + (tile & 3) * 144;
#pragma unroll
    for (int i = 0; i < 4; ++i) *reinterpret_cast<u32x4*>(c + (2 * i + (lane >> 5)) * 16) = x1p[i];
    *reinterpret_cast<u32x4*>(c + 128) = u32x4{mine, __float_as_uint(mb), __float_as_uint(rd), 0u};
  }
  lds_set((unsigned)(uintptr_t)smem + L_W + 32 + (tile & 3) * 4, epoch);
}

DI void attn_safe(rsrc_t rs, unsigned kvoff, const u32x8& q, const u32x4& ones, f32x16& o) {
  const s16x8 q0 = __builtin_bit_cast(s16x8, __builtin_shufflevector(q, q, 0, 1, 2, 3)), q1 = __builtin_bit_cast(s16x8, __builtin_shufflevector(q, q, 4, 5, 6, 7));
  f32x4 l = {0.f, 0.f, 0.f, 0.f};
#pragma unroll
  for (int e = 0; e < 16; ++e) o[e] = 0.f;
  float m = -INFINITY;
#pragma unroll 1
  for (int kt = 0; kt < 64; ++kt) {
    const unsigned p = kvoff + kt * 4096;
    const u32x4 k0 = ldg(rs, p), k1 = ldg(rs, p + 1024), v0 = ldg(rs, p + 2048), v1 = ldg(rs, p + 3072);
    f32x16 s = {};
    s = MFMA32(__builtin_bit_cast(s16x8, k0), q0, s);
    s = MFMA32(__builtin_bit_cast(s16x8, k1), q1, s);
    float mt = fmaxf(s[0], s[1]);
#pragma unroll
    for (int e = 2; e < 16; ++e) mt = fmaxf(mt, s[e]);
    const float mn = fmaxf(m, half_swap_max(mt));
    const float alpha = EX2(m - mn);
    m = mn;
#pragma unroll
    for (int e = 0; e < 16; ++e) { s[e] = EX2(s[e] - mn); o[e] *= alpha; }
    l[0] *= alpha;
    const bf16x16 pa = __builtin_bit_cast(bf16x16, __builtin_shufflevector(s, s, 0, 1, 2, 3, 4, 5, 6, 7));
    const bf16x16 pb = __builtin_bit_cast(bf16x16, __builtin_shufflevector(s, s, 8, 9, 10, 11, 12, 13, 14, 15));
    o = __builtin_amdgcn_smfmac_f32_32x32x32_bf16(__builtin_bit_cast(bf16x8, v0), pa, o, 0xdddd, 0, 0);
    l = __builtin_amdgcn_smfmac_f32_16x16x64_bf16(__builtin_bit_cast(bf16x8, ones), pa, l, 0xdddd, 0, 0);
    o = __builtin_amdgcn_smfmac_f32_32x32x32_bf16(__builtin_bit_cast(bf16x8, v1), pb, o, 0xdddd, 0, 0);
    l = __builtin_amdgcn_smfmac_f32_16x16x64_bf16(__builtin_bit_cast(bf16x8, ones), pb, l, 0xdddd, 0, 0);
  }
  o *= 1.0f / l[0];
}

extern "C" __global__ __launch_bounds__(512, 2) void mega_kernel(const u16* __restrict__ x0, const u16* __restrict__ wqkv, const u16* __restrict__ wo,
                                                              const u16* __restrict__ w_r, const u16* __restrict__ wg, const u16* __restrict__ wu,
                                                              const u16* __restrict__ wd, char* ws, u16* __restrict__ out) {
  __shared__ __attribute__((aligned(16))) char smem[LDS_BYTES];
  const int wid = __builtin_amdgcn_readfirstlane(threadIdx.x >> 6);
  STAMP_INIT;
  STAMPV(0);
  const int bid = blockIdx.x, xcd = bid & 7, loc = bid >> 3;
  const int tm = 8 * (xcd >> 2) + (loc >> 2), tn = 4 * (xcd & 3) + (loc & 3);
  const unsigned lbase = (unsigned)(uintptr_t)smem;
  const rsrc_t rs = __builtin_amdgcn_make_buffer_rsrc(ws, 0, (int)WS_BYTES, 0x00020000);
  volatile __attribute__((address_space(3))) unsigned* words = LDS_PTR(volatile unsigned, smem + L_W);
  u32x4 v[4];
  if (wid < 4)
    asm volatile("global_load_dwordx4 %0, %4, %5\n\tglobal_load_dwordx4 %1, %4, %5 offset:512\n\tglobal_load_dwordx4 %2, %4, %5 offset:1024\n\tglobal_load_dwordx4 %3, %4, %5 offset:1536"
                 : "=&v"(v[0]), "=&v"(v[1]), "=&v"(v[2]), "=&v"(v[3])
                 : "v"((threadIdx.x >> 5) * (D_ * 2) + (threadIdx.x & 31) * 16), "s"(x0 + (size_t)(tm * 128 + tn * 8) * D_) : "memory");
  else {
    asm volatile("" : "=v"(v[0]), "=v"(v[1]), "=v"(v[2]), "=v"(v[3]));
    if (wid == 6) {
      const char *c0, *c1;
      CODE_ADDR(mega_kernel, c0); CODE_ADDR(end_marker, c1);
      const unsigned o = (loc * 64 + (threadIdx.x & 63)) * 32;
      glds4(c0, (long long)o + 4 <= c1 - c0 ? o : 0u, lbase + NS * SLOTB);
    }
  }
  STAMPV(48);
  *reinterpret_cast<unsigned*>(smem + L_FULL + threadIdx.x * 4) = 0u;
  words[threadIdx.x & 31] = 0u;
  BAR_LGKM();
  if (wid < 4) {
    const int tid = threadIdx.x, lane = tid & 63, r32 = lane & 31, row = 2 * wid + (lane >> 5);
    char* sm = smem + L_XCH;
    unsigned dst = OFF_HF + (tm * 4 + (tn >> 2)) * TS + (tn & 3) * 128 + (tid >> 3) * 512 + (tid & 7) * 16;
    asm volatile("s_waitcnt vmcnt(0)" : "+v"(v[0]), "+v"(v[1]), "+v"(v[2]), "+v"(v[3]), "+v"(dst) :: "memory");
    STAMPV(49);
    float s = 0.f, s2 = 0.f;
#pragma unroll
    for (int i = 0; i < 4; ++i)
#pragma unroll
      for (int e = 0; e < 4; e += 2) { s = dot2(v[i][e], 0x3f803f80u, s); s2 = dot2(v[i][e + 1], 0x3f803f80u, s2); }
    const float mean = half_sum(s + s2) * (1.0f / 1024.0f);
    f32x2 m2 = {0.f, 0.f};
#pragma unroll
    for (int i = 0; i < 4; ++i)
#pragma unroll
      for (int e = 0; e < 4; ++e) { const f32x2 d = f32x2{lo16(v[i][e]), hi16(v[i][e])} - mean; m2 += d * d; }
    float mb, den;
    ln_scalars(mean, half_sum(m2[0] + m2[1]) * (1.0f / 1024.0f), mb, den);
    const float rd = 1.0f / den;
#pragma unroll
    for (int i = 0; i < 4; ++i) {
      char* p = sm + ((16 * i + (r32 >> 1)) * 2) * 144 + row * 16 + (r32 & 1) * 8;
      *reinterpret_cast<u32x2*>(p) = u32x2{ln_pair(v[i][0], mb, rd), ln_pair(v[i][1], mb, rd)};
      *reinterpret_cast<u32x2*>(p + 144) = u32x2{ln_pair(v[i][2], mb, rd), ln_pair(v[i][3], mb, rd)};
    }
    STAMPV(37);
    lds_set(lbase + L_W + 64 + 4 * wid, 1);
    lds_wait_ge<0>(lbase + L_W + 64 + 4 * (lane & 3), 1);
    STAMPV(50);
    u32x4 t[4];
#pragma unroll
    for (int i = 0; i < 4; ++i) t[i] = *reinterpret_cast<const u32x4*>(sm + (i * 32 + (tid >> 3)) * 144 + (tid & 7) * 16);
    SBAR();
#pragma unroll
    for (int i = 0; i < 4; ++i) stg(rs, dst + i * 16384, t[i]);
    STAMPV(51);
    DRAIN();
    lds_set(lbase + L_W + 64 + 4 * wid, 2);
    STAMPV(38);
  }
  const int lw = wid & 1;
  const char* wb = reinterpret_cast<const char*>(wqkv + (size_t)(16 * lw) * (3 * D_) + tn * 64);
  auto stage_lanes = [](unsigned (&wv)[3]) {
    const int lane = fresh_lane();
#pragma unroll
    for (int i = 0; i < 3; ++i) {
      const int li = lane + 64 * i, k = li / 24, cp = li % 24, c = cp ^ (4 * ((k >> 1) & 1));
      wv[i] = (unsigned)(k * (3 * D_ * 2) + (c >> 3) * (D_ * 2) + (c & 7) * 16 - i * 1024);
    }
  };
  auto stage = [&](const unsigned (&wv)[3], int s, int slot) {
    const char* p = wb + (size_t)(2 * kpos(tm, tn, s >> 1) + (s & 1)) * (32 * 3 * D_ * 2);
    glds_v3(p, wv, lbase + slot * SLOTB + lw * 6144);
    glds_v3(p + 8 * (3 * D_ * 2), wv, lbase + slot * SLOTB + lw * 6144 + 3072);
  };
  auto exchange = [&](const u32x2& tag, unsigned line0, int me, bool bar = false) {
    const int lane = fresh_lane();
    __builtin_amdgcn_raw_buffer_store_b64(tag, rs, lane == 0 ? line0 + me * 128 : OOB, 0, 16);
    if (bar) BARRIER();
    unsigned po = lane < 16 ? line0 + lane * 128 : OOB;
    const u32x4 srd = {(unsigned)(uintptr_t)ws, (unsigned)((uintptr_t)ws >> 32) & 0xffffu, WS_BYTES, 0x00020000u};
    for (unsigned spins = 0;; ++spins) {
      u32x2 v = {0u, 0u};
      asm volatile("buffer_atomic_or_x2 %0, %1, %2, 0 offen sc0 sc1\n\ts_waitcnt vmcnt(0)" : "+v"(v) : "v"(po), "s"(srd) : "memory");
      po = ((v[0] == tag[0]) & (v[1] == tag[1])) ? OOB : po;
      if (__all(po == OOB) || spins > SPIN_CAP) break;
    }
  };
  if ((wid >> 1) == 2) {
    unsigned wv[3];
    stage_lanes(wv);
    const int r = tm & 7;
#pragma unroll 1
    for (int i = 0; i < NS; ++i) {
      const int s = i == 0 ? r : i <= r ? i - 1 : i;
      stage(wv, s, s);
      const int j = i - 1, full = j < 1 ? (j == 0 && r == 0) : j < r ? j : j + 1;
      asm volatile("s_waitcnt vmcnt(%2)\n\tds_write_b32 %0, %1" :: "v"(lbase + L_FULL + lw * 256), "v"(full), "n"(6 * 1) : "memory");
    }
    asm volatile("s_waitcnt vmcnt(0)\n\tds_write_b32 %0, %1" :: "v"(lbase + L_FULL + lw * 256), "v"(NS) : "memory");
    STAMPV(1);
  }
  if (wid == 7) {
    u32x2 tag = __builtin_amdgcn_raw_buffer_load_b64(rs, FL_EPOCH, 0, 16);
    tag[0] += 1u; tag[1] += 0x9E3779B9u;
    *reinterpret_cast<u32x2*>(smem + L_TAG) = tag;
    lds_wait_ge<0>(lbase + L_W + 64 + 4 * (fresh_lane() & 3), 2);
    exchange(tag, FL_X + tm * 2048, tn);
    lds_set(lbase + L_W + 24, 1);
  }
  if (wid >= 4) {
    auto wo_rows = [&](int g0, int n, int gs) {
      const int lane = fresh_lane();
      unsigned wov[4];
#pragma unroll
      for (int i = 0; i < 4; ++i) wov[i] = (unsigned)((8 * i + (lane >> 3)) * (D_ * 2) + ((lane & 7) ^ (4 * ((lane >> 4) & 1))) * 16 - i * 1024);
#pragma unroll 1
      for (int i = 0, k = (2 * (tm & 7)) % n; i < n; ++i, k = k + 1 == n ? 0 : k + 1) {
        const int g = g0 + gs * k;
        glds_v4t<3, 1>(reinterpret_cast<const char*>(wo) + (size_t)((((tn >> 2) + 1) * 256 + 32 * g) & 1023) * (D_ * 2) + tn * 128, wov, lbase + g * 4096);
      }
    };
    if (wid < 6) {
      const int lane = fresh_lane();
      unsigned wv[3];
      stage_lanes(wv);
      __builtin_amdgcn_s_setprio(3);
      const unsigned fra = lbase + L_FREE + (lane & 3) * 256, fla = lbase + L_FULL + lw * 256 + lane * 4;
      const rsrc_t rw = __builtin_amdgcn_make_buffer_rsrc(const_cast<char*>(wb), 0, 6 * MBY - (16 * lw * 3 * D_ + tn * 64) * 2, 0x00020000);
#pragma unroll 1
      for (int L = 0; L < 2; ++L) {
        const int s0 = NS, lead = L ? 8 : NS - 1;
        int slot = 0;
#pragma unroll 1
        for (int s = NS; s < 32; ++s) {
          lds_wait_ge(fra, 32 * L + s - lead);
          stage(wv, s, slot);
          slot = (slot == NS - 1) ? 0 : slot + 1;
          if (s >= s0 + DD) asm volatile("s_waitcnt vmcnt(%0)\n\tds_write_b32 %1, %2" :: "n"(DD * 6), "v"(fla), "v"(32 * L + s - DD + 1) : "memory");
        }
#pragma unroll
        for (int d = DD - 1; d >= 0; --d) asm volatile("s_waitcnt vmcnt(%0)\n\tds_write_b32 %1, %2" :: "n"(d * 6), "v"(fla), "v"(32 * L + 32 - d) : "memory");
        u32x2 tag = *reinterpret_cast<const u32x2*>(smem + L_TAG);
        PIN(tag);
        if (lw == 0) {
          lds_wait_ge(lbase + L_W + 96 + 4 * (lane & 1), 1);
          lds_wait_ge<0>(lbase + L_W + 80 + 4 * (lane & 3), 2 * L + 1);
          exchange(tag, FL_KV + (L * 16 + tn) * 2048, tm, true);
          lds_wait_ge(fra, 32 * L + 32);
          lds_set(lbase + L_W + 16, L + 1);
        } else { BAR_LGKM(); lds_wait_ge<8>(lbase + L_W + 16, L + 1); }
        __builtin_amdgcn_s_sleep(24);
        wo_rows(lw, 15, 2);
        u32x4 pre[NS * 6];
        if (L == 0) {
#pragma unroll
          for (int s = 0; s < NS; ++s) {
#pragma unroll
            for (int i = 0; i < 6; ++i)
              pre[s * 6 + i] = __builtin_amdgcn_raw_buffer_load_b128(rw, wv[i % 3], (2 * kpos(tm, tn, s >> 1) + (s & 1)) * (32 * 3 * D_ * 2) + (i / 3) * (8 * 3 * D_ * 2) + (i % 3) * 1024, 0), PACE();
          }
        } else {
#pragma unroll
          for (int i = 0; i < NS * 6; ++i) asm volatile("" : "=v"(pre[i]));
        }
        STAMPV(7 + 16 * L);
        DRAIN();
        const unsigned fo = FL_O + (L * 16 + tm) * 2048;
        if (lw) {
          lds_set(lbase + L_W + 112, 2 * L + 1);
          lds_wait_ge<4>(lbase + L_W + 48 + 4 * (lane & 3), L + 1);
          lds_wait_ge<0>(lbase + L_W + 80 + 4 * (lane & 3), 2 * L + 2);
          __builtin_amdgcn_raw_buffer_store_b64(tag, rs, lane == 0 ? fo + tn * 128 : OOB, 0, 16);
          wo_rows(30, 2, 1);
          DRAIN();
          lds_set(lbase + L_W + 112, 2 * L + 2);
        } else {
          lds_wait_ge<4>(lbase + L_W + 112, 2 * L + 1);
          lds_wait_ge<4>(lbase + L_W + 48 + 4 * (lane & 3), L + 1);
          unsigned po = lane < 16 ? fo + ((lane + 4 + (tn & 12)) & 15) * 128 : OOB;
          const u32x4 srd = {(unsigned)(uintptr_t)ws, (unsigned)((uintptr_t)ws >> 32) & 0xffffu, WS_BYTES, 0x00020000u};
          for (unsigned spins = 0;; ++spins) {
            u32x2 v = {0u, 0u};
            asm volatile("buffer_atomic_or_x2 %0, %1, %2, 0 offen sc0 sc1\n\ts_waitcnt vmcnt(0)" : "+v"(v) : "v"(po), "s"(srd) : "memory");
            po = ((v[0] == tag[0]) & (v[1] == tag[1])) ? OOB : po;
            unsigned m = (unsigned)__ballot(po == OOB);
            m &= m >> 1; m &= m >> 2;
            m = (m & 1u) | ((m >> 3) & 2u) | ((m >> 6) & 4u) | ((m >> 9) & 8u);
            if (m == 15u || spins > SPIN_CAP) break;
            words[5 + 2 * L] = words[28] == 2u * L + 2u ? m : m & 7u;
          }
          words[5 + 2 * L] = words[28] == 2u * L + 2u ? 15u : 7u;
          lds_wait_ge<0>(lbase + L_W + 112, 2 * L + 2);
          words[5 + 2 * L] = 15u;
          __builtin_amdgcn_raw_buffer_store_b64(tag, rs, (bid == 0 && lane == 0) ? FL_EPOCH : OOB, 0, 16);
        }
        BAR_ALL();
        if (L == 0) {
          char* d = smem + lw * 6144 + fresh_lane() * 16;
#pragma unroll
          for (int s = 0; s < NS; ++s) {
#pragma unroll
            for (int i = 0; i < 6; ++i) *reinterpret_cast<u32x4*>(d + s * SLOTB + i * 1024) = pre[s * 6 + i];
            lds_set(fla, 32 + s + 1);
          }
          STAMPV(2);
          lds_wait_ge<2>(lbase + L_W + 24, 2);
        }
      }
      STAMP_END;
      return;
    }
    const int a = wid - 6;
    const bool ffn = loc < 28;
    const int grp = xcd * 7 + (ffn ? loc >> 2 : 0), qa = (loc & 3) * 2 + a;
    const int kr = loc, cg = xcd;
    const unsigned wa = lbase + L_W;
    lds_wait_ge<2>(wa + 24, 1);
    __builtin_amdgcn_s_sleep(16);
    auto transpose8 = [](u32x4* v) {
      u32x4 t[8];
#pragma unroll
      for (int j = 0; j < 8; ++j)
#pragma unroll
        for (int e = 0; e < 4; ++e) t[j][e] = (j & 1) ? PERM_HI(v[2 * e][j >> 1], v[2 * e + 1][j >> 1]) : PERM_LO(v[2 * e][j >> 1], v[2 * e + 1][j >> 1]);
#pragma unroll
      for (int j = 0; j < 8; ++j) { v[j] = t[j]; PIN(v[j]); }
    };
    u32x4 gf[32];
    u32x4 wdf[16];
    auto wait_go = [&](int L) {
      lds_wait_ge<8>(lbase + L_W + 16, L + 1);
      __builtin_amdgcn_s_sleep(24);
    };
    auto load_wd = [&](unsigned dv) {
      const rsrc_t rd = __builtin_amdgcn_make_buffer_rsrc(const_cast<u16*>(wd) + (size_t)grp * 64 * D_ + 128 * qa, 0, 64 * D_ * 2 - 256 * qa, 0x00020000);
#pragma unroll
      for (int i = 0; i < 16; ++i) {
        wdf[i] = __builtin_amdgcn_raw_buffer_load_b128(rd, dv, (32 * (i >> 3) + (i & 7)) * (D_ * 2), 0);
        asm volatile("s_sleep 2\n\ts_waitcnt vmcnt(3)" ::: "memory");
      }
    };
    {
      LANE_VARS;
      const u32x4 sv = {SENT, SENT, SENT, SENT};
#pragma unroll
      for (int i = 0; i < 6; ++i) {
        const unsigned o = ((i * 256 + bid) * 128 + a * 64 + lane) * 16;
        stg(rs, o < SX_BYTES ? OFF_SX + o : OOB, sv);
      }
      if (a == 0) {
        const u32x4 w0 = *reinterpret_cast<const u32x4*>(w_r + lane * 8), w1 = *reinterpret_cast<const u32x4*>(w_r + 512 + lane * 8);
        float t = 0.f;
#pragma unroll
        for (int e = 0; e < 4; ++e) t = fadd(fadd(t, fadd(lo16(w0[e]), hi16(w0[e]))), fadd(lo16(w1[e]), hi16(w1[e])));
        words[0] = __float_as_uint(wave_sum(t));
        if ((lane >> 3) == (tn & 7)) *reinterpret_cast<u32x4*>(smem + L_WR + (lane & 7) * 16) = tn < 8 ? w0 : w1;
      }
      const u16* p = wqkv + (size_t)(64 * tm + 32 * a + hi) * (3 * D_) + (c24 >> 3) * D_ + tn * 64 + (c24 & 7) * 8;
      float cs[8] = {0.f, 0.f, 0.f, 0.f, 0.f, 0.f, 0.f, 0.f};
      u32x4 v[16];
#pragma unroll
      for (int i = 0; i < 16; ++i) v[i] = *reinterpret_cast<const u32x4*>(p + (size_t)i * (2 * 3 * D_));
      SBAR();
#pragma unroll
      for (int i = 0; i < 16; ++i)
#pragma unroll
        for (int e = 0; e < 4; ++e) { cs[2 * e] = fadd(cs[2 * e], lo16(v[i][e])); cs[2 * e + 1] = fadd(cs[2 * e + 1], hi16(v[i][e])); }
      u32x4 o;
#pragma unroll
      for (int e = 0; e < 4; ++e) { const float x = half_swap_sum(cs[e]), y = half_swap_sum(cs[4 + e]); o[e] = __float_as_uint(hi ? y : x); }
      stg(rs, r32 < 24 ? OFF_CS + (tn * 32 + tm * 2 + a) * 768 + r32 * 32 + hi * 16 : OOB, o);
    }
    {
      DRAIN();
      lds_set(wa + 96 + 4 * a, 1);
      BAR_LGKM();
      LANE_VARS;
      const char* gp = reinterpret_cast<const char*>(((lane & 8) ? wu : wg) + (size_t)(128 * qa + 16 * hi + 4 * tblk) * F_ + 64 * grp + (lane & 7) * 8);
      unsigned dv = (lane >> 4) * (8 * D_ * 2) + (lane & 15) * 16;
      PIN(gp); PIN(dv);
      unsigned sv[3];
#pragma unroll
      for (int i = 0; i < 3; ++i) { const int li = lane + 64 * i, k = li / 24, c = li % 24; sv[i] = (unsigned)(k * (3 * D_ * 2) + (c >> 3) * (D_ * 2) + (c & 7) * 16 - i * 1024); PIN(sv[i]); }
      wait_go(0);
#pragma unroll
      for (int i = 0; i < 32; ++i) {
        gf[i] = *reinterpret_cast<const u32x4*>(gp + (size_t)(32 * (i >> 3) + 8 * ((i >> 2) & 1) + (i & 3)) * (F_ * 2));
        asm volatile("s_sleep 2\n\ts_waitcnt vmcnt(3)" ::: "memory");
      }
      load_wd(dv);
      STAMPV(7);
      lds_wait_ge<4>(wa + 80 + 4 * (lane & 3), 2);
#pragma unroll
      for (int g = 0; g < 4; ++g) glds_v3(wqkv + (size_t)(32 * kr + 8 * g) * (3 * D_) + (2 * cg + a) * 64, sv, lbase + L_SL + a * 12288 + g * 3072);
      transpose8(gf); transpose8(gf + 8); transpose8(gf + 16); transpose8(gf + 24);
      transpose8(wdf); transpose8(wdf + 8);
      {
        LANE_VARS;
        f32x4 t[2];
#pragma unroll
        for (int c = 0; c < 2; ++c) {
          u32x4 v[8];
#pragma unroll
          for (int i = 0; i < 8; ++i) v[i] = ldg(rs, OFF_CS + (tn * 32 + 16 * c + 2 * i + hi) * 768 + a * 384 + c24 * 16);
          SBAR();
          t[c] = sum4<8>(v);
          SBAR();
        }
#pragma unroll
        for (int e = 0; e < 4; ++e) t[0][e] = half_swap_sum(fadd(t[0][e], t[1][e]));
        if (lane < 24) *reinterpret_cast<f32x4*>(smem + L_CS + a * 384 + lane * 16) = t[0];
      }
      u32x2 tag = *reinterpret_cast<const u32x2*>(smem + L_TAG);
      PIN(tag);
      BAR_ALL();
      if (a) {
        lds_wait_ge<0>(wa + 64 + 4 * (lane & 3), 3);
        exchange(tag, FL_X + (16 + tm) * 2048, tn);
        lds_set(wa + 24, 2);
        STAMPV(12);
      }
    }
#pragma unroll 1
    for (int L = fresh_s(0);; ++L) {
      const unsigned sx = OFF_SX + L * SX_LAYER;
      STAMPV(1 + 16 * L);
      u32x4 hf[4];
      unsigned key;
      { LANE_VARS;
      lds_wait_ge<0>(wa + 32 + 4 * tp, L + 1);
      if (a == 0) {
        const char* cp = smem + L_CAND + (r32 >> 3) * 144;
        const u32x4 c = *reinterpret_cast<const u32x4*>(cp + (lane & 7) * 16), rec = *reinterpret_cast<const u32x4*>(cp + 128);
        u32x4 o;
#pragma unroll
        for (int e = 0; e < 4; ++e) { unsigned h = ln_pair_s(c[e], -U2F(rec[1]), U2F(rec[2])); PIN(h); o[e] = nz(hi ? c[e] : h); }
        stg(rs, sx + SX_ROW + hi * 131072 + (tm * 4 + (r32 >> 3)) * 2048 + tn * 128 + (lane & 7) * 16, o);
        STAMPV(8 + 16 * L);
        lds_wait_ge<0>(wa + 104, L + 1);
        key = __builtin_amdgcn_readfirstlane(words[27]);
      } else {
        const u32x4 srd = {(unsigned)(uintptr_t)ws, (unsigned)((uintptr_t)ws >> 32) & 0xffffu, WS_BYTES, 0x00020000u};
        unsigned po = sx + SX_CAND + (tn * 16 + (lane >> 2)) * 128 + tp * 4;
        key = 0u;
        for (unsigned spins = 0;; ++spins) {
          unsigned v = 0u;
          asm volatile("buffer_atomic_or %0, %1, %2, 0 offen sc0 sc1\n\ts_waitcnt vmcnt(0)" : "+v"(v) : "v"(po), "s"(srd) : "memory");
          key = v == SENT ? key : max(key, v);
          po = v == SENT ? po : OOB;
          if (__all(po == OOB) || spins > SPIN_CAP) break;
        }
        key = __builtin_amdgcn_readfirstlane(wave_max_u32(key));
        words[27] = key;
        words[3] = 2047u - (key & 2047u);
        lds_set(wa + 104, L + 1);
        STAMPV(13 + 16 * L);
      }
      poll_all<4>(hf, false, [&](u32x4 (&t)[4]) {
#pragma unroll
        for (int i = 0; i < 4; ++i) t[i] = ldg(rs, sx + SX_ROW + ((2047 - (int)(key & 2047u)) >> 5) * 2048 + (16 * qa + 4 * i + (lane >> 4)) * 16);
      }, [&](const u32x4 (&t)[4]) { return none_sent(t); });
      if (L == 1) lds_set(wa + 4 + 4 * a, 2);
      }
      STAMPV(2 + 16 * L);
      f32x4 d[8];
      const f32x4 zero = {0.f, 0.f, 0.f, 0.f};
      SBAR();
      __builtin_amdgcn_s_setprio(3);
#pragma unroll
      for (int t = 0; t < 32; ++t) d[t & 7] = MFMA16(__builtin_bit_cast(s16x8, hf[t >> 3]), __builtin_bit_cast(s16x8, gf[t]), t < 8 ? zero : d[t & 7]);
      SBAR();
      __builtin_amdgcn_s_setprio(0);
      LANE_VARS;
      const int idx = 2047 - (int)(key & 2047u), win = idx >> 7;
      const bool owner = win == tm;
      auto pick2 = [&](const f32x4 (&d)[8]) {
        u32x2 o;
#pragma unroll
        for (int e = 0; e < 2; ++e) {
          const float x = tblk ? d[2 + e][0] : d[e][0], y = tblk ? d[6 + e][0] : d[4 + e][0];
          o[e] = nzf(hi ? y : x);
        }
        return o;
      };
      if (ffn) {
        const unsigned ppos = (lane & 15) * 32 + (lane >> 4) * 8;
        __builtin_amdgcn_raw_buffer_store_b64(pick2(d), rs, sx + SX_GP + (grp * 8 + qa) * 512 + ppos, 0, 16);
        STAMPV(9 + 16 * L);
        __builtin_amdgcn_s_sleep(16);
        u32x2 gu[8];
        for (unsigned spins = 0;; ++spins) {
#pragma unroll
          for (int i = 0; i < 8; ++i) gu[i] = __builtin_amdgcn_raw_buffer_load_b64(rs, sx + SX_GP + (grp * 8 + i) * 512 + lane * 8, 0, 16);
          SBAR();
          unsigned m = 0u;
#pragma unroll
          for (int i = 0; i < 8; ++i) m = max(max(m, gu[i][0]), gu[i][1]);
          if (__all(m != SENT) || spins > SPIN_CAP) break;
          poll_pause(L == 0);
        }
        STAMPV(10 + 16 * L);
        float t0 = opq(U2F(gu[0][0])), t1 = opq(U2F(gu[0][1]));
#pragma unroll
        for (int i = 1; i < 8; ++i) { t0 = fadd(t0, U2F(gu[i][0])); t1 = fadd(t1, U2F(gu[i][1])); }
        auto sw = __builtin_amdgcn_permlane32_swap(__float_as_uint(t0), __float_as_uint(t1), false, false);
        const float gv = rbf(U2F(sw[0])), uv = rbf(U2F(sw[1]));
        reinterpret_cast<u16*>(smem + L_GU + a * 128)[2 * r32 + hi] = (u16)pack2(fmul(rbf(fmul(gv, RCP(fadd(1.0f, __expf(-gv))))), uv), 0.f);
        asm volatile("s_waitcnt lgkmcnt(0)" ::: "memory");
        const s16x8 a0 = *reinterpret_cast<const s16x8*>(smem + L_GU + a * 128 + (lane >> 4) * 16), a1 = *reinterpret_cast<const s16x8*>(smem + L_GU + a * 128 + 64 + (lane >> 4) * 16);
        SBAR();
        __builtin_amdgcn_s_setprio(3);
#pragma unroll
        for (int t = 0; t < 16; ++t) d[t & 7] = MFMA16(t < 8 ? a0 : a1, __builtin_bit_cast(s16x8, wdf[t]), t < 8 ? zero : d[t & 7]);
        SBAR();
        __builtin_amdgcn_s_setprio(0);
        __builtin_amdgcn_raw_buffer_store_b64(pick2(d), rs, sx + SX_YP + grp * 4096 + qa * 512 + ppos, 0, 16);
      }
      STAMPV(3 + 16 * L);
      if (L == 1 && (cg != (kr & 7) || a)) { STAMP_END; __builtin_amdgcn_endpgm(); }
      s16x8 wpf[12];
#pragma unroll
      for (int t = 0; t < 12; ++t) wpf[t] = tr2(smem + L_SL + a * 12288 + (16 * hi + 4 * tblk + tq) * 384 + t * 32 + tp * 8, 8 * 384);
#pragma unroll
      for (int t = 0; t < 12; ++t) PIN(wpf[t]);
      if (L == 0 && !owner) lds_set(wa + 4 + 4 * a, 1);
      __builtin_amdgcn_s_sleep(9);
      if (L == 0 && !ffn) __builtin_amdgcn_s_sleep(64);
      const unsigned rk = key >> 16;
      float rsig = rbf(RCP(fadd(1.0f, __expf(-lo16((rk & 0x8000u) ? (rk & 0x7fffu) : (~rk & 0xffffu))))));
      PIN(rsig);
      const int n = 4 * (lane & 7) + ((lane >> 3) & 3);
      const int fo = ((n >> 4) * 2 + ((n >> 2) & 1)) * 8 + ((n >> 3) & 1) * 4 + (n & 3);
      u32x4 pv[8];
      poll_all<8>(pv, L == 0, [&](u32x4 (&t)[8]) {
#pragma unroll
        for (int i = 0; i < 7; ++i) t[i] = ldg(rs, sx + SX_YP + (8 * i + (lane >> 3)) * 4096 + kr * 128 + (lane & 7) * 16);
        t[7][0] = t[7][2] = __builtin_amdgcn_raw_buffer_load_b32(rs, sx + SX_ROW + (idx >> 5) * 2048 + kr * 64 + (fo >> 1) * 4, 0, 16);
        t[7][1] = t[7][3] = __builtin_amdgcn_raw_buffer_load_b32(rs, sx + SX_ROW + 131072 + (idx >> 5) * 2048 + kr * 64 + (fo >> 1) * 4, 0, 16);
      }, [&](const u32x4 (&t)[8]) { return none_sent(t); });
      const float hv = (fo & 1) ? hi16(pv[7][0]) : lo16(pv[7][0]), xv = (fo & 1) ? hi16(pv[7][1]) : lo16(pv[7][1]);
      STAMPV(4 + 16 * L);
      float ys;
      {
        f32x4 t = sum4<7>(pv);
#pragma unroll
        for (int e = 0; e < 4; ++e) {
          t[e] = fadd(t[e], U2F(dpp<0x128>(__float_as_uint(t[e]))));
          t[e] = half_swap_sum(row_swap_sum(t[e]));
        }
        const float x = (lane & 8) ? t[1] : t[0], y = (lane & 8) ? t[3] : t[2];
        ys = tblk ? y : x;
      }
      const unsigned ob = pack2(fadd(xv, rbf(fadd(hv, rbf(fmul(rbf(ys), rsig))))), 0.f) & 0xffffu;
      if (L == 1) {
        if (lane < 32) out[(size_t)idx * D_ + kr * 32 + n] = (u16)ob;
        STAMPV(5 + 16 * L);
        STAMP_END;
        __builtin_amdgcn_endpgm();
      }
      reinterpret_cast<u16*>(smem + L_X2F + a * 64)[fo] = (u16)ob;
      asm volatile("s_waitcnt lgkmcnt(0)" ::: "memory");
      {
        const s16x8 xa = *reinterpret_cast<const s16x8*>(smem + L_X2F + a * 64 + (lane >> 4) * 16);
        const u32x4 xf = *reinterpret_cast<const u32x4*>(smem + L_X2F + a * 64 + tp * 16);
        f32x4 z[12];
        SBAR();
        __builtin_amdgcn_s_setprio(3);
#pragma unroll
        for (int t = 0; t < 12; ++t) z[t] = MFMA16(xa, wpf[t], zero);
        SBAR();
        __builtin_amdgcn_s_setprio(0);
        const float x2 = lo16(ob), s1 = half_sum(x2), s2 = half_sum(fmul(x2, x2));
        u32x4 o;
#pragma unroll
        for (int e = 0; e < 4; ++e) {
          const unsigned sp = lane < 52 ? (e == 0 ? __float_as_uint(s1) : __float_as_uint(s2)) : xf[e];
          const unsigned x = __float_as_uint(tblk ? z[4 + e][0] : z[e][0]), y = tblk ? sp : __float_as_uint(z[8 + e][0]);
          o[e] = nz(hi ? y : x);
        }
        const unsigned pos = lane < 24 ? lane * 16 : lane < 48 ? lane * 16 + 128 : lane < 52 ? (lane & 1) * 512 + 384 : 400 + tp * 16;
        stg(rs, lane < 56 ? OFF_SX + SX_QP + ((2 * cg + a) * 32 + kr) * 1024 + pos : OOB, o);
      }
      STAMPV(5 + 16 * L);
      if (owner) {
        __builtin_amdgcn_s_sleep(13);
        const int c25 = r32 < 25 ? r32 : 24, pl = 24 * a + c24, col = 64 * (pl >> 4) + (pl & 15);
        float cs[4];
#pragma unroll
        for (int e = 0; e < 4; ++e) { cs[e] = reinterpret_cast<const float*>(smem + L_CS)[col + 16 * e]; PIN(cs[e]); }
        u32x4 qv[17];
        poll_all<17>(qv, true, [&](u32x4 (&t)[17]) {
#pragma unroll
          for (int i = 0; i < 16; ++i) t[i] = ldg(rs, OFF_SX + SX_QP + (tn * 32 + 2 * i + hi) * 1024 + a * 512 + c25 * 16);
          t[16] = ldg(rs, OFF_SX + SX_QP + (tn * 34 + hi) * 1024 + 400 + tp * 16);
        }, [&](const u32x4 (&t)[17]) { return none_sent(t); });
        *reinterpret_cast<u32x4*>(smem + L_X2P + hi * 64 + tp * 16) = qv[16];
        f32x4 t = sum4<16>(qv);
#pragma unroll
        for (int e = 0; e < 4; ++e) t[e] = half_swap_sum(t[e]);
        const float mean = fmul(U2F(__builtin_amdgcn_readlane(__float_as_uint(t[0]), 24)), 1.0f / 1024.0f);
        const float var = fmaxf(ffma(-mean, mean, fmul(U2F(__builtin_amdgcn_readlane(__float_as_uint(t[1]), 24)), 1.0f / 1024.0f)), 0.f);
        float mb, den;
        ln_scalars(mean, var, mb, den);
        const float rd = RCP(den);
        if (lane < 24) {
#pragma unroll
          for (int e = 0; e < 4; ++e) reinterpret_cast<float*>(smem + L_PATCH)[col + 16 * e] = fmul(ffma(-mb, cs[e], t[e]), rd);
        }
        lds_set(wa + 4 + 4 * a, L + 1);
      }
      STAMPV(6 + 16 * L);
      BAR_ALL();
      unsigned dv = fresh_lane();
      dv = (dv >> 4) * (8 * D_ * 2) + (dv & 15) * 16;
      PIN(dv);
      wait_go(1);
      load_wd(dv);
      STAMPV(23);
      lds_wait_ge<4>(wa + 80 + 4 * (fresh_lane() & 3), 4);
      transpose8(wdf); transpose8(wdf + 8);
      BARRIER();
    }
  }
  const int tm_ = tm, tn_ = tn, wid_ = wid;
#define KSKIP "{s49}"(tn * 4096), "{s50}"(((tn + 1) & 15) * 4096)
  const u32x4 srd = {(unsigned)(uintptr_t)ws, (unsigned)((uintptr_t)ws >> 32) & 0xffffu, WS_BYTES, 0x00020000u};
  u32x4 x1p[4];
  asm volatile("" : "=v"(x1p[0]), "=v"(x1p[1]), "=v"(x1p[2]), "=v"(x1p[3]));
#pragma unroll 1
  for (int L = 0; L < 2; ++L) {
    const unsigned sx = OFF_SX + L * SX_LAYER;
    u32x8 q[4];
    u32x16 kva, kvb;
    f32x16 oz[5];
    {
      f32x16 acc[6];
      u32x16 stA, stB, xr0, xr1, xr2, xr3;
      u32x4 stC;
      asm volatile("" : "={v[112:127]}"(stB), "={v[128:131]}"(stC));
      { PHASE_VARS;
      stB[8] = lane * 16 + (L ? OFF_X1 : OFF_HF) + tile * TS;
      stB[9] = lbase + wrow * 384 + (((4 * hh + wch) ^ wsw) << 4) + 8 * (tp & 1);
      stB[10] = lbase + wrow * 384 + (((4 * (hh ^ 1) + wch) ^ wsw) << 4) + 8 * (tp & 1);
      stB[13] = lbase + L_FULL + (lane & 1) * 256;
      stB[15] = lbase + L_FREE + wid * 256 + lane * 4;
      stC[0] = 32 * L; }
      unsigned soff, slot, need = 32 * L + 1;
      if (L == 0) {
        lds_wait_ge<0>(lbase + L_W + 24, 1);
        STAMPV(39);
        soff = tn * 4096;
        asm volatile(
          "buffer_load_dwordx4 v[132:135], v120, s[40:43], s44 offen offset:0 sc1\n\t"
          "buffer_load_dwordx4 v[136:139], v120, s[40:43], s44 offen offset:1024 sc1\n\t"
          "buffer_load_dwordx4 v[140:143], v120, s[40:43], s44 offen offset:2048 sc1\n\t"
          "buffer_load_dwordx4 v[144:147], v120, s[40:43], s44 offen offset:3072 sc1\n\t"
          "s_add_u32 s44, s51, 0\n\t"
          "buffer_load_dwordx4 v[148:151], v120, s[40:43], s44 offen offset:0 sc1\n\t"
          "buffer_load_dwordx4 v[152:155], v120, s[40:43], s44 offen offset:1024 sc1\n\t"
          "buffer_load_dwordx4 v[156:159], v120, s[40:43], s44 offen offset:2048 sc1\n\t"
          "buffer_load_dwordx4 v[160:163], v120, s[40:43], s44 offen offset:3072 sc1\n\t"
          "s_add_u32 s44, s44, 0x1000\n\t"
          "s_and_b32 s44, s44, 0xffff\n\t"
          "s_cmp_eq_u32 s44, s49\n\t"
          "s_cselect_b32 s44, s50, s44\n\t"
          "buffer_load_dwordx4 v[164:167], v120, s[40:43], s44 offen offset:0 sc1\n\t"
          "buffer_load_dwordx4 v[168:171], v120, s[40:43], s44 offen offset:1024 sc1\n\t"
          "buffer_load_dwordx4 v[172:175], v120, s[40:43], s44 offen offset:2048 sc1\n\t"
          "buffer_load_dwordx4 v[176:179], v120, s[40:43], s44 offen offset:3072 sc1\n\t"
          "s_add_u32 s44, s44, 0x1000\n\t"
          "s_and_b32 s44, s44, 0xffff\n\t"
          "s_cmp_eq_u32 s44, s49\n\t"
          "s_cselect_b32 s44, s50, s44\n\t"
          "buffer_load_dwordx4 v[180:183], v120, s[40:43], s44 offen offset:0 sc1\n\t"
          "buffer_load_dwordx4 v[184:187], v120, s[40:43], s44 offen offset:1024 sc1\n\t"
          "buffer_load_dwordx4 v[188:191], v120, s[40:43], s44 offen offset:2048 sc1\n\t"
          "buffer_load_dwordx4 v[192:195], v120, s[40:43], s44 offen offset:3072 sc1\n\t"
          : "={v[132:147]}"(xr0), "={v[148:163]}"(xr1), "={v[164:179]}"(xr2), "={v[180:195]}"(xr3), "+{s44}"(soff) : "{v[112:127]}"(stB), "{s[40:43]}"(srd), KSKIP, "{s51}"(kpos(tm, tn, 1) * 4096) : "scc", "memory");
      } else {
        xr0 = cat4(x1p[0], x1p[1], x1p[2], x1p[3]);
        asm volatile("" : "={v[148:163]}"(xr1), "={v[164:179]}"(xr2), "={v[180:195]}"(xr3));
      }
      STAMPV(1 + 16 * L);
      asm volatile(
        "v_mov_b32 v123, v121\n\t"
        "v_mov_b32 v124, v122\n\t"
        "s_mov_b64 vcc, -1\n\t"
        "s_cbranch_vccnz 10f\n\t"
        "11:\n\t"
        "s_mov_b32 s46, 12288\n\t"
        "s_add_u32 s47, s47, 1\n\t"
        "ds_read_b64_tr_b16 v[96:97], v123 offset:0\n\t"
        "ds_read_b64_tr_b16 v[98:99], v123 offset:3072\n\t"
        "ds_read_b64_tr_b16 v[100:101], v124 offset:0\n\t"
        "ds_read_b64_tr_b16 v[102:103], v124 offset:3072\n\t"
        "ds_read_b64_tr_b16 v[104:105], v123 offset:128\n\t"
        "ds_read_b64_tr_b16 v[106:107], v123 offset:3200\n\t"
        "ds_read_b64_tr_b16 v[108:109], v124 offset:128\n\t"
        "ds_read_b64_tr_b16 v[110:111], v124 offset:3200\n\t"
        "ds_read_b64_tr_b16 v[112:113], v123 offset:256\n\t"
        "ds_read_b64_tr_b16 v[114:115], v123 offset:3328\n\t"
        "ds_write_b32 v127, v128\n\t"
        "ds_read_b64_tr_b16 v[116:117], v124 offset:256\n\t"
        "ds_read_b64_tr_b16 v[118:119], v124 offset:3328\n\t"
        "s_waitcnt vmcnt(15) lgkmcnt(11)\n\t"
        "v_mfma_f32_32x32x16_bf16 v[0:15], v[96:99], v[132:135], 0\n\t"
        "ds_read_b32 v126, v125\n\t"
        "ds_read_b64_tr_b16 v[96:97], v123 offset:6144\n\t"
        "ds_read_b64_tr_b16 v[98:99], v123 offset:9216\n\t"
        "s_waitcnt lgkmcnt(12)\n\t"
        "v_mfma_f32_32x32x16_bf16 v[16:31], v[100:103], v[132:135], 0\n\t"
        "v_add_u32 v129, s46, v121\n\t"
        "ds_read_b64_tr_b16 v[100:101], v124 offset:6144\n\t"
        "ds_read_b64_tr_b16 v[102:103], v124 offset:9216\n\t"
        "s_waitcnt lgkmcnt(12)\n\t"
        "v_mfma_f32_32x32x16_bf16 v[32:47], v[104:107], v[132:135], 0\n\t"
        "ds_read_b64_tr_b16 v[104:105], v123 offset:6272\n\t"
        "ds_read_b64_tr_b16 v[106:107], v123 offset:9344\n\t"
        "s_waitcnt lgkmcnt(12)\n\t"
        "v_mfma_f32_32x32x16_bf16 v[48:63], v[108:111], v[132:135], 0\n\t"
        "v_add_u32 v130, s46, v122\n\t"
        "ds_read_b64_tr_b16 v[108:109], v124 offset:6272\n\t"
        "ds_read_b64_tr_b16 v[110:111], v124 offset:9344\n\t"
        "s_waitcnt lgkmcnt(12)\n\t"
        "v_mfma_f32_32x32x16_bf16 v[64:79], v[132:135], v[112:115], 0\n\t"
        "ds_read_b64_tr_b16 v[112:113], v123 offset:6400\n\t"
        "ds_read_b64_tr_b16 v[114:115], v123 offset:9472\n\t"
        "s_waitcnt lgkmcnt(10)\n\t"
        "v_mfma_f32_32x32x16_bf16 v[80:95], v[132:135], v[116:119], 0\n\t"
        "v_cmp_gt_u32 vcc, s47, v126\n\t"
        "ds_read_b64_tr_b16 v[116:117], v124 offset:6400\n\t"
        "ds_read_b64_tr_b16 v[118:119], v124 offset:9472\n\t"
        "s_waitcnt vmcnt(14) lgkmcnt(10)\n\t"
        "v_mfma_f32_32x32x16_bf16 v[0:15], v[96:99], v[136:139], v[0:15]\n\t"
        "s_cbranch_vccnz 12f\n\t"
        "13:\n\t"
        "ds_read_b64_tr_b16 v[96:97], v129 offset:0\n\t"
        "ds_read_b64_tr_b16 v[98:99], v129 offset:3072\n\t"
        "s_waitcnt lgkmcnt(10)\n\t"
        "v_mfma_f32_32x32x16_bf16 v[16:31], v[100:103], v[136:139], v[16:31]\n\t"
        "v_add_u32 v128, 1, v128\n\t"
        "ds_read_b64_tr_b16 v[100:101], v130 offset:0\n\t"
        "ds_read_b64_tr_b16 v[102:103], v130 offset:3072\n\t"
        "s_waitcnt lgkmcnt(10)\n\t"
        "v_mfma_f32_32x32x16_bf16 v[32:47], v[104:107], v[136:139], v[32:47]\n\t"
        "ds_read_b64_tr_b16 v[104:105], v129 offset:128\n\t"
        "ds_read_b64_tr_b16 v[106:107], v129 offset:3200\n\t"
        "s_waitcnt lgkmcnt(10)\n\t"
        "v_mfma_f32_32x32x16_bf16 v[48:63], v[108:111], v[136:139], v[48:63]\n\t"
        "s_add_u32 s46, s46, 12288\n\t"
        "s_add_u32 s47, s47, 1\n\t"
        "ds_read_b64_tr_b16 v[108:109], v130 offset:128\n\t"
        "ds_read_b64_tr_b16 v[110:111], v130 offset:3200\n\t"
        "s_waitcnt lgkmcnt(10)\n\t"
        "v_mfma_f32_32x32x16_bf16 v[64:79], v[136:139], v[112:115], v[64:79]\n\t"
        "s_cmp_eq_u32 s46, 110592\n\t"
        "s_cselect_b32 s46, 0, s46\n\t"
        "ds_read_b64_tr_b16 v[112:113], v129 offset:256\n\t"
        "ds_read_b64_tr_b16 v[114:115], v129 offset:3328\n\t"
        "s_waitcnt lgkmcnt(10)\n\t"
        "v_mfma_f32_32x32x16_bf16 v[80:95], v[136:139], v[116:119], v[80:95]\n\t"
        "ds_write_b32 v127, v128\n\t"
        "ds_read_b64_tr_b16 v[116:117], v130 offset:256\n\t"
        "ds_read_b64_tr_b16 v[118:119], v130 offset:3328\n\t"
        "s_waitcnt vmcnt(13) lgkmcnt(11)\n\t"
        "v_mfma_f32_32x32x16_bf16 v[0:15], v[96:99], v[140:143], v[0:15]\n\t"
        "ds_read_b32 v126, v125\n\t"
        "ds_read_b64_tr_b16 v[96:97], v129 offset:6144\n\t"
        "ds_read_b64_tr_b16 v[98:99], v129 offset:9216\n\t"
        "s_waitcnt lgkmcnt(12)\n\t"
        "v_mfma_f32_32x32x16_bf16 v[16:31], v[100:103], v[140:143], v[16:31]\n\t"
        "v_add_u32 v123, s46, v121\n\t"
        "ds_read_b64_tr_b16 v[100:101], v130 offset:6144\n\t"
        "ds_read_b64_tr_b16 v[102:103], v130 offset:9216\n\t"
        "s_waitcnt lgkmcnt(12)\n\t"
        "v_mfma_f32_32x32x16_bf16 v[32:47], v[104:107], v[140:143], v[32:47]\n\t"
        "ds_read_b64_tr_b16 v[104:105], v129 offset:6272\n\t"
        "ds_read_b64_tr_b16 v[106:107], v129 offset:9344\n\t"
        "s_waitcnt lgkmcnt(12)\n\t"
        "v_mfma_f32_32x32x16_bf16 v[48:63], v[108:111], v[140:143], v[48:63]\n\t"
        "v_add_u32 v124, s46, v122\n\t"
        "ds_read_b64_tr_b16 v[108:109], v130 offset:6272\n\t"
        "ds_read_b64_tr_b16 v[110:111], v130 offset:9344\n\t"
        "s_waitcnt lgkmcnt(12)\n\t"
        "v_mfma_f32_32x32x16_bf16 v[64:79], v[140:143], v[112:115], v[64:79]\n\t"
        "ds_read_b64_tr_b16 v[112:113], v129 offset:6400\n\t"
        "ds_read_b64_tr_b16 v[114:115], v129 offset:9472\n\t"
        "s_waitcnt lgkmcnt(10)\n\t"
        "v_mfma_f32_32x32x16_bf16 v[80:95], v[140:143], v[116:119], v[80:95]\n\t"
        "v_cmp_gt_u32 vcc, s47, v126\n\t"
        "ds_read_b64_tr_b16 v[116:117], v130 offset:6400\n\t"
        "ds_read_b64_tr_b16 v[118:119], v130 offset:9472\n\t"
        "s_waitcnt vmcnt(12) lgkmcnt(10)\n\t"
        "v_mfma_f32_32x32x16_bf16 v[0:15], v[96:99], v[144:147], v[0:15]\n\t"
        "s_cbranch_vccnz 14f\n\t"
        "15:\n\t"
        "ds_read_b64_tr_b16 v[96:97], v123 offset:0\n\t"
        "ds_read_b64_tr_b16 v[98:99], v123 offset:3072\n\t"
        "s_waitcnt lgkmcnt(10)\n\t"
        "v_mfma_f32_32x32x16_bf16 v[16:31], v[100:103], v[144:147], v[16:31]\n\t"
        "v_add_u32 v128, 1, v128\n\t"
        "ds_read_b64_tr_b16 v[100:101], v124 offset:0\n\t"
        "ds_read_b64_tr_b16 v[102:103], v124 offset:3072\n\t"
        "s_waitcnt lgkmcnt(10)\n\t"
        "v_mfma_f32_32x32x16_bf16 v[32:47], v[104:107], v[144:147], v[32:47]\n\t"
        "ds_read_b64_tr_b16 v[104:105], v123 offset:128\n\t"
        "ds_read_b64_tr_b16 v[106:107], v123 offset:3200\n\t"
        "s_waitcnt lgkmcnt(10)\n\t"
        "v_mfma_f32_32x32x16_bf16 v[48:63], v[108:111], v[144:147], v[48:63]\n\t"
        "s_add_u32 s46, s46, 12288\n\t"
        "s_add_u32 s47, s47, 1\n\t"
        "ds_read_b64_tr_b16 v[108:109], v124 offset:128\n\t"
        "ds_read_b64_tr_b16 v[110:111], v124 offset:3200\n\t"
        "s_waitcnt lgkmcnt(10)\n\t"
        "v_mfma_f32_32x32x16_bf16 v[64:79], v[144:147], v[112:115], v[64:79]\n\t"
        "s_cmp_eq_u32 s46, 110592\n\t"
        "s_cselect_b32 s46, 0, s46\n\t"
        "ds_read_b64_tr_b16 v[112:113], v123 offset:256\n\t"
        "ds_read_b64_tr_b16 v[114:115], v123 offset:3328\n\t"
        "s_waitcnt lgkmcnt(10)\n\t"
        "v_mfma_f32_32x32x16_bf16 v[80:95], v[144:147], v[116:119], v[80:95]\n\t"
        "ds_write_b32 v127, v128\n\t"
        "ds_read_b64_tr_b16 v[116:117], v124 offset:256\n\t"
        "ds_read_b64_tr_b16 v[118:119], v124 offset:3328\n\t"
        "s_waitcnt lgkmcnt(0)\n\t"
        "s_nop 7\n\t"
        "s_branch 9f\n\t"
        "10:\n\t"
        "s_mov_b32 s48, 0x10000\n\t"
        "8:\n\t"
        "ds_read_b32 v126, v125\n\t"
        "s_sub_u32 s48, s48, 1\n\t"
        "s_waitcnt lgkmcnt(0)\n\t"
        "s_cbranch_scc1 11b\n\t"
        "v_cmp_gt_u32 vcc, s47, v126\n\t"
        "s_cbranch_vccnz 8b\n\t"
        "s_branch 11b\n\t"
        "12:\n\t"
        "s_mov_b32 s48, 0x10000\n\t"
        "8:\n\t"
        "ds_read_b32 v126, v125\n\t"
        "s_sub_u32 s48, s48, 1\n\t"
        "s_waitcnt lgkmcnt(0)\n\t"
        "s_cbranch_scc1 13b\n\t"
        "v_cmp_gt_u32 vcc, s47, v126\n\t"
        "s_cbranch_vccnz 8b\n\t"
        "s_branch 13b\n\t"
        "14:\n\t"
        "s_mov_b32 s48, 0x10000\n\t"
        "8:\n\t"
        "ds_read_b32 v126, v125\n\t"
        "s_sub_u32 s48, s48, 1\n\t"
        "s_waitcnt lgkmcnt(0)\n\t"
        "s_cbranch_scc1 15b\n\t"
        "v_cmp_gt_u32 vcc, s47, v126\n\t"
        "s_cbranch_vccnz 8b\n\t"
        "s_branch 15b\n\t"
        "9:\n\t"
        : "={v[0:15]}"(acc[0]), "={v[16:31]}"(acc[1]), "={v[32:47]}"(acc[2]), "={v[48:63]}"(acc[3]), "={v[64:79]}"(acc[4]), "={v[80:95]}"(acc[5]),
          "={v[96:111]}"(stA), "+{v[112:127]}"(stB), "+{v[128:131]}"(stC), "={s46}"(slot), "+{s47}"(need)
        : "{v[132:147]}"(xr0) : "s48", "vcc", "scc", "memory");
      STAMPV(2 + 16 * L);
      float al = 1.f;
      if (L == 1) {
        PHASE_VARS;
        float S, Q, Dd;
        row_stats_sum(rs, OFF_SX, tile, lane, S, Q, Dd);
        STAMPV(36);
        lds_wait_ge<0>(lbase + L_W + 24, 2);
        soff = kpos(tm, tn, 1) * 4096;
        asm volatile(
          "buffer_load_dwordx4 v[148:151], v120, s[40:43], s44 offen offset:0 sc1\n\t"
          "buffer_load_dwordx4 v[152:155], v120, s[40:43], s44 offen offset:1024 sc1\n\t"
          "buffer_load_dwordx4 v[156:159], v120, s[40:43], s44 offen offset:2048 sc1\n\t"
          "buffer_load_dwordx4 v[160:163], v120, s[40:43], s44 offen offset:3072 sc1\n\t"
          : "={v[148:163]}"(xr1), "={v[164:179]}"(xr2), "={v[180:195]}"(xr3), "+{s44}"(soff), "+v"(S), "+v"(Q), "+v"(Dd) : "{v[112:127]}"(stB), "{s[40:43]}"(srd), KSKIP, "{s51}"(kpos(tm, tn, 1) * 4096) : "scc", "memory");
        float mean, var, mb1, rd1, mb2, den2;
        row_stats_fin(rs, OFF_SX + SX_CAND + (tn * 16 + tm) * 128 + wid * 4, smem, 1, tile, lane, S, Q, Dd, U2F(words[0]), x1p, mean, var, mb1, rd1);
        ln_scalars(mean + (mean - mb1) * rd1, var * (1.0f + rd1) * (1.0f + rd1), mb2, den2);
        const float rd2 = RCP(den2);
        al = (1.0f + rd1) * rd2;
        const float be = -(mb1 * rd1 + mb2) * RCP(1.0f + rd1);
        reinterpret_cast<float*>(smem + L_AB)[wid * 64 + lane] = hi ? be : al;
        reinterpret_cast<float*>(smem + L_AB)[256 + wid * 64 + lane] = hi ? 1.0f + rd1 : -mb1 * rd1;
        soff = kpos(tm, tn, 1 + 0) * 4096;
        asm volatile(
          "s_nop 0\n\t"
          "s_add_u32 s44, s44, 0x1000\n\t"
          "s_and_b32 s44, s44, 0xffff\n\t"
          "s_cmp_eq_u32 s44, s49\n\t"
          "s_cselect_b32 s44, s50, s44\n\t"
          "buffer_load_dwordx4 v[164:167], v120, s[40:43], s44 offen offset:0 sc1\n\t"
          "buffer_load_dwordx4 v[168:171], v120, s[40:43], s44 offen offset:1024 sc1\n\t"
          "buffer_load_dwordx4 v[172:175], v120, s[40:43], s44 offen offset:2048 sc1\n\t"
          "buffer_load_dwordx4 v[176:179], v120, s[40:43], s44 offen offset:3072 sc1\n\t"
          "s_add_u32 s44, s44, 0x1000\n\t"
          "s_and_b32 s44, s44, 0xffff\n\t"
          "s_cmp_eq_u32 s44, s49\n\t"
          "s_cselect_b32 s44, s50, s44\n\t"
          "buffer_load_dwordx4 v[180:183], v120, s[40:43], s44 offen offset:0 sc1\n\t"
          "buffer_load_dwordx4 v[184:187], v120, s[40:43], s44 offen offset:1024 sc1\n\t"
          "buffer_load_dwordx4 v[188:191], v120, s[40:43], s44 offen offset:2048 sc1\n\t"
          "buffer_load_dwordx4 v[192:195], v120, s[40:43], s44 offen offset:3072 sc1\n\t"
          : "+{v[148:163]}"(xr1), "+{v[164:179]}"(xr2), "+{v[180:195]}"(xr3), "+{s44}"(soff) : "{v[112:127]}"(stB), "{s[40:43]}"(srd), KSKIP, "{s51}"(kpos(tm, tn, 1) * 4096) : "scc", "memory");
#pragma unroll
        for (int jj = 0; jj < 3; ++jj) {
          f32x4 cs[2][4];
          float cv[2];
#pragma unroll
          for (int j = 0; j < 2; ++j) {
            const int cb = jj * 64 + (j ^ hh) * 32;
            if (jj == 2) cv[j] = *reinterpret_cast<const float*>(smem + L_CS + (cb + r32) * 4);
#pragma unroll
            for (int g = 0; g < 4; ++g)
              if (jj < 2 || j == 0) cs[j][g] = *reinterpret_cast<const f32x4*>(smem + (jj < 2 ? L_CS + cb * 4 : L_AB + wid * 256 + 128) + (8 * g + 4 * hi) * 4);
          }
          SBAR();
#pragma unroll
          for (int j = 0; j < 2; ++j)
#pragma unroll
            for (int e = 0; e < 16; ++e) acc[2 * jj + j][e] += jj < 2 ? be * cs[j][e >> 2][e & 3] : cs[0][e >> 2][e & 3] * cv[j];
          SBAR();
        }
      }
      u32x4 ep;
      { PHASE_VARS;
        if (L == 0) reinterpret_cast<float*>(smem + L_AB)[wid * 64 + lane] = 1.0f;
        const unsigned dst = OFF_KV + L * (10 * MBY) + tn * 2 * HS + tile * 4096 + lane * 16;
        ep = u32x4{__float_as_uint(al), dst + hh * HS, dst + (hh ^ 1) * HS, lbase + L_AB + wid * 256 + hi * 16}; }
      STAMPV(3 + 16 * L);
      soff = kpos(tm, tn, 1 + 2) * 4096;
      asm volatile(
        "s_nop 0\n\t"
        "s_add_u32 s44, s44, 0x1000\n\t"
        "s_and_b32 s44, s44, 0xffff\n\t"
        "s_cmp_eq_u32 s44, s49\n\t"
        "s_cselect_b32 s44, s50, s44\n\t"
        "buffer_load_dwordx4 v[132:135], v120, s[40:43], s44 offen offset:0 sc1\n\t"
        "buffer_load_dwordx4 v[136:139], v120, s[40:43], s44 offen offset:1024 sc1\n\t"
        "buffer_load_dwordx4 v[140:143], v120, s[40:43], s44 offen offset:2048 sc1\n\t"
        "s_mov_b32 s45, 3\n\t"
        "1:\n\t"
        "s_waitcnt vmcnt(14) lgkmcnt(11)\n\t"
        "v_mfma_f32_32x32x16_bf16 v[0:15], v[96:99], v[148:151], v[0:15]\n\t"
        "ds_read_b32 v126, v125\n\t"
        "ds_read_b64_tr_b16 v[96:97], v123 offset:6144\n\t"
        "ds_read_b64_tr_b16 v[98:99], v123 offset:9216\n\t"
        "s_waitcnt lgkmcnt(12)\n\t"
        "v_mfma_f32_32x32x16_bf16 v[16:31], v[100:103], v[148:151], v[16:31]\n\t"
        "v_add_u32 v129, s46, v121\n\t"
        "ds_read_b64_tr_b16 v[100:101], v124 offset:6144\n\t"
        "ds_read_b64_tr_b16 v[102:103], v124 offset:9216\n\t"
        "s_waitcnt lgkmcnt(12)\n\t"
        "v_mfma_f32_32x32x16_bf16 v[32:47], v[104:107], v[148:151], v[32:47]\n\t"
        "ds_read_b64_tr_b16 v[104:105], v123 offset:6272\n\t"
        "ds_read_b64_tr_b16 v[106:107], v123 offset:9344\n\t"
        "buffer_load_dwordx4 v[144:147], v120, s[40:43], s44 offen offset:3072 sc1\n\t"
        "s_waitcnt lgkmcnt(12)\n\t"
        "v_mfma_f32_32x32x16_bf16 v[48:63], v[108:111], v[148:151], v[48:63]\n\t"
        "v_add_u32 v130, s46, v122\n\t"
        "ds_read_b64_tr_b16 v[108:109], v124 offset:6272\n\t"
        "ds_read_b64_tr_b16 v[110:111], v124 offset:9344\n\t"
        "s_waitcnt lgkmcnt(12)\n\t"
        "v_mfma_f32_32x32x16_bf16 v[64:79], v[148:151], v[112:115], v[64:79]\n\t"
        "ds_read_b64_tr_b16 v[112:113], v123 offset:6400\n\t"
        "ds_read_b64_tr_b16 v[114:115], v123 offset:9472\n\t"
        "s_waitcnt lgkmcnt(10)\n\t"
        "v_mfma_f32_32x32x16_bf16 v[80:95], v[148:151], v[116:119], v[80:95]\n\t"
        "v_cmp_gt_u32 vcc, s47, v126\n\t"
        "ds_read_b64_tr_b16 v[116:117], v124 offset:6400\n\t"
        "ds_read_b64_tr_b16 v[118:119], v124 offset:9472\n\t"
        "s_waitcnt vmcnt(14) lgkmcnt(10)\n\t"
        "v_mfma_f32_32x32x16_bf16 v[0:15], v[96:99], v[152:155], v[0:15]\n\t"
        "s_cbranch_vccnz 16f\n\t"
        "17:\n\t"
        "ds_read_b64_tr_b16 v[96:97], v129 offset:0\n\t"
        "ds_read_b64_tr_b16 v[98:99], v129 offset:3072\n\t"
        "s_waitcnt lgkmcnt(10)\n\t"
        "v_mfma_f32_32x32x16_bf16 v[16:31], v[100:103], v[152:155], v[16:31]\n\t"
        "v_add_u32 v128, 1, v128\n\t"
        "ds_read_b64_tr_b16 v[100:101], v130 offset:0\n\t"
        "ds_read_b64_tr_b16 v[102:103], v130 offset:3072\n\t"
        "s_add_u32 s44, s44, 0x1000\n\t"
        "s_and_b32 s44, s44, 0xffff\n\t"
        "s_waitcnt lgkmcnt(10)\n\t"
        "v_mfma_f32_32x32x16_bf16 v[32:47], v[104:107], v[152:155], v[32:47]\n\t"
        "ds_read_b64_tr_b16 v[104:105], v129 offset:128\n\t"
        "ds_read_b64_tr_b16 v[106:107], v129 offset:3200\n\t"
        "s_cmp_eq_u32 s44, s49\n\t"
        "s_cselect_b32 s44, s50, s44\n\t"
        "buffer_load_dwordx4 v[148:151], v120, s[40:43], s44 offen offset:0 sc1\n\t"
        "s_waitcnt lgkmcnt(10)\n\t"
        "v_mfma_f32_32x32x16_bf16 v[48:63], v[108:111], v[152:155], v[48:63]\n\t"
        "s_add_u32 s46, s46, 12288\n\t"
        "s_add_u32 s47, s47, 1\n\t"
        "ds_read_b64_tr_b16 v[108:109], v130 offset:128\n\t"
        "ds_read_b64_tr_b16 v[110:111], v130 offset:3200\n\t"
        "s_waitcnt lgkmcnt(10)\n\t"
        "v_mfma_f32_32x32x16_bf16 v[64:79], v[152:155], v[112:115], v[64:79]\n\t"
        "s_cmp_eq_u32 s46, 110592\n\t"
        "s_cselect_b32 s46, 0, s46\n\t"
        "ds_read_b64_tr_b16 v[112:113], v129 offset:256\n\t"
        "ds_read_b64_tr_b16 v[114:115], v129 offset:3328\n\t"
        "s_waitcnt lgkmcnt(10)\n\t"
        "v_mfma_f32_32x32x16_bf16 v[80:95], v[152:155], v[116:119], v[80:95]\n\t"
        "ds_write_b32 v127, v128\n\t"
        "ds_read_b64_tr_b16 v[116:117], v130 offset:256\n\t"
        "ds_read_b64_tr_b16 v[118:119], v130 offset:3328\n\t"
        "s_waitcnt vmcnt(14) lgkmcnt(11)\n\t"
        "v_mfma_f32_32x32x16_bf16 v[0:15], v[96:99], v[156:159], v[0:15]\n\t"
        "ds_read_b32 v126, v125\n\t"
        "ds_read_b64_tr_b16 v[96:97], v129 offset:6144\n\t"
        "ds_read_b64_tr_b16 v[98:99], v129 offset:9216\n\t"
        "s_waitcnt lgkmcnt(12)\n\t"
        "v_mfma_f32_32x32x16_bf16 v[16:31], v[100:103], v[156:159], v[16:31]\n\t"
        "v_add_u32 v123, s46, v121\n\t"
        "ds_read_b64_tr_b16 v[100:101], v130 offset:6144\n\t"
        "ds_read_b64_tr_b16 v[102:103], v130 offset:9216\n\t"
        "s_waitcnt lgkmcnt(12)\n\t"
        "v_mfma_f32_32x32x16_bf16 v[32:47], v[104:107], v[156:159], v[32:47]\n\t"
        "ds_read_b64_tr_b16 v[104:105], v129 offset:6272\n\t"
        "ds_read_b64_tr_b16 v[106:107], v129 offset:9344\n\t"
        "buffer_load_dwordx4 v[152:155], v120, s[40:43], s44 offen offset:1024 sc1\n\t"
        "s_waitcnt lgkmcnt(12)\n\t"
        "v_mfma_f32_32x32x16_bf16 v[48:63], v[108:111], v[156:159], v[48:63]\n\t"
        "v_add_u32 v124, s46, v122\n\t"
        "ds_read_b64_tr_b16 v[108:109], v130 offset:6272\n\t"
        "ds_read_b64_tr_b16 v[110:111], v130 offset:9344\n\t"
        "s_waitcnt lgkmcnt(12)\n\t"
        "v_mfma_f32_32x32x16_bf16 v[64:79], v[156:159], v[112:115], v[64:79]\n\t"
        "ds_read_b64_tr_b16 v[112:113], v129 offset:6400\n\t"
        "ds_read_b64_tr_b16 v[114:115], v129 offset:9472\n\t"
        "s_waitcnt lgkmcnt(10)\n\t"
        "v_mfma_f32_32x32x16_bf16 v[80:95], v[156:159], v[116:119], v[80:95]\n\t"
        "v_cmp_gt_u32 vcc, s47, v126\n\t"
        "ds_read_b64_tr_b16 v[116:117], v130 offset:6400\n\t"
        "ds_read_b64_tr_b16 v[118:119], v130 offset:9472\n\t"
        "s_waitcnt vmcnt(14) lgkmcnt(10)\n\t"
        "v_mfma_f32_32x32x16_bf16 v[0:15], v[96:99], v[160:163], v[0:15]\n\t"
        "s_cbranch_vccnz 18f\n\t"
        "19:\n\t"
        "ds_read_b64_tr_b16 v[96:97], v123 offset:0\n\t"
        "ds_read_b64_tr_b16 v[98:99], v123 offset:3072\n\t"
        "s_waitcnt lgkmcnt(10)\n\t"
        "v_mfma_f32_32x32x16_bf16 v[16:31], v[100:103], v[160:163], v[16:31]\n\t"
        "v_add_u32 v128, 1, v128\n\t"
        "ds_read_b64_tr_b16 v[100:101], v124 offset:0\n\t"
        "ds_read_b64_tr_b16 v[102:103], v124 offset:3072\n\t"
        "s_waitcnt lgkmcnt(10)\n\t"
        "v_mfma_f32_32x32x16_bf16 v[32:47], v[104:107], v[160:163], v[32:47]\n\t"
        "ds_read_b64_tr_b16 v[104:105], v123 offset:128\n\t"
        "ds_read_b64_tr_b16 v[106:107], v123 offset:3200\n\t"
        "buffer_load_dwordx4 v[156:159], v120, s[40:43], s44 offen offset:2048 sc1\n\t"
        "s_waitcnt lgkmcnt(10)\n\t"
        "v_mfma_f32_32x32x16_bf16 v[48:63], v[108:111], v[160:163], v[48:63]\n\t"
        "s_add_u32 s46, s46, 12288\n\t"
        "s_add_u32 s47, s47, 1\n\t"
        "ds_read_b64_tr_b16 v[108:109], v124 offset:128\n\t"
        "ds_read_b64_tr_b16 v[110:111], v124 offset:3200\n\t"
        "s_waitcnt lgkmcnt(10)\n\t"
        "v_mfma_f32_32x32x16_bf16 v[64:79], v[160:163], v[112:115], v[64:79]\n\t"
        "s_cmp_eq_u32 s46, 110592\n\t"
        "s_cselect_b32 s46, 0, s46\n\t"
        "ds_read_b64_tr_b16 v[112:113], v123 offset:256\n\t"
        "ds_read_b64_tr_b16 v[114:115], v123 offset:3328\n\t"
        "s_waitcnt lgkmcnt(10)\n\t"
        "v_mfma_f32_32x32x16_bf16 v[80:95], v[160:163], v[116:119], v[80:95]\n\t"
        "ds_write_b32 v127, v128\n\t"
        "ds_read_b64_tr_b16 v[116:117], v124 offset:256\n\t"
        "ds_read_b64_tr_b16 v[118:119], v124 offset:3328\n\t"
        "s_waitcnt vmcnt(14) lgkmcnt(11)\n\t"
        "v_mfma_f32_32x32x16_bf16 v[0:15], v[96:99], v[164:167], v[0:15]\n\t"
        "ds_read_b32 v126, v125\n\t"
        "ds_read_b64_tr_b16 v[96:97], v123 offset:6144\n\t"
        "ds_read_b64_tr_b16 v[98:99], v123 offset:9216\n\t"
        "s_waitcnt lgkmcnt(12)\n\t"
        "v_mfma_f32_32x32x16_bf16 v[16:31], v[100:103], v[164:167], v[16:31]\n\t"
        "v_add_u32 v129, s46, v121\n\t"
        "ds_read_b64_tr_b16 v[100:101], v124 offset:6144\n\t"
        "ds_read_b64_tr_b16 v[102:103], v124 offset:9216\n\t"
        "s_waitcnt lgkmcnt(12)\n\t"
        "v_mfma_f32_32x32x16_bf16 v[32:47], v[104:107], v[164:167], v[32:47]\n\t"
        "ds_read_b64_tr_b16 v[104:105], v123 offset:6272\n\t"
        "ds_read_b64_tr_b16 v[106:107], v123 offset:9344\n\t"
        "buffer_load_dwordx4 v[160:163], v120, s[40:43], s44 offen offset:3072 sc1\n\t"
        "s_waitcnt lgkmcnt(12)\n\t"
        "v_mfma_f32_32x32x16_bf16 v[48:63], v[108:111], v[164:167], v[48:63]\n\t"
        "v_add_u32 v130, s46, v122\n\t"
        "ds_read_b64_tr_b16 v[108:109], v124 offset:6272\n\t"
        "ds_read_b64_tr_b16 v[110:111], v124 offset:9344\n\t"
        "s_waitcnt lgkmcnt(12)\n\t"
        "v_mfma_f32_32x32x16_bf16 v[64:79], v[164:167], v[112:115], v[64:79]\n\t"
        "ds_read_b64_tr_b16 v[112:113], v123 offset:6400\n\t"
        "ds_read_b64_tr_b16 v[114:115], v123 offset:9472\n\t"
        "s_waitcnt lgkmcnt(10)\n\t"
        "v_mfma_f32_32x32x16_bf16 v[80:95], v[164:167], v[116:119], v[80:95]\n\t"
        "v_cmp_gt_u32 vcc, s47, v126\n\t"
        "ds_read_b64_tr_b16 v[116:117], v124 offset:6400\n\t"
        "ds_read_b64_tr_b16 v[118:119], v124 offset:9472\n\t"
        "s_waitcnt vmcnt(14) lgkmcnt(10)\n\t"
        "v_mfma_f32_32x32x16_bf16 v[0:15], v[96:99], v[168:171], v[0:15]\n\t"
        "s_cbranch_vccnz 20f\n\t"
        "21:\n\t"
        "ds_read_b64_tr_b16 v[96:97], v129 offset:0\n\t"
        "ds_read_b64_tr_b16 v[98:99], v129 offset:3072\n\t"
        "s_waitcnt lgkmcnt(10)\n\t"
        "v_mfma_f32_32x32x16_bf16 v[16:31], v[100:103], v[168:171], v[16:31]\n\t"
        "v_add_u32 v128, 1, v128\n\t"
        "ds_read_b64_tr_b16 v[100:101], v130 offset:0\n\t"
        "ds_read_b64_tr_b16 v[102:103], v130 offset:3072\n\t"
        "s_add_u32 s44, s44, 0x1000\n\t"
        "s_and_b32 s44, s44, 0xffff\n\t"
        "s_waitcnt lgkmcnt(10)\n\t"
        "v_mfma_f32_32x32x16_bf16 v[32:47], v[104:107], v[168:171], v[32:47]\n\t"
        "ds_read_b64_tr_b16 v[104:105], v129 offset:128\n\t"
        "ds_read_b64_tr_b16 v[106:107], v129 offset:3200\n\t"
        "s_cmp_eq_u32 s44, s49\n\t"
        "s_cselect_b32 s44, s50, s44\n\t"
        "buffer_load_dwordx4 v[164:167], v120, s[40:43], s44 offen offset:0 sc1\n\t"
        "s_waitcnt lgkmcnt(10)\n\t"
        "v_mfma_f32_32x32x16_bf16 v[48:63], v[108:111], v[168:171], v[48:63]\n\t"
        "s_add_u32 s46, s46, 12288\n\t"
        "s_add_u32 s47, s47, 1\n\t"
        "ds_read_b64_tr_b16 v[108:109], v130 offset:128\n\t"
        "ds_read_b64_tr_b16 v[110:111], v130 offset:3200\n\t"
        "s_waitcnt lgkmcnt(10)\n\t"
        "v_mfma_f32_32x32x16_bf16 v[64:79], v[168:171], v[112:115], v[64:79]\n\t"
        "s_cmp_eq_u32 s46, 110592\n\t"
        "s_cselect_b32 s46, 0, s46\n\t"
        "ds_read_b64_tr_b16 v[112:113], v129 offset:256\n\t"
        "ds_read_b64_tr_b16 v[114:115], v129 offset:3328\n\t"
        "s_waitcnt lgkmcnt(10)\n\t"
        "v_mfma_f32_32x32x16_bf16 v[80:95], v[168:171], v[116:119], v[80:95]\n\t"
        "ds_write_b32 v127, v128\n\t"
        "ds_read_b64_tr_b16 v[116:117], v130 offset:256\n\t"
        "ds_read_b64_tr_b16 v[118:119], v130 offset:3328\n\t"
        "s_waitcnt vmcnt(14) lgkmcnt(11)\n\t"
        "v_mfma_f32_32x32x16_bf16 v[0:15], v[96:99], v[172:175], v[0:15]\n\t"
        "ds_read_b32 v126, v125\n\t"
        "ds_read_b64_tr_b16 v[96:97], v129 offset:6144\n\t"
        "ds_read_b64_tr_b16 v[98:99], v129 offset:9216\n\t"
        "s_waitcnt lgkmcnt(12)\n\t"
        "v_mfma_f32_32x32x16_bf16 v[16:31], v[100:103], v[172:175], v[16:31]\n\t"
        "v_add_u32 v123, s46, v121\n\t"
        "ds_read_b64_tr_b16 v[100:101], v130 offset:6144\n\t"
        "ds_read_b64_tr_b16 v[102:103], v130 offset:9216\n\t"
        "s_waitcnt lgkmcnt(12)\n\t"
        "v_mfma_f32_32x32x16_bf16 v[32:47], v[104:107], v[172:175], v[32:47]\n\t"
        "ds_read_b64_tr_b16 v[104:105], v129 offset:6272\n\t"
        "ds_read_b64_tr_b16 v[106:107], v129 offset:9344\n\t"
        "buffer_load_dwordx4 v[168:171], v120, s[40:43], s44 offen offset:1024 sc1\n\t"
        "s_waitcnt lgkmcnt(12)\n\t"
        "v_mfma_f32_32x32x16_bf16 v[48:63], v[108:111], v[172:175], v[48:63]\n\t"
        "v_add_u32 v124, s46, v122\n\t"
        "ds_read_b64_tr_b16 v[108:109], v130 offset:6272\n\t"
        "ds_read_b64_tr_b16 v[110:111], v130 offset:9344\n\t"
        "s_waitcnt lgkmcnt(12)\n\t"
        "v_mfma_f32_32x32x16_bf16 v[64:79], v[172:175], v[112:115], v[64:79]\n\t"
        "ds_read_b64_tr_b16 v[112:113], v129 offset:6400\n\t"
        "ds_read_b64_tr_b16 v[114:115], v129 offset:9472\n\t"
        "s_waitcnt lgkmcnt(10)\n\t"
        "v_mfma_f32_32x32x16_bf16 v[80:95], v[172:175], v[116:119], v[80:95]\n\t"
        "v_cmp_gt_u32 vcc, s47, v126\n\t"
        "ds_read_b64_tr_b16 v[116:117], v130 offset:6400\n\t"
        "ds_read_b64_tr_b16 v[118:119], v130 offset:9472\n\t"
        "s_waitcnt vmcnt(14) lgkmcnt(10)\n\t"
        "v_mfma_f32_32x32x16_bf16 v[0:15], v[96:99], v[176:179], v[0:15]\n\t"
        "s_cbranch_vccnz 22f\n\t"
        "23:\n\t"
        "ds_read_b64_tr_b16 v[96:97], v123 offset:0\n\t"
        "ds_read_b64_tr_b16 v[98:99], v123 offset:3072\n\t"
        "s_waitcnt lgkmcnt(10)\n\t"
        "v_mfma_f32_32x32x16_bf16 v[16:31], v[100:103], v[176:179], v[16:31]\n\t"
        "v_add_u32 v128, 1, v128\n\t"
        "ds_read_b64_tr_b16 v[100:101], v124 offset:0\n\t"
        "ds_read_b64_tr_b16 v[102:103], v124 offset:3072\n\t"
        "s_waitcnt lgkmcnt(10)\n\t"
        "v_mfma_f32_32x32x16_bf16 v[32:47], v[104:107], v[176:179], v[32:47]\n\t"
        "ds_read_b64_tr_b16 v[104:105], v123 offset:128\n\t"
        "ds_read_b64_tr_b16 v[106:107], v123 offset:3200\n\t"
        "buffer_load_dwordx4 v[172:175], v120, s[40:43], s44 offen offset:2048 sc1\n\t"
        "s_waitcnt lgkmcnt(10)\n\t"
        "v_mfma_f32_32x32x16_bf16 v[48:63], v[108:111], v[176:179], v[48:63]\n\t"
        "s_add_u32 s46, s46, 12288\n\t"
        "s_add_u32 s47, s47, 1\n\t"
        "ds_read_b64_tr_b16 v[108:109], v124 offset:128\n\t"
        "ds_read_b64_tr_b16 v[110:111], v124 offset:3200\n\t"
        "s_waitcnt lgkmcnt(10)\n\t"
        "v_mfma_f32_32x32x16_bf16 v[64:79], v[176:179], v[112:115], v[64:79]\n\t"
        "s_cmp_eq_u32 s46, 110592\n\t"
        "s_cselect_b32 s46, 0, s46\n\t"
        "ds_read_b64_tr_b16 v[112:113], v123 offset:256\n\t"
        "ds_read_b64_tr_b16 v[114:115], v123 offset:3328\n\t"
        "s_waitcnt lgkmcnt(10)\n\t"
        "v_mfma_f32_32x32x16_bf16 v[80:95], v[176:179], v[116:119], v[80:95]\n\t"
        "ds_write_b32 v127, v128\n\t"
        "ds_read_b64_tr_b16 v[116:117], v124 offset:256\n\t"
        "ds_read_b64_tr_b16 v[118:119], v124 offset:3328\n\t"
        "s_waitcnt vmcnt(14) lgkmcnt(11)\n\t"
        "v_mfma_f32_32x32x16_bf16 v[0:15], v[96:99], v[180:183], v[0:15]\n\t"
        "ds_read_b32 v126, v125\n\t"
        "ds_read_b64_tr_b16 v[96:97], v123 offset:6144\n\t"
        "ds_read_b64_tr_b16 v[98:99], v123 offset:9216\n\t"
        "s_waitcnt lgkmcnt(12)\n\t"
        "v_mfma_f32_32x32x16_bf16 v[16:31], v[100:103], v[180:183], v[16:31]\n\t"
        "v_add_u32 v129, s46, v121\n\t"
        "ds_read_b64_tr_b16 v[100:101], v124 offset:6144\n\t"
        "ds_read_b64_tr_b16 v[102:103], v124 offset:9216\n\t"
        "s_waitcnt lgkmcnt(12)\n\t"
        "v_mfma_f32_32x32x16_bf16 v[32:47], v[104:107], v[180:183], v[32:47]\n\t"
        "ds_read_b64_tr_b16 v[104:105], v123 offset:6272\n\t"
        "ds_read_b64_tr_b16 v[106:107], v123 offset:9344\n\t"
        "buffer_load_dwordx4 v[176:179], v120, s[40:43], s44 offen offset:3072 sc1\n\t"
        "s_waitcnt lgkmcnt(12)\n\t"
        "v_mfma_f32_32x32x16_bf16 v[48:63], v[108:111], v[180:183], v[48:63]\n\t"
        "v_add_u32 v130, s46, v122\n\t"
        "ds_read_b64_tr_b16 v[108:109], v124 offset:6272\n\t"
        "ds_read_b64_tr_b16 v[110:111], v124 offset:9344\n\t"
        "s_waitcnt lgkmcnt(12)\n\t"
        "v_mfma_f32_32x32x16_bf16 v[64:79], v[180:183], v[112:115], v[64:79]\n\t"
        "ds_read_b64_tr_b16 v[112:113], v123 offset:6400\n\t"
        "ds_read_b64_tr_b16 v[114:115], v123 offset:9472\n\t"
        "s_waitcnt lgkmcnt(10)\n\t"
        "v_mfma_f32_32x32x16_bf16 v[80:95], v[180:183], v[116:119], v[80:95]\n\t"
        "v_cmp_gt_u32 vcc, s47, v126\n\t"
        "ds_read_b64_tr_b16 v[116:117], v124 offset:6400\n\t"
        "ds_read_b64_tr_b16 v[118:119], v124 offset:9472\n\t"
        "s_waitcnt vmcnt(14) lgkmcnt(10)\n\t"
        "v_mfma_f32_32x32x16_bf16 v[0:15], v[96:99], v[184:187], v[0:15]\n\t"
        "s_cbranch_vccnz 24f\n\t"
        "25:\n\t"
        "ds_read_b64_tr_b16 v[96:97], v129 offset:0\n\t"
        "ds_read_b64_tr_b16 v[98:99], v129 offset:3072\n\t"
        "s_waitcnt lgkmcnt(10)\n\t"
        "v_mfma_f32_32x32x16_bf16 v[16:31], v[100:103], v[184:187], v[16:31]\n\t"
        "v_add_u32 v128, 1, v128\n\t"
        "ds_read_b64_tr_b16 v[100:101], v130 offset:0\n\t"
        "ds_read_b64_tr_b16 v[102:103], v130 offset:3072\n\t"
        "s_add_u32 s44, s44, 0x1000\n\t"
        "s_and_b32 s44, s44, 0xffff\n\t"
        "s_waitcnt lgkmcnt(10)\n\t"
        "v_mfma_f32_32x32x16_bf16 v[32:47], v[104:107], v[184:187], v[32:47]\n\t"
        "ds_read_b64_tr_b16 v[104:105], v129 offset:128\n\t"
        "ds_read_b64_tr_b16 v[106:107], v129 offset:3200\n\t"
        "s_cmp_eq_u32 s44, s49\n\t"
        "s_cselect_b32 s44, s50, s44\n\t"
        "buffer_load_dwordx4 v[180:183], v120, s[40:43], s44 offen offset:0 sc1\n\t"
        "s_waitcnt lgkmcnt(10)\n\t"
        "v_mfma_f32_32x32x16_bf16 v[48:63], v[108:111], v[184:187], v[48:63]\n\t"
        "s_add_u32 s46, s46, 12288\n\t"
        "s_add_u32 s47, s47, 1\n\t"
        "ds_read_b64_tr_b16 v[108:109], v130 offset:128\n\t"
        "ds_read_b64_tr_b16 v[110:111], v130 offset:3200\n\t"
        "s_waitcnt lgkmcnt(10)\n\t"
        "v_mfma_f32_32x32x16_bf16 v[64:79], v[184:187], v[112:115], v[64:79]\n\t"
        "s_cmp_eq_u32 s46, 110592\n\t"
        "s_cselect_b32 s46, 0, s46\n\t"
        "ds_read_b64_tr_b16 v[112:113], v129 offset:256\n\t"
        "ds_read_b64_tr_b16 v[114:115], v129 offset:3328\n\t"
        "s_waitcnt lgkmcnt(10)\n\t"
        "v_mfma_f32_32x32x16_bf16 v[80:95], v[184:187], v[116:119], v[80:95]\n\t"
        "ds_write_b32 v127, v128\n\t"
        "ds_read_b64_tr_b16 v[116:117], v130 offset:256\n\t"
        "ds_read_b64_tr_b16 v[118:119], v130 offset:3328\n\t"
        "s_waitcnt vmcnt(14) lgkmcnt(11)\n\t"
        "v_mfma_f32_32x32x16_bf16 v[0:15], v[96:99], v[188:191], v[0:15]\n\t"
        "ds_read_b32 v126, v125\n\t"
        "ds_read_b64_tr_b16 v[96:97], v129 offset:6144\n\t"
        "ds_read_b64_tr_b16 v[98:99], v129 offset:9216\n\t"
        "s_waitcnt lgkmcnt(12)\n\t"
        "v_mfma_f32_32x32x16_bf16 v[16:31], v[100:103], v[188:191], v[16:31]\n\t"
        "v_add_u32 v123, s46, v121\n\t"
        "ds_read_b64_tr_b16 v[100:101], v130 offset:6144\n\t"
        "ds_read_b64_tr_b16 v[102:103], v130 offset:9216\n\t"
        "s_waitcnt lgkmcnt(12)\n\t"
        "v_mfma_f32_32x32x16_bf16 v[32:47], v[104:107], v[188:191], v[32:47]\n\t"
        "ds_read_b64_tr_b16 v[104:105], v129 offset:6272\n\t"
        "ds_read_b64_tr_b16 v[106:107], v129 offset:9344\n\t"
        "buffer_load_dwordx4 v[184:187], v120, s[40:43], s44 offen offset:1024 sc1\n\t"
        "s_waitcnt lgkmcnt(12)\n\t"
        "v_mfma_f32_32x32x16_bf16 v[48:63], v[108:111], v[188:191], v[48:63]\n\t"
        "v_add_u32 v124, s46, v122\n\t"
        "ds_read_b64_tr_b16 v[108:109], v130 offset:6272\n\t"
        "ds_read_b64_tr_b16 v[110:111], v130 offset:9344\n\t"
        "s_waitcnt lgkmcnt(12)\n\t"
        "v_mfma_f32_32x32x16_bf16 v[64:79], v[188:191], v[112:115], v[64:79]\n\t"
        "ds_read_b64_tr_b16 v[112:113], v129 offset:6400\n\t"
        "ds_read_b64_tr_b16 v[114:115], v129 offset:9472\n\t"
        "s_waitcnt lgkmcnt(10)\n\t"
        "v_mfma_f32_32x32x16_bf16 v[80:95], v[188:191], v[116:119], v[80:95]\n\t"
        "v_cmp_gt_u32 vcc, s47, v126\n\t"
        "ds_read_b64_tr_b16 v[116:117], v130 offset:6400\n\t"
        "ds_read_b64_tr_b16 v[118:119], v130 offset:9472\n\t"
        "s_waitcnt vmcnt(14) lgkmcnt(10)\n\t"
        "v_mfma_f32_32x32x16_bf16 v[0:15], v[96:99], v[192:195], v[0:15]\n\t"
        "s_cbranch_vccnz 26f\n\t"
        "27:\n\t"
        "ds_read_b64_tr_b16 v[96:97], v123 offset:0\n\t"
        "ds_read_b64_tr_b16 v[98:99], v123 offset:3072\n\t"
        "s_waitcnt lgkmcnt(10)\n\t"
        "v_mfma_f32_32x32x16_bf16 v[16:31], v[100:103], v[192:195], v[16:31]\n\t"
        "v_add_u32 v128, 1, v128\n\t"
        "ds_read_b64_tr_b16 v[100:101], v124 offset:0\n\t"
        "ds_read_b64_tr_b16 v[102:103], v124 offset:3072\n\t"
        "s_waitcnt lgkmcnt(10)\n\t"
        "v_mfma_f32_32x32x16_bf16 v[32:47], v[104:107], v[192:195], v[32:47]\n\t"
        "ds_read_b64_tr_b16 v[104:105], v123 offset:128\n\t"
        "ds_read_b64_tr_b16 v[106:107], v123 offset:3200\n\t"
        "buffer_load_dwordx4 v[188:191], v120, s[40:43], s44 offen offset:2048 sc1\n\t"
        "s_waitcnt lgkmcnt(10)\n\t"
        "v_mfma_f32_32x32x16_bf16 v[48:63], v[108:111], v[192:195], v[48:63]\n\t"
        "s_add_u32 s46, s46, 12288\n\t"
        "s_add_u32 s47, s47, 1\n\t"
        "ds_read_b64_tr_b16 v[108:109], v124 offset:128\n\t"
        "ds_read_b64_tr_b16 v[110:111], v124 offset:3200\n\t"
        "s_waitcnt lgkmcnt(10)\n\t"
        "v_mfma_f32_32x32x16_bf16 v[64:79], v[192:195], v[112:115], v[64:79]\n\t"
        "s_cmp_eq_u32 s46, 110592\n\t"
        "s_cselect_b32 s46, 0, s46\n\t"
        "ds_read_b64_tr_b16 v[112:113], v123 offset:256\n\t"
        "ds_read_b64_tr_b16 v[114:115], v123 offset:3328\n\t"
        "s_waitcnt lgkmcnt(10)\n\t"
        "v_mfma_f32_32x32x16_bf16 v[80:95], v[192:195], v[116:119], v[80:95]\n\t"
        "ds_write_b32 v127, v128\n\t"
        "ds_read_b64_tr_b16 v[116:117], v124 offset:256\n\t"
        "ds_read_b64_tr_b16 v[118:119], v124 offset:3328\n\t"
        "s_waitcnt vmcnt(14) lgkmcnt(11)\n\t"
        "v_mfma_f32_32x32x16_bf16 v[0:15], v[96:99], v[132:135], v[0:15]\n\t"
        "ds_read_b32 v126, v125\n\t"
        "ds_read_b64_tr_b16 v[96:97], v123 offset:6144\n\t"
        "ds_read_b64_tr_b16 v[98:99], v123 offset:9216\n\t"
        "s_waitcnt lgkmcnt(12)\n\t"
        "v_mfma_f32_32x32x16_bf16 v[16:31], v[100:103], v[132:135], v[16:31]\n\t"
        "v_add_u32 v129, s46, v121\n\t"
        "ds_read_b64_tr_b16 v[100:101], v124 offset:6144\n\t"
        "ds_read_b64_tr_b16 v[102:103], v124 offset:9216\n\t"
        "s_waitcnt lgkmcnt(12)\n\t"
        "v_mfma_f32_32x32x16_bf16 v[32:47], v[104:107], v[132:135], v[32:47]\n\t"
        "ds_read_b64_tr_b16 v[104:105], v123 offset:6272\n\t"
        "ds_read_b64_tr_b16 v[106:107], v123 offset:9344\n\t"
        "buffer_load_dwordx4 v[192:195], v120, s[40:43], s44 offen offset:3072 sc1\n\t"
        "s_waitcnt lgkmcnt(12)\n\t"
        "v_mfma_f32_32x32x16_bf16 v[48:63], v[108:111], v[132:135], v[48:63]\n\t"
        "v_add_u32 v130, s46, v122\n\t"
        "ds_read_b64_tr_b16 v[108:109], v124 offset:6272\n\t"
        "ds_read_b64_tr_b16 v[110:111], v124 offset:9344\n\t"
        "s_waitcnt lgkmcnt(12)\n\t"
        "v_mfma_f32_32x32x16_bf16 v[64:79], v[132:135], v[112:115], v[64:79]\n\t"
        "ds_read_b64_tr_b16 v[112:113], v123 offset:6400\n\t"
        "ds_read_b64_tr_b16 v[114:115], v123 offset:9472\n\t"
        "s_waitcnt lgkmcnt(10)\n\t"
        "v_mfma_f32_32x32x16_bf16 v[80:95], v[132:135], v[116:119], v[80:95]\n\t"
        "v_cmp_gt_u32 vcc, s47, v126\n\t"
        "ds_read_b64_tr_b16 v[116:117], v124 offset:6400\n\t"
        "ds_read_b64_tr_b16 v[118:119], v124 offset:9472\n\t"
        "s_waitcnt vmcnt(14) lgkmcnt(10)\n\t"
        "v_mfma_f32_32x32x16_bf16 v[0:15], v[96:99], v[136:139], v[0:15]\n\t"
        "s_cbranch_vccnz 28f\n\t"
        "29:\n\t"
        "ds_read_b64_tr_b16 v[96:97], v129 offset:0\n\t"
        "ds_read_b64_tr_b16 v[98:99], v129 offset:3072\n\t"
        "s_waitcnt lgkmcnt(10)\n\t"
        "v_mfma_f32_32x32x16_bf16 v[16:31], v[100:103], v[136:139], v[16:31]\n\t"
        "v_add_u32 v128, 1, v128\n\t"
        "ds_read_b64_tr_b16 v[100:101], v130 offset:0\n\t"
        "ds_read_b64_tr_b16 v[102:103], v130 offset:3072\n\t"
        "s_add_u32 s44, s44, 0x1000\n\t"
        "s_and_b32 s44, s44, 0xffff\n\t"
        "s_waitcnt lgkmcnt(10)\n\t"
        "v_mfma_f32_32x32x16_bf16 v[32:47], v[104:107], v[136:139], v[32:47]\n\t"
        "ds_read_b64_tr_b16 v[104:105], v129 offset:128\n\t"
        "ds_read_b64_tr_b16 v[106:107], v129 offset:3200\n\t"
        "s_cmp_eq_u32 s44, s49\n\t"
        "s_cselect_b32 s44, s50, s44\n\t"
        "buffer_load_dwordx4 v[132:135], v120, s[40:43], s44 offen offset:0 sc1\n\t"
        "s_waitcnt lgkmcnt(10)\n\t"
        "v_mfma_f32_32x32x16_bf16 v[48:63], v[108:111], v[136:139], v[48:63]\n\t"
        "s_add_u32 s46, s46, 12288\n\t"
        "s_add_u32 s47, s47, 1\n\t"
        "ds_read_b64_tr_b16 v[108:109], v130 offset:128\n\t"
        "ds_read_b64_tr_b16 v[110:111], v130 offset:3200\n\t"
        "s_waitcnt lgkmcnt(10)\n\t"
        "v_mfma_f32_32x32x16_bf16 v[64:79], v[136:139], v[112:115], v[64:79]\n\t"
        "s_cmp_eq_u32 s46, 110592\n\t"
        "s_cselect_b32 s46, 0, s46\n\t"
        "ds_read_b64_tr_b16 v[112:113], v129 offset:256\n\t"
        "ds_read_b64_tr_b16 v[114:115], v129 offset:3328\n\t"
        "s_waitcnt lgkmcnt(10)\n\t"
        "v_mfma_f32_32x32x16_bf16 v[80:95], v[136:139], v[116:119], v[80:95]\n\t"
        "ds_write_b32 v127, v128\n\t"
        "ds_read_b64_tr_b16 v[116:117], v130 offset:256\n\t"
        "ds_read_b64_tr_b16 v[118:119], v130 offset:3328\n\t"
        "s_waitcnt vmcnt(14) lgkmcnt(11)\n\t"
        "v_mfma_f32_32x32x16_bf16 v[0:15], v[96:99], v[140:143], v[0:15]\n\t"
        "ds_read_b32 v126, v125\n\t"
        "ds_read_b64_tr_b16 v[96:97], v129 offset:6144\n\t"
        "ds_read_b64_tr_b16 v[98:99], v129 offset:9216\n\t"
        "s_waitcnt lgkmcnt(12)\n\t"
        "v_mfma_f32_32x32x16_bf16 v[16:31], v[100:103], v[140:143], v[16:31]\n\t"
        "v_add_u32 v123, s46, v121\n\t"
        "ds_read_b64_tr_b16 v[100:101], v130 offset:6144\n\t"
        "ds_read_b64_tr_b16 v[102:103], v130 offset:9216\n\t"
        "s_waitcnt lgkmcnt(12)\n\t"
        "v_mfma_f32_32x32x16_bf16 v[32:47], v[104:107], v[140:143], v[32:47]\n\t"
        "ds_read_b64_tr_b16 v[104:105], v129 offset:6272\n\t"
        "ds_read_b64_tr_b16 v[106:107], v129 offset:9344\n\t"
        "buffer_load_dwordx4 v[136:139], v120, s[40:43], s44 offen offset:1024 sc1\n\t"
        "s_waitcnt lgkmcnt(12)\n\t"
        "v_mfma_f32_32x32x16_bf16 v[48:63], v[108:111], v[140:143], v[48:63]\n\t"
        "v_add_u32 v124, s46, v122\n\t"
        "ds_read_b64_tr_b16 v[108:109], v130 offset:6272\n\t"
        "ds_read_b64_tr_b16 v[110:111], v130 offset:9344\n\t"
        "s_waitcnt lgkmcnt(12)\n\t"
        "v_mfma_f32_32x32x16_bf16 v[64:79], v[140:143], v[112:115], v[64:79]\n\t"
        "ds_read_b64_tr_b16 v[112:113], v129 offset:6400\n\t"
        "ds_read_b64_tr_b16 v[114:115], v129 offset:9472\n\t"
        "s_waitcnt lgkmcnt(10)\n\t"
        "v_mfma_f32_32x32x16_bf16 v[80:95], v[140:143], v[116:119], v[80:95]\n\t"
        "v_cmp_gt_u32 vcc, s47, v126\n\t"
        "ds_read_b64_tr_b16 v[116:117], v130 offset:6400\n\t"
        "ds_read_b64_tr_b16 v[118:119], v130 offset:9472\n\t"
        "s_waitcnt vmcnt(14) lgkmcnt(10)\n\t"
        "v_mfma_f32_32x32x16_bf16 v[0:15], v[96:99], v[144:147], v[0:15]\n\t"
        "s_cbranch_vccnz 30f\n\t"
        "31:\n\t"
        "ds_read_b64_tr_b16 v[96:97], v123 offset:0\n\t"
        "ds_read_b64_tr_b16 v[98:99], v123 offset:3072\n\t"
        "s_waitcnt lgkmcnt(10)\n\t"
        "v_mfma_f32_32x32x16_bf16 v[16:31], v[100:103], v[144:147], v[16:31]\n\t"
        "v_add_u32 v128, 1, v128\n\t"
        "ds_read_b64_tr_b16 v[100:101], v124 offset:0\n\t"
        "ds_read_b64_tr_b16 v[102:103], v124 offset:3072\n\t"
        "s_waitcnt lgkmcnt(10)\n\t"
        "v_mfma_f32_32x32x16_bf16 v[32:47], v[104:107], v[144:147], v[32:47]\n\t"
        "ds_read_b64_tr_b16 v[104:105], v123 offset:128\n\t"
        "ds_read_b64_tr_b16 v[106:107], v123 offset:3200\n\t"
        "buffer_load_dwordx4 v[140:143], v120, s[40:43], s44 offen offset:2048 sc1\n\t"
        "s_waitcnt lgkmcnt(10)\n\t"
        "v_mfma_f32_32x32x16_bf16 v[48:63], v[108:111], v[144:147], v[48:63]\n\t"
        "s_add_u32 s46, s46, 12288\n\t"
        "s_add_u32 s47, s47, 1\n\t"
        "ds_read_b64_tr_b16 v[108:109], v124 offset:128\n\t"
        "ds_read_b64_tr_b16 v[110:111], v124 offset:3200\n\t"
        "s_waitcnt lgkmcnt(10)\n\t"
        "v_mfma_f32_32x32x16_bf16 v[64:79], v[144:147], v[112:115], v[64:79]\n\t"
        "s_cmp_eq_u32 s46, 110592\n\t"
        "s_cselect_b32 s46, 0, s46\n\t"
        "ds_read_b64_tr_b16 v[112:113], v123 offset:256\n\t"
        "ds_read_b64_tr_b16 v[114:115], v123 offset:3328\n\t"
        "s_waitcnt lgkmcnt(10)\n\t"
        "v_mfma_f32_32x32x16_bf16 v[80:95], v[144:147], v[116:119], v[80:95]\n\t"
        "ds_write_b32 v127, v128\n\t"
        "ds_read_b64_tr_b16 v[116:117], v124 offset:256\n\t"
        "ds_read_b64_tr_b16 v[118:119], v124 offset:3328\n\t"
        "s_sub_u32 s45, s45, 1\n\t"
        "s_cmp_lg_u32 s45, 0\n\t"
        "s_cbranch_scc1 1b\n\t"
        "s_waitcnt vmcnt(14) lgkmcnt(11)\n\t"
        "v_mfma_f32_32x32x16_bf16 v[0:15], v[96:99], v[148:151], v[0:15]\n\t"
        "ds_read_b32 v126, v125\n\t"
        "s_add_u32 s47, s47, 4\n\t"
        "ds_read_b64_tr_b16 v[96:97], v123 offset:6400\n\t"
        "ds_read_b64_tr_b16 v[98:99], v123 offset:9472\n\t"
        "s_waitcnt lgkmcnt(12)\n\t"
        "v_mfma_f32_32x32x16_bf16 v[16:31], v[100:103], v[148:151], v[16:31]\n\t"
        "ds_read_b64_tr_b16 v[100:101], v124 offset:6400\n\t"
        "ds_read_b64_tr_b16 v[102:103], v124 offset:9472\n\t"
        "s_waitcnt lgkmcnt(4)\n\t"
        "v_cmp_gt_u32 vcc, s47, v126\n\t"
        "v_mfma_f32_32x32x16_bf16 v[32:47], v[104:107], v[148:151], v[32:47]\n\t"
        "s_cbranch_vccnz 74f\n\t"
        "75:\n\t"
        "ds_read_b64_tr_b16 v[104:105], v121 offset:256\n\t"
        "ds_read_b64_tr_b16 v[106:107], v121 offset:3328\n\t"
        "s_waitcnt lgkmcnt(12)\n\t"
        "v_mfma_f32_32x32x16_bf16 v[48:63], v[108:111], v[148:151], v[48:63]\n\t"
        "ds_read_b64_tr_b16 v[108:109], v122 offset:256\n\t"
        "ds_read_b64_tr_b16 v[110:111], v122 offset:3328\n\t"
        "s_waitcnt lgkmcnt(12)\n\t"
        "v_mfma_f32_32x32x16_bf16 v[64:79], v[148:151], v[112:115], v[64:79]\n\t"
        "ds_read_b64_tr_b16 v[112:113], v121 offset:6400\n\t"
        "ds_read_b64_tr_b16 v[114:115], v121 offset:9472\n\t"
        "s_waitcnt lgkmcnt(11)\n\t"
        "v_mfma_f32_32x32x16_bf16 v[80:95], v[148:151], v[116:119], v[80:95]\n\t"
        "ds_read_b64_tr_b16 v[116:117], v122 offset:6400\n\t"
        "ds_read_b64_tr_b16 v[118:119], v122 offset:9472\n\t"
        "s_waitcnt vmcnt(13) lgkmcnt(10)\n\t"
        "v_mfma_f32_32x32x16_bf16 v[64:79], v[152:155], v[96:99], v[64:79]\n\t"
        "ds_read_b64_tr_b16 v[96:97], v121 offset:12544\n\t"
        "ds_read_b64_tr_b16 v[98:99], v121 offset:15616\n\t"
        "s_waitcnt lgkmcnt(10)\n\t"
        "v_mfma_f32_32x32x16_bf16 v[80:95], v[152:155], v[100:103], v[80:95]\n\t"
        "ds_read_b64_tr_b16 v[100:101], v122 offset:12544\n\t"
        "ds_read_b64_tr_b16 v[102:103], v122 offset:15616\n\t"
        "s_waitcnt vmcnt(12) lgkmcnt(10)\n\t"
        "v_mfma_f32_32x32x16_bf16 v[64:79], v[156:159], v[104:107], v[64:79]\n\t"
        "ds_read_b128 v[196:199], v215 offset:0\n\t"
        "ds_read_b64_tr_b16 v[104:105], v121 offset:18688\n\t"
        "ds_read_b64_tr_b16 v[106:107], v121 offset:21760\n\t"
        "s_waitcnt lgkmcnt(11)\n\t"
        "v_mfma_f32_32x32x16_bf16 v[80:95], v[156:159], v[108:111], v[80:95]\n\t"
        "ds_read_b128 v[200:203], v215 offset:32\n\t"
        "ds_read_b64_tr_b16 v[108:109], v122 offset:18688\n\t"
        "ds_read_b64_tr_b16 v[110:111], v122 offset:21760\n\t"
        "s_waitcnt vmcnt(11) lgkmcnt(12)\n\t"
        "v_mfma_f32_32x32x16_bf16 v[64:79], v[160:163], v[112:115], v[64:79]\n\t"
        "ds_read_b128 v[204:207], v215 offset:64\n\t"
        "ds_read_b64_tr_b16 v[112:113], v121 offset:24832\n\t"
        "ds_read_b64_tr_b16 v[114:115], v121 offset:27904\n\t"
        "s_waitcnt lgkmcnt(13)\n\t"
        "v_mfma_f32_32x32x16_bf16 v[80:95], v[160:163], v[116:119], v[80:95]\n\t"
        "ds_read_b128 v[208:211], v215 offset:96\n\t"
        "ds_read_b64_tr_b16 v[116:117], v122 offset:24832\n\t"
        "ds_read_b64_tr_b16 v[118:119], v122 offset:27904\n\t"
        "s_waitcnt vmcnt(10) lgkmcnt(14)\n\t"
        "v_mfma_f32_32x32x16_bf16 v[64:79], v[164:167], v[96:99], v[64:79]\n\t"
        "ds_read_b64_tr_b16 v[96:97], v121 offset:30976\n\t"
        "ds_read_b64_tr_b16 v[98:99], v121 offset:34048\n\t"
        "s_waitcnt lgkmcnt(14)\n\t"
        "v_mfma_f32_32x32x16_bf16 v[80:95], v[164:167], v[100:103], v[80:95]\n\t"
        "ds_read_b64_tr_b16 v[100:101], v122 offset:30976\n\t"
        "ds_read_b64_tr_b16 v[102:103], v122 offset:34048\n\t"
        "s_waitcnt vmcnt(9) lgkmcnt(13)\n\t"
        "v_mfma_f32_32x32x16_bf16 v[64:79], v[168:171], v[104:107], v[64:79]\n\t"
        "ds_read_b64_tr_b16 v[104:105], v121 offset:37120\n\t"
        "ds_read_b64_tr_b16 v[106:107], v121 offset:40192\n\t"
        "s_waitcnt lgkmcnt(12)\n\t"
        "v_mfma_f32_32x32x16_bf16 v[80:95], v[168:171], v[108:111], v[80:95]\n\t"
        "ds_read_b64_tr_b16 v[108:109], v122 offset:37120\n\t"
        "ds_read_b64_tr_b16 v[110:111], v122 offset:40192\n\t"
        "s_waitcnt vmcnt(8) lgkmcnt(11)\n\t"
        "v_mfma_f32_32x32x16_bf16 v[64:79], v[172:175], v[112:115], v[64:79]\n\t"
        "ds_read_b64_tr_b16 v[112:113], v121 offset:43264\n\t"
        "ds_read_b64_tr_b16 v[114:115], v121 offset:46336\n\t"
        "s_waitcnt lgkmcnt(10)\n\t"
        "v_mfma_f32_32x32x16_bf16 v[80:95], v[172:175], v[116:119], v[80:95]\n\t"
        "ds_read_b64_tr_b16 v[116:117], v122 offset:43264\n\t"
        "ds_read_b64_tr_b16 v[118:119], v122 offset:46336\n\t"
        "s_waitcnt vmcnt(7) lgkmcnt(10)\n\t"
        "v_mfma_f32_32x32x16_bf16 v[64:79], v[176:179], v[96:99], v[64:79]\n\t"
        "ds_read_b64_tr_b16 v[96:97], v121 offset:49408\n\t"
        "ds_read_b64_tr_b16 v[98:99], v121 offset:52480\n\t"
        "s_waitcnt lgkmcnt(10)\n\t"
        "v_mfma_f32_32x32x16_bf16 v[80:95], v[176:179], v[100:103], v[80:95]\n\t"
        "ds_read_b64_tr_b16 v[100:101], v122 offset:49408\n\t"
        "ds_read_b64_tr_b16 v[102:103], v122 offset:52480\n\t"
        "s_waitcnt vmcnt(6) lgkmcnt(10)\n\t"
        "v_mfma_f32_32x32x16_bf16 v[64:79], v[180:183], v[104:107], v[64:79]\n\t"
        "ds_read_b64_tr_b16 v[104:105], v121 offset:55552\n\t"
        "ds_read_b64_tr_b16 v[106:107], v121 offset:58624\n\t"
        "s_waitcnt lgkmcnt(10)\n\t"
        "v_mfma_f32_32x32x16_bf16 v[80:95], v[180:183], v[108:111], v[80:95]\n\t"
        "ds_read_b64_tr_b16 v[108:109], v122 offset:55552\n\t"
        "ds_read_b64_tr_b16 v[110:111], v122 offset:58624\n\t"
        "s_waitcnt vmcnt(5) lgkmcnt(10)\n\t"
        "v_mfma_f32_32x32x16_bf16 v[64:79], v[184:187], v[112:115], v[64:79]\n\t"
        "ds_read_b64_tr_b16 v[112:113], v123 offset:6272\n\t"
        "ds_read_b64_tr_b16 v[114:115], v123 offset:9344\n\t"
        "s_waitcnt lgkmcnt(10)\n\t"
        "v_mfma_f32_32x32x16_bf16 v[80:95], v[184:187], v[116:119], v[80:95]\n\t"
        "ds_read_b64_tr_b16 v[116:117], v124 offset:6272\n\t"
        "ds_read_b64_tr_b16 v[118:119], v124 offset:9344\n\t"
        "s_waitcnt vmcnt(4) lgkmcnt(10)\n\t"
        "v_mfma_f32_32x32x16_bf16 v[64:79], v[188:191], v[96:99], v[64:79]\n\t"
        "ds_read_b64_tr_b16 v[96:97], v121 offset:128\n\t"
        "ds_read_b64_tr_b16 v[98:99], v121 offset:3200\n\t"
        "s_waitcnt lgkmcnt(10)\n\t"
        "v_mfma_f32_32x32x16_bf16 v[80:95], v[188:191], v[100:103], v[80:95]\n\t"
        "ds_read_b64_tr_b16 v[100:101], v122 offset:128\n\t"
        "ds_read_b64_tr_b16 v[102:103], v122 offset:3200\n\t"
        "s_waitcnt vmcnt(0) lgkmcnt(10)\n\t"
        "v_mfma_f32_32x32x16_bf16 v[64:79], v[192:195], v[104:107], v[64:79]\n\t"
        "ds_read_b64_tr_b16 v[104:105], v121 offset:6272\n\t"
        "ds_read_b64_tr_b16 v[106:107], v121 offset:9344\n\t"
        "s_waitcnt lgkmcnt(10)\n\t"
        "v_mfma_f32_32x32x16_bf16 v[80:95], v[192:195], v[108:111], v[80:95]\n\t"
        "ds_read_b64_tr_b16 v[108:109], v122 offset:6272\n\t"
        "ds_read_b64_tr_b16 v[110:111], v122 offset:9344\n\t"
        "s_waitcnt lgkmcnt(10)\n\t"
        "v_mfma_f32_32x32x16_bf16 v[32:47], v[112:115], v[152:155], v[32:47]\n\t"
        "ds_read_b64_tr_b16 v[112:113], v121 offset:12416\n\t"
        "ds_read_b64_tr_b16 v[114:115], v121 offset:15488\n\t"
        "s_waitcnt lgkmcnt(10)\n\t"
        "v_mfma_f32_32x32x16_bf16 v[48:63], v[116:119], v[152:155], v[48:63]\n\t"
        "ds_read_b64_tr_b16 v[116:117], v122 offset:12416\n\t"
        "ds_read_b64_tr_b16 v[118:119], v122 offset:15488\n\t"
        "v_mul_f32 v64, v196, v64\n\t"
        "v_mul_f32 v65, v197, v65\n\t"
        "v_mul_f32 v66, v198, v66\n\t"
        "s_waitcnt lgkmcnt(10)\n\t"
        "v_mfma_f32_32x32x16_bf16 v[32:47], v[96:99], v[156:159], v[32:47]\n\t"
        "ds_read_b64_tr_b16 v[96:97], v121 offset:18560\n\t"
        "ds_read_b64_tr_b16 v[98:99], v121 offset:21632\n\t"
        "v_mul_f32 v67, v199, v67\n\t"
        "v_mul_f32 v68, v200, v68\n\t"
        "v_mul_f32 v69, v201, v69\n\t"
        "s_waitcnt lgkmcnt(10)\n\t"
        "v_mfma_f32_32x32x16_bf16 v[48:63], v[100:103], v[156:159], v[48:63]\n\t"
        "ds_read_b64_tr_b16 v[100:101], v122 offset:18560\n\t"
        "ds_read_b64_tr_b16 v[102:103], v122 offset:21632\n\t"
        "v_mul_f32 v70, v202, v70\n\t"
        "v_mul_f32 v71, v203, v71\n\t"
        "v_cvt_pk_bf16_f32 v64, v64, v65\n\t"
        "s_waitcnt lgkmcnt(10)\n\t"
        "v_mfma_f32_32x32x16_bf16 v[32:47], v[104:107], v[160:163], v[32:47]\n\t"
        "ds_read_b64_tr_b16 v[104:105], v121 offset:24704\n\t"
        "ds_read_b64_tr_b16 v[106:107], v121 offset:27776\n\t"
        "v_cvt_pk_bf16_f32 v65, v66, v67\n\t"
        "v_cvt_pk_bf16_f32 v66, v68, v69\n\t"
        "v_cvt_pk_bf16_f32 v67, v70, v71\n\t"
        "s_waitcnt lgkmcnt(10)\n\t"
        "v_mfma_f32_32x32x16_bf16 v[48:63], v[108:111], v[160:163], v[48:63]\n\t"
        "ds_read_b64_tr_b16 v[108:109], v122 offset:24704\n\t"
        "ds_read_b64_tr_b16 v[110:111], v122 offset:27776\n\t"
        "v_mul_f32 v72, v204, v72\n\t"
        "v_mul_f32 v73, v205, v73\n\t"
        "v_mul_f32 v74, v206, v74\n\t"
        "s_waitcnt lgkmcnt(10)\n\t"
        "v_mfma_f32_32x32x16_bf16 v[32:47], v[112:115], v[164:167], v[32:47]\n\t"
        "ds_read_b64_tr_b16 v[112:113], v121 offset:30848\n\t"
        "ds_read_b64_tr_b16 v[114:115], v121 offset:33920\n\t"
        "v_mul_f32 v75, v207, v75\n\t"
        "v_mul_f32 v76, v208, v76\n\t"
        "v_mul_f32 v77, v209, v77\n\t"
        "s_waitcnt lgkmcnt(10)\n\t"
        "v_mfma_f32_32x32x16_bf16 v[48:63], v[116:119], v[164:167], v[48:63]\n\t"
        "ds_read_b64_tr_b16 v[116:117], v122 offset:30848\n\t"
        "ds_read_b64_tr_b16 v[118:119], v122 offset:33920\n\t"
        "v_mul_f32 v78, v210, v78\n\t"
        "v_mul_f32 v79, v211, v79\n\t"
        "v_cvt_pk_bf16_f32 v68, v72, v73\n\t"
        "s_waitcnt lgkmcnt(10)\n\t"
        "v_mfma_f32_32x32x16_bf16 v[32:47], v[96:99], v[168:171], v[32:47]\n\t"
        "ds_read_b64_tr_b16 v[96:97], v121 offset:36992\n\t"
        "ds_read_b64_tr_b16 v[98:99], v121 offset:40064\n\t"
        "v_cvt_pk_bf16_f32 v69, v74, v75\n\t"
        "v_cvt_pk_bf16_f32 v70, v76, v77\n\t"
        "v_cvt_pk_bf16_f32 v71, v78, v79\n\t"
        "s_waitcnt lgkmcnt(10)\n\t"
        "v_mfma_f32_32x32x16_bf16 v[48:63], v[100:103], v[168:171], v[48:63]\n\t"
        "ds_read_b64_tr_b16 v[100:101], v122 offset:36992\n\t"
        "ds_read_b64_tr_b16 v[102:103], v122 offset:40064\n\t"
        "v_permlane32_swap_b32 v64, v66\n\t"
        "v_permlane32_swap_b32 v65, v67\n\t"
        "v_permlane32_swap_b32 v68, v70\n\t"
        "s_waitcnt lgkmcnt(10)\n\t"
        "v_mfma_f32_32x32x16_bf16 v[32:47], v[104:107], v[172:175], v[32:47]\n\t"
        "ds_read_b64_tr_b16 v[104:105], v121 offset:43136\n\t"
        "ds_read_b64_tr_b16 v[106:107], v121 offset:46208\n\t"
        "v_permlane32_swap_b32 v69, v71\n\t"
        "buffer_store_dwordx4 v[64:67], v213, s[40:43], 0 offen offset:2048 sc1\n\t"
        "buffer_store_dwordx4 v[68:71], v213, s[40:43], 0 offen offset:3072 sc1\n\t"
        "s_waitcnt lgkmcnt(10)\n\t"
        "v_mfma_f32_32x32x16_bf16 v[48:63], v[108:111], v[172:175], v[48:63]\n\t"
        "ds_read_b64_tr_b16 v[108:109], v122 offset:43136\n\t"
        "ds_read_b64_tr_b16 v[110:111], v122 offset:46208\n\t"
        "v_mul_f32 v80, v196, v80\n\t"
        "v_mul_f32 v81, v197, v81\n\t"
        "v_mul_f32 v82, v198, v82\n\t"
        "s_waitcnt lgkmcnt(10)\n\t"
        "v_mfma_f32_32x32x16_bf16 v[32:47], v[112:115], v[176:179], v[32:47]\n\t"
        "ds_read_b64_tr_b16 v[112:113], v121 offset:49280\n\t"
        "ds_read_b64_tr_b16 v[114:115], v121 offset:52352\n\t"
        "v_mul_f32 v83, v199, v83\n\t"
        "v_mul_f32 v84, v200, v84\n\t"
        "v_mul_f32 v85, v201, v85\n\t"
        "s_waitcnt lgkmcnt(10)\n\t"
        "v_mfma_f32_32x32x16_bf16 v[48:63], v[116:119], v[176:179], v[48:63]\n\t"
        "ds_read_b64_tr_b16 v[116:117], v122 offset:49280\n\t"
        "ds_read_b64_tr_b16 v[118:119], v122 offset:52352\n\t"
        "v_mul_f32 v86, v202, v86\n\t"
        "v_mul_f32 v87, v203, v87\n\t"
        "v_cvt_pk_bf16_f32 v80, v80, v81\n\t"
        "s_waitcnt lgkmcnt(10)\n\t"
        "v_mfma_f32_32x32x16_bf16 v[32:47], v[96:99], v[180:183], v[32:47]\n\t"
        "ds_read_b64_tr_b16 v[96:97], v121 offset:55424\n\t"
        "ds_read_b64_tr_b16 v[98:99], v121 offset:58496\n\t"
        "v_cvt_pk_bf16_f32 v81, v82, v83\n\t"
        "v_cvt_pk_bf16_f32 v82, v84, v85\n\t"
        "v_cvt_pk_bf16_f32 v83, v86, v87\n\t"
        "s_waitcnt lgkmcnt(10)\n\t"
        "v_mfma_f32_32x32x16_bf16 v[48:63], v[100:103], v[180:183], v[48:63]\n\t"
        "ds_read_b64_tr_b16 v[100:101], v122 offset:55424\n\t"
        "ds_read_b64_tr_b16 v[102:103], v122 offset:58496\n\t"
        "v_mul_f32 v88, v204, v88\n\t"
        "v_mul_f32 v89, v205, v89\n\t"
        "v_mul_f32 v90, v206, v90\n\t"
        "s_waitcnt lgkmcnt(10)\n\t"
        "v_mfma_f32_32x32x16_bf16 v[32:47], v[104:107], v[184:187], v[32:47]\n\t"
        "ds_read_b64_tr_b16 v[104:105], v123 offset:6144\n\t"
        "ds_read_b64_tr_b16 v[106:107], v123 offset:9216\n\t"
        "v_mul_f32 v91, v207, v91\n\t"
        "v_mul_f32 v92, v208, v92\n\t"
        "v_mul_f32 v93, v209, v93\n\t"
        "s_waitcnt lgkmcnt(10)\n\t"
        "v_mfma_f32_32x32x16_bf16 v[48:63], v[108:111], v[184:187], v[48:63]\n\t"
        "ds_read_b64_tr_b16 v[108:109], v124 offset:6144\n\t"
        "ds_read_b64_tr_b16 v[110:111], v124 offset:9216\n\t"
        "v_mul_f32 v94, v210, v94\n\t"
        "v_mul_f32 v95, v211, v95\n\t"
        "v_cvt_pk_bf16_f32 v84, v88, v89\n\t"
        "s_waitcnt lgkmcnt(10)\n\t"
        "v_mfma_f32_32x32x16_bf16 v[32:47], v[112:115], v[188:191], v[32:47]\n\t"
        "ds_read_b64_tr_b16 v[112:113], v121 offset:0\n\t"
        "ds_read_b64_tr_b16 v[114:115], v121 offset:3072\n\t"
        "v_cvt_pk_bf16_f32 v85, v90, v91\n\t"
        "v_cvt_pk_bf16_f32 v86, v92, v93\n\t"
        "v_cvt_pk_bf16_f32 v87, v94, v95\n\t"
        "s_waitcnt lgkmcnt(10)\n\t"
        "v_mfma_f32_32x32x16_bf16 v[48:63], v[116:119], v[188:191], v[48:63]\n\t"
        "ds_read_b64_tr_b16 v[116:117], v122 offset:0\n\t"
        "ds_read_b64_tr_b16 v[118:119], v122 offset:3072\n\t"
        "v_permlane32_swap_b32 v80, v82\n\t"
        "v_permlane32_swap_b32 v81, v83\n\t"
        "v_permlane32_swap_b32 v84, v86\n\t"
        "s_waitcnt lgkmcnt(10)\n\t"
        "v_mfma_f32_32x32x16_bf16 v[32:47], v[96:99], v[192:195], v[32:47]\n\t"
        "ds_read_b64_tr_b16 v[96:97], v121 offset:6144\n\t"
        "ds_read_b64_tr_b16 v[98:99], v121 offset:9216\n\t"
        "v_permlane32_swap_b32 v85, v87\n\t"
        "buffer_store_dwordx4 v[80:83], v214, s[40:43], 0 offen offset:2048 sc1\n\t"
        "buffer_store_dwordx4 v[84:87], v214, s[40:43], 0 offen offset:3072 sc1\n\t"
        "s_waitcnt lgkmcnt(10)\n\t"
        "v_mfma_f32_32x32x16_bf16 v[48:63], v[100:103], v[192:195], v[48:63]\n\t"
        "ds_read_b64_tr_b16 v[100:101], v122 offset:6144\n\t"
        "ds_read_b64_tr_b16 v[102:103], v122 offset:9216\n\t"
        "s_waitcnt lgkmcnt(10)\n\t"
        "v_mfma_f32_32x32x16_bf16 v[0:15], v[104:107], v[152:155], v[0:15]\n\t"
        "ds_read_b64_tr_b16 v[104:105], v121 offset:12288\n\t"
        "ds_read_b64_tr_b16 v[106:107], v121 offset:15360\n\t"
        "s_waitcnt lgkmcnt(10)\n\t"
        "v_mfma_f32_32x32x16_bf16 v[16:31], v[108:111], v[152:155], v[16:31]\n\t"
        "ds_read_b64_tr_b16 v[108:109], v122 offset:12288\n\t"
        "ds_read_b64_tr_b16 v[110:111], v122 offset:15360\n\t"
        "v_mul_f32 v32, v212, v32\n\t"
        "v_mul_f32 v33, v212, v33\n\t"
        "v_mul_f32 v34, v212, v34\n\t"
        "s_waitcnt lgkmcnt(10)\n\t"
        "v_mfma_f32_32x32x16_bf16 v[0:15], v[112:115], v[156:159], v[0:15]\n\t"
        "ds_read_b64_tr_b16 v[112:113], v121 offset:18432\n\t"
        "ds_read_b64_tr_b16 v[114:115], v121 offset:21504\n\t"
        "v_mul_f32 v35, v212, v35\n\t"
        "v_mul_f32 v36, v212, v36\n\t"
        "v_mul_f32 v37, v212, v37\n\t"
        "s_waitcnt lgkmcnt(10)\n\t"
        "v_mfma_f32_32x32x16_bf16 v[16:31], v[116:119], v[156:159], v[16:31]\n\t"
        "ds_read_b64_tr_b16 v[116:117], v122 offset:18432\n\t"
        "ds_read_b64_tr_b16 v[118:119], v122 offset:21504\n\t"
        "v_mul_f32 v38, v212, v38\n\t"
        "v_mul_f32 v39, v212, v39\n\t"
        "v_cvt_pk_bf16_f32 v32, v32, v33\n\t"
        "s_waitcnt lgkmcnt(10)\n\t"
        "v_mfma_f32_32x32x16_bf16 v[0:15], v[96:99], v[160:163], v[0:15]\n\t"
        "ds_read_b64_tr_b16 v[96:97], v121 offset:24576\n\t"
        "ds_read_b64_tr_b16 v[98:99], v121 offset:27648\n\t"
        "v_cvt_pk_bf16_f32 v33, v34, v35\n\t"
        "v_cvt_pk_bf16_f32 v34, v36, v37\n\t"
        "v_cvt_pk_bf16_f32 v35, v38, v39\n\t"
        "s_waitcnt lgkmcnt(10)\n\t"
        "v_mfma_f32_32x32x16_bf16 v[16:31], v[100:103], v[160:163], v[16:31]\n\t"
        "ds_read_b64_tr_b16 v[100:101], v122 offset:24576\n\t"
        "ds_read_b64_tr_b16 v[102:103], v122 offset:27648\n\t"
        "v_mul_f32 v40, v212, v40\n\t"
        "v_mul_f32 v41, v212, v41\n\t"
        "v_mul_f32 v42, v212, v42\n\t"
        "s_waitcnt lgkmcnt(10)\n\t"
        "v_mfma_f32_32x32x16_bf16 v[0:15], v[104:107], v[164:167], v[0:15]\n\t"
        "ds_read_b64_tr_b16 v[104:105], v121 offset:30720\n\t"
        "ds_read_b64_tr_b16 v[106:107], v121 offset:33792\n\t"
        "v_mul_f32 v43, v212, v43\n\t"
        "v_mul_f32 v44, v212, v44\n\t"
        "v_mul_f32 v45, v212, v45\n\t"
        "s_waitcnt lgkmcnt(10)\n\t"
        "v_mfma_f32_32x32x16_bf16 v[16:31], v[108:111], v[164:167], v[16:31]\n\t"
        "ds_read_b64_tr_b16 v[108:109], v122 offset:30720\n\t"
        "ds_read_b64_tr_b16 v[110:111], v122 offset:33792\n\t"
        "v_mul_f32 v46, v212, v46\n\t"
        "v_mul_f32 v47, v212, v47\n\t"
        "v_cvt_pk_bf16_f32 v36, v40, v41\n\t"
        "s_waitcnt lgkmcnt(10)\n\t"
        "v_mfma_f32_32x32x16_bf16 v[0:15], v[112:115], v[168:171], v[0:15]\n\t"
        "ds_read_b64_tr_b16 v[112:113], v121 offset:36864\n\t"
        "ds_read_b64_tr_b16 v[114:115], v121 offset:39936\n\t"
        "v_cvt_pk_bf16_f32 v37, v42, v43\n\t"
        "v_cvt_pk_bf16_f32 v38, v44, v45\n\t"
        "v_cvt_pk_bf16_f32 v39, v46, v47\n\t"
        "s_waitcnt lgkmcnt(10)\n\t"
        "v_mfma_f32_32x32x16_bf16 v[16:31], v[116:119], v[168:171], v[16:31]\n\t"
        "ds_read_b64_tr_b16 v[116:117], v122 offset:36864\n\t"
        "ds_read_b64_tr_b16 v[118:119], v122 offset:39936\n\t"
        "buffer_store_dwordx4 v[32:35], v213, s[40:43], 0 offen offset:0 sc1\n\t"
        "buffer_store_dwordx4 v[36:39], v213, s[40:43], 0 offen offset:1024 sc1\n\t"
        "v_mul_f32 v48, v212, v48\n\t"
        "s_waitcnt lgkmcnt(10)\n\t"
        "v_mfma_f32_32x32x16_bf16 v[0:15], v[96:99], v[172:175], v[0:15]\n\t"
        "ds_read_b64_tr_b16 v[96:97], v121 offset:43008\n\t"
        "ds_read_b64_tr_b16 v[98:99], v121 offset:46080\n\t"
        "v_mul_f32 v49, v212, v49\n\t"
        "v_mul_f32 v50, v212, v50\n\t"
        "v_mul_f32 v51, v212, v51\n\t"
        "s_waitcnt lgkmcnt(10)\n\t"
        "v_mfma_f32_32x32x16_bf16 v[16:31], v[100:103], v[172:175], v[16:31]\n\t"
        "ds_read_b64_tr_b16 v[100:101], v122 offset:43008\n\t"
        "ds_read_b64_tr_b16 v[102:103], v122 offset:46080\n\t"
        "v_mul_f32 v52, v212, v52\n\t"
        "v_mul_f32 v53, v212, v53\n\t"
        "v_mul_f32 v54, v212, v54\n\t"
        "s_waitcnt lgkmcnt(10)\n\t"
        "v_mfma_f32_32x32x16_bf16 v[0:15], v[104:107], v[176:179], v[0:15]\n\t"
        "ds_read_b64_tr_b16 v[104:105], v121 offset:49152\n\t"
        "ds_read_b64_tr_b16 v[106:107], v121 offset:52224\n\t"
        "v_mul_f32 v55, v212, v55\n\t"
        "v_cvt_pk_bf16_f32 v48, v48, v49\n\t"
        "v_cvt_pk_bf16_f32 v49, v50, v51\n\t"
        "s_waitcnt lgkmcnt(10)\n\t"
        "v_mfma_f32_32x32x16_bf16 v[16:31], v[108:111], v[176:179], v[16:31]\n\t"
        "ds_read_b64_tr_b16 v[108:109], v122 offset:49152\n\t"
        "ds_read_b64_tr_b16 v[110:111], v122 offset:52224\n\t"
        "v_cvt_pk_bf16_f32 v50, v52, v53\n\t"
        "v_cvt_pk_bf16_f32 v51, v54, v55\n\t"
        "v_mul_f32 v56, v212, v56\n\t"
        "s_waitcnt lgkmcnt(10)\n\t"
        "v_mfma_f32_32x32x16_bf16 v[0:15], v[112:115], v[180:183], v[0:15]\n\t"
        "ds_read_b64_tr_b16 v[112:113], v121 offset:55296\n\t"
        "ds_read_b64_tr_b16 v[114:115], v121 offset:58368\n\t"
        "v_mul_f32 v57, v212, v57\n\t"
        "v_mul_f32 v58, v212, v58\n\t"
        "v_mul_f32 v59, v212, v59\n\t"
        "s_waitcnt lgkmcnt(10)\n\t"
        "v_mfma_f32_32x32x16_bf16 v[16:31], v[116:119], v[180:183], v[16:31]\n\t"
        "ds_read_b64_tr_b16 v[116:117], v122 offset:55296\n\t"
        "ds_read_b64_tr_b16 v[118:119], v122 offset:58368\n\t"
        "v_mul_f32 v60, v212, v60\n\t"
        "v_mul_f32 v61, v212, v61\n\t"
        "v_mul_f32 v62, v212, v62\n\t"
        "s_waitcnt lgkmcnt(10)\n\t"
        "v_mfma_f32_32x32x16_bf16 v[0:15], v[96:99], v[184:187], v[0:15]\n\t"
        "v_mul_f32 v63, v212, v63\n\t"
        "v_cvt_pk_bf16_f32 v52, v56, v57\n\t"
        "v_cvt_pk_bf16_f32 v53, v58, v59\n\t"
        "v_cvt_pk_bf16_f32 v54, v60, v61\n\t"
        "v_cvt_pk_bf16_f32 v55, v62, v63\n\t"
        "s_waitcnt lgkmcnt(8)\n\t"
        "v_mfma_f32_32x32x16_bf16 v[16:31], v[100:103], v[184:187], v[16:31]\n\t"
        "buffer_store_dwordx4 v[48:51], v214, s[40:43], 0 offen offset:0 sc1\n\t"
        "buffer_store_dwordx4 v[52:55], v214, s[40:43], 0 offen offset:1024 sc1\n\t"
        "s_waitcnt lgkmcnt(6)\n\t"
        "v_mfma_f32_32x32x16_bf16 v[0:15], v[104:107], v[188:191], v[0:15]\n\t"
        "s_waitcnt lgkmcnt(4)\n\t"
        "v_mfma_f32_32x32x16_bf16 v[16:31], v[108:111], v[188:191], v[16:31]\n\t"
        "s_waitcnt lgkmcnt(2)\n\t"
        "v_mfma_f32_32x32x16_bf16 v[0:15], v[112:115], v[192:195], v[0:15]\n\t"
        "s_waitcnt lgkmcnt(0)\n\t"
        "v_mfma_f32_32x32x16_bf16 v[16:31], v[116:119], v[192:195], v[16:31]\n\t"
        "s_nop 11\n\t"
        "s_branch 9f\n\t"
        "16:\n\t"
        "s_mov_b32 s48, 0x10000\n\t"
        "8:\n\t"
        "ds_read_b32 v126, v125\n\t"
        "s_sub_u32 s48, s48, 1\n\t"
        "s_waitcnt lgkmcnt(0)\n\t"
        "s_cbranch_scc1 17b\n\t"
        "v_cmp_gt_u32 vcc, s47, v126\n\t"
        "s_cbranch_vccnz 8b\n\t"
        "s_branch 17b\n\t"
        "18:\n\t"
        "s_mov_b32 s48, 0x10000\n\t"
        "8:\n\t"
        "ds_read_b32 v126, v125\n\t"
        "s_sub_u32 s48, s48, 1\n\t"
        "s_waitcnt lgkmcnt(0)\n\t"
        "s_cbranch_scc1 19b\n\t"
        "v_cmp_gt_u32 vcc, s47, v126\n\t"
        "s_cbranch_vccnz 8b\n\t"
        "s_branch 19b\n\t"
        "20:\n\t"
        "s_mov_b32 s48, 0x10000\n\t"
        "8:\n\t"
        "ds_read_b32 v126, v125\n\t"
        "s_sub_u32 s48, s48, 1\n\t"
        "s_waitcnt lgkmcnt(0)\n\t"
        "s_cbranch_scc1 21b\n\t"
        "v_cmp_gt_u32 vcc, s47, v126\n\t"
        "s_cbranch_vccnz 8b\n\t"
        "s_branch 21b\n\t"
        "22:\n\t"
        "s_mov_b32 s48, 0x10000\n\t"
        "8:\n\t"
        "ds_read_b32 v126, v125\n\t"
        "s_sub_u32 s48, s48, 1\n\t"
        "s_waitcnt lgkmcnt(0)\n\t"
        "s_cbranch_scc1 23b\n\t"
        "v_cmp_gt_u32 vcc, s47, v126\n\t"
        "s_cbranch_vccnz 8b\n\t"
        "s_branch 23b\n\t"
        "24:\n\t"
        "s_mov_b32 s48, 0x10000\n\t"
        "8:\n\t"
        "ds_read_b32 v126, v125\n\t"
        "s_sub_u32 s48, s48, 1\n\t"
        "s_waitcnt lgkmcnt(0)\n\t"
        "s_cbranch_scc1 25b\n\t"
        "v_cmp_gt_u32 vcc, s47, v126\n\t"
        "s_cbranch_vccnz 8b\n\t"
        "s_branch 25b\n\t"
        "26:\n\t"
        "s_mov_b32 s48, 0x10000\n\t"
        "8:\n\t"
        "ds_read_b32 v126, v125\n\t"
        "s_sub_u32 s48, s48, 1\n\t"
        "s_waitcnt lgkmcnt(0)\n\t"
        "s_cbranch_scc1 27b\n\t"
        "v_cmp_gt_u32 vcc, s47, v126\n\t"
        "s_cbranch_vccnz 8b\n\t"
        "s_branch 27b\n\t"
        "28:\n\t"
        "s_mov_b32 s48, 0x10000\n\t"
        "8:\n\t"
        "ds_read_b32 v126, v125\n\t"
        "s_sub_u32 s48, s48, 1\n\t"
        "s_waitcnt lgkmcnt(0)\n\t"
        "s_cbranch_scc1 29b\n\t"
        "v_cmp_gt_u32 vcc, s47, v126\n\t"
        "s_cbranch_vccnz 8b\n\t"
        "s_branch 29b\n\t"
        "30:\n\t"
        "s_mov_b32 s48, 0x10000\n\t"
        "8:\n\t"
        "ds_read_b32 v126, v125\n\t"
        "s_sub_u32 s48, s48, 1\n\t"
        "s_waitcnt lgkmcnt(0)\n\t"
        "s_cbranch_scc1 31b\n\t"
        "v_cmp_gt_u32 vcc, s47, v126\n\t"
        "s_cbranch_vccnz 8b\n\t"
        "s_branch 31b\n\t"
        "74:\n\t"
        "s_mov_b32 s48, 0x10000\n\t"
        "8:\n\t"
        "ds_read_b32 v126, v125\n\t"
        "s_sub_u32 s48, s48, 1\n\t"
        "s_waitcnt lgkmcnt(0)\n\t"
        "s_cbranch_scc1 75b\n\t"
        "v_cmp_gt_u32 vcc, s47, v126\n\t"
        "s_cbranch_vccnz 8b\n\t"
        "s_branch 75b\n\t"
        "9:\n\t"
        : "+{v[0:15]}"(acc[0]), "+{v[16:31]}"(acc[1]), "+{v[32:47]}"(acc[2]), "+{v[48:63]}"(acc[3]), "+{v[64:79]}"(acc[4]), "+{v[80:95]}"(acc[5]),
          "+{v[96:111]}"(stA), "+{v[112:127]}"(stB), "+{v[128:131]}"(stC), "+{v[148:163]}"(xr1), "+{v[164:179]}"(xr2), "+{v[180:195]}"(xr3),
          "+{s44}"(soff), "+{s46}"(slot), "+{s47}"(need)
        : "{s[40:43]}"(srd), KSKIP, "{v[212:215]}"(ep) : "v196", "v197", "v198", "v199", "v200", "v201", "v202", "v203", "v204", "v205", "v206", "v207", "v208", "v209", "v210", "v211", "v132", "v133", "v134", "v135", "v136", "v137", "v138", "v139", "v140", "v141", "v142", "v143", "v144", "v145", "v146", "v147", "s45", "s48", "vcc", "scc", "memory");
      STAMPV(4 + 16 * L);
      PHASE_VARS;
      u32x4 kv[4][2];
#pragma unroll
      for (int j = 0; j < 4; ++j)
#pragma unroll
        for (int e = 0; e < 8; ++e) kv[j][e >> 2][e & 3] = __float_as_uint(acc[2 + j][e]);
      constexpr float QS = 0.17677669529663687f * 1.4426950408889634f;
      u32x4 qo[2][2];
      auto drained = [&]() { asm volatile("s_waitcnt vmcnt(0)\n\tds_write_b32 %0, %1" :: "v"(lbase + L_W + 80 + 4 * wid), "v"(2 * L + 1) : "memory"); };
      if (L == 0) {
#pragma unroll
        for (int e = 0; e < 16; ++e) {
          if (e == 8) { SBAR(); drained(); SBAR(); }
          const unsigned w = pack2(acc[e >> 3][2 * (e & 7)], acc[e >> 3][2 * (e & 7) + 1]);
          qo[e >> 3][(e >> 2) & 1][e & 3] = pack2(lo16(w) * QS, hi16(w) * QS);
        }
      } else {
        const float aq = U2F(ep[0]) * QS;
#pragma unroll
        for (int e = 0; e < 16; ++e) qo[e >> 3][(e >> 2) & 1][e & 3] = pack2(acc[e >> 3][2 * (e & 7)] * aq, acc[e >> 3][2 * (e & 7) + 1] * aq);
        SBAR();
        lds_wait_ge<0>(lbase + L_W + 4 + 4 * (lane & 1), 1);
        const int idx = (int)__builtin_amdgcn_readfirstlane(words[3]);
        if ((idx >> 5) == tile) {
          f32x4 cs[4][4];
          float cv[2];
#pragma unroll
          for (int j = 0; j < 6; ++j) {
            const int cb = (j >> 1) * 64 + ((j & 1) ^ hh) * 32;
            if (j >= 4) cv[j - 4] = *reinterpret_cast<const float*>(smem + L_PATCH + (cb + r32) * 4);
            else {
#pragma unroll
              for (int g = 0; g < 4; ++g) cs[j][g] = *reinterpret_cast<const f32x4*>(smem + L_PATCH + (cb + 8 * g + 4 * hi) * 4);
            }
          }
          SBAR();
          const bool me = r32 == (idx & 31);
          const int rel = (idx & 31) - 8 * hi;
#pragma unroll
          for (int j = 0; j < 2; ++j) {
            const unsigned pv = pack2(cv[j], cv[j]);
#pragma unroll
            for (int e = 0; e < 8; ++e) {
              unsigned a = pack2(cs[j][e >> 1][2 * (e & 1)] * QS, cs[j][e >> 1][2 * (e & 1) + 1] * QS), b = pack2(cs[2 + j][e >> 1][2 * (e & 1)], cs[2 + j][e >> 1][2 * (e & 1) + 1]);
              PIN(a); PIN(b);
              qo[j][e >> 2][e & 3] = me ? a : qo[j][e >> 2][e & 3];
              kv[j][e >> 2][e & 3] = me ? b : kv[j][e >> 2][e & 3];
              const int t0 = 16 * (e >> 2) + 2 * (e & 3);
              const unsigned mask = rel == t0 ? 0xffffu : rel == t0 + 1 ? 0xffff0000u : 0u;
              kv[2 + j][e >> 2][e & 3] = (kv[2 + j][e >> 2][e & 3] & ~mask) | (pv & mask);
            }
          }
          if (me) {
#pragma unroll
            for (int i = 0; i < 4; ++i) x1p[i] = *reinterpret_cast<const u32x4*>(smem + L_X2P + (i * 2 + hi) * 16);
            reinterpret_cast<float*>(smem + L_AB)[256 + wid * 64 + lane] = hi ? 1.0f : 0.0f;
          }
          DRAIN();
#pragma unroll
          for (int j = 0; j < 4; ++j) { stg(rs, ep[1 + (j & 1)] + (j >> 1) * 2048, kv[j][0]); stg(rs, ep[1 + (j & 1)] + (j >> 1) * 2048 + 1024, kv[j][1]); }
        }
        drained();
      }
      SBAR();
#pragma unroll
      for (int j = 0; j < 2; ++j) {
        char* p = smem + L_XCH + ((j ^ hh) * 4 + wid) * 2048 + lane * 16;
        *reinterpret_cast<u32x4*>(p) = qo[j][0]; *reinterpret_cast<u32x4*>(p + 1024) = qo[j][1];
        p = smem + L_XCH + 16384 + wid * 4096 + j * 2048 + lane * 16;
        *reinterpret_cast<u32x4*>(p) = kv[2 * j + 1][0]; *reinterpret_cast<u32x4*>(p + 1024) = kv[2 * j + 1][1];
      }
      STAMPV(41 + 4 * L);
      BAR_LGKM();
      STAMPV(43 + 4 * L);
      kva = cat4(kv[0][0], kv[0][1], kv[2][0], kv[2][1]);
      const u32x4* pk = reinterpret_cast<const u32x4*>(smem + L_XCH + 16384 + (wid ^ 1) * 4096 + lane * 16);
#pragma unroll
      for (int u = 0; u < 4; ++u) {
        const u32x4* p = reinterpret_cast<const u32x4*>(smem + L_XCH + (hh * 4 + ((2 * kh + 2 + u) & 3)) * 2048 + lane * 16);
        q[u] = __builtin_shufflevector(p[0], p[64], 0, 1, 2, 3, 4, 5, 6, 7);
      }
      kvb = cat4(pk[0], pk[64], pk[128], pk[192]);
      SBAR();
      asm volatile(
        "v_mov_b64 v[0:1], 0\n\t"
        "v_mov_b64 v[2:3], 0\n\t"
        "v_mov_b64 v[4:5], 0\n\t"
        "v_mov_b64 v[6:7], 0\n\t"
        "v_mov_b64 v[8:9], 0\n\t"
        "v_mov_b64 v[10:11], 0\n\t"
        "v_mov_b64 v[12:13], 0\n\t"
        "v_mov_b64 v[14:15], 0\n\t"
        "v_mov_b64 v[16:17], 0\n\t"
        "v_mov_b64 v[18:19], 0\n\t"
        "v_mov_b64 v[20:21], 0\n\t"
        "v_mov_b64 v[22:23], 0\n\t"
        "v_mov_b64 v[24:25], 0\n\t"
        "v_mov_b64 v[26:27], 0\n\t"
        "v_mov_b64 v[28:29], 0\n\t"
        "v_mov_b64 v[30:31], 0\n\t"
        "v_mov_b64 v[32:33], 0\n\t"
        "v_mov_b64 v[34:35], 0\n\t"
        "v_mov_b64 v[36:37], 0\n\t"
        "v_mov_b64 v[38:39], 0\n\t"
        "v_mov_b64 v[40:41], 0\n\t"
        "v_mov_b64 v[42:43], 0\n\t"
        "v_mov_b64 v[44:45], 0\n\t"
        "v_mov_b64 v[46:47], 0\n\t"
        "v_mov_b64 v[48:49], 0\n\t"
        "v_mov_b64 v[50:51], 0\n\t"
        "v_mov_b64 v[52:53], 0\n\t"
        "v_mov_b64 v[54:55], 0\n\t"
        "v_mov_b64 v[56:57], 0\n\t"
        "v_mov_b64 v[58:59], 0\n\t"
        "v_mov_b64 v[60:61], 0\n\t"
        "v_mov_b64 v[62:63], 0\n\t"
        "v_mov_b64 v[128:129], 0\n\t"
        "v_mov_b64 v[130:131], 0\n\t"
        "v_mov_b64 v[132:133], 0\n\t"
        "v_mov_b64 v[134:135], 0\n\t"
        "v_mov_b64 v[136:137], 0\n\t"
        "v_mov_b64 v[138:139], 0\n\t"
        "v_mov_b64 v[140:141], 0\n\t"
        "v_mov_b64 v[142:143], 0\n\t"
        : "={v[0:15]}"(oz[0]), "={v[16:31]}"(oz[1]), "={v[32:47]}"(oz[2]), "={v[48:63]}"(oz[3]), "={v[128:143]}"(oz[4]));
    }
    STAMPV(5 + 16 * L);
    {
      u32x4 ones;
      unsigned kvoff;
      u32x8 fv, cv;
      asm volatile("" : "={v[184:191]}"(fv), "={v[216:223]}"(cv));
      { PHASE_VARS;
        const unsigned v = (lane & 3) == 0 ? 0x3F803F80u : 0u; const bool up = (lane & 4) != 0; ones[0] = ones[1] = up ? 0u : v; ones[2] = ones[3] = up ? v : 0u;
        kvoff = OFF_KV + L * (10 * MBY) + (tn * 2 + hh) * HS + kh * 8192 + lane * 16;
        fv[1] = lbase + L_FREE + wid * 256 + lane * 4; fv[2] = 32 * L + 32;
        cv[0] = lbase + L_XCH + wid * 8192 + lane * 16; cv[1] = lbase + L_XCH + (wid ^ 2) * 8192 + lane * 16;
        cv[2] = lbase + L_XCH + 32768 + wid * 512 + lane * 4; cv[3] = lbase + L_XCH + 32768 + (wid ^ 2) * 512 + lane * 4;
        cv[4] = lbase + L_W + 48 + wid * 4; cv[5] = lbase + L_W + 48 + (wid ^ 2) * 4; cv[6] = L + 1;
        cv[7] = ((tile * 32 + r32) * D_ + tn * 64 + hi * 8) * 2; }
      f32x16 o0 = oz[0], o1 = oz[1], o2 = oz[2], o3 = oz[3], lv = oz[4];
      unsigned soff = (tm | 7) * 16384;
      STAMP_DECL;
      asm volatile(
        "s_nop 1\n\t"
        "v_mfma_f32_32x32x16_bf16 v[96:111], v[152:155], v[64:67], 0\n\t"
        "v_mfma_f32_32x32x16_bf16 v[96:111], v[156:159], v[68:71], v[96:111]\n\t"
        "s_mov_b32 s46, 0\n\t"
        "ds_write_b32 v185, v186\n\t"
        "s_nop 9\n\t"
        "v_exp_f32 v96, v96\n\t"
        "v_exp_f32 v97, v97\n\t"
        "v_exp_f32 v98, v98\n\t"
        "v_exp_f32 v99, v99\n\t"
        "v_exp_f32 v100, v100\n\t"
        "v_exp_f32 v101, v101\n\t"
        "v_exp_f32 v102, v102\n\t"
        "v_mfma_f32_32x32x16_bf16 v[112:127], v[152:155], v[72:75], 0\n\t"
        "v_exp_f32 v103, v103\n\t"
        "v_exp_f32 v104, v104\n\t"
        "v_exp_f32 v105, v105\n\t"
        "v_mfma_f32_32x32x16_bf16 v[112:127], v[156:159], v[76:79], v[112:127]\n\t"
        "v_exp_f32 v106, v106\n\t"
        "v_exp_f32 v107, v107\n\t"
        "v_exp_f32 v108, v108\n\t"
        "v_smfmac_f32_32x32x32_bf16 v[0:15], v[160:163], v[96:103], v148\n\t"
        "v_exp_f32 v109, v109\n\t"
        "v_exp_f32 v110, v110\n\t"
        "v_exp_f32 v111, v111\n\t"
        "v_smfmac_f32_16x16x64_bf16 v[128:131], v[144:147], v[96:103], v148\n\t"
        "v_exp_f32 v112, v112\n\t"
        "v_exp_f32 v113, v113\n\t"
        "v_smfmac_f32_32x32x32_bf16 v[0:15], v[164:167], v[104:111], v148\n\t"
        "v_exp_f32 v114, v114\n\t"
        "v_exp_f32 v115, v115\n\t"
        "v_exp_f32 v116, v116\n\t"
        "v_smfmac_f32_16x16x64_bf16 v[128:131], v[144:147], v[104:111], v148\n\t"
        "v_exp_f32 v117, v117\n\t"
        "v_exp_f32 v118, v118\n\t"
        "v_mfma_f32_32x32x16_bf16 v[96:111], v[152:155], v[80:83], 0\n\t"
        "v_exp_f32 v119, v119\n\t"
        "v_exp_f32 v120, v120\n\t"
        "v_exp_f32 v121, v121\n\t"
        "v_mfma_f32_32x32x16_bf16 v[96:111], v[156:159], v[84:87], v[96:111]\n\t"
        "ds_read_b32 v151, v224\n\t"
        "v_exp_f32 v122, v122\n\t"
        "v_exp_f32 v123, v123\n\t"
        "v_exp_f32 v124, v124\n\t"
        "v_smfmac_f32_32x32x32_bf16 v[16:31], v[160:163], v[112:119], v148\n\t"
        "v_exp_f32 v125, v125\n\t"
        "v_exp_f32 v126, v126\n\t"
        "v_exp_f32 v127, v127\n\t"
        "v_smfmac_f32_16x16x64_bf16 v[132:135], v[144:147], v[112:119], v148\n\t"
        "s_cmp_lg_u32 s46, 0\n\t"
        "s_cbranch_scc1 25f\n\t"
        "s_waitcnt lgkmcnt(0)\n\t"
        "v_cmp_ne_u32 vcc, v222, v151\n\t"
        "s_cbranch_vccz 24f\n\t"
        "25:\n\t"
        "v_exp_f32 v96, v96\n\t"
        "v_exp_f32 v97, v97\n\t"
        "v_smfmac_f32_32x32x32_bf16 v[16:31], v[164:167], v[120:127], v148\n\t"
        "v_exp_f32 v98, v98\n\t"
        "v_exp_f32 v99, v99\n\t"
        "v_exp_f32 v100, v100\n\t"
        "v_smfmac_f32_16x16x64_bf16 v[132:135], v[144:147], v[120:127], v148\n\t"
        "v_exp_f32 v101, v101\n\t"
        "v_exp_f32 v102, v102\n\t"
        "v_mfma_f32_32x32x16_bf16 v[112:127], v[152:155], v[88:91], 0\n\t"
        "v_exp_f32 v103, v103\n\t"
        "v_exp_f32 v104, v104\n\t"
        "v_exp_f32 v105, v105\n\t"
        "v_mfma_f32_32x32x16_bf16 v[112:127], v[156:159], v[92:95], v[112:127]\n\t"
        "ds_read_b32 v151, v224\n\t"
        "v_exp_f32 v106, v106\n\t"
        "v_exp_f32 v107, v107\n\t"
        "v_exp_f32 v108, v108\n\t"
        "v_smfmac_f32_32x32x32_bf16 v[32:47], v[160:163], v[96:103], v148\n\t"
        "v_exp_f32 v109, v109\n\t"
        "v_exp_f32 v110, v110\n\t"
        "v_exp_f32 v111, v111\n\t"
        "v_smfmac_f32_16x16x64_bf16 v[136:139], v[144:147], v[96:103], v148\n\t"
        "s_cmp_lg_u32 s46, 0\n\t"
        "s_cbranch_scc1 27f\n\t"
        "s_waitcnt lgkmcnt(0)\n\t"
        "v_cmp_ne_u32 vcc, v222, v151\n\t"
        "s_cbranch_vccz 26f\n\t"
        "27:\n\t"
        "v_exp_f32 v112, v112\n\t"
        "v_exp_f32 v113, v113\n\t"
        "v_smfmac_f32_32x32x32_bf16 v[32:47], v[164:167], v[104:111], v148\n\t"
        "v_exp_f32 v114, v114\n\t"
        "v_exp_f32 v115, v115\n\t"
        "v_exp_f32 v116, v116\n\t"
        "v_smfmac_f32_16x16x64_bf16 v[136:139], v[144:147], v[104:111], v148\n\t"
        "v_exp_f32 v117, v117\n\t"
        "v_exp_f32 v118, v118\n\t"
        "v_mfma_f32_32x32x16_bf16 v[96:111], v[168:171], v[64:67], 0\n\t"
        "v_exp_f32 v119, v119\n\t"
        "v_exp_f32 v120, v120\n\t"
        "v_exp_f32 v121, v121\n\t"
        "v_mfma_f32_32x32x16_bf16 v[96:111], v[172:175], v[68:71], v[96:111]\n\t"
        "ds_read_b32 v151, v224\n\t"
        "v_exp_f32 v122, v122\n\t"
        "v_exp_f32 v123, v123\n\t"
        "v_exp_f32 v124, v124\n\t"
        "v_smfmac_f32_32x32x32_bf16 v[48:63], v[160:163], v[112:119], v148\n\t"
        "v_exp_f32 v125, v125\n\t"
        "v_exp_f32 v126, v126\n\t"
        "v_exp_f32 v127, v127\n\t"
        "v_smfmac_f32_16x16x64_bf16 v[140:143], v[144:147], v[112:119], v148\n\t"
        "s_cmp_lg_u32 s46, 0\n\t"
        "s_cbranch_scc1 29f\n\t"
        "s_waitcnt lgkmcnt(0)\n\t"
        "v_cmp_ne_u32 vcc, v222, v151\n\t"
        "s_cbranch_vccz 28f\n\t"
        "29:\n\t"
        "v_exp_f32 v96, v96\n\t"
        "v_exp_f32 v97, v97\n\t"
        "v_smfmac_f32_32x32x32_bf16 v[48:63], v[164:167], v[120:127], v148\n\t"
        "v_exp_f32 v98, v98\n\t"
        "v_exp_f32 v99, v99\n\t"
        "v_exp_f32 v100, v100\n\t"
        "v_smfmac_f32_16x16x64_bf16 v[140:143], v[144:147], v[120:127], v148\n\t"
        "v_exp_f32 v101, v101\n\t"
        "v_exp_f32 v102, v102\n\t"
        "v_mfma_f32_32x32x16_bf16 v[112:127], v[168:171], v[72:75], 0\n\t"
        "v_exp_f32 v103, v103\n\t"
        "v_exp_f32 v104, v104\n\t"
        "v_exp_f32 v105, v105\n\t"
        "v_mfma_f32_32x32x16_bf16 v[112:127], v[172:175], v[76:79], v[112:127]\n\t"
        "ds_read_b32 v151, v224\n\t"
        "v_exp_f32 v106, v106\n\t"
        "v_exp_f32 v107, v107\n\t"
        "v_exp_f32 v108, v108\n\t"
        "v_smfmac_f32_32x32x32_bf16 v[0:15], v[176:179], v[96:103], v148\n\t"
        "v_exp_f32 v109, v109\n\t"
        "v_exp_f32 v110, v110\n\t"
        "v_exp_f32 v111, v111\n\t"
        "v_smfmac_f32_16x16x64_bf16 v[128:131], v[144:147], v[96:103], v148\n\t"
        "s_cmp_lg_u32 s46, 0\n\t"
        "s_cbranch_scc1 31f\n\t"
        "s_waitcnt lgkmcnt(0)\n\t"
        "v_cmp_ne_u32 vcc, v222, v151\n\t"
        "s_cbranch_vccz 30f\n\t"
        "31:\n\t"
        "v_exp_f32 v112, v112\n\t"
        "v_exp_f32 v113, v113\n\t"
        "v_smfmac_f32_32x32x32_bf16 v[0:15], v[180:183], v[104:111], v148\n\t"
        "v_exp_f32 v114, v114\n\t"
        "v_exp_f32 v115, v115\n\t"
        "v_exp_f32 v116, v116\n\t"
        "v_smfmac_f32_16x16x64_bf16 v[128:131], v[144:147], v[104:111], v148\n\t"
        "v_exp_f32 v117, v117\n\t"
        "v_exp_f32 v118, v118\n\t"
        "v_mfma_f32_32x32x16_bf16 v[96:111], v[168:171], v[80:83], 0\n\t"
        "v_exp_f32 v119, v119\n\t"
        "v_exp_f32 v120, v120\n\t"
        "v_exp_f32 v121, v121\n\t"
        "v_mfma_f32_32x32x16_bf16 v[96:111], v[172:175], v[84:87], v[96:111]\n\t"
        "ds_read_b32 v151, v224\n\t"
        "v_exp_f32 v122, v122\n\t"
        "v_exp_f32 v123, v123\n\t"
        "v_exp_f32 v124, v124\n\t"
        "v_smfmac_f32_32x32x32_bf16 v[16:31], v[176:179], v[112:119], v148\n\t"
        "v_exp_f32 v125, v125\n\t"
        "v_exp_f32 v126, v126\n\t"
        "v_exp_f32 v127, v127\n\t"
        "v_smfmac_f32_16x16x64_bf16 v[132:135], v[144:147], v[112:119], v148\n\t"
        "s_cmp_lg_u32 s46, 0\n\t"
        "s_cbranch_scc1 33f\n\t"
        "s_waitcnt lgkmcnt(0)\n\t"
        "v_cmp_ne_u32 vcc, v222, v151\n\t"
        "s_cbranch_vccz 32f\n\t"
        "33:\n\t"
        "v_exp_f32 v96, v96\n\t"
        "v_exp_f32 v97, v97\n\t"
        "v_smfmac_f32_32x32x32_bf16 v[16:31], v[180:183], v[120:127], v148\n\t"
        "v_exp_f32 v98, v98\n\t"
        "v_exp_f32 v99, v99\n\t"
        "v_exp_f32 v100, v100\n\t"
        "v_smfmac_f32_16x16x64_bf16 v[132:135], v[144:147], v[120:127], v148\n\t"
        "v_exp_f32 v101, v101\n\t"
        "v_exp_f32 v102, v102\n\t"
        "v_mfma_f32_32x32x16_bf16 v[112:127], v[168:171], v[88:91], 0\n\t"
        "v_exp_f32 v103, v103\n\t"
        "v_exp_f32 v104, v104\n\t"
        "v_exp_f32 v105, v105\n\t"
        "v_mfma_f32_32x32x16_bf16 v[112:127], v[172:175], v[92:95], v[112:127]\n\t"
        "v_exp_f32 v106, v106\n\t"
        "v_exp_f32 v107, v107\n\t"
        "v_exp_f32 v108, v108\n\t"
        "v_smfmac_f32_32x32x32_bf16 v[32:47], v[176:179], v[96:103], v148\n\t"
        "v_exp_f32 v109, v109\n\t"
        "v_exp_f32 v110, v110\n\t"
        "v_exp_f32 v111, v111\n\t"
        "v_smfmac_f32_16x16x64_bf16 v[136:139], v[144:147], v[96:103], v148\n\t"
        "s_cmp_lg_u32 s46, 0\n\t"
        "s_cbranch_scc1 5f\n\t"
        "s_mov_b32 s45, 0x200000\n\t"
        "2:\n\t"
        "ds_read_b32 v151, v224\n\t"
        "s_sub_u32 s45, s45, 1\n\t"
        "s_waitcnt lgkmcnt(0)\n\t"
        "s_cbranch_scc1 3f\n\t"
        "v_cmp_ne_u32 vcc, v222, v151\n\t"
        "s_cbranch_vccnz 2b\n\t"
        "3:\n\t"
        "s_add_u32 s44, s44, 0x4000\n\t"
        "s_and_b32 s44, s44, 0x3ffff\n\t"
        "s_cmp_eq_u32 s44, s49\n\t"
        "s_cselect_b32 s44, s50, s44\n\t"
        "buffer_load_dwordx4 v[184:187], v149, s[40:43], s44 offen offset:0 sc1\n\t"
        "buffer_load_dwordx4 v[188:191], v149, s[40:43], s44 offen offset:1024 sc1\n\t"
        "buffer_load_dwordx4 v[192:195], v149, s[40:43], s44 offen offset:2048 sc1\n\t"
        "buffer_load_dwordx4 v[196:199], v149, s[40:43], s44 offen offset:3072 sc1\n\t"
        "buffer_load_dwordx4 v[200:203], v150, s[40:43], s44 offen offset:0 sc1\n\t"
        "buffer_load_dwordx4 v[204:207], v150, s[40:43], s44 offen offset:1024 sc1\n\t"
        "buffer_load_dwordx4 v[208:211], v150, s[40:43], s44 offen offset:2048 sc1\n\t"
        "buffer_load_dwordx4 v[212:215], v150, s[40:43], s44 offen offset:3072 sc1\n\t"
        "5:\n\t"
        "s_add_u32 s44, s44, 0x4000\n\t"
        "s_and_b32 s44, s44, 0x3ffff\n\t"
        "s_cmp_eq_u32 s44, s49\n\t"
        "s_cselect_b32 s44, s50, s44\n\t"
        "buffer_load_dwordx4 v[152:155], v149, s[40:43], s44 offen offset:0 sc1\n\t"
        "buffer_load_dwordx4 v[156:159], v149, s[40:43], s44 offen offset:1024 sc1\n\t"
        "buffer_load_dwordx4 v[160:163], v149, s[40:43], s44 offen offset:2048 sc1\n\t"
        "buffer_load_dwordx4 v[164:167], v149, s[40:43], s44 offen offset:3072 sc1\n\t"
        "s_mov_b32 s45, 6\n\t"
        "1:\n\t"
        "v_exp_f32 v112, v112\n\t"
        "v_exp_f32 v113, v113\n\t"
        "v_smfmac_f32_32x32x32_bf16 v[32:47], v[180:183], v[104:111], v148\n\t"
        "v_exp_f32 v114, v114\n\t"
        "v_exp_f32 v115, v115\n\t"
        "v_exp_f32 v116, v116\n\t"
        "v_smfmac_f32_16x16x64_bf16 v[136:139], v[144:147], v[104:111], v148\n\t"
        "s_waitcnt vmcnt(8)\n\t"
        "v_exp_f32 v117, v117\n\t"
        "v_exp_f32 v118, v118\n\t"
        "v_mfma_f32_32x32x16_bf16 v[96:111], v[184:187], v[64:67], 0\n\t"
        "v_exp_f32 v119, v119\n\t"
        "v_exp_f32 v120, v120\n\t"
        "v_exp_f32 v121, v121\n\t"
        "v_mfma_f32_32x32x16_bf16 v[96:111], v[188:191], v[68:71], v[96:111]\n\t"
        "v_exp_f32 v122, v122\n\t"
        "v_exp_f32 v123, v123\n\t"
        "v_exp_f32 v124, v124\n\t"
        "v_smfmac_f32_32x32x32_bf16 v[48:63], v[176:179], v[112:119], v148\n\t"
        "v_exp_f32 v125, v125\n\t"
        "v_exp_f32 v126, v126\n\t"
        "v_exp_f32 v127, v127\n\t"
        "v_smfmac_f32_16x16x64_bf16 v[140:143], v[144:147], v[112:119], v148\n\t"
        "buffer_load_dwordx4 v[168:171], v150, s[40:43], s44 offen offset:0 sc1\n\t"
        "v_exp_f32 v96, v96\n\t"
        "v_exp_f32 v97, v97\n\t"
        "v_smfmac_f32_32x32x32_bf16 v[48:63], v[180:183], v[120:127], v148\n\t"
        "v_exp_f32 v98, v98\n\t"
        "v_exp_f32 v99, v99\n\t"
        "v_exp_f32 v100, v100\n\t"
        "v_smfmac_f32_16x16x64_bf16 v[140:143], v[144:147], v[120:127], v148\n\t"
        "v_exp_f32 v101, v101\n\t"
        "v_exp_f32 v102, v102\n\t"
        "v_mfma_f32_32x32x16_bf16 v[112:127], v[184:187], v[72:75], 0\n\t"
        "v_exp_f32 v103, v103\n\t"
        "v_exp_f32 v104, v104\n\t"
        "v_exp_f32 v105, v105\n\t"
        "v_mfma_f32_32x32x16_bf16 v[112:127], v[188:191], v[76:79], v[112:127]\n\t"
        "v_exp_f32 v106, v106\n\t"
        "v_exp_f32 v107, v107\n\t"
        "v_exp_f32 v108, v108\n\t"
        "v_smfmac_f32_32x32x32_bf16 v[0:15], v[192:195], v[96:103], v148\n\t"
        "v_exp_f32 v109, v109\n\t"
        "v_exp_f32 v110, v110\n\t"
        "v_exp_f32 v111, v111\n\t"
        "v_smfmac_f32_16x16x64_bf16 v[128:131], v[144:147], v[96:103], v148\n\t"
        "buffer_load_dwordx4 v[172:175], v150, s[40:43], s44 offen offset:1024 sc1\n\t"
        "v_exp_f32 v112, v112\n\t"
        "v_exp_f32 v113, v113\n\t"
        "v_smfmac_f32_32x32x32_bf16 v[0:15], v[196:199], v[104:111], v148\n\t"
        "v_exp_f32 v114, v114\n\t"
        "v_exp_f32 v115, v115\n\t"
        "v_exp_f32 v116, v116\n\t"
        "v_smfmac_f32_16x16x64_bf16 v[128:131], v[144:147], v[104:111], v148\n\t"
        "v_exp_f32 v117, v117\n\t"
        "v_exp_f32 v118, v118\n\t"
        "v_mfma_f32_32x32x16_bf16 v[96:111], v[184:187], v[80:83], 0\n\t"
        "v_exp_f32 v119, v119\n\t"
        "v_exp_f32 v120, v120\n\t"
        "v_exp_f32 v121, v121\n\t"
        "v_mfma_f32_32x32x16_bf16 v[96:111], v[188:191], v[84:87], v[96:111]\n\t"
        "v_exp_f32 v122, v122\n\t"
        "v_exp_f32 v123, v123\n\t"
        "v_exp_f32 v124, v124\n\t"
        "v_smfmac_f32_32x32x32_bf16 v[16:31], v[192:195], v[112:119], v148\n\t"
        "v_exp_f32 v125, v125\n\t"
        "v_exp_f32 v126, v126\n\t"
        "v_exp_f32 v127, v127\n\t"
        "v_smfmac_f32_16x16x64_bf16 v[132:135], v[144:147], v[112:119], v148\n\t"
        "buffer_load_dwordx4 v[176:179], v150, s[40:43], s44 offen offset:2048 sc1\n\t"
        "v_exp_f32 v96, v96\n\t"
        "v_exp_f32 v97, v97\n\t"
        "v_smfmac_f32_32x32x32_bf16 v[16:31], v[196:199], v[120:127], v148\n\t"
        "v_exp_f32 v98, v98\n\t"
        "v_exp_f32 v99, v99\n\t"
        "v_exp_f32 v100, v100\n\t"
        "v_smfmac_f32_16x16x64_bf16 v[132:135], v[144:147], v[120:127], v148\n\t"
        "v_exp_f32 v101, v101\n\t"
        "v_exp_f32 v102, v102\n\t"
        "v_mfma_f32_32x32x16_bf16 v[112:127], v[184:187], v[88:91], 0\n\t"
        "v_exp_f32 v103, v103\n\t"
        "v_exp_f32 v104, v104\n\t"
        "v_exp_f32 v105, v105\n\t"
        "v_mfma_f32_32x32x16_bf16 v[112:127], v[188:191], v[92:95], v[112:127]\n\t"
        "v_exp_f32 v106, v106\n\t"
        "v_exp_f32 v107, v107\n\t"
        "v_exp_f32 v108, v108\n\t"
        "v_smfmac_f32_32x32x32_bf16 v[32:47], v[192:195], v[96:103], v148\n\t"
        "v_exp_f32 v109, v109\n\t"
        "v_exp_f32 v110, v110\n\t"
        "v_exp_f32 v111, v111\n\t"
        "v_smfmac_f32_16x16x64_bf16 v[136:139], v[144:147], v[96:103], v148\n\t"
        "buffer_load_dwordx4 v[180:183], v150, s[40:43], s44 offen offset:3072 sc1\n\t"
        "v_exp_f32 v112, v112\n\t"
        "v_exp_f32 v113, v113\n\t"
        "v_smfmac_f32_32x32x32_bf16 v[32:47], v[196:199], v[104:111], v148\n\t"
        "s_add_u32 s44, s44, 0x4000\n\t"
        "s_and_b32 s44, s44, 0x3ffff\n\t"
        "v_exp_f32 v114, v114\n\t"
        "v_exp_f32 v115, v115\n\t"
        "v_exp_f32 v116, v116\n\t"
        "v_smfmac_f32_16x16x64_bf16 v[136:139], v[144:147], v[104:111], v148\n\t"
        "s_waitcnt vmcnt(8)\n\t"
        "v_exp_f32 v117, v117\n\t"
        "v_exp_f32 v118, v118\n\t"
        "v_mfma_f32_32x32x16_bf16 v[96:111], v[200:203], v[64:67], 0\n\t"
        "v_exp_f32 v119, v119\n\t"
        "v_exp_f32 v120, v120\n\t"
        "v_exp_f32 v121, v121\n\t"
        "v_mfma_f32_32x32x16_bf16 v[96:111], v[204:207], v[68:71], v[96:111]\n\t"
        "v_exp_f32 v122, v122\n\t"
        "v_exp_f32 v123, v123\n\t"
        "v_exp_f32 v124, v124\n\t"
        "v_smfmac_f32_32x32x32_bf16 v[48:63], v[192:195], v[112:119], v148\n\t"
        "s_cmp_eq_u32 s44, s49\n\t"
        "s_cselect_b32 s44, s50, s44\n\t"
        "v_exp_f32 v125, v125\n\t"
        "v_exp_f32 v126, v126\n\t"
        "v_exp_f32 v127, v127\n\t"
        "v_smfmac_f32_16x16x64_bf16 v[140:143], v[144:147], v[112:119], v148\n\t"
        "buffer_load_dwordx4 v[184:187], v149, s[40:43], s44 offen offset:0 sc1\n\t"
        "v_exp_f32 v96, v96\n\t"
        "v_exp_f32 v97, v97\n\t"
        "v_smfmac_f32_32x32x32_bf16 v[48:63], v[196:199], v[120:127], v148\n\t"
        "v_exp_f32 v98, v98\n\t"
        "v_exp_f32 v99, v99\n\t"
        "v_exp_f32 v100, v100\n\t"
        "v_smfmac_f32_16x16x64_bf16 v[140:143], v[144:147], v[120:127], v148\n\t"
        "v_exp_f32 v101, v101\n\t"
        "v_exp_f32 v102, v102\n\t"
        "v_mfma_f32_32x32x16_bf16 v[112:127], v[200:203], v[72:75], 0\n\t"
        "v_exp_f32 v103, v103\n\t"
        "v_exp_f32 v104, v104\n\t"
        "v_exp_f32 v105, v105\n\t"
        "v_mfma_f32_32x32x16_bf16 v[112:127], v[204:207], v[76:79], v[112:127]\n\t"
        "v_exp_f32 v106, v106\n\t"
        "v_exp_f32 v107, v107\n\t"
        "v_exp_f32 v108, v108\n\t"
        "v_smfmac_f32_32x32x32_bf16 v[0:15], v[208:211], v[96:103], v148\n\t"
        "v_exp_f32 v109, v109\n\t"
        "v_exp_f32 v110, v110\n\t"
        "v_exp_f32 v111, v111\n\t"
        "v_smfmac_f32_16x16x64_bf16 v[128:131], v[144:147], v[96:103], v148\n\t"
        "buffer_load_dwordx4 v[188:191], v149, s[40:43], s44 offen offset:1024 sc1\n\t"
        "v_exp_f32 v112, v112\n\t"
        "v_exp_f32 v113, v113\n\t"
        "v_smfmac_f32_32x32x32_bf16 v[0:15], v[212:215], v[104:111], v148\n\t"
        "v_exp_f32 v114, v114\n\t"
        "v_exp_f32 v115, v115\n\t"
        "v_exp_f32 v116, v116\n\t"
        "v_smfmac_f32_16x16x64_bf16 v[128:131], v[144:147], v[104:111], v148\n\t"
        "v_exp_f32 v117, v117\n\t"
        "v_exp_f32 v118, v118\n\t"
        "v_mfma_f32_32x32x16_bf16 v[96:111], v[200:203], v[80:83], 0\n\t"
        "v_exp_f32 v119, v119\n\t"
        "v_exp_f32 v120, v120\n\t"
        "v_exp_f32 v121, v121\n\t"
        "v_mfma_f32_32x32x16_bf16 v[96:111], v[204:207], v[84:87], v[96:111]\n\t"
        "v_exp_f32 v122, v122\n\t"
        "v_exp_f32 v123, v123\n\t"
        "v_exp_f32 v124, v124\n\t"
        "v_smfmac_f32_32x32x32_bf16 v[16:31], v[208:211], v[112:119], v148\n\t"
        "v_exp_f32 v125, v125\n\t"
        "v_exp_f32 v126, v126\n\t"
        "v_exp_f32 v127, v127\n\t"
        "v_smfmac_f32_16x16x64_bf16 v[132:135], v[144:147], v[112:119], v148\n\t"
        "buffer_load_dwordx4 v[192:195], v149, s[40:43], s44 offen offset:2048 sc1\n\t"
        "v_exp_f32 v96, v96\n\t"
        "v_exp_f32 v97, v97\n\t"
        "v_smfmac_f32_32x32x32_bf16 v[16:31], v[212:215], v[120:127], v148\n\t"
        "v_exp_f32 v98, v98\n\t"
        "v_exp_f32 v99, v99\n\t"
        "v_exp_f32 v100, v100\n\t"
        "v_smfmac_f32_16x16x64_bf16 v[132:135], v[144:147], v[120:127], v148\n\t"
        "v_exp_f32 v101, v101\n\t"
        "v_exp_f32 v102, v102\n\t"
        "v_mfma_f32_32x32x16_bf16 v[112:127], v[200:203], v[88:91], 0\n\t"
        "v_exp_f32 v103, v103\n\t"
        "v_exp_f32 v104, v104\n\t"
        "v_exp_f32 v105, v105\n\t"
        "v_mfma_f32_32x32x16_bf16 v[112:127], v[204:207], v[92:95], v[112:127]\n\t"
        "v_exp_f32 v106, v106\n\t"
        "v_exp_f32 v107, v107\n\t"
        "v_exp_f32 v108, v108\n\t"
        "v_smfmac_f32_32x32x32_bf16 v[32:47], v[208:211], v[96:103], v148\n\t"
        "v_exp_f32 v109, v109\n\t"
        "v_exp_f32 v110, v110\n\t"
        "v_exp_f32 v111, v111\n\t"
        "v_smfmac_f32_16x16x64_bf16 v[136:139], v[144:147], v[96:103], v148\n\t"
        "buffer_load_dwordx4 v[196:199], v149, s[40:43], s44 offen offset:3072 sc1\n\t"
        "v_exp_f32 v112, v112\n\t"
        "v_exp_f32 v113, v113\n\t"
        "v_smfmac_f32_32x32x32_bf16 v[32:47], v[212:215], v[104:111], v148\n\t"
        "v_exp_f32 v114, v114\n\t"
        "v_exp_f32 v115, v115\n\t"
        "v_exp_f32 v116, v116\n\t"
        "v_smfmac_f32_16x16x64_bf16 v[136:139], v[144:147], v[104:111], v148\n\t"
        "s_waitcnt vmcnt(8)\n\t"
        "v_exp_f32 v117, v117\n\t"
        "v_exp_f32 v118, v118\n\t"
        "v_mfma_f32_32x32x16_bf16 v[96:111], v[152:155], v[64:67], 0\n\t"
        "v_exp_f32 v119, v119\n\t"
        "v_exp_f32 v120, v120\n\t"
        "v_exp_f32 v121, v121\n\t"
        "v_mfma_f32_32x32x16_bf16 v[96:111], v[156:159], v[68:71], v[96:111]\n\t"
        "v_exp_f32 v122, v122\n\t"
        "v_exp_f32 v123, v123\n\t"
        "v_exp_f32 v124, v124\n\t"
        "v_smfmac_f32_32x32x32_bf16 v[48:63], v[208:211], v[112:119], v148\n\t"
        "v_exp_f32 v125, v125\n\t"
        "v_exp_f32 v126, v126\n\t"
        "v_exp_f32 v127, v127\n\t"
        "v_smfmac_f32_16x16x64_bf16 v[140:143], v[144:147], v[112:119], v148\n\t"
        "buffer_load_dwordx4 v[200:203], v150, s[40:43], s44 offen offset:0 sc1\n\t"
        "v_exp_f32 v96, v96\n\t"
        "v_exp_f32 v97, v97\n\t"
        "v_smfmac_f32_32x32x32_bf16 v[48:63], v[212:215], v[120:127], v148\n\t"
        "v_exp_f32 v98, v98\n\t"
        "v_exp_f32 v99, v99\n\t"
        "v_exp_f32 v100, v100\n\t"
        "v_smfmac_f32_16x16x64_bf16 v[140:143], v[144:147], v[120:127], v148\n\t"
        "v_exp_f32 v101, v101\n\t"
        "v_exp_f32 v102, v102\n\t"
        "v_mfma_f32_32x32x16_bf16 v[112:127], v[152:155], v[72:75], 0\n\t"
        "v_exp_f32 v103, v103\n\t"
        "v_exp_f32 v104, v104\n\t"
        "v_exp_f32 v105, v105\n\t"
        "v_mfma_f32_32x32x16_bf16 v[112:127], v[156:159], v[76:79], v[112:127]\n\t"
        "v_exp_f32 v106, v106\n\t"
        "v_exp_f32 v107, v107\n\t"
        "v_exp_f32 v108, v108\n\t"
        "v_smfmac_f32_32x32x32_bf16 v[0:15], v[160:163], v[96:103], v148\n\t"
        "v_exp_f32 v109, v109\n\t"
        "v_exp_f32 v110, v110\n\t"
        "v_exp_f32 v111, v111\n\t"
        "v_smfmac_f32_16x16x64_bf16 v[128:131], v[144:147], v[96:103], v148\n\t"
        "buffer_load_dwordx4 v[204:207], v150, s[40:43], s44 offen offset:1024 sc1\n\t"
        "v_exp_f32 v112, v112\n\t"
        "v_exp_f32 v113, v113\n\t"
        "v_smfmac_f32_32x32x32_bf16 v[0:15], v[164:167], v[104:111], v148\n\t"
        "v_exp_f32 v114, v114\n\t"
        "v_exp_f32 v115, v115\n\t"
        "v_exp_f32 v116, v116\n\t"
        "v_smfmac_f32_16x16x64_bf16 v[128:131], v[144:147], v[104:111], v148\n\t"
        "v_exp_f32 v117, v117\n\t"
        "v_exp_f32 v118, v118\n\t"
        "v_mfma_f32_32x32x16_bf16 v[96:111], v[152:155], v[80:83], 0\n\t"
        "v_exp_f32 v119, v119\n\t"
        "v_exp_f32 v120, v120\n\t"
        "v_exp_f32 v121, v121\n\t"
        "v_mfma_f32_32x32x16_bf16 v[96:111], v[156:159], v[84:87], v[96:111]\n\t"
        "v_exp_f32 v122, v122\n\t"
        "v_exp_f32 v123, v123\n\t"
        "v_exp_f32 v124, v124\n\t"
        "v_smfmac_f32_32x32x32_bf16 v[16:31], v[160:163], v[112:119], v148\n\t"
        "v_exp_f32 v125, v125\n\t"
        "v_exp_f32 v126, v126\n\t"
        "v_exp_f32 v127, v127\n\t"
        "v_smfmac_f32_16x16x64_bf16 v[132:135], v[144:147], v[112:119], v148\n\t"
        "buffer_load_dwordx4 v[208:211], v150, s[40:43], s44 offen offset:2048 sc1\n\t"
        "v_exp_f32 v96, v96\n\t"
        "v_exp_f32 v97, v97\n\t"
        "v_smfmac_f32_32x32x32_bf16 v[16:31], v[164:167], v[120:127], v148\n\t"
        "v_exp_f32 v98, v98\n\t"
        "v_exp_f32 v99, v99\n\t"
        "v_exp_f32 v100, v100\n\t"
        "v_smfmac_f32_16x16x64_bf16 v[132:135], v[144:147], v[120:127], v148\n\t"
        "v_exp_f32 v101, v101\n\t"
        "v_exp_f32 v102, v102\n\t"
        "v_mfma_f32_32x32x16_bf16 v[112:127], v[152:155], v[88:91], 0\n\t"
        "v_exp_f32 v103, v103\n\t"
        "v_exp_f32 v104, v104\n\t"
        "v_exp_f32 v105, v105\n\t"
        "v_mfma_f32_32x32x16_bf16 v[112:127], v[156:159], v[92:95], v[112:127]\n\t"
        "v_exp_f32 v106, v106\n\t"
        "v_exp_f32 v107, v107\n\t"
        "v_exp_f32 v108, v108\n\t"
        "v_smfmac_f32_32x32x32_bf16 v[32:47], v[160:163], v[96:103], v148\n\t"
        "v_exp_f32 v109, v109\n\t"
        "v_exp_f32 v110, v110\n\t"
        "v_exp_f32 v111, v111\n\t"
        "v_smfmac_f32_16x16x64_bf16 v[136:139], v[144:147], v[96:103], v148\n\t"
        "buffer_load_dwordx4 v[212:215], v150, s[40:43], s44 offen offset:3072 sc1\n\t"
        "v_exp_f32 v112, v112\n\t"
        "v_exp_f32 v113, v113\n\t"
        "v_smfmac_f32_32x32x32_bf16 v[32:47], v[164:167], v[104:111], v148\n\t"
        "s_add_u32 s44, s44, 0x4000\n\t"
        "s_and_b32 s44, s44, 0x3ffff\n\t"
        "v_exp_f32 v114, v114\n\t"
        "v_exp_f32 v115, v115\n\t"
        "v_exp_f32 v116, v116\n\t"
        "v_smfmac_f32_16x16x64_bf16 v[136:139], v[144:147], v[104:111], v148\n\t"
        "s_waitcnt vmcnt(8)\n\t"
        "v_exp_f32 v117, v117\n\t"
        "v_exp_f32 v118, v118\n\t"
        "v_mfma_f32_32x32x16_bf16 v[96:111], v[168:171], v[64:67], 0\n\t"
        "v_exp_f32 v119, v119\n\t"
        "v_exp_f32 v120, v120\n\t"
        "v_exp_f32 v121, v121\n\t"
        "v_mfma_f32_32x32x16_bf16 v[96:111], v[172:175], v[68:71], v[96:111]\n\t"
        "v_exp_f32 v122, v122\n\t"
        "v_exp_f32 v123, v123\n\t"
        "v_exp_f32 v124, v124\n\t"
        "v_smfmac_f32_32x32x32_bf16 v[48:63], v[160:163], v[112:119], v148\n\t"
        "s_cmp_eq_u32 s44, s49\n\t"
        "s_cselect_b32 s44, s50, s44\n\t"
        "v_exp_f32 v125, v125\n\t"
        "v_exp_f32 v126, v126\n\t"
        "v_exp_f32 v127, v127\n\t"
        "v_smfmac_f32_16x16x64_bf16 v[140:143], v[144:147], v[112:119], v148\n\t"
        "buffer_load_dwordx4 v[152:155], v149, s[40:43], s44 offen offset:0 sc1\n\t"
        "v_exp_f32 v96, v96\n\t"
        "v_exp_f32 v97, v97\n\t"
        "v_smfmac_f32_32x32x32_bf16 v[48:63], v[164:167], v[120:127], v148\n\t"
        "v_exp_f32 v98, v98\n\t"
        "v_exp_f32 v99, v99\n\t"
        "v_exp_f32 v100, v100\n\t"
        "v_smfmac_f32_16x16x64_bf16 v[140:143], v[144:147], v[120:127], v148\n\t"
        "v_exp_f32 v101, v101\n\t"
        "v_exp_f32 v102, v102\n\t"
        "v_mfma_f32_32x32x16_bf16 v[112:127], v[168:171], v[72:75], 0\n\t"
        "v_exp_f32 v103, v103\n\t"
        "v_exp_f32 v104, v104\n\t"
        "v_exp_f32 v105, v105\n\t"
        "v_mfma_f32_32x32x16_bf16 v[112:127], v[172:175], v[76:79], v[112:127]\n\t"
        "v_exp_f32 v106, v106\n\t"
        "v_exp_f32 v107, v107\n\t"
        "v_exp_f32 v108, v108\n\t"
        "v_smfmac_f32_32x32x32_bf16 v[0:15], v[176:179], v[96:103], v148\n\t"
        "v_exp_f32 v109, v109\n\t"
        "v_exp_f32 v110, v110\n\t"
        "v_exp_f32 v111, v111\n\t"
        "v_smfmac_f32_16x16x64_bf16 v[128:131], v[144:147], v[96:103], v148\n\t"
        "buffer_load_dwordx4 v[156:159], v149, s[40:43], s44 offen offset:1024 sc1\n\t"
        "v_exp_f32 v112, v112\n\t"
        "v_exp_f32 v113, v113\n\t"
        "v_smfmac_f32_32x32x32_bf16 v[0:15], v[180:183], v[104:111], v148\n\t"
        "v_exp_f32 v114, v114\n\t"
        "v_exp_f32 v115, v115\n\t"
        "v_exp_f32 v116, v116\n\t"
        "v_smfmac_f32_16x16x64_bf16 v[128:131], v[144:147], v[104:111], v148\n\t"
        "v_exp_f32 v117, v117\n\t"
        "v_exp_f32 v118, v118\n\t"
        "v_mfma_f32_32x32x16_bf16 v[96:111], v[168:171], v[80:83], 0\n\t"
        "v_exp_f32 v119, v119\n\t"
        "v_exp_f32 v120, v120\n\t"
        "v_exp_f32 v121, v121\n\t"
        "v_mfma_f32_32x32x16_bf16 v[96:111], v[172:175], v[84:87], v[96:111]\n\t"
        "v_exp_f32 v122, v122\n\t"
        "v_exp_f32 v123, v123\n\t"
        "v_exp_f32 v124, v124\n\t"
        "v_smfmac_f32_32x32x32_bf16 v[16:31], v[176:179], v[112:119], v148\n\t"
        "v_exp_f32 v125, v125\n\t"
        "v_exp_f32 v126, v126\n\t"
        "v_exp_f32 v127, v127\n\t"
        "v_smfmac_f32_16x16x64_bf16 v[132:135], v[144:147], v[112:119], v148\n\t"
        "buffer_load_dwordx4 v[160:163], v149, s[40:43], s44 offen offset:2048 sc1\n\t"
        "v_exp_f32 v96, v96\n\t"
        "v_exp_f32 v97, v97\n\t"
        "v_smfmac_f32_32x32x32_bf16 v[16:31], v[180:183], v[120:127], v148\n\t"
        "v_exp_f32 v98, v98\n\t"
        "v_exp_f32 v99, v99\n\t"
        "v_exp_f32 v100, v100\n\t"
        "v_smfmac_f32_16x16x64_bf16 v[132:135], v[144:147], v[120:127], v148\n\t"
        "v_exp_f32 v101, v101\n\t"
        "v_exp_f32 v102, v102\n\t"
        "v_mfma_f32_32x32x16_bf16 v[112:127], v[168:171], v[88:91], 0\n\t"
        "v_exp_f32 v103, v103\n\t"
        "v_exp_f32 v104, v104\n\t"
        "v_exp_f32 v105, v105\n\t"
        "v_mfma_f32_32x32x16_bf16 v[112:127], v[172:175], v[92:95], v[112:127]\n\t"
        "v_exp_f32 v106, v106\n\t"
        "v_exp_f32 v107, v107\n\t"
        "v_exp_f32 v108, v108\n\t"
        "v_smfmac_f32_32x32x32_bf16 v[32:47], v[176:179], v[96:103], v148\n\t"
        "s_sub_u32 s45, s45, 1\n\t"
        "v_exp_f32 v109, v109\n\t"
        "v_exp_f32 v110, v110\n\t"
        "v_exp_f32 v111, v111\n\t"
        "v_smfmac_f32_16x16x64_bf16 v[136:139], v[144:147], v[96:103], v148\n\t"
        "buffer_load_dwordx4 v[164:167], v149, s[40:43], s44 offen offset:3072 sc1\n\t"
        "s_cbranch_scc0 1b\n\t"
        "v_exp_f32 v112, v112\n\t"
        "v_exp_f32 v113, v113\n\t"
        "v_smfmac_f32_32x32x32_bf16 v[32:47], v[180:183], v[104:111], v148\n\t"
        "v_exp_f32 v114, v114\n\t"
        "v_exp_f32 v115, v115\n\t"
        "v_exp_f32 v116, v116\n\t"
        "v_smfmac_f32_16x16x64_bf16 v[136:139], v[144:147], v[104:111], v148\n\t"
        "s_waitcnt vmcnt(8)\n\t"
        "v_exp_f32 v117, v117\n\t"
        "v_exp_f32 v118, v118\n\t"
        "v_mfma_f32_32x32x16_bf16 v[96:111], v[184:187], v[64:67], 0\n\t"
        "v_exp_f32 v119, v119\n\t"
        "v_exp_f32 v120, v120\n\t"
        "v_exp_f32 v121, v121\n\t"
        "v_mfma_f32_32x32x16_bf16 v[96:111], v[188:191], v[68:71], v[96:111]\n\t"
        "v_exp_f32 v122, v122\n\t"
        "v_exp_f32 v123, v123\n\t"
        "v_exp_f32 v124, v124\n\t"
        "v_smfmac_f32_32x32x32_bf16 v[48:63], v[176:179], v[112:119], v148\n\t"
        "v_exp_f32 v125, v125\n\t"
        "v_exp_f32 v126, v126\n\t"
        "v_exp_f32 v127, v127\n\t"
        "v_smfmac_f32_16x16x64_bf16 v[140:143], v[144:147], v[112:119], v148\n\t"
        "v_exp_f32 v96, v96\n\t"
        "v_exp_f32 v97, v97\n\t"
        "v_smfmac_f32_32x32x32_bf16 v[48:63], v[180:183], v[120:127], v148\n\t"
        "v_exp_f32 v98, v98\n\t"
        "v_exp_f32 v99, v99\n\t"
        "v_exp_f32 v100, v100\n\t"
        "v_smfmac_f32_16x16x64_bf16 v[140:143], v[144:147], v[120:127], v148\n\t"
        "v_exp_f32 v101, v101\n\t"
        "v_exp_f32 v102, v102\n\t"
        "v_mfma_f32_32x32x16_bf16 v[112:127], v[184:187], v[72:75], 0\n\t"
        "v_exp_f32 v103, v103\n\t"
        "v_exp_f32 v104, v104\n\t"
        "v_exp_f32 v105, v105\n\t"
        "v_mfma_f32_32x32x16_bf16 v[112:127], v[188:191], v[76:79], v[112:127]\n\t"
        "v_exp_f32 v106, v106\n\t"
        "v_exp_f32 v107, v107\n\t"
        "v_exp_f32 v108, v108\n\t"
        "v_smfmac_f32_32x32x32_bf16 v[0:15], v[192:195], v[96:103], v148\n\t"
        "v_exp_f32 v109, v109\n\t"
        "v_exp_f32 v110, v110\n\t"
        "v_exp_f32 v111, v111\n\t"
        "v_smfmac_f32_16x16x64_bf16 v[128:131], v[144:147], v[96:103], v148\n\t"
        "v_exp_f32 v112, v112\n\t"
        "v_exp_f32 v113, v113\n\t"
        "v_smfmac_f32_32x32x32_bf16 v[0:15], v[196:199], v[104:111], v148\n\t"
        "v_exp_f32 v114, v114\n\t"
        "v_exp_f32 v115, v115\n\t"
        "v_exp_f32 v116, v116\n\t"
        "v_smfmac_f32_16x16x64_bf16 v[128:131], v[144:147], v[104:111], v148\n\t"
        "s_waitcnt vmcnt(4)\n\t"
        "v_exp_f32 v117, v117\n\t"
        "v_exp_f32 v118, v118\n\t"
        "v_mfma_f32_32x32x16_bf16 v[96:111], v[200:203], v[64:67], 0\n\t"
        "v_exp_f32 v119, v119\n\t"
        "v_exp_f32 v120, v120\n\t"
        "v_exp_f32 v121, v121\n\t"
        "v_mfma_f32_32x32x16_bf16 v[96:111], v[204:207], v[68:71], v[96:111]\n\t"
        "v_exp_f32 v122, v122\n\t"
        "v_exp_f32 v123, v123\n\t"
        "v_exp_f32 v124, v124\n\t"
        "v_smfmac_f32_32x32x32_bf16 v[16:31], v[192:195], v[112:119], v148\n\t"
        "v_exp_f32 v125, v125\n\t"
        "v_exp_f32 v126, v126\n\t"
        "v_exp_f32 v127, v127\n\t"
        "v_smfmac_f32_16x16x64_bf16 v[132:135], v[144:147], v[112:119], v148\n\t"
        "s_cmp_lg_u32 s47, 0\n\t"
        "s_cbranch_scc1 4f\n\t"
        "global_load_dwordx4 %[r0], v223, %[xb] offset:0\n\t"
        "global_load_dwordx4 %[r1], v223, %[xb] offset:32\n\t"
        "global_load_dwordx4 %[r2], v223, %[xb] offset:64\n\t"
        "global_load_dwordx4 %[r3], v223, %[xb] offset:96\n\t"
        "4:\n\t"
        "v_exp_f32 v96, v96\n\t"
        "v_exp_f32 v97, v97\n\t"
        "v_smfmac_f32_32x32x32_bf16 v[16:31], v[196:199], v[120:127], v148\n\t"
        "v_exp_f32 v98, v98\n\t"
        "v_exp_f32 v99, v99\n\t"
        "v_exp_f32 v100, v100\n\t"
        "v_smfmac_f32_16x16x64_bf16 v[132:135], v[144:147], v[120:127], v148\n\t"
        "v_exp_f32 v101, v101\n\t"
        "v_exp_f32 v102, v102\n\t"
        "v_mfma_f32_32x32x16_bf16 v[112:127], v[200:203], v[72:75], 0\n\t"
        "v_exp_f32 v103, v103\n\t"
        "v_exp_f32 v104, v104\n\t"
        "v_exp_f32 v105, v105\n\t"
        "v_mfma_f32_32x32x16_bf16 v[112:127], v[204:207], v[76:79], v[112:127]\n\t"
        "v_exp_f32 v106, v106\n\t"
        "v_exp_f32 v107, v107\n\t"
        "v_exp_f32 v108, v108\n\t"
        "v_smfmac_f32_32x32x32_bf16 v[0:15], v[208:211], v[96:103], v148\n\t"
        "v_exp_f32 v109, v109\n\t"
        "v_exp_f32 v110, v110\n\t"
        "v_exp_f32 v111, v111\n\t"
        "v_smfmac_f32_16x16x64_bf16 v[128:131], v[144:147], v[96:103], v148\n\t"
        "v_exp_f32 v112, v112\n\t"
        "v_exp_f32 v113, v113\n\t"
        "v_smfmac_f32_32x32x32_bf16 v[0:15], v[212:215], v[104:111], v148\n\t"
        "v_exp_f32 v114, v114\n\t"
        "v_exp_f32 v115, v115\n\t"
        "v_exp_f32 v116, v116\n\t"
        "v_smfmac_f32_16x16x64_bf16 v[128:131], v[144:147], v[104:111], v148\n\t"
        "v_exp_f32 v117, v117\n\t"
        "v_exp_f32 v118, v118\n\t"
        "v_mfma_f32_32x32x16_bf16 v[96:111], v[184:187], v[80:83], 0\n\t"
        "v_exp_f32 v119, v119\n\t"
        "v_exp_f32 v120, v120\n\t"
        "v_exp_f32 v121, v121\n\t"
        "v_mfma_f32_32x32x16_bf16 v[96:111], v[188:191], v[84:87], v[96:111]\n\t"
        "ds_write_b128 v216, v[0:3] offset:0\n\t"
        "ds_write_b128 v216, v[4:7] offset:1024\n\t"
        "v_exp_f32 v122, v122\n\t"
        "v_exp_f32 v123, v123\n\t"
        "v_exp_f32 v124, v124\n\t"
        "v_smfmac_f32_32x32x32_bf16 v[16:31], v[208:211], v[112:119], v148\n\t"
        "ds_write_b128 v216, v[8:11] offset:2048\n\t"
        "ds_write_b128 v216, v[12:15] offset:3072\n\t"
        "v_exp_f32 v125, v125\n\t"
        "v_exp_f32 v126, v126\n\t"
        "v_exp_f32 v127, v127\n\t"
        "v_smfmac_f32_16x16x64_bf16 v[132:135], v[144:147], v[112:119], v148\n\t"
        "ds_write_b32 v218, v128 offset:0\n\t"
        "v_exp_f32 v96, v96\n\t"
        "v_exp_f32 v97, v97\n\t"
        "v_smfmac_f32_32x32x32_bf16 v[16:31], v[212:215], v[120:127], v148\n\t"
        "v_exp_f32 v98, v98\n\t"
        "v_exp_f32 v99, v99\n\t"
        "v_exp_f32 v100, v100\n\t"
        "v_smfmac_f32_16x16x64_bf16 v[132:135], v[144:147], v[120:127], v148\n\t"
        "v_exp_f32 v101, v101\n\t"
        "v_exp_f32 v102, v102\n\t"
        "v_mfma_f32_32x32x16_bf16 v[112:127], v[184:187], v[88:91], 0\n\t"
        "v_exp_f32 v103, v103\n\t"
        "v_exp_f32 v104, v104\n\t"
        "v_exp_f32 v105, v105\n\t"
        "v_mfma_f32_32x32x16_bf16 v[112:127], v[188:191], v[92:95], v[112:127]\n\t"
        "ds_write_b128 v216, v[16:19] offset:4096\n\t"
        "ds_write_b128 v216, v[20:23] offset:5120\n\t"
        "v_exp_f32 v106, v106\n\t"
        "v_exp_f32 v107, v107\n\t"
        "v_exp_f32 v108, v108\n\t"
        "v_smfmac_f32_32x32x32_bf16 v[32:47], v[192:195], v[96:103], v148\n\t"
        "ds_write_b128 v216, v[24:27] offset:6144\n\t"
        "ds_write_b128 v216, v[28:31] offset:7168\n\t"
        "v_exp_f32 v109, v109\n\t"
        "v_exp_f32 v110, v110\n\t"
        "v_exp_f32 v111, v111\n\t"
        "v_smfmac_f32_16x16x64_bf16 v[136:139], v[144:147], v[96:103], v148\n\t"
        "ds_write_b32 v218, v132 offset:256\n\t"
        "ds_write_b32 v220, v222\n\t"
        "v_exp_f32 v112, v112\n\t"
        "v_exp_f32 v113, v113\n\t"
        "v_smfmac_f32_32x32x32_bf16 v[32:47], v[196:199], v[104:111], v148\n\t"
        "v_exp_f32 v114, v114\n\t"
        "v_exp_f32 v115, v115\n\t"
        "v_exp_f32 v116, v116\n\t"
        "v_smfmac_f32_16x16x64_bf16 v[136:139], v[144:147], v[104:111], v148\n\t"
        "v_exp_f32 v117, v117\n\t"
        "v_exp_f32 v118, v118\n\t"
        "v_mfma_f32_32x32x16_bf16 v[96:111], v[200:203], v[80:83], 0\n\t"
        "v_exp_f32 v119, v119\n\t"
        "v_exp_f32 v120, v120\n\t"
        "v_exp_f32 v121, v121\n\t"
        "v_mfma_f32_32x32x16_bf16 v[96:111], v[204:207], v[84:87], v[96:111]\n\t"
        "v_exp_f32 v122, v122\n\t"
        "v_exp_f32 v123, v123\n\t"
        "v_exp_f32 v124, v124\n\t"
        "v_smfmac_f32_32x32x32_bf16 v[48:63], v[192:195], v[112:119], v148\n\t"
        "v_exp_f32 v125, v125\n\t"
        "v_exp_f32 v126, v126\n\t"
        "v_exp_f32 v127, v127\n\t"
        "v_smfmac_f32_16x16x64_bf16 v[140:143], v[144:147], v[112:119], v148\n\t"
        "v_exp_f32 v96, v96\n\t"
        "v_exp_f32 v97, v97\n\t"
        "v_smfmac_f32_32x32x32_bf16 v[48:63], v[196:199], v[120:127], v148\n\t"
        "v_exp_f32 v98, v98\n\t"
        "v_exp_f32 v99, v99\n\t"
        "v_exp_f32 v100, v100\n\t"
        "v_smfmac_f32_16x16x64_bf16 v[140:143], v[144:147], v[120:127], v148\n\t"
        "v_exp_f32 v101, v101\n\t"
        "v_exp_f32 v102, v102\n\t"
        "v_mfma_f32_32x32x16_bf16 v[112:127], v[200:203], v[88:91], 0\n\t"
        "v_exp_f32 v103, v103\n\t"
        "v_exp_f32 v104, v104\n\t"
        "v_exp_f32 v105, v105\n\t"
        "v_mfma_f32_32x32x16_bf16 v[112:127], v[204:207], v[92:95], v[112:127]\n\t"
        "ds_read_b32 v151, v221\n\t"
        "v_exp_f32 v106, v106\n\t"
        "v_exp_f32 v107, v107\n\t"
        "v_exp_f32 v108, v108\n\t"
        "v_smfmac_f32_32x32x32_bf16 v[32:47], v[208:211], v[96:103], v148\n\t"
        "v_exp_f32 v109, v109\n\t"
        "v_exp_f32 v110, v110\n\t"
        "v_exp_f32 v111, v111\n\t"
        "v_smfmac_f32_16x16x64_bf16 v[136:139], v[144:147], v[96:103], v148\n\t"
        "s_waitcnt lgkmcnt(0)\n\t"
        "v_cmp_ne_u32 vcc, v222, v151\n\t"
        "s_cbranch_vccnz 40f\n\t"
        "41:\n\t"
        "v_exp_f32 v112, v112\n\t"
        "v_exp_f32 v113, v113\n\t"
        "v_smfmac_f32_32x32x32_bf16 v[32:47], v[212:215], v[104:111], v148\n\t"
        "ds_read_b128 v[0:3], v217 offset:0\n\t"
        "ds_read_b128 v[4:7], v217 offset:1024\n\t"
        "ds_read_b128 v[8:11], v217 offset:2048\n\t"
        "ds_read_b128 v[12:15], v217 offset:3072\n\t"
        "v_exp_f32 v114, v114\n\t"
        "v_exp_f32 v115, v115\n\t"
        "v_exp_f32 v116, v116\n\t"
        "v_smfmac_f32_16x16x64_bf16 v[136:139], v[144:147], v[104:111], v148\n\t"
        "ds_read_b32 v128, v219 offset:0\n\t"
        "ds_read_b128 v[16:19], v217 offset:4096\n\t"
        "ds_read_b128 v[20:23], v217 offset:5120\n\t"
        "v_exp_f32 v117, v117\n\t"
        "v_exp_f32 v118, v118\n\t"
        "v_exp_f32 v119, v119\n\t"
        "v_exp_f32 v120, v120\n\t"
        "v_exp_f32 v121, v121\n\t"
        "v_exp_f32 v122, v122\n\t"
        "v_exp_f32 v123, v123\n\t"
        "v_exp_f32 v124, v124\n\t"
        "v_smfmac_f32_32x32x32_bf16 v[48:63], v[208:211], v[112:119], v148\n\t"
        "ds_read_b128 v[24:27], v217 offset:6144\n\t"
        "ds_read_b128 v[28:31], v217 offset:7168\n\t"
        "ds_read_b32 v132, v219 offset:256\n\t"
        "v_exp_f32 v125, v125\n\t"
        "v_exp_f32 v126, v126\n\t"
        "v_exp_f32 v127, v127\n\t"
        "v_smfmac_f32_16x16x64_bf16 v[140:143], v[144:147], v[112:119], v148\n\t"
        "s_nop 1\n\t"
        "v_smfmac_f32_32x32x32_bf16 v[48:63], v[212:215], v[120:127], v148\n\t"
        "v_smfmac_f32_16x16x64_bf16 v[140:143], v[144:147], v[120:127], v148\n\t"
        "s_waitcnt vmcnt(0) lgkmcnt(0)\n\t"
        "s_nop 6\n\t"
        "s_branch 9f\n\t"
        "24:\n\t"
        "s_add_u32 s44, s44, 0x4000\n\t"
        "s_and_b32 s44, s44, 0x3ffff\n\t"
        "s_cmp_eq_u32 s44, s49\n\t"
        "s_cselect_b32 s44, s50, s44\n\t"
        "buffer_load_dwordx4 v[184:187], v149, s[40:43], s44 offen offset:0 sc1\n\t"
        "buffer_load_dwordx4 v[188:191], v149, s[40:43], s44 offen offset:1024 sc1\n\t"
        "buffer_load_dwordx4 v[192:195], v149, s[40:43], s44 offen offset:2048 sc1\n\t"
        "buffer_load_dwordx4 v[196:199], v149, s[40:43], s44 offen offset:3072 sc1\n\t"
        "buffer_load_dwordx4 v[200:203], v150, s[40:43], s44 offen offset:0 sc1\n\t"
        "buffer_load_dwordx4 v[204:207], v150, s[40:43], s44 offen offset:1024 sc1\n\t"
        "buffer_load_dwordx4 v[208:211], v150, s[40:43], s44 offen offset:2048 sc1\n\t"
        "buffer_load_dwordx4 v[212:215], v150, s[40:43], s44 offen offset:3072 sc1\n\t"
        "s_mov_b32 s46, 1\n\t"
        "s_branch 25b\n\t"
        "26:\n\t"
        "s_add_u32 s44, s44, 0x4000\n\t"
        "s_and_b32 s44, s44, 0x3ffff\n\t"
        "s_cmp_eq_u32 s44, s49\n\t"
        "s_cselect_b32 s44, s50, s44\n\t"
        "buffer_load_dwordx4 v[184:187], v149, s[40:43], s44 offen offset:0 sc1\n\t"
        "buffer_load_dwordx4 v[188:191], v149, s[40:43], s44 offen offset:1024 sc1\n\t"
        "buffer_load_dwordx4 v[192:195], v149, s[40:43], s44 offen offset:2048 sc1\n\t"
        "buffer_load_dwordx4 v[196:199], v149, s[40:43], s44 offen offset:3072 sc1\n\t"
        "buffer_load_dwordx4 v[200:203], v150, s[40:43], s44 offen offset:0 sc1\n\t"
        "buffer_load_dwordx4 v[204:207], v150, s[40:43], s44 offen offset:1024 sc1\n\t"
        "buffer_load_dwordx4 v[208:211], v150, s[40:43], s44 offen offset:2048 sc1\n\t"
        "buffer_load_dwordx4 v[212:215], v150, s[40:43], s44 offen offset:3072 sc1\n\t"
        "s_mov_b32 s46, 1\n\t"
        "s_branch 27b\n\t"
        "28:\n\t"
        "s_add_u32 s44, s44, 0x4000\n\t"
        "s_and_b32 s44, s44, 0x3ffff\n\t"
        "s_cmp_eq_u32 s44, s49\n\t"
        "s_cselect_b32 s44, s50, s44\n\t"
        "buffer_load_dwordx4 v[184:187], v149, s[40:43], s44 offen offset:0 sc1\n\t"
        "buffer_load_dwordx4 v[188:191], v149, s[40:43], s44 offen offset:1024 sc1\n\t"
        "buffer_load_dwordx4 v[192:195], v149, s[40:43], s44 offen offset:2048 sc1\n\t"
        "buffer_load_dwordx4 v[196:199], v149, s[40:43], s44 offen offset:3072 sc1\n\t"
        "buffer_load_dwordx4 v[200:203], v150, s[40:43], s44 offen offset:0 sc1\n\t"
        "buffer_load_dwordx4 v[204:207], v150, s[40:43], s44 offen offset:1024 sc1\n\t"
        "buffer_load_dwordx4 v[208:211], v150, s[40:43], s44 offen offset:2048 sc1\n\t"
        "buffer_load_dwordx4 v[212:215], v150, s[40:43], s44 offen offset:3072 sc1\n\t"
        "s_mov_b32 s46, 1\n\t"
        "s_branch 29b\n\t"
        "30:\n\t"
        "s_add_u32 s44, s44, 0x4000\n\t"
        "s_and_b32 s44, s44, 0x3ffff\n\t"
        "s_cmp_eq_u32 s44, s49\n\t"
        "s_cselect_b32 s44, s50, s44\n\t"
        "buffer_load_dwordx4 v[184:187], v149, s[40:43], s44 offen offset:0 sc1\n\t"
        "buffer_load_dwordx4 v[188:191], v149, s[40:43], s44 offen offset:1024 sc1\n\t"
        "buffer_load_dwordx4 v[192:195], v149, s[40:43], s44 offen offset:2048 sc1\n\t"
        "buffer_load_dwordx4 v[196:199], v149, s[40:43], s44 offen offset:3072 sc1\n\t"
        "buffer_load_dwordx4 v[200:203], v150, s[40:43], s44 offen offset:0 sc1\n\t"
        "buffer_load_dwordx4 v[204:207], v150, s[40:43], s44 offen offset:1024 sc1\n\t"
        "buffer_load_dwordx4 v[208:211], v150, s[40:43], s44 offen offset:2048 sc1\n\t"
        "buffer_load_dwordx4 v[212:215], v150, s[40:43], s44 offen offset:3072 sc1\n\t"
        "s_mov_b32 s46, 1\n\t"
        "s_branch 31b\n\t"
        "32:\n\t"
        "s_add_u32 s44, s44, 0x4000\n\t"
        "s_and_b32 s44, s44, 0x3ffff\n\t"
        "s_cmp_eq_u32 s44, s49\n\t"
        "s_cselect_b32 s44, s50, s44\n\t"
        "buffer_load_dwordx4 v[184:187], v149, s[40:43], s44 offen offset:0 sc1\n\t"
        "buffer_load_dwordx4 v[188:191], v149, s[40:43], s44 offen offset:1024 sc1\n\t"
        "buffer_load_dwordx4 v[192:195], v149, s[40:43], s44 offen offset:2048 sc1\n\t"
        "buffer_load_dwordx4 v[196:199], v149, s[40:43], s44 offen offset:3072 sc1\n\t"
        "buffer_load_dwordx4 v[200:203], v150, s[40:43], s44 offen offset:0 sc1\n\t"
        "buffer_load_dwordx4 v[204:207], v150, s[40:43], s44 offen offset:1024 sc1\n\t"
        "buffer_load_dwordx4 v[208:211], v150, s[40:43], s44 offen offset:2048 sc1\n\t"
        "buffer_load_dwordx4 v[212:215], v150, s[40:43], s44 offen offset:3072 sc1\n\t"
        "s_mov_b32 s46, 1\n\t"
        "s_branch 33b\n\t"
        "40:\n\t"
        "s_mov_b32 s45, 0x100000\n\t"
        "42:\n\t"
        "ds_read_b32 v151, v221\n\t"
        "s_sub_u32 s45, s45, 1\n\t"
        "s_waitcnt lgkmcnt(0)\n\t"
        "s_cbranch_scc1 41b\n\t"
        "v_cmp_ne_u32 vcc, v222, v151\n\t"
        "s_cbranch_vccnz 42b\n\t"
        "s_branch 41b\n\t"
        "9:\n\t"
        : "+{v[0:15]}"(o0), "+{v[16:31]}"(o1), "+{v[32:47]}"(o2), "+{v[48:63]}"(o3), "+{v[128:143]}"(lv), "+{s44}"(soff), "+{v[152:167]}"(kva), "+{v[168:183]}"(kvb), "+{v[184:191]}"(fv),
          [r0] "+v"(x1p[0]), [r1] "+v"(x1p[1]), [r2] "+v"(x1p[2]), [r3] "+v"(x1p[3]) STAMP_OUTS
        : "{v[64:71]}"(q[0]), "{v[72:79]}"(q[1]), "{v[80:87]}"(q[2]), "{v[88:95]}"(q[3]), "{v[144:147]}"(ones), "{v148}"(0xddddu), "{v149}"(kvoff), "{v150}"(kvoff + 4096),
          "{v[216:223]}"(cv), "{v224}"(lbase + L_W + 16), "{s[40:43]}"(srd), "{s47}"(L), "{s49}"(tm * 16384), "{s50}"(((tm + 1) & 15) * 16384), [xb] "s"(x0)
        : "v96", "v97", "v98", "v99", "v100", "v101", "v102", "v103", "v104", "v105", "v106", "v107", "v108", "v109", "v110", "v111", "v112", "v113", "v114", "v115", "v116", "v117", "v118", "v119", "v120", "v121", "v122", "v123", "v124", "v125", "v126", "v127", "v151", "v192", "v193", "v194", "v195", "v196", "v197", "v198", "v199", "v200", "v201", "v202", "v203", "v204", "v205", "v206", "v207", "v208", "v209", "v210", "v211", "v212", "v213", "v214", "v215", "s45", "s46", "vcc", "scc", "memory");
      STAMP_PUT(6 + 16 * L);
      STAMPV(9 + 16 * L);
      PHASE_VARS;
      const unsigned dst = OFF_OF + L * (5 * MBY) + (tm * 4 + 2 * kh) * TS + (tn * 2 + hh) * 2048 + lane * 16;
      auto put = [&](int u, const f32x16& o) {
#pragma unroll
        for (int c = 0; c < 2; ++c) {
          u32x4 w;
#pragma unroll
          for (int e = 0; e < 4; ++e) w[e] = pack2(o[8 * c + 2 * e], o[8 * c + 2 * e + 1]);
          stg(rs, dst + u * TS + c * 1024, w);
        }
      };
      const float la = lv[8] + lv[0], lb = lv[12] + lv[4];
      o2 = (o2 + o0) * RCP(la); put(0, o2);
      SBAR();
      o3 = (o3 + o1) * RCP(lb); put(1, o3);
      SBAR();
      {
        const f32x16 t = o2 + o3;
        float sum = 0.f;
#pragma unroll
        for (int e = 0; e < 16; e += 4) sum += (t[e] + t[e + 1]) + (t[e + 2] + t[e + 3]);
        if (__any(!(fabsf(sum) < 1e37f) | !(fmaxf(la, lb) < 1e27f) | !(fminf(la, lb) > 1e-9f) | !(la + lb == la + lb))) {
          DRAIN();
          attn_safe(rs, kvoff - kh * 8192, q[2], ones, o2); put(0, o2);
          attn_safe(rs, kvoff - kh * 8192, q[3], ones, o3); put(1, o3);
        }
      }
    }
    STAMPV(10 + 16 * L);
    {
      f32x16 acc[8];
      u32x2 wrc[8];
      STAMP_DECL;
      { PHASE_VARS;
      float r0 = 0.f, r1 = 1.f;
      if (L == 0) {
#pragma unroll
        for (int i = 0; i < 4; ++i) x1p[i] = fo_swap(x1p[i]);
      } else {
        r0 = reinterpret_cast<const float*>(smem + L_AB)[256 + wid * 64 + r32]; r1 = reinterpret_cast<const float*>(smem + L_AB)[256 + wid * 64 + 32 + r32];
      }
#pragma unroll
      for (int e = 0; e < 16; ++e) { acc[e >> 3][2 * (e & 7)] = lo16(x1p[e >> 2][e & 3]) * r1 + r0; acc[e >> 3][2 * (e & 7) + 1] = hi16(x1p[e >> 2][e & 3]) * r1 + r0; }
      PIN(acc[0]); PIN(acc[1]);
      asm volatile("s_waitcnt vmcnt(0)\n\tds_write_b32 %0, %1" :: "v"(lbase + L_W + 80 + 4 * wid), "v"(2 * L + 2) : "memory");
      STAMPV(11 + 16 * L);
      const unsigned a0 = lbase + wrow * 128 + ((wch ^ wsw) << 4) + 8 * (tp & 1), a1 = lbase + wrow * 128 + (((4 + wch) ^ wsw) << 4) + 8 * (tp & 1);
      asm volatile(
        "s_mov_b32 s46, 0\n\t"
        "ostart_%=:\n\t"
        "s_mov_b32 s48, 0x200000\n\t"
        "2:\n\t"
        "ds_read_b32 v233, v232\n\t"
        "s_sub_u32 s48, s48, 1\n\t"
        "s_waitcnt lgkmcnt(0)\n\t"
        "s_cselect_b32 s47, 15, 0\n\t"
        "v_readfirstlane_b32 s51, v233\n\t"
        "s_nop 3\n\t"
        "s_or_b32 s51, s51, s47\n\t"
        "s_andn2_b32 s47, s51, s46\n\t"
        "s_cbranch_scc0 2b\n\t"
        "s_andn2_b32 s51, s51, s46\n\t"
        "s_ff1_i32_b32 s47, s51\n\t"
        "s_add_u32 s45, s47, s49\n\t"
        "s_and_b32 s45, s45, 3\n\t"
        "s_lshl_b32 s45, s45, 14\n\t"
        "s_and_b32 s51, s47, 0x80000000\n\t"
        "v_or_b32 v225, s51, v224\n\t"
        "s_and_b32 s51, s47, 3\n\t"
        "s_lshl_b32 s51, s51, 15\n\t"
        "v_add_u32 v230, s51, v226\n\t"
        "v_add_u32 v231, s51, v227\n\t"
        "buffer_load_dwordx4 v[160:163], v225, s[40:43], s45 offen offset:0 sc1\n\t"
        "buffer_load_dwordx4 v[164:167], v225, s[40:43], s45 offen offset:1024 sc1\n\t"
        "buffer_load_dwordx4 v[168:171], v225, s[40:43], s45 offen offset:2048 sc1\n\t"
        "buffer_load_dwordx4 v[172:175], v225, s[40:43], s45 offen offset:3072 sc1\n\t"
        "s_add_u32 s45, s45, 0x1000\n\t"
        "buffer_load_dwordx4 v[176:179], v225, s[40:43], s45 offen offset:0 sc1\n\t"
        "buffer_load_dwordx4 v[180:183], v225, s[40:43], s45 offen offset:1024 sc1\n\t"
        "buffer_load_dwordx4 v[184:187], v225, s[40:43], s45 offen offset:2048 sc1\n\t"
        "buffer_load_dwordx4 v[188:191], v225, s[40:43], s45 offen offset:3072 sc1\n\t"
        "s_add_u32 s45, s45, 0x1000\n\t"
        "buffer_load_dwordx4 v[192:195], v225, s[40:43], s45 offen offset:0 sc1\n\t"
        "buffer_load_dwordx4 v[196:199], v225, s[40:43], s45 offen offset:1024 sc1\n\t"
        "buffer_load_dwordx4 v[200:203], v225, s[40:43], s45 offen offset:2048 sc1\n\t"
        "buffer_load_dwordx4 v[204:207], v225, s[40:43], s45 offen offset:3072 sc1\n\t"
        "ds_read_b64_tr_b16 v[128:129], v230 offset:0\n\t"
        "ds_read_b64_tr_b16 v[130:131], v230 offset:1024\n\t"
        "ds_read_b64_tr_b16 v[132:133], v231 offset:0\n\t"
        "ds_read_b64_tr_b16 v[134:135], v231 offset:1024\n\t"
        "ds_read_b64_tr_b16 v[136:137], v230 offset:2048\n\t"
        "ds_read_b64_tr_b16 v[138:139], v230 offset:3072\n\t"
        "ds_read_b64_tr_b16 v[140:141], v231 offset:2048\n\t"
        "ds_read_b64_tr_b16 v[142:143], v231 offset:3072\n\t"
        "ds_read_b64_tr_b16 v[144:145], v230 offset:4096\n\t"
        "ds_read_b64_tr_b16 v[146:147], v230 offset:5120\n\t"
        "ds_read_b64_tr_b16 v[148:149], v231 offset:4096\n\t"
        "ds_read_b64_tr_b16 v[150:151], v231 offset:5120\n\t"
        "ds_read_b64_tr_b16 v[152:153], v230 offset:6144\n\t"
        "ds_read_b64_tr_b16 v[154:155], v230 offset:7168\n\t"
        "ds_read_b64_tr_b16 v[156:157], v231 offset:6144\n\t"
        "ds_read_b64_tr_b16 v[158:159], v231 offset:7168\n\t"
        "s_mov_b32 s44, s45\n\t"
        "s_cmp_eq_u32 s47, 0\n\t"
        "s_cbranch_scc1 oseg0_%=\n\t"
        "s_cmp_eq_u32 s47, 1\n\t"
        "s_cbranch_scc1 oseg1_%=\n\t"
        "s_cmp_eq_u32 s47, 2\n\t"
        "s_cbranch_scc1 oseg2_%=\n\t"
        "s_cmp_eq_u32 s47, 3\n\t"
        "s_cbranch_scc1 oseg3_%=\n\t"
        "oseg0_%=:\n\t"
        "s_or_b32 s46, s46, 1\n\t"
        "s_waitcnt vmcnt(11) lgkmcnt(14)\n\t"
        "v_mfma_f32_32x32x16_bf16 v[0:15], v[128:131], v[160:163], v[0:15]\n\t"
        "ds_read_b64_tr_b16 v[128:129], v226 offset:8192\n\t"
        "ds_read_b64_tr_b16 v[130:131], v226 offset:9216\n\t"
        "s_waitcnt lgkmcnt(14)\n\t"
        "v_mfma_f32_32x32x16_bf16 v[16:31], v[132:135], v[160:163], v[16:31]\n\t"
        "ds_read_b64_tr_b16 v[132:133], v227 offset:8192\n\t"
        "ds_read_b64_tr_b16 v[134:135], v227 offset:9216\n\t"
        "s_add_u32 s44, s44, 0x1000\n\t"
        "buffer_load_dwordx4 v[208:211], v224, s[40:43], s44 offen offset:0 sc1\n\t"
        "s_waitcnt vmcnt(11) lgkmcnt(14)\n\t"
        "v_mfma_f32_32x32x16_bf16 v[0:15], v[136:139], v[164:167], v[0:15]\n\t"
        "ds_read_b32 v233, v232\n\t"
        "ds_read_b64_tr_b16 v[136:137], v226 offset:10240\n\t"
        "ds_read_b64_tr_b16 v[138:139], v226 offset:11264\n\t"
        "s_waitcnt lgkmcnt(14)\n\t"
        "v_mfma_f32_32x32x16_bf16 v[16:31], v[140:143], v[164:167], v[16:31]\n\t"
        "ds_read_b64_tr_b16 v[140:141], v227 offset:10240\n\t"
        "ds_read_b64_tr_b16 v[142:143], v227 offset:11264\n\t"
        "buffer_load_dwordx4 v[212:215], v224, s[40:43], s44 offen offset:1024 sc1\n\t"
        "s_waitcnt vmcnt(11) lgkmcnt(14)\n\t"
        "v_mfma_f32_32x32x16_bf16 v[0:15], v[144:147], v[168:171], v[0:15]\n\t"
        "ds_read_b64_tr_b16 v[144:145], v226 offset:12288\n\t"
        "ds_read_b64_tr_b16 v[146:147], v226 offset:13312\n\t"
        "s_waitcnt lgkmcnt(14)\n\t"
        "v_mfma_f32_32x32x16_bf16 v[16:31], v[148:151], v[168:171], v[16:31]\n\t"
        "ds_read_b64_tr_b16 v[148:149], v227 offset:12288\n\t"
        "ds_read_b64_tr_b16 v[150:151], v227 offset:13312\n\t"
        "buffer_load_dwordx4 v[216:219], v224, s[40:43], s44 offen offset:2048 sc1\n\t"
        "s_waitcnt vmcnt(11) lgkmcnt(8)\n\t"
        "v_mfma_f32_32x32x16_bf16 v[0:15], v[152:155], v[172:175], v[0:15]\n\t"
        "v_readfirstlane_b32 s51, v233\n\t"
        "ds_read_b64_tr_b16 v[152:153], v226 offset:14336\n\t"
        "ds_read_b64_tr_b16 v[154:155], v226 offset:15360\n\t"
        "s_waitcnt lgkmcnt(14)\n\t"
        "v_mfma_f32_32x32x16_bf16 v[16:31], v[156:159], v[172:175], v[16:31]\n\t"
        "s_andn2_b32 s51, s51, s46\n\t"
        "s_ff1_i32_b32 s47, s51\n\t"
        "s_add_u32 s45, s47, s49\n\t"
        "s_and_b32 s45, s45, 3\n\t"
        "s_lshl_b32 s45, s45, 14\n\t"
        "ds_read_b64_tr_b16 v[156:157], v227 offset:14336\n\t"
        "ds_read_b64_tr_b16 v[158:159], v227 offset:15360\n\t"
        "buffer_load_dwordx4 v[220:223], v224, s[40:43], s44 offen offset:3072 sc1\n\t"
        "s_waitcnt vmcnt(11) lgkmcnt(14)\n\t"
        "v_mfma_f32_32x32x16_bf16 v[0:15], v[128:131], v[176:179], v[0:15]\n\t"
        "s_and_b32 s51, s47, 0x80000000\n\t"
        "v_or_b32 v225, s51, v224\n\t"
        "s_and_b32 s51, s47, 3\n\t"
        "s_lshl_b32 s51, s51, 15\n\t"
        "v_add_u32 v230, s51, v226\n\t"
        "v_add_u32 v231, s51, v227\n\t"
        "ds_read_b64_tr_b16 v[128:129], v226 offset:16384\n\t"
        "ds_read_b64_tr_b16 v[130:131], v226 offset:17408\n\t"
        "s_waitcnt lgkmcnt(14)\n\t"
        "v_mfma_f32_32x32x16_bf16 v[16:31], v[132:135], v[176:179], v[16:31]\n\t"
        "ds_read_b64_tr_b16 v[132:133], v227 offset:16384\n\t"
        "ds_read_b64_tr_b16 v[134:135], v227 offset:17408\n\t"
        "buffer_load_dwordx4 v[160:163], v225, s[40:43], s45 offen offset:0 sc1\n\t"
        "s_waitcnt vmcnt(11) lgkmcnt(14)\n\t"
        "v_mfma_f32_32x32x16_bf16 v[0:15], v[136:139], v[180:183], v[0:15]\n\t"
        "ds_read_b64_tr_b16 v[136:137], v226 offset:18432\n\t"
        "ds_read_b64_tr_b16 v[138:139], v226 offset:19456\n\t"
        "s_waitcnt lgkmcnt(14)\n\t"
        "v_mfma_f32_32x32x16_bf16 v[16:31], v[140:143], v[180:183], v[16:31]\n\t"
        "ds_read_b64_tr_b16 v[140:141], v227 offset:18432\n\t"
        "ds_read_b64_tr_b16 v[142:143], v227 offset:19456\n\t"
        "buffer_load_dwordx4 v[164:167], v225, s[40:43], s45 offen offset:1024 sc1\n\t"
        "s_waitcnt vmcnt(11) lgkmcnt(14)\n\t"
        "v_mfma_f32_32x32x16_bf16 v[0:15], v[144:147], v[184:187], v[0:15]\n\t"
        "ds_read_b64_tr_b16 v[144:145], v226 offset:20480\n\t"
        "ds_read_b64_tr_b16 v[146:147], v226 offset:21504\n\t"
        "s_waitcnt lgkmcnt(14)\n\t"
        "v_mfma_f32_32x32x16_bf16 v[16:31], v[148:151], v[184:187], v[16:31]\n\t"
        "ds_read_b64_tr_b16 v[148:149], v227 offset:20480\n\t"
        "ds_read_b64_tr_b16 v[150:151], v227 offset:21504\n\t"
        "buffer_load_dwordx4 v[168:171], v225, s[40:43], s45 offen offset:2048 sc1\n\t"
        "s_waitcnt vmcnt(11) lgkmcnt(14)\n\t"
        "v_mfma_f32_32x32x16_bf16 v[0:15], v[152:155], v[188:191], v[0:15]\n\t"
        "ds_read_b64_tr_b16 v[152:153], v226 offset:22528\n\t"
        "ds_read_b64_tr_b16 v[154:155], v226 offset:23552\n\t"
        "s_waitcnt lgkmcnt(14)\n\t"
        "v_mfma_f32_32x32x16_bf16 v[16:31], v[156:159], v[188:191], v[16:31]\n\t"
        "ds_read_b64_tr_b16 v[156:157], v227 offset:22528\n\t"
        "ds_read_b64_tr_b16 v[158:159], v227 offset:23552\n\t"
        "buffer_load_dwordx4 v[172:175], v225, s[40:43], s45 offen offset:3072 sc1\n\t"
        "s_waitcnt vmcnt(11) lgkmcnt(14)\n\t"
        "v_mfma_f32_32x32x16_bf16 v[0:15], v[128:131], v[192:195], v[0:15]\n\t"
        "ds_read_b64_tr_b16 v[128:129], v226 offset:24576\n\t"
        "ds_read_b64_tr_b16 v[130:131], v226 offset:25600\n\t"
        "s_waitcnt lgkmcnt(14)\n\t"
        "v_mfma_f32_32x32x16_bf16 v[16:31], v[132:135], v[192:195], v[16:31]\n\t"
        "ds_read_b64_tr_b16 v[132:133], v227 offset:24576\n\t"
        "ds_read_b64_tr_b16 v[134:135], v227 offset:25600\n\t"
        "s_add_u32 s45, s45, 0x1000\n\t"
        "buffer_load_dwordx4 v[176:179], v225, s[40:43], s45 offen offset:0 sc1\n\t"
        "s_waitcnt vmcnt(11) lgkmcnt(14)\n\t"
        "v_mfma_f32_32x32x16_bf16 v[0:15], v[136:139], v[196:199], v[0:15]\n\t"
        "ds_read_b64_tr_b16 v[136:137], v226 offset:26624\n\t"
        "ds_read_b64_tr_b16 v[138:139], v226 offset:27648\n\t"
        "s_waitcnt lgkmcnt(14)\n\t"
        "v_mfma_f32_32x32x16_bf16 v[16:31], v[140:143], v[196:199], v[16:31]\n\t"
        "ds_read_b64_tr_b16 v[140:141], v227 offset:26624\n\t"
        "ds_read_b64_tr_b16 v[142:143], v227 offset:27648\n\t"
        "buffer_load_dwordx4 v[180:183], v225, s[40:43], s45 offen offset:1024 sc1\n\t"
        "s_waitcnt vmcnt(11) lgkmcnt(14)\n\t"
        "v_mfma_f32_32x32x16_bf16 v[0:15], v[144:147], v[200:203], v[0:15]\n\t"
        "ds_read_b64_tr_b16 v[144:145], v226 offset:28672\n\t"
        "ds_read_b64_tr_b16 v[146:147], v226 offset:29696\n\t"
        "s_waitcnt lgkmcnt(14)\n\t"
        "v_mfma_f32_32x32x16_bf16 v[16:31], v[148:151], v[200:203], v[16:31]\n\t"
        "ds_read_b64_tr_b16 v[148:149], v227 offset:28672\n\t"
        "ds_read_b64_tr_b16 v[150:151], v227 offset:29696\n\t"
        "buffer_load_dwordx4 v[184:187], v225, s[40:43], s45 offen offset:2048 sc1\n\t"
        "s_waitcnt vmcnt(11) lgkmcnt(14)\n\t"
        "v_mfma_f32_32x32x16_bf16 v[0:15], v[152:155], v[204:207], v[0:15]\n\t"
        "ds_read_b64_tr_b16 v[152:153], v226 offset:30720\n\t"
        "ds_read_b64_tr_b16 v[154:155], v226 offset:31744\n\t"
        "s_waitcnt lgkmcnt(14)\n\t"
        "v_mfma_f32_32x32x16_bf16 v[16:31], v[156:159], v[204:207], v[16:31]\n\t"
        "ds_read_b64_tr_b16 v[156:157], v227 offset:30720\n\t"
        "ds_read_b64_tr_b16 v[158:159], v227 offset:31744\n\t"
        "buffer_load_dwordx4 v[188:191], v225, s[40:43], s45 offen offset:3072 sc1\n\t"
        "s_waitcnt vmcnt(11) lgkmcnt(14)\n\t"
        "v_mfma_f32_32x32x16_bf16 v[0:15], v[128:131], v[208:211], v[0:15]\n\t"
        "ds_read_b64_tr_b16 v[128:129], v230 offset:0\n\t"
        "ds_read_b64_tr_b16 v[130:131], v230 offset:1024\n\t"
        "s_waitcnt lgkmcnt(14)\n\t"
        "v_mfma_f32_32x32x16_bf16 v[16:31], v[132:135], v[208:211], v[16:31]\n\t"
        "ds_read_b64_tr_b16 v[132:133], v231 offset:0\n\t"
        "ds_read_b64_tr_b16 v[134:135], v231 offset:1024\n\t"
        "s_add_u32 s45, s45, 0x1000\n\t"
        "buffer_load_dwordx4 v[192:195], v225, s[40:43], s45 offen offset:0 sc1\n\t"
        "s_waitcnt vmcnt(11) lgkmcnt(14)\n\t"
        "v_mfma_f32_32x32x16_bf16 v[0:15], v[136:139], v[212:215], v[0:15]\n\t"
        "ds_read_b64_tr_b16 v[136:137], v230 offset:2048\n\t"
        "ds_read_b64_tr_b16 v[138:139], v230 offset:3072\n\t"
        "s_waitcnt lgkmcnt(14)\n\t"
        "v_mfma_f32_32x32x16_bf16 v[16:31], v[140:143], v[212:215], v[16:31]\n\t"
        "ds_read_b64_tr_b16 v[140:141], v231 offset:2048\n\t"
        "ds_read_b64_tr_b16 v[142:143], v231 offset:3072\n\t"
        "buffer_load_dwordx4 v[196:199], v225, s[40:43], s45 offen offset:1024 sc1\n\t"
        "s_waitcnt vmcnt(11) lgkmcnt(14)\n\t"
        "v_mfma_f32_32x32x16_bf16 v[0:15], v[144:147], v[216:219], v[0:15]\n\t"
        "ds_read_b64_tr_b16 v[144:145], v230 offset:4096\n\t"
        "ds_read_b64_tr_b16 v[146:147], v230 offset:5120\n\t"
        "s_waitcnt lgkmcnt(14)\n\t"
        "v_mfma_f32_32x32x16_bf16 v[16:31], v[148:151], v[216:219], v[16:31]\n\t"
        "ds_read_b64_tr_b16 v[148:149], v231 offset:4096\n\t"
        "ds_read_b64_tr_b16 v[150:151], v231 offset:5120\n\t"
        "buffer_load_dwordx4 v[200:203], v225, s[40:43], s45 offen offset:2048 sc1\n\t"
        "s_waitcnt vmcnt(11) lgkmcnt(14)\n\t"
        "v_mfma_f32_32x32x16_bf16 v[0:15], v[152:155], v[220:223], v[0:15]\n\t"
        "ds_read_b64_tr_b16 v[152:153], v230 offset:6144\n\t"
        "ds_read_b64_tr_b16 v[154:155], v230 offset:7168\n\t"
        "s_waitcnt lgkmcnt(14)\n\t"
        "v_mfma_f32_32x32x16_bf16 v[16:31], v[156:159], v[220:223], v[16:31]\n\t"
        "ds_read_b64_tr_b16 v[156:157], v231 offset:6144\n\t"
        "ds_read_b64_tr_b16 v[158:159], v231 offset:7168\n\t"
        "buffer_load_dwordx4 v[204:207], v225, s[40:43], s45 offen offset:3072 sc1\n\t"
        "s_mov_b32 s44, s45\n\t"
        "s_cmp_eq_u32 s47, 0\n\t"
        "s_cbranch_scc1 oseg0_%=\n\t"
        "s_cmp_eq_u32 s47, 1\n\t"
        "s_cbranch_scc1 oseg1_%=\n\t"
        "s_cmp_eq_u32 s47, 2\n\t"
        "s_cbranch_scc1 oseg2_%=\n\t"
        "s_cmp_eq_u32 s47, 3\n\t"
        "s_cbranch_scc1 oseg3_%=\n\t"
        "s_cmp_lg_u32 s46, 15\n\t"
        "s_cbranch_scc1 ostart_%=\n\t"
        "s_branch oend_%=\n\t"
        "oseg1_%=:\n\t"
        "s_or_b32 s46, s46, 2\n\t"
        "s_waitcnt vmcnt(11) lgkmcnt(14)\n\t"
        "v_mfma_f32_32x32x16_bf16 v[32:47], v[128:131], v[160:163], 0\n\t"
        "ds_read_b64_tr_b16 v[128:129], v226 offset:40960\n\t"
        "ds_read_b64_tr_b16 v[130:131], v226 offset:41984\n\t"
        "s_waitcnt lgkmcnt(14)\n\t"
        "v_mfma_f32_32x32x16_bf16 v[48:63], v[132:135], v[160:163], 0\n\t"
        "ds_read_b64_tr_b16 v[132:133], v227 offset:40960\n\t"
        "ds_read_b64_tr_b16 v[134:135], v227 offset:41984\n\t"
        "s_add_u32 s44, s44, 0x1000\n\t"
        "buffer_load_dwordx4 v[208:211], v224, s[40:43], s44 offen offset:0 sc1\n\t"
        "s_waitcnt vmcnt(11) lgkmcnt(14)\n\t"
        "v_mfma_f32_32x32x16_bf16 v[32:47], v[136:139], v[164:167], v[32:47]\n\t"
        "ds_read_b32 v233, v232\n\t"
        "ds_read_b64_tr_b16 v[136:137], v226 offset:43008\n\t"
        "ds_read_b64_tr_b16 v[138:139], v226 offset:44032\n\t"
        "s_waitcnt lgkmcnt(14)\n\t"
        "v_mfma_f32_32x32x16_bf16 v[48:63], v[140:143], v[164:167], v[48:63]\n\t"
        "ds_read_b64_tr_b16 v[140:141], v227 offset:43008\n\t"
        "ds_read_b64_tr_b16 v[142:143], v227 offset:44032\n\t"
        "buffer_load_dwordx4 v[212:215], v224, s[40:43], s44 offen offset:1024 sc1\n\t"
        "s_waitcnt vmcnt(11) lgkmcnt(14)\n\t"
        "v_mfma_f32_32x32x16_bf16 v[32:47], v[144:147], v[168:171], v[32:47]\n\t"
        "ds_read_b64_tr_b16 v[144:145], v226 offset:45056\n\t"
        "ds_read_b64_tr_b16 v[146:147], v226 offset:46080\n\t"
        "s_waitcnt lgkmcnt(14)\n\t"
        "v_mfma_f32_32x32x16_bf16 v[48:63], v[148:151], v[168:171], v[48:63]\n\t"
        "ds_read_b64_tr_b16 v[148:149], v227 offset:45056\n\t"
        "ds_read_b64_tr_b16 v[150:151], v227 offset:46080\n\t"
        "buffer_load_dwordx4 v[216:219], v224, s[40:43], s44 offen offset:2048 sc1\n\t"
        "s_waitcnt vmcnt(11) lgkmcnt(8)\n\t"
        "v_mfma_f32_32x32x16_bf16 v[32:47], v[152:155], v[172:175], v[32:47]\n\t"
        "v_readfirstlane_b32 s51, v233\n\t"
        "ds_read_b64_tr_b16 v[152:153], v226 offset:47104\n\t"
        "ds_read_b64_tr_b16 v[154:155], v226 offset:48128\n\t"
        "s_waitcnt lgkmcnt(14)\n\t"
        "v_mfma_f32_32x32x16_bf16 v[48:63], v[156:159], v[172:175], v[48:63]\n\t"
        "s_andn2_b32 s51, s51, s46\n\t"
        "s_ff1_i32_b32 s47, s51\n\t"
        "s_add_u32 s45, s47, s49\n\t"
        "s_and_b32 s45, s45, 3\n\t"
        "s_lshl_b32 s45, s45, 14\n\t"
        "ds_read_b64_tr_b16 v[156:157], v227 offset:47104\n\t"
        "ds_read_b64_tr_b16 v[158:159], v227 offset:48128\n\t"
        "buffer_load_dwordx4 v[220:223], v224, s[40:43], s44 offen offset:3072 sc1\n\t"
        "s_waitcnt vmcnt(11) lgkmcnt(14)\n\t"
        "v_mfma_f32_32x32x16_bf16 v[32:47], v[128:131], v[176:179], v[32:47]\n\t"
        "s_and_b32 s51, s47, 0x80000000\n\t"
        "v_or_b32 v225, s51, v224\n\t"
        "s_and_b32 s51, s47, 3\n\t"
        "s_lshl_b32 s51, s51, 15\n\t"
        "v_add_u32 v230, s51, v226\n\t"
        "v_add_u32 v231, s51, v227\n\t"
        "ds_read_b64_tr_b16 v[128:129], v226 offset:49152\n\t"
        "ds_read_b64_tr_b16 v[130:131], v226 offset:50176\n\t"
        "s_waitcnt lgkmcnt(14)\n\t"
        "v_mfma_f32_32x32x16_bf16 v[48:63], v[132:135], v[176:179], v[48:63]\n\t"
        "ds_read_b64_tr_b16 v[132:133], v227 offset:49152\n\t"
        "ds_read_b64_tr_b16 v[134:135], v227 offset:50176\n\t"
        "buffer_load_dwordx4 v[160:163], v225, s[40:43], s45 offen offset:0 sc1\n\t"
        "s_waitcnt vmcnt(11) lgkmcnt(14)\n\t"
        "v_mfma_f32_32x32x16_bf16 v[32:47], v[136:139], v[180:183], v[32:47]\n\t"
        "ds_read_b64_tr_b16 v[136:137], v226 offset:51200\n\t"
        "ds_read_b64_tr_b16 v[138:139], v226 offset:52224\n\t"
        "s_waitcnt lgkmcnt(14)\n\t"
        "v_mfma_f32_32x32x16_bf16 v[48:63], v[140:143], v[180:183], v[48:63]\n\t"
        "ds_read_b64_tr_b16 v[140:141], v227 offset:51200\n\t"
        "ds_read_b64_tr_b16 v[142:143], v227 offset:52224\n\t"
        "buffer_load_dwordx4 v[164:167], v225, s[40:43], s45 offen offset:1024 sc1\n\t"
        "s_waitcnt vmcnt(11) lgkmcnt(14)\n\t"
        "v_mfma_f32_32x32x16_bf16 v[32:47], v[144:147], v[184:187], v[32:47]\n\t"
        "ds_read_b64_tr_b16 v[144:145], v226 offset:53248\n\t"
        "ds_read_b64_tr_b16 v[146:147], v226 offset:54272\n\t"
        "s_waitcnt lgkmcnt(14)\n\t"
        "v_mfma_f32_32x32x16_bf16 v[48:63], v[148:151], v[184:187], v[48:63]\n\t"
        "ds_read_b64_tr_b16 v[148:149], v227 offset:53248\n\t"
        "ds_read_b64_tr_b16 v[150:151], v227 offset:54272\n\t"
        "buffer_load_dwordx4 v[168:171], v225, s[40:43], s45 offen offset:2048 sc1\n\t"
        "s_waitcnt vmcnt(11) lgkmcnt(14)\n\t"
        "v_mfma_f32_32x32x16_bf16 v[32:47], v[152:155], v[188:191], v[32:47]\n\t"
        "ds_read_b64_tr_b16 v[152:153], v226 offset:55296\n\t"
        "ds_read_b64_tr_b16 v[154:155], v226 offset:56320\n\t"
        "s_waitcnt lgkmcnt(14)\n\t"
        "v_mfma_f32_32x32x16_bf16 v[48:63], v[156:159], v[188:191], v[48:63]\n\t"
        "ds_read_b64_tr_b16 v[156:157], v227 offset:55296\n\t"
        "ds_read_b64_tr_b16 v[158:159], v227 offset:56320\n\t"
        "buffer_load_dwordx4 v[172:175], v225, s[40:43], s45 offen offset:3072 sc1\n\t"
        "s_waitcnt vmcnt(11) lgkmcnt(14)\n\t"
        "v_mfma_f32_32x32x16_bf16 v[32:47], v[128:131], v[192:195], v[32:47]\n\t"
        "ds_read_b64_tr_b16 v[128:129], v226 offset:57344\n\t"
        "ds_read_b64_tr_b16 v[130:131], v226 offset:58368\n\t"
        "s_waitcnt lgkmcnt(14)\n\t"
        "v_mfma_f32_32x32x16_bf16 v[48:63], v[132:135], v[192:195], v[48:63]\n\t"
        "ds_read_b64_tr_b16 v[132:133], v227 offset:57344\n\t"
        "ds_read_b64_tr_b16 v[134:135], v227 offset:58368\n\t"
        "s_add_u32 s45, s45, 0x1000\n\t"
        "buffer_load_dwordx4 v[176:179], v225, s[40:43], s45 offen offset:0 sc1\n\t"
        "s_waitcnt vmcnt(11) lgkmcnt(14)\n\t"
        "v_mfma_f32_32x32x16_bf16 v[32:47], v[136:139], v[196:199], v[32:47]\n\t"
        "ds_read_b64_tr_b16 v[136:137], v226 offset:59392\n\t"
        "ds_read_b64_tr_b16 v[138:139], v226 offset:60416\n\t"
        "s_waitcnt lgkmcnt(14)\n\t"
        "v_mfma_f32_32x32x16_bf16 v[48:63], v[140:143], v[196:199], v[48:63]\n\t"
        "ds_read_b64_tr_b16 v[140:141], v227 offset:59392\n\t"
        "ds_read_b64_tr_b16 v[142:143], v227 offset:60416\n\t"
        "buffer_load_dwordx4 v[180:183], v225, s[40:43], s45 offen offset:1024 sc1\n\t"
        "s_waitcnt vmcnt(11) lgkmcnt(14)\n\t"
        "v_mfma_f32_32x32x16_bf16 v[32:47], v[144:147], v[200:203], v[32:47]\n\t"
        "ds_read_b64_tr_b16 v[144:145], v226 offset:61440\n\t"
        "ds_read_b64_tr_b16 v[146:147], v226 offset:62464\n\t"
        "s_waitcnt lgkmcnt(14)\n\t"
        "v_mfma_f32_32x32x16_bf16 v[48:63], v[148:151], v[200:203], v[48:63]\n\t"
        "ds_read_b64_tr_b16 v[148:149], v227 offset:61440\n\t"
        "ds_read_b64_tr_b16 v[150:151], v227 offset:62464\n\t"
        "buffer_load_dwordx4 v[184:187], v225, s[40:43], s45 offen offset:2048 sc1\n\t"
        "s_waitcnt vmcnt(11) lgkmcnt(14)\n\t"
        "v_mfma_f32_32x32x16_bf16 v[32:47], v[152:155], v[204:207], v[32:47]\n\t"
        "ds_read_b64_tr_b16 v[152:153], v226 offset:63488\n\t"
        "ds_read_b64_tr_b16 v[154:155], v226 offset:64512\n\t"
        "s_waitcnt lgkmcnt(14)\n\t"
        "v_mfma_f32_32x32x16_bf16 v[48:63], v[156:159], v[204:207], v[48:63]\n\t"
        "ds_read_b64_tr_b16 v[156:157], v227 offset:63488\n\t"
        "ds_read_b64_tr_b16 v[158:159], v227 offset:64512\n\t"
        "buffer_load_dwordx4 v[188:191], v225, s[40:43], s45 offen offset:3072 sc1\n\t"
        "s_waitcnt vmcnt(11) lgkmcnt(14)\n\t"
        "v_mfma_f32_32x32x16_bf16 v[32:47], v[128:131], v[208:211], v[32:47]\n\t"
        "ds_read_b64_tr_b16 v[128:129], v230 offset:0\n\t"
        "ds_read_b64_tr_b16 v[130:131], v230 offset:1024\n\t"
        "s_waitcnt lgkmcnt(14)\n\t"
        "v_mfma_f32_32x32x16_bf16 v[48:63], v[132:135], v[208:211], v[48:63]\n\t"
        "ds_read_b64_tr_b16 v[132:133], v231 offset:0\n\t"
        "ds_read_b64_tr_b16 v[134:135], v231 offset:1024\n\t"
        "s_add_u32 s45, s45, 0x1000\n\t"
        "buffer_load_dwordx4 v[192:195], v225, s[40:43], s45 offen offset:0 sc1\n\t"
        "s_waitcnt vmcnt(11) lgkmcnt(14)\n\t"
        "v_mfma_f32_32x32x16_bf16 v[32:47], v[136:139], v[212:215], v[32:47]\n\t"
        "ds_read_b64_tr_b16 v[136:137], v230 offset:2048\n\t"
        "ds_read_b64_tr_b16 v[138:139], v230 offset:3072\n\t"
        "s_waitcnt lgkmcnt(14)\n\t"
        "v_mfma_f32_32x32x16_bf16 v[48:63], v[140:143], v[212:215], v[48:63]\n\t"
        "ds_read_b64_tr_b16 v[140:141], v231 offset:2048\n\t"
        "ds_read_b64_tr_b16 v[142:143], v231 offset:3072\n\t"
        "buffer_load_dwordx4 v[196:199], v225, s[40:43], s45 offen offset:1024 sc1\n\t"
        "s_waitcnt vmcnt(11) lgkmcnt(14)\n\t"
        "v_mfma_f32_32x32x16_bf16 v[32:47], v[144:147], v[216:219], v[32:47]\n\t"
        "ds_read_b64_tr_b16 v[144:145], v230 offset:4096\n\t"
        "ds_read_b64_tr_b16 v[146:147], v230 offset:5120\n\t"
        "s_waitcnt lgkmcnt(14)\n\t"
        "v_mfma_f32_32x32x16_bf16 v[48:63], v[148:151], v[216:219], v[48:63]\n\t"
        "ds_read_b64_tr_b16 v[148:149], v231 offset:4096\n\t"
        "ds_read_b64_tr_b16 v[150:151], v231 offset:5120\n\t"
        "buffer_load_dwordx4 v[200:203], v225, s[40:43], s45 offen offset:2048 sc1\n\t"
        "s_waitcnt vmcnt(11) lgkmcnt(14)\n\t"
        "v_mfma_f32_32x32x16_bf16 v[32:47], v[152:155], v[220:223], v[32:47]\n\t"
        "ds_read_b64_tr_b16 v[152:153], v230 offset:6144\n\t"
        "ds_read_b64_tr_b16 v[154:155], v230 offset:7168\n\t"
        "s_waitcnt lgkmcnt(14)\n\t"
        "v_mfma_f32_32x32x16_bf16 v[48:63], v[156:159], v[220:223], v[48:63]\n\t"
        "ds_read_b64_tr_b16 v[156:157], v231 offset:6144\n\t"
        "ds_read_b64_tr_b16 v[158:159], v231 offset:7168\n\t"
        "buffer_load_dwordx4 v[204:207], v225, s[40:43], s45 offen offset:3072 sc1\n\t"
        "s_mov_b32 s44, s45\n\t"
        "s_cmp_eq_u32 s47, 0\n\t"
        "s_cbranch_scc1 oseg0_%=\n\t"
        "s_cmp_eq_u32 s47, 1\n\t"
        "s_cbranch_scc1 oseg1_%=\n\t"
        "s_cmp_eq_u32 s47, 2\n\t"
        "s_cbranch_scc1 oseg2_%=\n\t"
        "s_cmp_eq_u32 s47, 3\n\t"
        "s_cbranch_scc1 oseg3_%=\n\t"
        "s_cmp_lg_u32 s46, 15\n\t"
        "s_cbranch_scc1 ostart_%=\n\t"
        "s_branch oend_%=\n\t"
        "oseg2_%=:\n\t"
        "s_or_b32 s46, s46, 4\n\t"
        "s_waitcnt vmcnt(11) lgkmcnt(14)\n\t"
        "v_mfma_f32_32x32x16_bf16 v[64:79], v[128:131], v[160:163], 0\n\t"
        "ds_read_b64_tr_b16 v[128:129], v228 offset:8192\n\t"
        "ds_read_b64_tr_b16 v[130:131], v228 offset:9216\n\t"
        "s_waitcnt lgkmcnt(14)\n\t"
        "v_mfma_f32_32x32x16_bf16 v[80:95], v[132:135], v[160:163], 0\n\t"
        "ds_read_b64_tr_b16 v[132:133], v229 offset:8192\n\t"
        "ds_read_b64_tr_b16 v[134:135], v229 offset:9216\n\t"
        "s_add_u32 s44, s44, 0x1000\n\t"
        "buffer_load_dwordx4 v[208:211], v224, s[40:43], s44 offen offset:0 sc1\n\t"
        "s_waitcnt vmcnt(11) lgkmcnt(14)\n\t"
        "v_mfma_f32_32x32x16_bf16 v[64:79], v[136:139], v[164:167], v[64:79]\n\t"
        "ds_read_b32 v233, v232\n\t"
        "ds_read_b64_tr_b16 v[136:137], v228 offset:10240\n\t"
        "ds_read_b64_tr_b16 v[138:139], v228 offset:11264\n\t"
        "s_waitcnt lgkmcnt(14)\n\t"
        "v_mfma_f32_32x32x16_bf16 v[80:95], v[140:143], v[164:167], v[80:95]\n\t"
        "ds_read_b64_tr_b16 v[140:141], v229 offset:10240\n\t"
        "ds_read_b64_tr_b16 v[142:143], v229 offset:11264\n\t"
        "buffer_load_dwordx4 v[212:215], v224, s[40:43], s44 offen offset:1024 sc1\n\t"
        "s_waitcnt vmcnt(11) lgkmcnt(14)\n\t"
        "v_mfma_f32_32x32x16_bf16 v[64:79], v[144:147], v[168:171], v[64:79]\n\t"
        "ds_read_b64_tr_b16 v[144:145], v228 offset:12288\n\t"
        "ds_read_b64_tr_b16 v[146:147], v228 offset:13312\n\t"
        "s_waitcnt lgkmcnt(14)\n\t"
        "v_mfma_f32_32x32x16_bf16 v[80:95], v[148:151], v[168:171], v[80:95]\n\t"
        "ds_read_b64_tr_b16 v[148:149], v229 offset:12288\n\t"
        "ds_read_b64_tr_b16 v[150:151], v229 offset:13312\n\t"
        "buffer_load_dwordx4 v[216:219], v224, s[40:43], s44 offen offset:2048 sc1\n\t"
        "s_waitcnt vmcnt(11) lgkmcnt(8)\n\t"
        "v_mfma_f32_32x32x16_bf16 v[64:79], v[152:155], v[172:175], v[64:79]\n\t"
        "v_readfirstlane_b32 s51, v233\n\t"
        "ds_read_b64_tr_b16 v[152:153], v228 offset:14336\n\t"
        "ds_read_b64_tr_b16 v[154:155], v228 offset:15360\n\t"
        "s_waitcnt lgkmcnt(14)\n\t"
        "v_mfma_f32_32x32x16_bf16 v[80:95], v[156:159], v[172:175], v[80:95]\n\t"
        "s_andn2_b32 s51, s51, s46\n\t"
        "s_ff1_i32_b32 s47, s51\n\t"
        "s_add_u32 s45, s47, s49\n\t"
        "s_and_b32 s45, s45, 3\n\t"
        "s_lshl_b32 s45, s45, 14\n\t"
        "ds_read_b64_tr_b16 v[156:157], v229 offset:14336\n\t"
        "ds_read_b64_tr_b16 v[158:159], v229 offset:15360\n\t"
        "buffer_load_dwordx4 v[220:223], v224, s[40:43], s44 offen offset:3072 sc1\n\t"
        "s_waitcnt vmcnt(11) lgkmcnt(14)\n\t"
        "v_mfma_f32_32x32x16_bf16 v[64:79], v[128:131], v[176:179], v[64:79]\n\t"
        "s_and_b32 s51, s47, 0x80000000\n\t"
        "v_or_b32 v225, s51, v224\n\t"
        "s_and_b32 s51, s47, 3\n\t"
        "s_lshl_b32 s51, s51, 15\n\t"
        "v_add_u32 v230, s51, v226\n\t"
        "v_add_u32 v231, s51, v227\n\t"
        "ds_read_b64_tr_b16 v[128:129], v228 offset:16384\n\t"
        "ds_read_b64_tr_b16 v[130:131], v228 offset:17408\n\t"
        "s_waitcnt lgkmcnt(14)\n\t"
        "v_mfma_f32_32x32x16_bf16 v[80:95], v[132:135], v[176:179], v[80:95]\n\t"
        "ds_read_b64_tr_b16 v[132:133], v229 offset:16384\n\t"
        "ds_read_b64_tr_b16 v[134:135], v229 offset:17408\n\t"
        "buffer_load_dwordx4 v[160:163], v225, s[40:43], s45 offen offset:0 sc1\n\t"
        "s_waitcnt vmcnt(11) lgkmcnt(14)\n\t"
        "v_mfma_f32_32x32x16_bf16 v[64:79], v[136:139], v[180:183], v[64:79]\n\t"
        "ds_read_b64_tr_b16 v[136:137], v228 offset:18432\n\t"
        "ds_read_b64_tr_b16 v[138:139], v228 offset:19456\n\t"
        "s_waitcnt lgkmcnt(14)\n\t"
        "v_mfma_f32_32x32x16_bf16 v[80:95], v[140:143], v[180:183], v[80:95]\n\t"
        "ds_read_b64_tr_b16 v[140:141], v229 offset:18432\n\t"
        "ds_read_b64_tr_b16 v[142:143], v229 offset:19456\n\t"
        "buffer_load_dwordx4 v[164:167], v225, s[40:43], s45 offen offset:1024 sc1\n\t"
        "s_waitcnt vmcnt(11) lgkmcnt(14)\n\t"
        "v_mfma_f32_32x32x16_bf16 v[64:79], v[144:147], v[184:187], v[64:79]\n\t"
        "ds_read_b64_tr_b16 v[144:145], v228 offset:20480\n\t"
        "ds_read_b64_tr_b16 v[146:147], v228 offset:21504\n\t"
        "s_waitcnt lgkmcnt(14)\n\t"
        "v_mfma_f32_32x32x16_bf16 v[80:95], v[148:151], v[184:187], v[80:95]\n\t"
        "ds_read_b64_tr_b16 v[148:149], v229 offset:20480\n\t"
        "ds_read_b64_tr_b16 v[150:151], v229 offset:21504\n\t"
        "buffer_load_dwordx4 v[168:171], v225, s[40:43], s45 offen offset:2048 sc1\n\t"
        "s_waitcnt vmcnt(11) lgkmcnt(14)\n\t"
        "v_mfma_f32_32x32x16_bf16 v[64:79], v[152:155], v[188:191], v[64:79]\n\t"
        "ds_read_b64_tr_b16 v[152:153], v228 offset:22528\n\t"
        "ds_read_b64_tr_b16 v[154:155], v228 offset:23552\n\t"
        "s_waitcnt lgkmcnt(14)\n\t"
        "v_mfma_f32_32x32x16_bf16 v[80:95], v[156:159], v[188:191], v[80:95]\n\t"
        "ds_read_b64_tr_b16 v[156:157], v229 offset:22528\n\t"
        "ds_read_b64_tr_b16 v[158:159], v229 offset:23552\n\t"
        "buffer_load_dwordx4 v[172:175], v225, s[40:43], s45 offen offset:3072 sc1\n\t"
        "s_waitcnt vmcnt(11) lgkmcnt(14)\n\t"
        "v_mfma_f32_32x32x16_bf16 v[64:79], v[128:131], v[192:195], v[64:79]\n\t"
        "ds_read_b64_tr_b16 v[128:129], v228 offset:24576\n\t"
        "ds_read_b64_tr_b16 v[130:131], v228 offset:25600\n\t"
        "s_waitcnt lgkmcnt(14)\n\t"
        "v_mfma_f32_32x32x16_bf16 v[80:95], v[132:135], v[192:195], v[80:95]\n\t"
        "ds_read_b64_tr_b16 v[132:133], v229 offset:24576\n\t"
        "ds_read_b64_tr_b16 v[134:135], v229 offset:25600\n\t"
        "s_add_u32 s45, s45, 0x1000\n\t"
        "buffer_load_dwordx4 v[176:179], v225, s[40:43], s45 offen offset:0 sc1\n\t"
        "s_waitcnt vmcnt(11) lgkmcnt(14)\n\t"
        "v_mfma_f32_32x32x16_bf16 v[64:79], v[136:139], v[196:199], v[64:79]\n\t"
        "ds_read_b64_tr_b16 v[136:137], v228 offset:26624\n\t"
        "ds_read_b64_tr_b16 v[138:139], v228 offset:27648\n\t"
        "s_waitcnt lgkmcnt(14)\n\t"
        "v_mfma_f32_32x32x16_bf16 v[80:95], v[140:143], v[196:199], v[80:95]\n\t"
        "ds_read_b64_tr_b16 v[140:141], v229 offset:26624\n\t"
        "ds_read_b64_tr_b16 v[142:143], v229 offset:27648\n\t"
        "buffer_load_dwordx4 v[180:183], v225, s[40:43], s45 offen offset:1024 sc1\n\t"
        "s_waitcnt vmcnt(11) lgkmcnt(14)\n\t"
        "v_mfma_f32_32x32x16_bf16 v[64:79], v[144:147], v[200:203], v[64:79]\n\t"
        "ds_read_b64_tr_b16 v[144:145], v228 offset:28672\n\t"
        "ds_read_b64_tr_b16 v[146:147], v228 offset:29696\n\t"
        "s_waitcnt lgkmcnt(14)\n\t"
        "v_mfma_f32_32x32x16_bf16 v[80:95], v[148:151], v[200:203], v[80:95]\n\t"
        "ds_read_b64_tr_b16 v[148:149], v229 offset:28672\n\t"
        "ds_read_b64_tr_b16 v[150:151], v229 offset:29696\n\t"
        "buffer_load_dwordx4 v[184:187], v225, s[40:43], s45 offen offset:2048 sc1\n\t"
        "s_waitcnt vmcnt(11) lgkmcnt(14)\n\t"
        "v_mfma_f32_32x32x16_bf16 v[64:79], v[152:155], v[204:207], v[64:79]\n\t"
        "ds_read_b64_tr_b16 v[152:153], v228 offset:30720\n\t"
        "ds_read_b64_tr_b16 v[154:155], v228 offset:31744\n\t"
        "s_waitcnt lgkmcnt(14)\n\t"
        "v_mfma_f32_32x32x16_bf16 v[80:95], v[156:159], v[204:207], v[80:95]\n\t"
        "ds_read_b64_tr_b16 v[156:157], v229 offset:30720\n\t"
        "ds_read_b64_tr_b16 v[158:159], v229 offset:31744\n\t"
        "buffer_load_dwordx4 v[188:191], v225, s[40:43], s45 offen offset:3072 sc1\n\t"
        "s_waitcnt vmcnt(11) lgkmcnt(14)\n\t"
        "v_mfma_f32_32x32x16_bf16 v[64:79], v[128:131], v[208:211], v[64:79]\n\t"
        "ds_read_b64_tr_b16 v[128:129], v230 offset:0\n\t"
        "ds_read_b64_tr_b16 v[130:131], v230 offset:1024\n\t"
        "s_waitcnt lgkmcnt(14)\n\t"
        "v_mfma_f32_32x32x16_bf16 v[80:95], v[132:135], v[208:211], v[80:95]\n\t"
        "ds_read_b64_tr_b16 v[132:133], v231 offset:0\n\t"
        "ds_read_b64_tr_b16 v[134:135], v231 offset:1024\n\t"
        "s_add_u32 s45, s45, 0x1000\n\t"
        "buffer_load_dwordx4 v[192:195], v225, s[40:43], s45 offen offset:0 sc1\n\t"
        "s_waitcnt vmcnt(11) lgkmcnt(14)\n\t"
        "v_mfma_f32_32x32x16_bf16 v[64:79], v[136:139], v[212:215], v[64:79]\n\t"
        "ds_read_b64_tr_b16 v[136:137], v230 offset:2048\n\t"
        "ds_read_b64_tr_b16 v[138:139], v230 offset:3072\n\t"
        "s_waitcnt lgkmcnt(14)\n\t"
        "v_mfma_f32_32x32x16_bf16 v[80:95], v[140:143], v[212:215], v[80:95]\n\t"
        "ds_read_b64_tr_b16 v[140:141], v231 offset:2048\n\t"
        "ds_read_b64_tr_b16 v[142:143], v231 offset:3072\n\t"
        "buffer_load_dwordx4 v[196:199], v225, s[40:43], s45 offen offset:1024 sc1\n\t"
        "s_waitcnt vmcnt(11) lgkmcnt(14)\n\t"
        "v_mfma_f32_32x32x16_bf16 v[64:79], v[144:147], v[216:219], v[64:79]\n\t"
        "ds_read_b64_tr_b16 v[144:145], v230 offset:4096\n\t"
        "ds_read_b64_tr_b16 v[146:147], v230 offset:5120\n\t"
        "s_waitcnt lgkmcnt(14)\n\t"
        "v_mfma_f32_32x32x16_bf16 v[80:95], v[148:151], v[216:219], v[80:95]\n\t"
        "ds_read_b64_tr_b16 v[148:149], v231 offset:4096\n\t"
        "ds_read_b64_tr_b16 v[150:151], v231 offset:5120\n\t"
        "buffer_load_dwordx4 v[200:203], v225, s[40:43], s45 offen offset:2048 sc1\n\t"
        "s_waitcnt vmcnt(11) lgkmcnt(14)\n\t"
        "v_mfma_f32_32x32x16_bf16 v[64:79], v[152:155], v[220:223], v[64:79]\n\t"
        "ds_read_b64_tr_b16 v[152:153], v230 offset:6144\n\t"
        "ds_read_b64_tr_b16 v[154:155], v230 offset:7168\n\t"
        "s_waitcnt lgkmcnt(14)\n\t"
        "v_mfma_f32_32x32x16_bf16 v[80:95], v[156:159], v[220:223], v[80:95]\n\t"
        "ds_read_b64_tr_b16 v[156:157], v231 offset:6144\n\t"
        "ds_read_b64_tr_b16 v[158:159], v231 offset:7168\n\t"
        "buffer_load_dwordx4 v[204:207], v225, s[40:43], s45 offen offset:3072 sc1\n\t"
        "s_mov_b32 s44, s45\n\t"
        "s_cmp_eq_u32 s47, 0\n\t"
        "s_cbranch_scc1 oseg0_%=\n\t"
        "s_cmp_eq_u32 s47, 1\n\t"
        "s_cbranch_scc1 oseg1_%=\n\t"
        "s_cmp_eq_u32 s47, 2\n\t"
        "s_cbranch_scc1 oseg2_%=\n\t"
        "s_cmp_eq_u32 s47, 3\n\t"
        "s_cbranch_scc1 oseg3_%=\n\t"
        "s_cmp_lg_u32 s46, 15\n\t"
        "s_cbranch_scc1 ostart_%=\n\t"
        "s_branch oend_%=\n\t"
        "oseg3_%=:\n\t"
        "s_or_b32 s46, s46, 8\n\t"
        "s_waitcnt vmcnt(11) lgkmcnt(14)\n\t"
        "v_mfma_f32_32x32x16_bf16 v[96:111], v[128:131], v[160:163], 0\n\t"
        "ds_read_b64_tr_b16 v[128:129], v228 offset:40960\n\t"
        "ds_read_b64_tr_b16 v[130:131], v228 offset:41984\n\t"
        "s_waitcnt lgkmcnt(14)\n\t"
        "v_mfma_f32_32x32x16_bf16 v[112:127], v[132:135], v[160:163], 0\n\t"
        "ds_read_b64_tr_b16 v[132:133], v229 offset:40960\n\t"
        "ds_read_b64_tr_b16 v[134:135], v229 offset:41984\n\t"
        "s_add_u32 s44, s44, 0x1000\n\t"
        "buffer_load_dwordx4 v[208:211], v224, s[40:43], s44 offen offset:0 sc1\n\t"
        "s_waitcnt vmcnt(11) lgkmcnt(14)\n\t"
        "v_mfma_f32_32x32x16_bf16 v[96:111], v[136:139], v[164:167], v[96:111]\n\t"
        "ds_read_b32 v233, v232\n\t"
        "ds_read_b64_tr_b16 v[136:137], v228 offset:43008\n\t"
        "ds_read_b64_tr_b16 v[138:139], v228 offset:44032\n\t"
        "s_waitcnt lgkmcnt(14)\n\t"
        "v_mfma_f32_32x32x16_bf16 v[112:127], v[140:143], v[164:167], v[112:127]\n\t"
        "ds_read_b64_tr_b16 v[140:141], v229 offset:43008\n\t"
        "ds_read_b64_tr_b16 v[142:143], v229 offset:44032\n\t"
        "buffer_load_dwordx4 v[212:215], v224, s[40:43], s44 offen offset:1024 sc1\n\t"
        "s_waitcnt vmcnt(11) lgkmcnt(14)\n\t"
        "v_mfma_f32_32x32x16_bf16 v[96:111], v[144:147], v[168:171], v[96:111]\n\t"
        "ds_read_b64_tr_b16 v[144:145], v228 offset:45056\n\t"
        "ds_read_b64_tr_b16 v[146:147], v228 offset:46080\n\t"
        "s_waitcnt lgkmcnt(14)\n\t"
        "v_mfma_f32_32x32x16_bf16 v[112:127], v[148:151], v[168:171], v[112:127]\n\t"
        "ds_read_b64_tr_b16 v[148:149], v229 offset:45056\n\t"
        "ds_read_b64_tr_b16 v[150:151], v229 offset:46080\n\t"
        "buffer_load_dwordx4 v[216:219], v224, s[40:43], s44 offen offset:2048 sc1\n\t"
        "s_waitcnt vmcnt(11) lgkmcnt(8)\n\t"
        "v_mfma_f32_32x32x16_bf16 v[96:111], v[152:155], v[172:175], v[96:111]\n\t"
        "v_readfirstlane_b32 s51, v233\n\t"
        "ds_read_b64_tr_b16 v[152:153], v228 offset:47104\n\t"
        "ds_read_b64_tr_b16 v[154:155], v228 offset:48128\n\t"
        "s_waitcnt lgkmcnt(14)\n\t"
        "v_mfma_f32_32x32x16_bf16 v[112:127], v[156:159], v[172:175], v[112:127]\n\t"
        "s_andn2_b32 s51, s51, s46\n\t"
        "s_ff1_i32_b32 s47, s51\n\t"
        "s_add_u32 s45, s47, s49\n\t"
        "s_and_b32 s45, s45, 3\n\t"
        "s_lshl_b32 s45, s45, 14\n\t"
        "ds_read_b64_tr_b16 v[156:157], v229 offset:47104\n\t"
        "ds_read_b64_tr_b16 v[158:159], v229 offset:48128\n\t"
        "buffer_load_dwordx4 v[220:223], v224, s[40:43], s44 offen offset:3072 sc1\n\t"
        "s_waitcnt vmcnt(11) lgkmcnt(14)\n\t"
        "v_mfma_f32_32x32x16_bf16 v[96:111], v[128:131], v[176:179], v[96:111]\n\t"
        "s_and_b32 s51, s47, 0x80000000\n\t"
        "v_or_b32 v225, s51, v224\n\t"
        "s_and_b32 s51, s47, 3\n\t"
        "s_lshl_b32 s51, s51, 15\n\t"
        "v_add_u32 v230, s51, v226\n\t"
        "v_add_u32 v231, s51, v227\n\t"
        "ds_read_b64_tr_b16 v[128:129], v228 offset:49152\n\t"
        "ds_read_b64_tr_b16 v[130:131], v228 offset:50176\n\t"
        "s_waitcnt lgkmcnt(14)\n\t"
        "v_mfma_f32_32x32x16_bf16 v[112:127], v[132:135], v[176:179], v[112:127]\n\t"
        "ds_read_b64_tr_b16 v[132:133], v229 offset:49152\n\t"
        "ds_read_b64_tr_b16 v[134:135], v229 offset:50176\n\t"
        "buffer_load_dwordx4 v[160:163], v225, s[40:43], s45 offen offset:0 sc1\n\t"
        "s_waitcnt vmcnt(11) lgkmcnt(14)\n\t"
        "v_mfma_f32_32x32x16_bf16 v[96:111], v[136:139], v[180:183], v[96:111]\n\t"
        "ds_read_b64_tr_b16 v[136:137], v228 offset:51200\n\t"
        "ds_read_b64_tr_b16 v[138:139], v228 offset:52224\n\t"
        "s_waitcnt lgkmcnt(14)\n\t"
        "v_mfma_f32_32x32x16_bf16 v[112:127], v[140:143], v[180:183], v[112:127]\n\t"
        "ds_read_b64_tr_b16 v[140:141], v229 offset:51200\n\t"
        "ds_read_b64_tr_b16 v[142:143], v229 offset:52224\n\t"
        "buffer_load_dwordx4 v[164:167], v225, s[40:43], s45 offen offset:1024 sc1\n\t"
        "s_waitcnt vmcnt(11) lgkmcnt(14)\n\t"
        "v_mfma_f32_32x32x16_bf16 v[96:111], v[144:147], v[184:187], v[96:111]\n\t"
        "ds_read_b64_tr_b16 v[144:145], v228 offset:53248\n\t"
        "ds_read_b64_tr_b16 v[146:147], v228 offset:54272\n\t"
        "s_waitcnt lgkmcnt(14)\n\t"
        "v_mfma_f32_32x32x16_bf16 v[112:127], v[148:151], v[184:187], v[112:127]\n\t"
        "ds_read_b64_tr_b16 v[148:149], v229 offset:53248\n\t"
        "ds_read_b64_tr_b16 v[150:151], v229 offset:54272\n\t"
        "buffer_load_dwordx4 v[168:171], v225, s[40:43], s45 offen offset:2048 sc1\n\t"
        "s_waitcnt vmcnt(11) lgkmcnt(14)\n\t"
        "v_mfma_f32_32x32x16_bf16 v[96:111], v[152:155], v[188:191], v[96:111]\n\t"
        "ds_read_b64_tr_b16 v[152:153], v228 offset:55296\n\t"
        "ds_read_b64_tr_b16 v[154:155], v228 offset:56320\n\t"
        "s_waitcnt lgkmcnt(14)\n\t"
        "v_mfma_f32_32x32x16_bf16 v[112:127], v[156:159], v[188:191], v[112:127]\n\t"
        "ds_read_b64_tr_b16 v[156:157], v229 offset:55296\n\t"
        "ds_read_b64_tr_b16 v[158:159], v229 offset:56320\n\t"
        "buffer_load_dwordx4 v[172:175], v225, s[40:43], s45 offen offset:3072 sc1\n\t"
        "s_waitcnt vmcnt(11) lgkmcnt(14)\n\t"
        "v_mfma_f32_32x32x16_bf16 v[96:111], v[128:131], v[192:195], v[96:111]\n\t"
        "ds_read_b64_tr_b16 v[128:129], v228 offset:57344\n\t"
        "ds_read_b64_tr_b16 v[130:131], v228 offset:58368\n\t"
        "s_waitcnt lgkmcnt(14)\n\t"
        "v_mfma_f32_32x32x16_bf16 v[112:127], v[132:135], v[192:195], v[112:127]\n\t"
        "ds_read_b64_tr_b16 v[132:133], v229 offset:57344\n\t"
        "ds_read_b64_tr_b16 v[134:135], v229 offset:58368\n\t"
        "s_add_u32 s45, s45, 0x1000\n\t"
        "buffer_load_dwordx4 v[176:179], v225, s[40:43], s45 offen offset:0 sc1\n\t"
        "s_waitcnt vmcnt(11) lgkmcnt(14)\n\t"
        "v_mfma_f32_32x32x16_bf16 v[96:111], v[136:139], v[196:199], v[96:111]\n\t"
        "ds_read_b64_tr_b16 v[136:137], v228 offset:59392\n\t"
        "ds_read_b64_tr_b16 v[138:139], v228 offset:60416\n\t"
        "s_waitcnt lgkmcnt(14)\n\t"
        "v_mfma_f32_32x32x16_bf16 v[112:127], v[140:143], v[196:199], v[112:127]\n\t"
        "ds_read_b64_tr_b16 v[140:141], v229 offset:59392\n\t"
        "ds_read_b64_tr_b16 v[142:143], v229 offset:60416\n\t"
        "buffer_load_dwordx4 v[180:183], v225, s[40:43], s45 offen offset:1024 sc1\n\t"
        "s_waitcnt vmcnt(11) lgkmcnt(14)\n\t"
        "v_mfma_f32_32x32x16_bf16 v[96:111], v[144:147], v[200:203], v[96:111]\n\t"
        "ds_read_b64_tr_b16 v[144:145], v228 offset:61440\n\t"
        "ds_read_b64_tr_b16 v[146:147], v228 offset:62464\n\t"
        "s_waitcnt lgkmcnt(14)\n\t"
        "v_mfma_f32_32x32x16_bf16 v[112:127], v[148:151], v[200:203], v[112:127]\n\t"
        "ds_read_b64_tr_b16 v[148:149], v229 offset:61440\n\t"
        "ds_read_b64_tr_b16 v[150:151], v229 offset:62464\n\t"
        "buffer_load_dwordx4 v[184:187], v225, s[40:43], s45 offen offset:2048 sc1\n\t"
        "s_waitcnt vmcnt(11) lgkmcnt(14)\n\t"
        "v_mfma_f32_32x32x16_bf16 v[96:111], v[152:155], v[204:207], v[96:111]\n\t"
        "ds_read_b64_tr_b16 v[152:153], v228 offset:63488\n\t"
        "ds_read_b64_tr_b16 v[154:155], v228 offset:64512\n\t"
        "s_waitcnt lgkmcnt(14)\n\t"
        "v_mfma_f32_32x32x16_bf16 v[112:127], v[156:159], v[204:207], v[112:127]\n\t"
        "ds_read_b64_tr_b16 v[156:157], v229 offset:63488\n\t"
        "ds_read_b64_tr_b16 v[158:159], v229 offset:64512\n\t"
        "buffer_load_dwordx4 v[188:191], v225, s[40:43], s45 offen offset:3072 sc1\n\t"
        "s_waitcnt vmcnt(11) lgkmcnt(14)\n\t"
        "v_mfma_f32_32x32x16_bf16 v[96:111], v[128:131], v[208:211], v[96:111]\n\t"
        "ds_read_b64_tr_b16 v[128:129], v230 offset:0\n\t"
        "ds_read_b64_tr_b16 v[130:131], v230 offset:1024\n\t"
        "s_waitcnt lgkmcnt(14)\n\t"
        "v_mfma_f32_32x32x16_bf16 v[112:127], v[132:135], v[208:211], v[112:127]\n\t"
        "ds_read_b64_tr_b16 v[132:133], v231 offset:0\n\t"
        "ds_read_b64_tr_b16 v[134:135], v231 offset:1024\n\t"
        "s_add_u32 s45, s45, 0x1000\n\t"
        "buffer_load_dwordx4 v[192:195], v225, s[40:43], s45 offen offset:0 sc1\n\t"
        "s_waitcnt vmcnt(11) lgkmcnt(14)\n\t"
        "v_mfma_f32_32x32x16_bf16 v[96:111], v[136:139], v[212:215], v[96:111]\n\t"
        "ds_read_b64_tr_b16 v[136:137], v230 offset:2048\n\t"
        "ds_read_b64_tr_b16 v[138:139], v230 offset:3072\n\t"
        "s_waitcnt lgkmcnt(14)\n\t"
        "v_mfma_f32_32x32x16_bf16 v[112:127], v[140:143], v[212:215], v[112:127]\n\t"
        "ds_read_b64_tr_b16 v[140:141], v231 offset:2048\n\t"
        "ds_read_b64_tr_b16 v[142:143], v231 offset:3072\n\t"
        "buffer_load_dwordx4 v[196:199], v225, s[40:43], s45 offen offset:1024 sc1\n\t"
        "s_waitcnt vmcnt(11) lgkmcnt(14)\n\t"
        "v_mfma_f32_32x32x16_bf16 v[96:111], v[144:147], v[216:219], v[96:111]\n\t"
        "ds_read_b64_tr_b16 v[144:145], v230 offset:4096\n\t"
        "ds_read_b64_tr_b16 v[146:147], v230 offset:5120\n\t"
        "s_waitcnt lgkmcnt(14)\n\t"
        "v_mfma_f32_32x32x16_bf16 v[112:127], v[148:151], v[216:219], v[112:127]\n\t"
        "ds_read_b64_tr_b16 v[148:149], v231 offset:4096\n\t"
        "ds_read_b64_tr_b16 v[150:151], v231 offset:5120\n\t"
        "buffer_load_dwordx4 v[200:203], v225, s[40:43], s45 offen offset:2048 sc1\n\t"
        "s_waitcnt vmcnt(11) lgkmcnt(14)\n\t"
        "v_mfma_f32_32x32x16_bf16 v[96:111], v[152:155], v[220:223], v[96:111]\n\t"
        "ds_read_b64_tr_b16 v[152:153], v230 offset:6144\n\t"
        "ds_read_b64_tr_b16 v[154:155], v230 offset:7168\n\t"
        "s_waitcnt lgkmcnt(14)\n\t"
        "v_mfma_f32_32x32x16_bf16 v[112:127], v[156:159], v[220:223], v[112:127]\n\t"
        "ds_read_b64_tr_b16 v[156:157], v231 offset:6144\n\t"
        "ds_read_b64_tr_b16 v[158:159], v231 offset:7168\n\t"
        "buffer_load_dwordx4 v[204:207], v225, s[40:43], s45 offen offset:3072 sc1\n\t"
        "s_mov_b32 s44, s45\n\t"
        "s_cmp_eq_u32 s47, 0\n\t"
        "s_cbranch_scc1 oseg0_%=\n\t"
        "s_cmp_eq_u32 s47, 1\n\t"
        "s_cbranch_scc1 oseg1_%=\n\t"
        "s_cmp_eq_u32 s47, 2\n\t"
        "s_cbranch_scc1 oseg2_%=\n\t"
        "s_cmp_eq_u32 s47, 3\n\t"
        "s_cbranch_scc1 oseg3_%=\n\t"
        "s_cmp_lg_u32 s46, 15\n\t"
        "s_cbranch_scc1 ostart_%=\n\t"
        "s_branch oend_%=\n\t"
        "oend_%=:\n\t"
        "s_waitcnt vmcnt(0) lgkmcnt(0)\n\t"
        "s_barrier\n\t"
        "s_nop 3\n\t"
        : "+{v[0:15]}"(acc[0]), "+{v[16:31]}"(acc[1]), "={v[32:47]}"(acc[2]), "={v[48:63]}"(acc[3]), "={v[64:79]}"(acc[4]), "={v[80:95]}"(acc[5]),
          "={v[96:111]}"(acc[6]), "={v[112:127]}"(acc[7]) STAMP_OUTS
        : "{v224}"(OFF_OF + L * (5 * MBY) + tile * TS + lane * 16), "{v226}"(a0), "{v227}"(a1), "{v228}"(a0 + 65536), "{v229}"(a1 + 65536),
          "{v232}"(lbase + L_W + 20 + 8 * L), "{s49}"((tn >> 2) + 1), "{s[40:43]}"(srd)
        : "v128", "v129", "v130", "v131", "v132", "v133", "v134", "v135", "v136", "v137", "v138", "v139", "v140", "v141", "v142", "v143", "v144", "v145", "v146", "v147", "v148", "v149", "v150", "v151", "v152", "v153", "v154", "v155", "v156", "v157", "v158", "v159", "v160", "v161", "v162", "v163", "v164", "v165", "v166", "v167", "v168", "v169", "v170", "v171", "v172", "v173", "v174", "v175", "v176", "v177", "v178", "v179", "v180", "v181", "v182", "v183", "v184", "v185", "v186", "v187", "v188", "v189", "v190", "v191", "v192", "v193", "v194", "v195", "v196", "v197", "v198", "v199", "v200", "v201", "v202", "v203", "v204", "v205", "v206", "v207", "v208", "v209", "v210", "v211", "v212", "v213", "v214", "v215", "v216", "v217", "v218", "v219", "v220", "v221", "v222", "v223", "v225", "v230", "v231", "v233", "s44", "s45", "s46", "s47", "s48", "s51", "vcc", "scc", "memory");
      }
      PHASE_VARS;
      STAMP_PUT(12 + 16 * L);
      STAMPV(15 + 16 * L);
#pragma unroll
      for (int g = 0; g < 8; ++g) wrc[g] = *reinterpret_cast<const u32x2*>(smem + L_WR + g * 16 + hi * 8);
      SBAR();
      float s = 0.f, sq = 0.f, d = 0.f;
      const unsigned xdst = OFF_X1 + tile * TS + tn * 4096 + lane * 16;
#pragma unroll
      for (int i = 0; i < 4; ++i) {
#pragma unroll
        for (int e = 0; e < 4; ++e) {
          const int j = i >> 1, k = 8 * (i & 1) + 2 * e;
          x1p[i][e] = pack2((acc[j][k] + acc[2 + j][k]) + (acc[4 + j][k] + acc[6 + j][k]), (acc[j][k + 1] + acc[2 + j][k + 1]) + (acc[4 + j][k + 1] + acc[6 + j][k + 1]));
        }
        if (L == 0) stg(rs, xdst + i * 1024, x1p[i]);
        SBAR();
      }
#pragma unroll
      for (int g = 0; g < 8; ++g)
#pragma unroll
        for (int e = 0; e < 2; ++e) {
          const unsigned o = x1p[g >> 1][2 * (g & 1) + e];
          s = dot2(o, 0x3f803f80u, s); sq = dot2(o, o, sq); d = dot2(o, wrc[g][e], d);
        }
      const u32x4 pst = {nzf(half_swap_sum(s)), nzf(half_swap_sum(sq)), nzf(half_swap_sum(d)), 0u};
      asm volatile("buffer_store_dwordx4 %0, %1, %2, 0 offen sc1\n\ts_waitcnt vmcnt(1)\n\tds_write_b32 %3, %4"
                   :: "v"(pst), "v"(stats_dst(sx, tile, tn, lane)), "s"(srd), "v"(lbase + L_W + 64 + 4 * wid), "v"(3) : "memory");
    }
    STAMPV(16 + 16 * L);
  }
  {
    PHASE_VARS;
    float S, Q, Dd, mean, var, mb1, rd1;
    __builtin_amdgcn_s_sleep(13);
    row_stats_sum(rs, OFF_SX + SX_LAYER, tile, lane, S, Q, Dd);
    STAMPV(33);
    row_stats_fin(rs, OFF_SX + SX_LAYER + SX_CAND + (tn * 16 + tm) * 128 + wid * 4, smem, 2, tile, lane, S, Q, Dd, U2F(words[0]), x1p, mean, var, mb1, rd1);
    STAMPV(34);
    u32x4 xo[4];
#pragma unroll
    for (int i = 0; i < 4; ++i) {
#pragma unroll
      for (int e = 0; e < 4; ++e) {
        const unsigned h2 = ln_pair(x1p[i][e], mb1, rd1);
        xo[i][e] = pack2(lo16(x1p[i][e]) + lo16(h2), hi16(x1p[i][e]) + hi16(h2));
      }
      *reinterpret_cast<u32x4*>(smem + wid * 4096 + r32 * 128 + ((2 * i + hi) ^ (r32 & 7)) * 16) = fo_swap(xo[i]);
    }
    asm volatile("s_waitcnt lgkmcnt(0)" ::: "memory");
#pragma unroll
    for (int i = 0; i < 4; ++i) { xo[i] = *reinterpret_cast<const u32x4*>(smem + wid * 4096 + i * 1024 + (lane >> 3) * 128 + ((lane ^ (lane >> 3)) & 7) * 16); PIN(xo[i]); }
    u16* dst = out + (size_t)(tile * 32 + (lane >> 3)) * D_ + tn * 64 + (lane & 7) * 8;
    lds_wait_ge(lbase + L_W + 4 + 4 * (lane & 1), 2);
    const int rel = (int)words[3] - tile * 32 - (lane >> 3);
#pragma unroll
    for (int i = 0; i < 4; ++i)
      if (rel != 8 * i) st16(dst + (size_t)i * 8 * D_, xo[i]);
    STAMPV(35);
    STAMP_END;
  }
}

extern "C" __global__ void end_marker() {}

extern "C" void kernel_launch(void* const* d_in, const int* in_sizes, int n_in, void* d_out, int out_size,
                              void* d_ws, size_t ws_size, hipStream_t stream) {
  char* ws = (char*)d_ws;
  mega_kernel<<<256, 512, 0, stream>>>((const u16*)d_in[0], (const u16*)d_in[1], (const u16*)d_in[2], (const u16*)d_in[4], (const u16*)d_in[5],
                                       (const u16*)d_in[6], (const u16*)d_in[7], ws, (u16*)d_out);
}
```

```cpp
#include <hip/hip_runtime.h>
#include <hip/hip_bf16.h>
#include <stdint.h>

typedef unsigned short u16;
typedef short s16x4 __attribute__((ext_vector_type(4)));
typedef short s16x8 __attribute__((ext_vector_type(8)));
typedef float f32x16 __attribute__((ext_vector_type(16)));
typedef float f32x8 __attribute__((ext_vector_type(8)));
typedef float f32x4 __attribute__((ext_vector_type(4)));
typedef float f32x2 __attribute__((ext_vector_type(2)));
typedef unsigned int u32x16 __attribute__((ext_vector_type(16)));
typedef unsigned int u32x8 __attribute__((ext_vector_type(8)));
typedef unsigned int u32x4 __attribute__((ext_vector_type(4)));
typedef unsigned int u32x2 __attribute__((ext_vector_type(2)));
typedef __bf16 bf16x2 __attribute__((ext_vector_type(2)));
typedef __bf16 bf16x8 __attribute__((ext_vector_type(8)));
typedef __bf16 bf16x16 __attribute__((ext_vector_type(16)));

#define DI __device__ __forceinline__
#define LDS_PTR(T, p) ((__attribute__((address_space(3))) T*)(p))
#define BARRIER() asm volatile("s_barrier" ::: "memory")
#define BAR_LGKM() asm volatile("s_waitcnt lgkmcnt(0)\n\ts_barrier" ::: "memory")
#define BAR_ALL() asm volatile("s_waitcnt vmcnt(0) lgkmcnt(0)\n\ts_barrier" ::: "memory")
#define DRAIN() asm volatile("s_waitcnt vmcnt(0)" ::: "memory")
#define MFMA32(a, b, c) __builtin_amdgcn_mfma_f32_32x32x16_bf16(a, b, c, 0, 0, 0)
#define MFMA16(a, b, c) __builtin_amdgcn_mfma_f32_16x16x32_bf16(a, b, c, 0, 0, 0)
#define PIN(x) asm volatile("" : "+v"(x))
#define EX2(x) __builtin_amdgcn_exp2f(x)
#define RCP(x) __builtin_amdgcn_rcpf(x)
#define SBAR() __builtin_amdgcn_sched_barrier(0)

#define STAMP_INIT
#define STAMPV(i)
#define STAMP_END
#define STAMP_DECL
#define STAMP_OUTS
#define STAMP_PUT(i)
constexpr int D_ = 1024, F_ = 3584;

constexpr unsigned MBY = 1u << 20;
constexpr unsigned TS = 65536 + 4096, HS = 262144 + 4096;
constexpr unsigned OFF_HF = 0, OFF_X1 = 5 * MBY;
constexpr unsigned OFF_OF = 10 * MBY;
constexpr unsigned OFF_KV = 20 * MBY;
constexpr unsigned OFF_FL = 40 * MBY;
constexpr unsigned FL_EPOCH = OFF_FL;
constexpr unsigned FL_KV = OFF_FL + 4096;
constexpr unsigned FL_O = FL_KV + 65536;
constexpr unsigned FL_X = FL_O + 65536;
constexpr unsigned OFF_CS = 41 * MBY;
constexpr unsigned OFF_SX = 42 * MBY;
constexpr unsigned SX_ST = 0;
constexpr unsigned SX_ROW = 512 * 1024;
constexpr unsigned SX_CAND = SX_ROW + 256 * 1024;
constexpr unsigned SX_GP = SX_CAND + 32 * 1024;
constexpr unsigned SX_YP = SX_GP + 224 * 1024;
constexpr unsigned SX_LAYER = SX_YP + 224 * 1024;
constexpr unsigned SX_QP = 2 * SX_LAYER;
constexpr unsigned SX_BYTES = SX_QP + 512 * 1024;
static_assert(SX_BYTES <= 6 * 256 * 128 * 16, "sentinel fill");
constexpr unsigned WS_BYTES = 48 * MBY, OOB = 0x80000000u;
constexpr unsigned SENT = 0xFFFFFFFFu;

constexpr int NS = 9, DD = 3, SLOTB = 12288;
constexpr int L_XCH = 122880;
constexpr int L_SL = L_XCH + 8192;
constexpr int L_CAND = L_XCH + 32768;
constexpr int L_AB = L_XCH + 34816;
constexpr int L_FULL = 159744, L_FREE = L_FULL + 512;
constexpr int L_CS = L_FREE + 1024, L_PATCH = L_CS + 768;
constexpr int L_GU = L_PATCH + 768, L_TAG = L_GU + 256, L_WR = L_TAG + 256;
constexpr int L_X2F = L_WR + 128, L_X2P = L_X2F + 128;
constexpr int L_W = L_X2P + 128;
constexpr int LDS_BYTES = 163840;
static_assert(NS * SLOTB <= 54 * 2048 && L_W + 128 <= LDS_BYTES && (NS + 1) * SLOTB <= L_XCH && L_AB + 2048 <= L_FULL, "LDS map");

DI int fresh_lane() { int l; asm volatile("v_mbcnt_lo_u32_b32 %0, -1, 0\n\tv_mbcnt_hi_u32_b32 %0, -1, %0" : "=v"(l)); return l; }
#define LANE_VARS const int lane = fresh_lane(), r32 = lane & 31, hi = lane >> 5, tq = (lane & 15) >> 2, tp = lane & 3, tblk = (lane >> 4) & 1, \
  wrow = 4 * hi + tq, wsw = 4 * ((wrow >> 1) & 1), wch = 2 * tblk + (tp >> 1), c24 = r32 < 24 ? r32 : 23; \
  (void)r32; (void)hi; (void)wsw; (void)wch; (void)c24
DI int fresh_s(int v) { asm volatile("" : "+s"(v)); return v; }
#define PHASE_VARS const int tm = fresh_s(tm_), tn = fresh_s(tn_), wid = fresh_s(wid_), tile = tm * 4 + wid, hh = wid & 1, kh = wid >> 1; (void)tile; (void)hh; (void)kh; LANE_VARS
DI int kpos(int tm, int tn, int i) { return i == 0 ? tn : (tn + 1 + (1 * (3 - (tn & 3)) + i - 1) % 15) & 15; }
DI float bf2f(u16 v) { return __uint_as_float(((unsigned)v) << 16); }
DI unsigned pack2(float lo, float hi) { f32x2 v = {lo, hi}; return __builtin_bit_cast(unsigned, __builtin_convertvector(v, bf16x2)); }
DI float rbf(float f) { return __uint_as_float(pack2(f, 0.f) << 16); }
DI float lo16(unsigned u) { return __uint_as_float(u << 16); }
DI float hi16(unsigned u) { return __uint_as_float(u & 0xffff0000u); }
DI float opq(float r) { asm("" : "+v"(r)); return r; }
DI float fadd(float a, float b) { return opq(a + b); }
DI float fmul(float a, float b) { return opq(a * b); }
DI float ffma(float a, float b, float c) { return opq(__builtin_fmaf(a, b, c)); }
DI float U2F(unsigned u) { return __uint_as_float(u); }
DI float half_swap_sum(float v) {
  auto r = __builtin_amdgcn_permlane32_swap(__float_as_uint(v), __float_as_uint(v), false, false);
  return fadd(U2F(r[0]), U2F(r[1]));
}
DI float half_swap_max(float v) {
  auto r = __builtin_amdgcn_permlane32_swap(__float_as_uint(v), __float_as_uint(v), false, false);
  return fmaxf(__uint_as_float(r[0]), __uint_as_float(r[1]));
}
template <int CTRL> DI unsigned dpp(unsigned v) { return (unsigned)__builtin_amdgcn_update_dpp(0, (int)v, CTRL, 0xf, 0xf, false); }
DI float row_swap_sum(float v) {
  auto r = __builtin_amdgcn_permlane16_swap(__float_as_uint(v), __float_as_uint(v), false, false);
  return fadd(U2F(r[0]), U2F(r[1]));
}
DI float half_sum(float v) {
  v = fadd(v, U2F(dpp<0xB1>(__float_as_uint(v)))); v = fadd(v, U2F(dpp<0x4E>(__float_as_uint(v))));
  v = fadd(v, U2F(dpp<0x141>(__float_as_uint(v)))); v = fadd(v, U2F(dpp<0x140>(__float_as_uint(v))));
  return row_swap_sum(v);
}
DI float wave_sum(float v) { return half_swap_sum(half_sum(v)); }
DI unsigned half_max_u32(unsigned v) {
  v = max(v, dpp<0xB1>(v)); v = max(v, dpp<0x4E>(v)); v = max(v, dpp<0x141>(v)); v = max(v, dpp<0x140>(v));
  auto r = __builtin_amdgcn_permlane16_swap(v, v, false, false);
  return max(r[0], r[1]);
}
DI unsigned wave_max_u32(unsigned v) {
  v = half_max_u32(v);
  auto r = __builtin_amdgcn_permlane32_swap(v, v, false, false);
  return max(r[0], r[1]);
}
DI u32x4 fo_swap(u32x4 v) {
  auto a = __builtin_amdgcn_permlane32_swap(v[0], v[2], false, false);
  auto b = __builtin_amdgcn_permlane32_swap(v[1], v[3], false, false);
  u32x4 o; o[0] = a[0]; o[1] = b[0]; o[2] = a[1]; o[3] = b[1];
  return o;
}
DI void glds4(const void* sb, unsigned vo, unsigned lds_dst) {
  unsigned keep;
  asm volatile("s_mov_b32 %0, m0\n\ts_mov_b32 m0, %3\n\ts_nop 0\n\tglobal_load_lds_dword %1, %2\n\ts_mov_b32 m0, %0"
               : "=&s"(keep) : "v"(vo), "s"(sb), "s"(lds_dst) : "memory");
}
#define GL_ "global_load_lds_dwordx4 "
DI void glds_v4(const void* sb, const unsigned (&v)[4], unsigned m0v) {
  unsigned keep;
  asm volatile("s_mov_b32 %0, m0\n\ts_mov_b32 m0, %6\n\ts_nop 0\n\t" GL_ "%1, %5\n\t" GL_ "%2, %5 offset:1024\n\t" GL_ "%3, %5 offset:2048\n\t"
               GL_ "%4, %5 offset:3072\n\ts_mov_b32 m0, %0" : "=&s"(keep) : "v"(v[0]), "v"(v[1]), "v"(v[2]), "v"(v[3]), "s"(sb), "s"(m0v) : "memory");
}
#define GP_ "\n\ts_sleep %8\n\ts_waitcnt vmcnt(%7)\n\t"
template <int TH, int SL> DI void glds_v4t(const void* sb, const unsigned (&v)[4], unsigned m0v) {
  unsigned keep;
  asm volatile("s_mov_b32 %0, m0\n\ts_mov_b32 m0, %6\n\ts_nop 0\n\t" GL_ "%1, %5" GP_ GL_ "%2, %5 offset:1024" GP_ GL_ "%3, %5 offset:2048" GP_ GL_ "%4, %5 offset:3072" GP_ "s_mov_b32 m0, %0"
               : "=&s"(keep) : "v"(v[0]), "v"(v[1]), "v"(v[2]), "v"(v[3]), "s"(sb), "s"(m0v), "n"(TH), "n"(SL) : "memory");
}
DI void glds_v3(const void* sb, const unsigned (&v)[3], unsigned m0v) {
  unsigned keep;
  asm volatile("s_mov_b32 %0, m0\n\ts_mov_b32 m0, %5\n\ts_nop 0\n\t" GL_ "%1, %4\n\t" GL_ "%2, %4 offset:1024\n\t" GL_ "%3, %4 offset:2048\n\t"
               "s_mov_b32 m0, %0" : "=&s"(keep) : "v"(v[0]), "v"(v[1]), "v"(v[2]), "s"(sb), "s"(m0v) : "memory");
}
DI float dot2(unsigned a, unsigned b, float c) {
  return __builtin_amdgcn_fdot2_f32_bf16(__builtin_bit_cast(bf16x2, a), __builtin_bit_cast(bf16x2, b), c, false);
}
#define CODE_ADDR(sym, out) do { unsigned long long pc_; unsigned off_; \
  asm volatile("s_getpc_b64 %0\n1:\n\ts_mov_b32 %1, " #sym "-1b" : "=s"(pc_), "=s"(off_)); out = (const char*)(pc_ + (long long)(int)off_); } while (0)
DI void st16(void* p, u32x4 v) { asm volatile("global_store_dwordx4 %0, %1, off sc0 sc1\n\ts_nop 1" :: "v"(p), "v"(v) : "memory"); }
DI void ln_scalars(float mean, float var, float& mb, float& den) { mb = rbf(mean); den = rbf(__builtin_amdgcn_sqrtf(rbf(rbf(var) + 1e-5f))); }
DI unsigned ln_pair(unsigned v, float mb, float rd) {
  const unsigned c = pack2(lo16(v) - mb, hi16(v) - mb);
  return pack2(lo16(c) * rd, hi16(c) * rd);
}
DI unsigned ln_pair_s(unsigned v, float nmb, float rd) {
  const unsigned c = pack2(fadd(lo16(v), nmb), fadd(hi16(v), nmb));
  return pack2(fmul(lo16(c), rd), fmul(hi16(c), rd));
}
template <int N, int M> DI f32x4 sum4(const u32x4 (&v)[M]) {
  f32x4 t;
#pragma unroll
  for (int e = 0; e < 4; ++e) {
    t[e] = U2F(v[0][e]);
#pragma unroll
    for (int i = 1; i < N; ++i) t[e] = fadd(t[e], U2F(v[i][e]));
  }
  return t;
}
#define PERM_LO(x, y) __builtin_amdgcn_perm(y, x, 0x05040100u)
#define PERM_HI(x, y) __builtin_amdgcn_perm(y, x, 0x07060302u)
constexpr unsigned SPIN_CAP = 1u << 15;
DI unsigned nz(unsigned v) { return min(v, SENT - 1u); }
DI unsigned nzf(float v) { return nz(__float_as_uint(v)); }
DI unsigned mx4(unsigned m, const u32x4& v) { return max(max(max(m, v[0]), v[1]), max(v[2], v[3])); }
template <int N> DI bool none_sent(const u32x4 (&v)[N]) {
  unsigned m = 0u;
#pragma unroll
  for (int i = 0; i < N; ++i) m = mx4(m, v[i]);
  return m != SENT;
}
typedef __amdgpu_buffer_rsrc_t rsrc_t;
DI u32x4 ldg(rsrc_t rs, unsigned off) { return __builtin_amdgcn_raw_buffer_load_b128(rs, (int)off, 0, 16); }
DI void stg(rsrc_t rs, unsigned off, u32x4 v) { __builtin_amdgcn_raw_buffer_store_b128(v, rs, (int)off, 0, 16); }
DI void poll_pause(bool slow) { if (slow) __builtin_amdgcn_s_sleep(4); asm volatile("s_sleep 1" ::: "memory"); }
template <int N, class LD, class OKF> DI void poll_all(u32x4 (&out)[N], bool slow, LD ld, OKF okf) {
  for (unsigned spins = 0;; ++spins) {
    ld(out);
    SBAR();
    if (__all(okf(out)) || spins > SPIN_CAP) break;
    poll_pause(slow);
  }
}
DI void lds_set(unsigned addr, unsigned v) { asm volatile("s_waitcnt lgkmcnt(0)\n\tds_write_b32 %0, %1" :: "v"(addr), "v"(v) : "memory"); }
template <int SLEEP = 1> DI void lds_wait_ge(unsigned addr, unsigned need) {
  unsigned spin = 0x100000, v;
  asm volatile("1:\n\tds_read_b32 %0, %2\n\ts_sub_u32 %1, %1, 1\n\ts_waitcnt lgkmcnt(0)\n\ts_cbranch_scc1 2f\n\tv_cmp_gt_u32 vcc, %3, %0\n\ts_cbranch_vccz 2f\n\ts_sleep %4\n\ts_branch 1b\n2:"
               : "=&v"(v), "+s"(spin) : "v"(addr), "s"(need), "n"(SLEEP) : "vcc", "scc", "memory");
}
DI s16x8 tr2(const char* p, int second) {
  s16x4 lo = __builtin_amdgcn_ds_read_tr16_b64_v4i16(LDS_PTR(s16x4, p)), hi = __builtin_amdgcn_ds_read_tr16_b64_v4i16(LDS_PTR(s16x4, p + second));
  return __builtin_shufflevector(lo, hi, 0, 1, 2, 3, 4, 5, 6, 7);
}
DI u32x16 cat4(const u32x4& a, const u32x4& b, const u32x4& c, const u32x4& d) {
  const u32x8 x = __builtin_shufflevector(a, b, 0, 1, 2, 3, 4, 5, 6, 7), y = __builtin_shufflevector(c, d, 0, 1, 2, 3, 4, 5, 6, 7);
  return __builtin_shufflevector(x, y, 0, 1, 2, 3, 4, 5, 6, 7, 8, 9, 10, 11, 12, 13, 14, 15);
}

DI unsigned stats_dst(unsigned sx, int tile, int tn, int lane) { return lane < 32 ? sx + SX_ST + tile * 8192 + tn * 512 + lane * 16 : OOB; }
DI void row_stats_sum(rsrc_t rs, unsigned sx, int tile, int lane, float& S, float& Q, float& Dd) {
  const unsigned exb = sx + SX_ST + tile * 8192 + (lane & 31) * 16;
  u32x4 pv[8];
  poll_all<8>(pv, false, [&](u32x4 (&v)[8]) {
#pragma unroll
    for (int i = 0; i < 8; ++i) v[i] = ldg(rs, exb + ((lane >> 5) * 8 + i) * 512);
  }, [&](const u32x4 (&v)[8]) { return none_sent(v); });
  S = 0.f; Q = 0.f; Dd = 0.f;
#pragma unroll
  for (int i = 0; i < 8; ++i) { S += __uint_as_float(pv[i][0]); Q += __uint_as_float(pv[i][1]); Dd += __uint_as_float(pv[i][2]); }
}
DI void row_stats_fin(rsrc_t rs, unsigned kdst, char* smem, int epoch, int tile, int lane, float S, float Q, float Dd, float wsum, const u32x4 (&x1p)[4],
                      float& mean, float& var, float& mb, float& rd) {
  S = half_swap_sum(S); Q = half_swap_sum(Q); Dd = half_swap_sum(Dd);
  mean = S * (1.0f / 1024.0f); var = fmaxf(Q * (1.0f / 1024.0f) - mean * mean, 0.f);
  float den;
  ln_scalars(mean, var, mb, den);
  rd = RCP(den);
  const unsigned rb = pack2((Dd - mb * wsum) * rd, 0.f) & 0xffffu;
  const unsigned mine = (((rb & 0x8000u) ? (~rb & 0xffffu) : (rb | 0x8000u)) << 16) | (unsigned)(2047 - (tile * 32 + (lane & 31)));
  const bool best = half_max_u32(mine) == mine;
  __builtin_amdgcn_raw_buffer_store_b32(mine, rs, (best && lane < 32) ? kdst : OOB, 0, 16);
  if (best) {
    char* c = smem + L_CAND + (tile & 3) * 144;
#pragma unroll
    for (int i = 0; i < 4; ++i) *reinterpret_cast<u32x4*>(c + (2 * i + (lane >> 5)) * 16) = x1p[i];
    *reinterpret_cast<u32x4*>(c + 128) = u32x4{mine, __float_as_uint(mb), __float_as_uint(rd), 0u};
  }
  lds_set((unsigned)(uintptr_t)smem + L_W + 32 + (tile & 3) * 4, epoch);
}

DI void attn_safe(rsrc_t rs, unsigned kvoff, const u32x8& q, const u32x4& ones, f32x16& o) {
  const s16x8 q0 = __builtin_bit_cast(s16x8, __builtin_shufflevector(q, q, 0, 1, 2, 3)), q1 = __builtin_bit_cast(s16x8, __builtin_shufflevector(q, q, 4, 5, 6, 7));
  f32x4 l = {0.f, 0.f, 0.f, 0.f};
#pragma unroll
  for (int e = 0; e < 16; ++e) o[e] = 0.f;
  float m = -INFINITY;
#pragma unroll 1
  for (int kt = 0; kt < 64; ++kt) {
    const unsigned p = kvoff + kt * 4096;
    const u32x4 k0 = ldg(rs, p), k1 = ldg(rs, p + 1024), v0 = ldg(rs, p + 2048), v1 = ldg(rs, p + 3072);
    f32x16 s = {};
    s = MFMA32(__builtin_bit_cast(s16x8, k0), q0, s);
    s = MFMA32(__builtin_bit_cast(s16x8, k1), q1, s);
    float mt = fmaxf(s[0], s[1]);
#pragma unroll
    for (int e = 2; e < 16; ++e) mt = fmaxf(mt, s[e]);
    const float mn = fmaxf(m, half_swap_max(mt));
    const float alpha = EX2(m - mn);
    m = mn;
#pragma unroll
    for (int e = 0; e < 16; ++e) { s[e] = EX2(s[e] - mn); o[e] *= alpha; }
    l[0] *= alpha;
    const bf16x16 pa = __builtin_bit_cast(bf16x16, __builtin_shufflevector(s, s, 0, 1, 2, 3, 4, 5, 6, 7));
    const bf16x16 pb = __builtin_bit_cast(bf16x16, __builtin_shufflevector(s, s, 8, 9, 10, 11, 12, 13, 14, 15));
    o = __builtin_amdgcn_smfmac_f32_32x32x32_bf16(__builtin_bit_cast(bf16x8, v0), pa, o, 0xdddd, 0, 0);
    l = __builtin_amdgcn_smfmac_f32_16x16x64_bf16(__builtin_bit_cast(bf16x8, ones), pa, l, 0xdddd, 0, 0);
    o = __builtin_amdgcn_smfmac_f32_32x32x32_bf16(__builtin_bit_cast(bf16x8, v1), pb, o, 0xdddd, 0, 0);
    l = __builtin_amdgcn_smfmac_f32_16x16x64_bf16(__builtin_bit_cast(bf16x8, ones), pb, l, 0xdddd, 0, 0);
  }
  o *= 1.0f / l[0];
}

extern "C" __global__ __launch_bounds__(512, 2) void mega_kernel(const u16* __restrict__ x0, const u16* __restrict__ wqkv, const u16* __restrict__ wo,
                                                              const u16* __restrict__ w_r, const u16* __restrict__ wg, const u16* __restrict__ wu,
                                                              const u16* __restrict__ wd, char* ws, u16* __restrict__ out) {
  __shared__ __attribute__((aligned(16))) char smem[LDS_BYTES];
  const int wid = __builtin_amdgcn_readfirstlane(threadIdx.x >> 6);
  STAMP_INIT;
  STAMPV(0);
  const int bid = blockIdx.x, xcd = bid & 7, loc = bid >> 3;
  const int tm = 8 * (xcd >> 2) + (loc >> 2), tn = 4 * (xcd & 3) + (loc & 3);
  const unsigned lbase = (unsigned)(uintptr_t)smem;
  const rsrc_t rs = __builtin_amdgcn_make_buffer_rsrc(ws, 0, (int)WS_BYTES, 0x00020000);
  volatile __attribute__((address_space(3))) unsigned* words = LDS_PTR(volatile unsigned, smem + L_W);
  u32x4 v[4];
  if (wid < 4)
    asm volatile("global_load_dwordx4 %0, %4, %5\n\tglobal_load_dwordx4 %1, %4, %5 offset:512\n\tglobal_load_dwordx4 %2, %4, %5 offset:1024\n\tglobal_load_dwordx4 %3, %4, %5 offset:1536"
                 : "=&v"(v[0]), "=&v"(v[1]), "=&v"(v[2]), "=&v"(v[3])
                 : "v"((threadIdx.x >> 5) * (D_ * 2) + (threadIdx.x & 31) * 16), "s"(x0 + (size_t)(tm * 128 + tn * 8) * D_) : "memory");
  else {
    asm volatile("" : "=v"(v[0]), "=v"(v[1]), "=v"(v[2]), "=v"(v[3]));
    if (wid == 6) {
      const char *c0, *c1;
      CODE_ADDR(mega_kernel, c0); CODE_ADDR(end_marker, c1);
      const unsigned o = (loc * 64 + (threadIdx.x & 63)) * 32;
      glds4(c0, (long long)o + 4 <= c1 - c0 ? o : 0u, lbase + NS * SLOTB);
    }
  }
  STAMPV(48);
  *reinterpret_cast<unsigned*>(smem + L_FULL + threadIdx.x * 4) = 0u;
  words[threadIdx.x & 31] = 0u;
  BAR_LGKM();
  if (wid < 4) {
    const int tid = threadIdx.x, lane = tid & 63, r32 = lane & 31, row = 2 * wid + (lane >> 5);
    char* sm = smem + L_XCH;
    unsigned dst = OFF_HF + (tm * 4 + (tn >> 2)) * TS + (tn & 3) * 128 + (tid >> 3) * 512 + (tid & 7) * 16;
    asm volatile("s_waitcnt vmcnt(0)" : "+v"(v[0]), "+v"(v[1]), "+v"(v[2]), "+v"(v[3]), "+v"(dst) :: "memory");
    STAMPV(49);
    float s = 0.f, s2 = 0.f;
#pragma unroll
    for (int i = 0; i < 4; ++i)
#pragma unroll
      for (int e = 0; e < 4; e += 2) { s = dot2(v[i][e], 0x3f803f80u, s); s2 = dot2(v[i][e + 1], 0x3f803f80u, s2); }
    const float mean = half_sum(s + s2) * (1.0f / 1024.0f);
    f32x2 m2 = {0.f, 0.f};
#pragma unroll
    for (int i = 0; i < 4; ++i)
#pragma unroll
      for (int e = 0; e < 4; ++e) { const f32x2 d = f32x2{lo16(v[i][e]), hi16(v[i][e])} - mean; m2 += d * d; }
    float mb, den;
    ln_scalars(mean, half_sum(m2[0] + m2[1]) * (1.0f / 1024.0f), mb, den);
    const float rd = 1.0f / den;
#pragma unroll
    for (int i = 0; i < 4; ++i) {
      char* p = sm + ((16 * i + (r32 >> 1)) * 2) * 144 + row * 16 + (r32 & 1) * 8;
      *reinterpret_cast<u32x2*>(p) = u32x2{ln_pair(v[i][0], mb, rd), ln_pair(v[i][1], mb, rd)};
      *reinterpret_cast<u32x2*>(p + 144) = u32x2{ln_pair(v[i][2], mb, rd), ln_pair(v[i][3], mb, rd)};
    }
    STAMPV(37);
    lds_set(lbase + L_W + 64 + 4 * wid, 1);
    lds_wait_ge<0>(lbase + L_W + 64 + 4 * (lane & 3), 1);
    STAMPV(50);
    u32x4 t[4];
#pragma unroll
    for (int i = 0; i < 4; ++i) t[i] = *reinterpret_cast<const u32x4*>(sm + (i * 32 + (tid >> 3)) * 144 + (tid & 7) * 16);
    SBAR();
#pragma unroll
    for (int i = 0; i < 4; ++i) stg(rs, dst + i * 16384, t[i]);
    STAMPV(51);
    DRAIN();
    lds_set(lbase + L_W + 64 + 4 * wid, 2);
    STAMPV(38);
  }
  const int lw = wid & 1;
  const char* wb = reinterpret_cast<const char*>(wqkv + (size_t)(16 * lw) * (3 * D_) + tn * 64);
  auto stage_lanes = [](unsigned (&wv)[3]) {
    const int lane = fresh_lane();
#pragma unroll
    for (int i = 0; i < 3; ++i) {
      const int li = lane + 64 * i, k = li / 24, cp = li % 24, c = cp ^ (4 * ((k >> 1) & 1));
      wv[i] = (unsigned)(k * (3 * D_ * 2) + (c >> 3) * (D_ * 2) + (c & 7) * 16 - i * 1024);
    }
  };
  auto stage = [&](const unsigned (&wv)[3], int s, int slot) {
    const char* p = wb + (size_t)(2 * kpos(tm, tn, s >> 1) + (s & 1)) * (32 * 3 * D_ * 2);
    glds_v3(p, wv, lbase + slot * SLOTB + lw * 6144);
    glds_v3(p + 8 * (3 * D_ * 2), wv, lbase + slot * SLOTB + lw * 6144 + 3072);
  };
  auto exchange = [&](const u32x2& tag, unsigned line0, int me, bool bar = false) {
    const int lane = fresh_lane();
    __builtin_amdgcn_raw_buffer_store_b64(tag, rs, lane == 0 ? line0 + me * 128 : OOB, 0, 16);
    if (bar) BARRIER();
    unsigned po = lane < 16 ? line0 + lane * 128 : OOB;
    const u32x4 srd = {(unsigned)(uintptr_t)ws, (unsigned)((uintptr_t)ws >> 32) & 0xffffu, WS_BYTES, 0x00020000u};
    for (unsigned spins = 0;; ++spins) {
      u32x2 v = {0u, 0u};
      asm volatile("buffer_atomic_or_x2 %0, %1, %2, 0 offen sc0 sc1\n\ts_waitcnt vmcnt(0)" : "+v"(v) : "v"(po), "s"(srd) : "memory");
      po = ((v[0] == tag[0]) & (v[1] == tag[1])) ? OOB : po;
      if (__all(po == OOB) || spins > SPIN_CAP) break;
    }
  };
  if ((wid >> 1) == 2) {
    unsigned wv[3];
    stage_lanes(wv);
    const int r = tm & 7;
#pragma unroll 1
    for (int i = 0; i < NS; ++i) {
      const int s = i == 0 ? r : i <= r ? i - 1 : i;
      stage(wv, s, s);
      const int j = i - 1, full = j < 1 ? (j == 0 && r == 0) : j < r ? j : j + 1;
      asm volatile("s_waitcnt vmcnt(%2)\n\tds_write_b32 %0, %1" :: "v"(lbase + L_FULL + lw * 256), "v"(full), "n"(6 * 1) : "memory");
    }
    asm volatile("s_waitcnt vmcnt(0)\n\tds_write_b32 %0, %1" :: "v"(lbase + L_FULL + lw * 256), "v"(NS) : "memory");
    STAMPV(1);
  }
  if (wid == 7) {
    u32x2 tag = __builtin_amdgcn_raw_buffer_load_b64(rs, FL_EPOCH, 0, 16);
    tag[0] += 1u; tag[1] += 0x9E3779B9u;
    *reinterpret_cast<u32x2*>(smem + L_TAG) = tag;
    lds_wait_ge<0>(lbase + L_W + 64 + 4 * (fresh_lane() & 3), 2);
    exchange(tag, FL_X + tm * 2048, tn);
    lds_set(lbase + L_W + 24, 1);
  }
  if (wid >= 4) {
    auto wo_rows = [&](int g0, int n, int gs) {
      const int lane = fresh_lane();
      unsigned wov[4];
#pragma unroll
      for (int i = 0; i < 4; ++i) wov[i] = (unsigned)((8 * i + (lane >> 3)) * (D_ * 2) + ((lane & 7) ^ (4 * ((lane >> 4) & 1))) * 16 - i * 1024);
#pragma unroll 1
      for (int i = 0, k = (0 * (tm & 7)) % n; i < n; ++i, k = k + 1 == n ? 0 : k + 1) {
        const int g = g0 + gs * k;
        glds_v4t<3, 1>(reinterpret_cast<const char*>(wo) + (size_t)((((tn >> 2) + 1) * 256 + 32 * g) & 1023) * (D_ * 2) + tn * 128, wov, lbase + g * 4096);
      }
    };
    if (wid < 6) {
      const int lane = fresh_lane();
      unsigned wv[3];
      stage_lanes(wv);
      __builtin_amdgcn_s_setprio(3);
      const unsigned fra = lbase + L_FREE + (lane & 3) * 256, fla = lbase + L_FULL + lw * 256 + lane * 4;
      u32x4 pre[NS * 6];
#pragma unroll
      for (int i = 0; i < NS * 6; ++i) pre[i] = *reinterpret_cast<const u32x4*>(smem + lw * 6144 + lane * 16 + (i / 6) * SLOTB + (i % 6) * 1024);
#pragma unroll
      for (int i = 0; i < NS * 6; ++i) PIN(pre[i]);
#pragma unroll 1
      for (int L = 0; L < 2; ++L) {
        const int s0 = NS, lead = L ? 8 : NS - 1;
        int slot = 0;
#pragma unroll 1
        for (int s = NS; s < 32; ++s) {
          lds_wait_ge(fra, 32 * L + s - lead);
          stage(wv, s, slot);
          slot = (slot == NS - 1) ? 0 : slot + 1;
          if (s >= s0 + DD) asm volatile("s_waitcnt vmcnt(%0)\n\tds_write_b32 %1, %2" :: "n"(DD * 6), "v"(fla), "v"(32 * L + s - DD + 1) : "memory");
        }
#pragma unroll
        for (int d = DD - 1; d >= 0; --d) asm volatile("s_waitcnt vmcnt(%0)\n\tds_write_b32 %1, %2" :: "n"(d * 6), "v"(fla), "v"(32 * L + 32 - d) : "memory");
        u32x2 tag = *reinterpret_cast<const u32x2*>(smem + L_TAG);
        PIN(tag);
        if (lw == 0) {
          lds_wait_ge(lbase + L_W + 96 + 4 * (lane & 1), 1);
          lds_wait_ge<0>(lbase + L_W + 80 + 4 * (lane & 3), 2 * L + 1);
          exchange(tag, FL_KV + (L * 16 + tn) * 2048, tm, true);
          lds_wait_ge(fra, 32 * L + 32);
          lds_set(lbase + L_W + 16, L + 1);
        } else { BAR_LGKM(); lds_wait_ge<8>(lbase + L_W + 16, L + 1); }
        __builtin_amdgcn_s_sleep(24);
        wo_rows(lw, 15, 2);
        STAMPV(7 + 16 * L);
        DRAIN();
        const unsigned fo = FL_O + (L * 16 + tm) * 2048;
        if (lw) {
          lds_set(lbase + L_W + 112, 2 * L + 1);
          lds_wait_ge<4>(lbase + L_W + 48 + 4 * (lane & 3), L + 1);
          lds_wait_ge<0>(lbase + L_W + 80 + 4 * (lane & 3), 2 * L + 2);
          __builtin_amdgcn_raw_buffer_store_b64(tag, rs, lane == 0 ? fo + tn * 128 : OOB, 0, 16);
          wo_rows(30, 2, 1);
          DRAIN();
          lds_set(lbase + L_W + 112, 2 * L + 2);
        } else {
          lds_wait_ge<4>(lbase + L_W + 112, 2 * L + 1);
          lds_wait_ge<4>(lbase + L_W + 48 + 4 * (lane & 3), L + 1);
          unsigned po = lane < 16 ? fo + ((lane + 4 + (tn & 12)) & 15) * 128 : OOB;
          const u32x4 srd = {(unsigned)(uintptr_t)ws, (unsigned)((uintptr_t)ws >> 32) & 0xffffu, WS_BYTES, 0x00020000u};
          for (unsigned spins = 0;; ++spins) {
            u32x2 v = {0u, 0u};
            asm volatile("buffer_atomic_or_x2 %0, %1, %2, 0 offen sc0 sc1\n\ts_waitcnt vmcnt(0)" : "+v"(v) : "v"(po), "s"(srd) : "memory");
            po = ((v[0] == tag[0]) & (v[1] == tag[1])) ? OOB : po;
            unsigned m = (unsigned)__ballot(po == OOB);
            m &= m >> 1; m &= m >> 2;
            m = (m & 1u) | ((m >> 3) & 2u) | ((m >> 6) & 4u) | ((m >> 9) & 8u);
            if (m == 15u || spins > SPIN_CAP) break;
            words[5 + 2 * L] = words[28] == 2u * L + 2u ? m : m & 7u;
          }
          words[5 + 2 * L] = words[28] == 2u * L + 2u ? 15u : 7u;
          lds_wait_ge<0>(lbase + L_W + 112, 2 * L + 2);
          words[5 + 2 * L] = 15u;
          __builtin_amdgcn_raw_buffer_store_b64(tag, rs, (bid == 0 && lane == 0) ? FL_EPOCH : OOB, 0, 16);
        }
        BAR_ALL();
        if (L == 0) {
          char* d = smem + lw * 6144 + fresh_lane() * 16;
#pragma unroll
          for (int s = 0; s < NS; ++s) {
#pragma unroll
            for (int i = 0; i < 6; ++i) *reinterpret_cast<u32x4*>(d + s * SLOTB + i * 1024) = pre[s * 6 + i];
            lds_set(fla, 32 + s + 1);
          }
          STAMPV(2);
          lds_wait_ge<2>(lbase + L_W + 24, 2);
        }
      }
      STAMP_END;
      return;
    }
    const int a = wid - 6;
    const bool ffn = loc < 28;
    const int grp = xcd * 7 + (ffn ? loc >> 2 : 0), qa = (loc & 3) * 2 + a;
    const int kr = loc, cg = xcd;
    const unsigned wa = lbase + L_W;
    lds_wait_ge<2>(wa + 24, 1);
    __builtin_amdgcn_s_sleep(16);
    auto transpose8 = [](u32x4* v) {
      u32x4 t[8];
#pragma unroll
      for (int j = 0; j < 8; ++j)
#pragma unroll
        for (int e = 0; e < 4; ++e) t[j][e] = (j & 1) ? PERM_HI(v[2 * e][j >> 1], v[2 * e + 1][j >> 1]) : PERM_LO(v[2 * e][j >> 1], v[2 * e + 1][j >> 1]);
#pragma unroll
      for (int j = 0; j < 8; ++j) { v[j] = t[j]; PIN(v[j]); }
    };
    u32x4 gf[32];
    u32x4 wdf[16];
    auto wait_go = [&](int L) {
      __builtin_amdgcn_s_setprio(1);
      lds_wait_ge<8>(lbase + L_W + 16, L + 1);
      __builtin_amdgcn_s_sleep(24);
    };
    auto load_wd = [&](unsigned dv) {
      const rsrc_t rd = __builtin_amdgcn_make_buffer_rsrc(const_cast<u16*>(wd) + (size_t)grp * 64 * D_ + 128 * qa, 0, 64 * D_ * 2 - 256 * qa, 0x00020000);
#pragma unroll
      for (int i = 0; i < 16; ++i) {
        wdf[i] = __builtin_amdgcn_raw_buffer_load_b128(rd, dv, (32 * (i >> 3) + (i & 7)) * (D_ * 2), 0);
        asm volatile("s_sleep 2\n\ts_waitcnt vmcnt(2)" ::: "memory");
      }
    };
    {
      LANE_VARS;
      if (a == 0) {
        const u32x4 w0 = *reinterpret_cast<const u32x4*>(w_r + lane * 8), w1 = *reinterpret_cast<const u32x4*>(w_r + 512 + lane * 8);
        float t = 0.f;
#pragma unroll
        for (int e = 0; e < 4; ++e) t = fadd(fadd(t, fadd(lo16(w0[e]), hi16(w0[e]))), fadd(lo16(w1[e]), hi16(w1[e])));
        words[0] = __float_as_uint(wave_sum(t));
        if ((lane >> 3) == (tn & 7)) *reinterpret_cast<u32x4*>(smem + L_WR + (lane & 7) * 16) = tn < 8 ? w0 : w1;
      }
      const u16* p = wqkv + (size_t)(64 * tm + 32 * a + hi) * (3 * D_) + (c24 >> 3) * D_ + tn * 64 + (c24 & 7) * 8;
      float cs[8] = {0.f, 0.f, 0.f, 0.f, 0.f, 0.f, 0.f, 0.f};
      u32x4 v[16];
#pragma unroll
      for (int i = 0; i < 16; ++i) v[i] = *reinterpret_cast<const u32x4*>(p + (size_t)i * (2 * 3 * D_));
      SBAR();
      const u32x4 sv = {SENT, SENT, SENT, SENT};
#pragma unroll
      for (int i = 0; i < 6; ++i) {
        const unsigned o = ((i * 256 + bid) * 128 + a * 64 + lane) * 16;
        stg(rs, o < SX_BYTES ? OFF_SX + o : OOB, sv);
      }

#pragma unroll
      for (int i = 0; i < 16; ++i)
#pragma unroll
        for (int e = 0; e < 4; ++e) { cs[2 * e] = fadd(cs[2 * e], lo16(v[i][e])); cs[2 * e + 1] = fadd(cs[2 * e + 1], hi16(v[i][e])); }
      u32x4 o;
#pragma unroll
      for (int e = 0; e < 4; ++e) { const float x = half_swap_sum(cs[e]), y = half_swap_sum(cs[4 + e]); o[e] = __float_as_uint(hi ? y : x); }
      stg(rs, r32 < 24 ? OFF_CS + (tn * 32 + tm * 2 + a) * 768 + r32 * 32 + hi * 16 : OOB, o);
    }
    {
      DRAIN();
      lds_set(wa + 96 + 4 * a, 1);
      BAR_LGKM();
      LANE_VARS;
      const char* gp = reinterpret_cast<const char*>(((lane & 8) ? wu : wg) + (size_t)(128 * qa + 16 * hi + 4 * tblk) * F_ + 64 * grp + (lane & 7) * 8);
      unsigned dv = (lane >> 4) * (8 * D_ * 2) + (lane & 15) * 16;
      PIN(gp); PIN(dv);
      unsigned sv[3];
#pragma unroll
      for (int i = 0; i < 3; ++i) { const int li = lane + 64 * i, k = li / 24, c = li % 24; sv[i] = (unsigned)(k * (3 * D_ * 2) + (c >> 3) * (D_ * 2) + (c & 7) * 16 - i * 1024); PIN(sv[i]); }
      wait_go(0);
#pragma unroll
      for (int i = 0; i < 32; ++i) {
        gf[i] = *reinterpret_cast<const u32x4*>(gp + (size_t)(32 * (i >> 3) + 8 * ((i >> 2) & 1) + (i & 3)) * (F_ * 2));
        asm volatile("s_sleep 2\n\ts_waitcnt vmcnt(2)" ::: "memory");
      }
      load_wd(dv);
      STAMPV(7);
      if (1) __builtin_amdgcn_s_setprio(0);
      lds_wait_ge<4>(wa + 80 + 4 * (lane & 3), 2);
      __builtin_amdgcn_s_setprio(0);
#pragma unroll
      for (int g = 0; g < 4; ++g) glds_v3(wqkv + (size_t)(32 * kr + 8 * g) * (3 * D_) + (2 * cg + a) * 64, sv, lbase + L_SL + a * 12288 + g * 3072);
      transpose8(gf); transpose8(gf + 8); transpose8(gf + 16); transpose8(gf + 24);
      transpose8(wdf); transpose8(wdf + 8);
      {
        LANE_VARS;
        f32x4 t[2];
#pragma unroll
        for (int c = 0; c < 2; ++c) {
          u32x4 v[8];
#pragma unroll
          for (int i = 0; i < 8; ++i) v[i] = ldg(rs, OFF_CS + (tn * 32 + 16 * c + 2 * i + hi) * 768 + a * 384 + c24 * 16);
          SBAR();
          t[c] = sum4<8>(v);
          SBAR();
        }
#pragma unroll
        for (int e = 0; e < 4; ++e) t[0][e] = half_swap_sum(fadd(t[0][e], t[1][e]));
        if (lane < 24) *reinterpret_cast<f32x4*>(smem + L_CS + a * 384 + lane * 16) = t[0];
      }
      u32x2 tag = *reinterpret_cast<const u32x2*>(smem + L_TAG);
      PIN(tag);
      BAR_ALL();
      if (a) {
        lds_wait_ge<0>(wa + 64 + 4 * (lane & 3), 3);
        exchange(tag, FL_X + (16 + tm) * 2048, tn);
        lds_set(wa + 24, 2);
        STAMPV(12);
      }
    }
#pragma unroll 1
    for (int L = fresh_s(0);; ++L) {
      const unsigned sx = OFF_SX + L * SX_LAYER;
      STAMPV(1 + 16 * L);
      u32x4 hf[4];
      unsigned key;
      { LANE_VARS;
      lds_wait_ge<0>(wa + 32 + 4 * tp, L + 1);
      if (a == 0) {
        const char* cp = smem + L_CAND + (r32 >> 3) * 144;
        const u32x4 c = *reinterpret_cast<const u32x4*>(cp + (lane & 7) * 16), rec = *reinterpret_cast<const u32x4*>(cp + 128);
        u32x4 o;
#pragma unroll
        for (int e = 0; e < 4; ++e) { unsigned h = ln_pair_s(c[e], -U2F(rec[1]), U2F(rec[2])); PIN(h); o[e] = nz(hi ? c[e] : h); }
        stg(rs, sx + SX_ROW + hi * 131072 + (tm * 4 + (r32 >> 3)) * 2048 + tn * 128 + (lane & 7) * 16, o);
        STAMPV(8 + 16 * L);
        lds_wait_ge<0>(wa + 104, L + 1);
        key = __builtin_amdgcn_readfirstlane(words[27]);
      } else {
        const u32x4 srd = {(unsigned)(uintptr_t)ws, (unsigned)((uintptr_t)ws >> 32) & 0xffffu, WS_BYTES, 0x00020000u};
        unsigned po = sx + SX_CAND + (tn * 16 + (lane >> 2)) * 128 + tp * 4;
        key = 0u;
        for (unsigned spins = 0;; ++spins) {
          unsigned v = 0u;
          asm volatile("buffer_atomic_or %0, %1, %2, 0 offen sc0 sc1\n\ts_waitcnt vmcnt(0)" : "+v"(v) : "v"(po), "s"(srd) : "memory");
          key = v == SENT ? key : max(key, v);
          po = v == SENT ? po : OOB;
          if (__all(po == OOB) || spins > SPIN_CAP) break;
        }
        key = __builtin_amdgcn_readfirstlane(wave_max_u32(key));
        words[27] = key;
        words[3] = 2047u - (key & 2047u);
        lds_set(wa + 104, L + 1);
        STAMPV(13 + 16 * L);
      }
      poll_all<4>(hf, false, [&](u32x4 (&t)[4]) {
#pragma unroll
        for (int i = 0; i < 4; ++i) t[i] = ldg(rs, sx + SX_ROW + ((2047 - (int)(key & 2047u)) >> 5) * 2048 + (16 * qa + 4 * i + (lane >> 4)) * 16);
      }, [&](const u32x4 (&t)[4]) { return none_sent(t); });
      if (L == 1) lds_set(wa + 4 + 4 * a, 2);
      }
      STAMPV(2 + 16 * L);
      f32x4 d[8];
      const f32x4 zero = {0.f, 0.f, 0.f, 0.f};
      SBAR();
      __builtin_amdgcn_s_setprio(3);
#pragma unroll
      for (int t = 0; t < 32; ++t) d[t & 7] = MFMA16(__builtin_bit_cast(s16x8, hf[t >> 3]), __builtin_bit_cast(s16x8, gf[t]), t < 8 ? zero : d[t & 7]);
      SBAR();
      __builtin_amdgcn_s_setprio(0);
      LANE_VARS;
      const int idx = 2047 - (int)(key & 2047u), win = idx >> 7;
      const bool owner = win == tm;
      auto pick2 = [&](const f32x4 (&d)[8]) {
        u32x2 o;
#pragma unroll
        for (int e = 0; e < 2; ++e) {
          const float x = tblk ? d[2 + e][0] : d[e][0], y = tblk ? d[6 + e][0] : d[4 + e][0];
          o[e] = nzf(hi ? y : x);
        }
        return o;
      };
      if (ffn) {
        const unsigned ppos = (lane & 15) * 32 + (lane >> 4) * 8;
        __builtin_amdgcn_raw_buffer_store_b64(pick2(d), rs, sx + SX_GP + (grp * 8 + qa) * 512 + ppos, 0, 16);
        STAMPV(9 + 16 * L);
        __builtin_amdgcn_s_sleep(16);
        u32x2 gu[8];
        for (unsigned spins = 0;; ++spins) {
#pragma unroll
          for (int i = 0; i < 8; ++i) gu[i] = __builtin_amdgcn_raw_buffer_load_b64(rs, sx + SX_GP + (grp * 8 + i) * 512 + lane * 8, 0, 16);
          SBAR();
          unsigned m = 0u;
#pragma unroll
          for (int i = 0; i < 8; ++i) m = max(max(m, gu[i][0]), gu[i][1]);
          if (__all(m != SENT) || spins > SPIN_CAP) break;
          poll_pause(L == 0);
        }
        STAMPV(10 + 16 * L);
        float t0 = opq(U2F(gu[0][0])), t1 = opq(U2F(gu[0][1]));
#pragma unroll
        for (int i = 1; i < 8; ++i) { t0 = fadd(t0, U2F(gu[i][0])); t1 = fadd(t1, U2F(gu[i][1])); }
        auto sw = __builtin_amdgcn_permlane32_swap(__float_as_uint(t0), __float_as_uint(t1), false, false);
        const float gv = rbf(U2F(sw[0])), uv = rbf(U2F(sw[1]));
        reinterpret_cast<u16*>(smem + L_GU + a * 128)[2 * r32 + hi] = (u16)pack2(fmul(rbf(fmul(gv, RCP(fadd(1.0f, __expf(-gv))))), uv), 0.f);
        asm volatile("s_waitcnt lgkmcnt(0)" ::: "memory");
        const s16x8 a0 = *reinterpret_cast<const s16x8*>(smem + L_GU + a * 128 + (lane >> 4) * 16), a1 = *reinterpret_cast<const s16x8*>(smem + L_GU + a * 128 + 64 + (lane >> 4) * 16);
        SBAR();
        __builtin_amdgcn_s_setprio(3);
#pragma unroll
        for (int t = 0; t < 16; ++t) d[t & 7] = MFMA16(t < 8 ? a0 : a1, __builtin_bit_cast(s16x8, wdf[t]), t < 8 ? zero : d[t & 7]);
        SBAR();
        __builtin_amdgcn_s_setprio(0);
        __builtin_amdgcn_raw_buffer_store_b64(pick2(d), rs, sx + SX_YP + grp * 4096 + qa * 512 + ppos, 0, 16);
      }
      STAMPV(3 + 16 * L);
      if (L == 1 && (cg != (kr & 7) || a)) { STAMP_END; __builtin_amdgcn_endpgm(); }
      s16x8 wpf[12];
#pragma unroll
      for (int t = 0; t < 12; ++t) wpf[t] = tr2(smem + L_SL + a * 12288 + (16 * hi + 4 * tblk + tq) * 384 + t * 32 + tp * 8, 8 * 384);
#pragma unroll
      for (int t = 0; t < 12; ++t) PIN(wpf[t]);
      if (L == 0 && !owner) lds_set(wa + 4 + 4 * a, 1);
      __builtin_amdgcn_s_sleep(9);
      if (L == 0 && !ffn) __builtin_amdgcn_s_sleep(64);
      const unsigned rk = key >> 16;
      float rsig = rbf(RCP(fadd(1.0f, __expf(-lo16((rk & 0x8000u) ? (rk & 0x7fffu) : (~rk & 0xffffu))))));
      PIN(rsig);
      const int n = 4 * (lane & 7) + ((lane >> 3) & 3);
      const int fo = ((n >> 4) * 2 + ((n >> 2) & 1)) * 8 + ((n >> 3) & 1) * 4 + (n & 3);
      u32x4 pv[8];
      poll_all<8>(pv, L == 0, [&](u32x4 (&t)[8]) {
#pragma unroll
        for (int i = 0; i < 7; ++i) t[i] = ldg(rs, sx + SX_YP + (8 * i + (lane >> 3)) * 4096 + kr * 128 + (lane & 7) * 16);
        t[7][0] = t[7][2] = __builtin_amdgcn_raw_buffer_load_b32(rs, sx + SX_ROW + (idx >> 5) * 2048 + kr * 64 + (fo >> 1) * 4, 0, 16);
        t[7][1] = t[7][3] = __builtin_amdgcn_raw_buffer_load_b32(rs, sx + SX_ROW + 131072 + (idx >> 5) * 2048 + kr * 64 + (fo >> 1) * 4, 0, 16);
      }, [&](const u32x4 (&t)[8]) { return none_sent(t); });
      const float hv = (fo & 1) ? hi16(pv[7][0]) : lo16(pv[7][0]), xv = (fo & 1) ? hi16(pv[7][1]) : lo16(pv[7][1]);
      STAMPV(4 + 16 * L);
      float ys;
      {
        f32x4 t = sum4<7>(pv);
#pragma unroll
        for (int e = 0; e < 4; ++e) {
          t[e] = fadd(t[e], U2F(dpp<0x128>(__float_as_uint(t[e]))));
          t[e] = half_swap_sum(row_swap_sum(t[e]));
        }
        const float x = (lane & 8) ? t[1] : t[0], y = (lane & 8) ? t[3] : t[2];
        ys = tblk ? y : x;
      }
      const unsigned ob = pack2(fadd(xv, rbf(fadd(hv, rbf(fmul(rbf(ys), rsig))))), 0.f) & 0xffffu;
      if (L == 1) {
        if (lane < 32) out[(size_t)idx * D_ + kr * 32 + n] = (u16)ob;
        STAMPV(5 + 16 * L);
        STAMP_END;
        __builtin_amdgcn_endpgm();
      }
      reinterpret_cast<u16*>(smem + L_X2F + a * 64)[fo] = (u16)ob;
      asm volatile("s_waitcnt lgkmcnt(0)" ::: "memory");
      {
        const s16x8 xa = *reinterpret_cast<const s16x8*>(smem + L_X2F + a * 64 + (lane >> 4) * 16);
        const u32x4 xf = *reinterpret_cast<const u32x4*>(smem + L_X2F + a * 64 + tp * 16);
        f32x4 z[12];
        SBAR();
        __builtin_amdgcn_s_setprio(3);
#pragma unroll
        for (int t = 0; t < 12; ++t) z[t] = MFMA16(xa, wpf[t], zero);
        SBAR();
        __builtin_amdgcn_s_setprio(0);
        const float x2 = lo16(ob), s1 = half_sum(x2), s2 = half_sum(fmul(x2, x2));
        u32x4 o;
#pragma unroll
        for (int e = 0; e < 4; ++e) {
          const unsigned sp = lane < 52 ? (e == 0 ? __float_as_uint(s1) : __float_as_uint(s2)) : xf[e];
          const unsigned x = __float_as_uint(tblk ? z[4 + e][0] : z[e][0]), y = tblk ? sp : __float_as_uint(z[8 + e][0]);
          o[e] = nz(hi ? y : x);
        }
        const unsigned pos = lane < 24 ? lane * 16 : lane < 48 ? lane * 16 + 128 : lane < 52 ? (lane & 1) * 512 + 384 : 400 + tp * 16;
        stg(rs, lane < 56 ? OFF_SX + SX_QP + ((2 * cg + a) * 32 + kr) * 1024 + pos : OOB, o);
      }
      STAMPV(5 + 16 * L);
      if (owner) {
        __builtin_amdgcn_s_sleep(13);
        const int c25 = r32 < 25 ? r32 : 24, pl = 24 * a + c24, col = 64 * (pl >> 4) + (pl & 15);
        float cs[4];
#pragma unroll
        for (int e = 0; e < 4; ++e) { cs[e] = reinterpret_cast<const float*>(smem + L_CS)[col + 16 * e]; PIN(cs[e]); }
        u32x4 qv[17];
        poll_all<17>(qv, true, [&](u32x4 (&t)[17]) {
#pragma unroll
          for (int i = 0; i < 16; ++i) t[i] = ldg(rs, OFF_SX + SX_QP + (tn * 32 + 2 * i + hi) * 1024 + a * 512 + c25 * 16);
          t[16] = ldg(rs, OFF_SX + SX_QP + (tn * 34 + hi) * 1024 + 400 + tp * 16);
        }, [&](const u32x4 (&t)[17]) { return none_sent(t); });
        *reinterpret_cast<u32x4*>(smem + L_X2P + hi * 64 + tp * 16) = qv[16];
        f32x4 t = sum4<16>(qv);
#pragma unroll
        for (int e = 0; e < 4; ++e) t[e] = half_swap_sum(t[e]);
        const float mean = fmul(U2F(__builtin_amdgcn_readlane(__float_as_uint(t[0]), 24)), 1.0f / 1024.0f);
        const float var = fmaxf(ffma(-mean, mean, fmul(U2F(__builtin_amdgcn_readlane(__float_as_uint(t[1]), 24)), 1.0f / 1024.0f)), 0.f);
        float mb, den;
        ln_scalars(mean, var, mb, den);
        const float rd = RCP(den);
        if (lane < 24) {
#pragma unroll
          for (int e = 0; e < 4; ++e) reinterpret_cast<float*>(smem + L_PATCH)[col + 16 * e] = fmul(ffma(-mb, cs[e], t[e]), rd);
        }
        lds_set(wa + 4 + 4 * a, L + 1);
      }
      STAMPV(6 + 16 * L);
      BAR_ALL();
      unsigned dv = fresh_lane();
      dv = (dv >> 4) * (8 * D_ * 2) + (dv & 15) * 16;
      PIN(dv);
      wait_go(1);
      load_wd(dv);
      STAMPV(23);
      if (1) __builtin_amdgcn_s_setprio(0);
      lds_wait_ge<4>(wa + 80 + 4 * (fresh_lane() & 3), 4);
      __builtin_amdgcn_s_setprio(0);
      transpose8(wdf); transpose8(wdf + 8);
      BARRIER();
    }
  }
  const int tm_ = tm, tn_ = tn, wid_ = wid;
#define KSKIP "{s49}"(tn * 4096), "{s50}"(((tn + 1) & 15) * 4096)
  const u32x4 srd = {(unsigned)(uintptr_t)ws, (unsigned)((uintptr_t)ws >> 32) & 0xffffu, WS_BYTES, 0x00020000u};
  u32x4 x1p[4];
  asm volatile("" : "=v"(x1p[0]), "=v"(x1p[1]), "=v"(x1p[2]), "=v"(x1p[3]));
#pragma unroll 1
  for (int L = 0; L < 2; ++L) {
    const unsigned sx = OFF_SX + L * SX_LAYER;
    u32x8 q[4];
    u32x16 kva, kvb;
    f32x16 oz[5];
    {
      f32x16 acc[6];
      u32x16 stA, stB, xr0, xr1, xr2, xr3;
      u32x4 stC;
      asm volatile("" : "={v[112:127]}"(stB), "={v[128:131]}"(stC));
      { PHASE_VARS;
      stB[8] = lane * 16 + (L ? OFF_X1 : OFF_HF) + tile * TS;
      stB[9] = lbase + wrow * 384 + (((4 * hh + wch) ^ wsw) << 4) + 8 * (tp & 1);
      stB[10] = lbase + wrow * 384 + (((4 * (hh ^ 1) + wch) ^ wsw) << 4) + 8 * (tp & 1);
      stB[13] = lbase + L_FULL + (lane & 1) * 256;
      stB[15] = lbase + L_FREE + wid * 256 + lane * 4;
      stC[0] = 32 * L; }
      unsigned soff, slot, need = 32 * L + 1;
      if (L == 0) {
        lds_wait_ge<0>(lbase + L_W + 24, 1);
        STAMPV(39);
        soff = tn * 4096;
        asm volatile(
          "buffer_load_dwordx4 v[132:135], v120, s[40:43], s44 offen offset:0 sc1\n\t"
          "buffer_load_dwordx4 v[136:139], v120, s[40:43], s44 offen offset:1024 sc1\n\t"
          "buffer_load_dwordx4 v[140:143], v120, s[40:43], s44 offen offset:2048 sc1\n\t"
          "buffer_load_dwordx4 v[144:147], v120, s[40:43], s44 offen offset:3072 sc1\n\t"
          "s_add_u32 s44, s51, 0\n\t"
          "buffer_load_dwordx4 v[148:151], v120, s[40:43], s44 offen offset:0 sc1\n\t"
          "buffer_load_dwordx4 v[152:155], v120, s[40:43], s44 offen offset:1024 sc1\n\t"
          "buffer_load_dwordx4 v[156:159], v120, s[40:43], s44 offen offset:2048 sc1\n\t"
          "buffer_load_dwordx4 v[160:163], v120, s[40:43], s44 offen offset:3072 sc1\n\t"
          "s_add_u32 s44, s44, 0x1000\n\t"
          "s_and_b32 s44, s44, 0xffff\n\t"
          "s_cmp_eq_u32 s44, s49\n\t"
          "s_cselect_b32 s44, s50, s44\n\t"
          "buffer_load_dwordx4 v[164:167], v120, s[40:43], s44 offen offset:0 sc1\n\t"
          "buffer_load_dwordx4 v[168:171], v120, s[40:43], s44 offen offset:1024 sc1\n\t"
          "buffer_load_dwordx4 v[172:175], v120, s[40:43], s44 offen offset:2048 sc1\n\t"
          "buffer_load_dwordx4 v[176:179], v120, s[40:43], s44 offen offset:3072 sc1\n\t"
          "s_add_u32 s44, s44, 0x1000\n\t"
          "s_and_b32 s44, s44, 0xffff\n\t"
          "s_cmp_eq_u32 s44, s49\n\t"
          "s_cselect_b32 s44, s50, s44\n\t"
          "buffer_load_dwordx4 v[180:183], v120, s[40:43], s44 offen offset:0 sc1\n\t"
          "buffer_load_dwordx4 v[184:187], v120, s[40:43], s44 offen offset:1024 sc1\n\t"
          "buffer_load_dwordx4 v[188:191], v120, s[40:43], s44 offen offset:2048 sc1\n\t"
          "buffer_load_dwordx4 v[192:195], v120, s[40:43], s44 offen offset:3072 sc1\n\t"
          : "={v[132:147]}"(xr0), "={v[148:163]}"(xr1), "={v[164:179]}"(xr2), "={v[180:195]}"(xr3), "+{s44}"(soff) : "{v[112:127]}"(stB), "{s[40:43]}"(srd), KSKIP, "{s51}"(kpos(tm, tn, 1) * 4096) : "scc", "memory");
      } else {
        xr0 = cat4(x1p[0], x1p[1], x1p[2], x1p[3]);
        asm volatile("" : "={v[148:163]}"(xr1), "={v[164:179]}"(xr2), "={v[180:195]}"(xr3));
      }
      STAMPV(1 + 16 * L);
      asm volatile(
        "v_mov_b32 v123, v121\n\t"
        "v_mov_b32 v124, v122\n\t"
        "s_mov_b64 vcc, -1\n\t"
        "s_cbranch_vccnz 10f\n\t"
        "11:\n\t"
        "s_mov_b32 s46, 12288\n\t"
        "s_add_u32 s47, s47, 1\n\t"
        "ds_read_b64_tr_b16 v[96:97], v123 offset:0\n\t"
        "ds_read_b64_tr_b16 v[98:99], v123 offset:3072\n\t"
        "ds_read_b64_tr_b16 v[100:101], v124 offset:0\n\t"
        "ds_read_b64_tr_b16 v[102:103], v124 offset:3072\n\t"
        "ds_read_b64_tr_b16 v[104:105], v123 offset:128\n\t"
        "ds_read_b64_tr_b16 v[106:107], v123 offset:3200\n\t"
        "ds_read_b64_tr_b16 v[108:109], v124 offset:128\n\t"
        "ds_read_b64_tr_b16 v[110:111], v124 offset:3200\n\t"
        "ds_read_b64_tr_b16 v[112:113], v123 offset:256\n\t"
        "ds_read_b64_tr_b16 v[114:115], v123 offset:3328\n\t"
        "ds_write_b32 v127, v128\n\t"
        "ds_read_b64_tr_b16 v[116:117], v124 offset:256\n\t"
        "ds_read_b64_tr_b16 v[118:119], v124 offset:3328\n\t"
        "s_waitcnt vmcnt(15) lgkmcnt(11)\n\t"
        "v_mfma_f32_32x32x16_bf16 v[0:15], v[96:99], v[132:135], 0\n\t"
        "ds_read_b32 v126, v125\n\t"
        "ds_read_b64_tr_b16 v[96:97], v123 offset:6144\n\t"
        "ds_read_b64_tr_b16 v[98:99], v123 offset:9216\n\t"
        "s_waitcnt lgkmcnt(12)\n\t"
        "v_mfma_f32_32x32x16_bf16 v[16:31], v[100:103], v[132:135], 0\n\t"
        "v_add_u32 v129, s46, v121\n\t"
        "ds_read_b64_tr_b16 v[100:101], v124 offset:6144\n\t"
        "ds_read_b64_tr_b16 v[102:103], v124 offset:9216\n\t"
        "s_waitcnt lgkmcnt(12)\n\t"
        "v_mfma_f32_32x32x16_bf16 v[32:47], v[104:107], v[132:135], 0\n\t"
        "ds_read_b64_tr_b16 v[104:105], v123 offset:6272\n\t"
        "ds_read_b64_tr_b16 v[106:107], v123 offset:9344\n\t"
        "s_waitcnt lgkmcnt(12)\n\t"
        "v_mfma_f32_32x32x16_bf16 v[48:63], v[108:111], v[132:135], 0\n\t"
        "v_add_u32 v130, s46, v122\n\t"
        "ds_read_b64_tr_b16 v[108:109], v124 offset:6272\n\t"
        "ds_read_b64_tr_b16 v[110:111], v124 offset:9344\n\t"
        "s_waitcnt lgkmcnt(12)\n\t"
        "v_mfma_f32_32x32x16_bf16 v[64:79], v[132:135], v[112:115], 0\n\t"
        "ds_read_b64_tr_b16 v[112:113], v123 offset:6400\n\t"
        "ds_read_b64_tr_b16 v[114:115], v123 offset:9472\n\t"
        "s_waitcnt lgkmcnt(10)\n\t"
        "v_mfma_f32_32x32x16_bf16 v[80:95], v[132:135], v[116:119], 0\n\t"
        "v_cmp_gt_u32 vcc, s47, v126\n\t"
        "ds_read_b64_tr_b16 v[116:117], v124 offset:6400\n\t"
        "ds_read_b64_tr_b16 v[118:119], v124 offset:9472\n\t"
        "s_waitcnt vmcnt(14) lgkmcnt(10)\n\t"
        "v_mfma_f32_32x32x16_bf16 v[0:15], v[96:99], v[136:139], v[0:15]\n\t"
        "s_cbranch_vccnz 12f\n\t"
        "13:\n\t"
        "ds_read_b64_tr_b16 v[96:97], v129 offset:0\n\t"
        "ds_read_b64_tr_b16 v[98:99], v129 offset:3072\n\t"
        "s_waitcnt lgkmcnt(10)\n\t"
        "v_mfma_f32_32x32x16_bf16 v[16:31], v[100:103], v[136:139], v[16:31]\n\t"
        "v_add_u32 v128, 1, v128\n\t"
        "ds_read_b64_tr_b16 v[100:101], v130 offset:0\n\t"
        "ds_read_b64_tr_b16 v[102:103], v130 offset:3072\n\t"
        "s_waitcnt lgkmcnt(10)\n\t"
        "v_mfma_f32_32x32x16_bf16 v[32:47], v[104:107], v[136:139], v[32:47]\n\t"
        "ds_read_b64_tr_b16 v[104:105], v129 offset:128\n\t"
        "ds_read_b64_tr_b16 v[106:107], v129 offset:3200\n\t"
        "s_waitcnt lgkmcnt(10)\n\t"
        "v_mfma_f32_32x32x16_bf16 v[48:63], v[108:111], v[136:139], v[48:63]\n\t"
        "s_add_u32 s46, s46, 12288\n\t"
        "s_add_u32 s47, s47, 1\n\t"
        "ds_read_b64_tr_b16 v[108:109], v130 offset:128\n\t"
        "ds_read_b64_tr_b16 v[110:111], v130 offset:3200\n\t"
        "s_waitcnt lgkmcnt(10)\n\t"
        "v_mfma_f32_32x32x16_bf16 v[64:79], v[136:139], v[112:115], v[64:79]\n\t"
        "s_cmp_eq_u32 s46, 110592\n\t"
        "s_cselect_b32 s46, 0, s46\n\t"
        "ds_read_b64_tr_b16 v[112:113], v129 offset:256\n\t"
        "ds_read_b64_tr_b16 v[114:115], v129 offset:3328\n\t"
        "s_waitcnt lgkmcnt(10)\n\t"
        "v_mfma_f32_32x32x16_bf16 v[80:95], v[136:139], v[116:119], v[80:95]\n\t"
        "ds_write_b32 v127, v128\n\t"
        "ds_read_b64_tr_b16 v[116:117], v130 offset:256\n\t"
        "ds_read_b64_tr_b16 v[118:119], v130 offset:3328\n\t"
        "s_waitcnt vmcnt(13) lgkmcnt(11)\n\t"
        "v_mfma_f32_32x32x16_bf16 v[0:15], v[96:99], v[140:143], v[0:15]\n\t"
        "ds_read_b32 v126, v125\n\t"
        "ds_read_b64_tr_b16 v[96:97], v129 offset:6144\n\t"
        "ds_read_b64_tr_b16 v[98:99], v129 offset:9216\n\t"
        "s_waitcnt lgkmcnt(12)\n\t"
        "v_mfma_f32_32x32x16_bf16 v[16:31], v[100:103], v[140:143], v[16:31]\n\t"
        "v_add_u32 v123, s46, v121\n\t"
        "ds_read_b64_tr_b16 v[100:101], v130 offset:6144\n\t"
        "ds_read_b64_tr_b16 v[102:103], v130 offset:9216\n\t"
        "s_waitcnt lgkmcnt(12)\n\t"
        "v_mfma_f32_32x32x16_bf16 v[32:47], v[104:107], v[140:143], v[32:47]\n\t"
        "ds_read_b64_tr_b16 v[104:105], v129 offset:6272\n\t"
        "ds_read_b64_tr_b16 v[106:107], v129 offset:9344\n\t"
        "s_waitcnt lgkmcnt(12)\n\t"
        "v_mfma_f32_32x32x16_bf16 v[48:63], v[108:111], v[140:143], v[48:63]\n\t"
        "v_add_u32 v124, s46, v122\n\t"
        "ds_read_b64_tr_b16 v[108:109], v130 offset:6272\n\t"
        "ds_read_b64_tr_b16 v[110:111], v130 offset:9344\n\t"
        "s_waitcnt lgkmcnt(12)\n\t"
        "v_mfma_f32_32x32x16_bf16 v[64:79], v[140:143], v[112:115], v[64:79]\n\t"
        "ds_read_b64_tr_b16 v[112:113], v129 offset:6400\n\t"
        "ds_read_b64_tr_b16 v[114:115], v129 offset:9472\n\t"
        "s_waitcnt lgkmcnt(10)\n\t"
        "v_mfma_f32_32x32x16_bf16 v[80:95], v[140:143], v[116:119], v[80:95]\n\t"
        "v_cmp_gt_u32 vcc, s47, v126\n\t"
        "ds_read_b64_tr_b16 v[116:117], v130 offset:6400\n\t"
        "ds_read_b64_tr_b16 v[118:119], v130 offset:9472\n\t"
        "s_waitcnt vmcnt(12) lgkmcnt(10)\n\t"
        "v_mfma_f32_32x32x16_bf16 v[0:15], v[96:99], v[144:147], v[0:15]\n\t"
        "s_cbranch_vccnz 14f\n\t"
        "15:\n\t"
        "ds_read_b64_tr_b16 v[96:97], v123 offset:0\n\t"
        "ds_read_b64_tr_b16 v[98:99], v123 offset:3072\n\t"
        "s_waitcnt lgkmcnt(10)\n\t"
        "v_mfma_f32_32x32x16_bf16 v[16:31], v[100:103], v[144:147], v[16:31]\n\t"
        "v_add_u32 v128, 1, v128\n\t"
        "ds_read_b64_tr_b16 v[100:101], v124 offset:0\n\t"
        "ds_read_b64_tr_b16 v[102:103], v124 offset:3072\n\t"
        "s_waitcnt lgkmcnt(10)\n\t"
        "v_mfma_f32_32x32x16_bf16 v[32:47], v[104:107], v[144:147], v[32:47]\n\t"
        "ds_read_b64_tr_b16 v[104:105], v123 offset:128\n\t"
        "ds_read_b64_tr_b16 v[106:107], v123 offset:3200\n\t"
        "s_waitcnt lgkmcnt(10)\n\t"
        "v_mfma_f32_32x32x16_bf16 v[48:63], v[108:111], v[144:147], v[48:63]\n\t"
        "s_add_u32 s46, s46, 12288\n\t"
        "s_add_u32 s47, s47, 1\n\t"
        "ds_read_b64_tr_b16 v[108:109], v124 offset:128\n\t"
        "ds_read_b64_tr_b16 v[110:111], v124 offset:3200\n\t"
        "s_waitcnt lgkmcnt(10)\n\t"
        "v_mfma_f32_32x32x16_bf16 v[64:79], v[144:147], v[112:115], v[64:79]\n\t"
        "s_cmp_eq_u32 s46, 110592\n\t"
        "s_cselect_b32 s46, 0, s46\n\t"
        "ds_read_b64_tr_b16 v[112:113], v123 offset:256\n\t"
        "ds_read_b64_tr_b16 v[114:115], v123 offset:3328\n\t"
        "s_waitcnt lgkmcnt(10)\n\t"
        "v_mfma_f32_32x32x16_bf16 v[80:95], v[144:147], v[116:119], v[80:95]\n\t"
        "ds_write_b32 v127, v128\n\t"
        "ds_read_b64_tr_b16 v[116:117], v124 offset:256\n\t"
        "ds_read_b64_tr_b16 v[118:119], v124 offset:3328\n\t"
        "s_waitcnt lgkmcnt(0)\n\t"
        "s_nop 7\n\t"
        "s_branch 9f\n\t"
        "10:\n\t"
        "s_mov_b32 s48, 0x10000\n\t"
        "8:\n\t"
        "ds_read_b32 v126, v125\n\t"
        "s_sub_u32 s48, s48, 1\n\t"
        "s_waitcnt lgkmcnt(0)\n\t"
        "s_cbranch_scc1 11b\n\t"
        "v_cmp_gt_u32 vcc, s47, v126\n\t"
        "s_cbranch_vccnz 8b\n\t"
        "s_branch 11b\n\t"
        "12:\n\t"
        "s_mov_b32 s48, 0x10000\n\t"
        "8:\n\t"
        "ds_read_b32 v126, v125\n\t"
        "s_sub_u32 s48, s48, 1\n\t"
        "s_waitcnt lgkmcnt(0)\n\t"
        "s_cbranch_scc1 13b\n\t"
        "v_cmp_gt_u32 vcc, s47, v126\n\t"
        "s_cbranch_vccnz 8b\n\t"
        "s_branch 13b\n\t"
        "14:\n\t"
        "s_mov_b32 s48, 0x10000\n\t"
        "8:\n\t"
        "ds_read_b32 v126, v125\n\t"
        "s_sub_u32 s48, s48, 1\n\t"
        "s_waitcnt lgkmcnt(0)\n\t"
        "s_cbranch_scc1 15b\n\t"
        "v_cmp_gt_u32 vcc, s47, v126\n\t"
        "s_cbranch_vccnz 8b\n\t"
        "s_branch 15b\n\t"
        "9:\n\t"
        : "={v[0:15]}"(acc[0]), "={v[16:31]}"(acc[1]), "={v[32:47]}"(acc[2]), "={v[48:63]}"(acc[3]), "={v[64:79]}"(acc[4]), "={v[80:95]}"(acc[5]),
          "={v[96:111]}"(stA), "+{v[112:127]}"(stB), "+{v[128:131]}"(stC), "={s46}"(slot), "+{s47}"(need)
        : "{v[132:147]}"(xr0) : "s48", "vcc", "scc", "memory");
      STAMPV(2 + 16 * L);
      float al = 1.f;
      if (L == 1) {
        PHASE_VARS;
        float S, Q, Dd;
        row_stats_sum(rs, OFF_SX, tile, lane, S, Q, Dd);
        STAMPV(36);
        lds_wait_ge<0>(lbase + L_W + 24, 2);
        soff = kpos(tm, tn, 1) * 4096;
        asm volatile(
          "buffer_load_dwordx4 v[148:151], v120, s[40:43], s44 offen offset:0 sc1\n\t"
          "buffer_load_dwordx4 v[152:155], v120, s[40:43], s44 offen offset:1024 sc1\n\t"
          "buffer_load_dwordx4 v[156:159], v120, s[40:43], s44 offen offset:2048 sc1\n\t"
          "buffer_load_dwordx4 v[160:163], v120, s[40:43], s44 offen offset:3072 sc1\n\t"
          : "={v[148:163]}"(xr1), "={v[164:179]}"(xr2), "={v[180:195]}"(xr3), "+{s44}"(soff), "+v"(S), "+v"(Q), "+v"(Dd) : "{v[112:127]}"(stB), "{s[40:43]}"(srd), KSKIP, "{s51}"(kpos(tm, tn, 1) * 4096) : "scc", "memory");
        float mean, var, mb1, rd1, mb2, den2;
        row_stats_fin(rs, OFF_SX + SX_CAND + (tn * 16 + tm) * 128 + wid * 4, smem, 1, tile, lane, S, Q, Dd, U2F(words[0]), x1p, mean, var, mb1, rd1);
        ln_scalars(mean + (mean - mb1) * rd1, var * (1.0f + rd1) * (1.0f + rd1), mb2, den2);
        const float rd2 = RCP(den2);
        al = (1.0f + rd1) * rd2;
        const float be = -(mb1 * rd1 + mb2) * RCP(1.0f + rd1);
        reinterpret_cast<float*>(smem + L_AB)[wid * 64 + lane] = hi ? be : al;
        reinterpret_cast<float*>(smem + L_AB)[256 + wid * 64 + lane] = hi ? 1.0f + rd1 : -mb1 * rd1;
        soff = kpos(tm, tn, 1 + 0) * 4096;
        asm volatile(
          "s_nop 0\n\t"
          "s_add_u32 s44, s44, 0x1000\n\t"
          "s_and_b32 s44, s44, 0xffff\n\t"
          "s_cmp_eq_u32 s44, s49\n\t"
          "s_cselect_b32 s44, s50, s44\n\t"
          "buffer_load_dwordx4 v[164:167], v120, s[40:43], s44 offen offset:0 sc1\n\t"
          "buffer_load_dwordx4 v[168:171], v120, s[40:43], s44 offen offset:1024 sc1\n\t"
          "buffer_load_dwordx4 v[172:175], v120, s[40:43], s44 offen offset:2048 sc1\n\t"
          "buffer_load_dwordx4 v[176:179], v120, s[40:43], s44 offen offset:3072 sc1\n\t"
          "s_add_u32 s44, s44, 0x1000\n\t"
          "s_and_b32 s44, s44, 0xffff\n\t"
          "s_cmp_eq_u32 s44, s49\n\t"
          "s_cselect_b32 s44, s50, s44\n\t"
          "buffer_load_dwordx4 v[180:183], v120, s[40:43], s44 offen offset:0 sc1\n\t"
          "buffer_load_dwordx4 v[184:187], v120, s[40:43], s44 offen offset:1024 sc1\n\t"
          "buffer_load_dwordx4 v[188:191], v120, s[40:43], s44 offen offset:2048 sc1\n\t"
          "buffer_load_dwordx4 v[192:195], v120, s[40:43], s44 offen offset:3072 sc1\n\t"
          : "+{v[148:163]}"(xr1), "+{v[164:179]}"(xr2), "+{v[180:195]}"(xr3), "+{s44}"(soff) : "{v[112:127]}"(stB), "{s[40:43]}"(srd), KSKIP, "{s51}"(kpos(tm, tn, 1) * 4096) : "scc", "memory");
#pragma unroll
        for (int jj = 0; jj < 3; ++jj) {
          f32x4 cs[2][4];
          float cv[2];
#pragma unroll
          for (int j = 0; j < 2; ++j) {
            const int cb = jj * 64 + (j ^ hh) * 32;
            if (jj == 2) cv[j] = *reinterpret_cast<const float*>(smem + L_CS + (cb + r32) * 4);
#pragma unroll
            for (int g = 0; g < 4; ++g)
              if (jj < 2 || j == 0) cs[j][g] = *reinterpret_cast<const f32x4*>(smem + (jj < 2 ? L_CS + cb * 4 : L_AB + wid * 256 + 128) + (8 * g + 4 * hi) * 4);
          }
          SBAR();
#pragma unroll
          for (int j = 0; j < 2; ++j)
#pragma unroll
            for (int e = 0; e < 16; ++e) acc[2 * jj + j][e] += jj < 2 ? be * cs[j][e >> 2][e & 3] : cs[0][e >> 2][e & 3] * cv[j];
          SBAR();
        }
      }
      u32x4 ep;
      { PHASE_VARS;
        if (L == 0) reinterpret_cast<float*>(smem + L_AB)[wid * 64 + lane] = 1.0f;
        const unsigned dst = OFF_KV + L * (10 * MBY) + tn * 2 * HS + tile * 4096 + lane * 16;
        ep = u32x4{__float_as_uint(al), dst + hh * HS, dst + (hh ^ 1) * HS, lbase + L_AB + wid * 256 + hi * 16}; }
      STAMPV(3 + 16 * L);
      soff = kpos(tm, tn, 1 + 2) * 4096;
      asm volatile(
        "s_nop 0\n\t"
        "s_add_u32 s44, s44, 0x1000\n\t"
        "s_and_b32 s44, s44, 0xffff\n\t"
        "s_cmp_eq_u32 s44, s49\n\t"
        "s_cselect_b32 s44, s50, s44\n\t"
        "buffer_load_dwordx4 v[132:135], v120, s[40:43], s44 offen offset:0 sc1\n\t"
        "buffer_load_dwordx4 v[136:139], v120, s[40:43], s44 offen offset:1024 sc1\n\t"
        "buffer_load_dwordx4 v[140:143], v120, s[40:43], s44 offen offset:2048 sc1\n\t"
        "s_mov_b32 s45, 3\n\t"
        "1:\n\t"
        "s_waitcnt vmcnt(14) lgkmcnt(11)\n\t"
        "v_mfma_f32_32x32x16_bf16 v[0:15], v[96:99], v[148:151], v[0:15]\n\t"
        "ds_read_b32 v126, v125\n\t"
        "ds_read_b64_tr_b16 v[96:97], v123 offset:6144\n\t"
        "ds_read_b64_tr_b16 v[98:99], v123 offset:9216\n\t"
        "s_waitcnt lgkmcnt(12)\n\t"
        "v_mfma_f32_32x32x16_bf16 v[16:31], v[100:103], v[148:151], v[16:31]\n\t"
        "v_add_u32 v129, s46, v121\n\t"
        "ds_read_b64_tr_b16 v[100:101], v124 offset:6144\n\t"
        "ds_read_b64_tr_b16 v[102:103], v124 offset:9216\n\t"
        "s_waitcnt lgkmcnt(12)\n\t"
        "v_mfma_f32_32x32x16_bf16 v[32:47], v[104:107], v[148:151], v[32:47]\n\t"
        "ds_read_b64_tr_b16 v[104:105], v123 offset:6272\n\t"
        "ds_read_b64_tr_b16 v[106:107], v123 offset:9344\n\t"
        "buffer_load_dwordx4 v[144:147], v120, s[40:43], s44 offen offset:3072 sc1\n\t"
        "s_waitcnt lgkmcnt(12)\n\t"
        "v_mfma_f32_32x32x16_bf16 v[48:63], v[108:111], v[148:151], v[48:63]\n\t"
        "v_add_u32 v130, s46, v122\n\t"
        "ds_read_b64_tr_b16 v[108:109], v124 offset:6272\n\t"
        "ds_read_b64_tr_b16 v[110:111], v124 offset:9344\n\t"
        "s_waitcnt lgkmcnt(12)\n\t"
        "v_mfma_f32_32x32x16_bf16 v[64:79], v[148:151], v[112:115], v[64:79]\n\t"
        "ds_read_b64_tr_b16 v[112:113], v123 offset:6400\n\t"
        "ds_read_b64_tr_b16 v[114:115], v123 offset:9472\n\t"
        "s_waitcnt lgkmcnt(10)\n\t"
        "v_mfma_f32_32x32x16_bf16 v[80:95], v[148:151], v[116:119], v[80:95]\n\t"
        "v_cmp_gt_u32 vcc, s47, v126\n\t"
        "ds_read_b64_tr_b16 v[116:117], v124 offset:6400\n\t"
        "ds_read_b64_tr_b16 v[118:119], v124 offset:9472\n\t"
        "s_waitcnt vmcnt(14) lgkmcnt(10)\n\t"
        "v_mfma_f32_32x32x16_bf16 v[0:15], v[96:99], v[152:155], v[0:15]\n\t"
        "s_cbranch_vccnz 16f\n\t"
        "17:\n\t"
        "ds_read_b64_tr_b16 v[96:97], v129 offset:0\n\t"
        "ds_read_b64_tr_b16 v[98:99], v129 offset:3072\n\t"
        "s_waitcnt lgkmcnt(10)\n\t"
        "v_mfma_f32_32x32x16_bf16 v[16:31], v[100:103], v[152:155], v[16:31]\n\t"
        "v_add_u32 v128, 1, v128\n\t"
        "ds_read_b64_tr_b16 v[100:101], v130 offset:0\n\t"
        "ds_read_b64_tr_b16 v[102:103], v130 offset:3072\n\t"
        "s_add_u32 s44, s44, 0x1000\n\t"
        "s_and_b32 s44, s44, 0xffff\n\t"
        "s_waitcnt lgkmcnt(10)\n\t"
        "v_mfma_f32_32x32x16_bf16 v[32:47], v[104:107], v[152:155], v[32:47]\n\t"
        "ds_read_b64_tr_b16 v[104:105], v129 offset:128\n\t"
        "ds_read_b64_tr_b16 v[106:107], v129 offset:3200\n\t"
        "s_cmp_eq_u32 s44, s49\n\t"
        "s_cselect_b32 s44, s50, s44\n\t"
        "buffer_load_dwordx4 v[148:151], v120, s[40:43], s44 offen offset:0 sc1\n\t"
        "s_waitcnt lgkmcnt(10)\n\t"
        "v_mfma_f32_32x32x16_bf16 v[48:63], v[108:111], v[152:155], v[48:63]\n\t"
        "s_add_u32 s46, s46, 12288\n\t"
        "s_add_u32 s47, s47, 1\n\t"
        "ds_read_b64_tr_b16 v[108:109], v130 offset:128\n\t"
        "ds_read_b64_tr_b16 v[110:111], v130 offset:3200\n\t"
        "s_waitcnt lgkmcnt(10)\n\t"
        "v_mfma_f32_32x32x16_bf16 v[64:79], v[152:155], v[112:115], v[64:79]\n\t"
        "s_cmp_eq_u32 s46, 110592\n\t"
        "s_cselect_b32 s46, 0, s46\n\t"
        "ds_read_b64_tr_b16 v[112:113], v129 offset:256\n\t"
        "ds_read_b64_tr_b16 v[114:115], v129 offset:3328\n\t"
        "s_waitcnt lgkmcnt(10)\n\t"
        "v_mfma_f32_32x32x16_bf16 v[80:95], v[152:155], v[116:119], v[80:95]\n\t"
        "ds_write_b32 v127, v128\n\t"
        "ds_read_b64_tr_b16 v[116:117], v130 offset:256\n\t"
        "ds_read_b64_tr_b16 v[118:119], v130 offset:3328\n\t"
        "s_waitcnt vmcnt(14) lgkmcnt(11)\n\t"
        "v_mfma_f32_32x32x16_bf16 v[0:15], v[96:99], v[156:159], v[0:15]\n\t"
        "ds_read_b32 v126, v125\n\t"
        "ds_read_b64_tr_b16 v[96:97], v129 offset:6144\n\t"
        "ds_read_b64_tr_b16 v[98:99], v129 offset:9216\n\t"
        "s_waitcnt lgkmcnt(12)\n\t"
        "v_mfma_f32_32x32x16_bf16 v[16:31], v[100:103], v[156:159], v[16:31]\n\t"
        "v_add_u32 v123, s46, v121\n\t"
        "ds_read_b64_tr_b16 v[100:101], v130 offset:6144\n\t"
        "ds_read_b64_tr_b16 v[102:103], v130 offset:9216\n\t"
        "s_waitcnt lgkmcnt(12)\n\t"
        "v_mfma_f32_32x32x16_bf16 v[32:47], v[104:107], v[156:159], v[32:47]\n\t"
        "ds_read_b64_tr_b16 v[104:105], v129 offset:6272\n\t"
        "ds_read_b64_tr_b16 v[106:107], v129 offset:9344\n\t"
        "buffer_load_dwordx4 v[152:155], v120, s[40:43], s44 offen offset:1024 sc1\n\t"
        "s_waitcnt lgkmcnt(12)\n\t"
        "v_mfma_f32_32x32x16_bf16 v[48:63], v[108:111], v[156:159], v[48:63]\n\t"
        "v_add_u32 v124, s46, v122\n\t"
        "ds_read_b64_tr_b16 v[108:109], v130 offset:6272\n\t"
        "ds_read_b64_tr_b16 v[110:111], v130 offset:9344\n\t"
        "s_waitcnt lgkmcnt(12)\n\t"
        "v_mfma_f32_32x32x16_bf16 v[64:79], v[156:159], v[112:115], v[64:79]\n\t"
        "ds_read_b64_tr_b16 v[112:113], v129 offset:6400\n\t"
        "ds_read_b64_tr_b16 v[114:115], v129 offset:9472\n\t"
        "s_waitcnt lgkmcnt(10)\n\t"
        "v_mfma_f32_32x32x16_bf16 v[80:95], v[156:159], v[116:119], v[80:95]\n\t"
        "v_cmp_gt_u32 vcc, s47, v126\n\t"
        "ds_read_b64_tr_b16 v[116:117], v130 offset:6400\n\t"
        "ds_read_b64_tr_b16 v[118:119], v130 offset:9472\n\t"
        "s_waitcnt vmcnt(14) lgkmcnt(10)\n\t"
        "v_mfma_f32_32x32x16_bf16 v[0:15], v[96:99], v[160:163], v[0:15]\n\t"
        "s_cbranch_vccnz 18f\n\t"
        "19:\n\t"
        "ds_read_b64_tr_b16 v[96:97], v123 offset:0\n\t"
        "ds_read_b64_tr_b16 v[98:99], v123 offset:3072\n\t"
        "s_waitcnt lgkmcnt(10)\n\t"
        "v_mfma_f32_32x32x16_bf16 v[16:31], v[100:103], v[160:163], v[16:31]\n\t"
        "v_add_u32 v128, 1, v128\n\t"
        "ds_read_b64_tr_b16 v[100:101], v124 offset:0\n\t"
        "ds_read_b64_tr_b16 v[102:103], v124 offset:3072\n\t"
        "s_waitcnt lgkmcnt(10)\n\t"
        "v_mfma_f32_32x32x16_bf16 v[32:47], v[104:107], v[160:163], v[32:47]\n\t"
        "ds_read_b64_tr_b16 v[104:105], v123 offset:128\n\t"
        "ds_read_b64_tr_b16 v[106:107], v123 offset:3200\n\t"
        "buffer_load_dwordx4 v[156:159], v120, s[40:43], s44 offen offset:2048 sc1\n\t"
        "s_waitcnt lgkmcnt(10)\n\t"
        "v_mfma_f32_32x32x16_bf16 v[48:63], v[108:111], v[160:163], v[48:63]\n\t"
        "s_add_u32 s46, s46, 12288\n\t"
        "s_add_u32 s47, s47, 1\n\t"
        "ds_read_b64_tr_b16 v[108:109], v124 offset:128\n\t"
        "ds_read_b64_tr_b16 v[110:111], v124 offset:3200\n\t"
        "s_waitcnt lgkmcnt(10)\n\t"
        "v_mfma_f32_32x32x16_bf16 v[64:79], v[160:163], v[112:115], v[64:79]\n\t"
        "s_cmp_eq_u32 s46, 110592\n\t"
        "s_cselect_b32 s46, 0, s46\n\t"
        "ds_read_b64_tr_b16 v[112:113], v123 offset:256\n\t"
        "ds_read_b64_tr_b16 v[114:115], v123 offset:3328\n\t"
        "s_waitcnt lgkmcnt(10)\n\t"
        "v_mfma_f32_32x32x16_bf16 v[80:95], v[160:163], v[116:119], v[80:95]\n\t"
        "ds_write_b32 v127, v128\n\t"
        "ds_read_b64_tr_b16 v[116:117], v124 offset:256\n\t"
        "ds_read_b64_tr_b16 v[118:119], v124 offset:3328\n\t"
        "s_waitcnt vmcnt(14) lgkmcnt(11)\n\t"
        "v_mfma_f32_32x32x16_bf16 v[0:15], v[96:99], v[164:167], v[0:15]\n\t"
        "ds_read_b32 v126, v125\n\t"
        "ds_read_b64_tr_b16 v[96:97], v123 offset:6144\n\t"
        "ds_read_b64_tr_b16 v[98:99], v123 offset:9216\n\t"
        "s_waitcnt lgkmcnt(12)\n\t"
        "v_mfma_f32_32x32x16_bf16 v[16:31], v[100:103], v[164:167], v[16:31]\n\t"
        "v_add_u32 v129, s46, v121\n\t"
        "ds_read_b64_tr_b16 v[100:101], v124 offset:6144\n\t"
        "ds_read_b64_tr_b16 v[102:103], v124 offset:9216\n\t"
        "s_waitcnt lgkmcnt(12)\n\t"
        "v_mfma_f32_32x32x16_bf16 v[32:47], v[104:107], v[164:167], v[32:47]\n\t"
        "ds_read_b64_tr_b16 v[104:105], v123 offset:6272\n\t"
        "ds_read_b64_tr_b16 v[106:107], v123 offset:9344\n\t"
        "buffer_load_dwordx4 v[160:163], v120, s[40:43], s44 offen offset:3072 sc1\n\t"
        "s_waitcnt lgkmcnt(12)\n\t"
        "v_mfma_f32_32x32x16_bf16 v[48:63], v[108:111], v[164:167], v[48:63]\n\t"
        "v_add_u32 v130, s46, v122\n\t"
        "ds_read_b64_tr_b16 v[108:109], v124 offset:6272\n\t"
        "ds_read_b64_tr_b16 v[110:111], v124 offset:9344\n\t"
        "s_waitcnt lgkmcnt(12)\n\t"
        "v_mfma_f32_32x32x16_bf16 v[64:79], v[164:167], v[112:115], v[64:79]\n\t"
        "ds_read_b64_tr_b16 v[112:113], v123 offset:6400\n\t"
        "ds_read_b64_tr_b16 v[114:115], v123 offset:9472\n\t"
        "s_waitcnt lgkmcnt(10)\n\t"
        "v_mfma_f32_32x32x16_bf16 v[80:95], v[164:167], v[116:119], v[80:95]\n\t"
        "v_cmp_gt_u32 vcc, s47, v126\n\t"
        "ds_read_b64_tr_b16 v[116:117], v124 offset:6400\n\t"
        "ds_read_b64_tr_b16 v[118:119], v124 offset:9472\n\t"
        "s_waitcnt vmcnt(14) lgkmcnt(10)\n\t"
        "v_mfma_f32_32x32x16_bf16 v[0:15], v[96:99], v[168:171], v[0:15]\n\t"
        "s_cbranch_vccnz 20f\n\t"
        "21:\n\t"
        "ds_read_b64_tr_b16 v[96:97], v129 offset:0\n\t"
        "ds_read_b64_tr_b16 v[98:99], v129 offset:3072\n\t"
        "s_waitcnt lgkmcnt(10)\n\t"
        "v_mfma_f32_32x32x16_bf16 v[16:31], v[100:103], v[168:171], v[16:31]\n\t"
        "v_add_u32 v128, 1, v128\n\t"
        "ds_read_b64_tr_b16 v[100:101], v130 offset:0\n\t"
        "ds_read_b64_tr_b16 v[102:103], v130 offset:3072\n\t"
        "s_add_u32 s44, s44, 0x1000\n\t"
        "s_and_b32 s44, s44, 0xffff\n\t"
        "s_waitcnt lgkmcnt(10)\n\t"
        "v_mfma_f32_32x32x16_bf16 v[32:47], v[104:107], v[168:171], v[32:47]\n\t"
        "ds_read_b64_tr_b16 v[104:105], v129 offset:128\n\t"
        "ds_read_b64_tr_b16 v[106:107], v129 offset:3200\n\t"
        "s_cmp_eq_u32 s44, s49\n\t"
        "s_cselect_b32 s44, s50, s44\n\t"
        "buffer_load_dwordx4 v[164:167], v120, s[40:43], s44 offen offset:0 sc1\n\t"
        "s_waitcnt lgkmcnt(10)\n\t"
        "v_mfma_f32_32x32x16_bf16 v[48:63], v[108:111], v[168:171], v[48:63]\n\t"
        "s_add_u32 s46, s46, 12288\n\t"
        "s_add_u32 s47, s47, 1\n\t"
        "ds_read_b64_tr_b16 v[108:109], v130 offset:128\n\t"
        "ds_read_b64_tr_b16 v[110:111], v130 offset:3200\n\t"
        "s_waitcnt lgkmcnt(10)\n\t"
        "v_mfma_f32_32x32x16_bf16 v[64:79], v[168:171], v[112:115], v[64:79]\n\t"
        "s_cmp_eq_u32 s46, 110592\n\t"
        "s_cselect_b32 s46, 0, s46\n\t"
        "ds_read_b64_tr_b16 v[112:113], v129 offset:256\n\t"
        "ds_read_b64_tr_b16 v[114:115], v129 offset:3328\n\t"
        "s_waitcnt lgkmcnt(10)\n\t"
        "v_mfma_f32_32x32x16_bf16 v[80:95], v[168:171], v[116:119], v[80:95]\n\t"
        "ds_write_b32 v127, v128\n\t"
        "ds_read_b64_tr_b16 v[116:117], v130 offset:256\n\t"
        "ds_read_b64_tr_b16 v[118:119], v130 offset:3328\n\t"
        "s_waitcnt vmcnt(14) lgkmcnt(11)\n\t"
        "v_mfma_f32_32x32x16_bf16 v[0:15], v[96:99], v[172:175], v[0:15]\n\t"
        "ds_read_b32 v126, v125\n\t"
        "ds_read_b64_tr_b16 v[96:97], v129 offset:6144\n\t"
        "ds_read_b64_tr_b16 v[98:99], v129 offset:9216\n\t"
        "s_waitcnt lgkmcnt(12)\n\t"
        "v_mfma_f32_32x32x16_bf16 v[16:31], v[100:103], v[172:175], v[16:31]\n\t"
        "v_add_u32 v123, s46, v121\n\t"
        "ds_read_b64_tr_b16 v[100:101], v130 offset:6144\n\t"
        "ds_read_b64_tr_b16 v[102:103], v130 offset:9216\n\t"
        "s_waitcnt lgkmcnt(12)\n\t"
        "v_mfma_f32_32x32x16_bf16 v[32:47], v[104:107], v[172:175], v[32:47]\n\t"
        "ds_read_b64_tr_b16 v[104:105], v129 offset:6272\n\t"
        "ds_read_b64_tr_b16 v[106:107], v129 offset:9344\n\t"
        "buffer_load_dwordx4 v[168:171], v120, s[40:43], s44 offen offset:1024 sc1\n\t"
        "s_waitcnt lgkmcnt(12)\n\t"
        "v_mfma_f32_32x32x16_bf16 v[48:63], v[108:111], v[172:175], v[48:63]\n\t"
        "v_add_u32 v124, s46, v122\n\t"
        "ds_read_b64_tr_b16 v[108:109], v130 offset:6272\n\t"
        "ds_read_b64_tr_b16 v[110:111], v130 offset:9344\n\t"
        "s_waitcnt lgkmcnt(12)\n\t"
        "v_mfma_f32_32x32x16_bf16 v[64:79], v[172:175], v[112:115], v[64:79]\n\t"
        "ds_read_b64_tr_b16 v[112:113], v129 offset:6400\n\t"
        "ds_read_b64_tr_b16 v[114:115], v129 offset:9472\n\t"
        "s_waitcnt lgkmcnt(10)\n\t"
        "v_mfma_f32_32x32x16_bf16 v[80:95], v[172:175], v[116:119], v[80:95]\n\t"
        "v_cmp_gt_u32 vcc, s47, v126\n\t"
        "ds_read_b64_tr_b16 v[116:117], v130 offset:6400\n\t"
        "ds_read_b64_tr_b16 v[118:119], v130 offset:9472\n\t"
        "s_waitcnt vmcnt(14) lgkmcnt(10)\n\t"
        "v_mfma_f32_32x32x16_bf16 v[0:15], v[96:99], v[176:179], v[0:15]\n\t"
        "s_cbranch_vccnz 22f\n\t"
        "23:\n\t"
        "ds_read_b64_tr_b16 v[96:97], v123 offset:0\n\t"
        "ds_read_b64_tr_b16 v[98:99], v123 offset:3072\n\t"
        "s_waitcnt lgkmcnt(10)\n\t"
        "v_mfma_f32_32x32x16_bf16 v[16:31], v[100:103], v[176:179], v[16:31]\n\t"
        "v_add_u32 v128, 1, v128\n\t"
        "ds_read_b64_tr_b16 v[100:101], v124 offset:0\n\t"
        "ds_read_b64_tr_b16 v[102:103], v124 offset:3072\n\t"
        "s_waitcnt lgkmcnt(10)\n\t"
        "v_mfma_f32_32x32x16_bf16 v[32:47], v[104:107], v[176:179], v[32:47]\n\t"
        "ds_read_b64_tr_b16 v[104:105], v123 offset:128\n\t"
        "ds_read_b64_tr_b16 v[106:107], v123 offset:3200\n\t"
        "buffer_load_dwordx4 v[172:175], v120, s[40:43], s44 offen offset:2048 sc1\n\t"
        "s_waitcnt lgkmcnt(10)\n\t"
        "v_mfma_f32_32x32x16_bf16 v[48:63], v[108:111], v[176:179], v[48:63]\n\t"
        "s_add_u32 s46, s46, 12288\n\t"
        "s_add_u32 s47, s47, 1\n\t"
        "ds_read_b64_tr_b16 v[108:109], v124 offset:128\n\t"
        "ds_read_b64_tr_b16 v[110:111], v124 offset:3200\n\t"
        "s_waitcnt lgkmcnt(10)\n\t"
        "v_mfma_f32_32x32x16_bf16 v[64:79], v[176:179], v[112:115], v[64:79]\n\t"
        "s_cmp_eq_u32 s46, 110592\n\t"
        "s_cselect_b32 s46, 0, s46\n\t"
        "ds_read_b64_tr_b16 v[112:113], v123 offset:256\n\t"
        "ds_read_b64_tr_b16 v[114:115], v123 offset:3328\n\t"
        "s_waitcnt lgkmcnt(10)\n\t"
        "v_mfma_f32_32x32x16_bf16 v[80:95], v[176:179], v[116:119], v[80:95]\n\t"
        "ds_write_b32 v127, v128\n\t"
        "ds_read_b64_tr_b16 v[116:117], v124 offset:256\n\t"
        "ds_read_b64_tr_b16 v[118:119], v124 offset:3328\n\t"
        "s_waitcnt vmcnt(14) lgkmcnt(11)\n\t"
        "v_mfma_f32_32x32x16_bf16 v[0:15], v[96:99], v[180:183], v[0:15]\n\t"
        "ds_read_b32 v126, v125\n\t"
        "ds_read_b64_tr_b16 v[96:97], v123 offset:6144\n\t"
        "ds_read_b64_tr_b16 v[98:99], v123 offset:9216\n\t"
        "s_waitcnt lgkmcnt(12)\n\t"
        "v_mfma_f32_32x32x16_bf16 v[16:31], v[100:103], v[180:183], v[16:31]\n\t"
        "v_add_u32 v129, s46, v121\n\t"
        "ds_read_b64_tr_b16 v[100:101], v124 offset:6144\n\t"
        "ds_read_b64_tr_b16 v[102:103], v124 offset:9216\n\t"
        "s_waitcnt lgkmcnt(12)\n\t"
        "v_mfma_f32_32x32x16_bf16 v[32:47], v[104:107], v[180:183], v[32:47]\n\t"
        "ds_read_b64_tr_b16 v[104:105], v123 offset:6272\n\t"
        "ds_read_b64_tr_b16 v[106:107], v123 offset:9344\n\t"
        "buffer_load_dwordx4 v[176:179], v120, s[40:43], s44 offen offset:3072 sc1\n\t"
        "s_waitcnt lgkmcnt(12)\n\t"
        "v_mfma_f32_32x32x16_bf16 v[48:63], v[108:111], v[180:183], v[48:63]\n\t"
        "v_add_u32 v130, s46, v122\n\t"
        "ds_read_b64_tr_b16 v[108:109], v124 offset:6272\n\t"
        "ds_read_b64_tr_b16 v[110:111], v124 offset:9344\n\t"
        "s_waitcnt lgkmcnt(12)\n\t"
        "v_mfma_f32_32x32x16_bf16 v[64:79], v[180:183], v[112:115], v[64:79]\n\t"
        "ds_read_b64_tr_b16 v[112:113], v123 offset:6400\n\t"
        "ds_read_b64_tr_b16 v[114:115], v123 offset:9472\n\t"
        "s_waitcnt lgkmcnt(10)\n\t"
        "v_mfma_f32_32x32x16_bf16 v[80:95], v[180:183], v[116:119], v[80:95]\n\t"
        "v_cmp_gt_u32 vcc, s47, v126\n\t"
        "ds_read_b64_tr_b16 v[116:117], v124 offset:6400\n\t"
        "ds_read_b64_tr_b16 v[118:119], v124 offset:9472\n\t"
        "s_waitcnt vmcnt(14) lgkmcnt(10)\n\t"
        "v_mfma_f32_32x32x16_bf16 v[0:15], v[96:99], v[184:187], v[0:15]\n\t"
        "s_cbranch_vccnz 24f\n\t"
        "25:\n\t"
        "ds_read_b64_tr_b16 v[96:97], v129 offset:0\n\t"
        "ds_read_b64_tr_b16 v[98:99], v129 offset:3072\n\t"
        "s_waitcnt lgkmcnt(10)\n\t"
        "v_mfma_f32_32x32x16_bf16 v[16:31], v[100:103], v[184:187], v[16:31]\n\t"
        "v_add_u32 v128, 1, v128\n\t"
        "ds_read_b64_tr_b16 v[100:101], v130 offset:0\n\t"
        "ds_read_b64_tr_b16 v[102:103], v130 offset:3072\n\t"
        "s_add_u32 s44, s44, 0x1000\n\t"
        "s_and_b32 s44, s44, 0xffff\n\t"
        "s_waitcnt lgkmcnt(10)\n\t"
        "v_mfma_f32_32x32x16_bf16 v[32:47], v[104:107], v[184:187], v[32:47]\n\t"
        "ds_read_b64_tr_b16 v[104:105], v129 offset:128\n\t"
        "ds_read_b64_tr_b16 v[106:107], v129 offset:3200\n\t"
        "s_cmp_eq_u32 s44, s49\n\t"
        "s_cselect_b32 s44, s50, s44\n\t"
        "buffer_load_dwordx4 v[180:183], v120, s[40:43], s44 offen offset:0 sc1\n\t"
        "s_waitcnt lgkmcnt(10)\n\t"
        "v_mfma_f32_32x32x16_bf16 v[48:63], v[108:111], v[184:187], v[48:63]\n\t"
        "s_add_u32 s46, s46, 12288\n\t"
        "s_add_u32 s47, s47, 1\n\t"
        "ds_read_b64_tr_b16 v[108:109], v130 offset:128\n\t"
        "ds_read_b64_tr_b16 v[110:111], v130 offset:3200\n\t"
        "s_waitcnt lgkmcnt(10)\n\t"
        "v_mfma_f32_32x32x16_bf16 v[64:79], v[184:187], v[112:115], v[64:79]\n\t"
        "s_cmp_eq_u32 s46, 110592\n\t"
        "s_cselect_b32 s46, 0, s46\n\t"
        "ds_read_b64_tr_b16 v[112:113], v129 offset:256\n\t"
        "ds_read_b64_tr_b16 v[114:115], v129 offset:3328\n\t"
        "s_waitcnt lgkmcnt(10)\n\t"
        "v_mfma_f32_32x32x16_bf16 v[80:95], v[184:187], v[116:119], v[80:95]\n\t"
        "ds_write_b32 v127, v128\n\t"
        "ds_read_b64_tr_b16 v[116:117], v130 offset:256\n\t"
        "ds_read_b64_tr_b16 v[118:119], v130 offset:3328\n\t"
        "s_waitcnt vmcnt(14) lgkmcnt(11)\n\t"
        "v_mfma_f32_32x32x16_bf16 v[0:15], v[96:99], v[188:191], v[0:15]\n\t"
        "ds_read_b32 v126, v125\n\t"
        "ds_read_b64_tr_b16 v[96:97], v129 offset:6144\n\t"
        "ds_read_b64_tr_b16 v[98:99], v129 offset:9216\n\t"
        "s_waitcnt lgkmcnt(12)\n\t"
        "v_mfma_f32_32x32x16_bf16 v[16:31], v[100:103], v[188:191], v[16:31]\n\t"
        "v_add_u32 v123, s46, v121\n\t"
        "ds_read_b64_tr_b16 v[100:101], v130 offset:6144\n\t"
        "ds_read_b64_tr_b16 v[102:103], v130 offset:9216\n\t"
        "s_waitcnt lgkmcnt(12)\n\t"
        "v_mfma_f32_32x32x16_bf16 v[32:47], v[104:107], v[188:191], v[32:47]\n\t"
        "ds_read_b64_tr_b16 v[104:105], v129 offset:6272\n\t"
        "ds_read_b64_tr_b16 v[106:107], v129 offset:9344\n\t"
        "buffer_load_dwordx4 v[184:187], v120, s[40:43], s44 offen offset:1024 sc1\n\t"
        "s_waitcnt lgkmcnt(12)\n\t"
        "v_mfma_f32_32x32x16_bf16 v[48:63], v[108:111], v[188:191], v[48:63]\n\t"
        "v_add_u32 v124, s46, v122\n\t"
        "ds_read_b64_tr_b16 v[108:109], v130 offset:6272\n\t"
        "ds_read_b64_tr_b16 v[110:111], v130 offset:9344\n\t"
        "s_waitcnt lgkmcnt(12)\n\t"
        "v_mfma_f32_32x32x16_bf16 v[64:79], v[188:191], v[112:115], v[64:79]\n\t"
        "ds_read_b64_tr_b16 v[112:113], v129 offset:6400\n\t"
        "ds_read_b64_tr_b16 v[114:115], v129 offset:9472\n\t"
        "s_waitcnt lgkmcnt(10)\n\t"
        "v_mfma_f32_32x32x16_bf16 v[80:95], v[188:191], v[116:119], v[80:95]\n\t"
        "v_cmp_gt_u32 vcc, s47, v126\n\t"
        "ds_read_b64_tr_b16 v[116:117], v130 offset:6400\n\t"
        "ds_read_b64_tr_b16 v[118:119], v130 offset:9472\n\t"
        "s_waitcnt vmcnt(14) lgkmcnt(10)\n\t"
        "v_mfma_f32_32x32x16_bf16 v[0:15], v[96:99], v[192:195], v[0:15]\n\t"
        "s_cbranch_vccnz 26f\n\t"
        "27:\n\t"
        "ds_read_b64_tr_b16 v[96:97], v123 offset:0\n\t"
        "ds_read_b64_tr_b16 v[98:99], v123 offset:3072\n\t"
        "s_waitcnt lgkmcnt(10)\n\t"
        "v_mfma_f32_32x32x16_bf16 v[16:31], v[100:103], v[192:195], v[16:31]\n\t"
        "v_add_u32 v128, 1, v128\n\t"
        "ds_read_b64_tr_b16 v[100:101], v124 offset:0\n\t"
        "ds_read_b64_tr_b16 v[102:103], v124 offset:3072\n\t"
        "s_waitcnt lgkmcnt(10)\n\t"
        "v_mfma_f32_32x32x16_bf16 v[32:47], v[104:107], v[192:195], v[32:47]\n\t"
        "ds_read_b64_tr_b16 v[104:105], v123 offset:128\n\t"
        "ds_read_b64_tr_b16 v[106:107], v123 offset:3200\n\t"
        "buffer_load_dwordx4 v[188:191], v120, s[40:43], s44 offen offset:2048 sc1\n\t"
        "s_waitcnt lgkmcnt(10)\n\t"
        "v_mfma_f32_32x32x16_bf16 v[48:63], v[108:111], v[192:195], v[48:63]\n\t"
        "s_add_u32 s46, s46, 12288\n\t"
        "s_add_u32 s47, s47, 1\n\t"
        "ds_read_b64_tr_b16 v[108:109], v124 offset:128\n\t"
        "ds_read_b64_tr_b16 v[110:111], v124 offset:3200\n\t"
        "s_waitcnt lgkmcnt(10)\n\t"
        "v_mfma_f32_32x32x16_bf16 v[64:79], v[192:195], v[112:115], v[64:79]\n\t"
        "s_cmp_eq_u32 s46, 110592\n\t"
        "s_cselect_b32 s46, 0, s46\n\t"
        "ds_read_b64_tr_b16 v[112:113], v123 offset:256\n\t"
        "ds_read_b64_tr_b16 v[114:115], v123 offset:3328\n\t"
        "s_waitcnt lgkmcnt(10)\n\t"
        "v_mfma_f32_32x32x16_bf16 v[80:95], v[192:195], v[116:119], v[80:95]\n\t"
        "ds_write_b32 v127, v128\n\t"
        "ds_read_b64_tr_b16 v[116:117], v124 offset:256\n\t"
        "ds_read_b64_tr_b16 v[118:119], v124 offset:3328\n\t"
        "s_waitcnt vmcnt(14) lgkmcnt(11)\n\t"
        "v_mfma_f32_32x32x16_bf16 v[0:15], v[96:99], v[132:135], v[0:15]\n\t"
        "ds_read_b32 v126, v125\n\t"
        "ds_read_b64_tr_b16 v[96:97], v123 offset:6144\n\t"
        "ds_read_b64_tr_b16 v[98:99], v123 offset:9216\n\t"
        "s_waitcnt lgkmcnt(12)\n\t"
        "v_mfma_f32_32x32x16_bf16 v[16:31], v[100:103], v[132:135], v[16:31]\n\t"
        "v_add_u32 v129, s46, v121\n\t"
        "ds_read_b64_tr_b16 v[100:101], v124 offset:6144\n\t"
        "ds_read_b64_tr_b16 v[102:103], v124 offset:9216\n\t"
        "s_waitcnt lgkmcnt(12)\n\t"
        "v_mfma_f32_32x32x16_bf16 v[32:47], v[104:107], v[132:135], v[32:47]\n\t"
        "ds_read_b64_tr_b16 v[104:105], v123 offset:6272\n\t"
        "ds_read_b64_tr_b16 v[106:107], v123 offset:9344\n\t"
        "buffer_load_dwordx4 v[192:195], v120, s[40:43], s44 offen offset:3072 sc1\n\t"
        "s_waitcnt lgkmcnt(12)\n\t"
        "v_mfma_f32_32x32x16_bf16 v[48:63], v[108:111], v[132:135], v[48:63]\n\t"
        "v_add_u32 v130, s46, v122\n\t"
        "ds_read_b64_tr_b16 v[108:109], v124 offset:6272\n\t"
        "ds_read_b64_tr_b16 v[110:111], v124 offset:9344\n\t"
        "s_waitcnt lgkmcnt(12)\n\t"
        "v_mfma_f32_32x32x16_bf16 v[64:79], v[132:135], v[112:115], v[64:79]\n\t"
        "ds_read_b64_tr_b16 v[112:113], v123 offset:6400\n\t"
        "ds_read_b64_tr_b16 v[114:115], v123 offset:9472\n\t"
        "s_waitcnt lgkmcnt(10)\n\t"
        "v_mfma_f32_32x32x16_bf16 v[80:95], v[132:135], v[116:119], v[80:95]\n\t"
        "v_cmp_gt_u32 vcc, s47, v126\n\t"
        "ds_read_b64_tr_b16 v[116:117], v124 offset:6400\n\t"
        "ds_read_b64_tr_b16 v[118:119], v124 offset:9472\n\t"
        "s_waitcnt vmcnt(14) lgkmcnt(10)\n\t"
        "v_mfma_f32_32x32x16_bf16 v[0:15], v[96:99], v[136:139], v[0:15]\n\t"
        "s_cbranch_vccnz 28f\n\t"
        "29:\n\t"
        "ds_read_b64_tr_b16 v[96:97], v129 offset:0\n\t"
        "ds_read_b64_tr_b16 v[98:99], v129 offset:3072\n\t"
        "s_waitcnt lgkmcnt(10)\n\t"
        "v_mfma_f32_32x32x16_bf16 v[16:31], v[100:103], v[136:139], v[16:31]\n\t"
        "v_add_u32 v128, 1, v128\n\t"
        "ds_read_b64_tr_b16 v[100:101], v130 offset:0\n\t"
        "ds_read_b64_tr_b16 v[102:103], v130 offset:3072\n\t"
        "s_add_u32 s44, s44, 0x1000\n\t"
        "s_and_b32 s44, s44, 0xffff\n\t"
        "s_waitcnt lgkmcnt(10)\n\t"
        "v_mfma_f32_32x32x16_bf16 v[32:47], v[104:107], v[136:139], v[32:47]\n\t"
        "ds_read_b64_tr_b16 v[104:105], v129 offset:128\n\t"
        "ds_read_b64_tr_b16 v[106:107], v129 offset:3200\n\t"
        "s_cmp_eq_u32 s44, s49\n\t"
        "s_cselect_b32 s44, s50, s44\n\t"
        "buffer_load_dwordx4 v[132:135], v120, s[40:43], s44 offen offset:0 sc1\n\t"
        "s_waitcnt lgkmcnt(10)\n\t"
        "v_mfma_f32_32x32x16_bf16 v[48:63], v[108:111], v[136:139], v[48:63]\n\t"
        "s_add_u32 s46, s46, 12288\n\t"
        "s_add_u32 s47, s47, 1\n\t"
        "ds_read_b64_tr_b16 v[108:109], v130 offset:128\n\t"
        "ds_read_b64_tr_b16 v[110:111], v130 offset:3200\n\t"
        "s_waitcnt lgkmcnt(10)\n\t"
        "v_mfma_f32_32x32x16_bf16 v[64:79], v[136:139], v[112:115], v[64:79]\n\t"
        "s_cmp_eq_u32 s46, 110592\n\t"
        "s_cselect_b32 s46, 0, s46\n\t"
        "ds_read_b64_tr_b16 v[112:113], v129 offset:256\n\t"
        "ds_read_b64_tr_b16 v[114:115], v129 offset:3328\n\t"
        "s_waitcnt lgkmcnt(10)\n\t"
        "v_mfma_f32_32x32x16_bf16 v[80:95], v[136:139], v[116:119], v[80:95]\n\t"
        "ds_write_b32 v127, v128\n\t"
        "ds_read_b64_tr_b16 v[116:117], v130 offset:256\n\t"
        "ds_read_b64_tr_b16 v[118:119], v130 offset:3328\n\t"
        "s_waitcnt vmcnt(14) lgkmcnt(11)\n\t"
        "v_mfma_f32_32x32x16_bf16 v[0:15], v[96:99], v[140:143], v[0:15]\n\t"
        "ds_read_b32 v126, v125\n\t"
        "ds_read_b64_tr_b16 v[96:97], v129 offset:6144\n\t"
        "ds_read_b64_tr_b16 v[98:99], v129 offset:9216\n\t"
        "s_waitcnt lgkmcnt(12)\n\t"
        "v_mfma_f32_32x32x16_bf16 v[16:31], v[100:103], v[140:143], v[16:31]\n\t"
        "v_add_u32 v123, s46, v121\n\t"
        "ds_read_b64_tr_b16 v[100:101], v130 offset:6144\n\t"
        "ds_read_b64_tr_b16 v[102:103], v130 offset:9216\n\t"
        "s_waitcnt lgkmcnt(12)\n\t"
        "v_mfma_f32_32x32x16_bf16 v[32:47], v[104:107], v[140:143], v[32:47]\n\t"
        "ds_read_b64_tr_b16 v[104:105], v129 offset:6272\n\t"
        "ds_read_b64_tr_b16 v[106:107], v129 offset:9344\n\t"
        "buffer_load_dwordx4 v[136:139], v120, s[40:43], s44 offen offset:1024 sc1\n\t"
        "s_waitcnt lgkmcnt(12)\n\t"
        "v_mfma_f32_32x32x16_bf16 v[48:63], v[108:111], v[140:143], v[48:63]\n\t"
        "v_add_u32 v124, s46, v122\n\t"
        "ds_read_b64_tr_b16 v[108:109], v130 offset:6272\n\t"
        "ds_read_b64_tr_b16 v[110:111], v130 offset:9344\n\t"
        "s_waitcnt lgkmcnt(12)\n\t"
        "v_mfma_f32_32x32x16_bf16 v[64:79], v[140:143], v[112:115], v[64:79]\n\t"
        "ds_read_b64_tr_b16 v[112:113], v129 offset:6400\n\t"
        "ds_read_b64_tr_b16 v[114:115], v129 offset:9472\n\t"
        "s_waitcnt lgkmcnt(10)\n\t"
        "v_mfma_f32_32x32x16_bf16 v[80:95], v[140:143], v[116:119], v[80:95]\n\t"
        "v_cmp_gt_u32 vcc, s47, v126\n\t"
        "ds_read_b64_tr_b16 v[116:117], v130 offset:6400\n\t"
        "ds_read_b64_tr_b16 v[118:119], v130 offset:9472\n\t"
        "s_waitcnt vmcnt(14) lgkmcnt(10)\n\t"
        "v_mfma_f32_32x32x16_bf16 v[0:15], v[96:99], v[144:147], v[0:15]\n\t"
        "s_cbranch_vccnz 30f\n\t"
        "31:\n\t"
        "ds_read_b64_tr_b16 v[96:97], v123 offset:0\n\t"
        "ds_read_b64_tr_b16 v[98:99], v123 offset:3072\n\t"
        "s_waitcnt lgkmcnt(10)\n\t"
        "v_mfma_f32_32x32x16_bf16 v[16:31], v[100:103], v[144:147], v[16:31]\n\t"
        "v_add_u32 v128, 1, v128\n\t"
        "ds_read_b64_tr_b16 v[100:101], v124 offset:0\n\t"
        "ds_read_b64_tr_b16 v[102:103], v124 offset:3072\n\t"
        "s_waitcnt lgkmcnt(10)\n\t"
        "v_mfma_f32_32x32x16_bf16 v[32:47], v[104:107], v[144:147], v[32:47]\n\t"
        "ds_read_b64_tr_b16 v[104:105], v123 offset:128\n\t"
        "ds_read_b64_tr_b16 v[106:107], v123 offset:3200\n\t"
        "buffer_load_dwordx4 v[140:143], v120, s[40:43], s44 offen offset:2048 sc1\n\t"
        "s_waitcnt lgkmcnt(10)\n\t"
        "v_mfma_f32_32x32x16_bf16 v[48:63], v[108:111], v[144:147], v[48:63]\n\t"
        "s_add_u32 s46, s46, 12288\n\t"
        "s_add_u32 s47, s47, 1\n\t"
        "ds_read_b64_tr_b16 v[108:109], v124 offset:128\n\t"
        "ds_read_b64_tr_b16 v[110:111], v124 offset:3200\n\t"
        "s_waitcnt lgkmcnt(10)\n\t"
        "v_mfma_f32_32x32x16_bf16 v[64:79], v[144:147], v[112:115], v[64:79]\n\t"
        "s_cmp_eq_u32 s46, 110592\n\t"
        "s_cselect_b32 s46, 0, s46\n\t"
        "ds_read_b64_tr_b16 v[112:113], v123 offset:256\n\t"
        "ds_read_b64_tr_b16 v[114:115], v123 offset:3328\n\t"
        "s_waitcnt lgkmcnt(10)\n\t"
        "v_mfma_f32_32x32x16_bf16 v[80:95], v[144:147], v[116:119], v[80:95]\n\t"
        "ds_write_b32 v127, v128\n\t"
        "ds_read_b64_tr_b16 v[116:117], v124 offset:256\n\t"
        "ds_read_b64_tr_b16 v[118:119], v124 offset:3328\n\t"
        "s_sub_u32 s45, s45, 1\n\t"
        "s_cmp_lg_u32 s45, 0\n\t"
        "s_cbranch_scc1 1b\n\t"
        "s_waitcnt vmcnt(14) lgkmcnt(11)\n\t"
        "v_mfma_f32_32x32x16_bf16 v[0:15], v[96:99], v[148:151], v[0:15]\n\t"
        "ds_read_b32 v126, v125\n\t"
        "s_add_u32 s47, s47, 4\n\t"
        "ds_read_b64_tr_b16 v[96:97], v123 offset:6400\n\t"
        "ds_read_b64_tr_b16 v[98:99], v123 offset:9472\n\t"
        "s_waitcnt lgkmcnt(12)\n\t"
        "v_mfma_f32_32x32x16_bf16 v[16:31], v[100:103], v[148:151], v[16:31]\n\t"
        "ds_read_b64_tr_b16 v[100:101], v124 offset:6400\n\t"
        "ds_read_b64_tr_b16 v[102:103], v124 offset:9472\n\t"
        "s_waitcnt lgkmcnt(4)\n\t"
        "v_cmp_gt_u32 vcc, s47, v126\n\t"
        "v_mfma_f32_32x32x16_bf16 v[32:47], v[104:107], v[148:151], v[32:47]\n\t"
        "s_cbranch_vccnz 74f\n\t"
        "75:\n\t"
        "ds_read_b64_tr_b16 v[104:105], v121 offset:256\n\t"
        "ds_read_b64_tr_b16 v[106:107], v121 offset:3328\n\t"
        "s_waitcnt lgkmcnt(12)\n\t"
        "v_mfma_f32_32x32x16_bf16 v[48:63], v[108:111], v[148:151], v[48:63]\n\t"
        "ds_read_b64_tr_b16 v[108:109], v122 offset:256\n\t"
        "ds_read_b64_tr_b16 v[110:111], v122 offset:3328\n\t"
        "s_waitcnt lgkmcnt(12)\n\t"
        "v_mfma_f32_32x32x16_bf16 v[64:79], v[148:151], v[112:115], v[64:79]\n\t"
        "ds_read_b64_tr_b16 v[112:113], v121 offset:6400\n\t"
        "ds_read_b64_tr_b16 v[114:115], v121 offset:9472\n\t"
        "s_waitcnt lgkmcnt(11)\n\t"
        "v_mfma_f32_32x32x16_bf16 v[80:95], v[148:151], v[116:119], v[80:95]\n\t"
        "ds_read_b64_tr_b16 v[116:117], v122 offset:6400\n\t"
        "ds_read_b64_tr_b16 v[118:119], v122 offset:9472\n\t"
        "s_waitcnt vmcnt(13) lgkmcnt(10)\n\t"
        "v_mfma_f32_32x32x16_bf16 v[64:79], v[152:155], v[96:99], v[64:79]\n\t"
        "ds_read_b64_tr_b16 v[96:97], v121 offset:12544\n\t"
        "ds_read_b64_tr_b16 v[98:99], v121 offset:15616\n\t"
        "s_waitcnt lgkmcnt(10)\n\t"
        "v_mfma_f32_32x32x16_bf16 v[80:95], v[152:155], v[100:103], v[80:95]\n\t"
        "ds_read_b64_tr_b16 v[100:101], v122 offset:12544\n\t"
        "ds_read_b64_tr_b16 v[102:103], v122 offset:15616\n\t"
        "s_waitcnt vmcnt(12) lgkmcnt(10)\n\t"
        "v_mfma_f32_32x32x16_bf16 v[64:79], v[156:159], v[104:107], v[64:79]\n\t"
        "ds_read_b128 v[196:199], v215 offset:0\n\t"
        "ds_read_b64_tr_b16 v[104:105], v121 offset:18688\n\t"
        "ds_read_b64_tr_b16 v[106:107], v121 offset:21760\n\t"
        "s_waitcnt lgkmcnt(11)\n\t"
        "v_mfma_f32_32x32x16_bf16 v[80:95], v[156:159], v[108:111], v[80:95]\n\t"
        "ds_read_b128 v[200:203], v215 offset:32\n\t"
        "ds_read_b64_tr_b16 v[108:109], v122 offset:18688\n\t"
        "ds_read_b64_tr_b16 v[110:111], v122 offset:21760\n\t"
        "s_waitcnt vmcnt(11) lgkmcnt(12)\n\t"
        "v_mfma_f32_32x32x16_bf16 v[64:79], v[160:163], v[112:115], v[64:79]\n\t"
        "ds_read_b128 v[204:207], v215 offset:64\n\t"
        "ds_read_b64_tr_b16 v[112:113], v121 offset:24832\n\t"
        "ds_read_b64_tr_b16 v[114:115], v121 offset:27904\n\t"
        "s_waitcnt lgkmcnt(13)\n\t"
        "v_mfma_f32_32x32x16_bf16 v[80:95], v[160:163], v[116:119], v[80:95]\n\t"
        "ds_read_b128 v[208:211], v215 offset:96\n\t"
        "ds_read_b64_tr_b16 v[116:117], v122 offset:24832\n\t"
        "ds_read_b64_tr_b16 v[118:119], v122 offset:27904\n\t"
        "s_waitcnt vmcnt(10) lgkmcnt(14)\n\t"
        "v_mfma_f32_32x32x16_bf16 v[64:79], v[164:167], v[96:99], v[64:79]\n\t"
        "ds_read_b64_tr_b16 v[96:97], v121 offset:30976\n\t"
        "ds_read_b64_tr_b16 v[98:99], v121 offset:34048\n\t"
        "s_waitcnt lgkmcnt(14)\n\t"
        "v_mfma_f32_32x32x16_bf16 v[80:95], v[164:167], v[100:103], v[80:95]\n\t"
        "ds_read_b64_tr_b16 v[100:101], v122 offset:30976\n\t"
        "ds_read_b64_tr_b16 v[102:103], v122 offset:34048\n\t"
        "s_waitcnt vmcnt(9) lgkmcnt(13)\n\t"
        "v_mfma_f32_32x32x16_bf16 v[64:79], v[168:171], v[104:107], v[64:79]\n\t"
        "ds_read_b64_tr_b16 v[104:105], v121 offset:37120\n\t"
        "ds_read_b64_tr_b16 v[106:107], v121 offset:40192\n\t"
        "s_waitcnt lgkmcnt(12)\n\t"
        "v_mfma_f32_32x32x16_bf16 v[80:95], v[168:171], v[108:111], v[80:95]\n\t"
        "ds_read_b64_tr_b16 v[108:109], v122 offset:37120\n\t"
        "ds_read_b64_tr_b16 v[110:111], v122 offset:40192\n\t"
        "s_waitcnt vmcnt(8) lgkmcnt(11)\n\t"
        "v_mfma_f32_32x32x16_bf16 v[64:79], v[172:175], v[112:115], v[64:79]\n\t"
        "ds_read_b64_tr_b16 v[112:113], v121 offset:43264\n\t"
        "ds_read_b64_tr_b16 v[114:115], v121 offset:46336\n\t"
        "s_waitcnt lgkmcnt(10)\n\t"
        "v_mfma_f32_32x32x16_bf16 v[80:95], v[172:175], v[116:119], v[80:95]\n\t"
        "ds_read_b64_tr_b16 v[116:117], v122 offset:43264\n\t"
        "ds_read_b64_tr_b16 v[118:119], v122 offset:46336\n\t"
        "s_waitcnt vmcnt(7) lgkmcnt(10)\n\t"
        "v_mfma_f32_32x32x16_bf16 v[64:79], v[176:179], v[96:99], v[64:79]\n\t"
        "ds_read_b64_tr_b16 v[96:97], v121 offset:49408\n\t"
        "ds_read_b64_tr_b16 v[98:99], v121 offset:52480\n\t"
        "s_waitcnt lgkmcnt(10)\n\t"
        "v_mfma_f32_32x32x16_bf16 v[80:95], v[176:179], v[100:103], v[80:95]\n\t"
        "ds_read_b64_tr_b16 v[100:101], v122 offset:49408\n\t"
        "ds_read_b64_tr_b16 v[102:103], v122 offset:52480\n\t"
        "s_waitcnt vmcnt(6) lgkmcnt(10)\n\t"
        "v_mfma_f32_32x32x16_bf16 v[64:79], v[180:183], v[104:107], v[64:79]\n\t"
        "ds_read_b64_tr_b16 v[104:105], v121 offset:55552\n\t"
        "ds_read_b64_tr_b16 v[106:107], v121 offset:58624\n\t"
        "s_waitcnt lgkmcnt(10)\n\t"
        "v_mfma_f32_32x32x16_bf16 v[80:95], v[180:183], v[108:111], v[80:95]\n\t"
        "ds_read_b64_tr_b16 v[108:109], v122 offset:55552\n\t"
        "ds_read_b64_tr_b16 v[110:111], v122 offset:58624\n\t"
        "s_waitcnt vmcnt(5) lgkmcnt(10)\n\t"
        "v_mfma_f32_32x32x16_bf16 v[64:79], v[184:187], v[112:115], v[64:79]\n\t"
        "ds_read_b64_tr_b16 v[112:113], v123 offset:6272\n\t"
        "ds_read_b64_tr_b16 v[114:115], v123 offset:9344\n\t"
        "s_waitcnt lgkmcnt(10)\n\t"
        "v_mfma_f32_32x32x16_bf16 v[80:95], v[184:187], v[116:119], v[80:95]\n\t"
        "ds_read_b64_tr_b16 v[116:117], v124 offset:6272\n\t"
        "ds_read_b64_tr_b16 v[118:119], v124 offset:9344\n\t"
        "s_waitcnt vmcnt(4) lgkmcnt(10)\n\t"
        "v_mfma_f32_32x32x16_bf16 v[64:79], v[188:191], v[96:99], v[64:79]\n\t"
        "ds_read_b64_tr_b16 v[96:97], v121 offset:128\n\t"
        "ds_read_b64_tr_b16 v[98:99], v121 offset:3200\n\t"
        "s_waitcnt lgkmcnt(10)\n\t"
        "v_mfma_f32_32x32x16_bf16 v[80:95], v[188:191], v[100:103], v[80:95]\n\t"
        "ds_read_b64_tr_b16 v[100:101], v122 offset:128\n\t"
        "ds_read_b64_tr_b16 v[102:103], v122 offset:3200\n\t"
        "s_waitcnt vmcnt(0) lgkmcnt(10)\n\t"
        "v_mfma_f32_32x32x16_bf16 v[64:79], v[192:195], v[104:107], v[64:79]\n\t"
        "ds_read_b64_tr_b16 v[104:105], v121 offset:6272\n\t"
        "ds_read_b64_tr_b16 v[106:107], v121 offset:9344\n\t"
        "s_waitcnt lgkmcnt(10)\n\t"
        "v_mfma_f32_32x32x16_bf16 v[80:95], v[192:195], v[108:111], v[80:95]\n\t"
        "ds_read_b64_tr_b16 v[108:109], v122 offset:6272\n\t"
        "ds_read_b64_tr_b16 v[110:111], v122 offset:9344\n\t"
        "s_waitcnt lgkmcnt(10)\n\t"
        "v_mfma_f32_32x32x16_bf16 v[32:47], v[112:115], v[152:155], v[32:47]\n\t"
        "ds_read_b64_tr_b16 v[112:113], v121 offset:12416\n\t"
        "ds_read_b64_tr_b16 v[114:115], v121 offset:15488\n\t"
        "s_waitcnt lgkmcnt(10)\n\t"
        "v_mfma_f32_32x32x16_bf16 v[48:63], v[116:119], v[152:155], v[48:63]\n\t"
        "ds_read_b64_tr_b16 v[116:117], v122 offset:12416\n\t"
        "ds_read_b64_tr_b16 v[118:119], v122 offset:15488\n\t"
        "v_mul_f32 v64, v196, v64\n\t"
        "v_mul_f32 v65, v197, v65\n\t"
        "v_mul_f32 v66, v198, v66\n\t"
        "s_waitcnt lgkmcnt(10)\n\t"
        "v_mfma_f32_32x32x16_bf16 v[32:47], v[96:99], v[156:159], v[32:47]\n\t"
        "ds_read_b64_tr_b16 v[96:97], v121 offset:18560\n\t"
        "ds_read_b64_tr_b16 v[98:99], v121 offset:21632\n\t"
        "v_mul_f32 v67, v199, v67\n\t"
        "v_mul_f32 v68, v200, v68\n\t"
        "v_mul_f32 v69, v201, v69\n\t"
        "s_waitcnt lgkmcnt(10)\n\t"
        "v_mfma_f32_32x32x16_bf16 v[48:63], v[100:103], v[156:159], v[48:63]\n\t"
        "ds_read_b64_tr_b16 v[100:101], v122 offset:18560\n\t"
        "ds_read_b64_tr_b16 v[102:103], v122 offset:21632\n\t"
        "v_mul_f32 v70, v202, v70\n\t"
        "v_mul_f32 v71, v203, v71\n\t"
        "v_cvt_pk_bf16_f32 v64, v64, v65\n\t"
        "s_waitcnt lgkmcnt(10)\n\t"
        "v_mfma_f32_32x32x16_bf16 v[32:47], v[104:107], v[160:163], v[32:47]\n\t"
        "ds_read_b64_tr_b16 v[104:105], v121 offset:24704\n\t"
        "ds_read_b64_tr_b16 v[106:107], v121 offset:27776\n\t"
        "v_cvt_pk_bf16_f32 v65, v66, v67\n\t"
        "v_cvt_pk_bf16_f32 v66, v68, v69\n\t"
        "v_cvt_pk_bf16_f32 v67, v70, v71\n\t"
        "s_waitcnt lgkmcnt(10)\n\t"
        "v_mfma_f32_32x32x16_bf16 v[48:63], v[108:111], v[160:163], v[48:63]\n\t"
        "ds_read_b64_tr_b16 v[108:109], v122 offset:24704\n\t"
        "ds_read_b64_tr_b16 v[110:111], v122 offset:27776\n\t"
        "v_mul_f32 v72, v204, v72\n\t"
        "v_mul_f32 v73, v205, v73\n\t"
        "v_mul_f32 v74, v206, v74\n\t"
        "s_waitcnt lgkmcnt(10)\n\t"
        "v_mfma_f32_32x32x16_bf16 v[32:47], v[112:115], v[164:167], v[32:47]\n\t"
        "ds_read_b64_tr_b16 v[112:113], v121 offset:30848\n\t"
        "ds_read_b64_tr_b16 v[114:115], v121 offset:33920\n\t"
        "v_mul_f32 v75, v207, v75\n\t"
        "v_mul_f32 v76, v208, v76\n\t"
        "v_mul_f32 v77, v209, v77\n\t"
        "s_waitcnt lgkmcnt(10)\n\t"
        "v_mfma_f32_32x32x16_bf16 v[48:63], v[116:119], v[164:167], v[48:63]\n\t"
        "ds_read_b64_tr_b16 v[116:117], v122 offset:30848\n\t"
        "ds_read_b64_tr_b16 v[118:119], v122 offset:33920\n\t"
        "v_mul_f32 v78, v210, v78\n\t"
        "v_mul_f32 v79, v211, v79\n\t"
        "v_cvt_pk_bf16_f32 v68, v72, v73\n\t"
        "s_waitcnt lgkmcnt(10)\n\t"
        "v_mfma_f32_32x32x16_bf16 v[32:47], v[96:99], v[168:171], v[32:47]\n\t"
        "ds_read_b64_tr_b16 v[96:97], v121 offset:36992\n\t"
        "ds_read_b64_tr_b16 v[98:99], v121 offset:40064\n\t"
        "v_cvt_pk_bf16_f32 v69, v74, v75\n\t"
        "v_cvt_pk_bf16_f32 v70, v76, v77\n\t"
        "v_cvt_pk_bf16_f32 v71, v78, v79\n\t"
        "s_waitcnt lgkmcnt(10)\n\t"
        "v_mfma_f32_32x32x16_bf16 v[48:63], v[100:103], v[168:171], v[48:63]\n\t"
        "ds_read_b64_tr_b16 v[100:101], v122 offset:36992\n\t"
        "ds_read_b64_tr_b16 v[102:103], v122 offset:40064\n\t"
        "v_permlane32_swap_b32 v64, v66\n\t"
        "v_permlane32_swap_b32 v65, v67\n\t"
        "v_permlane32_swap_b32 v68, v70\n\t"
        "s_waitcnt lgkmcnt(10)\n\t"
        "v_mfma_f32_32x32x16_bf16 v[32:47], v[104:107], v[172:175], v[32:47]\n\t"
        "ds_read_b64_tr_b16 v[104:105], v121 offset:43136\n\t"
        "ds_read_b64_tr_b16 v[106:107], v121 offset:46208\n\t"
        "v_permlane32_swap_b32 v69, v71\n\t"
        "buffer_store_dwordx4 v[64:67], v213, s[40:43], 0 offen offset:2048 sc1\n\t"
        "buffer_store_dwordx4 v[68:71], v213, s[40:43], 0 offen offset:3072 sc1\n\t"
        "s_waitcnt lgkmcnt(10)\n\t"
        "v_mfma_f32_32x32x16_bf16 v[48:63], v[108:111], v[172:175], v[48:63]\n\t"
        "ds_read_b64_tr_b16 v[108:109], v122 offset:43136\n\t"
        "ds_read_b64_tr_b16 v[110:111], v122 offset:46208\n\t"
        "v_mul_f32 v80, v196, v80\n\t"
        "v_mul_f32 v81, v197, v81\n\t"
        "v_mul_f32 v82, v198, v82\n\t"
        "s_waitcnt lgkmcnt(10)\n\t"
        "v_mfma_f32_32x32x16_bf16 v[32:47], v[112:115], v[176:179], v[32:47]\n\t"
        "ds_read_b64_tr_b16 v[112:113], v121 offset:49280\n\t"
        "ds_read_b64_tr_b16 v[114:115], v121 offset:52352\n\t"
        "v_mul_f32 v83, v199, v83\n\t"
        "v_mul_f32 v84, v200, v84\n\t"
        "v_mul_f32 v85, v201, v85\n\t"
        "s_waitcnt lgkmcnt(10)\n\t"
        "v_mfma_f32_32x32x16_bf16 v[48:63], v[116:119], v[176:179], v[48:63]\n\t"
        "ds_read_b64_tr_b16 v[116:117], v122 offset:49280\n\t"
        "ds_read_b64_tr_b16 v[118:119], v122 offset:52352\n\t"
        "v_mul_f32 v86, v202, v86\n\t"
        "v_mul_f32 v87, v203, v87\n\t"
        "v_cvt_pk_bf16_f32 v80, v80, v81\n\t"
        "s_waitcnt lgkmcnt(10)\n\t"
        "v_mfma_f32_32x32x16_bf16 v[32:47], v[96:99], v[180:183], v[32:47]\n\t"
        "ds_read_b64_tr_b16 v[96:97], v121 offset:55424\n\t"
        "ds_read_b64_tr_b16 v[98:99], v121 offset:58496\n\t"
        "v_cvt_pk_bf16_f32 v81, v82, v83\n\t"
        "v_cvt_pk_bf16_f32 v82, v84, v85\n\t"
        "v_cvt_pk_bf16_f32 v83, v86, v87\n\t"
        "s_waitcnt lgkmcnt(10)\n\t"
        "v_mfma_f32_32x32x16_bf16 v[48:63], v[100:103], v[180:183], v[48:63]\n\t"
        "ds_read_b64_tr_b16 v[100:101], v122 offset:55424\n\t"
        "ds_read_b64_tr_b16 v[102:103], v122 offset:58496\n\t"
        "v_mul_f32 v88, v204, v88\n\t"
        "v_mul_f32 v89, v205, v89\n\t"
        "v_mul_f32 v90, v206, v90\n\t"
        "s_waitcnt lgkmcnt(10)\n\t"
        "v_mfma_f32_32x32x16_bf16 v[32:47], v[104:107], v[184:187], v[32:47]\n\t"
        "ds_read_b64_tr_b16 v[104:105], v123 offset:6144\n\t"
        "ds_read_b64_tr_b16 v[106:107], v123 offset:9216\n\t"
        "v_mul_f32 v91, v207, v91\n\t"
        "v_mul_f32 v92, v208, v92\n\t"
        "v_mul_f32 v93, v209, v93\n\t"
        "s_waitcnt lgkmcnt(10)\n\t"
        "v_mfma_f32_32x32x16_bf16 v[48:63], v[108:111], v[184:187], v[48:63]\n\t"
        "ds_read_b64_tr_b16 v[108:109], v124 offset:6144\n\t"
        "ds_read_b64_tr_b16 v[110:111], v124 offset:9216\n\t"
        "v_mul_f32 v94, v210, v94\n\t"
        "v_mul_f32 v95, v211, v95\n\t"
        "v_cvt_pk_bf16_f32 v84, v88, v89\n\t"
        "s_waitcnt lgkmcnt(10)\n\t"
        "v_mfma_f32_32x32x16_bf16 v[32:47], v[112:115], v[188:191], v[32:47]\n\t"
        "ds_read_b64_tr_b16 v[112:113], v121 offset:0\n\t"
        "ds_read_b64_tr_b16 v[114:115], v121 offset:3072\n\t"
        "v_cvt_pk_bf16_f32 v85, v90, v91\n\t"
        "v_cvt_pk_bf16_f32 v86, v92, v93\n\t"
        "v_cvt_pk_bf16_f32 v87, v94, v95\n\t"
        "s_waitcnt lgkmcnt(10)\n\t"
        "v_mfma_f32_32x32x16_bf16 v[48:63], v[116:119], v[188:191], v[48:63]\n\t"
        "ds_read_b64_tr_b16 v[116:117], v122 offset:0\n\t"
        "ds_read_b64_tr_b16 v[118:119], v122 offset:3072\n\t"
        "v_permlane32_swap_b32 v80, v82\n\t"
        "v_permlane32_swap_b32 v81, v83\n\t"
        "v_permlane32_swap_b32 v84, v86\n\t"
        "s_waitcnt lgkmcnt(10)\n\t"
        "v_mfma_f32_32x32x16_bf16 v[32:47], v[96:99], v[192:195], v[32:47]\n\t"
        "ds_read_b64_tr_b16 v[96:97], v121 offset:6144\n\t"
        "ds_read_b64_tr_b16 v[98:99], v121 offset:9216\n\t"
        "v_permlane32_swap_b32 v85, v87\n\t"
        "buffer_store_dwordx4 v[80:83], v214, s[40:43], 0 offen offset:2048 sc1\n\t"
        "buffer_store_dwordx4 v[84:87], v214, s[40:43], 0 offen offset:3072 sc1\n\t"
        "s_waitcnt lgkmcnt(10)\n\t"
        "v_mfma_f32_32x32x16_bf16 v[48:63], v[100:103], v[192:195], v[48:63]\n\t"
        "ds_read_b64_tr_b16 v[100:101], v122 offset:6144\n\t"
        "ds_read_b64_tr_b16 v[102:103], v122 offset:9216\n\t"
        "s_waitcnt lgkmcnt(10)\n\t"
        "v_mfma_f32_32x32x16_bf16 v[0:15], v[104:107], v[152:155], v[0:15]\n\t"
        "ds_read_b64_tr_b16 v[104:105], v121 offset:12288\n\t"
        "ds_read_b64_tr_b16 v[106:107], v121 offset:15360\n\t"
        "s_waitcnt lgkmcnt(10)\n\t"
        "v_mfma_f32_32x32x16_bf16 v[16:31], v[108:111], v[152:155], v[16:31]\n\t"
        "ds_read_b64_tr_b16 v[108:109], v122 offset:12288\n\t"
        "ds_read_b64_tr_b16 v[110:111], v122 offset:15360\n\t"
        "v_mul_f32 v32, v212, v32\n\t"
        "v_mul_f32 v33, v212, v33\n\t"
        "v_mul_f32 v34, v212, v34\n\t"
        "s_waitcnt lgkmcnt(10)\n\t"
        "v_mfma_f32_32x32x16_bf16 v[0:15], v[112:115], v[156:159], v[0:15]\n\t"
        "ds_read_b64_tr_b16 v[112:113], v121 offset:18432\n\t"
        "ds_read_b64_tr_b16 v[114:115], v121 offset:21504\n\t"
        "v_mul_f32 v35, v212, v35\n\t"
        "v_mul_f32 v36, v212, v36\n\t"
        "v_mul_f32 v37, v212, v37\n\t"
        "s_waitcnt lgkmcnt(10)\n\t"
        "v_mfma_f32_32x32x16_bf16 v[16:31], v[116:119], v[156:159], v[16:31]\n\t"
        "ds_read_b64_tr_b16 v[116:117], v122 offset:18432\n\t"
        "ds_read_b64_tr_b16 v[118:119], v122 offset:21504\n\t"
        "v_mul_f32 v38, v212, v38\n\t"
        "v_mul_f32 v39, v212, v39\n\t"
        "v_cvt_pk_bf16_f32 v32, v32, v33\n\t"
        "s_waitcnt lgkmcnt(10)\n\t"
        "v_mfma_f32_32x32x16_bf16 v[0:15], v[96:99], v[160:163], v[0:15]\n\t"
        "ds_read_b64_tr_b16 v[96:97], v121 offset:24576\n\t"
        "ds_read_b64_tr_b16 v[98:99], v121 offset:27648\n\t"
        "v_cvt_pk_bf16_f32 v33, v34, v35\n\t"
        "v_cvt_pk_bf16_f32 v34, v36, v37\n\t"
        "v_cvt_pk_bf16_f32 v35, v38, v39\n\t"
        "s_waitcnt lgkmcnt(10)\n\t"
        "v_mfma_f32_32x32x16_bf16 v[16:31], v[100:103], v[160:163], v[16:31]\n\t"
        "ds_read_b64_tr_b16 v[100:101], v122 offset:24576\n\t"
        "ds_read_b64_tr_b16 v[102:103], v122 offset:27648\n\t"
        "v_mul_f32 v40, v212, v40\n\t"
        "v_mul_f32 v41, v212, v41\n\t"
        "v_mul_f32 v42, v212, v42\n\t"
        "s_waitcnt lgkmcnt(10)\n\t"
        "v_mfma_f32_32x32x16_bf16 v[0:15], v[104:107], v[164:167], v[0:15]\n\t"
        "ds_read_b64_tr_b16 v[104:105], v121 offset:30720\n\t"
        "ds_read_b64_tr_b16 v[106:107], v121 offset:33792\n\t"
        "v_mul_f32 v43, v212, v43\n\t"
        "v_mul_f32 v44, v212, v44\n\t"
        "v_mul_f32 v45, v212, v45\n\t"
        "s_waitcnt lgkmcnt(10)\n\t"
        "v_mfma_f32_32x32x16_bf16 v[16:31], v[108:111], v[164:167], v[16:31]\n\t"
        "ds_read_b64_tr_b16 v[108:109], v122 offset:30720\n\t"
        "ds_read_b64_tr_b16 v[110:111], v122 offset:33792\n\t"
        "v_mul_f32 v46, v212, v46\n\t"
        "v_mul_f32 v47, v212, v47\n\t"
        "v_cvt_pk_bf16_f32 v36, v40, v41\n\t"
        "s_waitcnt lgkmcnt(10)\n\t"
        "v_mfma_f32_32x32x16_bf16 v[0:15], v[112:115], v[168:171], v[0:15]\n\t"
        "ds_read_b64_tr_b16 v[112:113], v121 offset:36864\n\t"
        "ds_read_b64_tr_b16 v[114:115], v121 offset:39936\n\t"
        "v_cvt_pk_bf16_f32 v37, v42, v43\n\t"
        "v_cvt_pk_bf16_f32 v38, v44, v45\n\t"
        "v_cvt_pk_bf16_f32 v39, v46, v47\n\t"
        "s_waitcnt lgkmcnt(10)\n\t"
        "v_mfma_f32_32x32x16_bf16 v[16:31], v[116:119], v[168:171], v[16:31]\n\t"
        "ds_read_b64_tr_b16 v[116:117], v122 offset:36864\n\t"
        "ds_read_b64_tr_b16 v[118:119], v122 offset:39936\n\t"
        "buffer_store_dwordx4 v[32:35], v213, s[40:43], 0 offen offset:0 sc1\n\t"
        "buffer_store_dwordx4 v[36:39], v213, s[40:43], 0 offen offset:1024 sc1\n\t"
        "v_mul_f32 v48, v212, v48\n\t"
        "s_waitcnt lgkmcnt(10)\n\t"
        "v_mfma_f32_32x32x16_bf16 v[0:15], v[96:99], v[172:175], v[0:15]\n\t"
        "ds_read_b64_tr_b16 v[96:97], v121 offset:43008\n\t"
        "ds_read_b64_tr_b16 v[98:99], v121 offset:46080\n\t"
        "v_mul_f32 v49, v212, v49\n\t"
        "v_mul_f32 v50, v212, v50\n\t"
        "v_mul_f32 v51, v212, v51\n\t"
        "s_waitcnt lgkmcnt(10)\n\t"
        "v_mfma_f32_32x32x16_bf16 v[16:31], v[100:103], v[172:175], v[16:31]\n\t"
        "ds_read_b64_tr_b16 v[100:101], v122 offset:43008\n\t"
        "ds_read_b64_tr_b16 v[102:103], v122 offset:46080\n\t"
        "v_mul_f32 v52, v212, v52\n\t"
        "v_mul_f32 v53, v212, v53\n\t"
        "v_mul_f32 v54, v212, v54\n\t"
        "s_waitcnt lgkmcnt(10)\n\t"
        "v_mfma_f32_32x32x16_bf16 v[0:15], v[104:107], v[176:179], v[0:15]\n\t"
        "ds_read_b64_tr_b16 v[104:105], v121 offset:49152\n\t"
        "ds_read_b64_tr_b16 v[106:107], v121 offset:52224\n\t"
        "v_mul_f32 v55, v212, v55\n\t"
        "v_cvt_pk_bf16_f32 v48, v48, v49\n\t"
        "v_cvt_pk_bf16_f32 v49, v50, v51\n\t"
        "s_waitcnt lgkmcnt(10)\n\t"
        "v_mfma_f32_32x32x16_bf16 v[16:31], v[108:111], v[176:179], v[16:31]\n\t"
        "ds_read_b64_tr_b16 v[108:109], v122 offset:49152\n\t"
        "ds_read_b64_tr_b16 v[110:111], v122 offset:52224\n\t"
        "v_cvt_pk_bf16_f32 v50, v52, v53\n\t"
        "v_cvt_pk_bf16_f32 v51, v54, v55\n\t"
        "v_mul_f32 v56, v212, v56\n\t"
        "s_waitcnt lgkmcnt(10)\n\t"
        "v_mfma_f32_32x32x16_bf16 v[0:15], v[112:115], v[180:183], v[0:15]\n\t"
        "ds_read_b64_tr_b16 v[112:113], v121 offset:55296\n\t"
        "ds_read_b64_tr_b16 v[114:115], v121 offset:58368\n\t"
        "v_mul_f32 v57, v212, v57\n\t"
        "v_mul_f32 v58, v212, v58\n\t"
        "v_mul_f32 v59, v212, v59\n\t"
        "s_waitcnt lgkmcnt(10)\n\t"
        "v_mfma_f32_32x32x16_bf16 v[16:31], v[116:119], v[180:183], v[16:31]\n\t"
        "ds_read_b64_tr_b16 v[116:117], v122 offset:55296\n\t"
        "ds_read_b64_tr_b16 v[118:119], v122 offset:58368\n\t"
        "v_mul_f32 v60, v212, v60\n\t"
        "v_mul_f32 v61, v212, v61\n\t"
        "v_mul_f32 v62, v212, v62\n\t"
        "s_waitcnt lgkmcnt(10)\n\t"
        "v_mfma_f32_32x32x16_bf16 v[0:15], v[96:99], v[184:187], v[0:15]\n\t"
        "v_mul_f32 v63, v212, v63\n\t"
        "v_cvt_pk_bf16_f32 v52, v56, v57\n\t"
        "v_cvt_pk_bf16_f32 v53, v58, v59\n\t"
        "v_cvt_pk_bf16_f32 v54, v60, v61\n\t"
        "v_cvt_pk_bf16_f32 v55, v62, v63\n\t"
        "s_waitcnt lgkmcnt(8)\n\t"
        "v_mfma_f32_32x32x16_bf16 v[16:31], v[100:103], v[184:187], v[16:31]\n\t"
        "buffer_store_dwordx4 v[48:51], v214, s[40:43], 0 offen offset:0 sc1\n\t"
        "buffer_store_dwordx4 v[52:55], v214, s[40:43], 0 offen offset:1024 sc1\n\t"
        "s_waitcnt lgkmcnt(6)\n\t"
        "v_mfma_f32_32x32x16_bf16 v[0:15], v[104:107], v[188:191], v[0:15]\n\t"
        "s_waitcnt lgkmcnt(4)\n\t"
        "v_mfma_f32_32x32x16_bf16 v[16:31], v[108:111], v[188:191], v[16:31]\n\t"
        "s_waitcnt lgkmcnt(2)\n\t"
        "v_mfma_f32_32x32x16_bf16 v[0:15], v[112:115], v[192:195], v[0:15]\n\t"
        "s_waitcnt lgkmcnt(0)\n\t"
        "v_mfma_f32_32x32x16_bf16 v[16:31], v[116:119], v[192:195], v[16:31]\n\t"
        "s_nop 11\n\t"
        "s_branch 9f\n\t"
        "16:\n\t"
        "s_mov_b32 s48, 0x10000\n\t"
        "8:\n\t"
        "ds_read_b32 v126, v125\n\t"
        "s_sub_u32 s48, s48, 1\n\t"
        "s_waitcnt lgkmcnt(0)\n\t"
        "s_cbranch_scc1 17b\n\t"
        "v_cmp_gt_u32 vcc, s47, v126\n\t"
        "s_cbranch_vccnz 8b\n\t"
        "s_branch 17b\n\t"
        "18:\n\t"
        "s_mov_b32 s48, 0x10000\n\t"
        "8:\n\t"
        "ds_read_b32 v126, v125\n\t"
        "s_sub_u32 s48, s48, 1\n\t"
        "s_waitcnt lgkmcnt(0)\n\t"
        "s_cbranch_scc1 19b\n\t"
        "v_cmp_gt_u32 vcc, s47, v126\n\t"
        "s_cbranch_vccnz 8b\n\t"
        "s_branch 19b\n\t"
        "20:\n\t"
        "s_mov_b32 s48, 0x10000\n\t"
        "8:\n\t"
        "ds_read_b32 v126, v125\n\t"
        "s_sub_u32 s48, s48, 1\n\t"
        "s_waitcnt lgkmcnt(0)\n\t"
        "s_cbranch_scc1 21b\n\t"
        "v_cmp_gt_u32 vcc, s47, v126\n\t"
        "s_cbranch_vccnz 8b\n\t"
        "s_branch 21b\n\t"
        "22:\n\t"
        "s_mov_b32 s48, 0x10000\n\t"
        "8:\n\t"
        "ds_read_b32 v126, v125\n\t"
        "s_sub_u32 s48, s48, 1\n\t"
        "s_waitcnt lgkmcnt(0)\n\t"
        "s_cbranch_scc1 23b\n\t"
        "v_cmp_gt_u32 vcc, s47, v126\n\t"
        "s_cbranch_vccnz 8b\n\t"
        "s_branch 23b\n\t"
        "24:\n\t"
        "s_mov_b32 s48, 0x10000\n\t"
        "8:\n\t"
        "ds_read_b32 v126, v125\n\t"
        "s_sub_u32 s48, s48, 1\n\t"
        "s_waitcnt lgkmcnt(0)\n\t"
        "s_cbranch_scc1 25b\n\t"
        "v_cmp_gt_u32 vcc, s47, v126\n\t"
        "s_cbranch_vccnz 8b\n\t"
        "s_branch 25b\n\t"
        "26:\n\t"
        "s_mov_b32 s48, 0x10000\n\t"
        "8:\n\t"
        "ds_read_b32 v126, v125\n\t"
        "s_sub_u32 s48, s48, 1\n\t"
        "s_waitcnt lgkmcnt(0)\n\t"
        "s_cbranch_scc1 27b\n\t"
        "v_cmp_gt_u32 vcc, s47, v126\n\t"
        "s_cbranch_vccnz 8b\n\t"
        "s_branch 27b\n\t"
        "28:\n\t"
        "s_mov_b32 s48, 0x10000\n\t"
        "8:\n\t"
        "ds_read_b32 v126, v125\n\t"
        "s_sub_u32 s48, s48, 1\n\t"
        "s_waitcnt lgkmcnt(0)\n\t"
        "s_cbranch_scc1 29b\n\t"
        "v_cmp_gt_u32 vcc, s47, v126\n\t"
        "s_cbranch_vccnz 8b\n\t"
        "s_branch 29b\n\t"
        "30:\n\t"
        "s_mov_b32 s48, 0x10000\n\t"
        "8:\n\t"
        "ds_read_b32 v126, v125\n\t"
        "s_sub_u32 s48, s48, 1\n\t"
        "s_waitcnt lgkmcnt(0)\n\t"
        "s_cbranch_scc1 31b\n\t"
        "v_cmp_gt_u32 vcc, s47, v126\n\t"
        "s_cbranch_vccnz 8b\n\t"
        "s_branch 31b\n\t"
        "74:\n\t"
        "s_mov_b32 s48, 0x10000\n\t"
        "8:\n\t"
        "ds_read_b32 v126, v125\n\t"
        "s_sub_u32 s48, s48, 1\n\t"
        "s_waitcnt lgkmcnt(0)\n\t"
        "s_cbranch_scc1 75b\n\t"
        "v_cmp_gt_u32 vcc, s47, v126\n\t"
        "s_cbranch_vccnz 8b\n\t"
        "s_branch 75b\n\t"
        "9:\n\t"
        : "+{v[0:15]}"(acc[0]), "+{v[16:31]}"(acc[1]), "+{v[32:47]}"(acc[2]), "+{v[48:63]}"(acc[3]), "+{v[64:79]}"(acc[4]), "+{v[80:95]}"(acc[5]),
          "+{v[96:111]}"(stA), "+{v[112:127]}"(stB), "+{v[128:131]}"(stC), "+{v[148:163]}"(xr1), "+{v[164:179]}"(xr2), "+{v[180:195]}"(xr3),
          "+{s44}"(soff), "+{s46}"(slot), "+{s47}"(need)
        : "{s[40:43]}"(srd), KSKIP, "{v[212:215]}"(ep) : "v196", "v197", "v198", "v199", "v200", "v201", "v202", "v203", "v204", "v205", "v206", "v207", "v208", "v209", "v210", "v211", "v132", "v133", "v134", "v135", "v136", "v137", "v138", "v139", "v140", "v141", "v142", "v143", "v144", "v145", "v146", "v147", "s45", "s48", "vcc", "scc", "memory");
      STAMPV(4 + 16 * L);
      PHASE_VARS;
      u32x4 kv[4][2];
#pragma unroll
      for (int j = 0; j < 4; ++j)
#pragma unroll
        for (int e = 0; e < 8; ++e) kv[j][e >> 2][e & 3] = __float_as_uint(acc[2 + j][e]);
      constexpr float QS = 0.17677669529663687f * 1.4426950408889634f;
      u32x4 qo[2][2];
      auto drained = [&]() { asm volatile("s_waitcnt vmcnt(0)\n\tds_write_b32 %0, %1" :: "v"(lbase + L_W + 80 + 4 * wid), "v"(2 * L + 1) : "memory"); };
      if (L == 0) {
#pragma unroll
        for (int e = 0; e < 16; ++e) {
          if (e == 8) { SBAR(); drained(); SBAR(); }
          const unsigned w = pack2(acc[e >> 3][2 * (e & 7)], acc[e >> 3][2 * (e & 7) + 1]);
          qo[e >> 3][(e >> 2) & 1][e & 3] = pack2(lo16(w) * QS, hi16(w) * QS);
        }
      } else {
        const float aq = U2F(ep[0]) * QS;
#pragma unroll
        for (int e = 0; e < 16; ++e) qo[e >> 3][(e >> 2) & 1][e & 3] = pack2(acc[e >> 3][2 * (e & 7)] * aq, acc[e >> 3][2 * (e & 7) + 1] * aq);
        SBAR();
        lds_wait_ge<0>(lbase + L_W + 4 + 4 * (lane & 1), 1);
        const int idx = (int)__builtin_amdgcn_readfirstlane(words[3]);
        if ((idx >> 5) == tile) {
          f32x4 cs[4][4];
          float cv[2];
#pragma unroll
          for (int j = 0; j < 6; ++j) {
            const int cb = (j >> 1) * 64 + ((j & 1) ^ hh) * 32;
            if (j >= 4) cv[j - 4] = *reinterpret_cast<const float*>(smem + L_PATCH + (cb + r32) * 4);
            else {
#pragma unroll
              for (int g = 0; g < 4; ++g) cs[j][g] = *reinterpret_cast<const f32x4*>(smem + L_PATCH + (cb + 8 * g + 4 * hi) * 4);
            }
          }
          SBAR();
          const bool me = r32 == (idx & 31);
          const int rel = (idx & 31) - 8 * hi;
#pragma unroll
          for (int j = 0; j < 2; ++j) {
            const unsigned pv = pack2(cv[j], cv[j]);
#pragma unroll
            for (int e = 0; e < 8; ++e) {
              unsigned a = pack2(cs[j][e >> 1][2 * (e & 1)] * QS, cs[j][e >> 1][2 * (e & 1) + 1] * QS), b = pack2(cs[2 + j][e >> 1][2 * (e & 1)], cs[2 + j][e >> 1][2 * (e & 1) + 1]);
              PIN(a); PIN(b);
              qo[j][e >> 2][e & 3] = me ? a : qo[j][e >> 2][e & 3];
              kv[j][e >> 2][e & 3] = me ? b : kv[j][e >> 2][e & 3];
              const int t0 = 16 * (e >> 2) + 2 * (e & 3);
              const unsigned mask = rel == t0 ? 0xffffu : rel == t0 + 1 ? 0xffff0000u : 0u;
              kv[2 + j][e >> 2][e & 3] = (kv[2 + j][e >> 2][e & 3] & ~mask) | (pv & mask);
            }
          }
          if (me) {
#pragma unroll
            for (int i = 0; i < 4; ++i) x1p[i] = *reinterpret_cast<const u32x4*>(smem + L_X2P + (i * 2 + hi) * 16);
            reinterpret_cast<float*>(smem + L_AB)[256 + wid * 64 + lane] = hi ? 1.0f : 0.0f;
          }
          DRAIN();
#pragma unroll
          for (int j = 0; j < 4; ++j) { stg(rs, ep[1 + (j & 1)] + (j >> 1) * 2048, kv[j][0]); stg(rs, ep[1 + (j & 1)] + (j >> 1) * 2048 + 1024, kv[j][1]); }
        }
        drained();
      }
      SBAR();
#pragma unroll
      for (int j = 0; j < 2; ++j) {
        char* p = smem + L_XCH + ((j ^ hh) * 4 + wid) * 2048 + lane * 16;
        *reinterpret_cast<u32x4*>(p) = qo[j][0]; *reinterpret_cast<u32x4*>(p + 1024) = qo[j][1];
        p = smem + L_XCH + 16384 + wid * 4096 + j * 2048 + lane * 16;
        *reinterpret_cast<u32x4*>(p) = kv[2 * j + 1][0]; *reinterpret_cast<u32x4*>(p + 1024) = kv[2 * j + 1][1];
      }
      STAMPV(41 + 4 * L);
      BAR_LGKM();
      STAMPV(43 + 4 * L);
      kva = cat4(kv[0][0], kv[0][1], kv[2][0], kv[2][1]);
      const u32x4* pk = reinterpret_cast<const u32x4*>(smem + L_XCH + 16384 + (wid ^ 1) * 4096 + lane * 16);
#pragma unroll
      for (int u = 0; u < 4; ++u) {
        const u32x4* p = reinterpret_cast<const u32x4*>(smem + L_XCH + (hh * 4 + ((2 * kh + 2 + u) & 3)) * 2048 + lane * 16);
        q[u] = __builtin_shufflevector(p[0], p[64], 0, 1, 2, 3, 4, 5, 6, 7);
      }
      kvb = cat4(pk[0], pk[64], pk[128], pk[192]);
      SBAR();
      asm volatile(
        "v_mov_b64 v[0:1], 0\n\t"
        "v_mov_b64 v[2:3], 0\n\t"
        "v_mov_b64 v[4:5], 0\n\t"
        "v_mov_b64 v[6:7], 0\n\t"
        "v_mov_b64 v[8:9], 0\n\t"
        "v_mov_b64 v[10:11], 0\n\t"
        "v_mov_b64 v[12:13], 0\n\t"
        "v_mov_b64 v[14:15], 0\n\t"
        "v_mov_b64 v[16:17], 0\n\t"
        "v_mov_b64 v[18:19], 0\n\t"
        "v_mov_b64 v[20:21], 0\n\t"
        "v_mov_b64 v[22:23], 0\n\t"
        "v_mov_b64 v[24:25], 0\n\t"
        "v_mov_b64 v[26:27], 0\n\t"
        "v_mov_b64 v[28:29], 0\n\t"
        "v_mov_b64 v[30:31], 0\n\t"
        "v_mov_b64 v[32:33], 0\n\t"
        "v_mov_b64 v[34:35], 0\n\t"
        "v_mov_b64 v[36:37], 0\n\t"
        "v_mov_b64 v[38:39], 0\n\t"
        "v_mov_b64 v[40:41], 0\n\t"
        "v_mov_b64 v[42:43], 0\n\t"
        "v_mov_b64 v[44:45], 0\n\t"
        "v_mov_b64 v[46:47], 0\n\t"
        "v_mov_b64 v[48:49], 0\n\t"
        "v_mov_b64 v[50:51], 0\n\t"
        "v_mov_b64 v[52:53], 0\n\t"
        "v_mov_b64 v[54:55], 0\n\t"
        "v_mov_b64 v[56:57], 0\n\t"
        "v_mov_b64 v[58:59], 0\n\t"
        "v_mov_b64 v[60:61], 0\n\t"
        "v_mov_b64 v[62:63], 0\n\t"
        "v_mov_b64 v[128:129], 0\n\t"
        "v_mov_b64 v[130:131], 0\n\t"
        "v_mov_b64 v[132:133], 0\n\t"
        "v_mov_b64 v[134:135], 0\n\t"
        "v_mov_b64 v[136:137], 0\n\t"
        "v_mov_b64 v[138:139], 0\n\t"
        "v_mov_b64 v[140:141], 0\n\t"
        "v_mov_b64 v[142:143], 0\n\t"
        : "={v[0:15]}"(oz[0]), "={v[16:31]}"(oz[1]), "={v[32:47]}"(oz[2]), "={v[48:63]}"(oz[3]), "={v[128:143]}"(oz[4]));
    }
    STAMPV(5 + 16 * L);
    {
      u32x4 ones;
      unsigned kvoff;
      u32x8 fv, cv;
      asm volatile("" : "={v[184:191]}"(fv), "={v[216:223]}"(cv));
      { PHASE_VARS;
        const unsigned v = (lane & 3) == 0 ? 0x3F803F80u : 0u; const bool up = (lane & 4) != 0; ones[0] = ones[1] = up ? 0u : v; ones[2] = ones[3] = up ? v : 0u;
        kvoff = OFF_KV + L * (10 * MBY) + (tn * 2 + hh) * HS + kh * 8192 + lane * 16;
        fv[1] = lbase + L_FREE + wid * 256 + lane * 4; fv[2] = 32 * L + 32;
        cv[0] = lbase + L_XCH + wid * 8192 + lane * 16; cv[1] = lbase + L_XCH + (wid ^ 2) * 8192 + lane * 16;
        cv[2] = lbase + L_XCH + 32768 + wid * 512 + lane * 4; cv[3] = lbase + L_XCH + 32768 + (wid ^ 2) * 512 + lane * 4;
        cv[4] = lbase + L_W + 48 + wid * 4; cv[5] = lbase + L_W + 48 + (wid ^ 2) * 4; cv[6] = L + 1;
        cv[7] = ((tile * 32 + r32) * D_ + tn * 64 + hi * 8) * 2; }
      f32x16 o0 = oz[0], o1 = oz[1], o2 = oz[2], o3 = oz[3], lv = oz[4];
      unsigned soff = (tm | 7) * 16384;
      STAMP_DECL;
      asm volatile(
        "s_nop 1\n\t"
        "v_mfma_f32_32x32x16_bf16 v[96:111], v[152:155], v[64:67], 0\n\t"
        "v_mfma_f32_32x32x16_bf16 v[96:111], v[156:159], v[68:71], v[96:111]\n\t"
        "s_mov_b32 s46, 0\n\t"
        "s_mov_b64 s[54:55], s[42:43]\n\t"
        "ds_write_b32 v185, v186\n\t"
        "s_nop 8\n\t"
        "v_exp_f32 v96, v96\n\t"
        "v_exp_f32 v97, v97\n\t"
        "v_exp_f32 v98, v98\n\t"
        "v_exp_f32 v99, v99\n\t"
        "v_exp_f32 v100, v100\n\t"
        "v_exp_f32 v101, v101\n\t"
        "v_exp_f32 v102, v102\n\t"
        "v_mfma_f32_32x32x16_bf16 v[112:127], v[152:155], v[72:75], 0\n\t"
        "v_exp_f32 v103, v103\n\t"
        "v_exp_f32 v104, v104\n\t"
        "v_exp_f32 v105, v105\n\t"
        "v_mfma_f32_32x32x16_bf16 v[112:127], v[156:159], v[76:79], v[112:127]\n\t"
        "v_exp_f32 v106, v106\n\t"
        "v_exp_f32 v107, v107\n\t"
        "v_exp_f32 v108, v108\n\t"
        "v_smfmac_f32_32x32x32_bf16 v[0:15], v[160:163], v[96:103], v148\n\t"
        "v_exp_f32 v109, v109\n\t"
        "v_exp_f32 v110, v110\n\t"
        "v_exp_f32 v111, v111\n\t"
        "v_smfmac_f32_16x16x64_bf16 v[128:131], v[144:147], v[96:103], v148\n\t"
        "v_exp_f32 v112, v112\n\t"
        "v_exp_f32 v113, v113\n\t"
        "v_smfmac_f32_32x32x32_bf16 v[0:15], v[164:167], v[104:111], v148\n\t"
        "v_exp_f32 v114, v114\n\t"
        "v_exp_f32 v115, v115\n\t"
        "v_exp_f32 v116, v116\n\t"
        "v_smfmac_f32_16x16x64_bf16 v[128:131], v[144:147], v[104:111], v148\n\t"
        "v_exp_f32 v117, v117\n\t"
        "v_exp_f32 v118, v118\n\t"
        "v_mfma_f32_32x32x16_bf16 v[96:111], v[152:155], v[80:83], 0\n\t"
        "v_exp_f32 v119, v119\n\t"
        "v_exp_f32 v120, v120\n\t"
        "v_exp_f32 v121, v121\n\t"
        "v_mfma_f32_32x32x16_bf16 v[96:111], v[156:159], v[84:87], v[96:111]\n\t"
        "ds_read_b32 v151, v224\n\t"
        "v_exp_f32 v122, v122\n\t"
        "v_exp_f32 v123, v123\n\t"
        "v_exp_f32 v124, v124\n\t"
        "v_smfmac_f32_32x32x32_bf16 v[16:31], v[160:163], v[112:119], v148\n\t"
        "v_exp_f32 v125, v125\n\t"
        "v_exp_f32 v126, v126\n\t"
        "v_exp_f32 v127, v127\n\t"
        "v_smfmac_f32_16x16x64_bf16 v[132:135], v[144:147], v[112:119], v148\n\t"
        "s_cmp_lg_u32 s46, 0\n\t"
        "s_cbranch_scc1 25f\n\t"
        "s_waitcnt lgkmcnt(0)\n\t"
        "v_cmp_ne_u32 vcc, v222, v151\n\t"
        "s_cbranch_vccz 24f\n\t"
        "25:\n\t"
        "v_exp_f32 v96, v96\n\t"
        "v_exp_f32 v97, v97\n\t"
        "v_smfmac_f32_32x32x32_bf16 v[16:31], v[164:167], v[120:127], v148\n\t"
        "v_exp_f32 v98, v98\n\t"
        "v_exp_f32 v99, v99\n\t"
        "v_exp_f32 v100, v100\n\t"
        "v_smfmac_f32_16x16x64_bf16 v[132:135], v[144:147], v[120:127], v148\n\t"
        "v_exp_f32 v101, v101\n\t"
        "v_exp_f32 v102, v102\n\t"
        "v_mfma_f32_32x32x16_bf16 v[112:127], v[152:155], v[88:91], 0\n\t"
        "v_exp_f32 v103, v103\n\t"
        "v_exp_f32 v104, v104\n\t"
        "v_exp_f32 v105, v105\n\t"
        "v_mfma_f32_32x32x16_bf16 v[112:127], v[156:159], v[92:95], v[112:127]\n\t"
        "ds_read_b32 v151, v224\n\t"
        "v_exp_f32 v106, v106\n\t"
        "v_exp_f32 v107, v107\n\t"
        "v_exp_f32 v108, v108\n\t"
        "v_smfmac_f32_32x32x32_bf16 v[32:47], v[160:163], v[96:103], v148\n\t"
        "v_exp_f32 v109, v109\n\t"
        "v_exp_f32 v110, v110\n\t"
        "v_exp_f32 v111, v111\n\t"
        "v_smfmac_f32_16x16x64_bf16 v[136:139], v[144:147], v[96:103], v148\n\t"
        "s_cmp_lg_u32 s46, 0\n\t"
        "s_cbranch_scc1 27f\n\t"
        "s_waitcnt lgkmcnt(0)\n\t"
        "v_cmp_ne_u32 vcc, v222, v151\n\t"
        "s_cbranch_vccz 26f\n\t"
        "27:\n\t"
        "v_exp_f32 v112, v112\n\t"
        "v_exp_f32 v113, v113\n\t"
        "v_smfmac_f32_32x32x32_bf16 v[32:47], v[164:167], v[104:111], v148\n\t"
        "v_exp_f32 v114, v114\n\t"
        "v_exp_f32 v115, v115\n\t"
        "v_exp_f32 v116, v116\n\t"
        "v_smfmac_f32_16x16x64_bf16 v[136:139], v[144:147], v[104:111], v148\n\t"
        "v_exp_f32 v117, v117\n\t"
        "v_exp_f32 v118, v118\n\t"
        "v_mfma_f32_32x32x16_bf16 v[96:111], v[168:171], v[64:67], 0\n\t"
        "v_exp_f32 v119, v119\n\t"
        "v_exp_f32 v120, v120\n\t"
        "v_exp_f32 v121, v121\n\t"
        "v_mfma_f32_32x32x16_bf16 v[96:111], v[172:175], v[68:71], v[96:111]\n\t"
        "ds_read_b32 v151, v224\n\t"
        "v_exp_f32 v122, v122\n\t"
        "v_exp_f32 v123, v123\n\t"
        "v_exp_f32 v124, v124\n\t"
        "v_smfmac_f32_32x32x32_bf16 v[48:63], v[160:163], v[112:119], v148\n\t"
        "v_exp_f32 v125, v125\n\t"
        "v_exp_f32 v126, v126\n\t"
        "v_exp_f32 v127, v127\n\t"
        "v_smfmac_f32_16x16x64_bf16 v[140:143], v[144:147], v[112:119], v148\n\t"
        "s_cmp_lg_u32 s46, 0\n\t"
        "s_cbranch_scc1 29f\n\t"
        "s_waitcnt lgkmcnt(0)\n\t"
        "v_cmp_ne_u32 vcc, v222, v151\n\t"
        "s_cbranch_vccz 28f\n\t"
        "29:\n\t"
        "v_exp_f32 v96, v96\n\t"
        "v_exp_f32 v97, v97\n\t"
        "v_smfmac_f32_32x32x32_bf16 v[48:63], v[164:167], v[120:127], v148\n\t"
        "v_exp_f32 v98, v98\n\t"
        "v_exp_f32 v99, v99\n\t"
        "v_exp_f32 v100, v100\n\t"
        "v_smfmac_f32_16x16x64_bf16 v[140:143], v[144:147], v[120:127], v148\n\t"
        "v_exp_f32 v101, v101\n\t"
        "v_exp_f32 v102, v102\n\t"
        "v_mfma_f32_32x32x16_bf16 v[112:127], v[168:171], v[72:75], 0\n\t"
        "v_exp_f32 v103, v103\n\t"
        "v_exp_f32 v104, v104\n\t"
        "v_exp_f32 v105, v105\n\t"
        "v_mfma_f32_32x32x16_bf16 v[112:127], v[172:175], v[76:79], v[112:127]\n\t"
        "ds_read_b32 v151, v224\n\t"
        "v_exp_f32 v106, v106\n\t"
        "v_exp_f32 v107, v107\n\t"
        "v_exp_f32 v108, v108\n\t"
        "v_smfmac_f32_32x32x32_bf16 v[0:15], v[176:179], v[96:103], v148\n\t"
        "v_exp_f32 v109, v109\n\t"
        "v_exp_f32 v110, v110\n\t"
        "v_exp_f32 v111, v111\n\t"
        "v_smfmac_f32_16x16x64_bf16 v[128:131], v[144:147], v[96:103], v148\n\t"
        "s_cmp_lg_u32 s46, 0\n\t"
        "s_cbranch_scc1 31f\n\t"
        "s_waitcnt lgkmcnt(0)\n\t"
        "v_cmp_ne_u32 vcc, v222, v151\n\t"
        "s_cbranch_vccz 30f\n\t"
        "31:\n\t"
        "v_exp_f32 v112, v112\n\t"
        "v_exp_f32 v113, v113\n\t"
        "v_smfmac_f32_32x32x32_bf16 v[0:15], v[180:183], v[104:111], v148\n\t"
        "v_exp_f32 v114, v114\n\t"
        "v_exp_f32 v115, v115\n\t"
        "v_exp_f32 v116, v116\n\t"
        "v_smfmac_f32_16x16x64_bf16 v[128:131], v[144:147], v[104:111], v148\n\t"
        "v_exp_f32 v117, v117\n\t"
        "v_exp_f32 v118, v118\n\t"
        "v_mfma_f32_32x32x16_bf16 v[96:111], v[168:171], v[80:83], 0\n\t"
        "v_exp_f32 v119, v119\n\t"
        "v_exp_f32 v120, v120\n\t"
        "v_exp_f32 v121, v121\n\t"
        "v_mfma_f32_32x32x16_bf16 v[96:111], v[172:175], v[84:87], v[96:111]\n\t"
        "ds_read_b32 v151, v224\n\t"
        "v_exp_f32 v122, v122\n\t"
        "v_exp_f32 v123, v123\n\t"
        "v_exp_f32 v124, v124\n\t"
        "v_smfmac_f32_32x32x32_bf16 v[16:31], v[176:179], v[112:119], v148\n\t"
        "v_exp_f32 v125, v125\n\t"
        "v_exp_f32 v126, v126\n\t"
        "v_exp_f32 v127, v127\n\t"
        "v_smfmac_f32_16x16x64_bf16 v[132:135], v[144:147], v[112:119], v148\n\t"
        "s_cmp_lg_u32 s46, 0\n\t"
        "s_cbranch_scc1 33f\n\t"
        "s_waitcnt lgkmcnt(0)\n\t"
        "v_cmp_ne_u32 vcc, v222, v151\n\t"
        "s_cbranch_vccz 32f\n\t"
        "33:\n\t"
        "v_exp_f32 v96, v96\n\t"
        "v_exp_f32 v97, v97\n\t"
        "v_smfmac_f32_32x32x32_bf16 v[16:31], v[180:183], v[120:127], v148\n\t"
        "v_exp_f32 v98, v98\n\t"
        "v_exp_f32 v99, v99\n\t"
        "v_exp_f32 v100, v100\n\t"
        "v_smfmac_f32_16x16x64_bf16 v[132:135], v[144:147], v[120:127], v148\n\t"
        "v_exp_f32 v101, v101\n\t"
        "v_exp_f32 v102, v102\n\t"
        "v_mfma_f32_32x32x16_bf16 v[112:127], v[168:171], v[88:91], 0\n\t"
        "v_exp_f32 v103, v103\n\t"
        "v_exp_f32 v104, v104\n\t"
        "v_exp_f32 v105, v105\n\t"
        "v_mfma_f32_32x32x16_bf16 v[112:127], v[172:175], v[92:95], v[112:127]\n\t"
        "v_exp_f32 v106, v106\n\t"
        "v_exp_f32 v107, v107\n\t"
        "v_exp_f32 v108, v108\n\t"
        "v_smfmac_f32_32x32x32_bf16 v[32:47], v[176:179], v[96:103], v148\n\t"
        "v_exp_f32 v109, v109\n\t"
        "v_exp_f32 v110, v110\n\t"
        "v_exp_f32 v111, v111\n\t"
        "v_smfmac_f32_16x16x64_bf16 v[136:139], v[144:147], v[96:103], v148\n\t"
        "s_cmp_lg_u32 s46, 0\n\t"
        "s_cbranch_scc1 5f\n\t"
        "s_mov_b32 s45, 0x200000\n\t"
        "2:\n\t"
        "ds_read_b32 v151, v224\n\t"
        "s_sub_u32 s45, s45, 1\n\t"
        "s_waitcnt lgkmcnt(0)\n\t"
        "s_cbranch_scc1 3f\n\t"
        "v_cmp_ne_u32 vcc, v222, v151\n\t"
        "s_cbranch_vccnz 2b\n\t"
        "3:\n\t"
        "s_add_u32 s44, s44, 0x4000\n\t"
        "s_and_b32 s44, s44, 0x3ffff\n\t"
        "s_cmp_eq_u32 s44, s49\n\t"
        "s_cselect_b32 s44, s50, s44\n\t"
        "s_add_u32 s52, s40, s44\n\t"
        "s_addc_u32 s53, s41, 0\n\t"
        "buffer_load_dwordx4 v[184:187], v149, s[52:55], 0 offen offset:0 sc1\n\t"
        "buffer_load_dwordx4 v[188:191], v149, s[52:55], 0 offen offset:1024 sc1\n\t"
        "buffer_load_dwordx4 v[192:195], v149, s[52:55], 0 offen offset:2048 sc1\n\t"
        "buffer_load_dwordx4 v[196:199], v149, s[52:55], 0 offen offset:3072 sc1\n\t"
        "buffer_load_dwordx4 v[200:203], v150, s[52:55], 0 offen offset:0 sc1\n\t"
        "buffer_load_dwordx4 v[204:207], v150, s[52:55], 0 offen offset:1024 sc1\n\t"
        "buffer_load_dwordx4 v[208:211], v150, s[52:55], 0 offen offset:2048 sc1\n\t"
        "buffer_load_dwordx4 v[212:215], v150, s[52:55], 0 offen offset:3072 sc1\n\t"
        "5:\n\t"
        "s_add_u32 s44, s44, 0x4000\n\t"
        "s_and_b32 s44, s44, 0x3ffff\n\t"
        "s_cmp_eq_u32 s44, s49\n\t"
        "s_cselect_b32 s44, s50, s44\n\t"
        "s_add_u32 s52, s40, s44\n\t"
        "s_addc_u32 s53, s41, 0\n\t"
        "buffer_load_dwordx4 v[152:155], v149, s[52:55], 0 offen offset:0 sc1\n\t"
        "buffer_load_dwordx4 v[156:159], v149, s[52:55], 0 offen offset:1024 sc1\n\t"
        "buffer_load_dwordx4 v[160:163], v149, s[52:55], 0 offen offset:2048 sc1\n\t"
        "buffer_load_dwordx4 v[164:167], v149, s[52:55], 0 offen offset:3072 sc1\n\t"
        "s_mov_b32 s45, 6\n\t"
        "1:\n\t"
        "v_exp_f32 v112, v112\n\t"
        "v_exp_f32 v113, v113\n\t"
        "v_smfmac_f32_32x32x32_bf16 v[32:47], v[180:183], v[104:111], v148\n\t"
        "v_exp_f32 v114, v114\n\t"
        "v_exp_f32 v115, v115\n\t"
        "v_exp_f32 v116, v116\n\t"
        "v_smfmac_f32_16x16x64_bf16 v[136:139], v[144:147], v[104:111], v148\n\t"
        "s_waitcnt vmcnt(8)\n\t"
        "v_exp_f32 v117, v117\n\t"
        "v_exp_f32 v118, v118\n\t"
        "v_mfma_f32_32x32x16_bf16 v[96:111], v[184:187], v[64:67], 0\n\t"
        "v_exp_f32 v119, v119\n\t"
        "v_exp_f32 v120, v120\n\t"
        "v_exp_f32 v121, v121\n\t"
        "v_mfma_f32_32x32x16_bf16 v[96:111], v[188:191], v[68:71], v[96:111]\n\t"
        "v_exp_f32 v122, v122\n\t"
        "v_exp_f32 v123, v123\n\t"
        "v_exp_f32 v124, v124\n\t"
        "v_smfmac_f32_32x32x32_bf16 v[48:63], v[176:179], v[112:119], v148\n\t"
        "v_exp_f32 v125, v125\n\t"
        "v_exp_f32 v126, v126\n\t"
        "v_exp_f32 v127, v127\n\t"
        "v_smfmac_f32_16x16x64_bf16 v[140:143], v[144:147], v[112:119], v148\n\t"
        "buffer_load_dwordx4 v[168:171], v150, s[52:55], 0 offen offset:0 sc1\n\t"
        "v_exp_f32 v96, v96\n\t"
        "v_exp_f32 v97, v97\n\t"
        "v_smfmac_f32_32x32x32_bf16 v[48:63], v[180:183], v[120:127], v148\n\t"
        "v_exp_f32 v98, v98\n\t"
        "v_exp_f32 v99, v99\n\t"
        "v_exp_f32 v100, v100\n\t"
        "v_smfmac_f32_16x16x64_bf16 v[140:143], v[144:147], v[120:127], v148\n\t"
        "v_exp_f32 v101, v101\n\t"
        "v_exp_f32 v102, v102\n\t"
        "v_mfma_f32_32x32x16_bf16 v[112:127], v[184:187], v[72:75], 0\n\t"
        "v_exp_f32 v103, v103\n\t"
        "v_exp_f32 v104, v104\n\t"
        "v_exp_f32 v105, v105\n\t"
        "v_mfma_f32_32x32x16_bf16 v[112:127], v[188:191], v[76:79], v[112:127]\n\t"
        "v_exp_f32 v106, v106\n\t"
        "v_exp_f32 v107, v107\n\t"
        "v_exp_f32 v108, v108\n\t"
        "v_smfmac_f32_32x32x32_bf16 v[0:15], v[192:195], v[96:103], v148\n\t"
        "v_exp_f32 v109, v109\n\t"
        "v_exp_f32 v110, v110\n\t"
        "v_exp_f32 v111, v111\n\t"
        "v_smfmac_f32_16x16x64_bf16 v[128:131], v[144:147], v[96:103], v148\n\t"
        "buffer_load_dwordx4 v[172:175], v150, s[52:55], 0 offen offset:1024 sc1\n\t"
        "v_exp_f32 v112, v112\n\t"
        "v_exp_f32 v113, v113\n\t"
        "v_smfmac_f32_32x32x32_bf16 v[0:15], v[196:199], v[104:111], v148\n\t"
        "v_exp_f32 v114, v114\n\t"
        "v_exp_f32 v115, v115\n\t"
        "v_exp_f32 v116, v116\n\t"
        "v_smfmac_f32_16x16x64_bf16 v[128:131], v[144:147], v[104:111], v148\n\t"
        "v_exp_f32 v117, v117\n\t"
        "v_exp_f32 v118, v118\n\t"
        "v_mfma_f32_32x32x16_bf16 v[96:111], v[184:187], v[80:83], 0\n\t"
        "v_exp_f32 v119, v119\n\t"
        "v_exp_f32 v120, v120\n\t"
        "v_exp_f32 v121, v121\n\t"
        "v_mfma_f32_32x32x16_bf16 v[96:111], v[188:191], v[84:87], v[96:111]\n\t"
        "v_exp_f32 v122, v122\n\t"
        "v_exp_f32 v123, v123\n\t"
        "v_exp_f32 v124, v124\n\t"
        "v_smfmac_f32_32x32x32_bf16 v[16:31], v[192:195], v[112:119], v148\n\t"
        "v_exp_f32 v125, v125\n\t"
        "v_exp_f32 v126, v126\n\t"
        "v_exp_f32 v127, v127\n\t"
        "v_smfmac_f32_16x16x64_bf16 v[132:135], v[144:147], v[112:119], v148\n\t"
        "buffer_load_dwordx4 v[176:179], v150, s[52:55], 0 offen offset:2048 sc1\n\t"
        "v_exp_f32 v96, v96\n\t"
        "v_exp_f32 v97, v97\n\t"
        "v_smfmac_f32_32x32x32_bf16 v[16:31], v[196:199], v[120:127], v148\n\t"
        "v_exp_f32 v98, v98\n\t"
        "v_exp_f32 v99, v99\n\t"
        "v_exp_f32 v100, v100\n\t"
        "v_smfmac_f32_16x16x64_bf16 v[132:135], v[144:147], v[120:127], v148\n\t"
        "v_exp_f32 v101, v101\n\t"
        "v_exp_f32 v102, v102\n\t"
        "v_mfma_f32_32x32x16_bf16 v[112:127], v[184:187], v[88:91], 0\n\t"
        "v_exp_f32 v103, v103\n\t"
        "v_exp_f32 v104, v104\n\t"
        "v_exp_f32 v105, v105\n\t"
        "v_mfma_f32_32x32x16_bf16 v[112:127], v[188:191], v[92:95], v[112:127]\n\t"
        "v_exp_f32 v106, v106\n\t"
        "v_exp_f32 v107, v107\n\t"
        "v_exp_f32 v108, v108\n\t"
        "v_smfmac_f32_32x32x32_bf16 v[32:47], v[192:195], v[96:103], v148\n\t"
        "v_exp_f32 v109, v109\n\t"
        "v_exp_f32 v110, v110\n\t"
        "v_exp_f32 v111, v111\n\t"
        "v_smfmac_f32_16x16x64_bf16 v[136:139], v[144:147], v[96:103], v148\n\t"
        "buffer_load_dwordx4 v[180:183], v150, s[52:55], 0 offen offset:3072 sc1\n\t"
        "v_exp_f32 v112, v112\n\t"
        "v_exp_f32 v113, v113\n\t"
        "v_smfmac_f32_32x32x32_bf16 v[32:47], v[196:199], v[104:111], v148\n\t"
        "s_add_u32 s44, s44, 0x4000\n\t"
        "s_and_b32 s44, s44, 0x3ffff\n\t"
        "v_exp_f32 v114, v114\n\t"
        "v_exp_f32 v115, v115\n\t"
        "v_exp_f32 v116, v116\n\t"
        "v_smfmac_f32_16x16x64_bf16 v[136:139], v[144:147], v[104:111], v148\n\t"
        "s_waitcnt vmcnt(8)\n\t"
        "v_exp_f32 v117, v117\n\t"
        "v_exp_f32 v118, v118\n\t"
        "v_mfma_f32_32x32x16_bf16 v[96:111], v[200:203], v[64:67], 0\n\t"
        "s_cmp_eq_u32 s44, s49\n\t"
        "v_exp_f32 v119, v119\n\t"
        "v_exp_f32 v120, v120\n\t"
        "v_exp_f32 v121, v121\n\t"
        "v_mfma_f32_32x32x16_bf16 v[96:111], v[204:207], v[68:71], v[96:111]\n\t"
        "s_cselect_b32 s44, s50, s44\n\t"
        "v_exp_f32 v122, v122\n\t"
        "v_exp_f32 v123, v123\n\t"
        "v_exp_f32 v124, v124\n\t"
        "v_smfmac_f32_32x32x32_bf16 v[48:63], v[192:195], v[112:119], v148\n\t"
        "s_add_u32 s52, s40, s44\n\t"
        "s_addc_u32 s53, s41, 0\n\t"
        "v_exp_f32 v125, v125\n\t"
        "v_exp_f32 v126, v126\n\t"
        "v_exp_f32 v127, v127\n\t"
        "v_smfmac_f32_16x16x64_bf16 v[140:143], v[144:147], v[112:119], v148\n\t"
        "buffer_load_dwordx4 v[184:187], v149, s[52:55], 0 offen offset:0 sc1\n\t"
        "v_exp_f32 v96, v96\n\t"
        "v_exp_f32 v97, v97\n\t"
        "v_smfmac_f32_32x32x32_bf16 v[48:63], v[196:199], v[120:127], v148\n\t"
        "v_exp_f32 v98, v98\n\t"
        "v_exp_f32 v99, v99\n\t"
        "v_exp_f32 v100, v100\n\t"
        "v_smfmac_f32_16x16x64_bf16 v[140:143], v[144:147], v[120:127], v148\n\t"
        "v_exp_f32 v101, v101\n\t"
        "v_exp_f32 v102, v102\n\t"
        "v_mfma_f32_32x32x16_bf16 v[112:127], v[200:203], v[72:75], 0\n\t"
        "v_exp_f32 v103, v103\n\t"
        "v_exp_f32 v104, v104\n\t"
        "v_exp_f32 v105, v105\n\t"
        "v_mfma_f32_32x32x16_bf16 v[112:127], v[204:207], v[76:79], v[112:127]\n\t"
        "v_exp_f32 v106, v106\n\t"
        "v_exp_f32 v107, v107\n\t"
        "v_exp_f32 v108, v108\n\t"
        "v_smfmac_f32_32x32x32_bf16 v[0:15], v[208:211], v[96:103], v148\n\t"
        "v_exp_f32 v109, v109\n\t"
        "v_exp_f32 v110, v110\n\t"
        "v_exp_f32 v111, v111\n\t"
        "v_smfmac_f32_16x16x64_bf16 v[128:131], v[144:147], v[96:103], v148\n\t"
        "buffer_load_dwordx4 v[188:191], v149, s[52:55], 0 offen offset:1024 sc1\n\t"
        "v_exp_f32 v112, v112\n\t"
        "v_exp_f32 v113, v113\n\t"
        "v_smfmac_f32_32x32x32_bf16 v[0:15], v[212:215], v[104:111], v148\n\t"
        "v_exp_f32 v114, v114\n\t"
        "v_exp_f32 v115, v115\n\t"
        "v_exp_f32 v116, v116\n\t"
        "v_smfmac_f32_16x16x64_bf16 v[128:131], v[144:147], v[104:111], v148\n\t"
        "v_exp_f32 v117, v117\n\t"
        "v_exp_f32 v118, v118\n\t"
        "v_mfma_f32_32x32x16_bf16 v[96:111], v[200:203], v[80:83], 0\n\t"
        "v_exp_f32 v119, v119\n\t"
        "v_exp_f32 v120, v120\n\t"
        "v_exp_f32 v121, v121\n\t"
        "v_mfma_f32_32x32x16_bf16 v[96:111], v[204:207], v[84:87], v[96:111]\n\t"
        "v_exp_f32 v122, v122\n\t"
        "v_exp_f32 v123, v123\n\t"
        "v_exp_f32 v124, v124\n\t"
        "v_smfmac_f32_32x32x32_bf16 v[16:31], v[208:211], v[112:119], v148\n\t"
        "v_exp_f32 v125, v125\n\t"
        "v_exp_f32 v126, v126\n\t"
        "v_exp_f32 v127, v127\n\t"
        "v_smfmac_f32_16x16x64_bf16 v[132:135], v[144:147], v[112:119], v148\n\t"
        "buffer_load_dwordx4 v[192:195], v149, s[52:55], 0 offen offset:2048 sc1\n\t"
        "v_exp_f32 v96, v96\n\t"
        "v_exp_f32 v97, v97\n\t"
        "v_smfmac_f32_32x32x32_bf16 v[16:31], v[212:215], v[120:127], v148\n\t"
        "v_exp_f32 v98, v98\n\t"
        "v_exp_f32 v99, v99\n\t"
        "v_exp_f32 v100, v100\n\t"
        "v_smfmac_f32_16x16x64_bf16 v[132:135], v[144:147], v[120:127], v148\n\t"
        "v_exp_f32 v101, v101\n\t"
        "v_exp_f32 v102, v102\n\t"
        "v_mfma_f32_32x32x16_bf16 v[112:127], v[200:203], v[88:91], 0\n\t"
        "v_exp_f32 v103, v103\n\t"
        "v_exp_f32 v104, v104\n\t"
        "v_exp_f32 v105, v105\n\t"
        "v_mfma_f32_32x32x16_bf16 v[112:127], v[204:207], v[92:95], v[112:127]\n\t"
        "v_exp_f32 v106, v106\n\t"
        "v_exp_f32 v107, v107\n\t"
        "v_exp_f32 v108, v108\n\t"
        "v_smfmac_f32_32x32x32_bf16 v[32:47], v[208:211], v[96:103], v148\n\t"
        "v_exp_f32 v109, v109\n\t"
        "v_exp_f32 v110, v110\n\t"
        "v_exp_f32 v111, v111\n\t"
        "v_smfmac_f32_16x16x64_bf16 v[136:139], v[144:147], v[96:103], v148\n\t"
        "buffer_load_dwordx4 v[196:199], v149, s[52:55], 0 offen offset:3072 sc1\n\t"
        "v_exp_f32 v112, v112\n\t"
        "v_exp_f32 v113, v113\n\t"
        "v_smfmac_f32_32x32x32_bf16 v[32:47], v[212:215], v[104:111], v148\n\t"
        "v_exp_f32 v114, v114\n\t"
        "v_exp_f32 v115, v115\n\t"
        "v_exp_f32 v116, v116\n\t"
        "v_smfmac_f32_16x16x64_bf16 v[136:139], v[144:147], v[104:111], v148\n\t"
        "s_waitcnt vmcnt(8)\n\t"
        "v_exp_f32 v117, v117\n\t"
        "v_exp_f32 v118, v118\n\t"
        "v_mfma_f32_32x32x16_bf16 v[96:111], v[152:155], v[64:67], 0\n\t"
        "v_exp_f32 v119, v119\n\t"
        "v_exp_f32 v120, v120\n\t"
        "v_exp_f32 v121, v121\n\t"
        "v_mfma_f32_32x32x16_bf16 v[96:111], v[156:159], v[68:71], v[96:111]\n\t"
        "v_exp_f32 v122, v122\n\t"
        "v_exp_f32 v123, v123\n\t"
        "v_exp_f32 v124, v124\n\t"
        "v_smfmac_f32_32x32x32_bf16 v[48:63], v[208:211], v[112:119], v148\n\t"
        "v_exp_f32 v125, v125\n\t"
        "v_exp_f32 v126, v126\n\t"
        "v_exp_f32 v127, v127\n\t"
        "v_smfmac_f32_16x16x64_bf16 v[140:143], v[144:147], v[112:119], v148\n\t"
        "buffer_load_dwordx4 v[200:203], v150, s[52:55], 0 offen offset:0 sc1\n\t"
        "v_exp_f32 v96, v96\n\t"
        "v_exp_f32 v97, v97\n\t"
        "v_smfmac_f32_32x32x32_bf16 v[48:63], v[212:215], v[120:127], v148\n\t"
        "v_exp_f32 v98, v98\n\t"
        "v_exp_f32 v99, v99\n\t"
        "v_exp_f32 v100, v100\n\t"
        "v_smfmac_f32_16x16x64_bf16 v[140:143], v[144:147], v[120:127], v148\n\t"
        "v_exp_f32 v101, v101\n\t"
        "v_exp_f32 v102, v102\n\t"
        "v_mfma_f32_32x32x16_bf16 v[112:127], v[152:155], v[72:75], 0\n\t"
        "v_exp_f32 v103, v103\n\t"
        "v_exp_f32 v104, v104\n\t"
        "v_exp_f32 v105, v105\n\t"
        "v_mfma_f32_32x32x16_bf16 v[112:127], v[156:159], v[76:79], v[112:127]\n\t"
        "v_exp_f32 v106, v106\n\t"
        "v_exp_f32 v107, v107\n\t"
        "v_exp_f32 v108, v108\n\t"
        "v_smfmac_f32_32x32x32_bf16 v[0:15], v[160:163], v[96:103], v148\n\t"
        "v_exp_f32 v109, v109\n\t"
        "v_exp_f32 v110, v110\n\t"
        "v_exp_f32 v111, v111\n\t"
        "v_smfmac_f32_16x16x64_bf16 v[128:131], v[144:147], v[96:103], v148\n\t"
        "buffer_load_dwordx4 v[204:207], v150, s[52:55], 0 offen offset:1024 sc1\n\t"
        "v_exp_f32 v112, v112\n\t"
        "v_exp_f32 v113, v113\n\t"
        "v_smfmac_f32_32x32x32_bf16 v[0:15], v[164:167], v[104:111], v148\n\t"
        "v_exp_f32 v114, v114\n\t"
        "v_exp_f32 v115, v115\n\t"
        "v_exp_f32 v116, v116\n\t"
        "v_smfmac_f32_16x16x64_bf16 v[128:131], v[144:147], v[104:111], v148\n\t"
        "v_exp_f32 v117, v117\n\t"
        "v_exp_f32 v118, v118\n\t"
        "v_mfma_f32_32x32x16_bf16 v[96:111], v[152:155], v[80:83], 0\n\t"
        "v_exp_f32 v119, v119\n\t"
        "v_exp_f32 v120, v120\n\t"
        "v_exp_f32 v121, v121\n\t"
        "v_mfma_f32_32x32x16_bf16 v[96:111], v[156:159], v[84:87], v[96:111]\n\t"
        "v_exp_f32 v122, v122\n\t"
        "v_exp_f32 v123, v123\n\t"
        "v_exp_f32 v124, v124\n\t"
        "v_smfmac_f32_32x32x32_bf16 v[16:31], v[160:163], v[112:119], v148\n\t"
        "v_exp_f32 v125, v125\n\t"
        "v_exp_f32 v126, v126\n\t"
        "v_exp_f32 v127, v127\n\t"
        "v_smfmac_f32_16x16x64_bf16 v[132:135], v[144:147], v[112:119], v148\n\t"
        "buffer_load_dwordx4 v[208:211], v150, s[52:55], 0 offen offset:2048 sc1\n\t"
        "v_exp_f32 v96, v96\n\t"
        "v_exp_f32 v97, v97\n\t"
        "v_smfmac_f32_32x32x32_bf16 v[16:31], v[164:167], v[120:127], v148\n\t"
        "v_exp_f32 v98, v98\n\t"
        "v_exp_f32 v99, v99\n\t"
        "v_exp_f32 v100, v100\n\t"
        "v_smfmac_f32_16x16x64_bf16 v[132:135], v[144:147], v[120:127], v148\n\t"
        "v_exp_f32 v101, v101\n\t"
        "v_exp_f32 v102, v102\n\t"
        "v_mfma_f32_32x32x16_bf16 v[112:127], v[152:155], v[88:91], 0\n\t"
        "v_exp_f32 v103, v103\n\t"
        "v_exp_f32 v104, v104\n\t"
        "v_exp_f32 v105, v105\n\t"
        "v_mfma_f32_32x32x16_bf16 v[112:127], v[156:159], v[92:95], v[112:127]\n\t"
        "v_exp_f32 v106, v106\n\t"
        "v_exp_f32 v107, v107\n\t"
        "v_exp_f32 v108, v108\n\t"
        "v_smfmac_f32_32x32x32_bf16 v[32:47], v[160:163], v[96:103], v148\n\t"
        "v_exp_f32 v109, v109\n\t"
        "v_exp_f32 v110, v110\n\t"
        "v_exp_f32 v111, v111\n\t"
        "v_smfmac_f32_16x16x64_bf16 v[136:139], v[144:147], v[96:103], v148\n\t"
        "buffer_load_dwordx4 v[212:215], v150, s[52:55], 0 offen offset:3072 sc1\n\t"
        "v_exp_f32 v112, v112\n\t"
        "v_exp_f32 v113, v113\n\t"
        "v_smfmac_f32_32x32x32_bf16 v[32:47], v[164:167], v[104:111], v148\n\t"
        "s_add_u32 s44, s44, 0x4000\n\t"
        "s_and_b32 s44, s44, 0x3ffff\n\t"
        "v_exp_f32 v114, v114\n\t"
        "v_exp_f32 v115, v115\n\t"
        "v_exp_f32 v116, v116\n\t"
        "v_smfmac_f32_16x16x64_bf16 v[136:139], v[144:147], v[104:111], v148\n\t"
        "s_waitcnt vmcnt(8)\n\t"
        "v_exp_f32 v117, v117\n\t"
        "v_exp_f32 v118, v118\n\t"
        "v_mfma_f32_32x32x16_bf16 v[96:111], v[168:171], v[64:67], 0\n\t"
        "s_cmp_eq_u32 s44, s49\n\t"
        "v_exp_f32 v119, v119\n\t"
        "v_exp_f32 v120, v120\n\t"
        "v_exp_f32 v121, v121\n\t"
        "v_mfma_f32_32x32x16_bf16 v[96:111], v[172:175], v[68:71], v[96:111]\n\t"
        "s_cselect_b32 s44, s50, s44\n\t"
        "v_exp_f32 v122, v122\n\t"
        "v_exp_f32 v123, v123\n\t"
        "v_exp_f32 v124, v124\n\t"
        "v_smfmac_f32_32x32x32_bf16 v[48:63], v[160:163], v[112:119], v148\n\t"
        "s_add_u32 s52, s40, s44\n\t"
        "s_addc_u32 s53, s41, 0\n\t"
        "v_exp_f32 v125, v125\n\t"
        "v_exp_f32 v126, v126\n\t"
        "v_exp_f32 v127, v127\n\t"
        "v_smfmac_f32_16x16x64_bf16 v[140:143], v[144:147], v[112:119], v148\n\t"
        "buffer_load_dwordx4 v[152:155], v149, s[52:55], 0 offen offset:0 sc1\n\t"
        "v_exp_f32 v96, v96\n\t"
        "v_exp_f32 v97, v97\n\t"
        "v_smfmac_f32_32x32x32_bf16 v[48:63], v[164:167], v[120:127], v148\n\t"
        "v_exp_f32 v98, v98\n\t"
        "v_exp_f32 v99, v99\n\t"
        "v_exp_f32 v100, v100\n\t"
        "v_smfmac_f32_16x16x64_bf16 v[140:143], v[144:147], v[120:127], v148\n\t"
        "v_exp_f32 v101, v101\n\t"
        "v_exp_f32 v102, v102\n\t"
        "v_mfma_f32_32x32x16_bf16 v[112:127], v[168:171], v[72:75], 0\n\t"
        "v_exp_f32 v103, v103\n\t"
        "v_exp_f32 v104, v104\n\t"
        "v_exp_f32 v105, v105\n\t"
        "v_mfma_f32_32x32x16_bf16 v[112:127], v[172:175], v[76:79], v[112:127]\n\t"
        "v_exp_f32 v106, v106\n\t"
        "v_exp_f32 v107, v107\n\t"
        "v_exp_f32 v108, v108\n\t"
        "v_smfmac_f32_32x32x32_bf16 v[0:15], v[176:179], v[96:103], v148\n\t"
        "v_exp_f32 v109, v109\n\t"
        "v_exp_f32 v110, v110\n\t"
        "v_exp_f32 v111, v111\n\t"
        "v_smfmac_f32_16x16x64_bf16 v[128:131], v[144:147], v[96:103], v148\n\t"
        "buffer_load_dwordx4 v[156:159], v149, s[52:55], 0 offen offset:1024 sc1\n\t"
        "v_exp_f32 v112, v112\n\t"
        "v_exp_f32 v113, v113\n\t"
        "v_smfmac_f32_32x32x32_bf16 v[0:15], v[180:183], v[104:111], v148\n\t"
        "v_exp_f32 v114, v114\n\t"
        "v_exp_f32 v115, v115\n\t"
        "v_exp_f32 v116, v116\n\t"
        "v_smfmac_f32_16x16x64_bf16 v[128:131], v[144:147], v[104:111], v148\n\t"
        "v_exp_f32 v117, v117\n\t"
        "v_exp_f32 v118, v118\n\t"
        "v_mfma_f32_32x32x16_bf16 v[96:111], v[168:171], v[80:83], 0\n\t"
        "v_exp_f32 v119, v119\n\t"
        "v_exp_f32 v120, v120\n\t"
        "v_exp_f32 v121, v121\n\t"
        "v_mfma_f32_32x32x16_bf16 v[96:111], v[172:175], v[84:87], v[96:111]\n\t"
        "v_exp_f32 v122, v122\n\t"
        "v_exp_f32 v123, v123\n\t"
        "v_exp_f32 v124, v124\n\t"
        "v_smfmac_f32_32x32x32_bf16 v[16:31], v[176:179], v[112:119], v148\n\t"
        "v_exp_f32 v125, v125\n\t"
        "v_exp_f32 v126, v126\n\t"
        "v_exp_f32 v127, v127\n\t"
        "v_smfmac_f32_16x16x64_bf16 v[132:135], v[144:147], v[112:119], v148\n\t"
        "buffer_load_dwordx4 v[160:163], v149, s[52:55], 0 offen offset:2048 sc1\n\t"
        "v_exp_f32 v96, v96\n\t"
        "v_exp_f32 v97, v97\n\t"
        "v_smfmac_f32_32x32x32_bf16 v[16:31], v[180:183], v[120:127], v148\n\t"
        "v_exp_f32 v98, v98\n\t"
        "v_exp_f32 v99, v99\n\t"
        "v_exp_f32 v100, v100\n\t"
        "v_smfmac_f32_16x16x64_bf16 v[132:135], v[144:147], v[120:127], v148\n\t"
        "v_exp_f32 v101, v101\n\t"
        "v_exp_f32 v102, v102\n\t"
        "v_mfma_f32_32x32x16_bf16 v[112:127], v[168:171], v[88:91], 0\n\t"
        "v_exp_f32 v103, v103\n\t"
        "v_exp_f32 v104, v104\n\t"
        "v_exp_f32 v105, v105\n\t"
        "v_mfma_f32_32x32x16_bf16 v[112:127], v[172:175], v[92:95], v[112:127]\n\t"
        "v_exp_f32 v106, v106\n\t"
        "v_exp_f32 v107, v107\n\t"
        "v_exp_f32 v108, v108\n\t"
        "v_smfmac_f32_32x32x32_bf16 v[32:47], v[176:179], v[96:103], v148\n\t"
        "s_sub_u32 s45, s45, 1\n\t"
        "v_exp_f32 v109, v109\n\t"
        "v_exp_f32 v110, v110\n\t"
        "v_exp_f32 v111, v111\n\t"
        "v_smfmac_f32_16x16x64_bf16 v[136:139], v[144:147], v[96:103], v148\n\t"
        "buffer_load_dwordx4 v[164:167], v149, s[52:55], 0 offen offset:3072 sc1\n\t"
        "s_cbranch_scc0 1b\n\t"
        "v_exp_f32 v112, v112\n\t"
        "v_exp_f32 v113, v113\n\t"
        "v_smfmac_f32_32x32x32_bf16 v[32:47], v[180:183], v[104:111], v148\n\t"
        "v_exp_f32 v114, v114\n\t"
        "v_exp_f32 v115, v115\n\t"
        "v_exp_f32 v116, v116\n\t"
        "v_smfmac_f32_16x16x64_bf16 v[136:139], v[144:147], v[104:111], v148\n\t"
        "s_waitcnt vmcnt(8)\n\t"
        "v_exp_f32 v117, v117\n\t"
        "v_exp_f32 v118, v118\n\t"
        "v_mfma_f32_32x32x16_bf16 v[96:111], v[184:187], v[64:67], 0\n\t"
        "v_exp_f32 v119, v119\n\t"
        "v_exp_f32 v120, v120\n\t"
        "v_exp_f32 v121, v121\n\t"
        "v_mfma_f32_32x32x16_bf16 v[96:111], v[188:191], v[68:71], v[96:111]\n\t"
        "v_exp_f32 v122, v122\n\t"
        "v_exp_f32 v123, v123\n\t"
        "v_exp_f32 v124, v124\n\t"
        "v_smfmac_f32_32x32x32_bf16 v[48:63], v[176:179], v[112:119], v148\n\t"
        "v_exp_f32 v125, v125\n\t"
        "v_exp_f32 v126, v126\n\t"
        "v_exp_f32 v127, v127\n\t"
        "v_smfmac_f32_16x16x64_bf16 v[140:143], v[144:147], v[112:119], v148\n\t"
        "v_exp_f32 v96, v96\n\t"
        "v_exp_f32 v97, v97\n\t"
        "v_smfmac_f32_32x32x32_bf16 v[48:63], v[180:183], v[120:127], v148\n\t"
        "v_exp_f32 v98, v98\n\t"
        "v_exp_f32 v99, v99\n\t"
        "v_exp_f32 v100, v100\n\t"
        "v_smfmac_f32_16x16x64_bf16 v[140:143], v[144:147], v[120:127], v148\n\t"
        "v_exp_f32 v101, v101\n\t"
        "v_exp_f32 v102, v102\n\t"
        "v_mfma_f32_32x32x16_bf16 v[112:127], v[184:187], v[72:75], 0\n\t"
        "v_exp_f32 v103, v103\n\t"
        "v_exp_f32 v104, v104\n\t"
        "v_exp_f32 v105, v105\n\t"
        "v_mfma_f32_32x32x16_bf16 v[112:127], v[188:191], v[76:79], v[112:127]\n\t"
        "v_exp_f32 v106, v106\n\t"
        "v_exp_f32 v107, v107\n\t"
        "v_exp_f32 v108, v108\n\t"
        "v_smfmac_f32_32x32x32_bf16 v[0:15], v[192:195], v[96:103], v148\n\t"
        "v_exp_f32 v109, v109\n\t"
        "v_exp_f32 v110, v110\n\t"
        "v_exp_f32 v111, v111\n\t"
        "v_smfmac_f32_16x16x64_bf16 v[128:131], v[144:147], v[96:103], v148\n\t"
        "v_exp_f32 v112, v112\n\t"
        "v_exp_f32 v113, v113\n\t"
        "v_smfmac_f32_32x32x32_bf16 v[0:15], v[196:199], v[104:111], v148\n\t"
        "v_exp_f32 v114, v114\n\t"
        "v_exp_f32 v115, v115\n\t"
        "v_exp_f32 v116, v116\n\t"
        "v_smfmac_f32_16x16x64_bf16 v[128:131], v[144:147], v[104:111], v148\n\t"
        "s_waitcnt vmcnt(4)\n\t"
        "v_exp_f32 v117, v117\n\t"
        "v_exp_f32 v118, v118\n\t"
        "v_mfma_f32_32x32x16_bf16 v[96:111], v[200:203], v[64:67], 0\n\t"
        "v_exp_f32 v119, v119\n\t"
        "v_exp_f32 v120, v120\n\t"
        "v_exp_f32 v121, v121\n\t"
        "v_mfma_f32_32x32x16_bf16 v[96:111], v[204:207], v[68:71], v[96:111]\n\t"
        "v_exp_f32 v122, v122\n\t"
        "v_exp_f32 v123, v123\n\t"
        "v_exp_f32 v124, v124\n\t"
        "v_smfmac_f32_32x32x32_bf16 v[16:31], v[192:195], v[112:119], v148\n\t"
        "v_exp_f32 v125, v125\n\t"
        "v_exp_f32 v126, v126\n\t"
        "v_exp_f32 v127, v127\n\t"
        "v_smfmac_f32_16x16x64_bf16 v[132:135], v[144:147], v[112:119], v148\n\t"
        "s_cmp_lg_u32 s47, 0\n\t"
        "s_cbranch_scc1 4f\n\t"
        "global_load_dwordx4 %[r0], v223, %[xb] offset:0\n\t"
        "global_load_dwordx4 %[r1], v223, %[xb] offset:32\n\t"
        "global_load_dwordx4 %[r2], v223, %[xb] offset:64\n\t"
        "global_load_dwordx4 %[r3], v223, %[xb] offset:96\n\t"
        "4:\n\t"
        "v_exp_f32 v96, v96\n\t"
        "v_exp_f32 v97, v97\n\t"
        "v_smfmac_f32_32x32x32_bf16 v[16:31], v[196:199], v[120:127], v148\n\t"
        "v_exp_f32 v98, v98\n\t"
        "v_exp_f32 v99, v99\n\t"
        "v_exp_f32 v100, v100\n\t"
        "v_smfmac_f32_16x16x64_bf16 v[132:135], v[144:147], v[120:127], v148\n\t"
        "v_exp_f32 v101, v101\n\t"
        "v_exp_f32 v102, v102\n\t"
        "v_mfma_f32_32x32x16_bf16 v[112:127], v[200:203], v[72:75], 0\n\t"
        "v_exp_f32 v103, v103\n\t"
        "v_exp_f32 v104, v104\n\t"
        "v_exp_f32 v105, v105\n\t"
        "v_mfma_f32_32x32x16_bf16 v[112:127], v[204:207], v[76:79], v[112:127]\n\t"
        "v_exp_f32 v106, v106\n\t"
        "v_exp_f32 v107, v107\n\t"
        "v_exp_f32 v108, v108\n\t"
        "v_smfmac_f32_32x32x32_bf16 v[0:15], v[208:211], v[96:103], v148\n\t"
        "v_exp_f32 v109, v109\n\t"
        "v_exp_f32 v110, v110\n\t"
        "v_exp_f32 v111, v111\n\t"
        "v_smfmac_f32_16x16x64_bf16 v[128:131], v[144:147], v[96:103], v148\n\t"
        "v_exp_f32 v112, v112\n\t"
        "v_exp_f32 v113, v113\n\t"
        "v_smfmac_f32_32x32x32_bf16 v[0:15], v[212:215], v[104:111], v148\n\t"
        "v_exp_f32 v114, v114\n\t"
        "v_exp_f32 v115, v115\n\t"
        "v_exp_f32 v116, v116\n\t"
        "v_smfmac_f32_16x16x64_bf16 v[128:131], v[144:147], v[104:111], v148\n\t"
        "v_exp_f32 v117, v117\n\t"
        "v_exp_f32 v118, v118\n\t"
        "v_mfma_f32_32x32x16_bf16 v[96:111], v[184:187], v[80:83], 0\n\t"
        "v_exp_f32 v119, v119\n\t"
        "v_exp_f32 v120, v120\n\t"
        "v_exp_f32 v121, v121\n\t"
        "v_mfma_f32_32x32x16_bf16 v[96:111], v[188:191], v[84:87], v[96:111]\n\t"
        "ds_write_b128 v216, v[0:3] offset:0\n\t"
        "ds_write_b128 v216, v[4:7] offset:1024\n\t"
        "v_exp_f32 v122, v122\n\t"
        "v_exp_f32 v123, v123\n\t"
        "v_exp_f32 v124, v124\n\t"
        "v_smfmac_f32_32x32x32_bf16 v[16:31], v[208:211], v[112:119], v148\n\t"
        "ds_write_b128 v216, v[8:11] offset:2048\n\t"
        "ds_write_b128 v216, v[12:15] offset:3072\n\t"
        "v_exp_f32 v125, v125\n\t"
        "v_exp_f32 v126, v126\n\t"
        "v_exp_f32 v127, v127\n\t"
        "v_smfmac_f32_16x16x64_bf16 v[132:135], v[144:147], v[112:119], v148\n\t"
        "ds_write_b32 v218, v128 offset:0\n\t"
        "v_exp_f32 v96, v96\n\t"
        "v_exp_f32 v97, v97\n\t"
        "v_smfmac_f32_32x32x32_bf16 v[16:31], v[212:215], v[120:127], v148\n\t"
        "v_exp_f32 v98, v98\n\t"
        "v_exp_f32 v99, v99\n\t"
        "v_exp_f32 v100, v100\n\t"
        "v_smfmac_f32_16x16x64_bf16 v[132:135], v[144:147], v[120:127], v148\n\t"
        "v_exp_f32 v101, v101\n\t"
        "v_exp_f32 v102, v102\n\t"
        "v_mfma_f32_32x32x16_bf16 v[112:127], v[184:187], v[88:91], 0\n\t"
        "v_exp_f32 v103, v103\n\t"
        "v_exp_f32 v104, v104\n\t"
        "v_exp_f32 v105, v105\n\t"
        "v_mfma_f32_32x32x16_bf16 v[112:127], v[188:191], v[92:95], v[112:127]\n\t"
        "ds_write_b128 v216, v[16:19] offset:4096\n\t"
        "ds_write_b128 v216, v[20:23] offset:5120\n\t"
        "v_exp_f32 v106, v106\n\t"
        "v_exp_f32 v107, v107\n\t"
        "v_exp_f32 v108, v108\n\t"
        "v_smfmac_f32_32x32x32_bf16 v[32:47], v[192:195], v[96:103], v148\n\t"
        "ds_write_b128 v216, v[24:27] offset:6144\n\t"
        "ds_write_b128 v216, v[28:31] offset:7168\n\t"
        "v_exp_f32 v109, v109\n\t"
        "v_exp_f32 v110, v110\n\t"
        "v_exp_f32 v111, v111\n\t"
        "v_smfmac_f32_16x16x64_bf16 v[136:139], v[144:147], v[96:103], v148\n\t"
        "ds_write_b32 v218, v132 offset:256\n\t"
        "ds_write_b32 v220, v222\n\t"
        "v_exp_f32 v112, v112\n\t"
        "v_exp_f32 v113, v113\n\t"
        "v_smfmac_f32_32x32x32_bf16 v[32:47], v[196:199], v[104:111], v148\n\t"
        "v_exp_f32 v114, v114\n\t"
        "v_exp_f32 v115, v115\n\t"
        "v_exp_f32 v116, v116\n\t"
        "v_smfmac_f32_16x16x64_bf16 v[136:139], v[144:147], v[104:111], v148\n\t"
        "v_exp_f32 v117, v117\n\t"
        "v_exp_f32 v118, v118\n\t"
        "v_mfma_f32_32x32x16_bf16 v[96:111], v[200:203], v[80:83], 0\n\t"
        "v_exp_f32 v119, v119\n\t"
        "v_exp_f32 v120, v120\n\t"
        "v_exp_f32 v121, v121\n\t"
        "v_mfma_f32_32x32x16_bf16 v[96:111], v[204:207], v[84:87], v[96:111]\n\t"
        "v_exp_f32 v122, v122\n\t"
        "v_exp_f32 v123, v123\n\t"
        "v_exp_f32 v124, v124\n\t"
        "v_smfmac_f32_32x32x32_bf16 v[48:63], v[192:195], v[112:119], v148\n\t"
        "v_exp_f32 v125, v125\n\t"
        "v_exp_f32 v126, v126\n\t"
        "v_exp_f32 v127, v127\n\t"
        "v_smfmac_f32_16x16x64_bf16 v[140:143], v[144:147], v[112:119], v148\n\t"
        "v_exp_f32 v96, v96\n\t"
        "v_exp_f32 v97, v97\n\t"
        "v_smfmac_f32_32x32x32_bf16 v[48:63], v[196:199], v[120:127], v148\n\t"
        "v_exp_f32 v98, v98\n\t"
        "v_exp_f32 v99, v99\n\t"
        "v_exp_f32 v100, v100\n\t"
        "v_smfmac_f32_16x16x64_bf16 v[140:143], v[144:147], v[120:127], v148\n\t"
        "v_exp_f32 v101, v101\n\t"
        "v_exp_f32 v102, v102\n\t"
        "v_mfma_f32_32x32x16_bf16 v[112:127], v[200:203], v[88:91], 0\n\t"
        "v_exp_f32 v103, v103\n\t"
        "v_exp_f32 v104, v104\n\t"
        "v_exp_f32 v105, v105\n\t"
        "v_mfma_f32_32x32x16_bf16 v[112:127], v[204:207], v[92:95], v[112:127]\n\t"
        "ds_read_b32 v151, v221\n\t"
        "v_exp_f32 v106, v106\n\t"
        "v_exp_f32 v107, v107\n\t"
        "v_exp_f32 v108, v108\n\t"
        "v_smfmac_f32_32x32x32_bf16 v[32:47], v[208:211], v[96:103], v148\n\t"
        "v_exp_f32 v109, v109\n\t"
        "v_exp_f32 v110, v110\n\t"
        "v_exp_f32 v111, v111\n\t"
        "v_smfmac_f32_16x16x64_bf16 v[136:139], v[144:147], v[96:103], v148\n\t"
        "s_waitcnt lgkmcnt(0)\n\t"
        "v_cmp_ne_u32 vcc, v222, v151\n\t"
        "s_cbranch_vccnz 40f\n\t"
        "41:\n\t"
        "v_exp_f32 v112, v112\n\t"
        "v_exp_f32 v113, v113\n\t"
        "v_smfmac_f32_32x32x32_bf16 v[32:47], v[212:215], v[104:111], v148\n\t"
        "ds_read_b128 v[0:3], v217 offset:0\n\t"
        "ds_read_b128 v[4:7], v217 offset:1024\n\t"
        "ds_read_b128 v[8:11], v217 offset:2048\n\t"
        "ds_read_b128 v[12:15], v217 offset:3072\n\t"
        "v_exp_f32 v114, v114\n\t"
        "v_exp_f32 v115, v115\n\t"
        "v_exp_f32 v116, v116\n\t"
        "v_smfmac_f32_16x16x64_bf16 v[136:139], v[144:147], v[104:111], v148\n\t"
        "ds_read_b32 v128, v219 offset:0\n\t"
        "ds_read_b128 v[16:19], v217 offset:4096\n\t"
        "ds_read_b128 v[20:23], v217 offset:5120\n\t"
        "v_exp_f32 v117, v117\n\t"
        "v_exp_f32 v118, v118\n\t"
        "v_exp_f32 v119, v119\n\t"
        "v_exp_f32 v120, v120\n\t"
        "v_exp_f32 v121, v121\n\t"
        "v_exp_f32 v122, v122\n\t"
        "v_exp_f32 v123, v123\n\t"
        "v_exp_f32 v124, v124\n\t"
        "v_smfmac_f32_32x32x32_bf16 v[48:63], v[208:211], v[112:119], v148\n\t"
        "ds_read_b128 v[24:27], v217 offset:6144\n\t"
        "ds_read_b128 v[28:31], v217 offset:7168\n\t"
        "ds_read_b32 v132, v219 offset:256\n\t"
        "v_exp_f32 v125, v125\n\t"
        "v_exp_f32 v126, v126\n\t"
        "v_exp_f32 v127, v127\n\t"
        "v_smfmac_f32_16x16x64_bf16 v[140:143], v[144:147], v[112:119], v148\n\t"
        "s_nop 1\n\t"
        "v_smfmac_f32_32x32x32_bf16 v[48:63], v[212:215], v[120:127], v148\n\t"
        "v_smfmac_f32_16x16x64_bf16 v[140:143], v[144:147], v[120:127], v148\n\t"
        "s_waitcnt vmcnt(0) lgkmcnt(0)\n\t"
        "s_nop 6\n\t"
        "s_branch 9f\n\t"
        "24:\n\t"
        "s_add_u32 s44, s44, 0x4000\n\t"
        "s_and_b32 s44, s44, 0x3ffff\n\t"
        "s_cmp_eq_u32 s44, s49\n\t"
        "s_cselect_b32 s44, s50, s44\n\t"
        "s_add_u32 s52, s40, s44\n\t"
        "s_addc_u32 s53, s41, 0\n\t"
        "buffer_load_dwordx4 v[184:187], v149, s[52:55], 0 offen offset:0 sc1\n\t"
        "buffer_load_dwordx4 v[188:191], v149, s[52:55], 0 offen offset:1024 sc1\n\t"
        "buffer_load_dwordx4 v[192:195], v149, s[52:55], 0 offen offset:2048 sc1\n\t"
        "buffer_load_dwordx4 v[196:199], v149, s[52:55], 0 offen offset:3072 sc1\n\t"
        "buffer_load_dwordx4 v[200:203], v150, s[52:55], 0 offen offset:0 sc1\n\t"
        "buffer_load_dwordx4 v[204:207], v150, s[52:55], 0 offen offset:1024 sc1\n\t"
        "buffer_load_dwordx4 v[208:211], v150, s[52:55], 0 offen offset:2048 sc1\n\t"
        "buffer_load_dwordx4 v[212:215], v150, s[52:55], 0 offen offset:3072 sc1\n\t"
        "s_mov_b32 s46, 1\n\t"
        "s_branch 25b\n\t"
        "26:\n\t"
        "s_add_u32 s44, s44, 0x4000\n\t"
        "s_and_b32 s44, s44, 0x3ffff\n\t"
        "s_cmp_eq_u32 s44, s49\n\t"
        "s_cselect_b32 s44, s50, s44\n\t"
        "s_add_u32 s52, s40, s44\n\t"
        "s_addc_u32 s53, s41, 0\n\t"
        "buffer_load_dwordx4 v[184:187], v149, s[52:55], 0 offen offset:0 sc1\n\t"
        "buffer_load_dwordx4 v[188:191], v149, s[52:55], 0 offen offset:1024 sc1\n\t"
        "buffer_load_dwordx4 v[192:195], v149, s[52:55], 0 offen offset:2048 sc1\n\t"
        "buffer_load_dwordx4 v[196:199], v149, s[52:55], 0 offen offset:3072 sc1\n\t"
        "buffer_load_dwordx4 v[200:203], v150, s[52:55], 0 offen offset:0 sc1\n\t"
        "buffer_load_dwordx4 v[204:207], v150, s[52:55], 0 offen offset:1024 sc1\n\t"
        "buffer_load_dwordx4 v[208:211], v150, s[52:55], 0 offen offset:2048 sc1\n\t"
        "buffer_load_dwordx4 v[212:215], v150, s[52:55], 0 offen offset:3072 sc1\n\t"
        "s_mov_b32 s46, 1\n\t"
        "s_branch 27b\n\t"
        "28:\n\t"
        "s_add_u32 s44, s44, 0x4000\n\t"
        "s_and_b32 s44, s44, 0x3ffff\n\t"
        "s_cmp_eq_u32 s44, s49\n\t"
        "s_cselect_b32 s44, s50, s44\n\t"
        "s_add_u32 s52, s40, s44\n\t"
        "s_addc_u32 s53, s41, 0\n\t"
        "buffer_load_dwordx4 v[184:187], v149, s[52:55], 0 offen offset:0 sc1\n\t"
        "buffer_load_dwordx4 v[188:191], v149, s[52:55], 0 offen offset:1024 sc1\n\t"
        "buffer_load_dwordx4 v[192:195], v149, s[52:55], 0 offen offset:2048 sc1\n\t"
        "buffer_load_dwordx4 v[196:199], v149, s[52:55], 0 offen offset:3072 sc1\n\t"
        "buffer_load_dwordx4 v[200:203], v150, s[52:55], 0 offen offset:0 sc1\n\t"
        "buffer_load_dwordx4 v[204:207], v150, s[52:55], 0 offen offset:1024 sc1\n\t"
        "buffer_load_dwordx4 v[208:211], v150, s[52:55], 0 offen offset:2048 sc1\n\t"
        "buffer_load_dwordx4 v[212:215], v150, s[52:55], 0 offen offset:3072 sc1\n\t"
        "s_mov_b32 s46, 1\n\t"
        "s_branch 29b\n\t"
        "30:\n\t"
        "s_add_u32 s44, s44, 0x4000\n\t"
        "s_and_b32 s44, s44, 0x3ffff\n\t"
        "s_cmp_eq_u32 s44, s49\n\t"
        "s_cselect_b32 s44, s50, s44\n\t"
        "s_add_u32 s52, s40, s44\n\t"
        "s_addc_u32 s53, s41, 0\n\t"
        "buffer_load_dwordx4 v[184:187], v149, s[52:55], 0 offen offset:0 sc1\n\t"
        "buffer_load_dwordx4 v[188:191], v149, s[52:55], 0 offen offset:1024 sc1\n\t"
        "buffer_load_dwordx4 v[192:195], v149, s[52:55], 0 offen offset:2048 sc1\n\t"
        "buffer_load_dwordx4 v[196:199], v149, s[52:55], 0 offen offset:3072 sc1\n\t"
        "buffer_load_dwordx4 v[200:203], v150, s[52:55], 0 offen offset:0 sc1\n\t"
        "buffer_load_dwordx4 v[204:207], v150, s[52:55], 0 offen offset:1024 sc1\n\t"
        "buffer_load_dwordx4 v[208:211], v150, s[52:55], 0 offen offset:2048 sc1\n\t"
        "buffer_load_dwordx4 v[212:215], v150, s[52:55], 0 offen offset:3072 sc1\n\t"
        "s_mov_b32 s46, 1\n\t"
        "s_branch 31b\n\t"
        "32:\n\t"
        "s_add_u32 s44, s44, 0x4000\n\t"
        "s_and_b32 s44, s44, 0x3ffff\n\t"
        "s_cmp_eq_u32 s44, s49\n\t"
        "s_cselect_b32 s44, s50, s44\n\t"
        "s_add_u32 s52, s40, s44\n\t"
        "s_addc_u32 s53, s41, 0\n\t"
        "buffer_load_dwordx4 v[184:187], v149, s[52:55], 0 offen offset:0 sc1\n\t"
        "buffer_load_dwordx4 v[188:191], v149, s[52:55], 0 offen offset:1024 sc1\n\t"
        "buffer_load_dwordx4 v[192:195], v149, s[52:55], 0 offen offset:2048 sc1\n\t"
        "buffer_load_dwordx4 v[196:199], v149, s[52:55], 0 offen offset:3072 sc1\n\t"
        "buffer_load_dwordx4 v[200:203], v150, s[52:55], 0 offen offset:0 sc1\n\t"
        "buffer_load_dwordx4 v[204:207], v150, s[52:55], 0 offen offset:1024 sc1\n\t"
        "buffer_load_dwordx4 v[208:211], v150, s[52:55], 0 offen offset:2048 sc1\n\t"
        "buffer_load_dwordx4 v[212:215], v150, s[52:55], 0 offen offset:3072 sc1\n\t"
        "s_mov_b32 s46, 1\n\t"
        "s_branch 33b\n\t"
        "40:\n\t"
        "s_mov_b32 s45, 0x100000\n\t"
        "42:\n\t"
        "ds_read_b32 v151, v221\n\t"
        "s_sub_u32 s45, s45, 1\n\t"
        "s_waitcnt lgkmcnt(0)\n\t"
        "s_cbranch_scc1 41b\n\t"
        "v_cmp_ne_u32 vcc, v222, v151\n\t"
        "s_cbranch_vccnz 42b\n\t"
        "s_branch 41b\n\t"
        "9:\n\t"
        : "+{v[0:15]}"(o0), "+{v[16:31]}"(o1), "+{v[32:47]}"(o2), "+{v[48:63]}"(o3), "+{v[128:143]}"(lv), "+{s44}"(soff), "+{v[152:167]}"(kva), "+{v[168:183]}"(kvb), "+{v[184:191]}"(fv),
          [r0] "+v"(x1p[0]), [r1] "+v"(x1p[1]), [r2] "+v"(x1p[2]), [r3] "+v"(x1p[3]) STAMP_OUTS
        : "{v[64:71]}"(q[0]), "{v[72:79]}"(q[1]), "{v[80:87]}"(q[2]), "{v[88:95]}"(q[3]), "{v[144:147]}"(ones), "{v148}"(0xddddu), "{v149}"(kvoff), "{v150}"(kvoff + 4096),
          "{v[216:223]}"(cv), "{v224}"(lbase + L_W + 16), "{s[40:43]}"(srd), "{s47}"(L), "{s49}"(tm * 16384), "{s50}"(((tm + 1) & 15) * 16384), [xb] "s"(x0)
        : "v96", "v97", "v98", "v99", "v100", "v101", "v102", "v103", "v104", "v105", "v106", "v107", "v108", "v109", "v110", "v111", "v112", "v113", "v114", "v115", "v116", "v117", "v118", "v119", "v120", "v121", "v122", "v123", "v124", "v125", "v126", "v127", "v151", "v192", "v193", "v194", "v195", "v196", "v197", "v198", "v199", "v200", "v201", "v202", "v203", "v204", "v205", "v206", "v207", "v208", "v209", "v210", "v211", "v212", "v213", "v214", "v215", "s45", "s46", "s52", "s53", "s54", "s55", "vcc", "scc", "memory");
      STAMP_PUT(6 + 16 * L);
      STAMPV(9 + 16 * L);
      PHASE_VARS;
      const unsigned dst = OFF_OF + L * (5 * MBY) + (tm * 4 + 2 * kh) * TS + (tn * 2 + hh) * 2048 + lane * 16;
      auto put = [&](int u, const f32x16& o) {
#pragma unroll
        for (int c = 0; c < 2; ++c) {
          u32x4 w;
#pragma unroll
          for (int e = 0; e < 4; ++e) w[e] = pack2(o[8 * c + 2 * e], o[8 * c + 2 * e + 1]);
          stg(rs, dst + u * TS + c * 1024, w);
        }
      };
      const float la = lv[8] + lv[0], lb = lv[12] + lv[4];
      o2 = (o2 + o0) * RCP(la); put(0, o2);
      SBAR();
      o3 = (o3 + o1) * RCP(lb); put(1, o3);
      SBAR();
      {
        const f32x16 t = o2 + o3;
        float sum = 0.f;
#pragma unroll
        for (int e = 0; e < 16; e += 4) sum += (t[e] + t[e + 1]) + (t[e + 2] + t[e + 3]);
        if (__any(!(fabsf(sum) < 1e37f) | !(fmaxf(la, lb) < 1e27f) | !(fminf(la, lb) > 1e-9f) | !(la + lb == la + lb))) {
          DRAIN();
          attn_safe(rs, kvoff - kh * 8192, q[2], ones, o2); put(0, o2);
          attn_safe(rs, kvoff - kh * 8192, q[3], ones, o3); put(1, o3);
        }
      }
    }
    STAMPV(10 + 16 * L);
    {
      f32x16 acc[8];
      u32x2 wrc[8];
      STAMP_DECL;
      { PHASE_VARS;
      float r0 = 0.f, r1 = 1.f;
      if (L == 0) {
#pragma unroll
        for (int i = 0; i < 4; ++i) x1p[i] = fo_swap(x1p[i]);
      } else {
        r0 = reinterpret_cast<const float*>(smem + L_AB)[256 + wid * 64 + r32]; r1 = reinterpret_cast<const float*>(smem + L_AB)[256 + wid * 64 + 32 + r32];
      }
#pragma unroll
      for (int e = 0; e < 16; ++e) { acc[e >> 3][2 * (e & 7)] = lo16(x1p[e >> 2][e & 3]) * r1 + r0; acc[e >> 3][2 * (e & 7) + 1] = hi16(x1p[e >> 2][e & 3]) * r1 + r0; }
      PIN(acc[0]); PIN(acc[1]);
      asm volatile("s_waitcnt vmcnt(0)\n\tds_write_b32 %0, %1" :: "v"(lbase + L_W + 80 + 4 * wid), "v"(2 * L + 2) : "memory");
      STAMPV(11 + 16 * L);
      const unsigned a0 = lbase + wrow * 128 + ((wch ^ wsw) << 4) + 8 * (tp & 1), a1 = lbase + wrow * 128 + (((4 + wch) ^ wsw) << 4) + 8 * (tp & 1);
      asm volatile(
        "s_mov_b32 s46, 0\n\t"
        "ostart_%=:\n\t"
        "s_mov_b32 s48, 0x200000\n\t"
        "2:\n\t"
        "ds_read_b32 v233, v232\n\t"
        "s_sub_u32 s48, s48, 1\n\t"
        "s_waitcnt lgkmcnt(0)\n\t"
        "s_cselect_b32 s47, 15, 0\n\t"
        "v_readfirstlane_b32 s51, v233\n\t"
        "s_nop 3\n\t"
        "s_or_b32 s51, s51, s47\n\t"
        "s_andn2_b32 s47, s51, s46\n\t"
        "s_cbranch_scc0 2b\n\t"
        "s_andn2_b32 s51, s51, s46\n\t"
        "s_ff1_i32_b32 s47, s51\n\t"
        "s_add_u32 s45, s47, s49\n\t"
        "s_and_b32 s45, s45, 3\n\t"
        "s_lshl_b32 s45, s45, 14\n\t"
        "s_and_b32 s51, s47, 0x80000000\n\t"
        "v_or_b32 v225, s51, v224\n\t"
        "s_and_b32 s51, s47, 3\n\t"
        "s_lshl_b32 s51, s51, 15\n\t"
        "v_add_u32 v230, s51, v226\n\t"
        "v_add_u32 v231, s51, v227\n\t"
        "buffer_load_dwordx4 v[160:163], v225, s[40:43], s45 offen offset:0 sc1\n\t"
        "buffer_load_dwordx4 v[164:167], v225, s[40:43], s45 offen offset:1024 sc1\n\t"
        "buffer_load_dwordx4 v[168:171], v225, s[40:43], s45 offen offset:2048 sc1\n\t"
        "buffer_load_dwordx4 v[172:175], v225, s[40:43], s45 offen offset:3072 sc1\n\t"
        "s_add_u32 s45, s45, 0x1000\n\t"
        "buffer_load_dwordx4 v[176:179], v225, s[40:43], s45 offen offset:0 sc1\n\t"
        "buffer_load_dwordx4 v[180:183], v225, s[40:43], s45 offen offset:1024 sc1\n\t"
        "buffer_load_dwordx4 v[184:187], v225, s[40:43], s45 offen offset:2048 sc1\n\t"
        "buffer_load_dwordx4 v[188:191], v225, s[40:43], s45 offen offset:3072 sc1\n\t"
        "s_add_u32 s45, s45, 0x1000\n\t"
        "buffer_load_dwordx4 v[192:195], v225, s[40:43], s45 offen offset:0 sc1\n\t"
        "buffer_load_dwordx4 v[196:199], v225, s[40:43], s45 offen offset:1024 sc1\n\t"
        "buffer_load_dwordx4 v[200:203], v225, s[40:43], s45 offen offset:2048 sc1\n\t"
        "buffer_load_dwordx4 v[204:207], v225, s[40:43], s45 offen offset:3072 sc1\n\t"
        "ds_read_b64_tr_b16 v[128:129], v230 offset:0\n\t"
        "ds_read_b64_tr_b16 v[130:131], v230 offset:1024\n\t"
        "ds_read_b64_tr_b16 v[132:133], v231 offset:0\n\t"
        "ds_read_b64_tr_b16 v[134:135], v231 offset:1024\n\t"
        "ds_read_b64_tr_b16 v[136:137], v230 offset:2048\n\t"
        "ds_read_b64_tr_b16 v[138:139], v230 offset:3072\n\t"
        "ds_read_b64_tr_b16 v[140:141], v231 offset:2048\n\t"
        "ds_read_b64_tr_b16 v[142:143], v231 offset:3072\n\t"
        "ds_read_b64_tr_b16 v[144:145], v230 offset:4096\n\t"
        "ds_read_b64_tr_b16 v[146:147], v230 offset:5120\n\t"
        "ds_read_b64_tr_b16 v[148:149], v231 offset:4096\n\t"
        "ds_read_b64_tr_b16 v[150:151], v231 offset:5120\n\t"
        "ds_read_b64_tr_b16 v[152:153], v230 offset:6144\n\t"
        "ds_read_b64_tr_b16 v[154:155], v230 offset:7168\n\t"
        "ds_read_b64_tr_b16 v[156:157], v231 offset:6144\n\t"
        "ds_read_b64_tr_b16 v[158:159], v231 offset:7168\n\t"
        "s_mov_b32 s44, s45\n\t"
        "s_cmp_eq_u32 s47, 0\n\t"
        "s_cbranch_scc1 oseg0_%=\n\t"
        "s_cmp_eq_u32 s47, 1\n\t"
        "s_cbranch_scc1 oseg1_%=\n\t"
        "s_cmp_eq_u32 s47, 2\n\t"
        "s_cbranch_scc1 oseg2_%=\n\t"
        "s_cmp_eq_u32 s47, 3\n\t"
        "s_cbranch_scc1 oseg3_%=\n\t"
        "oseg0_%=:\n\t"
        "s_or_b32 s46, s46, 1\n\t"
        "s_waitcnt vmcnt(11) lgkmcnt(14)\n\t"
        "v_mfma_f32_32x32x16_bf16 v[0:15], v[128:131], v[160:163], v[0:15]\n\t"
        "ds_read_b64_tr_b16 v[128:129], v226 offset:8192\n\t"
        "ds_read_b64_tr_b16 v[130:131], v226 offset:9216\n\t"
        "s_waitcnt lgkmcnt(14)\n\t"
        "v_mfma_f32_32x32x16_bf16 v[16:31], v[132:135], v[160:163], v[16:31]\n\t"
        "ds_read_b64_tr_b16 v[132:133], v227 offset:8192\n\t"
        "ds_read_b64_tr_b16 v[134:135], v227 offset:9216\n\t"
        "s_add_u32 s44, s44, 0x1000\n\t"
        "buffer_load_dwordx4 v[208:211], v224, s[40:43], s44 offen offset:0 sc1\n\t"
        "s_waitcnt vmcnt(11) lgkmcnt(14)\n\t"
        "v_mfma_f32_32x32x16_bf16 v[0:15], v[136:139], v[164:167], v[0:15]\n\t"
        "ds_read_b32 v233, v232\n\t"
        "ds_read_b64_tr_b16 v[136:137], v226 offset:10240\n\t"
        "ds_read_b64_tr_b16 v[138:139], v226 offset:11264\n\t"
        "s_waitcnt lgkmcnt(14)\n\t"
        "v_mfma_f32_32x32x16_bf16 v[16:31], v[140:143], v[164:167], v[16:31]\n\t"
        "ds_read_b64_tr_b16 v[140:141], v227 offset:10240\n\t"
        "ds_read_b64_tr_b16 v[142:143], v227 offset:11264\n\t"
        "buffer_load_dwordx4 v[212:215], v224, s[40:43], s44 offen offset:1024 sc1\n\t"
        "s_waitcnt vmcnt(11) lgkmcnt(14)\n\t"
        "v_mfma_f32_32x32x16_bf16 v[0:15], v[144:147], v[168:171], v[0:15]\n\t"
        "ds_read_b64_tr_b16 v[144:145], v226 offset:12288\n\t"
        "ds_read_b64_tr_b16 v[146:147], v226 offset:13312\n\t"
        "s_waitcnt lgkmcnt(14)\n\t"
        "v_mfma_f32_32x32x16_bf16 v[16:31], v[148:151], v[168:171], v[16:31]\n\t"
        "ds_read_b64_tr_b16 v[148:149], v227 offset:12288\n\t"
        "ds_read_b64_tr_b16 v[150:151], v227 offset:13312\n\t"
        "buffer_load_dwordx4 v[216:219], v224, s[40:43], s44 offen offset:2048 sc1\n\t"
        "s_waitcnt vmcnt(11) lgkmcnt(8)\n\t"
        "v_mfma_f32_32x32x16_bf16 v[0:15], v[152:155], v[172:175], v[0:15]\n\t"
        "v_readfirstlane_b32 s51, v233\n\t"
        "ds_read_b64_tr_b16 v[152:153], v226 offset:14336\n\t"
        "ds_read_b64_tr_b16 v[154:155], v226 offset:15360\n\t"
        "s_waitcnt lgkmcnt(14)\n\t"
        "v_mfma_f32_32x32x16_bf16 v[16:31], v[156:159], v[172:175], v[16:31]\n\t"
        "s_andn2_b32 s51, s51, s46\n\t"
        "s_ff1_i32_b32 s47, s51\n\t"
        "s_add_u32 s45, s47, s49\n\t"
        "s_and_b32 s45, s45, 3\n\t"
        "s_lshl_b32 s45, s45, 14\n\t"
        "ds_read_b64_tr_b16 v[156:157], v227 offset:14336\n\t"
        "ds_read_b64_tr_b16 v[158:159], v227 offset:15360\n\t"
        "buffer_load_dwordx4 v[220:223], v224, s[40:43], s44 offen offset:3072 sc1\n\t"
        "s_waitcnt vmcnt(11) lgkmcnt(14)\n\t"
        "v_mfma_f32_32x32x16_bf16 v[0:15], v[128:131], v[176:179], v[0:15]\n\t"
        "s_and_b32 s51, s47, 0x80000000\n\t"
        "v_or_b32 v225, s51, v224\n\t"
        "s_and_b32 s51, s47, 3\n\t"
        "s_lshl_b32 s51, s51, 15\n\t"
        "v_add_u32 v230, s51, v226\n\t"
        "v_add_u32 v231, s51, v227\n\t"
        "ds_read_b64_tr_b16 v[128:129], v226 offset:16384\n\t"
        "ds_read_b64_tr_b16 v[130:131], v226 offset:17408\n\t"
        "s_waitcnt lgkmcnt(14)\n\t"
        "v_mfma_f32_32x32x16_bf16 v[16:31], v[132:135], v[176:179], v[16:31]\n\t"
        "ds_read_b64_tr_b16 v[132:133], v227 offset:16384\n\t"
        "ds_read_b64_tr_b16 v[134:135], v227 offset:17408\n\t"
        "buffer_load_dwordx4 v[160:163], v225, s[40:43], s45 offen offset:0 sc1\n\t"
        "s_waitcnt vmcnt(11) lgkmcnt(14)\n\t"
        "v_mfma_f32_32x32x16_bf16 v[0:15], v[136:139], v[180:183], v[0:15]\n\t"
        "ds_read_b64_tr_b16 v[136:137], v226 offset:18432\n\t"
        "ds_read_b64_tr_b16 v[138:139], v226 offset:19456\n\t"
        "s_waitcnt lgkmcnt(14)\n\t"
        "v_mfma_f32_32x32x16_bf16 v[16:31], v[140:143], v[180:183], v[16:31]\n\t"
        "ds_read_b64_tr_b16 v[140:141], v227 offset:18432\n\t"
        "ds_read_b64_tr_b16 v[142:143], v227 offset:19456\n\t"
        "buffer_load_dwordx4 v[164:167], v225, s[40:43], s45 offen offset:1024 sc1\n\t"
        "s_waitcnt vmcnt(11) lgkmcnt(14)\n\t"
        "v_mfma_f32_32x32x16_bf16 v[0:15], v[144:147], v[184:187], v[0:15]\n\t"
        "ds_read_b64_tr_b16 v[144:145], v226 offset:20480\n\t"
        "ds_read_b64_tr_b16 v[146:147], v226 offset:21504\n\t"
        "s_waitcnt lgkmcnt(14)\n\t"
        "v_mfma_f32_32x32x16_bf16 v[16:31], v[148:151], v[184:187], v[16:31]\n\t"
        "ds_read_b64_tr_b16 v[148:149], v227 offset:20480\n\t"
        "ds_read_b64_tr_b16 v[150:151], v227 offset:21504\n\t"
        "buffer_load_dwordx4 v[168:171], v225, s[40:43], s45 offen offset:2048 sc1\n\t"
        "s_waitcnt vmcnt(11) lgkmcnt(14)\n\t"
        "v_mfma_f32_32x32x16_bf16 v[0:15], v[152:155], v[188:191], v[0:15]\n\t"
        "ds_read_b64_tr_b16 v[152:153], v226 offset:22528\n\t"
        "ds_read_b64_tr_b16 v[154:155], v226 offset:23552\n\t"
        "s_waitcnt lgkmcnt(14)\n\t"
        "v_mfma_f32_32x32x16_bf16 v[16:31], v[156:159], v[188:191], v[16:31]\n\t"
        "ds_read_b64_tr_b16 v[156:157], v227 offset:22528\n\t"
        "ds_read_b64_tr_b16 v[158:159], v227 offset:23552\n\t"
        "buffer_load_dwordx4 v[172:175], v225, s[40:43], s45 offen offset:3072 sc1\n\t"
        "s_waitcnt vmcnt(11) lgkmcnt(14)\n\t"
        "v_mfma_f32_32x32x16_bf16 v[0:15], v[128:131], v[192:195], v[0:15]\n\t"
        "ds_read_b64_tr_b16 v[128:129], v226 offset:24576\n\t"
        "ds_read_b64_tr_b16 v[130:131], v226 offset:25600\n\t"
        "s_waitcnt lgkmcnt(14)\n\t"
        "v_mfma_f32_32x32x16_bf16 v[16:31], v[132:135], v[192:195], v[16:31]\n\t"
        "ds_read_b64_tr_b16 v[132:133], v227 offset:24576\n\t"
        "ds_read_b64_tr_b16 v[134:135], v227 offset:25600\n\t"
        "s_add_u32 s45, s45, 0x1000\n\t"
        "buffer_load_dwordx4 v[176:179], v225, s[40:43], s45 offen offset:0 sc1\n\t"
        "s_waitcnt vmcnt(11) lgkmcnt(14)\n\t"
        "v_mfma_f32_32x32x16_bf16 v[0:15], v[136:139], v[196:199], v[0:15]\n\t"
        "ds_read_b64_tr_b16 v[136:137], v226 offset:26624\n\t"
        "ds_read_b64_tr_b16 v[138:139], v226 offset:27648\n\t"
        "s_waitcnt lgkmcnt(14)\n\t"
        "v_mfma_f32_32x32x16_bf16 v[16:31], v[140:143], v[196:199], v[16:31]\n\t"
        "ds_read_b64_tr_b16 v[140:141], v227 offset:26624\n\t"
        "ds_read_b64_tr_b16 v[142:143], v227 offset:27648\n\t"
        "buffer_load_dwordx4 v[180:183], v225, s[40:43], s45 offen offset:1024 sc1\n\t"
        "s_waitcnt vmcnt(11) lgkmcnt(14)\n\t"
        "v_mfma_f32_32x32x16_bf16 v[0:15], v[144:147], v[200:203], v[0:15]\n\t"
        "ds_read_b64_tr_b16 v[144:145], v226 offset:28672\n\t"
        "ds_read_b64_tr_b16 v[146:147], v226 offset:29696\n\t"
        "s_waitcnt lgkmcnt(14)\n\t"
        "v_mfma_f32_32x32x16_bf16 v[16:31], v[148:151], v[200:203], v[16:31]\n\t"
        "ds_read_b64_tr_b16 v[148:149], v227 offset:28672\n\t"
        "ds_read_b64_tr_b16 v[150:151], v227 offset:29696\n\t"
        "buffer_load_dwordx4 v[184:187], v225, s[40:43], s45 offen offset:2048 sc1\n\t"
        "s_waitcnt vmcnt(11) lgkmcnt(14)\n\t"
        "v_mfma_f32_32x32x16_bf16 v[0:15], v[152:155], v[204:207], v[0:15]\n\t"
        "ds_read_b64_tr_b16 v[152:153], v226 offset:30720\n\t"
        "ds_read_b64_tr_b16 v[154:155], v226 offset:31744\n\t"
        "s_waitcnt lgkmcnt(14)\n\t"
        "v_mfma_f32_32x32x16_bf16 v[16:31], v[156:159], v[204:207], v[16:31]\n\t"
        "ds_read_b64_tr_b16 v[156:157], v227 offset:30720\n\t"
        "ds_read_b64_tr_b16 v[158:159], v227 offset:31744\n\t"
        "buffer_load_dwordx4 v[188:191], v225, s[40:43], s45 offen offset:3072 sc1\n\t"
        "s_waitcnt vmcnt(11) lgkmcnt(14)\n\t"
        "v_mfma_f32_32x32x16_bf16 v[0:15], v[128:131], v[208:211], v[0:15]\n\t"
        "ds_read_b64_tr_b16 v[128:129], v230 offset:0\n\t"
        "ds_read_b64_tr_b16 v[130:131], v230 offset:1024\n\t"
        "s_waitcnt lgkmcnt(14)\n\t"
        "v_mfma_f32_32x32x16_bf16 v[16:31], v[132:135], v[208:211], v[16:31]\n\t"
        "ds_read_b64_tr_b16 v[132:133], v231 offset:0\n\t"
        "ds_read_b64_tr_b16 v[134:135], v231 offset:1024\n\t"
        "s_add_u32 s45, s45, 0x1000\n\t"
        "buffer_load_dwordx4 v[192:195], v225, s[40:43], s45 offen offset:0 sc1\n\t"
        "s_waitcnt vmcnt(11) lgkmcnt(14)\n\t"
        "v_mfma_f32_32x32x16_bf16 v[0:15], v[136:139], v[212:215], v[0:15]\n\t"
        "ds_read_b64_tr_b16 v[136:137], v230 offset:2048\n\t"
        "ds_read_b64_tr_b16 v[138:139], v230 offset:3072\n\t"
        "s_waitcnt lgkmcnt(14)\n\t"
        "v_mfma_f32_32x32x16_bf16 v[16:31], v[140:143], v[212:215], v[16:31]\n\t"
        "ds_read_b64_tr_b16 v[140:141], v231 offset:2048\n\t"
        "ds_read_b64_tr_b16 v[142:143], v231 offset:3072\n\t"
        "buffer_load_dwordx4 v[196:199], v225, s[40:43], s45 offen offset:1024 sc1\n\t"
        "s_waitcnt vmcnt(11) lgkmcnt(14)\n\t"
        "v_mfma_f32_32x32x16_bf16 v[0:15], v[144:147], v[216:219], v[0:15]\n\t"
        "ds_read_b64_tr_b16 v[144:145], v230 offset:4096\n\t"
        "ds_read_b64_tr_b16 v[146:147], v230 offset:5120\n\t"
        "s_waitcnt lgkmcnt(14)\n\t"
        "v_mfma_f32_32x32x16_bf16 v[16:31], v[148:151], v[216:219], v[16:31]\n\t"
        "ds_read_b64_tr_b16 v[148:149], v231 offset:4096\n\t"
        "ds_read_b64_tr_b16 v[150:151], v231 offset:5120\n\t"
        "buffer_load_dwordx4 v[200:203], v225, s[40:43], s45 offen offset:2048 sc1\n\t"
        "s_waitcnt vmcnt(11) lgkmcnt(14)\n\t"
        "v_mfma_f32_32x32x16_bf16 v[0:15], v[152:155], v[220:223], v[0:15]\n\t"
        "ds_read_b64_tr_b16 v[152:153], v230 offset:6144\n\t"
        "ds_read_b64_tr_b16 v[154:155], v230 offset:7168\n\t"
        "s_waitcnt lgkmcnt(14)\n\t"
        "v_mfma_f32_32x32x16_bf16 v[16:31], v[156:159], v[220:223], v[16:31]\n\t"
        "ds_read_b64_tr_b16 v[156:157], v231 offset:6144\n\t"
        "ds_read_b64_tr_b16 v[158:159], v231 offset:7168\n\t"
        "buffer_load_dwordx4 v[204:207], v225, s[40:43], s45 offen offset:3072 sc1\n\t"
        "s_mov_b32 s44, s45\n\t"
        "s_cmp_eq_u32 s47, 0\n\t"
        "s_cbranch_scc1 oseg0_%=\n\t"
        "s_cmp_eq_u32 s47, 1\n\t"
        "s_cbranch_scc1 oseg1_%=\n\t"
        "s_cmp_eq_u32 s47, 2\n\t"
        "s_cbranch_scc1 oseg2_%=\n\t"
        "s_cmp_eq_u32 s47, 3\n\t"
        "s_cbranch_scc1 oseg3_%=\n\t"
        "s_cmp_lg_u32 s46, 15\n\t"
        "s_cbranch_scc1 ostart_%=\n\t"
        "s_branch oend_%=\n\t"
        "oseg1_%=:\n\t"
        "s_or_b32 s46, s46, 2\n\t"
        "s_waitcnt vmcnt(11) lgkmcnt(14)\n\t"
        "v_mfma_f32_32x32x16_bf16 v[32:47], v[128:131], v[160:163], 0\n\t"
        "ds_read_b64_tr_b16 v[128:129], v226 offset:40960\n\t"
        "ds_read_b64_tr_b16 v[130:131], v226 offset:41984\n\t"
        "s_waitcnt lgkmcnt(14)\n\t"
        "v_mfma_f32_32x32x16_bf16 v[48:63], v[132:135], v[160:163], 0\n\t"
        "ds_read_b64_tr_b16 v[132:133], v227 offset:40960\n\t"
        "ds_read_b64_tr_b16 v[134:135], v227 offset:41984\n\t"
        "s_add_u32 s44, s44, 0x1000\n\t"
        "buffer_load_dwordx4 v[208:211], v224, s[40:43], s44 offen offset:0 sc1\n\t"
        "s_waitcnt vmcnt(11) lgkmcnt(14)\n\t"
        "v_mfma_f32_32x32x16_bf16 v[32:47], v[136:139], v[164:167], v[32:47]\n\t"
        "ds_read_b32 v233, v232\n\t"
        "ds_read_b64_tr_b16 v[136:137], v226 offset:43008\n\t"
        "ds_read_b64_tr_b16 v[138:139], v226 offset:44032\n\t"
        "s_waitcnt lgkmcnt(14)\n\t"
        "v_mfma_f32_32x32x16_bf16 v[48:63], v[140:143], v[164:167], v[48:63]\n\t"
        "ds_read_b64_tr_b16 v[140:141], v227 offset:43008\n\t"
        "ds_read_b64_tr_b16 v[142:143], v227 offset:44032\n\t"
        "buffer_load_dwordx4 v[212:215], v224, s[40:43], s44 offen offset:1024 sc1\n\t"
        "s_waitcnt vmcnt(11) lgkmcnt(14)\n\t"
        "v_mfma_f32_32x32x16_bf16 v[32:47], v[144:147], v[168:171], v[32:47]\n\t"
        "ds_read_b64_tr_b16 v[144:145], v226 offset:45056\n\t"
        "ds_read_b64_tr_b16 v[146:147], v226 offset:46080\n\t"
        "s_waitcnt lgkmcnt(14)\n\t"
        "v_mfma_f32_32x32x16_bf16 v[48:63], v[148:151], v[168:171], v[48:63]\n\t"
        "ds_read_b64_tr_b16 v[148:149], v227 offset:45056\n\t"
        "ds_read_b64_tr_b16 v[150:151], v227 offset:46080\n\t"
        "buffer_load_dwordx4 v[216:219], v224, s[40:43], s44 offen offset:2048 sc1\n\t"
        "s_waitcnt vmcnt(11) lgkmcnt(8)\n\t"
        "v_mfma_f32_32x32x16_bf16 v[32:47], v[152:155], v[172:175], v[32:47]\n\t"
        "v_readfirstlane_b32 s51, v233\n\t"
        "ds_read_b64_tr_b16 v[152:153], v226 offset:47104\n\t"
        "ds_read_b64_tr_b16 v[154:155], v226 offset:48128\n\t"
        "s_waitcnt lgkmcnt(14)\n\t"
        "v_mfma_f32_32x32x16_bf16 v[48:63], v[156:159], v[172:175], v[48:63]\n\t"
        "s_andn2_b32 s51, s51, s46\n\t"
        "s_ff1_i32_b32 s47, s51\n\t"
        "s_add_u32 s45, s47, s49\n\t"
        "s_and_b32 s45, s45, 3\n\t"
        "s_lshl_b32 s45, s45, 14\n\t"
        "ds_read_b64_tr_b16 v[156:157], v227 offset:47104\n\t"
        "ds_read_b64_tr_b16 v[158:159], v227 offset:48128\n\t"
        "buffer_load_dwordx4 v[220:223], v224, s[40:43], s44 offen offset:3072 sc1\n\t"
        "s_waitcnt vmcnt(11) lgkmcnt(14)\n\t"
        "v_mfma_f32_32x32x16_bf16 v[32:47], v[128:131], v[176:179], v[32:47]\n\t"
        "s_and_b32 s51, s47, 0x80000000\n\t"
        "v_or_b32 v225, s51, v224\n\t"
        "s_and_b32 s51, s47, 3\n\t"
        "s_lshl_b32 s51, s51, 15\n\t"
        "v_add_u32 v230, s51, v226\n\t"
        "v_add_u32 v231, s51, v227\n\t"
        "ds_read_b64_tr_b16 v[128:129], v226 offset:49152\n\t"
        "ds_read_b64_tr_b16 v[130:131], v226 offset:50176\n\t"
        "s_waitcnt lgkmcnt(14)\n\t"
        "v_mfma_f32_32x32x16_bf16 v[48:63], v[132:135], v[176:179], v[48:63]\n\t"
        "ds_read_b64_tr_b16 v[132:133], v227 offset:49152\n\t"
        "ds_read_b64_tr_b16 v[134:135], v227 offset:50176\n\t"
        "buffer_load_dwordx4 v[160:163], v225, s[40:43], s45 offen offset:0 sc1\n\t"
        "s_waitcnt vmcnt(11) lgkmcnt(14)\n\t"
        "v_mfma_f32_32x32x16_bf16 v[32:47], v[136:139], v[180:183], v[32:47]\n\t"
        "ds_read_b64_tr_b16 v[136:137], v226 offset:51200\n\t"
        "ds_read_b64_tr_b16 v[138:139], v226 offset:52224\n\t"
        "s_waitcnt lgkmcnt(14)\n\t"
        "v_mfma_f32_32x32x16_bf16 v[48:63], v[140:143], v[180:183], v[48:63]\n\t"
        "ds_read_b64_tr_b16 v[140:141], v227 offset:51200\n\t"
        "ds_read_b64_tr_b16 v[142:143], v227 offset:52224\n\t"
        "buffer_load_dwordx4 v[164:167], v225, s[40:43], s45 offen offset:1024 sc1\n\t"
        "s_waitcnt vmcnt(11) lgkmcnt(14)\n\t"
        "v_mfma_f32_32x32x16_bf16 v[32:47], v[144:147], v[184:187], v[32:47]\n\t"
        "ds_read_b64_tr_b16 v[144:145], v226 offset:53248\n\t"
        "ds_read_b64_tr_b16 v[146:147], v226 offset:54272\n\t"
        "s_waitcnt lgkmcnt(14)\n\t"
        "v_mfma_f32_32x32x16_bf16 v[48:63], v[148:151], v[184:187], v[48:63]\n\t"
        "ds_read_b64_tr_b16 v[148:149], v227 offset:53248\n\t"
        "ds_read_b64_tr_b16 v[150:151], v227 offset:54272\n\t"
        "buffer_load_dwordx4 v[168:171], v225, s[40:43], s45 offen offset:2048 sc1\n\t"
        "s_waitcnt vmcnt(11) lgkmcnt(14)\n\t"
        "v_mfma_f32_32x32x16_bf16 v[32:47], v[152:155], v[188:191], v[32:47]\n\t"
        "ds_read_b64_tr_b16 v[152:153], v226 offset:55296\n\t"
        "ds_read_b64_tr_b16 v[154:155], v226 offset:56320\n\t"
        "s_waitcnt lgkmcnt(14)\n\t"
        "v_mfma_f32_32x32x16_bf16 v[48:63], v[156:159], v[188:191], v[48:63]\n\t"
        "ds_read_b64_tr_b16 v[156:157], v227 offset:55296\n\t"
        "ds_read_b64_tr_b16 v[158:159], v227 offset:56320\n\t"
        "buffer_load_dwordx4 v[172:175], v225, s[40:43], s45 offen offset:3072 sc1\n\t"
        "s_waitcnt vmcnt(11) lgkmcnt(14)\n\t"
        "v_mfma_f32_32x32x16_bf16 v[32:47], v[128:131], v[192:195], v[32:47]\n\t"
        "ds_read_b64_tr_b16 v[128:129], v226 offset:57344\n\t"
        "ds_read_b64_tr_b16 v[130:131], v226 offset:58368\n\t"
        "s_waitcnt lgkmcnt(14)\n\t"
        "v_mfma_f32_32x32x16_bf16 v[48:63], v[132:135], v[192:195], v[48:63]\n\t"
        "ds_read_b64_tr_b16 v[132:133], v227 offset:57344\n\t"
        "ds_read_b64_tr_b16 v[134:135], v227 offset:58368\n\t"
        "s_add_u32 s45, s45, 0x1000\n\t"
        "buffer_load_dwordx4 v[176:179], v225, s[40:43], s45 offen offset:0 sc1\n\t"
        "s_waitcnt vmcnt(11) lgkmcnt(14)\n\t"
        "v_mfma_f32_32x32x16_bf16 v[32:47], v[136:139], v[196:199], v[32:47]\n\t"
        "ds_read_b64_tr_b16 v[136:137], v226 offset:59392\n\t"
        "ds_read_b64_tr_b16 v[138:139], v226 offset:60416\n\t"
        "s_waitcnt lgkmcnt(14)\n\t"
        "v_mfma_f32_32x32x16_bf16 v[48:63], v[140:143], v[196:199], v[48:63]\n\t"
        "ds_read_b64_tr_b16 v[140:141], v227 offset:59392\n\t"
        "ds_read_b64_tr_b16 v[142:143], v227 offset:60416\n\t"
        "buffer_load_dwordx4 v[180:183], v225, s[40:43], s45 offen offset:1024 sc1\n\t"
        "s_waitcnt vmcnt(11) lgkmcnt(14)\n\t"
        "v_mfma_f32_32x32x16_bf16 v[32:47], v[144:147], v[200:203], v[32:47]\n\t"
        "ds_read_b64_tr_b16 v[144:145], v226 offset:61440\n\t"
        "ds_read_b64_tr_b16 v[146:147], v226 offset:62464\n\t"
        "s_waitcnt lgkmcnt(14)\n\t"
        "v_mfma_f32_32x32x16_bf16 v[48:63], v[148:151], v[200:203], v[48:63]\n\t"
        "ds_read_b64_tr_b16 v[148:149], v227 offset:61440\n\t"
        "ds_read_b64_tr_b16 v[150:151], v227 offset:62464\n\t"
        "buffer_load_dwordx4 v[184:187], v225, s[40:43], s45 offen offset:2048 sc1\n\t"
        "s_waitcnt vmcnt(11) lgkmcnt(14)\n\t"
        "v_mfma_f32_32x32x16_bf16 v[32:47], v[152:155], v[204:207], v[32:47]\n\t"
        "ds_read_b64_tr_b16 v[152:153], v226 offset:63488\n\t"
        "ds_read_b64_tr_b16 v[154:155], v226 offset:64512\n\t"
        "s_waitcnt lgkmcnt(14)\n\t"
        "v_mfma_f32_32x32x16_bf16 v[48:63], v[156:159], v[204:207], v[48:63]\n\t"
        "ds_read_b64_tr_b16 v[156:157], v227 offset:63488\n\t"
        "ds_read_b64_tr_b16 v[158:159], v227 offset:64512\n\t"
        "buffer_load_dwordx4 v[188:191], v225, s[40:43], s45 offen offset:3072 sc1\n\t"
        "s_waitcnt vmcnt(11) lgkmcnt(14)\n\t"
        "v_mfma_f32_32x32x16_bf16 v[32:47], v[128:131], v[208:211], v[32:47]\n\t"
        "ds_read_b64_tr_b16 v[128:129], v230 offset:0\n\t"
        "ds_read_b64_tr_b16 v[130:131], v230 offset:1024\n\t"
        "s_waitcnt lgkmcnt(14)\n\t"
        "v_mfma_f32_32x32x16_bf16 v[48:63], v[132:135], v[208:211], v[48:63]\n\t"
        "ds_read_b64_tr_b16 v[132:133], v231 offset:0\n\t"
        "ds_read_b64_tr_b16 v[134:135], v231 offset:1024\n\t"
        "s_add_u32 s45, s45, 0x1000\n\t"
        "buffer_load_dwordx4 v[192:195], v225, s[40:43], s45 offen offset:0 sc1\n\t"
        "s_waitcnt vmcnt(11) lgkmcnt(14)\n\t"
        "v_mfma_f32_32x32x16_bf16 v[32:47], v[136:139], v[212:215], v[32:47]\n\t"
        "ds_read_b64_tr_b16 v[136:137], v230 offset:2048\n\t"
        "ds_read_b64_tr_b16 v[138:139], v230 offset:3072\n\t"
        "s_waitcnt lgkmcnt(14)\n\t"
        "v_mfma_f32_32x32x16_bf16 v[48:63], v[140:143], v[212:215], v[48:63]\n\t"
        "ds_read_b64_tr_b16 v[140:141], v231 offset:2048\n\t"
        "ds_read_b64_tr_b16 v[142:143], v231 offset:3072\n\t"
        "buffer_load_dwordx4 v[196:199], v225, s[40:43], s45 offen offset:1024 sc1\n\t"
        "s_waitcnt vmcnt(11) lgkmcnt(14)\n\t"
        "v_mfma_f32_32x32x16_bf16 v[32:47], v[144:147], v[216:219], v[32:47]\n\t"
        "ds_read_b64_tr_b16 v[144:145], v230 offset:4096\n\t"
        "ds_read_b64_tr_b16 v[146:147], v230 offset:5120\n\t"
        "s_waitcnt lgkmcnt(14)\n\t"
        "v_mfma_f32_32x32x16_bf16 v[48:63], v[148:151], v[216:219], v[48:63]\n\t"
        "ds_read_b64_tr_b16 v[148:149], v231 offset:4096\n\t"
        "ds_read_b64_tr_b16 v[150:151], v231 offset:5120\n\t"
        "buffer_load_dwordx4 v[200:203], v225, s[40:43], s45 offen offset:2048 sc1\n\t"
        "s_waitcnt vmcnt(11) lgkmcnt(14)\n\t"
        "v_mfma_f32_32x32x16_bf16 v[32:47], v[152:155], v[220:223], v[32:47]\n\t"
        "ds_read_b64_tr_b16 v[152:153], v230 offset:6144\n\t"
        "ds_read_b64_tr_b16 v[154:155], v230 offset:7168\n\t"
        "s_waitcnt lgkmcnt(14)\n\t"
        "v_mfma_f32_32x32x16_bf16 v[48:63], v[156:159], v[220:223], v[48:63]\n\t"
        "ds_read_b64_tr_b16 v[156:157], v231 offset:6144\n\t"
        "ds_read_b64_tr_b16 v[158:159], v231 offset:7168\n\t"
        "buffer_load_dwordx4 v[204:207], v225, s[40:43], s45 offen offset:3072 sc1\n\t"
        "s_mov_b32 s44, s45\n\t"
        "s_cmp_eq_u32 s47, 0\n\t"
        "s_cbranch_scc1 oseg0_%=\n\t"
        "s_cmp_eq_u32 s47, 1\n\t"
        "s_cbranch_scc1 oseg1_%=\n\t"
        "s_cmp_eq_u32 s47, 2\n\t"
        "s_cbranch_scc1 oseg2_%=\n\t"
        "s_cmp_eq_u32 s47, 3\n\t"
        "s_cbranch_scc1 oseg3_%=\n\t"
        "s_cmp_lg_u32 s46, 15\n\t"
        "s_cbranch_scc1 ostart_%=\n\t"
        "s_branch oend_%=\n\t"
        "oseg2_%=:\n\t"
        "s_or_b32 s46, s46, 4\n\t"
        "s_waitcnt vmcnt(11) lgkmcnt(14)\n\t"
        "v_mfma_f32_32x32x16_bf16 v[64:79], v[128:131], v[160:163], 0\n\t"
        "ds_read_b64_tr_b16 v[128:129], v228 offset:8192\n\t"
        "ds_read_b64_tr_b16 v[130:131], v228 offset:9216\n\t"
        "s_waitcnt lgkmcnt(14)\n\t"
        "v_mfma_f32_32x32x16_bf16 v[80:95], v[132:135], v[160:163], 0\n\t"
        "ds_read_b64_tr_b16 v[132:133], v229 offset:8192\n\t"
        "ds_read_b64_tr_b16 v[134:135], v229 offset:9216\n\t"
        "s_add_u32 s44, s44, 0x1000\n\t"
        "buffer_load_dwordx4 v[208:211], v224, s[40:43], s44 offen offset:0 sc1\n\t"
        "s_waitcnt vmcnt(11) lgkmcnt(14)\n\t"
        "v_mfma_f32_32x32x16_bf16 v[64:79], v[136:139], v[164:167], v[64:79]\n\t"
        "ds_read_b32 v233, v232\n\t"
        "ds_read_b64_tr_b16 v[136:137], v228 offset:10240\n\t"
        "ds_read_b64_tr_b16 v[138:139], v228 offset:11264\n\t"
        "s_waitcnt lgkmcnt(14)\n\t"
        "v_mfma_f32_32x32x16_bf16 v[80:95], v[140:143], v[164:167], v[80:95]\n\t"
        "ds_read_b64_tr_b16 v[140:141], v229 offset:10240\n\t"
        "ds_read_b64_tr_b16 v[142:143], v229 offset:11264\n\t"
        "buffer_load_dwordx4 v[212:215], v224, s[40:43], s44 offen offset:1024 sc1\n\t"
        "s_waitcnt vmcnt(11) lgkmcnt(14)\n\t"
        "v_mfma_f32_32x32x16_bf16 v[64:79], v[144:147], v[168:171], v[64:79]\n\t"
        "ds_read_b64_tr_b16 v[144:145], v228 offset:12288\n\t"
        "ds_read_b64_tr_b16 v[146:147], v228 offset:13312\n\t"
        "s_waitcnt lgkmcnt(14)\n\t"
        "v_mfma_f32_32x32x16_bf16 v[80:95], v[148:151], v[168:171], v[80:95]\n\t"
        "ds_read_b64_tr_b16 v[148:149], v229 offset:12288\n\t"
        "ds_read_b64_tr_b16 v[150:151], v229 offset:13312\n\t"
        "buffer_load_dwordx4 v[216:219], v224, s[40:43], s44 offen offset:2048 sc1\n\t"
        "s_waitcnt vmcnt(11) lgkmcnt(8)\n\t"
        "v_mfma_f32_32x32x16_bf16 v[64:79], v[152:155], v[172:175], v[64:79]\n\t"
        "v_readfirstlane_b32 s51, v233\n\t"
        "ds_read_b64_tr_b16 v[152:153], v228 offset:14336\n\t"
        "ds_read_b64_tr_b16 v[154:155], v228 offset:15360\n\t"
        "s_waitcnt lgkmcnt(14)\n\t"
        "v_mfma_f32_32x32x16_bf16 v[80:95], v[156:159], v[172:175], v[80:95]\n\t"
        "s_andn2_b32 s51, s51, s46\n\t"
        "s_ff1_i32_b32 s47, s51\n\t"
        "s_add_u32 s45, s47, s49\n\t"
        "s_and_b32 s45, s45, 3\n\t"
        "s_lshl_b32 s45, s45, 14\n\t"
        "ds_read_b64_tr_b16 v[156:157], v229 offset:14336\n\t"
        "ds_read_b64_tr_b16 v[158:159], v229 offset:15360\n\t"
        "buffer_load_dwordx4 v[220:223], v224, s[40:43], s44 offen offset:3072 sc1\n\t"
        "s_waitcnt vmcnt(11) lgkmcnt(14)\n\t"
        "v_mfma_f32_32x32x16_bf16 v[64:79], v[128:131], v[176:179], v[64:79]\n\t"
        "s_and_b32 s51, s47, 0x80000000\n\t"
        "v_or_b32 v225, s51, v224\n\t"
        "s_and_b32 s51, s47, 3\n\t"
        "s_lshl_b32 s51, s51, 15\n\t"
        "v_add_u32 v230, s51, v226\n\t"
        "v_add_u32 v231, s51, v227\n\t"
        "ds_read_b64_tr_b16 v[128:129], v228 offset:16384\n\t"
        "ds_read_b64_tr_b16 v[130:131], v228 offset:17408\n\t"
        "s_waitcnt lgkmcnt(14)\n\t"
        "v_mfma_f32_32x32x16_bf16 v[80:95], v[132:135], v[176:179], v[80:95]\n\t"
        "ds_read_b64_tr_b16 v[132:133], v229 offset:16384\n\t"
        "ds_read_b64_tr_b16 v[134:135], v229 offset:17408\n\t"
        "buffer_load_dwordx4 v[160:163], v225, s[40:43], s45 offen offset:0 sc1\n\t"
        "s_waitcnt vmcnt(11) lgkmcnt(14)\n\t"
        "v_mfma_f32_32x32x16_bf16 v[64:79], v[136:139], v[180:183], v[64:79]\n\t"
        "ds_read_b64_tr_b16 v[136:137], v228 offset:18432\n\t"
        "ds_read_b64_tr_b16 v[138:139], v228 offset:19456\n\t"
        "s_waitcnt lgkmcnt(14)\n\t"
        "v_mfma_f32_32x32x16_bf16 v[80:95], v[140:143], v[180:183], v[80:95]\n\t"
        "ds_read_b64_tr_b16 v[140:141], v229 offset:18432\n\t"
        "ds_read_b64_tr_b16 v[142:143], v229 offset:19456\n\t"
        "buffer_load_dwordx4 v[164:167], v225, s[40:43], s45 offen offset:1024 sc1\n\t"
        "s_waitcnt vmcnt(11) lgkmcnt(14)\n\t"
        "v_mfma_f32_32x32x16_bf16 v[64:79], v[144:147], v[184:187], v[64:79]\n\t"
        "ds_read_b64_tr_b16 v[144:145], v228 offset:20480\n\t"
        "ds_read_b64_tr_b16 v[146:147], v228 offset:21504\n\t"
        "s_waitcnt lgkmcnt(14)\n\t"
        "v_mfma_f32_32x32x16_bf16 v[80:95], v[148:151], v[184:187], v[80:95]\n\t"
        "ds_read_b64_tr_b16 v[148:149], v229 offset:20480\n\t"
        "ds_read_b64_tr_b16 v[150:151], v229 offset:21504\n\t"
        "buffer_load_dwordx4 v[168:171], v225, s[40:43], s45 offen offset:2048 sc1\n\t"
        "s_waitcnt vmcnt(11) lgkmcnt(14)\n\t"
        "v_mfma_f32_32x32x16_bf16 v[64:79], v[152:155], v[188:191], v[64:79]\n\t"
        "ds_read_b64_tr_b16 v[152:153], v228 offset:22528\n\t"
        "ds_read_b64_tr_b16 v[154:155], v228 offset:23552\n\t"
        "s_waitcnt lgkmcnt(14)\n\t"
        "v_mfma_f32_32x32x16_bf16 v[80:95], v[156:159], v[188:191], v[80:95]\n\t"
        "ds_read_b64_tr_b16 v[156:157], v229 offset:22528\n\t"
        "ds_read_b64_tr_b16 v[158:159], v229 offset:23552\n\t"
        "buffer_load_dwordx4 v[172:175], v225, s[40:43], s45 offen offset:3072 sc1\n\t"
        "s_waitcnt vmcnt(11) lgkmcnt(14)\n\t"
        "v_mfma_f32_32x32x16_bf16 v[64:79], v[128:131], v[192:195], v[64:79]\n\t"
        "ds_read_b64_tr_b16 v[128:129], v228 offset:24576\n\t"
        "ds_read_b64_tr_b16 v[130:131], v228 offset:25600\n\t"
        "s_waitcnt lgkmcnt(14)\n\t"
        "v_mfma_f32_32x32x16_bf16 v[80:95], v[132:135], v[192:195], v[80:95]\n\t"
        "ds_read_b64_tr_b16 v[132:133], v229 offset:24576\n\t"
        "ds_read_b64_tr_b16 v[134:135], v229 offset:25600\n\t"
        "s_add_u32 s45, s45, 0x1000\n\t"
        "buffer_load_dwordx4 v[176:179], v225, s[40:43], s45 offen offset:0 sc1\n\t"
        "s_waitcnt vmcnt(11) lgkmcnt(14)\n\t"
        "v_mfma_f32_32x32x16_bf16 v[64:79], v[136:139], v[196:199], v[64:79]\n\t"
        "ds_read_b64_tr_b16 v[136:137], v228 offset:26624\n\t"
        "ds_read_b64_tr_b16 v[138:139], v228 offset:27648\n\t"
        "s_waitcnt lgkmcnt(14)\n\t"
        "v_mfma_f32_32x32x16_bf16 v[80:95], v[140:143], v[196:199], v[80:95]\n\t"
        "ds_read_b64_tr_b16 v[140:141], v229 offset:26624\n\t"
        "ds_read_b64_tr_b16 v[142:143], v229 offset:27648\n\t"
        "buffer_load_dwordx4 v[180:183], v225, s[40:43], s45 offen offset:1024 sc1\n\t"
        "s_waitcnt vmcnt(11) lgkmcnt(14)\n\t"
        "v_mfma_f32_32x32x16_bf16 v[64:79], v[144:147], v[200:203], v[64:79]\n\t"
        "ds_read_b64_tr_b16 v[144:145], v228 offset:28672\n\t"
        "ds_read_b64_tr_b16 v[146:147], v228 offset:29696\n\t"
        "s_waitcnt lgkmcnt(14)\n\t"
        "v_mfma_f32_32x32x16_bf16 v[80:95], v[148:151], v[200:203], v[80:95]\n\t"
        "ds_read_b64_tr_b16 v[148:149], v229 offset:28672\n\t"
        "ds_read_b64_tr_b16 v[150:151], v229 offset:29696\n\t"
        "buffer_load_dwordx4 v[184:187], v225, s[40:43], s45 offen offset:2048 sc1\n\t"
        "s_waitcnt vmcnt(11) lgkmcnt(14)\n\t"
        "v_mfma_f32_32x32x16_bf16 v[64:79], v[152:155], v[204:207], v[64:79]\n\t"
        "ds_read_b64_tr_b16 v[152:153], v228 offset:30720\n\t"
        "ds_read_b64_tr_b16 v[154:155], v228 offset:31744\n\t"
        "s_waitcnt lgkmcnt(14)\n\t"
        "v_mfma_f32_32x32x16_bf16 v[80:95], v[156:159], v[204:207], v[80:95]\n\t"
        "ds_read_b64_tr_b16 v[156:157], v229 offset:30720\n\t"
        "ds_read_b64_tr_b16 v[158:159], v229 offset:31744\n\t"
        "buffer_load_dwordx4 v[188:191], v225, s[40:43], s45 offen offset:3072 sc1\n\t"
        "s_waitcnt vmcnt(11) lgkmcnt(14)\n\t"
        "v_mfma_f32_32x32x16_bf16 v[64:79], v[128:131], v[208:211], v[64:79]\n\t"
        "ds_read_b64_tr_b16 v[128:129], v230 offset:0\n\t"
        "ds_read_b64_tr_b16 v[130:131], v230 offset:1024\n\t"
        "s_waitcnt lgkmcnt(14)\n\t"
        "v_mfma_f32_32x32x16_bf16 v[80:95], v[132:135], v[208:211], v[80:95]\n\t"
        "ds_read_b64_tr_b16 v[132:133], v231 offset:0\n\t"
        "ds_read_b64_tr_b16 v[134:135], v231 offset:1024\n\t"
        "s_add_u32 s45, s45, 0x1000\n\t"
        "buffer_load_dwordx4 v[192:195], v225, s[40:43], s45 offen offset:0 sc1\n\t"
        "s_waitcnt vmcnt(11) lgkmcnt(14)\n\t"
        "v_mfma_f32_32x32x16_bf16 v[64:79], v[136:139], v[212:215], v[64:79]\n\t"
        "ds_read_b64_tr_b16 v[136:137], v230 offset:2048\n\t"
        "ds_read_b64_tr_b16 v[138:139], v230 offset:3072\n\t"
        "s_waitcnt lgkmcnt(14)\n\t"
        "v_mfma_f32_32x32x16_bf16 v[80:95], v[140:143], v[212:215], v[80:95]\n\t"
        "ds_read_b64_tr_b16 v[140:141], v231 offset:2048\n\t"
        "ds_read_b64_tr_b16 v[142:143], v231 offset:3072\n\t"
        "buffer_load_dwordx4 v[196:199], v225, s[40:43], s45 offen offset:1024 sc1\n\t"
        "s_waitcnt vmcnt(11) lgkmcnt(14)\n\t"
        "v_mfma_f32_32x32x16_bf16 v[64:79], v[144:147], v[216:219], v[64:79]\n\t"
        "ds_read_b64_tr_b16 v[144:145], v230 offset:4096\n\t"
        "ds_read_b64_tr_b16 v[146:147], v230 offset:5120\n\t"
        "s_waitcnt lgkmcnt(14)\n\t"
        "v_mfma_f32_32x32x16_bf16 v[80:95], v[148:151], v[216:219], v[80:95]\n\t"
        "ds_read_b64_tr_b16 v[148:149], v231 offset:4096\n\t"
        "ds_read_b64_tr_b16 v[150:151], v231 offset:5120\n\t"
        "buffer_load_dwordx4 v[200:203], v225, s[40:43], s45 offen offset:2048 sc1\n\t"
        "s_waitcnt vmcnt(11) lgkmcnt(14)\n\t"
        "v_mfma_f32_32x32x16_bf16 v[64:79], v[152:155], v[220:223], v[64:79]\n\t"
        "ds_read_b64_tr_b16 v[152:153], v230 offset:6144\n\t"
        "ds_read_b64_tr_b16 v[154:155], v230 offset:7168\n\t"
        "s_waitcnt lgkmcnt(14)\n\t"
        "v_mfma_f32_32x32x16_bf16 v[80:95], v[156:159], v[220:223], v[80:95]\n\t"
        "ds_read_b64_tr_b16 v[156:157], v231 offset:6144\n\t"
        "ds_read_b64_tr_b16 v[158:159], v231 offset:7168\n\t"
        "buffer_load_dwordx4 v[204:207], v225, s[40:43], s45 offen offset:3072 sc1\n\t"
        "s_mov_b32 s44, s45\n\t"
        "s_cmp_eq_u32 s47, 0\n\t"
        "s_cbranch_scc1 oseg0_%=\n\t"
        "s_cmp_eq_u32 s47, 1\n\t"
        "s_cbranch_scc1 oseg1_%=\n\t"
        "s_cmp_eq_u32 s47, 2\n\t"
        "s_cbranch_scc1 oseg2_%=\n\t"
        "s_cmp_eq_u32 s47, 3\n\t"
        "s_cbranch_scc1 oseg3_%=\n\t"
        "s_cmp_lg_u32 s46, 15\n\t"
        "s_cbranch_scc1 ostart_%=\n\t"
        "s_branch oend_%=\n\t"
        "oseg3_%=:\n\t"
        "s_or_b32 s46, s46, 8\n\t"
        "s_waitcnt vmcnt(11) lgkmcnt(14)\n\t"
        "v_mfma_f32_32x32x16_bf16 v[96:111], v[128:131], v[160:163], 0\n\t"
        "ds_read_b64_tr_b16 v[128:129], v228 offset:40960\n\t"
        "ds_read_b64_tr_b16 v[130:131], v228 offset:41984\n\t"
        "s_waitcnt lgkmcnt(14)\n\t"
        "v_mfma_f32_32x32x16_bf16 v[112:127], v[132:135], v[160:163], 0\n\t"
        "ds_read_b64_tr_b16 v[132:133], v229 offset:40960\n\t"
        "ds_read_b64_tr_b16 v[134:135], v229 offset:41984\n\t"
        "s_add_u32 s44, s44, 0x1000\n\t"
        "buffer_load_dwordx4 v[208:211], v224, s[40:43], s44 offen offset:0 sc1\n\t"
        "s_waitcnt vmcnt(11) lgkmcnt(14)\n\t"
        "v_mfma_f32_32x32x16_bf16 v[96:111], v[136:139], v[164:167], v[96:111]\n\t"
        "ds_read_b32 v233, v232\n\t"
        "ds_read_b64_tr_b16 v[136:137], v228 offset:43008\n\t"
        "ds_read_b64_tr_b16 v[138:139], v228 offset:44032\n\t"
        "s_waitcnt lgkmcnt(14)\n\t"
        "v_mfma_f32_32x32x16_bf16 v[112:127], v[140:143], v[164:167], v[112:127]\n\t"
        "ds_read_b64_tr_b16 v[140:141], v229 offset:43008\n\t"
        "ds_read_b64_tr_b16 v[142:143], v229 offset:44032\n\t"
        "buffer_load_dwordx4 v[212:215], v224, s[40:43], s44 offen offset:1024 sc1\n\t"
        "s_waitcnt vmcnt(11) lgkmcnt(14)\n\t"
        "v_mfma_f32_32x32x16_bf16 v[96:111], v[144:147], v[168:171], v[96:111]\n\t"
        "ds_read_b64_tr_b16 v[144:145], v228 offset:45056\n\t"
        "ds_read_b64_tr_b16 v[146:147], v228 offset:46080\n\t"
        "s_waitcnt lgkmcnt(14)\n\t"
        "v_mfma_f32_32x32x16_bf16 v[112:127], v[148:151], v[168:171], v[112:127]\n\t"
        "ds_read_b64_tr_b16 v[148:149], v229 offset:45056\n\t"
        "ds_read_b64_tr_b16 v[150:151], v229 offset:46080\n\t"
        "buffer_load_dwordx4 v[216:219], v224, s[40:43], s44 offen offset:2048 sc1\n\t"
        "s_waitcnt vmcnt(11) lgkmcnt(8)\n\t"
        "v_mfma_f32_32x32x16_bf16 v[96:111], v[152:155], v[172:175], v[96:111]\n\t"
        "v_readfirstlane_b32 s51, v233\n\t"
        "ds_read_b64_tr_b16 v[152:153], v228 offset:47104\n\t"
        "ds_read_b64_tr_b16 v[154:155], v228 offset:48128\n\t"
        "s_waitcnt lgkmcnt(14)\n\t"
        "v_mfma_f32_32x32x16_bf16 v[112:127], v[156:159], v[172:175], v[112:127]\n\t"
        "s_andn2_b32 s51, s51, s46\n\t"
        "s_ff1_i32_b32 s47, s51\n\t"
        "s_add_u32 s45, s47, s49\n\t"
        "s_and_b32 s45, s45, 3\n\t"
        "s_lshl_b32 s45, s45, 14\n\t"
        "ds_read_b64_tr_b16 v[156:157], v229 offset:47104\n\t"
        "ds_read_b64_tr_b16 v[158:159], v229 offset:48128\n\t"
        "buffer_load_dwordx4 v[220:223], v224, s[40:43], s44 offen offset:3072 sc1\n\t"
        "s_waitcnt vmcnt(11) lgkmcnt(14)\n\t"
        "v_mfma_f32_32x32x16_bf16 v[96:111], v[128:131], v[176:179], v[96:111]\n\t"
        "s_and_b32 s51, s47, 0x80000000\n\t"
        "v_or_b32 v225, s51, v224\n\t"
        "s_and_b32 s51, s47, 3\n\t"
        "s_lshl_b32 s51, s51, 15\n\t"
        "v_add_u32 v230, s51, v226\n\t"
        "v_add_u32 v231, s51, v227\n\t"
        "ds_read_b64_tr_b16 v[128:129], v228 offset:49152\n\t"
        "ds_read_b64_tr_b16 v[130:131], v228 offset:50176\n\t"
        "s_waitcnt lgkmcnt(14)\n\t"
        "v_mfma_f32_32x32x16_bf16 v[112:127], v[132:135], v[176:179], v[112:127]\n\t"
        "ds_read_b64_tr_b16 v[132:133], v229 offset:49152\n\t"
        "ds_read_b64_tr_b16 v[134:135], v229 offset:50176\n\t"
        "buffer_load_dwordx4 v[160:163], v225, s[40:43], s45 offen offset:0 sc1\n\t"
        "s_waitcnt vmcnt(11) lgkmcnt(14)\n\t"
        "v_mfma_f32_32x32x16_bf16 v[96:111], v[136:139], v[180:183], v[96:111]\n\t"
        "ds_read_b64_tr_b16 v[136:137], v228 offset:51200\n\t"
        "ds_read_b64_tr_b16 v[138:139], v228 offset:52224\n\t"
        "s_waitcnt lgkmcnt(14)\n\t"
        "v_mfma_f32_32x32x16_bf16 v[112:127], v[140:143], v[180:183], v[112:127]\n\t"
        "ds_read_b64_tr_b16 v[140:141], v229 offset:51200\n\t"
        "ds_read_b64_tr_b16 v[142:143], v229 offset:52224\n\t"
        "buffer_load_dwordx4 v[164:167], v225, s[40:43], s45 offen offset:1024 sc1\n\t"
        "s_waitcnt vmcnt(11) lgkmcnt(14)\n\t"
        "v_mfma_f32_32x32x16_bf16 v[96:111], v[144:147], v[184:187], v[96:111]\n\t"
        "ds_read_b64_tr_b16 v[144:145], v228 offset:53248\n\t"
        "ds_read_b64_tr_b16 v[146:147], v228 offset:54272\n\t"
        "s_waitcnt lgkmcnt(14)\n\t"
        "v_mfma_f32_32x32x16_bf16 v[112:127], v[148:151], v[184:187], v[112:127]\n\t"
        "ds_read_b64_tr_b16 v[148:149], v229 offset:53248\n\t"
        "ds_read_b64_tr_b16 v[150:151], v229 offset:54272\n\t"
        "buffer_load_dwordx4 v[168:171], v225, s[40:43], s45 offen offset:2048 sc1\n\t"
        "s_waitcnt vmcnt(11) lgkmcnt(14)\n\t"
        "v_mfma_f32_32x32x16_bf16 v[96:111], v[152:155], v[188:191], v[96:111]\n\t"
        "ds_read_b64_tr_b16 v[152:153], v228 offset:55296\n\t"
        "ds_read_b64_tr_b16 v[154:155], v228 offset:56320\n\t"
        "s_waitcnt lgkmcnt(14)\n\t"
        "v_mfma_f32_32x32x16_bf16 v[112:127], v[156:159], v[188:191], v[112:127]\n\t"
        "ds_read_b64_tr_b16 v[156:157], v229 offset:55296\n\t"
        "ds_read_b64_tr_b16 v[158:159], v229 offset:56320\n\t"
        "buffer_load_dwordx4 v[172:175], v225, s[40:43], s45 offen offset:3072 sc1\n\t"
        "s_waitcnt vmcnt(11) lgkmcnt(14)\n\t"
        "v_mfma_f32_32x32x16_bf16 v[96:111], v[128:131], v[192:195], v[96:111]\n\t"
        "ds_read_b64_tr_b16 v[128:129], v228 offset:57344\n\t"
        "ds_read_b64_tr_b16 v[130:131], v228 offset:58368\n\t"
        "s_waitcnt lgkmcnt(14)\n\t"
        "v_mfma_f32_32x32x16_bf16 v[112:127], v[132:135], v[192:195], v[112:127]\n\t"
        "ds_read_b64_tr_b16 v[132:133], v229 offset:57344\n\t"
        "ds_read_b64_tr_b16 v[134:135], v229 offset:58368\n\t"
        "s_add_u32 s45, s45, 0x1000\n\t"
        "buffer_load_dwordx4 v[176:179], v225, s[40:43], s45 offen offset:0 sc1\n\t"
        "s_waitcnt vmcnt(11) lgkmcnt(14)\n\t"
        "v_mfma_f32_32x32x16_bf16 v[96:111], v[136:139], v[196:199], v[96:111]\n\t"
        "ds_read_b64_tr_b16 v[136:137], v228 offset:59392\n\t"
        "ds_read_b64_tr_b16 v[138:139], v228 offset:60416\n\t"
        "s_waitcnt lgkmcnt(14)\n\t"
        "v_mfma_f32_32x32x16_bf16 v[112:127], v[140:143], v[196:199], v[112:127]\n\t"
        "ds_read_b64_tr_b16 v[140:141], v229 offset:59392\n\t"
        "ds_read_b64_tr_b16 v[142:143], v229 offset:60416\n\t"
        "buffer_load_dwordx4 v[180:183], v225, s[40:43], s45 offen offset:1024 sc1\n\t"
        "s_waitcnt vmcnt(11) lgkmcnt(14)\n\t"
        "v_mfma_f32_32x32x16_bf16 v[96:111], v[144:147], v[200:203], v[96:111]\n\t"
        "ds_read_b64_tr_b16 v[144:145], v228 offset:61440\n\t"
        "ds_read_b64_tr_b16 v[146:147], v228 offset:62464\n\t"
        "s_waitcnt lgkmcnt(14)\n\t"
        "v_mfma_f32_32x32x16_bf16 v[112:127], v[148:151], v[200:203], v[112:127]\n\t"
        "ds_read_b64_tr_b16 v[148:149], v229 offset:61440\n\t"
        "ds_read_b64_tr_b16 v[150:151], v229 offset:62464\n\t"
        "buffer_load_dwordx4 v[184:187], v225, s[40:43], s45 offen offset:2048 sc1\n\t"
        "s_waitcnt vmcnt(11) lgkmcnt(14)\n\t"
        "v_mfma_f32_32x32x16_bf16 v[96:111], v[152:155], v[204:207], v[96:111]\n\t"
        "ds_read_b64_tr_b16 v[152:153], v228 offset:63488\n\t"
        "ds_read_b64_tr_b16 v[154:155], v228 offset:64512\n\t"
        "s_waitcnt lgkmcnt(14)\n\t"
        "v_mfma_f32_32x32x16_bf16 v[112:127], v[156:159], v[204:207], v[112:127]\n\t"
        "ds_read_b64_tr_b16 v[156:157], v229 offset:63488\n\t"
        "ds_read_b64_tr_b16 v[158:159], v229 offset:64512\n\t"
        "buffer_load_dwordx4 v[188:191], v225, s[40:43], s45 offen offset:3072 sc1\n\t"
        "s_waitcnt vmcnt(11) lgkmcnt(14)\n\t"
        "v_mfma_f32_32x32x16_bf16 v[96:111], v[128:131], v[208:211], v[96:111]\n\t"
        "ds_read_b64_tr_b16 v[128:129], v230 offset:0\n\t"
        "ds_read_b64_tr_b16 v[130:131], v230 offset:1024\n\t"
        "s_waitcnt lgkmcnt(14)\n\t"
        "v_mfma_f32_32x32x16_bf16 v[112:127], v[132:135], v[208:211], v[112:127]\n\t"
        "ds_read_b64_tr_b16 v[132:133], v231 offset:0\n\t"
        "ds_read_b64_tr_b16 v[134:135], v231 offset:1024\n\t"
        "s_add_u32 s45, s45, 0x1000\n\t"
        "buffer_load_dwordx4 v[192:195], v225, s[40:43], s45 offen offset:0 sc1\n\t"
        "s_waitcnt vmcnt(11) lgkmcnt(14)\n\t"
        "v_mfma_f32_32x32x16_bf16 v[96:111], v[136:139], v[212:215], v[96:111]\n\t"
        "ds_read_b64_tr_b16 v[136:137], v230 offset:2048\n\t"
        "ds_read_b64_tr_b16 v[138:139], v230 offset:3072\n\t"
        "s_waitcnt lgkmcnt(14)\n\t"
        "v_mfma_f32_32x32x16_bf16 v[112:127], v[140:143], v[212:215], v[112:127]\n\t"
        "ds_read_b64_tr_b16 v[140:141], v231 offset:2048\n\t"
        "ds_read_b64_tr_b16 v[142:143], v231 offset:3072\n\t"
        "buffer_load_dwordx4 v[196:199], v225, s[40:43], s45 offen offset:1024 sc1\n\t"
        "s_waitcnt vmcnt(11) lgkmcnt(14)\n\t"
        "v_mfma_f32_32x32x16_bf16 v[96:111], v[144:147], v[216:219], v[96:111]\n\t"
        "ds_read_b64_tr_b16 v[144:145], v230 offset:4096\n\t"
        "ds_read_b64_tr_b16 v[146:147], v230 offset:5120\n\t"
        "s_waitcnt lgkmcnt(14)\n\t"
        "v_mfma_f32_32x32x16_bf16 v[112:127], v[148:151], v[216:219], v[112:127]\n\t"
        "ds_read_b64_tr_b16 v[148:149], v231 offset:4096\n\t"
        "ds_read_b64_tr_b16 v[150:151], v231 offset:5120\n\t"
        "buffer_load_dwordx4 v[200:203], v225, s[40:43], s45 offen offset:2048 sc1\n\t"
        "s_waitcnt vmcnt(11) lgkmcnt(14)\n\t"
        "v_mfma_f32_32x32x16_bf16 v[96:111], v[152:155], v[220:223], v[96:111]\n\t"
        "ds_read_b64_tr_b16 v[152:153], v230 offset:6144\n\t"
        "ds_read_b64_tr_b16 v[154:155], v230 offset:7168\n\t"
        "s_waitcnt lgkmcnt(14)\n\t"
        "v_mfma_f32_32x32x16_bf16 v[112:127], v[156:159], v[220:223], v[112:127]\n\t"
        "ds_read_b64_tr_b16 v[156:157], v231 offset:6144\n\t"
        "ds_read_b64_tr_b16 v[158:159], v231 offset:7168\n\t"
        "buffer_load_dwordx4 v[204:207], v225, s[40:43], s45 offen offset:3072 sc1\n\t"
        "s_mov_b32 s44, s45\n\t"
        "s_cmp_eq_u32 s47, 0\n\t"
        "s_cbranch_scc1 oseg0_%=\n\t"
        "s_cmp_eq_u32 s47, 1\n\t"
        "s_cbranch_scc1 oseg1_%=\n\t"
        "s_cmp_eq_u32 s47, 2\n\t"
        "s_cbranch_scc1 oseg2_%=\n\t"
        "s_cmp_eq_u32 s47, 3\n\t"
        "s_cbranch_scc1 oseg3_%=\n\t"
        "s_cmp_lg_u32 s46, 15\n\t"
        "s_cbranch_scc1 ostart_%=\n\t"
        "s_branch oend_%=\n\t"
        "oend_%=:\n\t"
        "s_waitcnt vmcnt(0) lgkmcnt(0)\n\t"
        "s_barrier\n\t"
        "s_nop 3\n\t"
        : "+{v[0:15]}"(acc[0]), "+{v[16:31]}"(acc[1]), "={v[32:47]}"(acc[2]), "={v[48:63]}"(acc[3]), "={v[64:79]}"(acc[4]), "={v[80:95]}"(acc[5]),
          "={v[96:111]}"(acc[6]), "={v[112:127]}"(acc[7]) STAMP_OUTS
        : "{v224}"(OFF_OF + L * (5 * MBY) + tile * TS + lane * 16), "{v226}"(a0), "{v227}"(a1), "{v228}"(a0 + 65536), "{v229}"(a1 + 65536),
          "{v232}"(lbase + L_W + 20 + 8 * L), "{s49}"((tn >> 2) + 1), "{s[40:43]}"(srd)
        : "v128", "v129", "v130", "v131", "v132", "v133", "v134", "v135", "v136", "v137", "v138", "v139", "v140", "v141", "v142", "v143", "v144", "v145", "v146", "v147", "v148", "v149", "v150", "v151", "v152", "v153", "v154", "v155", "v156", "v157", "v158", "v159", "v160", "v161", "v162", "v163", "v164", "v165", "v166", "v167", "v168", "v169", "v170", "v171", "v172", "v173", "v174", "v175", "v176", "v177", "v178", "v179", "v180", "v181", "v182", "v183", "v184", "v185", "v186", "v187", "v188", "v189", "v190", "v191", "v192", "v193", "v194", "v195", "v196", "v197", "v198", "v199", "v200", "v201", "v202", "v203", "v204", "v205", "v206", "v207", "v208", "v209", "v210", "v211", "v212", "v213", "v214", "v215", "v216", "v217", "v218", "v219", "v220", "v221", "v222", "v223", "v225", "v230", "v231", "v233", "s44", "s45", "s46", "s47", "s48", "s51", "vcc", "scc", "memory");
      }
      PHASE_VARS;
      STAMP_PUT(12 + 16 * L);
      STAMPV(15 + 16 * L);
#pragma unroll
      for (int g = 0; g < 8; ++g) wrc[g] = *reinterpret_cast<const u32x2*>(smem + L_WR + g * 16 + hi * 8);
      SBAR();
      float s = 0.f, sq = 0.f, d = 0.f;
      const unsigned xdst = OFF_X1 + tile * TS + tn * 4096 + lane * 16;
#pragma unroll
      for (int i = 0; i < 4; ++i) {
#pragma unroll
        for (int e = 0; e < 4; ++e) {
          const int j = i >> 1, k = 8 * (i & 1) + 2 * e;
          x1p[i][e] = pack2((acc[j][k] + acc[2 + j][k]) + (acc[4 + j][k] + acc[6 + j][k]), (acc[j][k + 1] + acc[2 + j][k + 1]) + (acc[4 + j][k + 1] + acc[6 + j][k + 1]));
        }
        if (L == 0) stg(rs, xdst + i * 1024, x1p[i]);
        SBAR();
      }
#pragma unroll
      for (int g = 0; g < 8; ++g)
#pragma unroll
        for (int e = 0; e < 2; ++e) {
          const unsigned o = x1p[g >> 1][2 * (g & 1) + e];
          s = dot2(o, 0x3f803f80u, s); sq = dot2(o, o, sq); d = dot2(o, wrc[g][e], d);
        }
      const u32x4 pst = {nzf(half_swap_sum(s)), nzf(half_swap_sum(sq)), nzf(half_swap_sum(d)), 0u};
      asm volatile("buffer_store_dwordx4 %0, %1, %2, 0 offen sc1\n\ts_waitcnt vmcnt(1)\n\tds_write_b32 %3, %4"
                   :: "v"(pst), "v"(stats_dst(sx, tile, tn, lane)), "s"(srd), "v"(lbase + L_W + 64 + 4 * wid), "v"(3) : "memory");
    }
    STAMPV(16 + 16 * L);
  }
  {
    PHASE_VARS;
    float S, Q, Dd, mean, var, mb1, rd1;
    __builtin_amdgcn_s_sleep(13);
    row_stats_sum(rs, OFF_SX + SX_LAYER, tile, lane, S, Q, Dd);
    STAMPV(33);
    row_stats_fin(rs, OFF_SX + SX_LAYER + SX_CAND + (tn * 16 + tm) * 128 + wid * 4, smem, 2, tile, lane, S, Q, Dd, U2F(words[0]), x1p, mean, var, mb1, rd1);
    STAMPV(34);
    u32x4 xo[4];
#pragma unroll
    for (int i = 0; i < 4; ++i) {
#pragma unroll
      for (int e = 0; e < 4; ++e) {
        const unsigned h2 = ln_pair(x1p[i][e], mb1, rd1);
        xo[i][e] = pack2(lo16(x1p[i][e]) + lo16(h2), hi16(x1p[i][e]) + hi16(h2));
      }
      *reinterpret_cast<u32x4*>(smem + wid * 4096 + r32 * 128 + ((2 * i + hi) ^ (r32 & 7)) * 16) = fo_swap(xo[i]);
    }
    asm volatile("s_waitcnt lgkmcnt(0)" ::: "memory");
#pragma unroll
    for (int i = 0; i < 4; ++i) { xo[i] = *reinterpret_cast<const u32x4*>(smem + wid * 4096 + i * 1024 + (lane >> 3) * 128 + ((lane ^ (lane >> 3)) & 7) * 16); PIN(xo[i]); }
    u16* dst = out + (size_t)(tile * 32 + (lane >> 3)) * D_ + tn * 64 + (lane & 7) * 8;
    lds_wait_ge(lbase + L_W + 4 + 4 * (lane & 1), 2);
    const int rel = (int)words[3] - tile * 32 - (lane >> 3);
#pragma unroll
    for (int i = 0; i < 4; ++i)
      if (rel != 8 * i) st16(dst + (size_t)i * 8 * D_, xo[i]);
    STAMPV(35);
    STAMP_END;
  }
}

extern "C" __global__ void end_marker() {}

extern "C" void kernel_launch(void* const* d_in, const int* in_sizes, int n_in, void* d_out, int out_size,
                              void* d_ws, size_t ws_size, hipStream_t stream) {
  char* ws = (char*)d_ws;
  mega_kernel<<<256, 512, 0, stream>>>((const u16*)d_in[0], (const u16*)d_in[1], (const u16*)d_in[2], (const u16*)d_in[4], (const u16*)d_in[5],
                                       (const u16*)d_in[6], (const u16*)d_in[7], ws, (u16*)d_out);
}
```

```cpp
#include <hip/hip_runtime.h>
#include <hip/hip_bf16.h>
#include <stdint.h>

typedef unsigned short u16;
typedef short s16x4 __attribute__((ext_vector_type(4)));
typedef short s16x8 __attribute__((ext_vector_type(8)));
typedef float f32x16 __attribute__((ext_vector_type(16)));
typedef float f32x8 __attribute__((ext_vector_type(8)));
typedef float f32x4 __attribute__((ext_vector_type(4)));
typedef float f32x2 __attribute__((ext_vector_type(2)));
typedef unsigned int u32x16 __attribute__((ext_vector_type(16)));
typedef unsigned int u32x8 __attribute__((ext_vector_type(8)));
typedef unsigned int u32x4 __attribute__((ext_vector_type(4)));
typedef unsigned int u32x2 __attribute__((ext_vector_type(2)));
typedef __bf16 bf16x2 __attribute__((ext_vector_type(2)));
typedef __bf16 bf16x8 __attribute__((ext_vector_type(8)));
typedef __bf16 bf16x16 __attribute__((ext_vector_type(16)));

#define DI __device__ __forceinline__
#define LDS_PTR(T, p) ((__attribute__((address_space(3))) T*)(p))
#define BARRIER() asm volatile("s_barrier" ::: "memory")
#define BAR_LGKM() asm volatile("s_waitcnt lgkmcnt(0)\n\ts_barrier" ::: "memory")
#define BAR_ALL() asm volatile("s_waitcnt vmcnt(0) lgkmcnt(0)\n\ts_barrier" ::: "memory")
#define DRAIN() asm volatile("s_waitcnt vmcnt(0)" ::: "memory")
#define MFMA32(a, b, c) __builtin_amdgcn_mfma_f32_32x32x16_bf16(a, b, c, 0, 0, 0)
#define MFMA16(a, b, c) __builtin_amdgcn_mfma_f32_16x16x32_bf16(a, b, c, 0, 0, 0)
#define PIN(x) asm volatile("" : "+v"(x))
#define EX2(x) __builtin_amdgcn_exp2f(x)
#define RCP(x) __builtin_amdgcn_rcpf(x)
#define SBAR() __builtin_amdgcn_sched_barrier(0)

#define STAMP_INIT
#define STAMPV(i)
#define STAMP_END
#define STAMP_DECL
#define STAMP_OUTS
#define STAMP_PUT(i)
#define JIT()
constexpr int D_ = 1024, F_ = 3584;
constexpr float QS = 0.17677669529663687f * 1.4426950408889634f;

constexpr unsigned MBY = 1u << 20;
constexpr unsigned TS = 65536 + 4096, HS = 262144 + 4096;
constexpr unsigned OFF_HF = 0, OFF_X1 = 5 * MBY;
constexpr unsigned OFF_OF = 10 * MBY;
constexpr unsigned OFF_KV = 20 * MBY;
constexpr unsigned OFF_FL = 40 * MBY;
constexpr unsigned FL_EPOCH = OFF_FL;
constexpr unsigned FL_KV = OFF_FL + 4096;
constexpr unsigned FL_O = FL_KV + 65536;
constexpr unsigned FL_X = FL_O + 65536;
constexpr unsigned OFF_CS = 41 * MBY;
constexpr unsigned OFF_SX = 42 * MBY;
constexpr unsigned SX_ST = 0;
constexpr unsigned SX_ROW = 512 * 1024;
constexpr unsigned SX_CAND = SX_ROW + 256 * 1024;
constexpr unsigned SX_GP = SX_CAND + 32 * 1024;
constexpr unsigned SX_YP = SX_GP + 224 * 1024;
constexpr unsigned SX_LAYER = SX_YP + 224 * 1024;
constexpr unsigned SX_QP = 2 * SX_LAYER;
constexpr unsigned SX_BYTES = SX_QP + 512 * 1024;
static_assert(SX_BYTES <= 6 * 256 * 128 * 16, "sentinel fill");
constexpr unsigned WS_BYTES = 48 * MBY, OOB = 0x80000000u;
constexpr unsigned SENT = 0xFFFFFFFFu;

constexpr int NS = 9, DD = 3, SLOTB = 12288;
constexpr int L_XCH = 122880;
constexpr int L_SL = L_XCH + 8192;
constexpr int L_CAND = L_XCH + 32768;
constexpr int L_AB = L_XCH + 34816;
constexpr int L_FULL = 159744, L_FREE = L_FULL + 512;
constexpr int L_CS = L_FREE + 1024, L_PATCH = L_CS + 768;
constexpr int L_GU = L_PATCH + 768, L_TAG = L_GU + 256, L_WR = L_TAG + 256;
constexpr int L_X2F = L_WR + 128, L_X2P = L_X2F + 128;
constexpr int L_W = L_X2P + 128;
constexpr int LDS_BYTES = 163840;
static_assert(NS * SLOTB <= 54 * 2048 && L_W + 128 <= LDS_BYTES && (NS + 1) * SLOTB <= L_XCH && L_AB + 2048 <= L_FULL, "LDS map");

DI int fresh_lane() { int l; asm volatile("v_mbcnt_lo_u32_b32 %0, -1, 0\n\tv_mbcnt_hi_u32_b32 %0, -1, %0" : "=v"(l)); return l; }
#define LANE_VARS const int lane = fresh_lane(), r32 = lane & 31, hi = lane >> 5, tq = (lane & 15) >> 2, tp = lane & 3, tblk = (lane >> 4) & 1, \
  wrow = 4 * hi + tq, wsw = 4 * ((wrow >> 1) & 1), wch = 2 * tblk + (tp >> 1), c24 = r32 < 24 ? r32 : 23; \
  (void)r32; (void)hi; (void)wsw; (void)wch; (void)c24
DI int fresh_s(int v) { asm volatile("" : "+s"(v)); return v; }
#define PHASE_VARS const int tm = fresh_s(tm_), tn = fresh_s(tn_), wid = fresh_s(wid_), tile = tm * 4 + wid, hh = wid & 1, kh = wid >> 1; (void)tile; (void)hh; (void)kh; LANE_VARS
DI int kpos(int tm, int tn, int i) { return i == 0 ? tn : (tn + 1 + (1 * (3 - (tn & 3)) + i - 1) % 15) & 15; }
DI float bf2f(u16 v) { return __uint_as_float(((unsigned)v) << 16); }
DI unsigned pack2(float lo, float hi) { f32x2 v = {lo, hi}; return __builtin_bit_cast(unsigned, __builtin_convertvector(v, bf16x2)); }
DI float rbf(float f) { return __uint_as_float(pack2(f, 0.f) << 16); }
DI float lo16(unsigned u) { return __uint_as_float(u << 16); }
DI float hi16(unsigned u) { return __uint_as_float(u & 0xffff0000u); }
DI float opq(float r) { asm("" : "+v"(r)); return r; }
DI float fadd(float a, float b) { return opq(a + b); }
DI float fmul(float a, float b) { return opq(a * b); }
DI float ffma(float a, float b, float c) { return opq(__builtin_fmaf(a, b, c)); }
DI float U2F(unsigned u) { return __uint_as_float(u); }
DI float half_swap_sum(float v) {
  auto r = __builtin_amdgcn_permlane32_swap(__float_as_uint(v), __float_as_uint(v), false, false);
  return fadd(U2F(r[0]), U2F(r[1]));
}
DI float half_swap_max(float v) {
  auto r = __builtin_amdgcn_permlane32_swap(__float_as_uint(v), __float_as_uint(v), false, false);
  return fmaxf(__uint_as_float(r[0]), __uint_as_float(r[1]));
}
template <int CTRL> DI unsigned dpp(unsigned v) { return (unsigned)__builtin_amdgcn_update_dpp(0, (int)v, CTRL, 0xf, 0xf, false); }
DI float row_swap_sum(float v) {
  auto r = __builtin_amdgcn_permlane16_swap(__float_as_uint(v), __float_as_uint(v), false, false);
  return fadd(U2F(r[0]), U2F(r[1]));
}
DI float half_sum(float v) {
  v = fadd(v, U2F(dpp<0xB1>(__float_as_uint(v)))); v = fadd(v, U2F(dpp<0x4E>(__float_as_uint(v))));
  v = fadd(v, U2F(dpp<0x141>(__float_as_uint(v)))); v = fadd(v, U2F(dpp<0x140>(__float_as_uint(v))));
  return row_swap_sum(v);
}
DI float wave_sum(float v) { return half_swap_sum(half_sum(v)); }
DI unsigned half_max_u32(unsigned v) {
  v = max(v, dpp<0xB1>(v)); v = max(v, dpp<0x4E>(v)); v = max(v, dpp<0x141>(v)); v = max(v, dpp<0x140>(v));
  auto r = __builtin_amdgcn_permlane16_swap(v, v, false, false);
  return max(r[0], r[1]);
}
DI unsigned wave_max_u32(unsigned v) {
  v = half_max_u32(v);
  auto r = __builtin_amdgcn_permlane32_swap(v, v, false, false);
  return max(r[0], r[1]);
}
DI u32x4 fo_swap(u32x4 v) {
  auto a = __builtin_amdgcn_permlane32_swap(v[0], v[2], false, false);
  auto b = __builtin_amdgcn_permlane32_swap(v[1], v[3], false, false);
  u32x4 o; o[0] = a[0]; o[1] = b[0]; o[2] = a[1]; o[3] = b[1];
  return o;
}
DI void glds4(const void* sb, unsigned vo, unsigned lds_dst) {
  unsigned keep;
  asm volatile("s_mov_b32 %0, m0\n\ts_mov_b32 m0, %3\n\ts_nop 0\n\tglobal_load_lds_dword %1, %2\n\ts_mov_b32 m0, %0"
               : "=&s"(keep) : "v"(vo), "s"(sb), "s"(lds_dst) : "memory");
}
#define GL_ "global_load_lds_dwordx4 "
DI void glds_v4(const void* sb, const unsigned (&v)[4], unsigned m0v) {
  unsigned keep;
  asm volatile("s_mov_b32 %0, m0\n\ts_mov_b32 m0, %6\n\ts_nop 0\n\t" GL_ "%1, %5\n\t" GL_ "%2, %5 offset:1024\n\t" GL_ "%3, %5 offset:2048\n\t"
               GL_ "%4, %5 offset:3072\n\ts_mov_b32 m0, %0" : "=&s"(keep) : "v"(v[0]), "v"(v[1]), "v"(v[2]), "v"(v[3]), "s"(sb), "s"(m0v) : "memory");
}
#define GP_ "\n\ts_sleep %8\n\ts_waitcnt vmcnt(%7)\n\t"
template <int TH, int SL> DI void glds_v4t(const void* sb, const unsigned (&v)[4], unsigned m0v) {
  unsigned keep;
  asm volatile("s_mov_b32 %0, m0\n\ts_mov_b32 m0, %6\n\ts_nop 0\n\t" GL_ "%1, %5" GP_ GL_ "%2, %5 offset:1024" GP_ GL_ "%3, %5 offset:2048" GP_ GL_ "%4, %5 offset:3072" GP_ "s_mov_b32 m0, %0"
               : "=&s"(keep) : "v"(v[0]), "v"(v[1]), "v"(v[2]), "v"(v[3]), "s"(sb), "s"(m0v), "n"(TH), "n"(SL) : "memory");
}
DI void glds_v3(const void* sb, const unsigned (&v)[3], unsigned m0v) {
  unsigned keep;
  asm volatile("s_mov_b32 %0, m0\n\ts_mov_b32 m0, %5\n\ts_nop 0\n\t" GL_ "%1, %4\n\t" GL_ "%2, %4 offset:1024\n\t" GL_ "%3, %4 offset:2048\n\t"
               "s_mov_b32 m0, %0" : "=&s"(keep) : "v"(v[0]), "v"(v[1]), "v"(v[2]), "s"(sb), "s"(m0v) : "memory");
}
DI float dot2(unsigned a, unsigned b, float c) {
  return __builtin_amdgcn_fdot2_f32_bf16(__builtin_bit_cast(bf16x2, a), __builtin_bit_cast(bf16x2, b), c, false);
}
#define CODE_ADDR(sym, out) do { unsigned long long pc_; unsigned off_; \
  asm volatile("s_getpc_b64 %0\n1:\n\ts_mov_b32 %1, " #sym "-1b" : "=s"(pc_), "=s"(off_)); out = (const char*)(pc_ + (long long)(int)off_); } while (0)
DI void st16(void* p, u32x4 v) { asm volatile("global_store_dwordx4 %0, %1, off sc0 sc1\n\ts_nop 1" :: "v"(p), "v"(v) : "memory"); }
DI void ln_scalars(float mean, float var, float& mb, float& den) { mb = rbf(mean); den = rbf(__builtin_amdgcn_sqrtf(rbf(rbf(var) + 1e-5f))); }
DI unsigned ln_pair(unsigned v, float mb, float rd) {
  const unsigned c = pack2(lo16(v) - mb, hi16(v) - mb);
  return pack2(lo16(c) * rd, hi16(c) * rd);
}
DI unsigned ln_pair_s(unsigned v, float nmb, float rd) {
  const unsigned c = pack2(fadd(lo16(v), nmb), fadd(hi16(v), nmb));
  return pack2(fmul(lo16(c), rd), fmul(hi16(c), rd));
}
template <int N, int M> DI f32x4 sum4(const u32x4 (&v)[M]) {
  f32x4 t;
#pragma unroll
  for (int e = 0; e < 4; ++e) {
    t[e] = U2F(v[0][e]);
#pragma unroll
    for (int i = 1; i < N; ++i) t[e] = fadd(t[e], U2F(v[i][e]));
  }
  return t;
}
#define PERM_LO(x, y) __builtin_amdgcn_perm(y, x, 0x05040100u)
#define PERM_HI(x, y) __builtin_amdgcn_perm(y, x, 0x07060302u)
constexpr unsigned SPIN_CAP = 1u << 15;
DI unsigned nz(unsigned v) { return min(v, SENT - 1u); }
DI unsigned nzf(float v) { return nz(__float_as_uint(v)); }
DI unsigned mx4(unsigned m, const u32x4& v) { return max(max(max(m, v[0]), v[1]), max(v[2], v[3])); }
template <int N> DI bool none_sent(const u32x4 (&v)[N]) {
  unsigned m = 0u;
#pragma unroll
  for (int i = 0; i < N; ++i) m = mx4(m, v[i]);
  return m != SENT;
}
typedef __amdgpu_buffer_rsrc_t rsrc_t;
DI u32x4 ldg(rsrc_t rs, unsigned off) { return __builtin_amdgcn_raw_buffer_load_b128(rs, (int)off, 0, 16); }
DI void stg(rsrc_t rs, unsigned off, u32x4 v) { __builtin_amdgcn_raw_buffer_store_b128(v, rs, (int)off, 0, 16); }
DI void poll_pause(bool slow) { if (slow) __builtin_amdgcn_s_sleep(4); asm volatile("s_sleep 1" ::: "memory"); }
template <int N, class LD, class OKF> DI void poll_all(u32x4 (&out)[N], bool slow, LD ld, OKF okf) {
  for (unsigned spins = 0;; ++spins) {
    ld(out);
    SBAR();
    if (__all(okf(out)) || spins > SPIN_CAP) break;
    poll_pause(slow);
  }
}
DI void lds_set(unsigned addr, unsigned v) { asm volatile("s_waitcnt lgkmcnt(0)\n\tds_write_b32 %0, %1" :: "v"(addr), "v"(v) : "memory"); }
template <int SLEEP = 1> DI void lds_wait_ge(unsigned addr, unsigned need) {
  unsigned spin = 0x100000, v;
  asm volatile("1:\n\tds_read_b32 %0, %2\n\ts_sub_u32 %1, %1, 1\n\ts_waitcnt lgkmcnt(0)\n\ts_cbranch_scc1 2f\n\tv_cmp_gt_u32 vcc, %3, %0\n\ts_cbranch_vccz 2f\n\ts_sleep %4\n\ts_branch 1b\n2:"
               : "=&v"(v), "+s"(spin) : "v"(addr), "s"(need), "n"(SLEEP) : "vcc", "scc", "memory");
}
DI s16x8 tr2(const char* p, int second) {
  s16x4 lo = __builtin_amdgcn_ds_read_tr16_b64_v4i16(LDS_PTR(s16x4, p)), hi = __builtin_amdgcn_ds_read_tr16_b64_v4i16(LDS_PTR(s16x4, p + second));
  return __builtin_shufflevector(lo, hi, 0, 1, 2, 3, 4, 5, 6, 7);
}
DI u32x16 cat4(const u32x4& a, const u32x4& b, const u32x4& c, const u32x4& d) {
  const u32x8 x = __builtin_shufflevector(a, b, 0, 1, 2, 3, 4, 5, 6, 7), y = __builtin_shufflevector(c, d, 0, 1, 2, 3, 4, 5, 6, 7);
  return __builtin_shufflevector(x, y, 0, 1, 2, 3, 4, 5, 6, 7, 8, 9, 10, 11, 12, 13, 14, 15);
}

DI unsigned stats_dst(unsigned sx, int tile, int tn, int lane) { return lane < 32 ? sx + SX_ST + tile * 8192 + tn * 512 + lane * 16 : OOB; }
DI void row_stats_sum(rsrc_t rs, unsigned sx, int tile, int lane, float& S, float& Q, float& Dd) {
  const unsigned exb = sx + SX_ST + tile * 8192 + (lane & 31) * 16;
  u32x4 pv[8];
  poll_all<8>(pv, false, [&](u32x4 (&v)[8]) {
#pragma unroll
    for (int i = 0; i < 8; ++i) v[i] = ldg(rs, exb + ((lane >> 5) * 8 + i) * 512);
  }, [&](const u32x4 (&v)[8]) { return none_sent(v); });
  S = 0.f; Q = 0.f; Dd = 0.f;
#pragma unroll
  for (int i = 0; i < 8; ++i) { S += __uint_as_float(pv[i][0]); Q += __uint_as_float(pv[i][1]); Dd += __uint_as_float(pv[i][2]); }
}
DI void row_stats_fin(rsrc_t rs, unsigned kdst, char* smem, int epoch, int tile, int lane, float S, float Q, float Dd, float wsum, const u32x4 (&x1p)[4],
                      float& mean, float& var, float& mb, float& rd) {
  S = half_swap_sum(S); Q = half_swap_sum(Q); Dd = half_swap_sum(Dd);
  mean = S * (1.0f / 1024.0f); var = fmaxf(Q * (1.0f / 1024.0f) - mean * mean, 0.f);
  float den;
  ln_scalars(mean, var, mb, den);
  rd = RCP(den);
  const unsigned rb = pack2((Dd - mb * wsum) * rd, 0.f) & 0xffffu;
  const unsigned mine = (((rb & 0x8000u) ? (~rb & 0xffffu) : (rb | 0x8000u)) << 16) | (unsigned)(2047 - (tile * 32 + (lane & 31)));
  const bool best = half_max_u32(mine) == mine;
  __builtin_amdgcn_raw_buffer_store_b32(mine, rs, (best && lane < 32) ? kdst : OOB, 0, 16);
  if (best) {
    char* c = smem + L_CAND + (tile & 3) * 144;
#pragma unroll
    for (int i = 0; i < 4; ++i) *reinterpret_cast<u32x4*>(c + (2 * i + (lane >> 5)) * 16) = x1p[i];
    *reinterpret_cast<u32x4*>(c + 128) = u32x4{mine, __float_as_uint(mb), __float_as_uint(rd), 0u};
  }
  lds_set((unsigned)(uintptr_t)smem + L_W + 32 + (tile & 3) * 4, epoch);
}

DI void attn_safe(rsrc_t rs, unsigned kvoff, const u32x8& q, const u32x4& ones, f32x16& o) {
  const s16x8 q0 = __builtin_bit_cast(s16x8, __builtin_shufflevector(q, q, 0, 1, 2, 3)), q1 = __builtin_bit_cast(s16x8, __builtin_shufflevector(q, q, 4, 5, 6, 7));
  f32x4 l = {0.f, 0.f, 0.f, 0.f};
#pragma unroll
  for (int e = 0; e < 16; ++e) o[e] = 0.f;
  float m = -INFINITY;
#pragma unroll 1
  for (int kt = 0; kt < 64; ++kt) {
    const unsigned p = kvoff + kt * 4096;
    const u32x4 k0 = ldg(rs, p), k1 = ldg(rs, p + 1024), v0 = ldg(rs, p + 2048), v1 = ldg(rs, p + 3072);
    f32x16 s = {};
    s = MFMA32(__builtin_bit_cast(s16x8, k0), q0, s);
    s = MFMA32(__builtin_bit_cast(s16x8, k1), q1, s);
    float mt = fmaxf(s[0], s[1]);
#pragma unroll
    for (int e = 2; e < 16; ++e) mt = fmaxf(mt, s[e]);
    const float mn = fmaxf(m, half_swap_max(mt));
    const float alpha = EX2(m - mn);
    m = mn;
#pragma unroll
    for (int e = 0; e < 16; ++e) { s[e] = EX2(s[e] - mn); o[e] *= alpha; }
    l[0] *= alpha;
    const bf16x16 pa = __builtin_bit_cast(bf16x16, __builtin_shufflevector(s, s, 0, 1, 2, 3, 4, 5, 6, 7));
    const bf16x16 pb = __builtin_bit_cast(bf16x16, __builtin_shufflevector(s, s, 8, 9, 10, 11, 12, 13, 14, 15));
    o = __builtin_amdgcn_smfmac_f32_32x32x32_bf16(__builtin_bit_cast(bf16x8, v0), pa, o, 0xdddd, 0, 0);
    l = __builtin_amdgcn_smfmac_f32_16x16x64_bf16(__builtin_bit_cast(bf16x8, ones), pa, l, 0xdddd, 0, 0);
    o = __builtin_amdgcn_smfmac_f32_32x32x32_bf16(__builtin_bit_cast(bf16x8, v1), pb, o, 0xdddd, 0, 0);
    l = __builtin_amdgcn_smfmac_f32_16x16x64_bf16(__builtin_bit_cast(bf16x8, ones), pb, l, 0xdddd, 0, 0);
  }
  o *= 1.0f / l[0];
}

extern "C" __global__ __launch_bounds__(512, 2) void mega_kernel(const u16* __restrict__ x0, const u16* __restrict__ wqkv, const u16* __restrict__ wo,
                                                              const u16* __restrict__ w_r, const u16* __restrict__ wg, const u16* __restrict__ wu,
                                                              const u16* __restrict__ wd, char* ws, u16* __restrict__ out) {
  __shared__ __attribute__((aligned(16))) char smem[LDS_BYTES];
  const int wid = __builtin_amdgcn_readfirstlane(threadIdx.x >> 6);
  STAMP_INIT;
  STAMPV(0);
  const int bid = blockIdx.x, xcd = bid & 7, loc = bid >> 3;
  const int tm = 8 * (xcd >> 2) + (loc >> 2), tn = 4 * (xcd & 3) + (loc & 3);
  const unsigned lbase = (unsigned)(uintptr_t)smem;
  const rsrc_t rs = __builtin_amdgcn_make_buffer_rsrc(ws, 0, (int)WS_BYTES, 0x00020000);
  volatile __attribute__((address_space(3))) unsigned* words = LDS_PTR(volatile unsigned, smem + L_W);
  u32x4 v[4];
  if (wid < 4)
    asm volatile("global_load_dwordx4 %0, %4, %5\n\tglobal_load_dwordx4 %1, %4, %5 offset:512\n\tglobal_load_dwordx4 %2, %4, %5 offset:1024\n\tglobal_load_dwordx4 %3, %4, %5 offset:1536"
                 : "=&v"(v[0]), "=&v"(v[1]), "=&v"(v[2]), "=&v"(v[3])
                 : "v"((threadIdx.x >> 5) * (D_ * 2) + (threadIdx.x & 31) * 16), "s"(x0 + (size_t)(tm * 128 + tn * 8) * D_) : "memory");
  else {
    asm volatile("" : "=v"(v[0]), "=v"(v[1]), "=v"(v[2]), "=v"(v[3]));
    if (wid == 6) {
      const char *c0, *c1;
      CODE_ADDR(mega_kernel, c0); CODE_ADDR(end_marker, c1);
      const unsigned o = (loc * 64 + (threadIdx.x & 63)) * 32;
      glds4(c0, (long long)o + 4 <= c1 - c0 ? o : 0u, lbase + NS * SLOTB);
    }
  }
  STAMPV(48);
  *reinterpret_cast<unsigned*>(smem + L_FULL + threadIdx.x * 4) = 0u;
  words[threadIdx.x & 31] = 0u;
  BAR_LGKM();
  if (wid < 4) {
    const int tid = threadIdx.x, lane = tid & 63, r32 = lane & 31, row = 2 * wid + (lane >> 5);
    char* sm = smem + L_XCH;
    unsigned dst = OFF_HF + (tm * 4 + (tn >> 2)) * TS + (tn & 3) * 128 + (tid >> 3) * 512 + (tid & 7) * 16;
    asm volatile("s_waitcnt vmcnt(0)" : "+v"(v[0]), "+v"(v[1]), "+v"(v[2]), "+v"(v[3]), "+v"(dst) :: "memory");
    STAMPV(49);
    float s = 0.f, s2 = 0.f;
#pragma unroll
    for (int i = 0; i < 4; ++i)
#pragma unroll
      for (int e = 0; e < 4; e += 2) { s = dot2(v[i][e], 0x3f803f80u, s); s2 = dot2(v[i][e + 1], 0x3f803f80u, s2); }
    const float mean = half_sum(s + s2) * (1.0f / 1024.0f);
    f32x2 m2 = {0.f, 0.f};
#pragma unroll
    for (int i = 0; i < 4; ++i)
#pragma unroll
      for (int e = 0; e < 4; ++e) { const f32x2 d = f32x2{lo16(v[i][e]), hi16(v[i][e])} - mean; m2 += d * d; }
    float mb, den;
    ln_scalars(mean, half_sum(m2[0] + m2[1]) * (1.0f / 1024.0f), mb, den);
    const float rd = 1.0f / den;
#pragma unroll
    for (int i = 0; i < 4; ++i) {
      char* p = sm + ((16 * i + (r32 >> 1)) * 2) * 144 + row * 16 + (r32 & 1) * 8;
      *reinterpret_cast<u32x2*>(p) = u32x2{ln_pair(v[i][0], mb, rd), ln_pair(v[i][1], mb, rd)};
      *reinterpret_cast<u32x2*>(p + 144) = u32x2{ln_pair(v[i][2], mb, rd), ln_pair(v[i][3], mb, rd)};
    }
    STAMPV(37);
    lds_set(lbase + L_W + 64 + 4 * wid, 1);
    lds_wait_ge<0>(lbase + L_W + 64 + 4 * (lane & 3), 1);
    STAMPV(50);
    u32x4 t[4];
#pragma unroll
    for (int i = 0; i < 4; ++i) t[i] = *reinterpret_cast<const u32x4*>(sm + (i * 32 + (tid >> 3)) * 144 + (tid & 7) * 16);
    SBAR();
#pragma unroll
    for (int i = 0; i < 4; ++i) stg(rs, dst + i * 16384, t[i]);
    STAMPV(51);
    DRAIN();
    lds_set(lbase + L_W + 64 + 4 * wid, 2);
    STAMPV(38);
  }
  const int lw = wid & 1;
  const char* wb = reinterpret_cast<const char*>(wqkv + (size_t)(16 * lw) * (3 * D_) + tn * 64);
  auto stage_lanes = [](unsigned (&wv)[3]) {
    const int lane = fresh_lane();
#pragma unroll
    for (int i = 0; i < 3; ++i) {
      const int li = lane + 64 * i, k = li / 24, cp = li % 24, c = cp ^ (4 * ((k >> 1) & 1));
      wv[i] = (unsigned)(k * (3 * D_ * 2) + (c >> 3) * (D_ * 2) + (c & 7) * 16 - i * 1024);
    }
  };
  auto stage = [&](const unsigned (&wv)[3], int s, int slot) {
    const char* p = wb + (size_t)(2 * kpos(tm, tn, s >> 1) + (s & 1)) * (32 * 3 * D_ * 2);
    glds_v3(p, wv, lbase + slot * SLOTB + lw * 6144);
    glds_v3(p + 8 * (3 * D_ * 2), wv, lbase + slot * SLOTB + lw * 6144 + 3072);
  };
  auto exchange = [&](const u32x2& tag, unsigned line0, int me, bool bar = false) {
    const int lane = fresh_lane();
    __builtin_amdgcn_raw_buffer_store_b64(tag, rs, lane == 0 ? line0 + me * 128 : OOB, 0, 16);
    if (bar) BARRIER();
    JIT();
    unsigned po = lane < 16 ? line0 + lane * 128 : OOB;
    const u32x4 srd = {(unsigned)(uintptr_t)ws, (unsigned)((uintptr_t)ws >> 32) & 0xffffu, WS_BYTES, 0x00020000u};
    for (unsigned spins = 0;; ++spins) {
      u32x2 v = {0u, 0u};
      asm volatile("buffer_atomic_or_x2 %0, %1, %2, 0 offen sc0 sc1\n\ts_waitcnt vmcnt(0)" : "+v"(v) : "v"(po), "s"(srd) : "memory");
      po = ((v[0] == tag[0]) & (v[1] == tag[1])) ? OOB : po;
      if (__all(po == OOB) || spins > SPIN_CAP) break;
    }
  };
  if ((wid >> 1) == 2) {
    unsigned wv[3];
    stage_lanes(wv);
    const int r = tm & 7;
#pragma unroll 1
    for (int i = 0; i < NS; ++i) {
      const int s = i == 0 ? r : i <= r ? i - 1 : i;
      stage(wv, s, s);
      const int j = i - 1, full = j < 1 ? (j == 0 && r == 0) : j < r ? j : j + 1;
      asm volatile("s_waitcnt vmcnt(%2)\n\tds_write_b32 %0, %1" :: "v"(lbase + L_FULL + lw * 256), "v"(full), "n"(6 * 1) : "memory");
    }
    asm volatile("s_waitcnt vmcnt(0)\n\tds_write_b32 %0, %1" :: "v"(lbase + L_FULL + lw * 256), "v"(NS) : "memory");
    STAMPV(1);
  }
  if (wid == 7) {
    u32x2 tag = __builtin_amdgcn_raw_buffer_load_b64(rs, FL_EPOCH, 0, 16);
    tag[0] += 1u; tag[1] += 0x9E3779B9u;
    *reinterpret_cast<u32x2*>(smem + L_TAG) = tag;
    lds_wait_ge<0>(lbase + L_W + 64 + 4 * (fresh_lane() & 3), 2);
    exchange(tag, FL_X + tm * 2048, tn);
    lds_set(lbase + L_W + 24, 1);
  }
  if (wid >= 4) {
    auto wo_rows = [&](int g0, int n, int gs) {
      const int lane = fresh_lane();
      unsigned wov[4];
#pragma unroll
      for (int i = 0; i < 4; ++i) wov[i] = (unsigned)((8 * i + (lane >> 3)) * (D_ * 2) + ((lane & 7) ^ (4 * ((lane >> 4) & 1))) * 16 - i * 1024);
#pragma unroll 1
      for (int i = 0, k = (0 * (tm & 7)) % n; i < n; ++i, k = k + 1 == n ? 0 : k + 1) {
        const int g = g0 + gs * k;
        glds_v4t<3, 1>(reinterpret_cast<const char*>(wo) + (size_t)((((tn >> 2) + 1) * 256 + 32 * g) & 1023) * (D_ * 2) + tn * 128, wov, lbase + g * 4096);
      }
    };
    if (wid < 6) {
      const int lane = fresh_lane();
      unsigned wv[3];
      stage_lanes(wv);
      __builtin_amdgcn_s_setprio(3);
      const unsigned fra = lbase + L_FREE + (lane & 3) * 256, fla = lbase + L_FULL + lw * 256 + lane * 4;
      u32x4 pre[NS * 6];
#pragma unroll
      for (int i = 0; i < NS * 6; ++i) pre[i] = *reinterpret_cast<const u32x4*>(smem + lw * 6144 + lane * 16 + (i / 6) * SLOTB + (i % 6) * 1024);
#pragma unroll
      for (int i = 0; i < NS * 6; ++i) PIN(pre[i]);
#pragma unroll 1
      for (int L = 0; L < 2; ++L) {
        const int s0 = NS, lead = L ? 8 : NS - 1;
        int slot = 0;
#pragma unroll 1
        for (int s = NS; s < 32; ++s) {
          lds_wait_ge(fra, 32 * L + s - lead);
          stage(wv, s, slot);
          JIT();
          slot = (slot == NS - 1) ? 0 : slot + 1;
          if (s >= s0 + DD) asm volatile("s_waitcnt vmcnt(%0)\n\tds_write_b32 %1, %2" :: "n"(DD * 6), "v"(fla), "v"(32 * L + s - DD + 1) : "memory");
        }
#pragma unroll
        for (int d = DD - 1; d >= 0; --d) asm volatile("s_waitcnt vmcnt(%0)\n\tds_write_b32 %1, %2" :: "n"(d * 6), "v"(fla), "v"(32 * L + 32 - d) : "memory");
        u32x2 tag = *reinterpret_cast<const u32x2*>(smem + L_TAG);
        PIN(tag);
        if (lw == 0) {
          lds_wait_ge(lbase + L_W + 96 + 4 * (lane & 1), 1);
          lds_wait_ge<0>(lbase + L_W + 80 + 4 * (lane & 3), 2 * L + 1);
          exchange(tag, FL_KV + (L * 16 + tn) * 2048, tm, true);
          lds_wait_ge(fra, 32 * L + 32);
          lds_set(lbase + L_W + 16, L + 1);
        } else { BAR_LGKM(); lds_wait_ge<8>(lbase + L_W + 16, L + 1); }
        __builtin_amdgcn_s_sleep(24);
        wo_rows(lw, 15, 2);
        STAMPV(7 + 16 * L);
        DRAIN();
        const unsigned fo = FL_O + (L * 16 + tm) * 2048;
        if (lw) {
          lds_set(lbase + L_W + 112, 2 * L + 1);
          lds_wait_ge<4>(lbase + L_W + 48 + 4 * (lane & 3), L + 1);
          lds_wait_ge<0>(lbase + L_W + 80 + 4 * (lane & 3), 2 * L + 2);
          __builtin_amdgcn_raw_buffer_store_b64(tag, rs, lane == 0 ? fo + tn * 128 : OOB, 0, 16);
          JIT();
          wo_rows(30, 2, 1);
          DRAIN();
          lds_set(lbase + L_W + 112, 2 * L + 2);
        } else {
          lds_wait_ge<4>(lbase + L_W + 112, 2 * L + 1);
          lds_wait_ge<4>(lbase + L_W + 48 + 4 * (lane & 3), L + 1);
          unsigned po = lane < 16 ? fo + ((lane + 4 + (tn & 12)) & 15) * 128 : OOB;
          const u32x4 srd = {(unsigned)(uintptr_t)ws, (unsigned)((uintptr_t)ws >> 32) & 0xffffu, WS_BYTES, 0x00020000u};
          for (unsigned spins = 0;; ++spins) {
            u32x2 v = {0u, 0u};
            asm volatile("buffer_atomic_or_x2 %0, %1, %2, 0 offen sc0 sc1\n\ts_waitcnt vmcnt(0)" : "+v"(v) : "v"(po), "s"(srd) : "memory");
            po = ((v[0] == tag[0]) & (v[1] == tag[1])) ? OOB : po;
            unsigned m = (unsigned)__ballot(po == OOB);
            m &= m >> 1; m &= m >> 2;
            m = (m & 1u) | ((m >> 3) & 2u) | ((m >> 6) & 4u) | ((m >> 9) & 8u);
            if (m == 15u || spins > SPIN_CAP) break;
            words[5 + 2 * L] = words[28] == 2u * L + 2u ? m : m & 7u;
          }
          words[5 + 2 * L] = words[28] == 2u * L + 2u ? 15u : 7u;
          lds_wait_ge<0>(lbase + L_W + 112, 2 * L + 2);
          words[5 + 2 * L] = 15u;
          __builtin_amdgcn_raw_buffer_store_b64(tag, rs, (bid == 0 && lane == 0) ? FL_EPOCH : OOB, 0, 16);
        }
        BAR_ALL();
        if (L == 0) {
          char* d = smem + lw * 6144 + fresh_lane() * 16;
#pragma unroll
          for (int s = 0; s < NS; ++s) {
#pragma unroll
            for (int i = 0; i < 6; ++i) *reinterpret_cast<u32x4*>(d + s * SLOTB + i * 1024) = pre[s * 6 + i];
            lds_set(fla, 32 + s + 1);
          }
          STAMPV(2);
          lds_wait_ge<2>(lbase + L_W + 24, 2);
        }
      }
      STAMP_END;
      return;
    }
    const int a = wid - 6;
    const bool ffn = loc < 28;
    const int grp = xcd * 7 + (ffn ? loc >> 2 : 0), qa = (loc & 3) * 2 + a;
    const int kr = loc, cg = xcd;
    const unsigned wa = lbase + L_W;
    lds_wait_ge<2>(wa + 24, 1);
    __builtin_amdgcn_s_sleep(16);
    auto transpose8 = [](u32x4* v) {
      u32x4 t[8];
#pragma unroll
      for (int j = 0; j < 8; ++j)
#pragma unroll
        for (int e = 0; e < 4; ++e) t[j][e] = (j & 1) ? PERM_HI(v[2 * e][j >> 1], v[2 * e + 1][j >> 1]) : PERM_LO(v[2 * e][j >> 1], v[2 * e + 1][j >> 1]);
#pragma unroll
      for (int j = 0; j < 8; ++j) { v[j] = t[j]; PIN(v[j]); }
    };
    u32x4 gf[32];
    u32x4 wdf[16];
    auto wait_go = [&](int L) {
      __builtin_amdgcn_s_setprio(1);
      lds_wait_ge<8>(lbase + L_W + 16, L + 1);
      __builtin_amdgcn_s_sleep(24);
    };
    auto load_wd = [&](unsigned dv) {
      const rsrc_t rd = __builtin_amdgcn_make_buffer_rsrc(const_cast<u16*>(wd) + (size_t)grp * 64 * D_ + 128 * qa, 0, 64 * D_ * 2 - 256 * qa, 0x00020000);
#pragma unroll
      for (int i = 0; i < 16; ++i) {
        wdf[i] = __builtin_amdgcn_raw_buffer_load_b128(rd, dv, (32 * (i >> 3) + (i & 7)) * (D_ * 2), 0);
        asm volatile("s_sleep 2\n\ts_waitcnt vmcnt(2)" ::: "memory");
      }
    };
    {
      LANE_VARS;
      if (a == 0) {
        const u32x4 w0 = *reinterpret_cast<const u32x4*>(w_r + lane * 8), w1 = *reinterpret_cast<const u32x4*>(w_r + 512 + lane * 8);
        float t = 0.f;
#pragma unroll
        for (int e = 0; e < 4; ++e) t = fadd(fadd(t, fadd(lo16(w0[e]), hi16(w0[e]))), fadd(lo16(w1[e]), hi16(w1[e])));
        words[0] = __float_as_uint(wave_sum(t));
        if ((lane >> 3) == (tn & 7)) *reinterpret_cast<u32x4*>(smem + L_WR + (lane & 7) * 16) = tn < 8 ? w0 : w1;
      }
      const u16* p = wqkv + (size_t)(64 * tm + 32 * a + hi) * (3 * D_) + (c24 >> 3) * D_ + tn * 64 + (c24 & 7) * 8;
      float cs[8] = {0.f, 0.f, 0.f, 0.f, 0.f, 0.f, 0.f, 0.f};
      u32x4 v[16];
#pragma unroll
      for (int i = 0; i < 16; ++i) v[i] = *reinterpret_cast<const u32x4*>(p + (size_t)i * (2 * 3 * D_));
      SBAR();
      const u32x4 sv = {SENT, SENT, SENT, SENT};
#pragma unroll
      for (int i = 0; i < 6; ++i) {
        const unsigned o = ((i * 256 + bid) * 128 + a * 64 + lane) * 16;
        stg(rs, o < SX_BYTES ? OFF_SX + o : OOB, sv);
      }

#pragma unroll
      for (int i = 0; i < 16; ++i)
#pragma unroll
        for (int e = 0; e < 4; ++e) { cs[2 * e] = fadd(cs[2 * e], lo16(v[i][e])); cs[2 * e + 1] = fadd(cs[2 * e + 1], hi16(v[i][e])); }
      u32x4 o;
#pragma unroll
      for (int e = 0; e < 4; ++e) { const float x = half_swap_sum(cs[e]), y = half_swap_sum(cs[4 + e]); o[e] = __float_as_uint(hi ? y : x); }
      stg(rs, r32 < 24 ? OFF_CS + (tn * 32 + tm * 2 + a) * 768 + r32 * 32 + hi * 16 : OOB, o);
    }
    {
      DRAIN();
      lds_set(wa + 96 + 4 * a, 1);
      BAR_LGKM();
      LANE_VARS;
      const char* gp = reinterpret_cast<const char*>(((lane & 8) ? wu : wg) + (size_t)(128 * qa + 16 * hi + 4 * tblk) * F_ + 64 * grp + (lane & 7) * 8);
      unsigned dv = (lane >> 4) * (8 * D_ * 2) + (lane & 15) * 16;
      PIN(gp); PIN(dv);
      unsigned sv[3];
#pragma unroll
      for (int i = 0; i < 3; ++i) { const int li = lane + 64 * i, k = li / 24, c = li % 24; sv[i] = (unsigned)(k * (3 * D_ * 2) + (c >> 3) * (D_ * 2) + (c & 7) * 16 - i * 1024); PIN(sv[i]); }
      wait_go(0);
#pragma unroll
      for (int i = 0; i < 32; ++i) {
        gf[i] = *reinterpret_cast<const u32x4*>(gp + (size_t)(32 * (i >> 3) + 8 * ((i >> 2) & 1) + (i & 3)) * (F_ * 2));
        asm volatile("s_sleep 2\n\ts_waitcnt vmcnt(2)" ::: "memory");
      }
      load_wd(dv);
      STAMPV(7);
      if (1) __builtin_amdgcn_s_setprio(0);
      lds_wait_ge<4>(wa + 80 + 4 * (lane & 3), 2);
      __builtin_amdgcn_s_setprio(0);
#pragma unroll
      for (int g = 0; g < 4; ++g) glds_v3(wqkv + (size_t)(32 * kr + 8 * g) * (3 * D_) + (2 * cg + a) * 64, sv, lbase + L_SL + a * 12288 + g * 3072);
      transpose8(gf); transpose8(gf + 8); transpose8(gf + 16); transpose8(gf + 24);
      transpose8(wdf); transpose8(wdf + 8);
      {
        LANE_VARS;
        f32x4 t[2];
#pragma unroll
        for (int c = 0; c < 2; ++c) {
          u32x4 v[8];
#pragma unroll
          for (int i = 0; i < 8; ++i) v[i] = ldg(rs, OFF_CS + (tn * 32 + 16 * c + 2 * i + hi) * 768 + a * 384 + c24 * 16);
          SBAR();
          t[c] = sum4<8>(v);
          SBAR();
        }
#pragma unroll
        for (int e = 0; e < 4; ++e) t[0][e] = half_swap_sum(fadd(t[0][e], t[1][e]));
        if (lane < 24) *reinterpret_cast<f32x4*>(smem + L_CS + a * 384 + lane * 16) = t[0];
      }
      u32x2 tag = *reinterpret_cast<const u32x2*>(smem + L_TAG);
      PIN(tag);
      BAR_ALL();
      if (a) {
        lds_wait_ge<0>(wa + 64 + 4 * (lane & 3), 3);
        exchange(tag, FL_X + (16 + tm) * 2048, tn);
        lds_set(wa + 24, 2);
        STAMPV(12);
      }
    }
#pragma unroll 1
    for (int L = fresh_s(0);; ++L) {
      const unsigned sx = OFF_SX + L * SX_LAYER;
      STAMPV(1 + 16 * L);
      u32x4 hf[4];
      unsigned key;
      { LANE_VARS;
      lds_wait_ge<0>(wa + 32 + 4 * tp, L + 1);
      if (a == 0) {
        const char* cp = smem + L_CAND + (r32 >> 3) * 144;
        const u32x4 c = *reinterpret_cast<const u32x4*>(cp + (lane & 7) * 16), rec = *reinterpret_cast<const u32x4*>(cp + 128);
        u32x4 o;
#pragma unroll
        for (int e = 0; e < 4; ++e) { unsigned h = ln_pair_s(c[e], -U2F(rec[1]), U2F(rec[2])); PIN(h); o[e] = nz(hi ? c[e] : h); }
        stg(rs, sx + SX_ROW + hi * 131072 + (tm * 4 + (r32 >> 3)) * 2048 + tn * 128 + (lane & 7) * 16, o);
        STAMPV(8 + 16 * L);
        lds_wait_ge<0>(wa + 104, L + 1);
        key = __builtin_amdgcn_readfirstlane(words[27]);
      } else {
        const u32x4 srd = {(unsigned)(uintptr_t)ws, (unsigned)((uintptr_t)ws >> 32) & 0xffffu, WS_BYTES, 0x00020000u};
        unsigned po = sx + SX_CAND + (tn * 16 + (lane >> 2)) * 128 + tp * 4;
        key = 0u;
        for (unsigned spins = 0;; ++spins) {
          unsigned v = 0u;
          asm volatile("buffer_atomic_or %0, %1, %2, 0 offen sc0 sc1\n\ts_waitcnt vmcnt(0)" : "+v"(v) : "v"(po), "s"(srd) : "memory");
          key = v == SENT ? key : max(key, v);
          po = v == SENT ? po : OOB;
          if (__all(po == OOB) || spins > SPIN_CAP) break;
        }
        key = __builtin_amdgcn_readfirstlane(wave_max_u32(key));
        words[27] = key;
        words[3] = 2047u - (key & 2047u);
        lds_set(wa + 104, L + 1);
        STAMPV(13 + 16 * L);
      }
      poll_all<4>(hf, false, [&](u32x4 (&t)[4]) {
#pragma unroll
        for (int i = 0; i < 4; ++i) t[i] = ldg(rs, sx + SX_ROW + ((2047 - (int)(key & 2047u)) >> 5) * 2048 + (16 * qa + 4 * i + (lane >> 4)) * 16);
      }, [&](const u32x4 (&t)[4]) { return none_sent(t); });
      if (L == 1) lds_set(wa + 4 + 4 * a, 2);
      }
      STAMPV(2 + 16 * L);
      f32x4 d[8];
      const f32x4 zero = {0.f, 0.f, 0.f, 0.f};
      SBAR();
      __builtin_amdgcn_s_setprio(3);
#pragma unroll
      for (int t = 0; t < 32; ++t) d[t & 7] = MFMA16(__builtin_bit_cast(s16x8, hf[t >> 3]), __builtin_bit_cast(s16x8, gf[t]), t < 8 ? zero : d[t & 7]);
      SBAR();
      __builtin_amdgcn_s_setprio(0);
      LANE_VARS;
      const int idx = 2047 - (int)(key & 2047u), win = idx >> 7;
      const bool owner = win == tm;
      auto pick2 = [&](const f32x4 (&d)[8]) {
        u32x2 o;
#pragma unroll
        for (int e = 0; e < 2; ++e) {
          const float x = tblk ? d[2 + e][0] : d[e][0], y = tblk ? d[6 + e][0] : d[4 + e][0];
          o[e] = nzf(hi ? y : x);
        }
        return o;
      };
      if (ffn) {
        const unsigned ppos = (lane & 15) * 32 + (lane >> 4) * 8;
        __builtin_amdgcn_raw_buffer_store_b64(pick2(d), rs, sx + SX_GP + (grp * 8 + qa) * 512 + ppos, 0, 16);
        STAMPV(9 + 16 * L);
        __builtin_amdgcn_s_sleep(16);
        u32x2 gu[8];
        for (unsigned spins = 0;; ++spins) {
#pragma unroll
          for (int i = 0; i < 8; ++i) gu[i] = __builtin_amdgcn_raw_buffer_load_b64(rs, sx + SX_GP + (grp * 8 + i) * 512 + lane * 8, 0, 16);
          SBAR();
          unsigned m = 0u;
#pragma unroll
          for (int i = 0; i < 8; ++i) m = max(max(m, gu[i][0]), gu[i][1]);
          if (__all(m != SENT) || spins > SPIN_CAP) break;
          poll_pause(L == 0);
        }
        STAMPV(10 + 16 * L);
        float t0 = opq(U2F(gu[0][0])), t1 = opq(U2F(gu[0][1]));
#pragma unroll
        for (int i = 1; i < 8; ++i) { t0 = fadd(t0, U2F(gu[i][0])); t1 = fadd(t1, U2F(gu[i][1])); }
        auto sw = __builtin_amdgcn_permlane32_swap(__float_as_uint(t0), __float_as_uint(t1), false, false);
        const float gv = rbf(U2F(sw[0])), uv = rbf(U2F(sw[1]));
        reinterpret_cast<u16*>(smem + L_GU + a * 128)[2 * r32 + hi] = (u16)pack2(fmul(rbf(fmul(gv, RCP(fadd(1.0f, __expf(-gv))))), uv), 0.f);
        asm volatile("s_waitcnt lgkmcnt(0)" ::: "memory");
        const s16x8 a0 = *reinterpret_cast<const s16x8*>(smem + L_GU + a * 128 + (lane >> 4) * 16), a1 = *reinterpret_cast<const s16x8*>(smem + L_GU + a * 128 + 64 + (lane >> 4) * 16);
        SBAR();
        __builtin_amdgcn_s_setprio(3);
#pragma unroll
        for (int t = 0; t < 16; ++t) d[t & 7] = MFMA16(t < 8 ? a0 : a1, __builtin_bit_cast(s16x8, wdf[t]), t < 8 ? zero : d[t & 7]);
        SBAR();
        __builtin_amdgcn_s_setprio(0);
        __builtin_amdgcn_raw_buffer_store_b64(pick2(d), rs, sx + SX_YP + grp * 4096 + qa * 512 + ppos, 0, 16);
      }
      STAMPV(3 + 16 * L);
      if (L == 1 && (cg != (kr & 7) || a)) { STAMP_END; __builtin_amdgcn_endpgm(); }
      s16x8 wpf[12];
#pragma unroll
      for (int t = 0; t < 12; ++t) wpf[t] = tr2(smem + L_SL + a * 12288 + (16 * hi + 4 * tblk + tq) * 384 + t * 32 + tp * 8, 8 * 384);
#pragma unroll
      for (int t = 0; t < 12; ++t) PIN(wpf[t]);
      if (L == 0 && !owner) lds_set(wa + 4 + 4 * a, 1);
      __builtin_amdgcn_s_sleep(9);
      if (L == 0 && !ffn) __builtin_amdgcn_s_sleep(64);
      const unsigned rk = key >> 16;
      float rsig = rbf(RCP(fadd(1.0f, __expf(-lo16((rk & 0x8000u) ? (rk & 0x7fffu) : (~rk & 0xffffu))))));
      PIN(rsig);
      const int n = 4 * (lane & 7) + 2 * tblk + hi;
      const int fo = ((n >> 4) * 2 + ((n >> 2) & 1)) * 8 + ((n >> 3) & 1) * 4 + (n & 3);
      unsigned odst = (L == 1 && !(lane & 8)) ? (idx * D_ + kr * 32 + n) * 2 : OOB;
      PIN(odst);
      u32x4 pv[8];
      poll_all<8>(pv, L == 0, [&](u32x4 (&t)[8]) {
#pragma unroll
        for (int i = 0; i < 7; ++i) t[i] = ldg(rs, sx + SX_YP + (8 * i + (lane >> 3)) * 4096 + kr * 128 + (lane & 7) * 16);
        t[7][0] = t[7][2] = __builtin_amdgcn_raw_buffer_load_b32(rs, sx + SX_ROW + (idx >> 5) * 2048 + kr * 64 + (fo >> 1) * 4, 0, 16);
        t[7][1] = t[7][3] = __builtin_amdgcn_raw_buffer_load_b32(rs, sx + SX_ROW + 131072 + (idx >> 5) * 2048 + kr * 64 + (fo >> 1) * 4, 0, 16);
      }, [&](const u32x4 (&t)[8]) { return none_sent(t); });
      const float hv = (fo & 1) ? hi16(pv[7][0]) : lo16(pv[7][0]), xv = (fo & 1) ? hi16(pv[7][1]) : lo16(pv[7][1]);
      STAMPV(4 + 16 * L);
      float ys;
      {
        const f32x4 t = sum4<7>(pv);
        auto p = __builtin_amdgcn_permlane32_swap(__float_as_uint(t[0]), __float_as_uint(t[1]), false, false);
        auto q = __builtin_amdgcn_permlane32_swap(__float_as_uint(t[2]), __float_as_uint(t[3]), false, false);
        auto r = __builtin_amdgcn_permlane16_swap(__float_as_uint(fadd(U2F(p[0]), U2F(p[1]))), __float_as_uint(fadd(U2F(q[0]), U2F(q[1]))), false, false);
        ys = fadd(U2F(r[0]), U2F(r[1]));
        ys = fadd(ys, U2F(dpp<0x128>(__float_as_uint(ys))));
      }
      const unsigned ob = pack2(fadd(xv, rbf(fadd(hv, rbf(fmul(rbf(ys), rsig))))), 0.f) & 0xffffu;
      __builtin_amdgcn_raw_buffer_store_b16((u16)ob, __builtin_amdgcn_make_buffer_rsrc(out, 0, 2048 * D_ * 2, 0x00020000), odst, 0, 0);
      if (L == 1) {
        STAMPV(5 + 16 * L);
        STAMP_END;
        __builtin_amdgcn_endpgm();
      }
      reinterpret_cast<u16*>(smem + L_X2F + a * 64)[fo] = (u16)ob;
      asm volatile("s_waitcnt lgkmcnt(0)" ::: "memory");
      {
        const s16x8 xa = *reinterpret_cast<const s16x8*>(smem + L_X2F + a * 64 + (lane >> 4) * 16);
        const u32x4 xf = *reinterpret_cast<const u32x4*>(smem + L_X2F + a * 64 + tp * 16);
        f32x4 z[12];
        SBAR();
        __builtin_amdgcn_s_setprio(3);
#pragma unroll
        for (int t = 0; t < 12; ++t) z[t] = MFMA16(xa, wpf[t], zero);
        SBAR();
        __builtin_amdgcn_s_setprio(0);
        const float x2 = lo16(ob), s1 = fmul(wave_sum(x2), 0.5f), s2 = fmul(wave_sum(fmul(x2, x2)), 0.5f);
        u32x4 o;
#pragma unroll
        for (int e = 0; e < 4; ++e) {
          const unsigned sp = lane < 52 ? (e == 0 ? __float_as_uint(s1) : __float_as_uint(s2)) : xf[e];
          const unsigned x = __float_as_uint(tblk ? z[4 + e][0] : z[e][0]), y = tblk ? sp : __float_as_uint(z[8 + e][0]);
          o[e] = nz(hi ? y : x);
        }
        const unsigned pos = lane < 24 ? lane * 16 : lane < 48 ? lane * 16 + 128 : lane < 52 ? (lane & 1) * 512 + 384 : 400 + tp * 16;
        stg(rs, lane < 56 ? OFF_SX + SX_QP + ((2 * cg + a) * 32 + kr) * 1024 + pos : OOB, o);
      }
      STAMPV(5 + 16 * L);
      if (owner) {
        __builtin_amdgcn_s_sleep(13);
        const int c25 = r32 < 25 ? r32 : 24, pl = 24 * a + c24, col = 64 * (pl >> 4) + (pl & 15);
        const int pfo = (col & ~15) + ((col >> 2) & 1) * 8 + ((col >> 3) & 1) * 4 + (col & 3);
        float cs[4];
#pragma unroll
        for (int e = 0; e < 4; ++e) { cs[e] = reinterpret_cast<const float*>(smem + L_CS)[col + 16 * e]; PIN(cs[e]); }
        u32x4 qv[17];
        poll_all<17>(qv, true, [&](u32x4 (&t)[17]) {
#pragma unroll
          for (int i = 0; i < 16; ++i) t[i] = ldg(rs, OFF_SX + SX_QP + (tn * 32 + 2 * i + hi) * 1024 + a * 512 + c25 * 16);
          t[16] = ldg(rs, OFF_SX + SX_QP + (tn * 34 + hi) * 1024 + 400 + tp * 16);
        }, [&](const u32x4 (&t)[17]) { return none_sent(t); });
        *reinterpret_cast<u32x4*>(smem + L_X2P + hi * 64 + tp * 16) = qv[16];
        f32x4 t = sum4<16>(qv);
#pragma unroll
        for (int e = 0; e < 4; ++e) t[e] = half_swap_sum(t[e]);
        const float mean = fmul(U2F(__builtin_amdgcn_readlane(__float_as_uint(t[0]), 24)), 1.0f / 1024.0f);
        const float var = fmaxf(ffma(-mean, mean, fmul(U2F(__builtin_amdgcn_readlane(__float_as_uint(t[1]), 24)), 1.0f / 1024.0f)), 0.f);
        float mb, den;
        ln_scalars(mean, var, mb, den);
        const float rd = RCP(den);
        if (lane < 24) {
#pragma unroll
          for (int e = 0; e < 4; ++e) reinterpret_cast<u16*>(smem + L_PATCH)[pfo + 16 * e] = (u16)pack2(fmul(ffma(-mb, cs[e], t[e]), pl < 16 ? fmul(rd, QS) : rd), 0.f);
        }
        lds_set(wa + 4 + 4 * a, L + 1);
      }
      STAMPV(6 + 16 * L);
      BAR_ALL();
      unsigned dv = fresh_lane();
      dv = (dv >> 4) * (8 * D_ * 2) + (dv & 15) * 16;
      PIN(dv);
      wait_go(1);
      load_wd(dv);
      STAMPV(23);
      if (1) __builtin_amdgcn_s_setprio(0);
      lds_wait_ge<4>(wa + 80 + 4 * (fresh_lane() & 3), 4);
      __builtin_amdgcn_s_setprio(0);
      transpose8(wdf); transpose8(wdf + 8);
      BARRIER();
    }
  }
  const int tm_ = tm, tn_ = tn, wid_ = wid;
#define KSKIP "{s49}"(tn * 4096), "{s50}"(((tn + 1) & 15) * 4096)
  const u32x4 srd = {(unsigned)(uintptr_t)ws, (unsigned)((uintptr_t)ws >> 32) & 0xffffu, WS_BYTES, 0x00020000u};
  u32x4 x1p[4];
  asm volatile("" : "=v"(x1p[0]), "=v"(x1p[1]), "=v"(x1p[2]), "=v"(x1p[3]));
#pragma unroll 1
  for (int L = 0; L < 2; ++L) {
    const unsigned sx = OFF_SX + L * SX_LAYER;
    u32x8 q[4];
    u32x16 kva, kvb;
    f32x16 oz[5];
    {
      f32x16 acc[6];
      u32x16 stA, stB, xr0, xr1, xr2, xr3;
      u32x4 stC;
      asm volatile("" : "={v[112:127]}"(stB), "={v[128:131]}"(stC));
      { PHASE_VARS;
      stB[8] = lane * 16 + (L ? OFF_X1 : OFF_HF) + tile * TS;
      stB[9] = lbase + wrow * 384 + (((4 * hh + wch) ^ wsw) << 4) + 8 * (tp & 1);
      stB[10] = lbase + wrow * 384 + (((4 * (hh ^ 1) + wch) ^ wsw) << 4) + 8 * (tp & 1);
      stB[13] = lbase + L_FULL + (lane & 1) * 256;
      stB[15] = lbase + L_FREE + wid * 256 + lane * 4;
      stC[0] = 32 * L; }
      unsigned soff, slot, need = 32 * L + 1;
      if (L == 0) {
        lds_wait_ge<0>(lbase + L_W + 24, 1);
        STAMPV(39);
        soff = tn * 4096;
        asm volatile(
          "buffer_load_dwordx4 v[132:135], v120, s[40:43], s44 offen offset:0 sc1\n\t"
          "buffer_load_dwordx4 v[136:139], v120, s[40:43], s44 offen offset:1024 sc1\n\t"
          "buffer_load_dwordx4 v[140:143], v120, s[40:43], s44 offen offset:2048 sc1\n\t"
          "buffer_load_dwordx4 v[144:147], v120, s[40:43], s44 offen offset:3072 sc1\n\t"
          "s_add_u32 s44, s51, 0\n\t"
          "buffer_load_dwordx4 v[148:151], v120, s[40:43], s44 offen offset:0 sc1\n\t"
          "buffer_load_dwordx4 v[152:155], v120, s[40:43], s44 offen offset:1024 sc1\n\t"
          "buffer_load_dwordx4 v[156:159], v120, s[40:43], s44 offen offset:2048 sc1\n\t"
          "buffer_load_dwordx4 v[160:163], v120, s[40:43], s44 offen offset:3072 sc1\n\t"
          "s_add_u32 s44, s44, 0x1000\n\t"
          "s_and_b32 s44, s44, 0xffff\n\t"
          "s_cmp_eq_u32 s44, s49\n\t"
          "s_cselect_b32 s44, s50, s44\n\t"
          "buffer_load_dwordx4 v[164:167], v120, s[40:43], s44 offen offset:0 sc1\n\t"
          "buffer_load_dwordx4 v[168:171], v120, s[40:43], s44 offen offset:1024 sc1\n\t"
          "buffer_load_dwordx4 v[172:175], v120, s[40:43], s44 offen offset:2048 sc1\n\t"
          "buffer_load_dwordx4 v[176:179], v120, s[40:43], s44 offen offset:3072 sc1\n\t"
          "s_add_u32 s44, s44, 0x1000\n\t"
          "s_and_b32 s44, s44, 0xffff\n\t"
          "s_cmp_eq_u32 s44, s49\n\t"
          "s_cselect_b32 s44, s50, s44\n\t"
          "buffer_load_dwordx4 v[180:183], v120, s[40:43], s44 offen offset:0 sc1\n\t"
          "buffer_load_dwordx4 v[184:187], v120, s[40:43], s44 offen offset:1024 sc1\n\t"
          "buffer_load_dwordx4 v[188:191], v120, s[40:43], s44 offen offset:2048 sc1\n\t"
          "buffer_load_dwordx4 v[192:195], v120, s[40:43], s44 offen offset:3072 sc1\n\t"
          : "={v[132:147]}"(xr0), "={v[148:163]}"(xr1), "={v[164:179]}"(xr2), "={v[180:195]}"(xr3), "+{s44}"(soff) : "{v[112:127]}"(stB), "{s[40:43]}"(srd), KSKIP, "{s51}"(kpos(tm, tn, 1) * 4096) : "scc", "memory");
      } else {
        xr0 = cat4(x1p[0], x1p[1], x1p[2], x1p[3]);
        asm volatile("" : "={v[148:163]}"(xr1), "={v[164:179]}"(xr2), "={v[180:195]}"(xr3));
      }
      STAMPV(1 + 16 * L);
      asm volatile(
        "v_mov_b32 v123, v121\n\t"
        "v_mov_b32 v124, v122\n\t"
        "s_mov_b64 vcc, -1\n\t"
        "s_cbranch_vccnz 10f\n\t"
        "11:\n\t"
        "s_mov_b32 s46, 12288\n\t"
        "s_add_u32 s47, s47, 1\n\t"
        "ds_read_b64_tr_b16 v[96:97], v123 offset:0\n\t"
        "ds_read_b64_tr_b16 v[98:99], v123 offset:3072\n\t"
        "ds_read_b64_tr_b16 v[100:101], v124 offset:0\n\t"
        "ds_read_b64_tr_b16 v[102:103], v124 offset:3072\n\t"
        "ds_read_b64_tr_b16 v[104:105], v123 offset:128\n\t"
        "ds_read_b64_tr_b16 v[106:107], v123 offset:3200\n\t"
        "ds_read_b64_tr_b16 v[108:109], v124 offset:128\n\t"
        "ds_read_b64_tr_b16 v[110:111], v124 offset:3200\n\t"
        "ds_read_b64_tr_b16 v[112:113], v123 offset:256\n\t"
        "ds_read_b64_tr_b16 v[114:115], v123 offset:3328\n\t"
        "ds_write_b32 v127, v128\n\t"
        "ds_read_b64_tr_b16 v[116:117], v124 offset:256\n\t"
        "ds_read_b64_tr_b16 v[118:119], v124 offset:3328\n\t"
        "s_waitcnt vmcnt(15) lgkmcnt(11)\n\t"
        "v_mfma_f32_32x32x16_bf16 v[0:15], v[96:99], v[132:135], 0\n\t"
        "ds_read_b32 v126, v125\n\t"
        "ds_read_b64_tr_b16 v[96:97], v123 offset:6144\n\t"
        "ds_read_b64_tr_b16 v[98:99], v123 offset:9216\n\t"
        "s_waitcnt lgkmcnt(12)\n\t"
        "v_mfma_f32_32x32x16_bf16 v[16:31], v[100:103], v[132:135], 0\n\t"
        "v_add_u32 v129, s46, v121\n\t"
        "ds_read_b64_tr_b16 v[100:101], v124 offset:6144\n\t"
        "ds_read_b64_tr_b16 v[102:103], v124 offset:9216\n\t"
        "s_waitcnt lgkmcnt(12)\n\t"
        "v_mfma_f32_32x32x16_bf16 v[32:47], v[104:107], v[132:135], 0\n\t"
        "ds_read_b64_tr_b16 v[104:105], v123 offset:6272\n\t"
        "ds_read_b64_tr_b16 v[106:107], v123 offset:9344\n\t"
        "s_waitcnt lgkmcnt(12)\n\t"
        "v_mfma_f32_32x32x16_bf16 v[48:63], v[108:111], v[132:135], 0\n\t"
        "v_add_u32 v130, s46, v122\n\t"
        "ds_read_b64_tr_b16 v[108:109], v124 offset:6272\n\t"
        "ds_read_b64_tr_b16 v[110:111], v124 offset:9344\n\t"
        "s_waitcnt lgkmcnt(12)\n\t"
        "v_mfma_f32_32x32x16_bf16 v[64:79], v[132:135], v[112:115], 0\n\t"
        "ds_read_b64_tr_b16 v[112:113], v123 offset:6400\n\t"
        "ds_read_b64_tr_b16 v[114:115], v123 offset:9472\n\t"
        "s_waitcnt lgkmcnt(10)\n\t"
        "v_mfma_f32_32x32x16_bf16 v[80:95], v[132:135], v[116:119], 0\n\t"
        "v_cmp_gt_u32 vcc, s47, v126\n\t"
        "ds_read_b64_tr_b16 v[116:117], v124 offset:6400\n\t"
        "ds_read_b64_tr_b16 v[118:119], v124 offset:9472\n\t"
        "s_waitcnt vmcnt(14) lgkmcnt(10)\n\t"
        "v_mfma_f32_32x32x16_bf16 v[0:15], v[96:99], v[136:139], v[0:15]\n\t"
        "s_cbranch_vccnz 12f\n\t"
        "13:\n\t"
        "ds_read_b64_tr_b16 v[96:97], v129 offset:0\n\t"
        "ds_read_b64_tr_b16 v[98:99], v129 offset:3072\n\t"
        "s_waitcnt lgkmcnt(10)\n\t"
        "v_mfma_f32_32x32x16_bf16 v[16:31], v[100:103], v[136:139], v[16:31]\n\t"
        "v_add_u32 v128, 1, v128\n\t"
        "ds_read_b64_tr_b16 v[100:101], v130 offset:0\n\t"
        "ds_read_b64_tr_b16 v[102:103], v130 offset:3072\n\t"
        "s_waitcnt lgkmcnt(10)\n\t"
        "v_mfma_f32_32x32x16_bf16 v[32:47], v[104:107], v[136:139], v[32:47]\n\t"
        "ds_read_b64_tr_b16 v[104:105], v129 offset:128\n\t"
        "ds_read_b64_tr_b16 v[106:107], v129 offset:3200\n\t"
        "s_waitcnt lgkmcnt(10)\n\t"
        "v_mfma_f32_32x32x16_bf16 v[48:63], v[108:111], v[136:139], v[48:63]\n\t"
        "s_add_u32 s46, s46, 12288\n\t"
        "s_add_u32 s47, s47, 1\n\t"
        "ds_read_b64_tr_b16 v[108:109], v130 offset:128\n\t"
        "ds_read_b64_tr_b16 v[110:111], v130 offset:3200\n\t"
        "s_waitcnt lgkmcnt(10)\n\t"
        "v_mfma_f32_32x32x16_bf16 v[64:79], v[136:139], v[112:115], v[64:79]\n\t"
        "s_cmp_eq_u32 s46, 110592\n\t"
        "s_cselect_b32 s46, 0, s46\n\t"
        "ds_read_b64_tr_b16 v[112:113], v129 offset:256\n\t"
        "ds_read_b64_tr_b16 v[114:115], v129 offset:3328\n\t"
        "s_waitcnt lgkmcnt(10)\n\t"
        "v_mfma_f32_32x32x16_bf16 v[80:95], v[136:139], v[116:119], v[80:95]\n\t"
        "ds_write_b32 v127, v128\n\t"
        "ds_read_b64_tr_b16 v[116:117], v130 offset:256\n\t"
        "ds_read_b64_tr_b16 v[118:119], v130 offset:3328\n\t"
        "s_waitcnt vmcnt(13) lgkmcnt(11)\n\t"
        "v_mfma_f32_32x32x16_bf16 v[0:15], v[96:99], v[140:143], v[0:15]\n\t"
        "ds_read_b32 v126, v125\n\t"
        "ds_read_b64_tr_b16 v[96:97], v129 offset:6144\n\t"
        "ds_read_b64_tr_b16 v[98:99], v129 offset:9216\n\t"
        "s_waitcnt lgkmcnt(12)\n\t"
        "v_mfma_f32_32x32x16_bf16 v[16:31], v[100:103], v[140:143], v[16:31]\n\t"
        "v_add_u32 v123, s46, v121\n\t"
        "ds_read_b64_tr_b16 v[100:101], v130 offset:6144\n\t"
        "ds_read_b64_tr_b16 v[102:103], v130 offset:9216\n\t"
        "s_waitcnt lgkmcnt(12)\n\t"
        "v_mfma_f32_32x32x16_bf16 v[32:47], v[104:107], v[140:143], v[32:47]\n\t"
        "ds_read_b64_tr_b16 v[104:105], v129 offset:6272\n\t"
        "ds_read_b64_tr_b16 v[106:107], v129 offset:9344\n\t"
        "s_waitcnt lgkmcnt(12)\n\t"
        "v_mfma_f32_32x32x16_bf16 v[48:63], v[108:111], v[140:143], v[48:63]\n\t"
        "v_add_u32 v124, s46, v122\n\t"
        "ds_read_b64_tr_b16 v[108:109], v130 offset:6272\n\t"
        "ds_read_b64_tr_b16 v[110:111], v130 offset:9344\n\t"
        "s_waitcnt lgkmcnt(12)\n\t"
        "v_mfma_f32_32x32x16_bf16 v[64:79], v[140:143], v[112:115], v[64:79]\n\t"
        "ds_read_b64_tr_b16 v[112:113], v129 offset:6400\n\t"
        "ds_read_b64_tr_b16 v[114:115], v129 offset:9472\n\t"
        "s_waitcnt lgkmcnt(10)\n\t"
        "v_mfma_f32_32x32x16_bf16 v[80:95], v[140:143], v[116:119], v[80:95]\n\t"
        "v_cmp_gt_u32 vcc, s47, v126\n\t"
        "ds_read_b64_tr_b16 v[116:117], v130 offset:6400\n\t"
        "ds_read_b64_tr_b16 v[118:119], v130 offset:9472\n\t"
        "s_waitcnt vmcnt(12) lgkmcnt(10)\n\t"
        "v_mfma_f32_32x32x16_bf16 v[0:15], v[96:99], v[144:147], v[0:15]\n\t"
        "s_cbranch_vccnz 14f\n\t"
        "15:\n\t"
        "ds_read_b64_tr_b16 v[96:97], v123 offset:0\n\t"
        "ds_read_b64_tr_b16 v[98:99], v123 offset:3072\n\t"
        "s_waitcnt lgkmcnt(10)\n\t"
        "v_mfma_f32_32x32x16_bf16 v[16:31], v[100:103], v[144:147], v[16:31]\n\t"
        "v_add_u32 v128, 1, v128\n\t"
        "ds_read_b64_tr_b16 v[100:101], v124 offset:0\n\t"
        "ds_read_b64_tr_b16 v[102:103], v124 offset:3072\n\t"
        "s_waitcnt lgkmcnt(10)\n\t"
        "v_mfma_f32_32x32x16_bf16 v[32:47], v[104:107], v[144:147], v[32:47]\n\t"
        "ds_read_b64_tr_b16 v[104:105], v123 offset:128\n\t"
        "ds_read_b64_tr_b16 v[106:107], v123 offset:3200\n\t"
        "s_waitcnt lgkmcnt(10)\n\t"
        "v_mfma_f32_32x32x16_bf16 v[48:63], v[108:111], v[144:147], v[48:63]\n\t"
        "s_add_u32 s46, s46, 12288\n\t"
        "s_add_u32 s47, s47, 1\n\t"
        "ds_read_b64_tr_b16 v[108:109], v124 offset:128\n\t"
        "ds_read_b64_tr_b16 v[110:111], v124 offset:3200\n\t"
        "s_waitcnt lgkmcnt(10)\n\t"
        "v_mfma_f32_32x32x16_bf16 v[64:79], v[144:147], v[112:115], v[64:79]\n\t"
        "s_cmp_eq_u32 s46, 110592\n\t"
        "s_cselect_b32 s46, 0, s46\n\t"
        "ds_read_b64_tr_b16 v[112:113], v123 offset:256\n\t"
        "ds_read_b64_tr_b16 v[114:115], v123 offset:3328\n\t"
        "s_waitcnt lgkmcnt(10)\n\t"
        "v_mfma_f32_32x32x16_bf16 v[80:95], v[144:147], v[116:119], v[80:95]\n\t"
        "ds_write_b32 v127, v128\n\t"
        "ds_read_b64_tr_b16 v[116:117], v124 offset:256\n\t"
        "ds_read_b64_tr_b16 v[118:119], v124 offset:3328\n\t"
        "s_waitcnt lgkmcnt(0)\n\t"
        "s_nop 7\n\t"
        "s_branch 9f\n\t"
        "10:\n\t"
        "s_mov_b32 s48, 0x10000\n\t"
        "8:\n\t"
        "ds_read_b32 v126, v125\n\t"
        "s_sub_u32 s48, s48, 1\n\t"
        "s_waitcnt lgkmcnt(0)\n\t"
        "s_cbranch_scc1 11b\n\t"
        "v_cmp_gt_u32 vcc, s47, v126\n\t"
        "s_cbranch_vccnz 8b\n\t"
        "s_branch 11b\n\t"
        "12:\n\t"
        "s_mov_b32 s48, 0x10000\n\t"
        "8:\n\t"
        "ds_read_b32 v126, v125\n\t"
        "s_sub_u32 s48, s48, 1\n\t"
        "s_waitcnt lgkmcnt(0)\n\t"
        "s_cbranch_scc1 13b\n\t"
        "v_cmp_gt_u32 vcc, s47, v126\n\t"
        "s_cbranch_vccnz 8b\n\t"
        "s_branch 13b\n\t"
        "14:\n\t"
        "s_mov_b32 s48, 0x10000\n\t"
        "8:\n\t"
        "ds_read_b32 v126, v125\n\t"
        "s_sub_u32 s48, s48, 1\n\t"
        "s_waitcnt lgkmcnt(0)\n\t"
        "s_cbranch_scc1 15b\n\t"
        "v_cmp_gt_u32 vcc, s47, v126\n\t"
        "s_cbranch_vccnz 8b\n\t"
        "s_branch 15b\n\t"
        "9:\n\t"
        : "={v[0:15]}"(acc[0]), "={v[16:31]}"(acc[1]), "={v[32:47]}"(acc[2]), "={v[48:63]}"(acc[3]), "={v[64:79]}"(acc[4]), "={v[80:95]}"(acc[5]),
          "={v[96:111]}"(stA), "+{v[112:127]}"(stB), "+{v[128:131]}"(stC), "={s46}"(slot), "+{s47}"(need)
        : "{v[132:147]}"(xr0) : "s48", "vcc", "scc", "memory");
      STAMPV(2 + 16 * L);
      float al = 1.f;
      if (L == 1) {
        PHASE_VARS;
        float S, Q, Dd;
        row_stats_sum(rs, OFF_SX, tile, lane, S, Q, Dd);
        STAMPV(36);
        lds_wait_ge<0>(lbase + L_W + 24, 2);
        soff = kpos(tm, tn, 1) * 4096;
        asm volatile(
          "buffer_load_dwordx4 v[148:151], v120, s[40:43], s44 offen offset:0 sc1\n\t"
          "buffer_load_dwordx4 v[152:155], v120, s[40:43], s44 offen offset:1024 sc1\n\t"
          "buffer_load_dwordx4 v[156:159], v120, s[40:43], s44 offen offset:2048 sc1\n\t"
          "buffer_load_dwordx4 v[160:163], v120, s[40:43], s44 offen offset:3072 sc1\n\t"
          : "={v[148:163]}"(xr1), "={v[164:179]}"(xr2), "={v[180:195]}"(xr3), "+{s44}"(soff), "+v"(S), "+v"(Q), "+v"(Dd) : "{v[112:127]}"(stB), "{s[40:43]}"(srd), KSKIP, "{s51}"(kpos(tm, tn, 1) * 4096) : "scc", "memory");
        float mean, var, mb1, rd1, mb2, den2;
        row_stats_fin(rs, OFF_SX + SX_CAND + (tn * 16 + tm) * 128 + wid * 4, smem, 1, tile, lane, S, Q, Dd, U2F(words[0]), x1p, mean, var, mb1, rd1);
        ln_scalars(mean + (mean - mb1) * rd1, var * (1.0f + rd1) * (1.0f + rd1), mb2, den2);
        const float rd2 = RCP(den2);
        al = (1.0f + rd1) * rd2;
        const float be = -(mb1 * rd1 + mb2) * RCP(1.0f + rd1);
        reinterpret_cast<float*>(smem + L_AB)[wid * 64 + lane] = hi ? be : al;
        reinterpret_cast<float*>(smem + L_AB)[256 + wid * 64 + lane] = hi ? 1.0f + rd1 : -mb1 * rd1;
        soff = kpos(tm, tn, 1 + 0) * 4096;
        asm volatile(
          "s_nop 0\n\t"
          "s_add_u32 s44, s44, 0x1000\n\t"
          "s_and_b32 s44, s44, 0xffff\n\t"
          "s_cmp_eq_u32 s44, s49\n\t"
          "s_cselect_b32 s44, s50, s44\n\t"
          "buffer_load_dwordx4 v[164:167], v120, s[40:43], s44 offen offset:0 sc1\n\t"
          "buffer_load_dwordx4 v[168:171], v120, s[40:43], s44 offen offset:1024 sc1\n\t"
          "buffer_load_dwordx4 v[172:175], v120, s[40:43], s44 offen offset:2048 sc1\n\t"
          "buffer_load_dwordx4 v[176:179], v120, s[40:43], s44 offen offset:3072 sc1\n\t"
          "s_add_u32 s44, s44, 0x1000\n\t"
          "s_and_b32 s44, s44, 0xffff\n\t"
          "s_cmp_eq_u32 s44, s49\n\t"
          "s_cselect_b32 s44, s50, s44\n\t"
          "buffer_load_dwordx4 v[180:183], v120, s[40:43], s44 offen offset:0 sc1\n\t"
          "buffer_load_dwordx4 v[184:187], v120, s[40:43], s44 offen offset:1024 sc1\n\t"
          "buffer_load_dwordx4 v[188:191], v120, s[40:43], s44 offen offset:2048 sc1\n\t"
          "buffer_load_dwordx4 v[192:195], v120, s[40:43], s44 offen offset:3072 sc1\n\t"
          : "+{v[148:163]}"(xr1), "+{v[164:179]}"(xr2), "+{v[180:195]}"(xr3), "+{s44}"(soff) : "{v[112:127]}"(stB), "{s[40:43]}"(srd), KSKIP, "{s51}"(kpos(tm, tn, 1) * 4096) : "scc", "memory");
#pragma unroll
        for (int jj = 0; jj < 3; ++jj) {
          f32x4 cs[2][4];
          float cv[2];
#pragma unroll
          for (int j = 0; j < 2; ++j) {
            const int cb = jj * 64 + (j ^ hh) * 32;
            if (jj == 2) cv[j] = *reinterpret_cast<const float*>(smem + L_CS + (cb + r32) * 4);
#pragma unroll
            for (int g = 0; g < 4; ++g)
              if (jj < 2 || j == 0) cs[j][g] = *reinterpret_cast<const f32x4*>(smem + (jj < 2 ? L_CS + cb * 4 : L_AB + wid * 256 + 128) + (8 * g + 4 * hi) * 4);
          }
          SBAR();
#pragma unroll
          for (int j = 0; j < 2; ++j)
#pragma unroll
            for (int e = 0; e < 16; ++e) acc[2 * jj + j][e] += jj < 2 ? be * cs[j][e >> 2][e & 3] : cs[0][e >> 2][e & 3] * cv[j];
          SBAR();
        }
      }
      u32x4 ep;
      { PHASE_VARS;
        if (L == 0) reinterpret_cast<float*>(smem + L_AB)[wid * 64 + lane] = 1.0f;
        const unsigned dst = OFF_KV + L * (10 * MBY) + tn * 2 * HS + tile * 4096 + lane * 16;
        ep = u32x4{__float_as_uint(al), dst + hh * HS, dst + (hh ^ 1) * HS, lbase + L_AB + wid * 256 + hi * 16}; }
      STAMPV(3 + 16 * L);
      soff = kpos(tm, tn, 1 + 2) * 4096;
      asm volatile(
        "s_nop 0\n\t"
        "s_add_u32 s44, s44, 0x1000\n\t"
        "s_and_b32 s44, s44, 0xffff\n\t"
        "s_cmp_eq_u32 s44, s49\n\t"
        "s_cselect_b32 s44, s50, s44\n\t"
        "buffer_load_dwordx4 v[132:135], v120, s[40:43], s44 offen offset:0 sc1\n\t"
        "buffer_load_dwordx4 v[136:139], v120, s[40:43], s44 offen offset:1024 sc1\n\t"
        "buffer_load_dwordx4 v[140:143], v120, s[40:43], s44 offen offset:2048 sc1\n\t"
        "s_mov_b32 s45, 3\n\t"
        "1:\n\t"
        "s_waitcnt vmcnt(14) lgkmcnt(11)\n\t"
        "v_mfma_f32_32x32x16_bf16 v[0:15], v[96:99], v[148:151], v[0:15]\n\t"
        "ds_read_b32 v126, v125\n\t"
        "ds_read_b64_tr_b16 v[96:97], v123 offset:6144\n\t"
        "ds_read_b64_tr_b16 v[98:99], v123 offset:9216\n\t"
        "s_waitcnt lgkmcnt(12)\n\t"
        "v_mfma_f32_32x32x16_bf16 v[16:31], v[100:103], v[148:151], v[16:31]\n\t"
        "v_add_u32 v129, s46, v121\n\t"
        "ds_read_b64_tr_b16 v[100:101], v124 offset:6144\n\t"
        "ds_read_b64_tr_b16 v[102:103], v124 offset:9216\n\t"
        "s_waitcnt lgkmcnt(12)\n\t"
        "v_mfma_f32_32x32x16_bf16 v[32:47], v[104:107], v[148:151], v[32:47]\n\t"
        "ds_read_b64_tr_b16 v[104:105], v123 offset:6272\n\t"
        "ds_read_b64_tr_b16 v[106:107], v123 offset:9344\n\t"
        "buffer_load_dwordx4 v[144:147], v120, s[40:43], s44 offen offset:3072 sc1\n\t"
        "s_waitcnt lgkmcnt(12)\n\t"
        "v_mfma_f32_32x32x16_bf16 v[48:63], v[108:111], v[148:151], v[48:63]\n\t"
        "v_add_u32 v130, s46, v122\n\t"
        "ds_read_b64_tr_b16 v[108:109], v124 offset:6272\n\t"
        "ds_read_b64_tr_b16 v[110:111], v124 offset:9344\n\t"
        "s_waitcnt lgkmcnt(12)\n\t"
        "v_mfma_f32_32x32x16_bf16 v[64:79], v[148:151], v[112:115], v[64:79]\n\t"
        "ds_read_b64_tr_b16 v[112:113], v123 offset:6400\n\t"
        "ds_read_b64_tr_b16 v[114:115], v123 offset:9472\n\t"
        "s_waitcnt lgkmcnt(10)\n\t"
        "v_mfma_f32_32x32x16_bf16 v[80:95], v[148:151], v[116:119], v[80:95]\n\t"
        "v_cmp_gt_u32 vcc, s47, v126\n\t"
        "ds_read_b64_tr_b16 v[116:117], v124 offset:6400\n\t"
        "ds_read_b64_tr_b16 v[118:119], v124 offset:9472\n\t"
        "s_waitcnt vmcnt(14) lgkmcnt(10)\n\t"
        "v_mfma_f32_32x32x16_bf16 v[0:15], v[96:99], v[152:155], v[0:15]\n\t"
        "s_cbranch_vccnz 16f\n\t"
        "17:\n\t"
        "ds_read_b64_tr_b16 v[96:97], v129 offset:0\n\t"
        "ds_read_b64_tr_b16 v[98:99], v129 offset:3072\n\t"
        "s_waitcnt lgkmcnt(10)\n\t"
        "v_mfma_f32_32x32x16_bf16 v[16:31], v[100:103], v[152:155], v[16:31]\n\t"
        "v_add_u32 v128, 1, v128\n\t"
        "ds_read_b64_tr_b16 v[100:101], v130 offset:0\n\t"
        "ds_read_b64_tr_b16 v[102:103], v130 offset:3072\n\t"
        "s_add_u32 s44, s44, 0x1000\n\t"
        "s_and_b32 s44, s44, 0xffff\n\t"
        "s_waitcnt lgkmcnt(10)\n\t"
        "v_mfma_f32_32x32x16_bf16 v[32:47], v[104:107], v[152:155], v[32:47]\n\t"
        "ds_read_b64_tr_b16 v[104:105], v129 offset:128\n\t"
        "ds_read_b64_tr_b16 v[106:107], v129 offset:3200\n\t"
        "s_cmp_eq_u32 s44, s49\n\t"
        "s_cselect_b32 s44, s50, s44\n\t"
        "buffer_load_dwordx4 v[148:151], v120, s[40:43], s44 offen offset:0 sc1\n\t"
        "s_waitcnt lgkmcnt(10)\n\t"
        "v_mfma_f32_32x32x16_bf16 v[48:63], v[108:111], v[152:155], v[48:63]\n\t"
        "s_add_u32 s46, s46, 12288\n\t"
        "s_add_u32 s47, s47, 1\n\t"
        "ds_read_b64_tr_b16 v[108:109], v130 offset:128\n\t"
        "ds_read_b64_tr_b16 v[110:111], v130 offset:3200\n\t"
        "s_waitcnt lgkmcnt(10)\n\t"
        "v_mfma_f32_32x32x16_bf16 v[64:79], v[152:155], v[112:115], v[64:79]\n\t"
        "s_cmp_eq_u32 s46, 110592\n\t"
        "s_cselect_b32 s46, 0, s46\n\t"
        "ds_read_b64_tr_b16 v[112:113], v129 offset:256\n\t"
        "ds_read_b64_tr_b16 v[114:115], v129 offset:3328\n\t"
        "s_waitcnt lgkmcnt(10)\n\t"
        "v_mfma_f32_32x32x16_bf16 v[80:95], v[152:155], v[116:119], v[80:95]\n\t"
        "ds_write_b32 v127, v128\n\t"
        "ds_read_b64_tr_b16 v[116:117], v130 offset:256\n\t"
        "ds_read_b64_tr_b16 v[118:119], v130 offset:3328\n\t"
        "s_waitcnt vmcnt(14) lgkmcnt(11)\n\t"
        "v_mfma_f32_32x32x16_bf16 v[0:15], v[96:99], v[156:159], v[0:15]\n\t"
        "ds_read_b32 v126, v125\n\t"
        "ds_read_b64_tr_b16 v[96:97], v129 offset:6144\n\t"
        "ds_read_b64_tr_b16 v[98:99], v129 offset:9216\n\t"
        "s_waitcnt lgkmcnt(12)\n\t"
        "v_mfma_f32_32x32x16_bf16 v[16:31], v[100:103], v[156:159], v[16:31]\n\t"
        "v_add_u32 v123, s46, v121\n\t"
        "ds_read_b64_tr_b16 v[100:101], v130 offset:6144\n\t"
        "ds_read_b64_tr_b16 v[102:103], v130 offset:9216\n\t"
        "s_waitcnt lgkmcnt(12)\n\t"
        "v_mfma_f32_32x32x16_bf16 v[32:47], v[104:107], v[156:159], v[32:47]\n\t"
        "ds_read_b64_tr_b16 v[104:105], v129 offset:6272\n\t"
        "ds_read_b64_tr_b16 v[106:107], v129 offset:9344\n\t"
        "buffer_load_dwordx4 v[152:155], v120, s[40:43], s44 offen offset:1024 sc1\n\t"
        "s_waitcnt lgkmcnt(12)\n\t"
        "v_mfma_f32_32x32x16_bf16 v[48:63], v[108:111], v[156:159], v[48:63]\n\t"
        "v_add_u32 v124, s46, v122\n\t"
        "ds_read_b64_tr_b16 v[108:109], v130 offset:6272\n\t"
        "ds_read_b64_tr_b16 v[110:111], v130 offset:9344\n\t"
        "s_waitcnt lgkmcnt(12)\n\t"
        "v_mfma_f32_32x32x16_bf16 v[64:79], v[156:159], v[112:115], v[64:79]\n\t"
        "ds_read_b64_tr_b16 v[112:113], v129 offset:6400\n\t"
        "ds_read_b64_tr_b16 v[114:115], v129 offset:9472\n\t"
        "s_waitcnt lgkmcnt(10)\n\t"
        "v_mfma_f32_32x32x16_bf16 v[80:95], v[156:159], v[116:119], v[80:95]\n\t"
        "v_cmp_gt_u32 vcc, s47, v126\n\t"
        "ds_read_b64_tr_b16 v[116:117], v130 offset:6400\n\t"
        "ds_read_b64_tr_b16 v[118:119], v130 offset:9472\n\t"
        "s_waitcnt vmcnt(14) lgkmcnt(10)\n\t"
        "v_mfma_f32_32x32x16_bf16 v[0:15], v[96:99], v[160:163], v[0:15]\n\t"
        "s_cbranch_vccnz 18f\n\t"
        "19:\n\t"
        "ds_read_b64_tr_b16 v[96:97], v123 offset:0\n\t"
        "ds_read_b64_tr_b16 v[98:99], v123 offset:3072\n\t"
        "s_waitcnt lgkmcnt(10)\n\t"
        "v_mfma_f32_32x32x16_bf16 v[16:31], v[100:103], v[160:163], v[16:31]\n\t"
        "v_add_u32 v128, 1, v128\n\t"
        "ds_read_b64_tr_b16 v[100:101], v124 offset:0\n\t"
        "ds_read_b64_tr_b16 v[102:103], v124 offset:3072\n\t"
        "s_waitcnt lgkmcnt(10)\n\t"
        "v_mfma_f32_32x32x16_bf16 v[32:47], v[104:107], v[160:163], v[32:47]\n\t"
        "ds_read_b64_tr_b16 v[104:105], v123 offset:128\n\t"
        "ds_read_b64_tr_b16 v[106:107], v123 offset:3200\n\t"
        "buffer_load_dwordx4 v[156:159], v120, s[40:43], s44 offen offset:2048 sc1\n\t"
        "s_waitcnt lgkmcnt(10)\n\t"
        "v_mfma_f32_32x32x16_bf16 v[48:63], v[108:111], v[160:163], v[48:63]\n\t"
        "s_add_u32 s46, s46, 12288\n\t"
        "s_add_u32 s47, s47, 1\n\t"
        "ds_read_b64_tr_b16 v[108:109], v124 offset:128\n\t"
        "ds_read_b64_tr_b16 v[110:111], v124 offset:3200\n\t"
        "s_waitcnt lgkmcnt(10)\n\t"
        "v_mfma_f32_32x32x16_bf16 v[64:79], v[160:163], v[112:115], v[64:79]\n\t"
        "s_cmp_eq_u32 s46, 110592\n\t"
        "s_cselect_b32 s46, 0, s46\n\t"
        "ds_read_b64_tr_b16 v[112:113], v123 offset:256\n\t"
        "ds_read_b64_tr_b16 v[114:115], v123 offset:3328\n\t"
        "s_waitcnt lgkmcnt(10)\n\t"
        "v_mfma_f32_32x32x16_bf16 v[80:95], v[160:163], v[116:119], v[80:95]\n\t"
        "ds_write_b32 v127, v128\n\t"
        "ds_read_b64_tr_b16 v[116:117], v124 offset:256\n\t"
        "ds_read_b64_tr_b16 v[118:119], v124 offset:3328\n\t"
        "s_waitcnt vmcnt(14) lgkmcnt(11)\n\t"
        "v_mfma_f32_32x32x16_bf16 v[0:15], v[96:99], v[164:167], v[0:15]\n\t"
        "ds_read_b32 v126, v125\n\t"
        "ds_read_b64_tr_b16 v[96:97], v123 offset:6144\n\t"
        "ds_read_b64_tr_b16 v[98:99], v123 offset:9216\n\t"
        "s_waitcnt lgkmcnt(12)\n\t"
        "v_mfma_f32_32x32x16_bf16 v[16:31], v[100:103], v[164:167], v[16:31]\n\t"
        "v_add_u32 v129, s46, v121\n\t"
        "ds_read_b64_tr_b16 v[100:101], v124 offset:6144\n\t"
        "ds_read_b64_tr_b16 v[102:103], v124 offset:9216\n\t"
        "s_waitcnt lgkmcnt(12)\n\t"
        "v_mfma_f32_32x32x16_bf16 v[32:47], v[104:107], v[164:167], v[32:47]\n\t"
        "ds_read_b64_tr_b16 v[104:105], v123 offset:6272\n\t"
        "ds_read_b64_tr_b16 v[106:107], v123 offset:9344\n\t"
        "buffer_load_dwordx4 v[160:163], v120, s[40:43], s44 offen offset:3072 sc1\n\t"
        "s_waitcnt lgkmcnt(12)\n\t"
        "v_mfma_f32_32x32x16_bf16 v[48:63], v[108:111], v[164:167], v[48:63]\n\t"
        "v_add_u32 v130, s46, v122\n\t"
        "ds_read_b64_tr_b16 v[108:109], v124 offset:6272\n\t"
        "ds_read_b64_tr_b16 v[110:111], v124 offset:9344\n\t"
        "s_waitcnt lgkmcnt(12)\n\t"
        "v_mfma_f32_32x32x16_bf16 v[64:79], v[164:167], v[112:115], v[64:79]\n\t"
        "ds_read_b64_tr_b16 v[112:113], v123 offset:6400\n\t"
        "ds_read_b64_tr_b16 v[114:115], v123 offset:9472\n\t"
        "s_waitcnt lgkmcnt(10)\n\t"
        "v_mfma_f32_32x32x16_bf16 v[80:95], v[164:167], v[116:119], v[80:95]\n\t"
        "v_cmp_gt_u32 vcc, s47, v126\n\t"
        "ds_read_b64_tr_b16 v[116:117], v124 offset:6400\n\t"
        "ds_read_b64_tr_b16 v[118:119], v124 offset:9472\n\t"
        "s_waitcnt vmcnt(14) lgkmcnt(10)\n\t"
        "v_mfma_f32_32x32x16_bf16 v[0:15], v[96:99], v[168:171], v[0:15]\n\t"
        "s_cbranch_vccnz 20f\n\t"
        "21:\n\t"
        "ds_read_b64_tr_b16 v[96:97], v129 offset:0\n\t"
        "ds_read_b64_tr_b16 v[98:99], v129 offset:3072\n\t"
        "s_waitcnt lgkmcnt(10)\n\t"
        "v_mfma_f32_32x32x16_bf16 v[16:31], v[100:103], v[168:171], v[16:31]\n\t"
        "v_add_u32 v128, 1, v128\n\t"
        "ds_read_b64_tr_b16 v[100:101], v130 offset:0\n\t"
        "ds_read_b64_tr_b16 v[102:103], v130 offset:3072\n\t"
        "s_add_u32 s44, s44, 0x1000\n\t"
        "s_and_b32 s44, s44, 0xffff\n\t"
        "s_waitcnt lgkmcnt(10)\n\t"
        "v_mfma_f32_32x32x16_bf16 v[32:47], v[104:107], v[168:171], v[32:47]\n\t"
        "ds_read_b64_tr_b16 v[104:105], v129 offset:128\n\t"
        "ds_read_b64_tr_b16 v[106:107], v129 offset:3200\n\t"
        "s_cmp_eq_u32 s44, s49\n\t"
        "s_cselect_b32 s44, s50, s44\n\t"
        "buffer_load_dwordx4 v[164:167], v120, s[40:43], s44 offen offset:0 sc1\n\t"
        "s_waitcnt lgkmcnt(10)\n\t"
        "v_mfma_f32_32x32x16_bf16 v[48:63], v[108:111], v[168:171], v[48:63]\n\t"
        "s_add_u32 s46, s46, 12288\n\t"
        "s_add_u32 s47, s47, 1\n\t"
        "ds_read_b64_tr_b16 v[108:109], v130 offset:128\n\t"
        "ds_read_b64_tr_b16 v[110:111], v130 offset:3200\n\t"
        "s_waitcnt lgkmcnt(10)\n\t"
        "v_mfma_f32_32x32x16_bf16 v[64:79], v[168:171], v[112:115], v[64:79]\n\t"
        "s_cmp_eq_u32 s46, 110592\n\t"
        "s_cselect_b32 s46, 0, s46\n\t"
        "ds_read_b64_tr_b16 v[112:113], v129 offset:256\n\t"
        "ds_read_b64_tr_b16 v[114:115], v129 offset:3328\n\t"
        "s_waitcnt lgkmcnt(10)\n\t"
        "v_mfma_f32_32x32x16_bf16 v[80:95], v[168:171], v[116:119], v[80:95]\n\t"
        "ds_write_b32 v127, v128\n\t"
        "ds_read_b64_tr_b16 v[116:117], v130 offset:256\n\t"
        "ds_read_b64_tr_b16 v[118:119], v130 offset:3328\n\t"
        "s_waitcnt vmcnt(14) lgkmcnt(11)\n\t"
        "v_mfma_f32_32x32x16_bf16 v[0:15], v[96:99], v[172:175], v[0:15]\n\t"
        "ds_read_b32 v126, v125\n\t"
        "ds_read_b64_tr_b16 v[96:97], v129 offset:6144\n\t"
        "ds_read_b64_tr_b16 v[98:99], v129 offset:9216\n\t"
        "s_waitcnt lgkmcnt(12)\n\t"
        "v_mfma_f32_32x32x16_bf16 v[16:31], v[100:103], v[172:175], v[16:31]\n\t"
        "v_add_u32 v123, s46, v121\n\t"
        "ds_read_b64_tr_b16 v[100:101], v130 offset:6144\n\t"
        "ds_read_b64_tr_b16 v[102:103], v130 offset:9216\n\t"
        "s_waitcnt lgkmcnt(12)\n\t"
        "v_mfma_f32_32x32x16_bf16 v[32:47], v[104:107], v[172:175], v[32:47]\n\t"
        "ds_read_b64_tr_b16 v[104:105], v129 offset:6272\n\t"
        "ds_read_b64_tr_b16 v[106:107], v129 offset:9344\n\t"
        "buffer_load_dwordx4 v[168:171], v120, s[40:43], s44 offen offset:1024 sc1\n\t"
        "s_waitcnt lgkmcnt(12)\n\t"
        "v_mfma_f32_32x32x16_bf16 v[48:63], v[108:111], v[172:175], v[48:63]\n\t"
        "v_add_u32 v124, s46, v122\n\t"
        "ds_read_b64_tr_b16 v[108:109], v130 offset:6272\n\t"
        "ds_read_b64_tr_b16 v[110:111], v130 offset:9344\n\t"
        "s_waitcnt lgkmcnt(12)\n\t"
        "v_mfma_f32_32x32x16_bf16 v[64:79], v[172:175], v[112:115], v[64:79]\n\t"
        "ds_read_b64_tr_b16 v[112:113], v129 offset:6400\n\t"
        "ds_read_b64_tr_b16 v[114:115], v129 offset:9472\n\t"
        "s_waitcnt lgkmcnt(10)\n\t"
        "v_mfma_f32_32x32x16_bf16 v[80:95], v[172:175], v[116:119], v[80:95]\n\t"
        "v_cmp_gt_u32 vcc, s47, v126\n\t"
        "ds_read_b64_tr_b16 v[116:117], v130 offset:6400\n\t"
        "ds_read_b64_tr_b16 v[118:119], v130 offset:9472\n\t"
        "s_waitcnt vmcnt(14) lgkmcnt(10)\n\t"
        "v_mfma_f32_32x32x16_bf16 v[0:15], v[96:99], v[176:179], v[0:15]\n\t"
        "s_cbranch_vccnz 22f\n\t"
        "23:\n\t"
        "ds_read_b64_tr_b16 v[96:97], v123 offset:0\n\t"
        "ds_read_b64_tr_b16 v[98:99], v123 offset:3072\n\t"
        "s_waitcnt lgkmcnt(10)\n\t"
        "v_mfma_f32_32x32x16_bf16 v[16:31], v[100:103], v[176:179], v[16:31]\n\t"
        "v_add_u32 v128, 1, v128\n\t"
        "ds_read_b64_tr_b16 v[100:101], v124 offset:0\n\t"
        "ds_read_b64_tr_b16 v[102:103], v124 offset:3072\n\t"
        "s_waitcnt lgkmcnt(10)\n\t"
        "v_mfma_f32_32x32x16_bf16 v[32:47], v[104:107], v[176:179], v[32:47]\n\t"
        "ds_read_b64_tr_b16 v[104:105], v123 offset:128\n\t"
        "ds_read_b64_tr_b16 v[106:107], v123 offset:3200\n\t"
        "buffer_load_dwordx4 v[172:175], v120, s[40:43], s44 offen offset:2048 sc1\n\t"
        "s_waitcnt lgkmcnt(10)\n\t"
        "v_mfma_f32_32x32x16_bf16 v[48:63], v[108:111], v[176:179], v[48:63]\n\t"
        "s_add_u32 s46, s46, 12288\n\t"
        "s_add_u32 s47, s47, 1\n\t"
        "ds_read_b64_tr_b16 v[108:109], v124 offset:128\n\t"
        "ds_read_b64_tr_b16 v[110:111], v124 offset:3200\n\t"
        "s_waitcnt lgkmcnt(10)\n\t"
        "v_mfma_f32_32x32x16_bf16 v[64:79], v[176:179], v[112:115], v[64:79]\n\t"
        "s_cmp_eq_u32 s46, 110592\n\t"
        "s_cselect_b32 s46, 0, s46\n\t"
        "ds_read_b64_tr_b16 v[112:113], v123 offset:256\n\t"
        "ds_read_b64_tr_b16 v[114:115], v123 offset:3328\n\t"
        "s_waitcnt lgkmcnt(10)\n\t"
        "v_mfma_f32_32x32x16_bf16 v[80:95], v[176:179], v[116:119], v[80:95]\n\t"
        "ds_write_b32 v127, v128\n\t"
        "ds_read_b64_tr_b16 v[116:117], v124 offset:256\n\t"
        "ds_read_b64_tr_b16 v[118:119], v124 offset:3328\n\t"
        "s_waitcnt vmcnt(14) lgkmcnt(11)\n\t"
        "v_mfma_f32_32x32x16_bf16 v[0:15], v[96:99], v[180:183], v[0:15]\n\t"
        "ds_read_b32 v126, v125\n\t"
        "ds_read_b64_tr_b16 v[96:97], v123 offset:6144\n\t"
        "ds_read_b64_tr_b16 v[98:99], v123 offset:9216\n\t"
        "s_waitcnt lgkmcnt(12)\n\t"
        "v_mfma_f32_32x32x16_bf16 v[16:31], v[100:103], v[180:183], v[16:31]\n\t"
        "v_add_u32 v129, s46, v121\n\t"
        "ds_read_b64_tr_b16 v[100:101], v124 offset:6144\n\t"
        "ds_read_b64_tr_b16 v[102:103], v124 offset:9216\n\t"
        "s_waitcnt lgkmcnt(12)\n\t"
        "v_mfma_f32_32x32x16_bf16 v[32:47], v[104:107], v[180:183], v[32:47]\n\t"
        "ds_read_b64_tr_b16 v[104:105], v123 offset:6272\n\t"
        "ds_read_b64_tr_b16 v[106:107], v123 offset:9344\n\t"
        "buffer_load_dwordx4 v[176:179], v120, s[40:43], s44 offen offset:3072 sc1\n\t"
        "s_waitcnt lgkmcnt(12)\n\t"
        "v_mfma_f32_32x32x16_bf16 v[48:63], v[108:111], v[180:183], v[48:63]\n\t"
        "v_add_u32 v130, s46, v122\n\t"
        "ds_read_b64_tr_b16 v[108:109], v124 offset:6272\n\t"
        "ds_read_b64_tr_b16 v[110:111], v124 offset:9344\n\t"
        "s_waitcnt lgkmcnt(12)\n\t"
        "v_mfma_f32_32x32x16_bf16 v[64:79], v[180:183], v[112:115], v[64:79]\n\t"
        "ds_read_b64_tr_b16 v[112:113], v123 offset:6400\n\t"
        "ds_read_b64_tr_b16 v[114:115], v123 offset:9472\n\t"
        "s_waitcnt lgkmcnt(10)\n\t"
        "v_mfma_f32_32x32x16_bf16 v[80:95], v[180:183], v[116:119], v[80:95]\n\t"
        "v_cmp_gt_u32 vcc, s47, v126\n\t"
        "ds_read_b64_tr_b16 v[116:117], v124 offset:6400\n\t"
        "ds_read_b64_tr_b16 v[118:119], v124 offset:9472\n\t"
        "s_waitcnt vmcnt(14) lgkmcnt(10)\n\t"
        "v_mfma_f32_32x32x16_bf16 v[0:15], v[96:99], v[184:187], v[0:15]\n\t"
        "s_cbranch_vccnz 24f\n\t"
        "25:\n\t"
        "ds_read_b64_tr_b16 v[96:97], v129 offset:0\n\t"
        "ds_read_b64_tr_b16 v[98:99], v129 offset:3072\n\t"
        "s_waitcnt lgkmcnt(10)\n\t"
        "v_mfma_f32_32x32x16_bf16 v[16:31], v[100:103], v[184:187], v[16:31]\n\t"
        "v_add_u32 v128, 1, v128\n\t"
        "ds_read_b64_tr_b16 v[100:101], v130 offset:0\n\t"
        "ds_read_b64_tr_b16 v[102:103], v130 offset:3072\n\t"
        "s_add_u32 s44, s44, 0x1000\n\t"
        "s_and_b32 s44, s44, 0xffff\n\t"
        "s_waitcnt lgkmcnt(10)\n\t"
        "v_mfma_f32_32x32x16_bf16 v[32:47], v[104:107], v[184:187], v[32:47]\n\t"
        "ds_read_b64_tr_b16 v[104:105], v129 offset:128\n\t"
        "ds_read_b64_tr_b16 v[106:107], v129 offset:3200\n\t"
        "s_cmp_eq_u32 s44, s49\n\t"
        "s_cselect_b32 s44, s50, s44\n\t"
        "buffer_load_dwordx4 v[180:183], v120, s[40:43], s44 offen offset:0 sc1\n\t"
        "s_waitcnt lgkmcnt(10)\n\t"
        "v_mfma_f32_32x32x16_bf16 v[48:63], v[108:111], v[184:187], v[48:63]\n\t"
        "s_add_u32 s46, s46, 12288\n\t"
        "s_add_u32 s47, s47, 1\n\t"
        "ds_read_b64_tr_b16 v[108:109], v130 offset:128\n\t"
        "ds_read_b64_tr_b16 v[110:111], v130 offset:3200\n\t"
        "s_waitcnt lgkmcnt(10)\n\t"
        "v_mfma_f32_32x32x16_bf16 v[64:79], v[184:187], v[112:115], v[64:79]\n\t"
        "s_cmp_eq_u32 s46, 110592\n\t"
        "s_cselect_b32 s46, 0, s46\n\t"
        "ds_read_b64_tr_b16 v[112:113], v129 offset:256\n\t"
        "ds_read_b64_tr_b16 v[114:115], v129 offset:3328\n\t"
        "s_waitcnt lgkmcnt(10)\n\t"
        "v_mfma_f32_32x32x16_bf16 v[80:95], v[184:187], v[116:119], v[80:95]\n\t"
        "ds_write_b32 v127, v128\n\t"
        "ds_read_b64_tr_b16 v[116:117], v130 offset:256\n\t"
        "ds_read_b64_tr_b16 v[118:119], v130 offset:3328\n\t"
        "s_waitcnt vmcnt(14) lgkmcnt(11)\n\t"
        "v_mfma_f32_32x32x16_bf16 v[0:15], v[96:99], v[188:191], v[0:15]\n\t"
        "ds_read_b32 v126, v125\n\t"
        "ds_read_b64_tr_b16 v[96:97], v129 offset:6144\n\t"
        "ds_read_b64_tr_b16 v[98:99], v129 offset:9216\n\t"
        "s_waitcnt lgkmcnt(12)\n\t"
        "v_mfma_f32_32x32x16_bf16 v[16:31], v[100:103], v[188:191], v[16:31]\n\t"
        "v_add_u32 v123, s46, v121\n\t"
        "ds_read_b64_tr_b16 v[100:101], v130 offset:6144\n\t"
        "ds_read_b64_tr_b16 v[102:103], v130 offset:9216\n\t"
        "s_waitcnt lgkmcnt(12)\n\t"
        "v_mfma_f32_32x32x16_bf16 v[32:47], v[104:107], v[188:191], v[32:47]\n\t"
        "ds_read_b64_tr_b16 v[104:105], v129 offset:6272\n\t"
        "ds_read_b64_tr_b16 v[106:107], v129 offset:9344\n\t"
        "buffer_load_dwordx4 v[184:187], v120, s[40:43], s44 offen offset:1024 sc1\n\t"
        "s_waitcnt lgkmcnt(12)\n\t"
        "v_mfma_f32_32x32x16_bf16 v[48:63], v[108:111], v[188:191], v[48:63]\n\t"
        "v_add_u32 v124, s46, v122\n\t"
        "ds_read_b64_tr_b16 v[108:109], v130 offset:6272\n\t"
        "ds_read_b64_tr_b16 v[110:111], v130 offset:9344\n\t"
        "s_waitcnt lgkmcnt(12)\n\t"
        "v_mfma_f32_32x32x16_bf16 v[64:79], v[188:191], v[112:115], v[64:79]\n\t"
        "ds_read_b64_tr_b16 v[112:113], v129 offset:6400\n\t"
        "ds_read_b64_tr_b16 v[114:115], v129 offset:9472\n\t"
        "s_waitcnt lgkmcnt(10)\n\t"
        "v_mfma_f32_32x32x16_bf16 v[80:95], v[188:191], v[116:119], v[80:95]\n\t"
        "v_cmp_gt_u32 vcc, s47, v126\n\t"
        "ds_read_b64_tr_b16 v[116:117], v130 offset:6400\n\t"
        "ds_read_b64_tr_b16 v[118:119], v130 offset:9472\n\t"
        "s_waitcnt vmcnt(14) lgkmcnt(10)\n\t"
        "v_mfma_f32_32x32x16_bf16 v[0:15], v[96:99], v[192:195], v[0:15]\n\t"
        "s_cbranch_vccnz 26f\n\t"
        "27:\n\t"
        "ds_read_b64_tr_b16 v[96:97], v123 offset:0\n\t"
        "ds_read_b64_tr_b16 v[98:99], v123 offset:3072\n\t"
        "s_waitcnt lgkmcnt(10)\n\t"
        "v_mfma_f32_32x32x16_bf16 v[16:31], v[100:103], v[192:195], v[16:31]\n\t"
        "v_add_u32 v128, 1, v128\n\t"
        "ds_read_b64_tr_b16 v[100:101], v124 offset:0\n\t"
        "ds_read_b64_tr_b16 v[102:103], v124 offset:3072\n\t"
        "s_waitcnt lgkmcnt(10)\n\t"
        "v_mfma_f32_32x32x16_bf16 v[32:47], v[104:107], v[192:195], v[32:47]\n\t"
        "ds_read_b64_tr_b16 v[104:105], v123 offset:128\n\t"
        "ds_read_b64_tr_b16 v[106:107], v123 offset:3200\n\t"
        "buffer_load_dwordx4 v[188:191], v120, s[40:43], s44 offen offset:2048 sc1\n\t"
        "s_waitcnt lgkmcnt(10)\n\t"
        "v_mfma_f32_32x32x16_bf16 v[48:63], v[108:111], v[192:195], v[48:63]\n\t"
        "s_add_u32 s46, s46, 12288\n\t"
        "s_add_u32 s47, s47, 1\n\t"
        "ds_read_b64_tr_b16 v[108:109], v124 offset:128\n\t"
        "ds_read_b64_tr_b16 v[110:111], v124 offset:3200\n\t"
        "s_waitcnt lgkmcnt(10)\n\t"
        "v_mfma_f32_32x32x16_bf16 v[64:79], v[192:195], v[112:115], v[64:79]\n\t"
        "s_cmp_eq_u32 s46, 110592\n\t"
        "s_cselect_b32 s46, 0, s46\n\t"
        "ds_read_b64_tr_b16 v[112:113], v123 offset:256\n\t"
        "ds_read_b64_tr_b16 v[114:115], v123 offset:3328\n\t"
        "s_waitcnt lgkmcnt(10)\n\t"
        "v_mfma_f32_32x32x16_bf16 v[80:95], v[192:195], v[116:119], v[80:95]\n\t"
        "ds_write_b32 v127, v128\n\t"
        "ds_read_b64_tr_b16 v[116:117], v124 offset:256\n\t"
        "ds_read_b64_tr_b16 v[118:119], v124 offset:3328\n\t"
        "s_waitcnt vmcnt(14) lgkmcnt(11)\n\t"
        "v_mfma_f32_32x32x16_bf16 v[0:15], v[96:99], v[132:135], v[0:15]\n\t"
        "ds_read_b32 v126, v125\n\t"
        "ds_read_b64_tr_b16 v[96:97], v123 offset:6144\n\t"
        "ds_read_b64_tr_b16 v[98:99], v123 offset:9216\n\t"
        "s_waitcnt lgkmcnt(12)\n\t"
        "v_mfma_f32_32x32x16_bf16 v[16:31], v[100:103], v[132:135], v[16:31]\n\t"
        "v_add_u32 v129, s46, v121\n\t"
        "ds_read_b64_tr_b16 v[100:101], v124 offset:6144\n\t"
        "ds_read_b64_tr_b16 v[102:103], v124 offset:9216\n\t"
        "s_waitcnt lgkmcnt(12)\n\t"
        "v_mfma_f32_32x32x16_bf16 v[32:47], v[104:107], v[132:135], v[32:47]\n\t"
        "ds_read_b64_tr_b16 v[104:105], v123 offset:6272\n\t"
        "ds_read_b64_tr_b16 v[106:107], v123 offset:9344\n\t"
        "buffer_load_dwordx4 v[192:195], v120, s[40:43], s44 offen offset:3072 sc1\n\t"
        "s_waitcnt lgkmcnt(12)\n\t"
        "v_mfma_f32_32x32x16_bf16 v[48:63], v[108:111], v[132:135], v[48:63]\n\t"
        "v_add_u32 v130, s46, v122\n\t"
        "ds_read_b64_tr_b16 v[108:109], v124 offset:6272\n\t"
        "ds_read_b64_tr_b16 v[110:111], v124 offset:9344\n\t"
        "s_waitcnt lgkmcnt(12)\n\t"
        "v_mfma_f32_32x32x16_bf16 v[64:79], v[132:135], v[112:115], v[64:79]\n\t"
        "ds_read_b64_tr_b16 v[112:113], v123 offset:6400\n\t"
        "ds_read_b64_tr_b16 v[114:115], v123 offset:9472\n\t"
        "s_waitcnt lgkmcnt(10)\n\t"
        "v_mfma_f32_32x32x16_bf16 v[80:95], v[132:135], v[116:119], v[80:95]\n\t"
        "v_cmp_gt_u32 vcc, s47, v126\n\t"
        "ds_read_b64_tr_b16 v[116:117], v124 offset:6400\n\t"
        "ds_read_b64_tr_b16 v[118:119], v124 offset:9472\n\t"
        "s_waitcnt vmcnt(14) lgkmcnt(10)\n\t"
        "v_mfma_f32_32x32x16_bf16 v[0:15], v[96:99], v[136:139], v[0:15]\n\t"
        "s_cbranch_vccnz 28f\n\t"
        "29:\n\t"
        "ds_read_b64_tr_b16 v[96:97], v129 offset:0\n\t"
        "ds_read_b64_tr_b16 v[98:99], v129 offset:3072\n\t"
        "s_waitcnt lgkmcnt(10)\n\t"
        "v_mfma_f32_32x32x16_bf16 v[16:31], v[100:103], v[136:139], v[16:31]\n\t"
        "v_add_u32 v128, 1, v128\n\t"
        "ds_read_b64_tr_b16 v[100:101], v130 offset:0\n\t"
        "ds_read_b64_tr_b16 v[102:103], v130 offset:3072\n\t"
        "s_add_u32 s44, s44, 0x1000\n\t"
        "s_and_b32 s44, s44, 0xffff\n\t"
        "s_waitcnt lgkmcnt(10)\n\t"
        "v_mfma_f32_32x32x16_bf16 v[32:47], v[104:107], v[136:139], v[32:47]\n\t"
        "ds_read_b64_tr_b16 v[104:105], v129 offset:128\n\t"
        "ds_read_b64_tr_b16 v[106:107], v129 offset:3200\n\t"
        "s_cmp_eq_u32 s44, s49\n\t"
        "s_cselect_b32 s44, s50, s44\n\t"
        "buffer_load_dwordx4 v[132:135], v120, s[40:43], s44 offen offset:0 sc1\n\t"
        "s_waitcnt lgkmcnt(10)\n\t"
        "v_mfma_f32_32x32x16_bf16 v[48:63], v[108:111], v[136:139], v[48:63]\n\t"
        "s_add_u32 s46, s46, 12288\n\t"
        "s_add_u32 s47, s47, 1\n\t"
        "ds_read_b64_tr_b16 v[108:109], v130 offset:128\n\t"
        "ds_read_b64_tr_b16 v[110:111], v130 offset:3200\n\t"
        "s_waitcnt lgkmcnt(10)\n\t"
        "v_mfma_f32_32x32x16_bf16 v[64:79], v[136:139], v[112:115], v[64:79]\n\t"
        "s_cmp_eq_u32 s46, 110592\n\t"
        "s_cselect_b32 s46, 0, s46\n\t"
        "ds_read_b64_tr_b16 v[112:113], v129 offset:256\n\t"
        "ds_read_b64_tr_b16 v[114:115], v129 offset:3328\n\t"
        "s_waitcnt lgkmcnt(10)\n\t"
        "v_mfma_f32_32x32x16_bf16 v[80:95], v[136:139], v[116:119], v[80:95]\n\t"
        "ds_write_b32 v127, v128\n\t"
        "ds_read_b64_tr_b16 v[116:117], v130 offset:256\n\t"
        "ds_read_b64_tr_b16 v[118:119], v130 offset:3328\n\t"
        "s_waitcnt vmcnt(14) lgkmcnt(11)\n\t"
        "v_mfma_f32_32x32x16_bf16 v[0:15], v[96:99], v[140:143], v[0:15]\n\t"
        "ds_read_b32 v126, v125\n\t"
        "ds_read_b64_tr_b16 v[96:97], v129 offset:6144\n\t"
        "ds_read_b64_tr_b16 v[98:99], v129 offset:9216\n\t"
        "s_waitcnt lgkmcnt(12)\n\t"
        "v_mfma_f32_32x32x16_bf16 v[16:31], v[100:103], v[140:143], v[16:31]\n\t"
        "v_add_u32 v123, s46, v121\n\t"
        "ds_read_b64_tr_b16 v[100:101], v130 offset:6144\n\t"
        "ds_read_b64_tr_b16 v[102:103], v130 offset:9216\n\t"
        "s_waitcnt lgkmcnt(12)\n\t"
        "v_mfma_f32_32x32x16_bf16 v[32:47], v[104:107], v[140:143], v[32:47]\n\t"
        "ds_read_b64_tr_b16 v[104:105], v129 offset:6272\n\t"
        "ds_read_b64_tr_b16 v[106:107], v129 offset:9344\n\t"
        "buffer_load_dwordx4 v[136:139], v120, s[40:43], s44 offen offset:1024 sc1\n\t"
        "s_waitcnt lgkmcnt(12)\n\t"
        "v_mfma_f32_32x32x16_bf16 v[48:63], v[108:111], v[140:143], v[48:63]\n\t"
        "v_add_u32 v124, s46, v122\n\t"
        "ds_read_b64_tr_b16 v[108:109], v130 offset:6272\n\t"
        "ds_read_b64_tr_b16 v[110:111], v130 offset:9344\n\t"
        "s_waitcnt lgkmcnt(12)\n\t"
        "v_mfma_f32_32x32x16_bf16 v[64:79], v[140:143], v[112:115], v[64:79]\n\t"
        "ds_read_b64_tr_b16 v[112:113], v129 offset:6400\n\t"
        "ds_read_b64_tr_b16 v[114:115], v129 offset:9472\n\t"
        "s_waitcnt lgkmcnt(10)\n\t"
        "v_mfma_f32_32x32x16_bf16 v[80:95], v[140:143], v[116:119], v[80:95]\n\t"
        "v_cmp_gt_u32 vcc, s47, v126\n\t"
        "ds_read_b64_tr_b16 v[116:117], v130 offset:6400\n\t"
        "ds_read_b64_tr_b16 v[118:119], v130 offset:9472\n\t"
        "s_waitcnt vmcnt(14) lgkmcnt(10)\n\t"
        "v_mfma_f32_32x32x16_bf16 v[0:15], v[96:99], v[144:147], v[0:15]\n\t"
        "s_cbranch_vccnz 30f\n\t"
        "31:\n\t"
        "ds_read_b64_tr_b16 v[96:97], v123 offset:0\n\t"
        "ds_read_b64_tr_b16 v[98:99], v123 offset:3072\n\t"
        "s_waitcnt lgkmcnt(10)\n\t"
        "v_mfma_f32_32x32x16_bf16 v[16:31], v[100:103], v[144:147], v[16:31]\n\t"
        "v_add_u32 v128, 1, v128\n\t"
        "ds_read_b64_tr_b16 v[100:101], v124 offset:0\n\t"
        "ds_read_b64_tr_b16 v[102:103], v124 offset:3072\n\t"
        "s_waitcnt lgkmcnt(10)\n\t"
        "v_mfma_f32_32x32x16_bf16 v[32:47], v[104:107], v[144:147], v[32:47]\n\t"
        "ds_read_b64_tr_b16 v[104:105], v123 offset:128\n\t"
        "ds_read_b64_tr_b16 v[106:107], v123 offset:3200\n\t"
        "buffer_load_dwordx4 v[140:143], v120, s[40:43], s44 offen offset:2048 sc1\n\t"
        "s_waitcnt lgkmcnt(10)\n\t"
        "v_mfma_f32_32x32x16_bf16 v[48:63], v[108:111], v[144:147], v[48:63]\n\t"
        "s_add_u32 s46, s46, 12288\n\t"
        "s_add_u32 s47, s47, 1\n\t"
        "ds_read_b64_tr_b16 v[108:109], v124 offset:128\n\t"
        "ds_read_b64_tr_b16 v[110:111], v124 offset:3200\n\t"
        "s_waitcnt lgkmcnt(10)\n\t"
        "v_mfma_f32_32x32x16_bf16 v[64:79], v[144:147], v[112:115], v[64:79]\n\t"
        "s_cmp_eq_u32 s46, 110592\n\t"
        "s_cselect_b32 s46, 0, s46\n\t"
        "ds_read_b64_tr_b16 v[112:113], v123 offset:256\n\t"
        "ds_read_b64_tr_b16 v[114:115], v123 offset:3328\n\t"
        "s_waitcnt lgkmcnt(10)\n\t"
        "v_mfma_f32_32x32x16_bf16 v[80:95], v[144:147], v[116:119], v[80:95]\n\t"
        "ds_write_b32 v127, v128\n\t"
        "ds_read_b64_tr_b16 v[116:117], v124 offset:256\n\t"
        "ds_read_b64_tr_b16 v[118:119], v124 offset:3328\n\t"
        "s_sub_u32 s45, s45, 1\n\t"
        "s_cmp_lg_u32 s45, 0\n\t"
        "s_cbranch_scc1 1b\n\t"
        "s_waitcnt vmcnt(14) lgkmcnt(11)\n\t"
        "v_mfma_f32_32x32x16_bf16 v[0:15], v[96:99], v[148:151], v[0:15]\n\t"
        "ds_read_b32 v126, v125\n\t"
        "s_add_u32 s47, s47, 4\n\t"
        "ds_read_b64_tr_b16 v[96:97], v123 offset:6400\n\t"
        "ds_read_b64_tr_b16 v[98:99], v123 offset:9472\n\t"
        "s_waitcnt lgkmcnt(12)\n\t"
        "v_mfma_f32_32x32x16_bf16 v[16:31], v[100:103], v[148:151], v[16:31]\n\t"
        "ds_read_b64_tr_b16 v[100:101], v124 offset:6400\n\t"
        "ds_read_b64_tr_b16 v[102:103], v124 offset:9472\n\t"
        "s_waitcnt lgkmcnt(4)\n\t"
        "v_cmp_gt_u32 vcc, s47, v126\n\t"
        "v_mfma_f32_32x32x16_bf16 v[32:47], v[104:107], v[148:151], v[32:47]\n\t"
        "s_cbranch_vccnz 74f\n\t"
        "75:\n\t"
        "ds_read_b64_tr_b16 v[104:105], v121 offset:256\n\t"
        "ds_read_b64_tr_b16 v[106:107], v121 offset:3328\n\t"
        "s_waitcnt lgkmcnt(12)\n\t"
        "v_mfma_f32_32x32x16_bf16 v[48:63], v[108:111], v[148:151], v[48:63]\n\t"
        "ds_read_b64_tr_b16 v[108:109], v122 offset:256\n\t"
        "ds_read_b64_tr_b16 v[110:111], v122 offset:3328\n\t"
        "s_waitcnt lgkmcnt(12)\n\t"
        "v_mfma_f32_32x32x16_bf16 v[64:79], v[148:151], v[112:115], v[64:79]\n\t"
        "ds_read_b64_tr_b16 v[112:113], v121 offset:6400\n\t"
        "ds_read_b64_tr_b16 v[114:115], v121 offset:9472\n\t"
        "s_waitcnt lgkmcnt(11)\n\t"
        "v_mfma_f32_32x32x16_bf16 v[80:95], v[148:151], v[116:119], v[80:95]\n\t"
        "ds_read_b64_tr_b16 v[116:117], v122 offset:6400\n\t"
        "ds_read_b64_tr_b16 v[118:119], v122 offset:9472\n\t"
        "s_waitcnt vmcnt(13) lgkmcnt(10)\n\t"
        "v_mfma_f32_32x32x16_bf16 v[64:79], v[152:155], v[96:99], v[64:79]\n\t"
        "ds_read_b64_tr_b16 v[96:97], v121 offset:12544\n\t"
        "ds_read_b64_tr_b16 v[98:99], v121 offset:15616\n\t"
        "s_waitcnt lgkmcnt(10)\n\t"
        "v_mfma_f32_32x32x16_bf16 v[80:95], v[152:155], v[100:103], v[80:95]\n\t"
        "ds_read_b64_tr_b16 v[100:101], v122 offset:12544\n\t"
        "ds_read_b64_tr_b16 v[102:103], v122 offset:15616\n\t"
        "s_waitcnt vmcnt(12) lgkmcnt(10)\n\t"
        "v_mfma_f32_32x32x16_bf16 v[64:79], v[156:159], v[104:107], v[64:79]\n\t"
        "ds_read_b128 v[196:199], v215 offset:0\n\t"
        "ds_read_b64_tr_b16 v[104:105], v121 offset:18688\n\t"
        "ds_read_b64_tr_b16 v[106:107], v121 offset:21760\n\t"
        "s_waitcnt lgkmcnt(11)\n\t"
        "v_mfma_f32_32x32x16_bf16 v[80:95], v[156:159], v[108:111], v[80:95]\n\t"
        "ds_read_b128 v[200:203], v215 offset:32\n\t"
        "ds_read_b64_tr_b16 v[108:109], v122 offset:18688\n\t"
        "ds_read_b64_tr_b16 v[110:111], v122 offset:21760\n\t"
        "s_waitcnt vmcnt(11) lgkmcnt(12)\n\t"
        "v_mfma_f32_32x32x16_bf16 v[64:79], v[160:163], v[112:115], v[64:79]\n\t"
        "ds_read_b128 v[204:207], v215 offset:64\n\t"
        "ds_read_b64_tr_b16 v[112:113], v121 offset:24832\n\t"
        "ds_read_b64_tr_b16 v[114:115], v121 offset:27904\n\t"
        "s_waitcnt lgkmcnt(13)\n\t"
        "v_mfma_f32_32x32x16_bf16 v[80:95], v[160:163], v[116:119], v[80:95]\n\t"
        "ds_read_b128 v[208:211], v215 offset:96\n\t"
        "ds_read_b64_tr_b16 v[116:117], v122 offset:24832\n\t"
        "ds_read_b64_tr_b16 v[118:119], v122 offset:27904\n\t"
        "s_waitcnt vmcnt(10) lgkmcnt(14)\n\t"
        "v_mfma_f32_32x32x16_bf16 v[64:79], v[164:167], v[96:99], v[64:79]\n\t"
        "ds_read_b64_tr_b16 v[96:97], v121 offset:30976\n\t"
        "ds_read_b64_tr_b16 v[98:99], v121 offset:34048\n\t"
        "s_waitcnt lgkmcnt(14)\n\t"
        "v_mfma_f32_32x32x16_bf16 v[80:95], v[164:167], v[100:103], v[80:95]\n\t"
        "ds_read_b64_tr_b16 v[100:101], v122 offset:30976\n\t"
        "ds_read_b64_tr_b16 v[102:103], v122 offset:34048\n\t"
        "s_waitcnt vmcnt(9) lgkmcnt(13)\n\t"
        "v_mfma_f32_32x32x16_bf16 v[64:79], v[168:171], v[104:107], v[64:79]\n\t"
        "ds_read_b64_tr_b16 v[104:105], v121 offset:37120\n\t"
        "ds_read_b64_tr_b16 v[106:107], v121 offset:40192\n\t"
        "s_waitcnt lgkmcnt(12)\n\t"
        "v_mfma_f32_32x32x16_bf16 v[80:95], v[168:171], v[108:111], v[80:95]\n\t"
        "ds_read_b64_tr_b16 v[108:109], v122 offset:37120\n\t"
        "ds_read_b64_tr_b16 v[110:111], v122 offset:40192\n\t"
        "s_waitcnt vmcnt(8) lgkmcnt(11)\n\t"
        "v_mfma_f32_32x32x16_bf16 v[64:79], v[172:175], v[112:115], v[64:79]\n\t"
        "ds_read_b64_tr_b16 v[112:113], v121 offset:43264\n\t"
        "ds_read_b64_tr_b16 v[114:115], v121 offset:46336\n\t"
        "s_waitcnt lgkmcnt(10)\n\t"
        "v_mfma_f32_32x32x16_bf16 v[80:95], v[172:175], v[116:119], v[80:95]\n\t"
        "ds_read_b64_tr_b16 v[116:117], v122 offset:43264\n\t"
        "ds_read_b64_tr_b16 v[118:119], v122 offset:46336\n\t"
        "s_waitcnt vmcnt(7) lgkmcnt(10)\n\t"
        "v_mfma_f32_32x32x16_bf16 v[64:79], v[176:179], v[96:99], v[64:79]\n\t"
        "ds_read_b64_tr_b16 v[96:97], v121 offset:49408\n\t"
        "ds_read_b64_tr_b16 v[98:99], v121 offset:52480\n\t"
        "s_waitcnt lgkmcnt(10)\n\t"
        "v_mfma_f32_32x32x16_bf16 v[80:95], v[176:179], v[100:103], v[80:95]\n\t"
        "ds_read_b64_tr_b16 v[100:101], v122 offset:49408\n\t"
        "ds_read_b64_tr_b16 v[102:103], v122 offset:52480\n\t"
        "s_waitcnt vmcnt(6) lgkmcnt(10)\n\t"
        "v_mfma_f32_32x32x16_bf16 v[64:79], v[180:183], v[104:107], v[64:79]\n\t"
        "ds_read_b64_tr_b16 v[104:105], v121 offset:55552\n\t"
        "ds_read_b64_tr_b16 v[106:107], v121 offset:58624\n\t"
        "s_waitcnt lgkmcnt(10)\n\t"
        "v_mfma_f32_32x32x16_bf16 v[80:95], v[180:183], v[108:111], v[80:95]\n\t"
        "ds_read_b64_tr_b16 v[108:109], v122 offset:55552\n\t"
        "ds_read_b64_tr_b16 v[110:111], v122 offset:58624\n\t"
        "s_waitcnt vmcnt(5) lgkmcnt(10)\n\t"
        "v_mfma_f32_32x32x16_bf16 v[64:79], v[184:187], v[112:115], v[64:79]\n\t"
        "ds_read_b64_tr_b16 v[112:113], v123 offset:6272\n\t"
        "ds_read_b64_tr_b16 v[114:115], v123 offset:9344\n\t"
        "s_waitcnt lgkmcnt(10)\n\t"
        "v_mfma_f32_32x32x16_bf16 v[80:95], v[184:187], v[116:119], v[80:95]\n\t"
        "ds_read_b64_tr_b16 v[116:117], v124 offset:6272\n\t"
        "ds_read_b64_tr_b16 v[118:119], v124 offset:9344\n\t"
        "s_waitcnt vmcnt(4) lgkmcnt(10)\n\t"
        "v_mfma_f32_32x32x16_bf16 v[64:79], v[188:191], v[96:99], v[64:79]\n\t"
        "ds_read_b64_tr_b16 v[96:97], v121 offset:128\n\t"
        "ds_read_b64_tr_b16 v[98:99], v121 offset:3200\n\t"
        "s_waitcnt lgkmcnt(10)\n\t"
        "v_mfma_f32_32x32x16_bf16 v[80:95], v[188:191], v[100:103], v[80:95]\n\t"
        "ds_read_b64_tr_b16 v[100:101], v122 offset:128\n\t"
        "ds_read_b64_tr_b16 v[102:103], v122 offset:3200\n\t"
        "s_waitcnt vmcnt(0) lgkmcnt(10)\n\t"
        "v_mfma_f32_32x32x16_bf16 v[64:79], v[192:195], v[104:107], v[64:79]\n\t"
        "ds_read_b64_tr_b16 v[104:105], v121 offset:6272\n\t"
        "ds_read_b64_tr_b16 v[106:107], v121 offset:9344\n\t"
        "s_waitcnt lgkmcnt(10)\n\t"
        "v_mfma_f32_32x32x16_bf16 v[80:95], v[192:195], v[108:111], v[80:95]\n\t"
        "ds_read_b64_tr_b16 v[108:109], v122 offset:6272\n\t"
        "ds_read_b64_tr_b16 v[110:111], v122 offset:9344\n\t"
        "s_waitcnt lgkmcnt(10)\n\t"
        "v_mfma_f32_32x32x16_bf16 v[32:47], v[112:115], v[152:155], v[32:47]\n\t"
        "ds_read_b64_tr_b16 v[112:113], v121 offset:12416\n\t"
        "ds_read_b64_tr_b16 v[114:115], v121 offset:15488\n\t"
        "s_waitcnt lgkmcnt(10)\n\t"
        "v_mfma_f32_32x32x16_bf16 v[48:63], v[116:119], v[152:155], v[48:63]\n\t"
        "ds_read_b64_tr_b16 v[116:117], v122 offset:12416\n\t"
        "ds_read_b64_tr_b16 v[118:119], v122 offset:15488\n\t"
        "v_mul_f32 v64, v196, v64\n\t"
        "v_mul_f32 v65, v197, v65\n\t"
        "v_mul_f32 v66, v198, v66\n\t"
        "s_waitcnt lgkmcnt(10)\n\t"
        "v_mfma_f32_32x32x16_bf16 v[32:47], v[96:99], v[156:159], v[32:47]\n\t"
        "ds_read_b64_tr_b16 v[96:97], v121 offset:18560\n\t"
        "ds_read_b64_tr_b16 v[98:99], v121 offset:21632\n\t"
        "v_mul_f32 v67, v199, v67\n\t"
        "v_mul_f32 v68, v200, v68\n\t"
        "v_mul_f32 v69, v201, v69\n\t"
        "s_waitcnt lgkmcnt(10)\n\t"
        "v_mfma_f32_32x32x16_bf16 v[48:63], v[100:103], v[156:159], v[48:63]\n\t"
        "ds_read_b64_tr_b16 v[100:101], v122 offset:18560\n\t"
        "ds_read_b64_tr_b16 v[102:103], v122 offset:21632\n\t"
        "v_mul_f32 v70, v202, v70\n\t"
        "v_mul_f32 v71, v203, v71\n\t"
        "v_cvt_pk_bf16_f32 v64, v64, v65\n\t"
        "s_waitcnt lgkmcnt(10)\n\t"
        "v_mfma_f32_32x32x16_bf16 v[32:47], v[104:107], v[160:163], v[32:47]\n\t"
        "ds_read_b64_tr_b16 v[104:105], v121 offset:24704\n\t"
        "ds_read_b64_tr_b16 v[106:107], v121 offset:27776\n\t"
        "v_cvt_pk_bf16_f32 v65, v66, v67\n\t"
        "v_cvt_pk_bf16_f32 v66, v68, v69\n\t"
        "v_cvt_pk_bf16_f32 v67, v70, v71\n\t"
        "s_waitcnt lgkmcnt(10)\n\t"
        "v_mfma_f32_32x32x16_bf16 v[48:63], v[108:111], v[160:163], v[48:63]\n\t"
        "ds_read_b64_tr_b16 v[108:109], v122 offset:24704\n\t"
        "ds_read_b64_tr_b16 v[110:111], v122 offset:27776\n\t"
        "v_mul_f32 v72, v204, v72\n\t"
        "v_mul_f32 v73, v205, v73\n\t"
        "v_mul_f32 v74, v206, v74\n\t"
        "s_waitcnt lgkmcnt(10)\n\t"
        "v_mfma_f32_32x32x16_bf16 v[32:47], v[112:115], v[164:167], v[32:47]\n\t"
        "ds_read_b64_tr_b16 v[112:113], v121 offset:30848\n\t"
        "ds_read_b64_tr_b16 v[114:115], v121 offset:33920\n\t"
        "v_mul_f32 v75, v207, v75\n\t"
        "v_mul_f32 v76, v208, v76\n\t"
        "v_mul_f32 v77, v209, v77\n\t"
        "s_waitcnt lgkmcnt(10)\n\t"
        "v_mfma_f32_32x32x16_bf16 v[48:63], v[116:119], v[164:167], v[48:63]\n\t"
        "ds_read_b64_tr_b16 v[116:117], v122 offset:30848\n\t"
        "ds_read_b64_tr_b16 v[118:119], v122 offset:33920\n\t"
        "v_mul_f32 v78, v210, v78\n\t"
        "v_mul_f32 v79, v211, v79\n\t"
        "v_cvt_pk_bf16_f32 v68, v72, v73\n\t"
        "s_waitcnt lgkmcnt(10)\n\t"
        "v_mfma_f32_32x32x16_bf16 v[32:47], v[96:99], v[168:171], v[32:47]\n\t"
        "ds_read_b64_tr_b16 v[96:97], v121 offset:36992\n\t"
        "ds_read_b64_tr_b16 v[98:99], v121 offset:40064\n\t"
        "v_cvt_pk_bf16_f32 v69, v74, v75\n\t"
        "v_cvt_pk_bf16_f32 v70, v76, v77\n\t"
        "v_cvt_pk_bf16_f32 v71, v78, v79\n\t"
        "s_waitcnt lgkmcnt(10)\n\t"
        "v_mfma_f32_32x32x16_bf16 v[48:63], v[100:103], v[168:171], v[48:63]\n\t"
        "ds_read_b64_tr_b16 v[100:101], v122 offset:36992\n\t"
        "ds_read_b64_tr_b16 v[102:103], v122 offset:40064\n\t"
        "v_permlane32_swap_b32 v64, v66\n\t"
        "v_permlane32_swap_b32 v65, v67\n\t"
        "v_permlane32_swap_b32 v68, v70\n\t"
        "s_waitcnt lgkmcnt(10)\n\t"
        "v_mfma_f32_32x32x16_bf16 v[32:47], v[104:107], v[172:175], v[32:47]\n\t"
        "ds_read_b64_tr_b16 v[104:105], v121 offset:43136\n\t"
        "ds_read_b64_tr_b16 v[106:107], v121 offset:46208\n\t"
        "v_permlane32_swap_b32 v69, v71\n\t"
        "buffer_store_dwordx4 v[64:67], v213, s[40:43], 0 offen offset:2048 sc1\n\t"
        "buffer_store_dwordx4 v[68:71], v213, s[40:43], 0 offen offset:3072 sc1\n\t"
        "s_waitcnt lgkmcnt(10)\n\t"
        "v_mfma_f32_32x32x16_bf16 v[48:63], v[108:111], v[172:175], v[48:63]\n\t"
        "ds_read_b64_tr_b16 v[108:109], v122 offset:43136\n\t"
        "ds_read_b64_tr_b16 v[110:111], v122 offset:46208\n\t"
        "v_mul_f32 v80, v196, v80\n\t"
        "v_mul_f32 v81, v197, v81\n\t"
        "v_mul_f32 v82, v198, v82\n\t"
        "s_waitcnt lgkmcnt(10)\n\t"
        "v_mfma_f32_32x32x16_bf16 v[32:47], v[112:115], v[176:179], v[32:47]\n\t"
        "ds_read_b64_tr_b16 v[112:113], v121 offset:49280\n\t"
        "ds_read_b64_tr_b16 v[114:115], v121 offset:52352\n\t"
        "v_mul_f32 v83, v199, v83\n\t"
        "v_mul_f32 v84, v200, v84\n\t"
        "v_mul_f32 v85, v201, v85\n\t"
        "s_waitcnt lgkmcnt(10)\n\t"
        "v_mfma_f32_32x32x16_bf16 v[48:63], v[116:119], v[176:179], v[48:63]\n\t"
        "ds_read_b64_tr_b16 v[116:117], v122 offset:49280\n\t"
        "ds_read_b64_tr_b16 v[118:119], v122 offset:52352\n\t"
        "v_mul_f32 v86, v202, v86\n\t"
        "v_mul_f32 v87, v203, v87\n\t"
        "v_cvt_pk_bf16_f32 v80, v80, v81\n\t"
        "s_waitcnt lgkmcnt(10)\n\t"
        "v_mfma_f32_32x32x16_bf16 v[32:47], v[96:99], v[180:183], v[32:47]\n\t"
        "ds_read_b64_tr_b16 v[96:97], v121 offset:55424\n\t"
        "ds_read_b64_tr_b16 v[98:99], v121 offset:58496\n\t"
        "v_cvt_pk_bf16_f32 v81, v82, v83\n\t"
        "v_cvt_pk_bf16_f32 v82, v84, v85\n\t"
        "v_cvt_pk_bf16_f32 v83, v86, v87\n\t"
        "s_waitcnt lgkmcnt(10)\n\t"
        "v_mfma_f32_32x32x16_bf16 v[48:63], v[100:103], v[180:183], v[48:63]\n\t"
        "ds_read_b64_tr_b16 v[100:101], v122 offset:55424\n\t"
        "ds_read_b64_tr_b16 v[102:103], v122 offset:58496\n\t"
        "v_mul_f32 v88, v204, v88\n\t"
        "v_mul_f32 v89, v205, v89\n\t"
        "v_mul_f32 v90, v206, v90\n\t"
        "s_waitcnt lgkmcnt(10)\n\t"
        "v_mfma_f32_32x32x16_bf16 v[32:47], v[104:107], v[184:187], v[32:47]\n\t"
        "ds_read_b64_tr_b16 v[104:105], v123 offset:6144\n\t"
        "ds_read_b64_tr_b16 v[106:107], v123 offset:9216\n\t"
        "v_mul_f32 v91, v207, v91\n\t"
        "v_mul_f32 v92, v208, v92\n\t"
        "v_mul_f32 v93, v209, v93\n\t"
        "s_waitcnt lgkmcnt(10)\n\t"
        "v_mfma_f32_32x32x16_bf16 v[48:63], v[108:111], v[184:187], v[48:63]\n\t"
        "ds_read_b64_tr_b16 v[108:109], v124 offset:6144\n\t"
        "ds_read_b64_tr_b16 v[110:111], v124 offset:9216\n\t"
        "v_mul_f32 v94, v210, v94\n\t"
        "v_mul_f32 v95, v211, v95\n\t"
        "v_cvt_pk_bf16_f32 v84, v88, v89\n\t"
        "s_waitcnt lgkmcnt(10)\n\t"
        "v_mfma_f32_32x32x16_bf16 v[32:47], v[112:115], v[188:191], v[32:47]\n\t"
        "ds_read_b64_tr_b16 v[112:113], v121 offset:0\n\t"
        "ds_read_b64_tr_b16 v[114:115], v121 offset:3072\n\t"
        "v_cvt_pk_bf16_f32 v85, v90, v91\n\t"
        "v_cvt_pk_bf16_f32 v86, v92, v93\n\t"
        "v_cvt_pk_bf16_f32 v87, v94, v95\n\t"
        "s_waitcnt lgkmcnt(10)\n\t"
        "v_mfma_f32_32x32x16_bf16 v[48:63], v[116:119], v[188:191], v[48:63]\n\t"
        "ds_read_b64_tr_b16 v[116:117], v122 offset:0\n\t"
        "ds_read_b64_tr_b16 v[118:119], v122 offset:3072\n\t"
        "v_permlane32_swap_b32 v80, v82\n\t"
        "v_permlane32_swap_b32 v81, v83\n\t"
        "v_permlane32_swap_b32 v84, v86\n\t"
        "s_waitcnt lgkmcnt(10)\n\t"
        "v_mfma_f32_32x32x16_bf16 v[32:47], v[96:99], v[192:195], v[32:47]\n\t"
        "ds_read_b64_tr_b16 v[96:97], v121 offset:6144\n\t"
        "ds_read_b64_tr_b16 v[98:99], v121 offset:9216\n\t"
        "v_permlane32_swap_b32 v85, v87\n\t"
        "buffer_store_dwordx4 v[80:83], v214, s[40:43], 0 offen offset:2048 sc1\n\t"
        "buffer_store_dwordx4 v[84:87], v214, s[40:43], 0 offen offset:3072 sc1\n\t"
        "s_waitcnt lgkmcnt(10)\n\t"
        "v_mfma_f32_32x32x16_bf16 v[48:63], v[100:103], v[192:195], v[48:63]\n\t"
        "ds_read_b64_tr_b16 v[100:101], v122 offset:6144\n\t"
        "ds_read_b64_tr_b16 v[102:103], v122 offset:9216\n\t"
        "s_waitcnt lgkmcnt(10)\n\t"
        "v_mfma_f32_32x32x16_bf16 v[0:15], v[104:107], v[152:155], v[0:15]\n\t"
        "ds_read_b64_tr_b16 v[104:105], v121 offset:12288\n\t"
        "ds_read_b64_tr_b16 v[106:107], v121 offset:15360\n\t"
        "s_waitcnt lgkmcnt(10)\n\t"
        "v_mfma_f32_32x32x16_bf16 v[16:31], v[108:111], v[152:155], v[16:31]\n\t"
        "ds_read_b64_tr_b16 v[108:109], v122 offset:12288\n\t"
        "ds_read_b64_tr_b16 v[110:111], v122 offset:15360\n\t"
        "v_mul_f32 v32, v212, v32\n\t"
        "v_mul_f32 v33, v212, v33\n\t"
        "v_mul_f32 v34, v212, v34\n\t"
        "s_waitcnt lgkmcnt(10)\n\t"
        "v_mfma_f32_32x32x16_bf16 v[0:15], v[112:115], v[156:159], v[0:15]\n\t"
        "ds_read_b64_tr_b16 v[112:113], v121 offset:18432\n\t"
        "ds_read_b64_tr_b16 v[114:115], v121 offset:21504\n\t"
        "v_mul_f32 v35, v212, v35\n\t"
        "v_mul_f32 v36, v212, v36\n\t"
        "v_mul_f32 v37, v212, v37\n\t"
        "s_waitcnt lgkmcnt(10)\n\t"
        "v_mfma_f32_32x32x16_bf16 v[16:31], v[116:119], v[156:159], v[16:31]\n\t"
        "ds_read_b64_tr_b16 v[116:117], v122 offset:18432\n\t"
        "ds_read_b64_tr_b16 v[118:119], v122 offset:21504\n\t"
        "v_mul_f32 v38, v212, v38\n\t"
        "v_mul_f32 v39, v212, v39\n\t"
        "v_cvt_pk_bf16_f32 v32, v32, v33\n\t"
        "s_waitcnt lgkmcnt(10)\n\t"
        "v_mfma_f32_32x32x16_bf16 v[0:15], v[96:99], v[160:163], v[0:15]\n\t"
        "ds_read_b64_tr_b16 v[96:97], v121 offset:24576\n\t"
        "ds_read_b64_tr_b16 v[98:99], v121 offset:27648\n\t"
        "v_cvt_pk_bf16_f32 v33, v34, v35\n\t"
        "v_cvt_pk_bf16_f32 v34, v36, v37\n\t"
        "v_cvt_pk_bf16_f32 v35, v38, v39\n\t"
        "s_waitcnt lgkmcnt(10)\n\t"
        "v_mfma_f32_32x32x16_bf16 v[16:31], v[100:103], v[160:163], v[16:31]\n\t"
        "ds_read_b64_tr_b16 v[100:101], v122 offset:24576\n\t"
        "ds_read_b64_tr_b16 v[102:103], v122 offset:27648\n\t"
        "v_mul_f32 v40, v212, v40\n\t"
        "v_mul_f32 v41, v212, v41\n\t"
        "v_mul_f32 v42, v212, v42\n\t"
        "s_waitcnt lgkmcnt(10)\n\t"
        "v_mfma_f32_32x32x16_bf16 v[0:15], v[104:107], v[164:167], v[0:15]\n\t"
        "ds_read_b64_tr_b16 v[104:105], v121 offset:30720\n\t"
        "ds_read_b64_tr_b16 v[106:107], v121 offset:33792\n\t"
        "v_mul_f32 v43, v212, v43\n\t"
        "v_mul_f32 v44, v212, v44\n\t"
        "v_mul_f32 v45, v212, v45\n\t"
        "s_waitcnt lgkmcnt(10)\n\t"
        "v_mfma_f32_32x32x16_bf16 v[16:31], v[108:111], v[164:167], v[16:31]\n\t"
        "ds_read_b64_tr_b16 v[108:109], v122 offset:30720\n\t"
        "ds_read_b64_tr_b16 v[110:111], v122 offset:33792\n\t"
        "v_mul_f32 v46, v212, v46\n\t"
        "v_mul_f32 v47, v212, v47\n\t"
        "v_cvt_pk_bf16_f32 v36, v40, v41\n\t"
        "s_waitcnt lgkmcnt(10)\n\t"
        "v_mfma_f32_32x32x16_bf16 v[0:15], v[112:115], v[168:171], v[0:15]\n\t"
        "ds_read_b64_tr_b16 v[112:113], v121 offset:36864\n\t"
        "ds_read_b64_tr_b16 v[114:115], v121 offset:39936\n\t"
        "v_cvt_pk_bf16_f32 v37, v42, v43\n\t"
        "v_cvt_pk_bf16_f32 v38, v44, v45\n\t"
        "v_cvt_pk_bf16_f32 v39, v46, v47\n\t"
        "s_waitcnt lgkmcnt(10)\n\t"
        "v_mfma_f32_32x32x16_bf16 v[16:31], v[116:119], v[168:171], v[16:31]\n\t"
        "ds_read_b64_tr_b16 v[116:117], v122 offset:36864\n\t"
        "ds_read_b64_tr_b16 v[118:119], v122 offset:39936\n\t"
        "buffer_store_dwordx4 v[32:35], v213, s[40:43], 0 offen offset:0 sc1\n\t"
        "buffer_store_dwordx4 v[36:39], v213, s[40:43], 0 offen offset:1024 sc1\n\t"
        "v_mul_f32 v48, v212, v48\n\t"
        "s_waitcnt lgkmcnt(10)\n\t"
        "v_mfma_f32_32x32x16_bf16 v[0:15], v[96:99], v[172:175], v[0:15]\n\t"
        "ds_read_b64_tr_b16 v[96:97], v121 offset:43008\n\t"
        "ds_read_b64_tr_b16 v[98:99], v121 offset:46080\n\t"
        "v_mul_f32 v49, v212, v49\n\t"
        "v_mul_f32 v50, v212, v50\n\t"
        "v_mul_f32 v51, v212, v51\n\t"
        "s_waitcnt lgkmcnt(10)\n\t"
        "v_mfma_f32_32x32x16_bf16 v[16:31], v[100:103], v[172:175], v[16:31]\n\t"
        "ds_read_b64_tr_b16 v[100:101], v122 offset:43008\n\t"
        "ds_read_b64_tr_b16 v[102:103], v122 offset:46080\n\t"
        "v_mul_f32 v52, v212, v52\n\t"
        "v_mul_f32 v53, v212, v53\n\t"
        "v_mul_f32 v54, v212, v54\n\t"
        "s_waitcnt lgkmcnt(10)\n\t"
        "v_mfma_f32_32x32x16_bf16 v[0:15], v[104:107], v[176:179], v[0:15]\n\t"
        "ds_read_b64_tr_b16 v[104:105], v121 offset:49152\n\t"
        "ds_read_b64_tr_b16 v[106:107], v121 offset:52224\n\t"
        "v_mul_f32 v55, v212, v55\n\t"
        "v_cvt_pk_bf16_f32 v48, v48, v49\n\t"
        "v_cvt_pk_bf16_f32 v49, v50, v51\n\t"
        "s_waitcnt lgkmcnt(10)\n\t"
        "v_mfma_f32_32x32x16_bf16 v[16:31], v[108:111], v[176:179], v[16:31]\n\t"
        "ds_read_b64_tr_b16 v[108:109], v122 offset:49152\n\t"
        "ds_read_b64_tr_b16 v[110:111], v122 offset:52224\n\t"
        "v_cvt_pk_bf16_f32 v50, v52, v53\n\t"
        "v_cvt_pk_bf16_f32 v51, v54, v55\n\t"
        "v_mul_f32 v56, v212, v56\n\t"
        "s_waitcnt lgkmcnt(10)\n\t"
        "v_mfma_f32_32x32x16_bf16 v[0:15], v[112:115], v[180:183], v[0:15]\n\t"
        "ds_read_b64_tr_b16 v[112:113], v121 offset:55296\n\t"
        "ds_read_b64_tr_b16 v[114:115], v121 offset:58368\n\t"
        "v_mul_f32 v57, v212, v57\n\t"
        "v_mul_f32 v58, v212, v58\n\t"
        "v_mul_f32 v59, v212, v59\n\t"
        "s_waitcnt lgkmcnt(10)\n\t"
        "v_mfma_f32_32x32x16_bf16 v[16:31], v[116:119], v[180:183], v[16:31]\n\t"
        "ds_read_b64_tr_b16 v[116:117], v122 offset:55296\n\t"
        "ds_read_b64_tr_b16 v[118:119], v122 offset:58368\n\t"
        "v_mul_f32 v60, v212, v60\n\t"
        "v_mul_f32 v61, v212, v61\n\t"
        "v_mul_f32 v62, v212, v62\n\t"
        "s_waitcnt lgkmcnt(10)\n\t"
        "v_mfma_f32_32x32x16_bf16 v[0:15], v[96:99], v[184:187], v[0:15]\n\t"
        "v_mul_f32 v63, v212, v63\n\t"
        "v_cvt_pk_bf16_f32 v52, v56, v57\n\t"
        "v_cvt_pk_bf16_f32 v53, v58, v59\n\t"
        "v_cvt_pk_bf16_f32 v54, v60, v61\n\t"
        "v_cvt_pk_bf16_f32 v55, v62, v63\n\t"
        "s_waitcnt lgkmcnt(8)\n\t"
        "v_mfma_f32_32x32x16_bf16 v[16:31], v[100:103], v[184:187], v[16:31]\n\t"
        "buffer_store_dwordx4 v[48:51], v214, s[40:43], 0 offen offset:0 sc1\n\t"
        "buffer_store_dwordx4 v[52:55], v214, s[40:43], 0 offen offset:1024 sc1\n\t"
        "s_waitcnt lgkmcnt(6)\n\t"
        "v_mfma_f32_32x32x16_bf16 v[0:15], v[104:107], v[188:191], v[0:15]\n\t"
        "s_waitcnt lgkmcnt(4)\n\t"
        "v_mfma_f32_32x32x16_bf16 v[16:31], v[108:111], v[188:191], v[16:31]\n\t"
        "s_waitcnt lgkmcnt(2)\n\t"
        "v_mfma_f32_32x32x16_bf16 v[0:15], v[112:115], v[192:195], v[0:15]\n\t"
        "s_waitcnt lgkmcnt(0)\n\t"
        "v_mfma_f32_32x32x16_bf16 v[16:31], v[116:119], v[192:195], v[16:31]\n\t"
        "s_nop 11\n\t"
        "s_branch 9f\n\t"
        "16:\n\t"
        "s_mov_b32 s48, 0x10000\n\t"
        "8:\n\t"
        "ds_read_b32 v126, v125\n\t"
        "s_sub_u32 s48, s48, 1\n\t"
        "s_waitcnt lgkmcnt(0)\n\t"
        "s_cbranch_scc1 17b\n\t"
        "v_cmp_gt_u32 vcc, s47, v126\n\t"
        "s_cbranch_vccnz 8b\n\t"
        "s_branch 17b\n\t"
        "18:\n\t"
        "s_mov_b32 s48, 0x10000\n\t"
        "8:\n\t"
        "ds_read_b32 v126, v125\n\t"
        "s_sub_u32 s48, s48, 1\n\t"
        "s_waitcnt lgkmcnt(0)\n\t"
        "s_cbranch_scc1 19b\n\t"
        "v_cmp_gt_u32 vcc, s47, v126\n\t"
        "s_cbranch_vccnz 8b\n\t"
        "s_branch 19b\n\t"
        "20:\n\t"
        "s_mov_b32 s48, 0x10000\n\t"
        "8:\n\t"
        "ds_read_b32 v126, v125\n\t"
        "s_sub_u32 s48, s48, 1\n\t"
        "s_waitcnt lgkmcnt(0)\n\t"
        "s_cbranch_scc1 21b\n\t"
        "v_cmp_gt_u32 vcc, s47, v126\n\t"
        "s_cbranch_vccnz 8b\n\t"
        "s_branch 21b\n\t"
        "22:\n\t"
        "s_mov_b32 s48, 0x10000\n\t"
        "8:\n\t"
        "ds_read_b32 v126, v125\n\t"
        "s_sub_u32 s48, s48, 1\n\t"
        "s_waitcnt lgkmcnt(0)\n\t"
        "s_cbranch_scc1 23b\n\t"
        "v_cmp_gt_u32 vcc, s47, v126\n\t"
        "s_cbranch_vccnz 8b\n\t"
        "s_branch 23b\n\t"
        "24:\n\t"
        "s_mov_b32 s48, 0x10000\n\t"
        "8:\n\t"
        "ds_read_b32 v126, v125\n\t"
        "s_sub_u32 s48, s48, 1\n\t"
        "s_waitcnt lgkmcnt(0)\n\t"
        "s_cbranch_scc1 25b\n\t"
        "v_cmp_gt_u32 vcc, s47, v126\n\t"
        "s_cbranch_vccnz 8b\n\t"
        "s_branch 25b\n\t"
        "26:\n\t"
        "s_mov_b32 s48, 0x10000\n\t"
        "8:\n\t"
        "ds_read_b32 v126, v125\n\t"
        "s_sub_u32 s48, s48, 1\n\t"
        "s_waitcnt lgkmcnt(0)\n\t"
        "s_cbranch_scc1 27b\n\t"
        "v_cmp_gt_u32 vcc, s47, v126\n\t"
        "s_cbranch_vccnz 8b\n\t"
        "s_branch 27b\n\t"
        "28:\n\t"
        "s_mov_b32 s48, 0x10000\n\t"
        "8:\n\t"
        "ds_read_b32 v126, v125\n\t"
        "s_sub_u32 s48, s48, 1\n\t"
        "s_waitcnt lgkmcnt(0)\n\t"
        "s_cbranch_scc1 29b\n\t"
        "v_cmp_gt_u32 vcc, s47, v126\n\t"
        "s_cbranch_vccnz 8b\n\t"
        "s_branch 29b\n\t"
        "30:\n\t"
        "s_mov_b32 s48, 0x10000\n\t"
        "8:\n\t"
        "ds_read_b32 v126, v125\n\t"
        "s_sub_u32 s48, s48, 1\n\t"
        "s_waitcnt lgkmcnt(0)\n\t"
        "s_cbranch_scc1 31b\n\t"
        "v_cmp_gt_u32 vcc, s47, v126\n\t"
        "s_cbranch_vccnz 8b\n\t"
        "s_branch 31b\n\t"
        "74:\n\t"
        "s_mov_b32 s48, 0x10000\n\t"
        "8:\n\t"
        "ds_read_b32 v126, v125\n\t"
        "s_sub_u32 s48, s48, 1\n\t"
        "s_waitcnt lgkmcnt(0)\n\t"
        "s_cbranch_scc1 75b\n\t"
        "v_cmp_gt_u32 vcc, s47, v126\n\t"
        "s_cbranch_vccnz 8b\n\t"
        "s_branch 75b\n\t"
        "9:\n\t"
        : "+{v[0:15]}"(acc[0]), "+{v[16:31]}"(acc[1]), "+{v[32:47]}"(acc[2]), "+{v[48:63]}"(acc[3]), "+{v[64:79]}"(acc[4]), "+{v[80:95]}"(acc[5]),
          "+{v[96:111]}"(stA), "+{v[112:127]}"(stB), "+{v[128:131]}"(stC), "+{v[148:163]}"(xr1), "+{v[164:179]}"(xr2), "+{v[180:195]}"(xr3),
          "+{s44}"(soff), "+{s46}"(slot), "+{s47}"(need)
        : "{s[40:43]}"(srd), KSKIP, "{v[212:215]}"(ep) : "v196", "v197", "v198", "v199", "v200", "v201", "v202", "v203", "v204", "v205", "v206", "v207", "v208", "v209", "v210", "v211", "v132", "v133", "v134", "v135", "v136", "v137", "v138", "v139", "v140", "v141", "v142", "v143", "v144", "v145", "v146", "v147", "s45", "s48", "vcc", "scc", "memory");
      STAMPV(4 + 16 * L);
      PHASE_VARS;
      u32x4 kv[4][2];
#pragma unroll
      for (int j = 0; j < 4; ++j)
#pragma unroll
        for (int e = 0; e < 8; ++e) kv[j][e >> 2][e & 3] = __float_as_uint(acc[2 + j][e]);
      u32x4 qo[2][2];
      auto drained = [&]() { asm volatile("s_waitcnt vmcnt(0)\n\tds_write_b32 %0, %1" :: "v"(lbase + L_W + 80 + 4 * wid), "v"(2 * L + 1) : "memory"); };
      if (L == 0) {
#pragma unroll
        for (int e = 0; e < 16; ++e) {
          if (e == 8) { SBAR(); drained(); SBAR(); }
          const unsigned w = pack2(acc[e >> 3][2 * (e & 7)], acc[e >> 3][2 * (e & 7) + 1]);
          qo[e >> 3][(e >> 2) & 1][e & 3] = pack2(lo16(w) * QS, hi16(w) * QS);
        }
      } else {
        const float aq = U2F(ep[0]) * QS;
#pragma unroll
        for (int e = 0; e < 16; ++e) qo[e >> 3][(e >> 2) & 1][e & 3] = pack2(acc[e >> 3][2 * (e & 7)] * aq, acc[e >> 3][2 * (e & 7) + 1] * aq);
        SBAR();
        lds_wait_ge<0>(lbase + L_W + 4 + 4 * (lane & 1), 1);
        const int idx = (int)__builtin_amdgcn_readfirstlane(words[3]);
        if ((idx >> 5) == tile) {
          const int rel = (idx & 31) - 8 * hi, dfo = (r32 & 16) + ((r32 >> 2) & 1) * 8 + ((r32 >> 3) & 1) * 4 + (r32 & 3);
          unsigned pv[2];
#pragma unroll
          for (int j = 0; j < 2; ++j) pv[j] = reinterpret_cast<const u16*>(smem + L_PATCH)[128 + (j ^ hh) * 32 + dfo] * 0x10001u;
          DRAIN();
          if (r32 == (idx & 31)) {
#pragma unroll
            for (int i = 0; i < 4; ++i) {
              const char* p = smem + L_PATCH + ((i >> 1) ^ hh) * 64 + (i & 1) * 32 + hi * 16;
              qo[i >> 1][i & 1] = *reinterpret_cast<const u32x4*>(p);
              kv[i >> 1][i & 1] = *reinterpret_cast<const u32x4*>(p + 128);
              x1p[i] = *reinterpret_cast<const u32x4*>(smem + L_X2P + (i * 2 + hi) * 16);
            }
            reinterpret_cast<float*>(smem + L_AB)[256 + wid * 64 + lane] = hi ? 1.0f : 0.0f;
#pragma unroll
            for (int i = 0; i < 4; ++i) stg(rs, ep[1 + (i >> 1)] + (i & 1) * 1024, kv[i >> 1][i & 1]);
          }
#pragma unroll
          for (int i = 0; i < 4; ++i) {
#pragma unroll
            for (int e = 0; e < 4; ++e) {
              const int t0 = 16 * (i & 1) + 2 * e;
              const unsigned mask = rel == t0 ? 0xffffu : rel == t0 + 1 ? 0xffff0000u : 0u;
              kv[2 + (i >> 1)][i & 1][e] = (kv[2 + (i >> 1)][i & 1][e] & ~mask) | (pv[i >> 1] & mask);
            }
            stg(rs, (rel >> 3) == 2 * (i & 1) ? ep[1 + (i >> 1)] + 2048 + (i & 1) * 1024 : OOB, kv[2 + (i >> 1)][i & 1]);
          }
        }
        drained();
      }
      SBAR();
#pragma unroll
      for (int j = 0; j < 2; ++j) {
        char* p = smem + L_XCH + ((j ^ hh) * 4 + wid) * 2048 + lane * 16;
        *reinterpret_cast<u32x4*>(p) = qo[j][0]; *reinterpret_cast<u32x4*>(p + 1024) = qo[j][1];
        p = smem + L_XCH + 16384 + wid * 4096 + j * 2048 + lane * 16;
        *reinterpret_cast<u32x4*>(p) = kv[2 * j + 1][0]; *reinterpret_cast<u32x4*>(p + 1024) = kv[2 * j + 1][1];
      }
      STAMPV(41 + 4 * L);
      BAR_LGKM();
      STAMPV(43 + 4 * L);
      kva = cat4(kv[0][0], kv[0][1], kv[2][0], kv[2][1]);
      const u32x4* pk = reinterpret_cast<const u32x4*>(smem + L_XCH + 16384 + (wid ^ 1) * 4096 + lane * 16);
#pragma unroll
      for (int u = 0; u < 4; ++u) {
        const u32x4* p = reinterpret_cast<const u32x4*>(smem + L_XCH + (hh * 4 + ((2 * kh + 2 + u) & 3)) * 2048 + lane * 16);
        q[u] = __builtin_shufflevector(p[0], p[64], 0, 1, 2, 3, 4, 5, 6, 7);
      }
      kvb = cat4(pk[0], pk[64], pk[128], pk[192]);
      SBAR();
      asm volatile(
        "v_mov_b64 v[0:1], 0\n\t"
        "v_mov_b64 v[2:3], 0\n\t"
        "v_mov_b64 v[4:5], 0\n\t"
        "v_mov_b64 v[6:7], 0\n\t"
        "v_mov_b64 v[8:9], 0\n\t"
        "v_mov_b64 v[10:11], 0\n\t"
        "v_mov_b64 v[12:13], 0\n\t"
        "v_mov_b64 v[14:15], 0\n\t"
        "v_mov_b64 v[16:17], 0\n\t"
        "v_mov_b64 v[18:19], 0\n\t"
        "v_mov_b64 v[20:21], 0\n\t"
        "v_mov_b64 v[22:23], 0\n\t"
        "v_mov_b64 v[24:25], 0\n\t"
        "v_mov_b64 v[26:27], 0\n\t"
        "v_mov_b64 v[28:29], 0\n\t"
        "v_mov_b64 v[30:31], 0\n\t"
        "v_mov_b64 v[32:33], 0\n\t"
        "v_mov_b64 v[34:35], 0\n\t"
        "v_mov_b64 v[36:37], 0\n\t"
        "v_mov_b64 v[38:39], 0\n\t"
        "v_mov_b64 v[40:41], 0\n\t"
        "v_mov_b64 v[42:43], 0\n\t"
        "v_mov_b64 v[44:45], 0\n\t"
        "v_mov_b64 v[46:47], 0\n\t"
        "v_mov_b64 v[48:49], 0\n\t"
        "v_mov_b64 v[50:51], 0\n\t"
        "v_mov_b64 v[52:53], 0\n\t"
        "v_mov_b64 v[54:55], 0\n\t"
        "v_mov_b64 v[56:57], 0\n\t"
        "v_mov_b64 v[58:59], 0\n\t"
        "v_mov_b64 v[60:61], 0\n\t"
        "v_mov_b64 v[62:63], 0\n\t"
        "v_mov_b64 v[128:129], 0\n\t"
        "v_mov_b64 v[130:131], 0\n\t"
        "v_mov_b64 v[132:133], 0\n\t"
        "v_mov_b64 v[134:135], 0\n\t"
        "v_mov_b64 v[136:137], 0\n\t"
        "v_mov_b64 v[138:139], 0\n\t"
        "v_mov_b64 v[140:141], 0\n\t"
        "v_mov_b64 v[142:143], 0\n\t"
        : "={v[0:15]}"(oz[0]), "={v[16:31]}"(oz[1]), "={v[32:47]}"(oz[2]), "={v[48:63]}"(oz[3]), "={v[128:143]}"(oz[4]));
    }
    STAMPV(5 + 16 * L);
    {
      u32x4 ones;
      unsigned kvoff;
      u32x8 fv, cv;
      asm volatile("" : "={v[184:191]}"(fv), "={v[216:223]}"(cv));
      { PHASE_VARS;
        const unsigned v = (lane & 3) == 0 ? 0x3F803F80u : 0u; const bool up = (lane & 4) != 0; ones[0] = ones[1] = up ? 0u : v; ones[2] = ones[3] = up ? v : 0u;
        kvoff = OFF_KV + L * (10 * MBY) + (tn * 2 + hh) * HS + kh * 8192 + lane * 16;
        fv[1] = lbase + L_FREE + wid * 256 + lane * 4; fv[2] = 32 * L + 32;
        cv[0] = lbase + L_XCH + wid * 8192 + lane * 16; cv[1] = lbase + L_XCH + (wid ^ 2) * 8192 + lane * 16;
        cv[2] = lbase + L_XCH + 32768 + wid * 512 + lane * 4; cv[3] = lbase + L_XCH + 32768 + (wid ^ 2) * 512 + lane * 4;
        cv[4] = lbase + L_W + 48 + wid * 4; cv[5] = lbase + L_W + 48 + (wid ^ 2) * 4; cv[6] = L + 1;
        cv[7] = ((tile * 32 + r32) * D_ + tn * 64 + hi * 8) * 2; }
      f32x16 o0 = oz[0], o1 = oz[1], o2 = oz[2], o3 = oz[3], lv = oz[4];
      unsigned soff = (tm | 7) * 16384;
      STAMP_DECL;
      asm volatile(
        "s_nop 1\n\t"
        "v_mfma_f32_32x32x16_bf16 v[96:111], v[152:155], v[64:67], 0\n\t"
        "v_mfma_f32_32x32x16_bf16 v[96:111], v[156:159], v[68:71], v[96:111]\n\t"
        "s_mov_b32 s46, 0\n\t"
        "s_mov_b64 s[54:55], s[42:43]\n\t"
        "ds_write_b32 v185, v186\n\t"
        "s_nop 8\n\t"
        "v_exp_f32 v96, v96\n\t"
        "v_exp_f32 v97, v97\n\t"
        "v_exp_f32 v98, v98\n\t"
        "v_exp_f32 v99, v99\n\t"
        "v_exp_f32 v100, v100\n\t"
        "v_exp_f32 v101, v101\n\t"
        "v_exp_f32 v102, v102\n\t"
        "v_mfma_f32_32x32x16_bf16 v[112:127], v[152:155], v[72:75], 0\n\t"
        "v_exp_f32 v103, v103\n\t"
        "v_exp_f32 v104, v104\n\t"
        "v_exp_f32 v105, v105\n\t"
        "v_mfma_f32_32x32x16_bf16 v[112:127], v[156:159], v[76:79], v[112:127]\n\t"
        "v_exp_f32 v106, v106\n\t"
        "v_exp_f32 v107, v107\n\t"
        "v_exp_f32 v108, v108\n\t"
        "v_smfmac_f32_32x32x32_bf16 v[0:15], v[160:163], v[96:103], v148\n\t"
        "v_exp_f32 v109, v109\n\t"
        "v_exp_f32 v110, v110\n\t"
        "v_exp_f32 v111, v111\n\t"
        "v_smfmac_f32_16x16x64_bf16 v[128:131], v[144:147], v[96:103], v148\n\t"
        "v_exp_f32 v112, v112\n\t"
        "v_exp_f32 v113, v113\n\t"
        "v_smfmac_f32_32x32x32_bf16 v[0:15], v[164:167], v[104:111], v148\n\t"
        "v_exp_f32 v114, v114\n\t"
        "v_exp_f32 v115, v115\n\t"
        "v_exp_f32 v116, v116\n\t"
        "v_smfmac_f32_16x16x64_bf16 v[128:131], v[144:147], v[104:111], v148\n\t"
        "v_exp_f32 v117, v117\n\t"
        "v_exp_f32 v118, v118\n\t"
        "v_mfma_f32_32x32x16_bf16 v[96:111], v[152:155], v[80:83], 0\n\t"
        "v_exp_f32 v119, v119\n\t"
        "v_exp_f32 v120, v120\n\t"
        "v_exp_f32 v121, v121\n\t"
        "v_mfma_f32_32x32x16_bf16 v[96:111], v[156:159], v[84:87], v[96:111]\n\t"
        "ds_read_b32 v151, v224\n\t"
        "v_exp_f32 v122, v122\n\t"
        "v_exp_f32 v123, v123\n\t"
        "v_exp_f32 v124, v124\n\t"
        "v_smfmac_f32_32x32x32_bf16 v[16:31], v[160:163], v[112:119], v148\n\t"
        "v_exp_f32 v125, v125\n\t"
        "v_exp_f32 v126, v126\n\t"
        "v_exp_f32 v127, v127\n\t"
        "v_smfmac_f32_16x16x64_bf16 v[132:135], v[144:147], v[112:119], v148\n\t"
        "s_cmp_lg_u32 s46, 0\n\t"
        "s_cbranch_scc1 25f\n\t"
        "s_waitcnt lgkmcnt(0)\n\t"
        "v_cmp_ne_u32 vcc, v222, v151\n\t"
        "s_cbranch_vccz 24f\n\t"
        "25:\n\t"
        "v_exp_f32 v96, v96\n\t"
        "v_exp_f32 v97, v97\n\t"
        "v_smfmac_f32_32x32x32_bf16 v[16:31], v[164:167], v[120:127], v148\n\t"
        "v_exp_f32 v98, v98\n\t"
        "v_exp_f32 v99, v99\n\t"
        "v_exp_f32 v100, v100\n\t"
        "v_smfmac_f32_16x16x64_bf16 v[132:135], v[144:147], v[120:127], v148\n\t"
        "v_exp_f32 v101, v101\n\t"
        "v_exp_f32 v102, v102\n\t"
        "v_mfma_f32_32x32x16_bf16 v[112:127], v[152:155], v[88:91], 0\n\t"
        "v_exp_f32 v103, v103\n\t"
        "v_exp_f32 v104, v104\n\t"
        "v_exp_f32 v105, v105\n\t"
        "v_mfma_f32_32x32x16_bf16 v[112:127], v[156:159], v[92:95], v[112:127]\n\t"
        "ds_read_b32 v151, v224\n\t"
        "v_exp_f32 v106, v106\n\t"
        "v_exp_f32 v107, v107\n\t"
        "v_exp_f32 v108, v108\n\t"
        "v_smfmac_f32_32x32x32_bf16 v[32:47], v[160:163], v[96:103], v148\n\t"
        "v_exp_f32 v109, v109\n\t"
        "v_exp_f32 v110, v110\n\t"
        "v_exp_f32 v111, v111\n\t"
        "v_smfmac_f32_16x16x64_bf16 v[136:139], v[144:147], v[96:103], v148\n\t"
        "s_cmp_lg_u32 s46, 0\n\t"
        "s_cbranch_scc1 27f\n\t"
        "s_waitcnt lgkmcnt(0)\n\t"
        "v_cmp_ne_u32 vcc, v222, v151\n\t"
        "s_cbranch_vccz 26f\n\t"
        "27:\n\t"
        "v_exp_f32 v112, v112\n\t"
        "v_exp_f32 v113, v113\n\t"
        "v_smfmac_f32_32x32x32_bf16 v[32:47], v[164:167], v[104:111], v148\n\t"
        "v_exp_f32 v114, v114\n\t"
        "v_exp_f32 v115, v115\n\t"
        "v_exp_f32 v116, v116\n\t"
        "v_smfmac_f32_16x16x64_bf16 v[136:139], v[144:147], v[104:111], v148\n\t"
        "v_exp_f32 v117, v117\n\t"
        "v_exp_f32 v118, v118\n\t"
        "v_mfma_f32_32x32x16_bf16 v[96:111], v[168:171], v[64:67], 0\n\t"
        "v_exp_f32 v119, v119\n\t"
        "v_exp_f32 v120, v120\n\t"
        "v_exp_f32 v121, v121\n\t"
        "v_mfma_f32_32x32x16_bf16 v[96:111], v[172:175], v[68:71], v[96:111]\n\t"
        "ds_read_b32 v151, v224\n\t"
        "v_exp_f32 v122, v122\n\t"
        "v_exp_f32 v123, v123\n\t"
        "v_exp_f32 v124, v124\n\t"
        "v_smfmac_f32_32x32x32_bf16 v[48:63], v[160:163], v[112:119], v148\n\t"
        "v_exp_f32 v125, v125\n\t"
        "v_exp_f32 v126, v126\n\t"
        "v_exp_f32 v127, v127\n\t"
        "v_smfmac_f32_16x16x64_bf16 v[140:143], v[144:147], v[112:119], v148\n\t"
        "s_cmp_lg_u32 s46, 0\n\t"
        "s_cbranch_scc1 29f\n\t"
        "s_waitcnt lgkmcnt(0)\n\t"
        "v_cmp_ne_u32 vcc, v222, v151\n\t"
        "s_cbranch_vccz 28f\n\t"
        "29:\n\t"
        "v_exp_f32 v96, v96\n\t"
        "v_exp_f32 v97, v97\n\t"
        "v_smfmac_f32_32x32x32_bf16 v[48:63], v[164:167], v[120:127], v148\n\t"
        "v_exp_f32 v98, v98\n\t"
        "v_exp_f32 v99, v99\n\t"
        "v_exp_f32 v100, v100\n\t"
        "v_smfmac_f32_16x16x64_bf16 v[140:143], v[144:147], v[120:127], v148\n\t"
        "v_exp_f32 v101, v101\n\t"
        "v_exp_f32 v102, v102\n\t"
        "v_mfma_f32_32x32x16_bf16 v[112:127], v[168:171], v[72:75], 0\n\t"
        "v_exp_f32 v103, v103\n\t"
        "v_exp_f32 v104, v104\n\t"
        "v_exp_f32 v105, v105\n\t"
        "v_mfma_f32_32x32x16_bf16 v[112:127], v[172:175], v[76:79], v[112:127]\n\t"
        "ds_read_b32 v151, v224\n\t"
        "v_exp_f32 v106, v106\n\t"
        "v_exp_f32 v107, v107\n\t"
        "v_exp_f32 v108, v108\n\t"
        "v_smfmac_f32_32x32x32_bf16 v[0:15], v[176:179], v[96:103], v148\n\t"
        "v_exp_f32 v109, v109\n\t"
        "v_exp_f32 v110, v110\n\t"
        "v_exp_f32 v111, v111\n\t"
        "v_smfmac_f32_16x16x64_bf16 v[128:131], v[144:147], v[96:103], v148\n\t"
        "s_cmp_lg_u32 s46, 0\n\t"
        "s_cbranch_scc1 31f\n\t"
        "s_waitcnt lgkmcnt(0)\n\t"
        "v_cmp_ne_u32 vcc, v222, v151\n\t"
        "s_cbranch_vccz 30f\n\t"
        "31:\n\t"
        "v_exp_f32 v112, v112\n\t"
        "v_exp_f32 v113, v113\n\t"
        "v_smfmac_f32_32x32x32_bf16 v[0:15], v[180:183], v[104:111], v148\n\t"
        "v_exp_f32 v114, v114\n\t"
        "v_exp_f32 v115, v115\n\t"
        "v_exp_f32 v116, v116\n\t"
        "v_smfmac_f32_16x16x64_bf16 v[128:131], v[144:147], v[104:111], v148\n\t"
        "v_exp_f32 v117, v117\n\t"
        "v_exp_f32 v118, v118\n\t"
        "v_mfma_f32_32x32x16_bf16 v[96:111], v[168:171], v[80:83], 0\n\t"
        "v_exp_f32 v119, v119\n\t"
        "v_exp_f32 v120, v120\n\t"
        "v_exp_f32 v121, v121\n\t"
        "v_mfma_f32_32x32x16_bf16 v[96:111], v[172:175], v[84:87], v[96:111]\n\t"
        "ds_read_b32 v151, v224\n\t"
        "v_exp_f32 v122, v122\n\t"
        "v_exp_f32 v123, v123\n\t"
        "v_exp_f32 v124, v124\n\t"
        "v_smfmac_f32_32x32x32_bf16 v[16:31], v[176:179], v[112:119], v148\n\t"
        "v_exp_f32 v125, v125\n\t"
        "v_exp_f32 v126, v126\n\t"
        "v_exp_f32 v127, v127\n\t"
        "v_smfmac_f32_16x16x64_bf16 v[132:135], v[144:147], v[112:119], v148\n\t"
        "s_cmp_lg_u32 s46, 0\n\t"
        "s_cbranch_scc1 33f\n\t"
        "s_waitcnt lgkmcnt(0)\n\t"
        "v_cmp_ne_u32 vcc, v222, v151\n\t"
        "s_cbranch_vccz 32f\n\t"
        "33:\n\t"
        "v_exp_f32 v96, v96\n\t"
        "v_exp_f32 v97, v97\n\t"
        "v_smfmac_f32_32x32x32_bf16 v[16:31], v[180:183], v[120:127], v148\n\t"
        "v_exp_f32 v98, v98\n\t"
        "v_exp_f32 v99, v99\n\t"
        "v_exp_f32 v100, v100\n\t"
        "v_smfmac_f32_16x16x64_bf16 v[132:135], v[144:147], v[120:127], v148\n\t"
        "v_exp_f32 v101, v101\n\t"
        "v_exp_f32 v102, v102\n\t"
        "v_mfma_f32_32x32x16_bf16 v[112:127], v[168:171], v[88:91], 0\n\t"
        "v_exp_f32 v103, v103\n\t"
        "v_exp_f32 v104, v104\n\t"
        "v_exp_f32 v105, v105\n\t"
        "v_mfma_f32_32x32x16_bf16 v[112:127], v[172:175], v[92:95], v[112:127]\n\t"
        "v_exp_f32 v106, v106\n\t"
        "v_exp_f32 v107, v107\n\t"
        "v_exp_f32 v108, v108\n\t"
        "v_smfmac_f32_32x32x32_bf16 v[32:47], v[176:179], v[96:103], v148\n\t"
        "v_exp_f32 v109, v109\n\t"
        "v_exp_f32 v110, v110\n\t"
        "v_exp_f32 v111, v111\n\t"
        "v_smfmac_f32_16x16x64_bf16 v[136:139], v[144:147], v[96:103], v148\n\t"
        "s_cmp_lg_u32 s46, 0\n\t"
        "s_cbranch_scc1 5f\n\t"
        "s_mov_b32 s45, 0x200000\n\t"
        "2:\n\t"
        "ds_read_b32 v151, v224\n\t"
        "s_sub_u32 s45, s45, 1\n\t"
        "s_waitcnt lgkmcnt(0)\n\t"
        "s_cbranch_scc1 3f\n\t"
        "v_cmp_ne_u32 vcc, v222, v151\n\t"
        "s_cbranch_vccnz 2b\n\t"
        "3:\n\t"
        "s_add_u32 s44, s44, 0x4000\n\t"
        "s_and_b32 s44, s44, 0x3ffff\n\t"
        "s_cmp_eq_u32 s44, s49\n\t"
        "s_cselect_b32 s44, s50, s44\n\t"
        "s_add_u32 s52, s40, s44\n\t"
        "s_addc_u32 s53, s41, 0\n\t"
        "buffer_load_dwordx4 v[184:187], v149, s[52:55], 0 offen offset:0 sc1\n\t"
        "buffer_load_dwordx4 v[188:191], v149, s[52:55], 0 offen offset:1024 sc1\n\t"
        "buffer_load_dwordx4 v[192:195], v149, s[52:55], 0 offen offset:2048 sc1\n\t"
        "buffer_load_dwordx4 v[196:199], v149, s[52:55], 0 offen offset:3072 sc1\n\t"
        "buffer_load_dwordx4 v[200:203], v150, s[52:55], 0 offen offset:0 sc1\n\t"
        "buffer_load_dwordx4 v[204:207], v150, s[52:55], 0 offen offset:1024 sc1\n\t"
        "buffer_load_dwordx4 v[208:211], v150, s[52:55], 0 offen offset:2048 sc1\n\t"
        "buffer_load_dwordx4 v[212:215], v150, s[52:55], 0 offen offset:3072 sc1\n\t"
        "5:\n\t"
        "s_add_u32 s44, s44, 0x4000\n\t"
        "s_and_b32 s44, s44, 0x3ffff\n\t"
        "s_cmp_eq_u32 s44, s49\n\t"
        "s_cselect_b32 s44, s50, s44\n\t"
        "s_add_u32 s52, s40, s44\n\t"
        "s_addc_u32 s53, s41, 0\n\t"
        "buffer_load_dwordx4 v[152:155], v149, s[52:55], 0 offen offset:0 sc1\n\t"
        "buffer_load_dwordx4 v[156:159], v149, s[52:55], 0 offen offset:1024 sc1\n\t"
        "buffer_load_dwordx4 v[160:163], v149, s[52:55], 0 offen offset:2048 sc1\n\t"
        "buffer_load_dwordx4 v[164:167], v149, s[52:55], 0 offen offset:3072 sc1\n\t"
        "s_mov_b32 s45, 6\n\t"
        "1:\n\t"
        "v_exp_f32 v112, v112\n\t"
        "v_exp_f32 v113, v113\n\t"
        "v_smfmac_f32_32x32x32_bf16 v[32:47], v[180:183], v[104:111], v148\n\t"
        "v_exp_f32 v114, v114\n\t"
        "v_exp_f32 v115, v115\n\t"
        "v_exp_f32 v116, v116\n\t"
        "v_smfmac_f32_16x16x64_bf16 v[136:139], v[144:147], v[104:111], v148\n\t"
        "s_waitcnt vmcnt(8)\n\t"
        "v_exp_f32 v117, v117\n\t"
        "v_exp_f32 v118, v118\n\t"
        "v_mfma_f32_32x32x16_bf16 v[96:111], v[184:187], v[64:67], 0\n\t"
        "v_exp_f32 v119, v119\n\t"
        "v_exp_f32 v120, v120\n\t"
        "v_exp_f32 v121, v121\n\t"
        "v_mfma_f32_32x32x16_bf16 v[96:111], v[188:191], v[68:71], v[96:111]\n\t"
        "v_exp_f32 v122, v122\n\t"
        "v_exp_f32 v123, v123\n\t"
        "v_exp_f32 v124, v124\n\t"
        "v_smfmac_f32_32x32x32_bf16 v[48:63], v[176:179], v[112:119], v148\n\t"
        "v_exp_f32 v125, v125\n\t"
        "v_exp_f32 v126, v126\n\t"
        "v_exp_f32 v127, v127\n\t"
        "v_smfmac_f32_16x16x64_bf16 v[140:143], v[144:147], v[112:119], v148\n\t"
        "buffer_load_dwordx4 v[168:171], v150, s[52:55], 0 offen offset:0 sc1\n\t"
        "v_exp_f32 v96, v96\n\t"
        "v_exp_f32 v97, v97\n\t"
        "v_smfmac_f32_32x32x32_bf16 v[48:63], v[180:183], v[120:127], v148\n\t"
        "v_exp_f32 v98, v98\n\t"
        "v_exp_f32 v99, v99\n\t"
        "v_exp_f32 v100, v100\n\t"
        "v_smfmac_f32_16x16x64_bf16 v[140:143], v[144:147], v[120:127], v148\n\t"
        "v_exp_f32 v101, v101\n\t"
        "v_exp_f32 v102, v102\n\t"
        "v_mfma_f32_32x32x16_bf16 v[112:127], v[184:187], v[72:75], 0\n\t"
        "v_exp_f32 v103, v103\n\t"
        "v_exp_f32 v104, v104\n\t"
        "v_exp_f32 v105, v105\n\t"
        "v_mfma_f32_32x32x16_bf16 v[112:127], v[188:191], v[76:79], v[112:127]\n\t"
        "v_exp_f32 v106, v106\n\t"
        "v_exp_f32 v107, v107\n\t"
        "v_exp_f32 v108, v108\n\t"
        "v_smfmac_f32_32x32x32_bf16 v[0:15], v[192:195], v[96:103], v148\n\t"
        "v_exp_f32 v109, v109\n\t"
        "v_exp_f32 v110, v110\n\t"
        "v_exp_f32 v111, v111\n\t"
        "v_smfmac_f32_16x16x64_bf16 v[128:131], v[144:147], v[96:103], v148\n\t"
        "buffer_load_dwordx4 v[172:175], v150, s[52:55], 0 offen offset:1024 sc1\n\t"
        "v_exp_f32 v112, v112\n\t"
        "v_exp_f32 v113, v113\n\t"
        "v_smfmac_f32_32x32x32_bf16 v[0:15], v[196:199], v[104:111], v148\n\t"
        "v_exp_f32 v114, v114\n\t"
        "v_exp_f32 v115, v115\n\t"
        "v_exp_f32 v116, v116\n\t"
        "v_smfmac_f32_16x16x64_bf16 v[128:131], v[144:147], v[104:111], v148\n\t"
        "v_exp_f32 v117, v117\n\t"
        "v_exp_f32 v118, v118\n\t"
        "v_mfma_f32_32x32x16_bf16 v[96:111], v[184:187], v[80:83], 0\n\t"
        "v_exp_f32 v119, v119\n\t"
        "v_exp_f32 v120, v120\n\t"
        "v_exp_f32 v121, v121\n\t"
        "v_mfma_f32_32x32x16_bf16 v[96:111], v[188:191], v[84:87], v[96:111]\n\t"
        "v_exp_f32 v122, v122\n\t"
        "v_exp_f32 v123, v123\n\t"
        "v_exp_f32 v124, v124\n\t"
        "v_smfmac_f32_32x32x32_bf16 v[16:31], v[192:195], v[112:119], v148\n\t"
        "v_exp_f32 v125, v125\n\t"
        "v_exp_f32 v126, v126\n\t"
        "v_exp_f32 v127, v127\n\t"
        "v_smfmac_f32_16x16x64_bf16 v[132:135], v[144:147], v[112:119], v148\n\t"
        "buffer_load_dwordx4 v[176:179], v150, s[52:55], 0 offen offset:2048 sc1\n\t"
        "v_exp_f32 v96, v96\n\t"
        "v_exp_f32 v97, v97\n\t"
        "v_smfmac_f32_32x32x32_bf16 v[16:31], v[196:199], v[120:127], v148\n\t"
        "v_exp_f32 v98, v98\n\t"
        "v_exp_f32 v99, v99\n\t"
        "v_exp_f32 v100, v100\n\t"
        "v_smfmac_f32_16x16x64_bf16 v[132:135], v[144:147], v[120:127], v148\n\t"
        "v_exp_f32 v101, v101\n\t"
        "v_exp_f32 v102, v102\n\t"
        "v_mfma_f32_32x32x16_bf16 v[112:127], v[184:187], v[88:91], 0\n\t"
        "v_exp_f32 v103, v103\n\t"
        "v_exp_f32 v104, v104\n\t"
        "v_exp_f32 v105, v105\n\t"
        "v_mfma_f32_32x32x16_bf16 v[112:127], v[188:191], v[92:95], v[112:127]\n\t"
        "v_exp_f32 v106, v106\n\t"
        "v_exp_f32 v107, v107\n\t"
        "v_exp_f32 v108, v108\n\t"
        "v_smfmac_f32_32x32x32_bf16 v[32:47], v[192:195], v[96:103], v148\n\t"
        "v_exp_f32 v109, v109\n\t"
        "v_exp_f32 v110, v110\n\t"
        "v_exp_f32 v111, v111\n\t"
        "v_smfmac_f32_16x16x64_bf16 v[136:139], v[144:147], v[96:103], v148\n\t"
        "buffer_load_dwordx4 v[180:183], v150, s[52:55], 0 offen offset:3072 sc1\n\t"
        "v_exp_f32 v112, v112\n\t"
        "v_exp_f32 v113, v113\n\t"
        "v_smfmac_f32_32x32x32_bf16 v[32:47], v[196:199], v[104:111], v148\n\t"
        "s_add_u32 s44, s44, 0x4000\n\t"
        "s_and_b32 s44, s44, 0x3ffff\n\t"
        "v_exp_f32 v114, v114\n\t"
        "v_exp_f32 v115, v115\n\t"
        "v_exp_f32 v116, v116\n\t"
        "v_smfmac_f32_16x16x64_bf16 v[136:139], v[144:147], v[104:111], v148\n\t"
        "s_waitcnt vmcnt(8)\n\t"
        "v_exp_f32 v117, v117\n\t"
        "v_exp_f32 v118, v118\n\t"
        "v_mfma_f32_32x32x16_bf16 v[96:111], v[200:203], v[64:67], 0\n\t"
        "s_cmp_eq_u32 s44, s49\n\t"
        "v_exp_f32 v119, v119\n\t"
        "v_exp_f32 v120, v120\n\t"
        "v_exp_f32 v121, v121\n\t"
        "v_mfma_f32_32x32x16_bf16 v[96:111], v[204:207], v[68:71], v[96:111]\n\t"
        "s_cselect_b32 s44, s50, s44\n\t"
        "v_exp_f32 v122, v122\n\t"
        "v_exp_f32 v123, v123\n\t"
        "v_exp_f32 v124, v124\n\t"
        "v_smfmac_f32_32x32x32_bf16 v[48:63], v[192:195], v[112:119], v148\n\t"
        "s_add_u32 s52, s40, s44\n\t"
        "s_addc_u32 s53, s41, 0\n\t"
        "v_exp_f32 v125, v125\n\t"
        "v_exp_f32 v126, v126\n\t"
        "v_exp_f32 v127, v127\n\t"
        "v_smfmac_f32_16x16x64_bf16 v[140:143], v[144:147], v[112:119], v148\n\t"
        "buffer_load_dwordx4 v[184:187], v149, s[52:55], 0 offen offset:0 sc1\n\t"
        "v_exp_f32 v96, v96\n\t"
        "v_exp_f32 v97, v97\n\t"
        "v_smfmac_f32_32x32x32_bf16 v[48:63], v[196:199], v[120:127], v148\n\t"
        "v_exp_f32 v98, v98\n\t"
        "v_exp_f32 v99, v99\n\t"
        "v_exp_f32 v100, v100\n\t"
        "v_smfmac_f32_16x16x64_bf16 v[140:143], v[144:147], v[120:127], v148\n\t"
        "v_exp_f32 v101, v101\n\t"
        "v_exp_f32 v102, v102\n\t"
        "v_mfma_f32_32x32x16_bf16 v[112:127], v[200:203], v[72:75], 0\n\t"
        "v_exp_f32 v103, v103\n\t"
        "v_exp_f32 v104, v104\n\t"
        "v_exp_f32 v105, v105\n\t"
        "v_mfma_f32_32x32x16_bf16 v[112:127], v[204:207], v[76:79], v[112:127]\n\t"
        "v_exp_f32 v106, v106\n\t"
        "v_exp_f32 v107, v107\n\t"
        "v_exp_f32 v108, v108\n\t"
        "v_smfmac_f32_32x32x32_bf16 v[0:15], v[208:211], v[96:103], v148\n\t"
        "v_exp_f32 v109, v109\n\t"
        "v_exp_f32 v110, v110\n\t"
        "v_exp_f32 v111, v111\n\t"
        "v_smfmac_f32_16x16x64_bf16 v[128:131], v[144:147], v[96:103], v148\n\t"
        "buffer_load_dwordx4 v[188:191], v149, s[52:55], 0 offen offset:1024 sc1\n\t"
        "v_exp_f32 v112, v112\n\t"
        "v_exp_f32 v113, v113\n\t"
        "v_smfmac_f32_32x32x32_bf16 v[0:15], v[212:215], v[104:111], v148\n\t"
        "v_exp_f32 v114, v114\n\t"
        "v_exp_f32 v115, v115\n\t"
        "v_exp_f32 v116, v116\n\t"
        "v_smfmac_f32_16x16x64_bf16 v[128:131], v[144:147], v[104:111], v148\n\t"
        "v_exp_f32 v117, v117\n\t"
        "v_exp_f32 v118, v118\n\t"
        "v_mfma_f32_32x32x16_bf16 v[96:111], v[200:203], v[80:83], 0\n\t"
        "v_exp_f32 v119, v119\n\t"
        "v_exp_f32 v120, v120\n\t"
        "v_exp_f32 v121, v121\n\t"
        "v_mfma_f32_32x32x16_bf16 v[96:111], v[204:207], v[84:87], v[96:111]\n\t"
        "v_exp_f32 v122, v122\n\t"
        "v_exp_f32 v123, v123\n\t"
        "v_exp_f32 v124, v124\n\t"
        "v_smfmac_f32_32x32x32_bf16 v[16:31], v[208:211], v[112:119], v148\n\t"
        "v_exp_f32 v125, v125\n\t"
        "v_exp_f32 v126, v126\n\t"
        "v_exp_f32 v127, v127\n\t"
        "v_smfmac_f32_16x16x64_bf16 v[132:135], v[144:147], v[112:119], v148\n\t"
        "buffer_load_dwordx4 v[192:195], v149, s[52:55], 0 offen offset:2048 sc1\n\t"
        "v_exp_f32 v96, v96\n\t"
        "v_exp_f32 v97, v97\n\t"
        "v_smfmac_f32_32x32x32_bf16 v[16:31], v[212:215], v[120:127], v148\n\t"
        "v_exp_f32 v98, v98\n\t"
        "v_exp_f32 v99, v99\n\t"
        "v_exp_f32 v100, v100\n\t"
        "v_smfmac_f32_16x16x64_bf16 v[132:135], v[144:147], v[120:127], v148\n\t"
        "v_exp_f32 v101, v101\n\t"
        "v_exp_f32 v102, v102\n\t"
        "v_mfma_f32_32x32x16_bf16 v[112:127], v[200:203], v[88:91], 0\n\t"
        "v_exp_f32 v103, v103\n\t"
        "v_exp_f32 v104, v104\n\t"
        "v_exp_f32 v105, v105\n\t"
        "v_mfma_f32_32x32x16_bf16 v[112:127], v[204:207], v[92:95], v[112:127]\n\t"
        "v_exp_f32 v106, v106\n\t"
        "v_exp_f32 v107, v107\n\t"
        "v_exp_f32 v108, v108\n\t"
        "v_smfmac_f32_32x32x32_bf16 v[32:47], v[208:211], v[96:103], v148\n\t"
        "v_exp_f32 v109, v109\n\t"
        "v_exp_f32 v110, v110\n\t"
        "v_exp_f32 v111, v111\n\t"
        "v_smfmac_f32_16x16x64_bf16 v[136:139], v[144:147], v[96:103], v148\n\t"
        "buffer_load_dwordx4 v[196:199], v149, s[52:55], 0 offen offset:3072 sc1\n\t"
        "v_exp_f32 v112, v112\n\t"
        "v_exp_f32 v113, v113\n\t"
        "v_smfmac_f32_32x32x32_bf16 v[32:47], v[212:215], v[104:111], v148\n\t"
        "v_exp_f32 v114, v114\n\t"
        "v_exp_f32 v115, v115\n\t"
        "v_exp_f32 v116, v116\n\t"
        "v_smfmac_f32_16x16x64_bf16 v[136:139], v[144:147], v[104:111], v148\n\t"
        "s_waitcnt vmcnt(8)\n\t"
        "v_exp_f32 v117, v117\n\t"
        "v_exp_f32 v118, v118\n\t"
        "v_mfma_f32_32x32x16_bf16 v[96:111], v[152:155], v[64:67], 0\n\t"
        "v_exp_f32 v119, v119\n\t"
        "v_exp_f32 v120, v120\n\t"
        "v_exp_f32 v121, v121\n\t"
        "v_mfma_f32_32x32x16_bf16 v[96:111], v[156:159], v[68:71], v[96:111]\n\t"
        "v_exp_f32 v122, v122\n\t"
        "v_exp_f32 v123, v123\n\t"
        "v_exp_f32 v124, v124\n\t"
        "v_smfmac_f32_32x32x32_bf16 v[48:63], v[208:211], v[112:119], v148\n\t"
        "v_exp_f32 v125, v125\n\t"
        "v_exp_f32 v126, v126\n\t"
        "v_exp_f32 v127, v127\n\t"
        "v_smfmac_f32_16x16x64_bf16 v[140:143], v[144:147], v[112:119], v148\n\t"
        "buffer_load_dwordx4 v[200:203], v150, s[52:55], 0 offen offset:0 sc1\n\t"
        "v_exp_f32 v96, v96\n\t"
        "v_exp_f32 v97, v97\n\t"
        "v_smfmac_f32_32x32x32_bf16 v[48:63], v[212:215], v[120:127], v148\n\t"
        "v_exp_f32 v98, v98\n\t"
        "v_exp_f32 v99, v99\n\t"
        "v_exp_f32 v100, v100\n\t"
        "v_smfmac_f32_16x16x64_bf16 v[140:143], v[144:147], v[120:127], v148\n\t"
        "v_exp_f32 v101, v101\n\t"
        "v_exp_f32 v102, v102\n\t"
        "v_mfma_f32_32x32x16_bf16 v[112:127], v[152:155], v[72:75], 0\n\t"
        "v_exp_f32 v103, v103\n\t"
        "v_exp_f32 v104, v104\n\t"
        "v_exp_f32 v105, v105\n\t"
        "v_mfma_f32_32x32x16_bf16 v[112:127], v[156:159], v[76:79], v[112:127]\n\t"
        "v_exp_f32 v106, v106\n\t"
        "v_exp_f32 v107, v107\n\t"
        "v_exp_f32 v108, v108\n\t"
        "v_smfmac_f32_32x32x32_bf16 v[0:15], v[160:163], v[96:103], v148\n\t"
        "v_exp_f32 v109, v109\n\t"
        "v_exp_f32 v110, v110\n\t"
        "v_exp_f32 v111, v111\n\t"
        "v_smfmac_f32_16x16x64_bf16 v[128:131], v[144:147], v[96:103], v148\n\t"
        "buffer_load_dwordx4 v[204:207], v150, s[52:55], 0 offen offset:1024 sc1\n\t"
        "v_exp_f32 v112, v112\n\t"
        "v_exp_f32 v113, v113\n\t"
        "v_smfmac_f32_32x32x32_bf16 v[0:15], v[164:167], v[104:111], v148\n\t"
        "v_exp_f32 v114, v114\n\t"
        "v_exp_f32 v115, v115\n\t"
        "v_exp_f32 v116, v116\n\t"
        "v_smfmac_f32_16x16x64_bf16 v[128:131], v[144:147], v[104:111], v148\n\t"
        "v_exp_f32 v117, v117\n\t"
        "v_exp_f32 v118, v118\n\t"
        "v_mfma_f32_32x32x16_bf16 v[96:111], v[152:155], v[80:83], 0\n\t"
        "v_exp_f32 v119, v119\n\t"
        "v_exp_f32 v120, v120\n\t"
        "v_exp_f32 v121, v121\n\t"
        "v_mfma_f32_32x32x16_bf16 v[96:111], v[156:159], v[84:87], v[96:111]\n\t"
        "v_exp_f32 v122, v122\n\t"
        "v_exp_f32 v123, v123\n\t"
        "v_exp_f32 v124, v124\n\t"
        "v_smfmac_f32_32x32x32_bf16 v[16:31], v[160:163], v[112:119], v148\n\t"
        "v_exp_f32 v125, v125\n\t"
        "v_exp_f32 v126, v126\n\t"
        "v_exp_f32 v127, v127\n\t"
        "v_smfmac_f32_16x16x64_bf16 v[132:135], v[144:147], v[112:119], v148\n\t"
        "buffer_load_dwordx4 v[208:211], v150, s[52:55], 0 offen offset:2048 sc1\n\t"
        "v_exp_f32 v96, v96\n\t"
        "v_exp_f32 v97, v97\n\t"
        "v_smfmac_f32_32x32x32_bf16 v[16:31], v[164:167], v[120:127], v148\n\t"
        "v_exp_f32 v98, v98\n\t"
        "v_exp_f32 v99, v99\n\t"
        "v_exp_f32 v100, v100\n\t"
        "v_smfmac_f32_16x16x64_bf16 v[132:135], v[144:147], v[120:127], v148\n\t"
        "v_exp_f32 v101, v101\n\t"
        "v_exp_f32 v102, v102\n\t"
        "v_mfma_f32_32x32x16_bf16 v[112:127], v[152:155], v[88:91], 0\n\t"
        "v_exp_f32 v103, v103\n\t"
        "v_exp_f32 v104, v104\n\t"
        "v_exp_f32 v105, v105\n\t"
        "v_mfma_f32_32x32x16_bf16 v[112:127], v[156:159], v[92:95], v[112:127]\n\t"
        "v_exp_f32 v106, v106\n\t"
        "v_exp_f32 v107, v107\n\t"
        "v_exp_f32 v108, v108\n\t"
        "v_smfmac_f32_32x32x32_bf16 v[32:47], v[160:163], v[96:103], v148\n\t"
        "v_exp_f32 v109, v109\n\t"
        "v_exp_f32 v110, v110\n\t"
        "v_exp_f32 v111, v111\n\t"
        "v_smfmac_f32_16x16x64_bf16 v[136:139], v[144:147], v[96:103], v148\n\t"
        "buffer_load_dwordx4 v[212:215], v150, s[52:55], 0 offen offset:3072 sc1\n\t"
        "v_exp_f32 v112, v112\n\t"
        "v_exp_f32 v113, v113\n\t"
        "v_smfmac_f32_32x32x32_bf16 v[32:47], v[164:167], v[104:111], v148\n\t"
        "s_add_u32 s44, s44, 0x4000\n\t"
        "s_and_b32 s44, s44, 0x3ffff\n\t"
        "v_exp_f32 v114, v114\n\t"
        "v_exp_f32 v115, v115\n\t"
        "v_exp_f32 v116, v116\n\t"
        "v_smfmac_f32_16x16x64_bf16 v[136:139], v[144:147], v[104:111], v148\n\t"
        "s_waitcnt vmcnt(8)\n\t"
        "v_exp_f32 v117, v117\n\t"
        "v_exp_f32 v118, v118\n\t"
        "v_mfma_f32_32x32x16_bf16 v[96:111], v[168:171], v[64:67], 0\n\t"
        "s_cmp_eq_u32 s44, s49\n\t"
        "v_exp_f32 v119, v119\n\t"
        "v_exp_f32 v120, v120\n\t"
        "v_exp_f32 v121, v121\n\t"
        "v_mfma_f32_32x32x16_bf16 v[96:111], v[172:175], v[68:71], v[96:111]\n\t"
        "s_cselect_b32 s44, s50, s44\n\t"
        "v_exp_f32 v122, v122\n\t"
        "v_exp_f32 v123, v123\n\t"
        "v_exp_f32 v124, v124\n\t"
        "v_smfmac_f32_32x32x32_bf16 v[48:63], v[160:163], v[112:119], v148\n\t"
        "s_add_u32 s52, s40, s44\n\t"
        "s_addc_u32 s53, s41, 0\n\t"
        "v_exp_f32 v125, v125\n\t"
        "v_exp_f32 v126, v126\n\t"
        "v_exp_f32 v127, v127\n\t"
        "v_smfmac_f32_16x16x64_bf16 v[140:143], v[144:147], v[112:119], v148\n\t"
        "buffer_load_dwordx4 v[152:155], v149, s[52:55], 0 offen offset:0 sc1\n\t"
        "v_exp_f32 v96, v96\n\t"
        "v_exp_f32 v97, v97\n\t"
        "v_smfmac_f32_32x32x32_bf16 v[48:63], v[164:167], v[120:127], v148\n\t"
        "v_exp_f32 v98, v98\n\t"
        "v_exp_f32 v99, v99\n\t"
        "v_exp_f32 v100, v100\n\t"
        "v_smfmac_f32_16x16x64_bf16 v[140:143], v[144:147], v[120:127], v148\n\t"
        "v_exp_f32 v101, v101\n\t"
        "v_exp_f32 v102, v102\n\t"
        "v_mfma_f32_32x32x16_bf16 v[112:127], v[168:171], v[72:75], 0\n\t"
        "v_exp_f32 v103, v103\n\t"
        "v_exp_f32 v104, v104\n\t"
        "v_exp_f32 v105, v105\n\t"
        "v_mfma_f32_32x32x16_bf16 v[112:127], v[172:175], v[76:79], v[112:127]\n\t"
        "v_exp_f32 v106, v106\n\t"
        "v_exp_f32 v107, v107\n\t"
        "v_exp_f32 v108, v108\n\t"
        "v_smfmac_f32_32x32x32_bf16 v[0:15], v[176:179], v[96:103], v148\n\t"
        "v_exp_f32 v109, v109\n\t"
        "v_exp_f32 v110, v110\n\t"
        "v_exp_f32 v111, v111\n\t"
        "v_smfmac_f32_16x16x64_bf16 v[128:131], v[144:147], v[96:103], v148\n\t"
        "buffer_load_dwordx4 v[156:159], v149, s[52:55], 0 offen offset:1024 sc1\n\t"
        "v_exp_f32 v112, v112\n\t"
        "v_exp_f32 v113, v113\n\t"
        "v_smfmac_f32_32x32x32_bf16 v[0:15], v[180:183], v[104:111], v148\n\t"
        "v_exp_f32 v114, v114\n\t"
        "v_exp_f32 v115, v115\n\t"
        "v_exp_f32 v116, v116\n\t"
        "v_smfmac_f32_16x16x64_bf16 v[128:131], v[144:147], v[104:111], v148\n\t"
        "v_exp_f32 v117, v117\n\t"
        "v_exp_f32 v118, v118\n\t"
        "v_mfma_f32_32x32x16_bf16 v[96:111], v[168:171], v[80:83], 0\n\t"
        "v_exp_f32 v119, v119\n\t"
        "v_exp_f32 v120, v120\n\t"
        "v_exp_f32 v121, v121\n\t"
        "v_mfma_f32_32x32x16_bf16 v[96:111], v[172:175], v[84:87], v[96:111]\n\t"
        "v_exp_f32 v122, v122\n\t"
        "v_exp_f32 v123, v123\n\t"
        "v_exp_f32 v124, v124\n\t"
        "v_smfmac_f32_32x32x32_bf16 v[16:31], v[176:179], v[112:119], v148\n\t"
        "v_exp_f32 v125, v125\n\t"
        "v_exp_f32 v126, v126\n\t"
        "v_exp_f32 v127, v127\n\t"
        "v_smfmac_f32_16x16x64_bf16 v[132:135], v[144:147], v[112:119], v148\n\t"
        "buffer_load_dwordx4 v[160:163], v149, s[52:55], 0 offen offset:2048 sc1\n\t"
        "v_exp_f32 v96, v96\n\t"
        "v_exp_f32 v97, v97\n\t"
        "v_smfmac_f32_32x32x32_bf16 v[16:31], v[180:183], v[120:127], v148\n\t"
        "v_exp_f32 v98, v98\n\t"
        "v_exp_f32 v99, v99\n\t"
        "v_exp_f32 v100, v100\n\t"
        "v_smfmac_f32_16x16x64_bf16 v[132:135], v[144:147], v[120:127], v148\n\t"
        "v_exp_f32 v101, v101\n\t"
        "v_exp_f32 v102, v102\n\t"
        "v_mfma_f32_32x32x16_bf16 v[112:127], v[168:171], v[88:91], 0\n\t"
        "v_exp_f32 v103, v103\n\t"
        "v_exp_f32 v104, v104\n\t"
        "v_exp_f32 v105, v105\n\t"
        "v_mfma_f32_32x32x16_bf16 v[112:127], v[172:175], v[92:95], v[112:127]\n\t"
        "v_exp_f32 v106, v106\n\t"
        "v_exp_f32 v107, v107\n\t"
        "v_exp_f32 v108, v108\n\t"
        "v_smfmac_f32_32x32x32_bf16 v[32:47], v[176:179], v[96:103], v148\n\t"
        "s_sub_u32 s45, s45, 1\n\t"
        "v_exp_f32 v109, v109\n\t"
        "v_exp_f32 v110, v110\n\t"
        "v_exp_f32 v111, v111\n\t"
        "v_smfmac_f32_16x16x64_bf16 v[136:139], v[144:147], v[96:103], v148\n\t"
        "buffer_load_dwordx4 v[164:167], v149, s[52:55], 0 offen offset:3072 sc1\n\t"
        "s_cbranch_scc0 1b\n\t"
        "v_exp_f32 v112, v112\n\t"
        "v_exp_f32 v113, v113\n\t"
        "v_smfmac_f32_32x32x32_bf16 v[32:47], v[180:183], v[104:111], v148\n\t"
        "v_exp_f32 v114, v114\n\t"
        "v_exp_f32 v115, v115\n\t"
        "v_exp_f32 v116, v116\n\t"
        "v_smfmac_f32_16x16x64_bf16 v[136:139], v[144:147], v[104:111], v148\n\t"
        "s_waitcnt vmcnt(8)\n\t"
        "v_exp_f32 v117, v117\n\t"
        "v_exp_f32 v118, v118\n\t"
        "v_mfma_f32_32x32x16_bf16 v[96:111], v[184:187], v[64:67], 0\n\t"
        "v_exp_f32 v119, v119\n\t"
        "v_exp_f32 v120, v120\n\t"
        "v_exp_f32 v121, v121\n\t"
        "v_mfma_f32_32x32x16_bf16 v[96:111], v[188:191], v[68:71], v[96:111]\n\t"
        "v_exp_f32 v122, v122\n\t"
        "v_exp_f32 v123, v123\n\t"
        "v_exp_f32 v124, v124\n\t"
        "v_smfmac_f32_32x32x32_bf16 v[48:63], v[176:179], v[112:119], v148\n\t"
        "v_exp_f32 v125, v125\n\t"
        "v_exp_f32 v126, v126\n\t"
        "v_exp_f32 v127, v127\n\t"
        "v_smfmac_f32_16x16x64_bf16 v[140:143], v[144:147], v[112:119], v148\n\t"
        "v_exp_f32 v96, v96\n\t"
        "v_exp_f32 v97, v97\n\t"
        "v_smfmac_f32_32x32x32_bf16 v[48:63], v[180:183], v[120:127], v148\n\t"
        "v_exp_f32 v98, v98\n\t"
        "v_exp_f32 v99, v99\n\t"
        "v_exp_f32 v100, v100\n\t"
        "v_smfmac_f32_16x16x64_bf16 v[140:143], v[144:147], v[120:127], v148\n\t"
        "v_exp_f32 v101, v101\n\t"
        "v_exp_f32 v102, v102\n\t"
        "v_mfma_f32_32x32x16_bf16 v[112:127], v[184:187], v[72:75], 0\n\t"
        "v_exp_f32 v103, v103\n\t"
        "v_exp_f32 v104, v104\n\t"
        "v_exp_f32 v105, v105\n\t"
        "v_mfma_f32_32x32x16_bf16 v[112:127], v[188:191], v[76:79], v[112:127]\n\t"
        "v_exp_f32 v106, v106\n\t"
        "v_exp_f32 v107, v107\n\t"
        "v_exp_f32 v108, v108\n\t"
        "v_smfmac_f32_32x32x32_bf16 v[0:15], v[192:195], v[96:103], v148\n\t"
        "v_exp_f32 v109, v109\n\t"
        "v_exp_f32 v110, v110\n\t"
        "v_exp_f32 v111, v111\n\t"
        "v_smfmac_f32_16x16x64_bf16 v[128:131], v[144:147], v[96:103], v148\n\t"
        "v_exp_f32 v112, v112\n\t"
        "v_exp_f32 v113, v113\n\t"
        "v_smfmac_f32_32x32x32_bf16 v[0:15], v[196:199], v[104:111], v148\n\t"
        "v_exp_f32 v114, v114\n\t"
        "v_exp_f32 v115, v115\n\t"
        "v_exp_f32 v116, v116\n\t"
        "v_smfmac_f32_16x16x64_bf16 v[128:131], v[144:147], v[104:111], v148\n\t"
        "s_waitcnt vmcnt(4)\n\t"
        "v_exp_f32 v117, v117\n\t"
        "v_exp_f32 v118, v118\n\t"
        "v_mfma_f32_32x32x16_bf16 v[96:111], v[200:203], v[64:67], 0\n\t"
        "v_exp_f32 v119, v119\n\t"
        "v_exp_f32 v120, v120\n\t"
        "v_exp_f32 v121, v121\n\t"
        "v_mfma_f32_32x32x16_bf16 v[96:111], v[204:207], v[68:71], v[96:111]\n\t"
        "v_exp_f32 v122, v122\n\t"
        "v_exp_f32 v123, v123\n\t"
        "v_exp_f32 v124, v124\n\t"
        "v_smfmac_f32_32x32x32_bf16 v[16:31], v[192:195], v[112:119], v148\n\t"
        "v_exp_f32 v125, v125\n\t"
        "v_exp_f32 v126, v126\n\t"
        "v_exp_f32 v127, v127\n\t"
        "v_smfmac_f32_16x16x64_bf16 v[132:135], v[144:147], v[112:119], v148\n\t"
        "s_cmp_lg_u32 s47, 0\n\t"
        "s_cbranch_scc1 4f\n\t"
        "global_load_dwordx4 %[r0], v223, %[xb] offset:0\n\t"
        "global_load_dwordx4 %[r1], v223, %[xb] offset:32\n\t"
        "global_load_dwordx4 %[r2], v223, %[xb] offset:64\n\t"
        "global_load_dwordx4 %[r3], v223, %[xb] offset:96\n\t"
        "4:\n\t"
        "v_exp_f32 v96, v96\n\t"
        "v_exp_f32 v97, v97\n\t"
        "v_smfmac_f32_32x32x32_bf16 v[16:31], v[196:199], v[120:127], v148\n\t"
        "v_exp_f32 v98, v98\n\t"
        "v_exp_f32 v99, v99\n\t"
        "v_exp_f32 v100, v100\n\t"
        "v_smfmac_f32_16x16x64_bf16 v[132:135], v[144:147], v[120:127], v148\n\t"
        "v_exp_f32 v101, v101\n\t"
        "v_exp_f32 v102, v102\n\t"
        "v_mfma_f32_32x32x16_bf16 v[112:127], v[200:203], v[72:75], 0\n\t"
        "v_exp_f32 v103, v103\n\t"
        "v_exp_f32 v104, v104\n\t"
        "v_exp_f32 v105, v105\n\t"
        "v_mfma_f32_32x32x16_bf16 v[112:127], v[204:207], v[76:79], v[112:127]\n\t"
        "v_exp_f32 v106, v106\n\t"
        "v_exp_f32 v107, v107\n\t"
        "v_exp_f32 v108, v108\n\t"
        "v_smfmac_f32_32x32x32_bf16 v[0:15], v[208:211], v[96:103], v148\n\t"
        "v_exp_f32 v109, v109\n\t"
        "v_exp_f32 v110, v110\n\t"
        "v_exp_f32 v111, v111\n\t"
        "v_smfmac_f32_16x16x64_bf16 v[128:131], v[144:147], v[96:103], v148\n\t"
        "v_exp_f32 v112, v112\n\t"
        "v_exp_f32 v113, v113\n\t"
        "v_smfmac_f32_32x32x32_bf16 v[0:15], v[212:215], v[104:111], v148\n\t"
        "v_exp_f32 v114, v114\n\t"
        "v_exp_f32 v115, v115\n\t"
        "v_exp_f32 v116, v116\n\t"
        "v_smfmac_f32_16x16x64_bf16 v[128:131], v[144:147], v[104:111], v148\n\t"
        "v_exp_f32 v117, v117\n\t"
        "v_exp_f32 v118, v118\n\t"
        "v_mfma_f32_32x32x16_bf16 v[96:111], v[184:187], v[80:83], 0\n\t"
        "v_exp_f32 v119, v119\n\t"
        "v_exp_f32 v120, v120\n\t"
        "v_exp_f32 v121, v121\n\t"
        "v_mfma_f32_32x32x16_bf16 v[96:111], v[188:191], v[84:87], v[96:111]\n\t"
        "ds_write_b128 v216, v[0:3] offset:0\n\t"
        "ds_write_b128 v216, v[4:7] offset:1024\n\t"
        "v_exp_f32 v122, v122\n\t"
        "v_exp_f32 v123, v123\n\t"
        "v_exp_f32 v124, v124\n\t"
        "v_smfmac_f32_32x32x32_bf16 v[16:31], v[208:211], v[112:119], v148\n\t"
        "ds_write_b128 v216, v[8:11] offset:2048\n\t"
        "ds_write_b128 v216, v[12:15] offset:3072\n\t"
        "v_exp_f32 v125, v125\n\t"
        "v_exp_f32 v126, v126\n\t"
        "v_exp_f32 v127, v127\n\t"
        "v_smfmac_f32_16x16x64_bf16 v[132:135], v[144:147], v[112:119], v148\n\t"
        "ds_write_b32 v218, v128 offset:0\n\t"
        "v_exp_f32 v96, v96\n\t"
        "v_exp_f32 v97, v97\n\t"
        "v_smfmac_f32_32x32x32_bf16 v[16:31], v[212:215], v[120:127], v148\n\t"
        "v_exp_f32 v98, v98\n\t"
        "v_exp_f32 v99, v99\n\t"
        "v_exp_f32 v100, v100\n\t"
        "v_smfmac_f32_16x16x64_bf16 v[132:135], v[144:147], v[120:127], v148\n\t"
        "v_exp_f32 v101, v101\n\t"
        "v_exp_f32 v102, v102\n\t"
        "v_mfma_f32_32x32x16_bf16 v[112:127], v[184:187], v[88:91], 0\n\t"
        "v_exp_f32 v103, v103\n\t"
        "v_exp_f32 v104, v104\n\t"
        "v_exp_f32 v105, v105\n\t"
        "v_mfma_f32_32x32x16_bf16 v[112:127], v[188:191], v[92:95], v[112:127]\n\t"
        "ds_write_b128 v216, v[16:19] offset:4096\n\t"
        "ds_write_b128 v216, v[20:23] offset:5120\n\t"
        "v_exp_f32 v106, v106\n\t"
        "v_exp_f32 v107, v107\n\t"
        "v_exp_f32 v108, v108\n\t"
        "v_smfmac_f32_32x32x32_bf16 v[32:47], v[192:195], v[96:103], v148\n\t"
        "ds_write_b128 v216, v[24:27] offset:6144\n\t"
        "ds_write_b128 v216, v[28:31] offset:7168\n\t"
        "v_exp_f32 v109, v109\n\t"
        "v_exp_f32 v110, v110\n\t"
        "v_exp_f32 v111, v111\n\t"
        "v_smfmac_f32_16x16x64_bf16 v[136:139], v[144:147], v[96:103], v148\n\t"
        "ds_write_b32 v218, v132 offset:256\n\t"
        "ds_write_b32 v220, v222\n\t"
        "v_exp_f32 v112, v112\n\t"
        "v_exp_f32 v113, v113\n\t"
        "v_smfmac_f32_32x32x32_bf16 v[32:47], v[196:199], v[104:111], v148\n\t"
        "v_exp_f32 v114, v114\n\t"
        "v_exp_f32 v115, v115\n\t"
        "v_exp_f32 v116, v116\n\t"
        "v_smfmac_f32_16x16x64_bf16 v[136:139], v[144:147], v[104:111], v148\n\t"
        "v_exp_f32 v117, v117\n\t"
        "v_exp_f32 v118, v118\n\t"
        "v_mfma_f32_32x32x16_bf16 v[96:111], v[200:203], v[80:83], 0\n\t"
        "v_exp_f32 v119, v119\n\t"
        "v_exp_f32 v120, v120\n\t"
        "v_exp_f32 v121, v121\n\t"
        "v_mfma_f32_32x32x16_bf16 v[96:111], v[204:207], v[84:87], v[96:111]\n\t"
        "v_exp_f32 v122, v122\n\t"
        "v_exp_f32 v123, v123\n\t"
        "v_exp_f32 v124, v124\n\t"
        "v_smfmac_f32_32x32x32_bf16 v[48:63], v[192:195], v[112:119], v148\n\t"
        "v_exp_f32 v125, v125\n\t"
        "v_exp_f32 v126, v126\n\t"
        "v_exp_f32 v127, v127\n\t"
        "v_smfmac_f32_16x16x64_bf16 v[140:143], v[144:147], v[112:119], v148\n\t"
        "v_exp_f32 v96, v96\n\t"
        "v_exp_f32 v97, v97\n\t"
        "v_smfmac_f32_32x32x32_bf16 v[48:63], v[196:199], v[120:127], v148\n\t"
        "v_exp_f32 v98, v98\n\t"
        "v_exp_f32 v99, v99\n\t"
        "v_exp_f32 v100, v100\n\t"
        "v_smfmac_f32_16x16x64_bf16 v[140:143], v[144:147], v[120:127], v148\n\t"
        "v_exp_f32 v101, v101\n\t"
        "v_exp_f32 v102, v102\n\t"
        "v_mfma_f32_32x32x16_bf16 v[112:127], v[200:203], v[88:91], 0\n\t"
        "v_exp_f32 v103, v103\n\t"
        "v_exp_f32 v104, v104\n\t"
        "v_exp_f32 v105, v105\n\t"
        "v_mfma_f32_32x32x16_bf16 v[112:127], v[204:207], v[92:95], v[112:127]\n\t"
        "ds_read_b32 v151, v221\n\t"
        "v_exp_f32 v106, v106\n\t"
        "v_exp_f32 v107, v107\n\t"
        "v_exp_f32 v108, v108\n\t"
        "v_smfmac_f32_32x32x32_bf16 v[32:47], v[208:211], v[96:103], v148\n\t"
        "v_exp_f32 v109, v109\n\t"
        "v_exp_f32 v110, v110\n\t"
        "v_exp_f32 v111, v111\n\t"
        "v_smfmac_f32_16x16x64_bf16 v[136:139], v[144:147], v[96:103], v148\n\t"
        "s_waitcnt lgkmcnt(0)\n\t"
        "v_cmp_ne_u32 vcc, v222, v151\n\t"
        "s_cbranch_vccnz 40f\n\t"
        "41:\n\t"
        "v_exp_f32 v112, v112\n\t"
        "v_exp_f32 v113, v113\n\t"
        "v_smfmac_f32_32x32x32_bf16 v[32:47], v[212:215], v[104:111], v148\n\t"
        "ds_read_b128 v[0:3], v217 offset:0\n\t"
        "ds_read_b128 v[4:7], v217 offset:1024\n\t"
        "ds_read_b128 v[8:11], v217 offset:2048\n\t"
        "ds_read_b128 v[12:15], v217 offset:3072\n\t"
        "v_exp_f32 v114, v114\n\t"
        "v_exp_f32 v115, v115\n\t"
        "v_exp_f32 v116, v116\n\t"
        "v_smfmac_f32_16x16x64_bf16 v[136:139], v[144:147], v[104:111], v148\n\t"
        "ds_read_b32 v128, v219 offset:0\n\t"
        "ds_read_b128 v[16:19], v217 offset:4096\n\t"
        "ds_read_b128 v[20:23], v217 offset:5120\n\t"
        "v_exp_f32 v117, v117\n\t"
        "v_exp_f32 v118, v118\n\t"
        "v_exp_f32 v119, v119\n\t"
        "v_exp_f32 v120, v120\n\t"
        "v_exp_f32 v121, v121\n\t"
        "v_exp_f32 v122, v122\n\t"
        "v_exp_f32 v123, v123\n\t"
        "v_exp_f32 v124, v124\n\t"
        "v_smfmac_f32_32x32x32_bf16 v[48:63], v[208:211], v[112:119], v148\n\t"
        "ds_read_b128 v[24:27], v217 offset:6144\n\t"
        "ds_read_b128 v[28:31], v217 offset:7168\n\t"
        "ds_read_b32 v132, v219 offset:256\n\t"
        "v_exp_f32 v125, v125\n\t"
        "v_exp_f32 v126, v126\n\t"
        "v_exp_f32 v127, v127\n\t"
        "v_smfmac_f32_16x16x64_bf16 v[140:143], v[144:147], v[112:119], v148\n\t"
        "s_nop 1\n\t"
        "v_smfmac_f32_32x32x32_bf16 v[48:63], v[212:215], v[120:127], v148\n\t"
        "v_smfmac_f32_16x16x64_bf16 v[140:143], v[144:147], v[120:127], v148\n\t"
        "s_waitcnt vmcnt(0) lgkmcnt(0)\n\t"
        "s_nop 6\n\t"
        "s_branch 9f\n\t"
        "24:\n\t"
        "s_add_u32 s44, s44, 0x4000\n\t"
        "s_and_b32 s44, s44, 0x3ffff\n\t"
        "s_cmp_eq_u32 s44, s49\n\t"
        "s_cselect_b32 s44, s50, s44\n\t"
        "s_add_u32 s52, s40, s44\n\t"
        "s_addc_u32 s53, s41, 0\n\t"
        "buffer_load_dwordx4 v[184:187], v149, s[52:55], 0 offen offset:0 sc1\n\t"
        "buffer_load_dwordx4 v[188:191], v149, s[52:55], 0 offen offset:1024 sc1\n\t"
        "buffer_load_dwordx4 v[192:195], v149, s[52:55], 0 offen offset:2048 sc1\n\t"
        "buffer_load_dwordx4 v[196:199], v149, s[52:55], 0 offen offset:3072 sc1\n\t"
        "buffer_load_dwordx4 v[200:203], v150, s[52:55], 0 offen offset:0 sc1\n\t"
        "buffer_load_dwordx4 v[204:207], v150, s[52:55], 0 offen offset:1024 sc1\n\t"
        "buffer_load_dwordx4 v[208:211], v150, s[52:55], 0 offen offset:2048 sc1\n\t"
        "buffer_load_dwordx4 v[212:215], v150, s[52:55], 0 offen offset:3072 sc1\n\t"
        "s_mov_b32 s46, 1\n\t"
        "s_branch 25b\n\t"
        "26:\n\t"
        "s_add_u32 s44, s44, 0x4000\n\t"
        "s_and_b32 s44, s44, 0x3ffff\n\t"
        "s_cmp_eq_u32 s44, s49\n\t"
        "s_cselect_b32 s44, s50, s44\n\t"
        "s_add_u32 s52, s40, s44\n\t"
        "s_addc_u32 s53, s41, 0\n\t"
        "buffer_load_dwordx4 v[184:187], v149, s[52:55], 0 offen offset:0 sc1\n\t"
        "buffer_load_dwordx4 v[188:191], v149, s[52:55], 0 offen offset:1024 sc1\n\t"
        "buffer_load_dwordx4 v[192:195], v149, s[52:55], 0 offen offset:2048 sc1\n\t"
        "buffer_load_dwordx4 v[196:199], v149, s[52:55], 0 offen offset:3072 sc1\n\t"
        "buffer_load_dwordx4 v[200:203], v150, s[52:55], 0 offen offset:0 sc1\n\t"
        "buffer_load_dwordx4 v[204:207], v150, s[52:55], 0 offen offset:1024 sc1\n\t"
        "buffer_load_dwordx4 v[208:211], v150, s[52:55], 0 offen offset:2048 sc1\n\t"
        "buffer_load_dwordx4 v[212:215], v150, s[52:55], 0 offen offset:3072 sc1\n\t"
        "s_mov_b32 s46, 1\n\t"
        "s_branch 27b\n\t"
        "28:\n\t"
        "s_add_u32 s44, s44, 0x4000\n\t"
        "s_and_b32 s44, s44, 0x3ffff\n\t"
        "s_cmp_eq_u32 s44, s49\n\t"
        "s_cselect_b32 s44, s50, s44\n\t"
        "s_add_u32 s52, s40, s44\n\t"
        "s_addc_u32 s53, s41, 0\n\t"
        "buffer_load_dwordx4 v[184:187], v149, s[52:55], 0 offen offset:0 sc1\n\t"
        "buffer_load_dwordx4 v[188:191], v149, s[52:55], 0 offen offset:1024 sc1\n\t"
        "buffer_load_dwordx4 v[192:195], v149, s[52:55], 0 offen offset:2048 sc1\n\t"
        "buffer_load_dwordx4 v[196:199], v149, s[52:55], 0 offen offset:3072 sc1\n\t"
        "buffer_load_dwordx4 v[200:203], v150, s[52:55], 0 offen offset:0 sc1\n\t"
        "buffer_load_dwordx4 v[204:207], v150, s[52:55], 0 offen offset:1024 sc1\n\t"
        "buffer_load_dwordx4 v[208:211], v150, s[52:55], 0 offen offset:2048 sc1\n\t"
        "buffer_load_dwordx4 v[212:215], v150, s[52:55], 0 offen offset:3072 sc1\n\t"
        "s_mov_b32 s46, 1\n\t"
        "s_branch 29b\n\t"
        "30:\n\t"
        "s_add_u32 s44, s44, 0x4000\n\t"
        "s_and_b32 s44, s44, 0x3ffff\n\t"
        "s_cmp_eq_u32 s44, s49\n\t"
        "s_cselect_b32 s44, s50, s44\n\t"
        "s_add_u32 s52, s40, s44\n\t"
        "s_addc_u32 s53, s41, 0\n\t"
        "buffer_load_dwordx4 v[184:187], v149, s[52:55], 0 offen offset:0 sc1\n\t"
        "buffer_load_dwordx4 v[188:191], v149, s[52:55], 0 offen offset:1024 sc1\n\t"
        "buffer_load_dwordx4 v[192:195], v149, s[52:55], 0 offen offset:2048 sc1\n\t"
        "buffer_load_dwordx4 v[196:199], v149, s[52:55], 0 offen offset:3072 sc1\n\t"
        "buffer_load_dwordx4 v[200:203], v150, s[52:55], 0 offen offset:0 sc1\n\t"
        "buffer_load_dwordx4 v[204:207], v150, s[52:55], 0 offen offset:1024 sc1\n\t"
        "buffer_load_dwordx4 v[208:211], v150, s[52:55], 0 offen offset:2048 sc1\n\t"
        "buffer_load_dwordx4 v[212:215], v150, s[52:55], 0 offen offset:3072 sc1\n\t"
        "s_mov_b32 s46, 1\n\t"
        "s_branch 31b\n\t"
        "32:\n\t"
        "s_add_u32 s44, s44, 0x4000\n\t"
        "s_and_b32 s44, s44, 0x3ffff\n\t"
        "s_cmp_eq_u32 s44, s49\n\t"
        "s_cselect_b32 s44, s50, s44\n\t"
        "s_add_u32 s52, s40, s44\n\t"
        "s_addc_u32 s53, s41, 0\n\t"
        "buffer_load_dwordx4 v[184:187], v149, s[52:55], 0 offen offset:0 sc1\n\t"
        "buffer_load_dwordx4 v[188:191], v149, s[52:55], 0 offen offset:1024 sc1\n\t"
        "buffer_load_dwordx4 v[192:195], v149, s[52:55], 0 offen offset:2048 sc1\n\t"
        "buffer_load_dwordx4 v[196:199], v149, s[52:55], 0 offen offset:3072 sc1\n\t"
        "buffer_load_dwordx4 v[200:203], v150, s[52:55], 0 offen offset:0 sc1\n\t"
        "buffer_load_dwordx4 v[204:207], v150, s[52:55], 0 offen offset:1024 sc1\n\t"
        "buffer_load_dwordx4 v[208:211], v150, s[52:55], 0 offen offset:2048 sc1\n\t"
        "buffer_load_dwordx4 v[212:215], v150, s[52:55], 0 offen offset:3072 sc1\n\t"
        "s_mov_b32 s46, 1\n\t"
        "s_branch 33b\n\t"
        "40:\n\t"
        "s_mov_b32 s45, 0x100000\n\t"
        "42:\n\t"
        "ds_read_b32 v151, v221\n\t"
        "s_sub_u32 s45, s45, 1\n\t"
        "s_waitcnt lgkmcnt(0)\n\t"
        "s_cbranch_scc1 41b\n\t"
        "v_cmp_ne_u32 vcc, v222, v151\n\t"
        "s_cbranch_vccnz 42b\n\t"
        "s_branch 41b\n\t"
        "9:\n\t"
        : "+{v[0:15]}"(o0), "+{v[16:31]}"(o1), "+{v[32:47]}"(o2), "+{v[48:63]}"(o3), "+{v[128:143]}"(lv), "+{s44}"(soff), "+{v[152:167]}"(kva), "+{v[168:183]}"(kvb), "+{v[184:191]}"(fv),
          [r0] "+v"(x1p[0]), [r1] "+v"(x1p[1]), [r2] "+v"(x1p[2]), [r3] "+v"(x1p[3]) STAMP_OUTS
        : "{v[64:71]}"(q[0]), "{v[72:79]}"(q[1]), "{v[80:87]}"(q[2]), "{v[88:95]}"(q[3]), "{v[144:147]}"(ones), "{v148}"(0xddddu), "{v149}"(kvoff), "{v150}"(kvoff + 4096),
          "{v[216:223]}"(cv), "{v224}"(lbase + L_W + 16), "{s[40:43]}"(srd), "{s47}"(L), "{s49}"(tm * 16384), "{s50}"(((tm + 1) & 15) * 16384), [xb] "s"(x0)
        : "v96", "v97", "v98", "v99", "v100", "v101", "v102", "v103", "v104", "v105", "v106", "v107", "v108", "v109", "v110", "v111", "v112", "v113", "v114", "v115", "v116", "v117", "v118", "v119", "v120", "v121", "v122", "v123", "v124", "v125", "v126", "v127", "v151", "v192", "v193", "v194", "v195", "v196", "v197", "v198", "v199", "v200", "v201", "v202", "v203", "v204", "v205", "v206", "v207", "v208", "v209", "v210", "v211", "v212", "v213", "v214", "v215", "s45", "s46", "s52", "s53", "s54", "s55", "vcc", "scc", "memory");
      STAMP_PUT(6 + 16 * L);
      STAMPV(9 + 16 * L);
      PHASE_VARS;
      const unsigned dst = OFF_OF + L * (5 * MBY) + (tm * 4 + 2 * kh) * TS + (tn * 2 + hh) * 2048 + lane * 16;
      auto put = [&](int u, const f32x16& o) {
#pragma unroll
        for (int c = 0; c < 2; ++c) {
          u32x4 w;
#pragma unroll
          for (int e = 0; e < 4; ++e) w[e] = pack2(o[8 * c + 2 * e], o[8 * c + 2 * e + 1]);
          stg(rs, dst + u * TS + c * 1024, w);
        }
      };
      const float la = lv[8] + lv[0], lb = lv[12] + lv[4];
      o2 = (o2 + o0) * RCP(la); put(0, o2);
      SBAR();
      o3 = (o3 + o1) * RCP(lb); put(1, o3);
      SBAR();
      {
        const f32x16 t = o2 + o3;
        float sum = 0.f;
#pragma unroll
        for (int e = 0; e < 16; e += 4) sum += (t[e] + t[e + 1]) + (t[e + 2] + t[e + 3]);
        if (__any(!(fabsf(sum) < 1e37f) | !(fmaxf(la, lb) < 1e27f) | !(fminf(la, lb) > 1e-9f) | !(la + lb == la + lb))) {
          DRAIN();
          attn_safe(rs, kvoff - kh * 8192, q[2], ones, o2); put(0, o2);
          attn_safe(rs, kvoff - kh * 8192, q[3], ones, o3); put(1, o3);
        }
      }
    }
    STAMPV(10 + 16 * L);
    {
      f32x16 acc[8];
      u32x16 wrc;
      STAMP_DECL;
      { PHASE_VARS;
      float r0 = 0.f, r1 = 1.f;
      if (L == 0) {
#pragma unroll
        for (int i = 0; i < 4; ++i) x1p[i] = fo_swap(x1p[i]);
      } else {
        r0 = reinterpret_cast<const float*>(smem + L_AB)[256 + wid * 64 + r32]; r1 = reinterpret_cast<const float*>(smem + L_AB)[256 + wid * 64 + 32 + r32];
      }
#pragma unroll
      for (int e = 0; e < 16; ++e) { acc[e >> 3][2 * (e & 7)] = lo16(x1p[e >> 2][e & 3]) * r1 + r0; acc[e >> 3][2 * (e & 7) + 1] = hi16(x1p[e >> 2][e & 3]) * r1 + r0; }
      PIN(acc[0]); PIN(acc[1]);
      asm volatile("s_waitcnt vmcnt(0)\n\tds_write_b32 %0, %1" :: "v"(lbase + L_W + 80 + 4 * wid), "v"(2 * L + 2) : "memory");
      STAMPV(11 + 16 * L);
      const unsigned a0 = lbase + wrow * 128 + ((wch ^ wsw) << 4) + 8 * (tp & 1), a1 = lbase + wrow * 128 + (((4 + wch) ^ wsw) << 4) + 8 * (tp & 1);
      asm volatile(
        "s_mov_b32 s46, 0\n\t"
        "ostart_%=:\n\t"
        "s_mov_b32 s48, 0x200000\n\t"
        "2:\n\t"
        "ds_read_b32 v233, v232\n\t"
        "s_sub_u32 s48, s48, 1\n\t"
        "s_waitcnt lgkmcnt(0)\n\t"
        "s_cselect_b32 s47, 15, 0\n\t"
        "v_readfirstlane_b32 s51, v233\n\t"
        "s_nop 3\n\t"
        "s_or_b32 s51, s51, s47\n\t"
        "s_andn2_b32 s47, s51, s46\n\t"
        "s_cbranch_scc0 2b\n\t"
        "s_andn2_b32 s51, s51, s46\n\t"
        "s_ff1_i32_b32 s47, s51\n\t"
        "s_add_u32 s45, s47, s49\n\t"
        "s_and_b32 s45, s45, 3\n\t"
        "s_lshl_b32 s45, s45, 14\n\t"
        "s_and_b32 s51, s47, 0x80000000\n\t"
        "v_or_b32 v225, s51, v224\n\t"
        "s_and_b32 s51, s47, 3\n\t"
        "s_lshl_b32 s51, s51, 15\n\t"
        "v_add_u32 v230, s51, v226\n\t"
        "v_add_u32 v231, s51, v227\n\t"
        "buffer_load_dwordx4 v[160:163], v225, s[40:43], s45 offen offset:0 sc1\n\t"
        "buffer_load_dwordx4 v[164:167], v225, s[40:43], s45 offen offset:1024 sc1\n\t"
        "buffer_load_dwordx4 v[168:171], v225, s[40:43], s45 offen offset:2048 sc1\n\t"
        "buffer_load_dwordx4 v[172:175], v225, s[40:43], s45 offen offset:3072 sc1\n\t"
        "s_add_u32 s45, s45, 0x1000\n\t"
        "buffer_load_dwordx4 v[176:179], v225, s[40:43], s45 offen offset:0 sc1\n\t"
        "buffer_load_dwordx4 v[180:183], v225, s[40:43], s45 offen offset:1024 sc1\n\t"
        "buffer_load_dwordx4 v[184:187], v225, s[40:43], s45 offen offset:2048 sc1\n\t"
        "buffer_load_dwordx4 v[188:191], v225, s[40:43], s45 offen offset:3072 sc1\n\t"
        "s_add_u32 s45, s45, 0x1000\n\t"
        "buffer_load_dwordx4 v[192:195], v225, s[40:43], s45 offen offset:0 sc1\n\t"
        "buffer_load_dwordx4 v[196:199], v225, s[40:43], s45 offen offset:1024 sc1\n\t"
        "buffer_load_dwordx4 v[200:203], v225, s[40:43], s45 offen offset:2048 sc1\n\t"
        "buffer_load_dwordx4 v[204:207], v225, s[40:43], s45 offen offset:3072 sc1\n\t"
        "ds_read_b64_tr_b16 v[128:129], v230 offset:0\n\t"
        "ds_read_b64_tr_b16 v[130:131], v230 offset:1024\n\t"
        "ds_read_b64_tr_b16 v[132:133], v231 offset:0\n\t"
        "ds_read_b64_tr_b16 v[134:135], v231 offset:1024\n\t"
        "ds_read_b64_tr_b16 v[136:137], v230 offset:2048\n\t"
        "ds_read_b64_tr_b16 v[138:139], v230 offset:3072\n\t"
        "ds_read_b64_tr_b16 v[140:141], v231 offset:2048\n\t"
        "ds_read_b64_tr_b16 v[142:143], v231 offset:3072\n\t"
        "ds_read_b64_tr_b16 v[144:145], v230 offset:4096\n\t"
        "ds_read_b64_tr_b16 v[146:147], v230 offset:5120\n\t"
        "ds_read_b64_tr_b16 v[148:149], v231 offset:4096\n\t"
        "ds_read_b64_tr_b16 v[150:151], v231 offset:5120\n\t"
        "ds_read_b64_tr_b16 v[152:153], v230 offset:6144\n\t"
        "ds_read_b64_tr_b16 v[154:155], v230 offset:7168\n\t"
        "ds_read_b64_tr_b16 v[156:157], v231 offset:6144\n\t"
        "ds_read_b64_tr_b16 v[158:159], v231 offset:7168\n\t"
        "s_mov_b32 s44, s45\n\t"
        "s_cmp_eq_u32 s47, 0\n\t"
        "s_cbranch_scc1 oseg0_%=\n\t"
        "s_cmp_eq_u32 s47, 1\n\t"
        "s_cbranch_scc1 oseg1_%=\n\t"
        "s_cmp_eq_u32 s47, 2\n\t"
        "s_cbranch_scc1 oseg2_%=\n\t"
        "s_cmp_eq_u32 s47, 3\n\t"
        "s_cbranch_scc1 oseg3_%=\n\t"
        "oseg0_%=:\n\t"
        "s_or_b32 s46, s46, 1\n\t"
        "s_waitcnt vmcnt(11) lgkmcnt(14)\n\t"
        "v_mfma_f32_32x32x16_bf16 v[0:15], v[128:131], v[160:163], v[0:15]\n\t"
        "ds_read_b64_tr_b16 v[128:129], v226 offset:8192\n\t"
        "ds_read_b64_tr_b16 v[130:131], v226 offset:9216\n\t"
        "s_waitcnt lgkmcnt(14)\n\t"
        "v_mfma_f32_32x32x16_bf16 v[16:31], v[132:135], v[160:163], v[16:31]\n\t"
        "ds_read_b64_tr_b16 v[132:133], v227 offset:8192\n\t"
        "ds_read_b64_tr_b16 v[134:135], v227 offset:9216\n\t"
        "s_add_u32 s44, s44, 0x1000\n\t"
        "buffer_load_dwordx4 v[208:211], v224, s[40:43], s44 offen offset:0 sc1\n\t"
        "s_waitcnt vmcnt(11) lgkmcnt(14)\n\t"
        "v_mfma_f32_32x32x16_bf16 v[0:15], v[136:139], v[164:167], v[0:15]\n\t"
        "ds_read_b32 v233, v232\n\t"
        "ds_read_b64_tr_b16 v[136:137], v226 offset:10240\n\t"
        "ds_read_b64_tr_b16 v[138:139], v226 offset:11264\n\t"
        "s_waitcnt lgkmcnt(14)\n\t"
        "v_mfma_f32_32x32x16_bf16 v[16:31], v[140:143], v[164:167], v[16:31]\n\t"
        "ds_read_b64_tr_b16 v[140:141], v227 offset:10240\n\t"
        "ds_read_b64_tr_b16 v[142:143], v227 offset:11264\n\t"
        "buffer_load_dwordx4 v[212:215], v224, s[40:43], s44 offen offset:1024 sc1\n\t"
        "s_waitcnt vmcnt(11) lgkmcnt(14)\n\t"
        "v_mfma_f32_32x32x16_bf16 v[0:15], v[144:147], v[168:171], v[0:15]\n\t"
        "ds_read_b64_tr_b16 v[144:145], v226 offset:12288\n\t"
        "ds_read_b64_tr_b16 v[146:147], v226 offset:13312\n\t"
        "s_waitcnt lgkmcnt(14)\n\t"
        "v_mfma_f32_32x32x16_bf16 v[16:31], v[148:151], v[168:171], v[16:31]\n\t"
        "ds_read_b64_tr_b16 v[148:149], v227 offset:12288\n\t"
        "ds_read_b64_tr_b16 v[150:151], v227 offset:13312\n\t"
        "buffer_load_dwordx4 v[216:219], v224, s[40:43], s44 offen offset:2048 sc1\n\t"
        "s_waitcnt vmcnt(11) lgkmcnt(8)\n\t"
        "v_mfma_f32_32x32x16_bf16 v[0:15], v[152:155], v[172:175], v[0:15]\n\t"
        "v_readfirstlane_b32 s51, v233\n\t"
        "ds_read_b64_tr_b16 v[152:153], v226 offset:14336\n\t"
        "ds_read_b64_tr_b16 v[154:155], v226 offset:15360\n\t"
        "s_waitcnt lgkmcnt(14)\n\t"
        "v_mfma_f32_32x32x16_bf16 v[16:31], v[156:159], v[172:175], v[16:31]\n\t"
        "s_andn2_b32 s51, s51, s46\n\t"
        "s_ff1_i32_b32 s47, s51\n\t"
        "s_add_u32 s45, s47, s49\n\t"
        "s_and_b32 s45, s45, 3\n\t"
        "s_lshl_b32 s45, s45, 14\n\t"
        "ds_read_b64_tr_b16 v[156:157], v227 offset:14336\n\t"
        "ds_read_b64_tr_b16 v[158:159], v227 offset:15360\n\t"
        "buffer_load_dwordx4 v[220:223], v224, s[40:43], s44 offen offset:3072 sc1\n\t"
        "s_waitcnt vmcnt(11) lgkmcnt(14)\n\t"
        "v_mfma_f32_32x32x16_bf16 v[0:15], v[128:131], v[176:179], v[0:15]\n\t"
        "s_and_b32 s51, s47, 0x80000000\n\t"
        "v_or_b32 v225, s51, v224\n\t"
        "s_and_b32 s51, s47, 3\n\t"
        "s_lshl_b32 s51, s51, 15\n\t"
        "v_add_u32 v230, s51, v226\n\t"
        "v_add_u32 v231, s51, v227\n\t"
        "ds_read_b64_tr_b16 v[128:129], v226 offset:16384\n\t"
        "ds_read_b64_tr_b16 v[130:131], v226 offset:17408\n\t"
        "s_waitcnt lgkmcnt(14)\n\t"
        "v_mfma_f32_32x32x16_bf16 v[16:31], v[132:135], v[176:179], v[16:31]\n\t"
        "ds_read_b64_tr_b16 v[132:133], v227 offset:16384\n\t"
        "ds_read_b64_tr_b16 v[134:135], v227 offset:17408\n\t"
        "buffer_load_dwordx4 v[160:163], v225, s[40:43], s45 offen offset:0 sc1\n\t"
        "s_waitcnt vmcnt(11) lgkmcnt(14)\n\t"
        "v_mfma_f32_32x32x16_bf16 v[0:15], v[136:139], v[180:183], v[0:15]\n\t"
        "ds_read_b64_tr_b16 v[136:137], v226 offset:18432\n\t"
        "ds_read_b64_tr_b16 v[138:139], v226 offset:19456\n\t"
        "s_waitcnt lgkmcnt(14)\n\t"
        "v_mfma_f32_32x32x16_bf16 v[16:31], v[140:143], v[180:183], v[16:31]\n\t"
        "ds_read_b64_tr_b16 v[140:141], v227 offset:18432\n\t"
        "ds_read_b64_tr_b16 v[142:143], v227 offset:19456\n\t"
        "buffer_load_dwordx4 v[164:167], v225, s[40:43], s45 offen offset:1024 sc1\n\t"
        "s_waitcnt vmcnt(11) lgkmcnt(14)\n\t"
        "v_mfma_f32_32x32x16_bf16 v[0:15], v[144:147], v[184:187], v[0:15]\n\t"
        "ds_read_b64_tr_b16 v[144:145], v226 offset:20480\n\t"
        "ds_read_b64_tr_b16 v[146:147], v226 offset:21504\n\t"
        "s_waitcnt lgkmcnt(14)\n\t"
        "v_mfma_f32_32x32x16_bf16 v[16:31], v[148:151], v[184:187], v[16:31]\n\t"
        "ds_read_b64_tr_b16 v[148:149], v227 offset:20480\n\t"
        "ds_read_b64_tr_b16 v[150:151], v227 offset:21504\n\t"
        "buffer_load_dwordx4 v[168:171], v225, s[40:43], s45 offen offset:2048 sc1\n\t"
        "s_waitcnt vmcnt(11) lgkmcnt(14)\n\t"
        "v_mfma_f32_32x32x16_bf16 v[0:15], v[152:155], v[188:191], v[0:15]\n\t"
        "ds_read_b64_tr_b16 v[152:153], v226 offset:22528\n\t"
        "ds_read_b64_tr_b16 v[154:155], v226 offset:23552\n\t"
        "s_waitcnt lgkmcnt(14)\n\t"
        "v_mfma_f32_32x32x16_bf16 v[16:31], v[156:159], v[188:191], v[16:31]\n\t"
        "ds_read_b64_tr_b16 v[156:157], v227 offset:22528\n\t"
        "ds_read_b64_tr_b16 v[158:159], v227 offset:23552\n\t"
        "buffer_load_dwordx4 v[172:175], v225, s[40:43], s45 offen offset:3072 sc1\n\t"
        "s_waitcnt vmcnt(11) lgkmcnt(14)\n\t"
        "v_mfma_f32_32x32x16_bf16 v[0:15], v[128:131], v[192:195], v[0:15]\n\t"
        "ds_read_b64_tr_b16 v[128:129], v226 offset:24576\n\t"
        "ds_read_b64_tr_b16 v[130:131], v226 offset:25600\n\t"
        "s_waitcnt lgkmcnt(14)\n\t"
        "v_mfma_f32_32x32x16_bf16 v[16:31], v[132:135], v[192:195], v[16:31]\n\t"
        "ds_read_b64_tr_b16 v[132:133], v227 offset:24576\n\t"
        "ds_read_b64_tr_b16 v[134:135], v227 offset:25600\n\t"
        "s_add_u32 s45, s45, 0x1000\n\t"
        "buffer_load_dwordx4 v[176:179], v225, s[40:43], s45 offen offset:0 sc1\n\t"
        "s_waitcnt vmcnt(11) lgkmcnt(14)\n\t"
        "v_mfma_f32_32x32x16_bf16 v[0:15], v[136:139], v[196:199], v[0:15]\n\t"
        "ds_read_b64_tr_b16 v[136:137], v226 offset:26624\n\t"
        "ds_read_b64_tr_b16 v[138:139], v226 offset:27648\n\t"
        "s_waitcnt lgkmcnt(14)\n\t"
        "v_mfma_f32_32x32x16_bf16 v[16:31], v[140:143], v[196:199], v[16:31]\n\t"
        "ds_read_b64_tr_b16 v[140:141], v227 offset:26624\n\t"
        "ds_read_b64_tr_b16 v[142:143], v227 offset:27648\n\t"
        "buffer_load_dwordx4 v[180:183], v225, s[40:43], s45 offen offset:1024 sc1\n\t"
        "s_waitcnt vmcnt(11) lgkmcnt(14)\n\t"
        "v_mfma_f32_32x32x16_bf16 v[0:15], v[144:147], v[200:203], v[0:15]\n\t"
        "ds_read_b64_tr_b16 v[144:145], v226 offset:28672\n\t"
        "ds_read_b64_tr_b16 v[146:147], v226 offset:29696\n\t"
        "s_waitcnt lgkmcnt(14)\n\t"
        "v_mfma_f32_32x32x16_bf16 v[16:31], v[148:151], v[200:203], v[16:31]\n\t"
        "ds_read_b64_tr_b16 v[148:149], v227 offset:28672\n\t"
        "ds_read_b64_tr_b16 v[150:151], v227 offset:29696\n\t"
        "buffer_load_dwordx4 v[184:187], v225, s[40:43], s45 offen offset:2048 sc1\n\t"
        "s_waitcnt vmcnt(11) lgkmcnt(14)\n\t"
        "v_mfma_f32_32x32x16_bf16 v[0:15], v[152:155], v[204:207], v[0:15]\n\t"
        "ds_read_b64_tr_b16 v[152:153], v226 offset:30720\n\t"
        "ds_read_b64_tr_b16 v[154:155], v226 offset:31744\n\t"
        "s_waitcnt lgkmcnt(14)\n\t"
        "v_mfma_f32_32x32x16_bf16 v[16:31], v[156:159], v[204:207], v[16:31]\n\t"
        "ds_read_b64_tr_b16 v[156:157], v227 offset:30720\n\t"
        "ds_read_b64_tr_b16 v[158:159], v227 offset:31744\n\t"
        "buffer_load_dwordx4 v[188:191], v225, s[40:43], s45 offen offset:3072 sc1\n\t"
        "s_waitcnt vmcnt(11) lgkmcnt(14)\n\t"
        "v_mfma_f32_32x32x16_bf16 v[0:15], v[128:131], v[208:211], v[0:15]\n\t"
        "ds_read_b64_tr_b16 v[128:129], v230 offset:0\n\t"
        "ds_read_b64_tr_b16 v[130:131], v230 offset:1024\n\t"
        "s_waitcnt lgkmcnt(14)\n\t"
        "v_mfma_f32_32x32x16_bf16 v[16:31], v[132:135], v[208:211], v[16:31]\n\t"
        "ds_read_b64_tr_b16 v[132:133], v231 offset:0\n\t"
        "ds_read_b64_tr_b16 v[134:135], v231 offset:1024\n\t"
        "s_add_u32 s45, s45, 0x1000\n\t"
        "buffer_load_dwordx4 v[192:195], v225, s[40:43], s45 offen offset:0 sc1\n\t"
        "s_waitcnt vmcnt(11) lgkmcnt(14)\n\t"
        "v_mfma_f32_32x32x16_bf16 v[0:15], v[136:139], v[212:215], v[0:15]\n\t"
        "ds_read_b64_tr_b16 v[136:137], v230 offset:2048\n\t"
        "ds_read_b64_tr_b16 v[138:139], v230 offset:3072\n\t"
        "s_waitcnt lgkmcnt(14)\n\t"
        "v_mfma_f32_32x32x16_bf16 v[16:31], v[140:143], v[212:215], v[16:31]\n\t"
        "ds_read_b64_tr_b16 v[140:141], v231 offset:2048\n\t"
        "ds_read_b64_tr_b16 v[142:143], v231 offset:3072\n\t"
        "buffer_load_dwordx4 v[196:199], v225, s[40:43], s45 offen offset:1024 sc1\n\t"
        "s_waitcnt vmcnt(11) lgkmcnt(14)\n\t"
        "v_mfma_f32_32x32x16_bf16 v[0:15], v[144:147], v[216:219], v[0:15]\n\t"
        "ds_read_b64_tr_b16 v[144:145], v230 offset:4096\n\t"
        "ds_read_b64_tr_b16 v[146:147], v230 offset:5120\n\t"
        "s_waitcnt lgkmcnt(14)\n\t"
        "v_mfma_f32_32x32x16_bf16 v[16:31], v[148:151], v[216:219], v[16:31]\n\t"
        "ds_read_b64_tr_b16 v[148:149], v231 offset:4096\n\t"
        "ds_read_b64_tr_b16 v[150:151], v231 offset:5120\n\t"
        "buffer_load_dwordx4 v[200:203], v225, s[40:43], s45 offen offset:2048 sc1\n\t"
        "s_waitcnt vmcnt(11) lgkmcnt(14)\n\t"
        "v_mfma_f32_32x32x16_bf16 v[0:15], v[152:155], v[220:223], v[0:15]\n\t"
        "ds_read_b64_tr_b16 v[152:153], v230 offset:6144\n\t"
        "ds_read_b64_tr_b16 v[154:155], v230 offset:7168\n\t"
        "s_waitcnt lgkmcnt(14)\n\t"
        "v_mfma_f32_32x32x16_bf16 v[16:31], v[156:159], v[220:223], v[16:31]\n\t"
        "ds_read_b64_tr_b16 v[156:157], v231 offset:6144\n\t"
        "ds_read_b64_tr_b16 v[158:159], v231 offset:7168\n\t"
        "buffer_load_dwordx4 v[204:207], v225, s[40:43], s45 offen offset:3072 sc1\n\t"
        "s_mov_b32 s44, s45\n\t"
        "s_cmp_eq_u32 s47, 0\n\t"
        "s_cbranch_scc1 oseg0_%=\n\t"
        "s_cmp_eq_u32 s47, 1\n\t"
        "s_cbranch_scc1 oseg1_%=\n\t"
        "s_cmp_eq_u32 s47, 2\n\t"
        "s_cbranch_scc1 oseg2_%=\n\t"
        "s_cmp_eq_u32 s47, 3\n\t"
        "s_cbranch_scc1 oseg3_%=\n\t"
        "s_cmp_lg_u32 s46, 15\n\t"
        "s_cbranch_scc1 ostart_%=\n\t"
        "s_branch oend_%=\n\t"
        "oseg1_%=:\n\t"
        "s_or_b32 s46, s46, 2\n\t"
        "s_waitcnt vmcnt(11) lgkmcnt(14)\n\t"
        "v_mfma_f32_32x32x16_bf16 v[32:47], v[128:131], v[160:163], 0\n\t"
        "ds_read_b64_tr_b16 v[128:129], v226 offset:40960\n\t"
        "ds_read_b64_tr_b16 v[130:131], v226 offset:41984\n\t"
        "s_waitcnt lgkmcnt(14)\n\t"
        "v_mfma_f32_32x32x16_bf16 v[48:63], v[132:135], v[160:163], 0\n\t"
        "ds_read_b64_tr_b16 v[132:133], v227 offset:40960\n\t"
        "ds_read_b64_tr_b16 v[134:135], v227 offset:41984\n\t"
        "s_add_u32 s44, s44, 0x1000\n\t"
        "buffer_load_dwordx4 v[208:211], v224, s[40:43], s44 offen offset:0 sc1\n\t"
        "s_waitcnt vmcnt(11) lgkmcnt(14)\n\t"
        "v_mfma_f32_32x32x16_bf16 v[32:47], v[136:139], v[164:167], v[32:47]\n\t"
        "ds_read_b32 v233, v232\n\t"
        "ds_read_b64_tr_b16 v[136:137], v226 offset:43008\n\t"
        "ds_read_b64_tr_b16 v[138:139], v226 offset:44032\n\t"
        "s_waitcnt lgkmcnt(14)\n\t"
        "v_mfma_f32_32x32x16_bf16 v[48:63], v[140:143], v[164:167], v[48:63]\n\t"
        "ds_read_b64_tr_b16 v[140:141], v227 offset:43008\n\t"
        "ds_read_b64_tr_b16 v[142:143], v227 offset:44032\n\t"
        "buffer_load_dwordx4 v[212:215], v224, s[40:43], s44 offen offset:1024 sc1\n\t"
        "s_waitcnt vmcnt(11) lgkmcnt(14)\n\t"
        "v_mfma_f32_32x32x16_bf16 v[32:47], v[144:147], v[168:171], v[32:47]\n\t"
        "ds_read_b64_tr_b16 v[144:145], v226 offset:45056\n\t"
        "ds_read_b64_tr_b16 v[146:147], v226 offset:46080\n\t"
        "s_waitcnt lgkmcnt(14)\n\t"
        "v_mfma_f32_32x32x16_bf16 v[48:63], v[148:151], v[168:171], v[48:63]\n\t"
        "ds_read_b64_tr_b16 v[148:149], v227 offset:45056\n\t"
        "ds_read_b64_tr_b16 v[150:151], v227 offset:46080\n\t"
        "buffer_load_dwordx4 v[216:219], v224, s[40:43], s44 offen offset:2048 sc1\n\t"
        "s_waitcnt vmcnt(11) lgkmcnt(8)\n\t"
        "v_mfma_f32_32x32x16_bf16 v[32:47], v[152:155], v[172:175], v[32:47]\n\t"
        "v_readfirstlane_b32 s51, v233\n\t"
        "ds_read_b64_tr_b16 v[152:153], v226 offset:47104\n\t"
        "ds_read_b64_tr_b16 v[154:155], v226 offset:48128\n\t"
        "s_waitcnt lgkmcnt(14)\n\t"
        "v_mfma_f32_32x32x16_bf16 v[48:63], v[156:159], v[172:175], v[48:63]\n\t"
        "s_andn2_b32 s51, s51, s46\n\t"
        "s_ff1_i32_b32 s47, s51\n\t"
        "s_add_u32 s45, s47, s49\n\t"
        "s_and_b32 s45, s45, 3\n\t"
        "s_lshl_b32 s45, s45, 14\n\t"
        "ds_read_b64_tr_b16 v[156:157], v227 offset:47104\n\t"
        "ds_read_b64_tr_b16 v[158:159], v227 offset:48128\n\t"
        "buffer_load_dwordx4 v[220:223], v224, s[40:43], s44 offen offset:3072 sc1\n\t"
        "s_waitcnt vmcnt(11) lgkmcnt(14)\n\t"
        "v_mfma_f32_32x32x16_bf16 v[32:47], v[128:131], v[176:179], v[32:47]\n\t"
        "s_and_b32 s51, s47, 0x80000000\n\t"
        "v_or_b32 v225, s51, v224\n\t"
        "s_and_b32 s51, s47, 3\n\t"
        "s_lshl_b32 s51, s51, 15\n\t"
        "v_add_u32 v230, s51, v226\n\t"
        "v_add_u32 v231, s51, v227\n\t"
        "ds_read_b64_tr_b16 v[128:129], v226 offset:49152\n\t"
        "ds_read_b64_tr_b16 v[130:131], v226 offset:50176\n\t"
        "s_waitcnt lgkmcnt(14)\n\t"
        "v_mfma_f32_32x32x16_bf16 v[48:63], v[132:135], v[176:179], v[48:63]\n\t"
        "ds_read_b64_tr_b16 v[132:133], v227 offset:49152\n\t"
        "ds_read_b64_tr_b16 v[134:135], v227 offset:50176\n\t"
        "buffer_load_dwordx4 v[160:163], v225, s[40:43], s45 offen offset:0 sc1\n\t"
        "s_waitcnt vmcnt(11) lgkmcnt(14)\n\t"
        "v_mfma_f32_32x32x16_bf16 v[32:47], v[136:139], v[180:183], v[32:47]\n\t"
        "ds_read_b64_tr_b16 v[136:137], v226 offset:51200\n\t"
        "ds_read_b64_tr_b16 v[138:139], v226 offset:52224\n\t"
        "s_waitcnt lgkmcnt(14)\n\t"
        "v_mfma_f32_32x32x16_bf16 v[48:63], v[140:143], v[180:183], v[48:63]\n\t"
        "ds_read_b64_tr_b16 v[140:141], v227 offset:51200\n\t"
        "ds_read_b64_tr_b16 v[142:143], v227 offset:52224\n\t"
        "buffer_load_dwordx4 v[164:167], v225, s[40:43], s45 offen offset:1024 sc1\n\t"
        "s_waitcnt vmcnt(11) lgkmcnt(14)\n\t"
        "v_mfma_f32_32x32x16_bf16 v[32:47], v[144:147], v[184:187], v[32:47]\n\t"
        "ds_read_b64_tr_b16 v[144:145], v226 offset:53248\n\t"
        "ds_read_b64_tr_b16 v[146:147], v226 offset:54272\n\t"
        "s_waitcnt lgkmcnt(14)\n\t"
        "v_mfma_f32_32x32x16_bf16 v[48:63], v[148:151], v[184:187], v[48:63]\n\t"
        "ds_read_b64_tr_b16 v[148:149], v227 offset:53248\n\t"
        "ds_read_b64_tr_b16 v[150:151], v227 offset:54272\n\t"
        "buffer_load_dwordx4 v[168:171], v225, s[40:43], s45 offen offset:2048 sc1\n\t"
        "s_waitcnt vmcnt(11) lgkmcnt(14)\n\t"
        "v_mfma_f32_32x32x16_bf16 v[32:47], v[152:155], v[188:191], v[32:47]\n\t"
        "ds_read_b64_tr_b16 v[152:153], v226 offset:55296\n\t"
        "ds_read_b64_tr_b16 v[154:155], v226 offset:56320\n\t"
        "s_waitcnt lgkmcnt(14)\n\t"
        "v_mfma_f32_32x32x16_bf16 v[48:63], v[156:159], v[188:191], v[48:63]\n\t"
        "ds_read_b64_tr_b16 v[156:157], v227 offset:55296\n\t"
        "ds_read_b64_tr_b16 v[158:159], v227 offset:56320\n\t"
        "buffer_load_dwordx4 v[172:175], v225, s[40:43], s45 offen offset:3072 sc1\n\t"
        "s_waitcnt vmcnt(11) lgkmcnt(14)\n\t"
        "v_mfma_f32_32x32x16_bf16 v[32:47], v[128:131], v[192:195], v[32:47]\n\t"
        "ds_read_b64_tr_b16 v[128:129], v226 offset:57344\n\t"
        "ds_read_b64_tr_b16 v[130:131], v226 offset:58368\n\t"
        "s_waitcnt lgkmcnt(14)\n\t"
        "v_mfma_f32_32x32x16_bf16 v[48:63], v[132:135], v[192:195], v[48:63]\n\t"
        "ds_read_b64_tr_b16 v[132:133], v227 offset:57344\n\t"
        "ds_read_b64_tr_b16 v[134:135], v227 offset:58368\n\t"
        "s_add_u32 s45, s45, 0x1000\n\t"
        "buffer_load_dwordx4 v[176:179], v225, s[40:43], s45 offen offset:0 sc1\n\t"
        "s_waitcnt vmcnt(11) lgkmcnt(14)\n\t"
        "v_mfma_f32_32x32x16_bf16 v[32:47], v[136:139], v[196:199], v[32:47]\n\t"
        "ds_read_b64_tr_b16 v[136:137], v226 offset:59392\n\t"
        "ds_read_b64_tr_b16 v[138:139], v226 offset:60416\n\t"
        "s_waitcnt lgkmcnt(14)\n\t"
        "v_mfma_f32_32x32x16_bf16 v[48:63], v[140:143], v[196:199], v[48:63]\n\t"
        "ds_read_b64_tr_b16 v[140:141], v227 offset:59392\n\t"
        "ds_read_b64_tr_b16 v[142:143], v227 offset:60416\n\t"
        "buffer_load_dwordx4 v[180:183], v225, s[40:43], s45 offen offset:1024 sc1\n\t"
        "s_waitcnt vmcnt(11) lgkmcnt(14)\n\t"
        "v_mfma_f32_32x32x16_bf16 v[32:47], v[144:147], v[200:203], v[32:47]\n\t"
        "ds_read_b64_tr_b16 v[144:145], v226 offset:61440\n\t"
        "ds_read_b64_tr_b16 v[146:147], v226 offset:62464\n\t"
        "s_waitcnt lgkmcnt(14)\n\t"
        "v_mfma_f32_32x32x16_bf16 v[48:63], v[148:151], v[200:203], v[48:63]\n\t"
        "ds_read_b64_tr_b16 v[148:149], v227 offset:61440\n\t"
        "ds_read_b64_tr_b16 v[150:151], v227 offset:62464\n\t"
        "buffer_load_dwordx4 v[184:187], v225, s[40:43], s45 offen offset:2048 sc1\n\t"
        "s_waitcnt vmcnt(11) lgkmcnt(14)\n\t"
        "v_mfma_f32_32x32x16_bf16 v[32:47], v[152:155], v[204:207], v[32:47]\n\t"
        "ds_read_b64_tr_b16 v[152:153], v226 offset:63488\n\t"
        "ds_read_b64_tr_b16 v[154:155], v226 offset:64512\n\t"
        "s_waitcnt lgkmcnt(14)\n\t"
        "v_mfma_f32_32x32x16_bf16 v[48:63], v[156:159], v[204:207], v[48:63]\n\t"
        "ds_read_b64_tr_b16 v[156:157], v227 offset:63488\n\t"
        "ds_read_b64_tr_b16 v[158:159], v227 offset:64512\n\t"
        "buffer_load_dwordx4 v[188:191], v225, s[40:43], s45 offen offset:3072 sc1\n\t"
        "s_waitcnt vmcnt(11) lgkmcnt(14)\n\t"
        "v_mfma_f32_32x32x16_bf16 v[32:47], v[128:131], v[208:211], v[32:47]\n\t"
        "ds_read_b64_tr_b16 v[128:129], v230 offset:0\n\t"
        "ds_read_b64_tr_b16 v[130:131], v230 offset:1024\n\t"
        "s_waitcnt lgkmcnt(14)\n\t"
        "v_mfma_f32_32x32x16_bf16 v[48:63], v[132:135], v[208:211], v[48:63]\n\t"
        "ds_read_b64_tr_b16 v[132:133], v231 offset:0\n\t"
        "ds_read_b64_tr_b16 v[134:135], v231 offset:1024\n\t"
        "s_add_u32 s45, s45, 0x1000\n\t"
        "buffer_load_dwordx4 v[192:195], v225, s[40:43], s45 offen offset:0 sc1\n\t"
        "s_waitcnt vmcnt(11) lgkmcnt(14)\n\t"
        "v_mfma_f32_32x32x16_bf16 v[32:47], v[136:139], v[212:215], v[32:47]\n\t"
        "ds_read_b64_tr_b16 v[136:137], v230 offset:2048\n\t"
        "ds_read_b64_tr_b16 v[138:139], v230 offset:3072\n\t"
        "s_waitcnt lgkmcnt(14)\n\t"
        "v_mfma_f32_32x32x16_bf16 v[48:63], v[140:143], v[212:215], v[48:63]\n\t"
        "ds_read_b64_tr_b16 v[140:141], v231 offset:2048\n\t"
        "ds_read_b64_tr_b16 v[142:143], v231 offset:3072\n\t"
        "buffer_load_dwordx4 v[196:199], v225, s[40:43], s45 offen offset:1024 sc1\n\t"
        "s_waitcnt vmcnt(11) lgkmcnt(14)\n\t"
        "v_mfma_f32_32x32x16_bf16 v[32:47], v[144:147], v[216:219], v[32:47]\n\t"
        "ds_read_b64_tr_b16 v[144:145], v230 offset:4096\n\t"
        "ds_read_b64_tr_b16 v[146:147], v230 offset:5120\n\t"
        "s_waitcnt lgkmcnt(14)\n\t"
        "v_mfma_f32_32x32x16_bf16 v[48:63], v[148:151], v[216:219], v[48:63]\n\t"
        "ds_read_b64_tr_b16 v[148:149], v231 offset:4096\n\t"
        "ds_read_b64_tr_b16 v[150:151], v231 offset:5120\n\t"
        "buffer_load_dwordx4 v[200:203], v225, s[40:43], s45 offen offset:2048 sc1\n\t"
        "s_waitcnt vmcnt(11) lgkmcnt(14)\n\t"
        "v_mfma_f32_32x32x16_bf16 v[32:47], v[152:155], v[220:223], v[32:47]\n\t"
        "ds_read_b64_tr_b16 v[152:153], v230 offset:6144\n\t"
        "ds_read_b64_tr_b16 v[154:155], v230 offset:7168\n\t"
        "s_waitcnt lgkmcnt(14)\n\t"
        "v_mfma_f32_32x32x16_bf16 v[48:63], v[156:159], v[220:223], v[48:63]\n\t"
        "ds_read_b64_tr_b16 v[156:157], v231 offset:6144\n\t"
        "ds_read_b64_tr_b16 v[158:159], v231 offset:7168\n\t"
        "buffer_load_dwordx4 v[204:207], v225, s[40:43], s45 offen offset:3072 sc1\n\t"
        "s_mov_b32 s44, s45\n\t"
        "s_cmp_eq_u32 s47, 0\n\t"
        "s_cbranch_scc1 oseg0_%=\n\t"
        "s_cmp_eq_u32 s47, 1\n\t"
        "s_cbranch_scc1 oseg1_%=\n\t"
        "s_cmp_eq_u32 s47, 2\n\t"
        "s_cbranch_scc1 oseg2_%=\n\t"
        "s_cmp_eq_u32 s47, 3\n\t"
        "s_cbranch_scc1 oseg3_%=\n\t"
        "s_cmp_lg_u32 s46, 15\n\t"
        "s_cbranch_scc1 ostart_%=\n\t"
        "s_branch oend_%=\n\t"
        "oseg2_%=:\n\t"
        "s_or_b32 s46, s46, 4\n\t"
        "s_waitcnt vmcnt(11) lgkmcnt(14)\n\t"
        "v_mfma_f32_32x32x16_bf16 v[64:79], v[128:131], v[160:163], 0\n\t"
        "ds_read_b64_tr_b16 v[128:129], v228 offset:8192\n\t"
        "ds_read_b64_tr_b16 v[130:131], v228 offset:9216\n\t"
        "s_waitcnt lgkmcnt(14)\n\t"
        "v_mfma_f32_32x32x16_bf16 v[80:95], v[132:135], v[160:163], 0\n\t"
        "ds_read_b64_tr_b16 v[132:133], v229 offset:8192\n\t"
        "ds_read_b64_tr_b16 v[134:135], v229 offset:9216\n\t"
        "s_add_u32 s44, s44, 0x1000\n\t"
        "buffer_load_dwordx4 v[208:211], v224, s[40:43], s44 offen offset:0 sc1\n\t"
        "s_waitcnt vmcnt(11) lgkmcnt(14)\n\t"
        "v_mfma_f32_32x32x16_bf16 v[64:79], v[136:139], v[164:167], v[64:79]\n\t"
        "ds_read_b32 v233, v232\n\t"
        "ds_read_b64_tr_b16 v[136:137], v228 offset:10240\n\t"
        "ds_read_b64_tr_b16 v[138:139], v228 offset:11264\n\t"
        "s_waitcnt lgkmcnt(14)\n\t"
        "v_mfma_f32_32x32x16_bf16 v[80:95], v[140:143], v[164:167], v[80:95]\n\t"
        "ds_read_b64_tr_b16 v[140:141], v229 offset:10240\n\t"
        "ds_read_b64_tr_b16 v[142:143], v229 offset:11264\n\t"
        "buffer_load_dwordx4 v[212:215], v224, s[40:43], s44 offen offset:1024 sc1\n\t"
        "s_waitcnt vmcnt(11) lgkmcnt(14)\n\t"
        "v_mfma_f32_32x32x16_bf16 v[64:79], v[144:147], v[168:171], v[64:79]\n\t"
        "ds_read_b64_tr_b16 v[144:145], v228 offset:12288\n\t"
        "ds_read_b64_tr_b16 v[146:147], v228 offset:13312\n\t"
        "s_waitcnt lgkmcnt(14)\n\t"
        "v_mfma_f32_32x32x16_bf16 v[80:95], v[148:151], v[168:171], v[80:95]\n\t"
        "ds_read_b64_tr_b16 v[148:149], v229 offset:12288\n\t"
        "ds_read_b64_tr_b16 v[150:151], v229 offset:13312\n\t"
        "buffer_load_dwordx4 v[216:219], v224, s[40:43], s44 offen offset:2048 sc1\n\t"
        "s_waitcnt vmcnt(11) lgkmcnt(8)\n\t"
        "v_mfma_f32_32x32x16_bf16 v[64:79], v[152:155], v[172:175], v[64:79]\n\t"
        "v_readfirstlane_b32 s51, v233\n\t"
        "ds_read_b64_tr_b16 v[152:153], v228 offset:14336\n\t"
        "ds_read_b64_tr_b16 v[154:155], v228 offset:15360\n\t"
        "s_waitcnt lgkmcnt(14)\n\t"
        "v_mfma_f32_32x32x16_bf16 v[80:95], v[156:159], v[172:175], v[80:95]\n\t"
        "s_andn2_b32 s51, s51, s46\n\t"
        "s_ff1_i32_b32 s47, s51\n\t"
        "s_add_u32 s45, s47, s49\n\t"
        "s_and_b32 s45, s45, 3\n\t"
        "s_lshl_b32 s45, s45, 14\n\t"
        "ds_read_b64_tr_b16 v[156:157], v229 offset:14336\n\t"
        "ds_read_b64_tr_b16 v[158:159], v229 offset:15360\n\t"
        "buffer_load_dwordx4 v[220:223], v224, s[40:43], s44 offen offset:3072 sc1\n\t"
        "s_waitcnt vmcnt(11) lgkmcnt(14)\n\t"
        "v_mfma_f32_32x32x16_bf16 v[64:79], v[128:131], v[176:179], v[64:79]\n\t"
        "s_and_b32 s51, s47, 0x80000000\n\t"
        "v_or_b32 v225, s51, v224\n\t"
        "s_and_b32 s51, s47, 3\n\t"
        "s_lshl_b32 s51, s51, 15\n\t"
        "v_add_u32 v230, s51, v226\n\t"
        "v_add_u32 v231, s51, v227\n\t"
        "ds_read_b64_tr_b16 v[128:129], v228 offset:16384\n\t"
        "ds_read_b64_tr_b16 v[130:131], v228 offset:17408\n\t"
        "s_waitcnt lgkmcnt(14)\n\t"
        "v_mfma_f32_32x32x16_bf16 v[80:95], v[132:135], v[176:179], v[80:95]\n\t"
        "ds_read_b64_tr_b16 v[132:133], v229 offset:16384\n\t"
        "ds_read_b64_tr_b16 v[134:135], v229 offset:17408\n\t"
        "buffer_load_dwordx4 v[160:163], v225, s[40:43], s45 offen offset:0 sc1\n\t"
        "s_waitcnt vmcnt(11) lgkmcnt(14)\n\t"
        "v_mfma_f32_32x32x16_bf16 v[64:79], v[136:139], v[180:183], v[64:79]\n\t"
        "ds_read_b64_tr_b16 v[136:137], v228 offset:18432\n\t"
        "ds_read_b64_tr_b16 v[138:139], v228 offset:19456\n\t"
        "s_waitcnt lgkmcnt(14)\n\t"
        "v_mfma_f32_32x32x16_bf16 v[80:95], v[140:143], v[180:183], v[80:95]\n\t"
        "ds_read_b64_tr_b16 v[140:141], v229 offset:18432\n\t"
        "ds_read_b64_tr_b16 v[142:143], v229 offset:19456\n\t"
        "buffer_load_dwordx4 v[164:167], v225, s[40:43], s45 offen offset:1024 sc1\n\t"
        "s_waitcnt vmcnt(11) lgkmcnt(14)\n\t"
        "v_mfma_f32_32x32x16_bf16 v[64:79], v[144:147], v[184:187], v[64:79]\n\t"
        "ds_read_b64_tr_b16 v[144:145], v228 offset:20480\n\t"
        "ds_read_b64_tr_b16 v[146:147], v228 offset:21504\n\t"
        "s_waitcnt lgkmcnt(14)\n\t"
        "v_mfma_f32_32x32x16_bf16 v[80:95], v[148:151], v[184:187], v[80:95]\n\t"
        "ds_read_b64_tr_b16 v[148:149], v229 offset:20480\n\t"
        "ds_read_b64_tr_b16 v[150:151], v229 offset:21504\n\t"
        "buffer_load_dwordx4 v[168:171], v225, s[40:43], s45 offen offset:2048 sc1\n\t"
        "s_waitcnt vmcnt(11) lgkmcnt(14)\n\t"
        "v_mfma_f32_32x32x16_bf16 v[64:79], v[152:155], v[188:191], v[64:79]\n\t"
        "ds_read_b64_tr_b16 v[152:153], v228 offset:22528\n\t"
        "ds_read_b64_tr_b16 v[154:155], v228 offset:23552\n\t"
        "s_waitcnt lgkmcnt(14)\n\t"
        "v_mfma_f32_32x32x16_bf16 v[80:95], v[156:159], v[188:191], v[80:95]\n\t"
        "ds_read_b64_tr_b16 v[156:157], v229 offset:22528\n\t"
        "ds_read_b64_tr_b16 v[158:159], v229 offset:23552\n\t"
        "buffer_load_dwordx4 v[172:175], v225, s[40:43], s45 offen offset:3072 sc1\n\t"
        "s_waitcnt vmcnt(11) lgkmcnt(14)\n\t"
        "v_mfma_f32_32x32x16_bf16 v[64:79], v[128:131], v[192:195], v[64:79]\n\t"
        "ds_read_b64_tr_b16 v[128:129], v228 offset:24576\n\t"
        "ds_read_b64_tr_b16 v[130:131], v228 offset:25600\n\t"
        "s_waitcnt lgkmcnt(14)\n\t"
        "v_mfma_f32_32x32x16_bf16 v[80:95], v[132:135], v[192:195], v[80:95]\n\t"
        "ds_read_b64_tr_b16 v[132:133], v229 offset:24576\n\t"
        "ds_read_b64_tr_b16 v[134:135], v229 offset:25600\n\t"
        "s_add_u32 s45, s45, 0x1000\n\t"
        "buffer_load_dwordx4 v[176:179], v225, s[40:43], s45 offen offset:0 sc1\n\t"
        "s_waitcnt vmcnt(11) lgkmcnt(14)\n\t"
        "v_mfma_f32_32x32x16_bf16 v[64:79], v[136:139], v[196:199], v[64:79]\n\t"
        "ds_read_b64_tr_b16 v[136:137], v228 offset:26624\n\t"
        "ds_read_b64_tr_b16 v[138:139], v228 offset:27648\n\t"
        "s_waitcnt lgkmcnt(14)\n\t"
        "v_mfma_f32_32x32x16_bf16 v[80:95], v[140:143], v[196:199], v[80:95]\n\t"
        "ds_read_b64_tr_b16 v[140:141], v229 offset:26624\n\t"
        "ds_read_b64_tr_b16 v[142:143], v229 offset:27648\n\t"
        "buffer_load_dwordx4 v[180:183], v225, s[40:43], s45 offen offset:1024 sc1\n\t"
        "s_waitcnt vmcnt(11) lgkmcnt(14)\n\t"
        "v_mfma_f32_32x32x16_bf16 v[64:79], v[144:147], v[200:203], v[64:79]\n\t"
        "ds_read_b64_tr_b16 v[144:145], v228 offset:28672\n\t"
        "ds_read_b64_tr_b16 v[146:147], v228 offset:29696\n\t"
        "s_waitcnt lgkmcnt(14)\n\t"
        "v_mfma_f32_32x32x16_bf16 v[80:95], v[148:151], v[200:203], v[80:95]\n\t"
        "ds_read_b64_tr_b16 v[148:149], v229 offset:28672\n\t"
        "ds_read_b64_tr_b16 v[150:151], v229 offset:29696\n\t"
        "buffer_load_dwordx4 v[184:187], v225, s[40:43], s45 offen offset:2048 sc1\n\t"
        "s_waitcnt vmcnt(11) lgkmcnt(14)\n\t"
        "v_mfma_f32_32x32x16_bf16 v[64:79], v[152:155], v[204:207], v[64:79]\n\t"
        "ds_read_b64_tr_b16 v[152:153], v228 offset:30720\n\t"
        "ds_read_b64_tr_b16 v[154:155], v228 offset:31744\n\t"
        "s_waitcnt lgkmcnt(14)\n\t"
        "v_mfma_f32_32x32x16_bf16 v[80:95], v[156:159], v[204:207], v[80:95]\n\t"
        "ds_read_b64_tr_b16 v[156:157], v229 offset:30720\n\t"
        "ds_read_b64_tr_b16 v[158:159], v229 offset:31744\n\t"
        "buffer_load_dwordx4 v[188:191], v225, s[40:43], s45 offen offset:3072 sc1\n\t"
        "s_waitcnt vmcnt(11) lgkmcnt(14)\n\t"
        "v_mfma_f32_32x32x16_bf16 v[64:79], v[128:131], v[208:211], v[64:79]\n\t"
        "ds_read_b64_tr_b16 v[128:129], v230 offset:0\n\t"
        "ds_read_b64_tr_b16 v[130:131], v230 offset:1024\n\t"
        "s_waitcnt lgkmcnt(14)\n\t"
        "v_mfma_f32_32x32x16_bf16 v[80:95], v[132:135], v[208:211], v[80:95]\n\t"
        "ds_read_b64_tr_b16 v[132:133], v231 offset:0\n\t"
        "ds_read_b64_tr_b16 v[134:135], v231 offset:1024\n\t"
        "s_add_u32 s45, s45, 0x1000\n\t"
        "buffer_load_dwordx4 v[192:195], v225, s[40:43], s45 offen offset:0 sc1\n\t"
        "s_waitcnt vmcnt(11) lgkmcnt(14)\n\t"
        "v_mfma_f32_32x32x16_bf16 v[64:79], v[136:139], v[212:215], v[64:79]\n\t"
        "ds_read_b64_tr_b16 v[136:137], v230 offset:2048\n\t"
        "ds_read_b64_tr_b16 v[138:139], v230 offset:3072\n\t"
        "s_waitcnt lgkmcnt(14)\n\t"
        "v_mfma_f32_32x32x16_bf16 v[80:95], v[140:143], v[212:215], v[80:95]\n\t"
        "ds_read_b64_tr_b16 v[140:141], v231 offset:2048\n\t"
        "ds_read_b64_tr_b16 v[142:143], v231 offset:3072\n\t"
        "buffer_load_dwordx4 v[196:199], v225, s[40:43], s45 offen offset:1024 sc1\n\t"
        "s_waitcnt vmcnt(11) lgkmcnt(14)\n\t"
        "v_mfma_f32_32x32x16_bf16 v[64:79], v[144:147], v[216:219], v[64:79]\n\t"
        "ds_read_b64_tr_b16 v[144:145], v230 offset:4096\n\t"
        "ds_read_b64_tr_b16 v[146:147], v230 offset:5120\n\t"
        "s_waitcnt lgkmcnt(14)\n\t"
        "v_mfma_f32_32x32x16_bf16 v[80:95], v[148:151], v[216:219], v[80:95]\n\t"
        "ds_read_b64_tr_b16 v[148:149], v231 offset:4096\n\t"
        "ds_read_b64_tr_b16 v[150:151], v231 offset:5120\n\t"
        "buffer_load_dwordx4 v[200:203], v225, s[40:43], s45 offen offset:2048 sc1\n\t"
        "s_waitcnt vmcnt(11) lgkmcnt(14)\n\t"
        "v_mfma_f32_32x32x16_bf16 v[64:79], v[152:155], v[220:223], v[64:79]\n\t"
        "ds_read_b64_tr_b16 v[152:153], v230 offset:6144\n\t"
        "ds_read_b64_tr_b16 v[154:155], v230 offset:7168\n\t"
        "s_waitcnt lgkmcnt(14)\n\t"
        "v_mfma_f32_32x32x16_bf16 v[80:95], v[156:159], v[220:223], v[80:95]\n\t"
        "ds_read_b64_tr_b16 v[156:157], v231 offset:6144\n\t"
        "ds_read_b64_tr_b16 v[158:159], v231 offset:7168\n\t"
        "buffer_load_dwordx4 v[204:207], v225, s[40:43], s45 offen offset:3072 sc1\n\t"
        "s_mov_b32 s44, s45\n\t"
        "s_cmp_eq_u32 s47, 0\n\t"
        "s_cbranch_scc1 oseg0_%=\n\t"
        "s_cmp_eq_u32 s47, 1\n\t"
        "s_cbranch_scc1 oseg1_%=\n\t"
        "s_cmp_eq_u32 s47, 2\n\t"
        "s_cbranch_scc1 oseg2_%=\n\t"
        "s_cmp_eq_u32 s47, 3\n\t"
        "s_cbranch_scc1 oseg3_%=\n\t"
        "s_cmp_lg_u32 s46, 15\n\t"
        "s_cbranch_scc1 ostart_%=\n\t"
        "s_branch oend_%=\n\t"
        "oseg3_%=:\n\t"
        "s_or_b32 s46, s46, 8\n\t"
        "s_waitcnt vmcnt(11) lgkmcnt(14)\n\t"
        "v_mfma_f32_32x32x16_bf16 v[96:111], v[128:131], v[160:163], 0\n\t"
        "ds_read_b64_tr_b16 v[128:129], v228 offset:40960\n\t"
        "ds_read_b64_tr_b16 v[130:131], v228 offset:41984\n\t"
        "s_waitcnt lgkmcnt(14)\n\t"
        "v_mfma_f32_32x32x16_bf16 v[112:127], v[132:135], v[160:163], 0\n\t"
        "ds_read_b64_tr_b16 v[132:133], v229 offset:40960\n\t"
        "ds_read_b64_tr_b16 v[134:135], v229 offset:41984\n\t"
        "s_add_u32 s44, s44, 0x1000\n\t"
        "buffer_load_dwordx4 v[208:211], v224, s[40:43], s44 offen offset:0 sc1\n\t"
        "s_waitcnt vmcnt(11) lgkmcnt(14)\n\t"
        "v_mfma_f32_32x32x16_bf16 v[96:111], v[136:139], v[164:167], v[96:111]\n\t"
        "ds_read_b32 v233, v232\n\t"
        "ds_read_b64_tr_b16 v[136:137], v228 offset:43008\n\t"
        "ds_read_b64_tr_b16 v[138:139], v228 offset:44032\n\t"
        "s_waitcnt lgkmcnt(14)\n\t"
        "v_mfma_f32_32x32x16_bf16 v[112:127], v[140:143], v[164:167], v[112:127]\n\t"
        "ds_read_b64_tr_b16 v[140:141], v229 offset:43008\n\t"
        "ds_read_b64_tr_b16 v[142:143], v229 offset:44032\n\t"
        "buffer_load_dwordx4 v[212:215], v224, s[40:43], s44 offen offset:1024 sc1\n\t"
        "s_waitcnt vmcnt(11) lgkmcnt(14)\n\t"
        "v_mfma_f32_32x32x16_bf16 v[96:111], v[144:147], v[168:171], v[96:111]\n\t"
        "ds_read_b64_tr_b16 v[144:145], v228 offset:45056\n\t"
        "ds_read_b64_tr_b16 v[146:147], v228 offset:46080\n\t"
        "s_waitcnt lgkmcnt(14)\n\t"
        "v_mfma_f32_32x32x16_bf16 v[112:127], v[148:151], v[168:171], v[112:127]\n\t"
        "ds_read_b64_tr_b16 v[148:149], v229 offset:45056\n\t"
        "ds_read_b64_tr_b16 v[150:151], v229 offset:46080\n\t"
        "buffer_load_dwordx4 v[216:219], v224, s[40:43], s44 offen offset:2048 sc1\n\t"
        "s_waitcnt vmcnt(11) lgkmcnt(8)\n\t"
        "v_mfma_f32_32x32x16_bf16 v[96:111], v[152:155], v[172:175], v[96:111]\n\t"
        "v_readfirstlane_b32 s51, v233\n\t"
        "ds_read_b64_tr_b16 v[152:153], v228 offset:47104\n\t"
        "ds_read_b64_tr_b16 v[154:155], v228 offset:48128\n\t"
        "s_waitcnt lgkmcnt(14)\n\t"
        "v_mfma_f32_32x32x16_bf16 v[112:127], v[156:159], v[172:175], v[112:127]\n\t"
        "s_andn2_b32 s51, s51, s46\n\t"
        "s_ff1_i32_b32 s47, s51\n\t"
        "s_add_u32 s45, s47, s49\n\t"
        "s_and_b32 s45, s45, 3\n\t"
        "s_lshl_b32 s45, s45, 14\n\t"
        "ds_read_b64_tr_b16 v[156:157], v229 offset:47104\n\t"
        "ds_read_b64_tr_b16 v[158:159], v229 offset:48128\n\t"
        "buffer_load_dwordx4 v[220:223], v224, s[40:43], s44 offen offset:3072 sc1\n\t"
        "s_waitcnt vmcnt(11) lgkmcnt(14)\n\t"
        "v_mfma_f32_32x32x16_bf16 v[96:111], v[128:131], v[176:179], v[96:111]\n\t"
        "s_and_b32 s51, s47, 0x80000000\n\t"
        "v_or_b32 v225, s51, v224\n\t"
        "s_and_b32 s51, s47, 3\n\t"
        "s_lshl_b32 s51, s51, 15\n\t"
        "v_add_u32 v230, s51, v226\n\t"
        "v_add_u32 v231, s51, v227\n\t"
        "ds_read_b64_tr_b16 v[128:129], v228 offset:49152\n\t"
        "ds_read_b64_tr_b16 v[130:131], v228 offset:50176\n\t"
        "s_waitcnt lgkmcnt(14)\n\t"
        "v_mfma_f32_32x32x16_bf16 v[112:127], v[132:135], v[176:179], v[112:127]\n\t"
        "ds_read_b64_tr_b16 v[132:133], v229 offset:49152\n\t"
        "ds_read_b64_tr_b16 v[134:135], v229 offset:50176\n\t"
        "buffer_load_dwordx4 v[160:163], v225, s[40:43], s45 offen offset:0 sc1\n\t"
        "s_waitcnt vmcnt(11) lgkmcnt(14)\n\t"
        "v_mfma_f32_32x32x16_bf16 v[96:111], v[136:139], v[180:183], v[96:111]\n\t"
        "ds_read_b64_tr_b16 v[136:137], v228 offset:51200\n\t"
        "ds_read_b64_tr_b16 v[138:139], v228 offset:52224\n\t"
        "s_waitcnt lgkmcnt(14)\n\t"
        "v_mfma_f32_32x32x16_bf16 v[112:127], v[140:143], v[180:183], v[112:127]\n\t"
        "ds_read_b64_tr_b16 v[140:141], v229 offset:51200\n\t"
        "ds_read_b64_tr_b16 v[142:143], v229 offset:52224\n\t"
        "buffer_load_dwordx4 v[164:167], v225, s[40:43], s45 offen offset:1024 sc1\n\t"
        "s_waitcnt vmcnt(11) lgkmcnt(14)\n\t"
        "v_mfma_f32_32x32x16_bf16 v[96:111], v[144:147], v[184:187], v[96:111]\n\t"
        "ds_read_b64_tr_b16 v[144:145], v228 offset:53248\n\t"
        "ds_read_b64_tr_b16 v[146:147], v228 offset:54272\n\t"
        "s_waitcnt lgkmcnt(14)\n\t"
        "v_mfma_f32_32x32x16_bf16 v[112:127], v[148:151], v[184:187], v[112:127]\n\t"
        "ds_read_b64_tr_b16 v[148:149], v229 offset:53248\n\t"
        "ds_read_b64_tr_b16 v[150:151], v229 offset:54272\n\t"
        "buffer_load_dwordx4 v[168:171], v225, s[40:43], s45 offen offset:2048 sc1\n\t"
        "s_waitcnt vmcnt(11) lgkmcnt(14)\n\t"
        "v_mfma_f32_32x32x16_bf16 v[96:111], v[152:155], v[188:191], v[96:111]\n\t"
        "ds_read_b64_tr_b16 v[152:153], v228 offset:55296\n\t"
        "ds_read_b64_tr_b16 v[154:155], v228 offset:56320\n\t"
        "s_waitcnt lgkmcnt(14)\n\t"
        "v_mfma_f32_32x32x16_bf16 v[112:127], v[156:159], v[188:191], v[112:127]\n\t"
        "ds_read_b64_tr_b16 v[156:157], v229 offset:55296\n\t"
        "ds_read_b64_tr_b16 v[158:159], v229 offset:56320\n\t"
        "buffer_load_dwordx4 v[172:175], v225, s[40:43], s45 offen offset:3072 sc1\n\t"
        "s_waitcnt vmcnt(11) lgkmcnt(14)\n\t"
        "v_mfma_f32_32x32x16_bf16 v[96:111], v[128:131], v[192:195], v[96:111]\n\t"
        "ds_read_b64_tr_b16 v[128:129], v228 offset:57344\n\t"
        "ds_read_b64_tr_b16 v[130:131], v228 offset:58368\n\t"
        "s_waitcnt lgkmcnt(14)\n\t"
        "v_mfma_f32_32x32x16_bf16 v[112:127], v[132:135], v[192:195], v[112:127]\n\t"
        "ds_read_b64_tr_b16 v[132:133], v229 offset:57344\n\t"
        "ds_read_b64_tr_b16 v[134:135], v229 offset:58368\n\t"
        "s_add_u32 s45, s45, 0x1000\n\t"
        "buffer_load_dwordx4 v[176:179], v225, s[40:43], s45 offen offset:0 sc1\n\t"
        "s_waitcnt vmcnt(11) lgkmcnt(14)\n\t"
        "v_mfma_f32_32x32x16_bf16 v[96:111], v[136:139], v[196:199], v[96:111]\n\t"
        "ds_read_b64_tr_b16 v[136:137], v228 offset:59392\n\t"
        "ds_read_b64_tr_b16 v[138:139], v228 offset:60416\n\t"
        "s_waitcnt lgkmcnt(14)\n\t"
        "v_mfma_f32_32x32x16_bf16 v[112:127], v[140:143], v[196:199], v[112:127]\n\t"
        "ds_read_b64_tr_b16 v[140:141], v229 offset:59392\n\t"
        "ds_read_b64_tr_b16 v[142:143], v229 offset:60416\n\t"
        "buffer_load_dwordx4 v[180:183], v225, s[40:43], s45 offen offset:1024 sc1\n\t"
        "s_waitcnt vmcnt(11) lgkmcnt(14)\n\t"
        "v_mfma_f32_32x32x16_bf16 v[96:111], v[144:147], v[200:203], v[96:111]\n\t"
        "ds_read_b64_tr_b16 v[144:145], v228 offset:61440\n\t"
        "ds_read_b64_tr_b16 v[146:147], v228 offset:62464\n\t"
        "s_waitcnt lgkmcnt(14)\n\t"
        "v_mfma_f32_32x32x16_bf16 v[112:127], v[148:151], v[200:203], v[112:127]\n\t"
        "ds_read_b64_tr_b16 v[148:149], v229 offset:61440\n\t"
        "ds_read_b64_tr_b16 v[150:151], v229 offset:62464\n\t"
        "buffer_load_dwordx4 v[184:187], v225, s[40:43], s45 offen offset:2048 sc1\n\t"
        "s_waitcnt vmcnt(11) lgkmcnt(14)\n\t"
        "v_mfma_f32_32x32x16_bf16 v[96:111], v[152:155], v[204:207], v[96:111]\n\t"
        "ds_read_b64_tr_b16 v[152:153], v228 offset:63488\n\t"
        "ds_read_b64_tr_b16 v[154:155], v228 offset:64512\n\t"
        "s_waitcnt lgkmcnt(14)\n\t"
        "v_mfma_f32_32x32x16_bf16 v[112:127], v[156:159], v[204:207], v[112:127]\n\t"
        "ds_read_b64_tr_b16 v[156:157], v229 offset:63488\n\t"
        "ds_read_b64_tr_b16 v[158:159], v229 offset:64512\n\t"
        "buffer_load_dwordx4 v[188:191], v225, s[40:43], s45 offen offset:3072 sc1\n\t"
        "s_waitcnt vmcnt(11) lgkmcnt(14)\n\t"
        "v_mfma_f32_32x32x16_bf16 v[96:111], v[128:131], v[208:211], v[96:111]\n\t"
        "ds_read_b64_tr_b16 v[128:129], v230 offset:0\n\t"
        "ds_read_b64_tr_b16 v[130:131], v230 offset:1024\n\t"
        "s_waitcnt lgkmcnt(14)\n\t"
        "v_mfma_f32_32x32x16_bf16 v[112:127], v[132:135], v[208:211], v[112:127]\n\t"
        "ds_read_b64_tr_b16 v[132:133], v231 offset:0\n\t"
        "ds_read_b64_tr_b16 v[134:135], v231 offset:1024\n\t"
        "s_add_u32 s45, s45, 0x1000\n\t"
        "buffer_load_dwordx4 v[192:195], v225, s[40:43], s45 offen offset:0 sc1\n\t"
        "s_waitcnt vmcnt(11) lgkmcnt(14)\n\t"
        "v_mfma_f32_32x32x16_bf16 v[96:111], v[136:139], v[212:215], v[96:111]\n\t"
        "ds_read_b64_tr_b16 v[136:137], v230 offset:2048\n\t"
        "ds_read_b64_tr_b16 v[138:139], v230 offset:3072\n\t"
        "s_waitcnt lgkmcnt(14)\n\t"
        "v_mfma_f32_32x32x16_bf16 v[112:127], v[140:143], v[212:215], v[112:127]\n\t"
        "ds_read_b64_tr_b16 v[140:141], v231 offset:2048\n\t"
        "ds_read_b64_tr_b16 v[142:143], v231 offset:3072\n\t"
        "buffer_load_dwordx4 v[196:199], v225, s[40:43], s45 offen offset:1024 sc1\n\t"
        "s_waitcnt vmcnt(11) lgkmcnt(14)\n\t"
        "v_mfma_f32_32x32x16_bf16 v[96:111], v[144:147], v[216:219], v[96:111]\n\t"
        "ds_read_b64_tr_b16 v[144:145], v230 offset:4096\n\t"
        "ds_read_b64_tr_b16 v[146:147], v230 offset:5120\n\t"
        "s_waitcnt lgkmcnt(14)\n\t"
        "v_mfma_f32_32x32x16_bf16 v[112:127], v[148:151], v[216:219], v[112:127]\n\t"
        "ds_read_b64_tr_b16 v[148:149], v231 offset:4096\n\t"
        "ds_read_b64_tr_b16 v[150:151], v231 offset:5120\n\t"
        "buffer_load_dwordx4 v[200:203], v225, s[40:43], s45 offen offset:2048 sc1\n\t"
        "s_waitcnt vmcnt(11) lgkmcnt(14)\n\t"
        "v_mfma_f32_32x32x16_bf16 v[96:111], v[152:155], v[220:223], v[96:111]\n\t"
        "ds_read_b64_tr_b16 v[152:153], v230 offset:6144\n\t"
        "ds_read_b64_tr_b16 v[154:155], v230 offset:7168\n\t"
        "s_waitcnt lgkmcnt(14)\n\t"
        "v_mfma_f32_32x32x16_bf16 v[112:127], v[156:159], v[220:223], v[112:127]\n\t"
        "ds_read_b64_tr_b16 v[156:157], v231 offset:6144\n\t"
        "ds_read_b64_tr_b16 v[158:159], v231 offset:7168\n\t"
        "buffer_load_dwordx4 v[204:207], v225, s[40:43], s45 offen offset:3072 sc1\n\t"
        "s_mov_b32 s44, s45\n\t"
        "s_cmp_eq_u32 s47, 0\n\t"
        "s_cbranch_scc1 oseg0_%=\n\t"
        "s_cmp_eq_u32 s47, 1\n\t"
        "s_cbranch_scc1 oseg1_%=\n\t"
        "s_cmp_eq_u32 s47, 2\n\t"
        "s_cbranch_scc1 oseg2_%=\n\t"
        "s_cmp_eq_u32 s47, 3\n\t"
        "s_cbranch_scc1 oseg3_%=\n\t"
        "s_cmp_lg_u32 s46, 15\n\t"
        "s_cbranch_scc1 ostart_%=\n\t"
        "s_branch oend_%=\n\t"
        "oend_%=:\n\t"
        "ds_read_b64 v[128:129], v234 offset:0\n\t"
        "ds_read_b64 v[130:131], v234 offset:16\n\t"
        "ds_read_b64 v[132:133], v234 offset:32\n\t"
        "ds_read_b64 v[134:135], v234 offset:48\n\t"
        "ds_read_b64 v[136:137], v234 offset:64\n\t"
        "ds_read_b64 v[138:139], v234 offset:80\n\t"
        "ds_read_b64 v[140:141], v234 offset:96\n\t"
        "ds_read_b64 v[142:143], v234 offset:112\n\t"
        "s_waitcnt vmcnt(0) lgkmcnt(0)\n\t"
        "s_barrier\n\t"
        : "+{v[0:15]}"(acc[0]), "+{v[16:31]}"(acc[1]), "={v[32:47]}"(acc[2]), "={v[48:63]}"(acc[3]), "={v[64:79]}"(acc[4]), "={v[80:95]}"(acc[5]),
          "={v[96:111]}"(acc[6]), "={v[112:127]}"(acc[7]), "={v[128:143]}"(wrc) STAMP_OUTS
        : "{v224}"(OFF_OF + L * (5 * MBY) + tile * TS + lane * 16), "{v226}"(a0), "{v227}"(a1), "{v228}"(a0 + 65536), "{v229}"(a1 + 65536),
          "{v232}"(lbase + L_W + 20 + 8 * L), "{v234}"(lbase + L_WR + hi * 8), "{s49}"((tn >> 2) + 1), "{s[40:43]}"(srd)
        : "v144", "v145", "v146", "v147", "v148", "v149", "v150", "v151", "v152", "v153", "v154", "v155", "v156", "v157", "v158", "v159", "v160", "v161", "v162", "v163", "v164", "v165", "v166", "v167", "v168", "v169", "v170", "v171", "v172", "v173", "v174", "v175", "v176", "v177", "v178", "v179", "v180", "v181", "v182", "v183", "v184", "v185", "v186", "v187", "v188", "v189", "v190", "v191", "v192", "v193", "v194", "v195", "v196", "v197", "v198", "v199", "v200", "v201", "v202", "v203", "v204", "v205", "v206", "v207", "v208", "v209", "v210", "v211", "v212", "v213", "v214", "v215", "v216", "v217", "v218", "v219", "v220", "v221", "v222", "v223", "v225", "v230", "v231", "v233", "s44", "s45", "s46", "s47", "s48", "s51", "vcc", "scc", "memory");
      }
      PHASE_VARS;
      STAMP_PUT(12 + 16 * L);
      STAMPV(15 + 16 * L);
      float s = 0.f, sq = 0.f, d = 0.f;
      const unsigned xdst = OFF_X1 + tile * TS + tn * 4096 + lane * 16;
#pragma unroll
      for (int i = 0; i < 4; ++i) {
#pragma unroll
        for (int e = 0; e < 4; ++e) {
          const int j = i >> 1, k = 8 * (i & 1) + 2 * e;
          x1p[i][e] = pack2((acc[j][k] + acc[2 + j][k]) + (acc[4 + j][k] + acc[6 + j][k]), (acc[j][k + 1] + acc[2 + j][k + 1]) + (acc[4 + j][k + 1] + acc[6 + j][k + 1]));
        }
        if (L == 0) stg(rs, xdst + i * 1024, x1p[i]);
        SBAR();
      }
#pragma unroll
      for (int g = 0; g < 8; ++g)
#pragma unroll
        for (int e = 0; e < 2; ++e) {
          const unsigned o = x1p[g >> 1][2 * (g & 1) + e];
          s = dot2(o, 0x3f803f80u, s); sq = dot2(o, o, sq); d = dot2(o, wrc[2 * g + e], d);
        }
      const u32x4 pst = {nzf(half_swap_sum(s)), nzf(half_swap_sum(sq)), nzf(half_swap_sum(d)), 0u};
      asm volatile("buffer_store_dwordx4 %0, %1, %2, 0 offen sc1\n\ts_waitcnt vmcnt(1)\n\tds_write_b32 %3, %4"
                   :: "v"(pst), "v"(stats_dst(sx, tile, tn, lane)), "s"(srd), "v"(lbase + L_W + 64 + 4 * wid), "v"(3) : "memory");
    }
    STAMPV(16 + 16 * L);
  }
  {
    PHASE_VARS;
    float S, Q, Dd, mean, var, mb1, rd1;
    __builtin_amdgcn_s_sleep(13);
    row_stats_sum(rs, OFF_SX + SX_LAYER, tile, lane, S, Q, Dd);
    STAMPV(33);
    row_stats_fin(rs, OFF_SX + SX_LAYER + SX_CAND + (tn * 16 + tm) * 128 + wid * 4, smem, 2, tile, lane, S, Q, Dd, U2F(words[0]), x1p, mean, var, mb1, rd1);
    STAMPV(34);
    u32x4 xo[4];
#pragma unroll
    for (int i = 0; i < 4; ++i) {
#pragma unroll
      for (int e = 0; e < 4; ++e) {
        const unsigned h2 = ln_pair(x1p[i][e], mb1, rd1);
        xo[i][e] = pack2(lo16(x1p[i][e]) + lo16(h2), hi16(x1p[i][e]) + hi16(h2));
      }
      *reinterpret_cast<u32x4*>(smem + wid * 4096 + r32 * 128 + ((2 * i + hi) ^ (r32 & 7)) * 16) = fo_swap(xo[i]);
    }
    asm volatile("s_waitcnt lgkmcnt(0)" ::: "memory");
#pragma unroll
    for (int i = 0; i < 4; ++i) { xo[i] = *reinterpret_cast<const u32x4*>(smem + wid * 4096 + i * 1024 + (lane >> 3) * 128 + ((lane ^ (lane >> 3)) & 7) * 16); PIN(xo[i]); }
    u16* dst = out + (size_t)(tile * 32 + (lane >> 3)) * D_ + tn * 64 + (lane & 7) * 8;
    lds_wait_ge(lbase + L_W + 4 + 4 * (lane & 1), 2);
    const int rel = (int)words[3] - tile * 32 - (lane >> 3);
#pragma unroll
    for (int i = 0; i < 4; ++i)
      if (rel != 8 * i) st16(dst + (size_t)i * 8 * D_, xo[i]);
    STAMPV(35);
    STAMP_END;
  }
}

extern "C" __global__ void end_marker() {}

extern "C" void kernel_launch(void* const* d_in, const int* in_sizes, int n_in, void* d_out, int out_size,
                              void* d_ws, size_t ws_size, hipStream_t stream) {
  char* ws = (char*)d_ws;
  mega_kernel<<<256, 512, 0, stream>>>((const u16*)d_in[0], (const u16*)d_in[1], (const u16*)d_in[2], (const u16*)d_in[4], (const u16*)d_in[5],
                                       (const u16*)d_in[6], (const u16*)d_in[7], ws, (u16*)d_out);
}
```

```cpp
#include <hip/hip_runtime.h>
#include <hip/hip_bf16.h>
#include <stdint.h>

typedef unsigned short u16;
typedef short s16x4 __attribute__((ext_vector_type(4)));
typedef short s16x8 __attribute__((ext_vector_type(8)));
typedef float f32x16 __attribute__((ext_vector_type(16)));
typedef float f32x8 __attribute__((ext_vector_type(8)));
typedef float f32x4 __attribute__((ext_vector_type(4)));
typedef float f32x2 __attribute__((ext_vector_type(2)));
typedef unsigned int u32x16 __attribute__((ext_vector_type(16)));
typedef unsigned int u32x8 __attribute__((ext_vector_type(8)));
typedef unsigned int u32x4 __attribute__((ext_vector_type(4)));
typedef unsigned int u32x2 __attribute__((ext_vector_type(2)));
typedef __bf16 bf16x2 __attribute__((ext_vector_type(2)));
typedef __bf16 bf16x8 __attribute__((ext_vector_type(8)));
typedef __bf16 bf16x16 __attribute__((ext_vector_type(16)));

#define DI __device__ __forceinline__
#define LDS_PTR(T, p) ((__attribute__((address_space(3))) T*)(p))
#define BARRIER() asm volatile("s_barrier" ::: "memory")
#define BAR_LGKM() asm volatile("s_waitcnt lgkmcnt(0)\n\ts_barrier" ::: "memory")
#define BAR_ALL() asm volatile("s_waitcnt vmcnt(0) lgkmcnt(0)\n\ts_barrier" ::: "memory")
#define DRAIN() asm volatile("s_waitcnt vmcnt(0)" ::: "memory")
#define MFMA32(a, b, c) __builtin_amdgcn_mfma_f32_32x32x16_bf16(a, b, c, 0, 0, 0)
#define MFMA16(a, b, c) __builtin_amdgcn_mfma_f32_16x16x32_bf16(a, b, c, 0, 0, 0)
#define PIN(x) asm volatile("" : "+v"(x))
#define EX2(x) __builtin_amdgcn_exp2f(x)
#define RCP(x) __builtin_amdgcn_rcpf(x)
#define SBAR() __builtin_amdgcn_sched_barrier(0)

#define STAMP_INIT
#define STAMPV(i)
#define STAMP_END
#define STAMP_DECL
#define STAMP_OUTS
#define STAMP_PUT(i)
#define JIT()
constexpr int D_ = 1024, F_ = 3584;
constexpr float QS = 0.17677669529663687f * 1.4426950408889634f;

constexpr unsigned MBY = 1u << 20;
constexpr unsigned TS = 65536 + 4096, HS = 262144 + 4096;
constexpr unsigned OFF_HF = 0, OFF_X1 = 5 * MBY;
constexpr unsigned OFF_OF = 10 * MBY;
constexpr unsigned OFF_KV = 20 * MBY;
constexpr unsigned OFF_FL = 40 * MBY;
constexpr unsigned FL_EPOCH = OFF_FL;
constexpr unsigned FL_KV = OFF_FL + 4096;
constexpr unsigned FL_O = FL_KV + 65536;
constexpr unsigned FL_X = FL_O + 65536;
constexpr unsigned OFF_CS = 41 * MBY;
constexpr unsigned OFF_SX = 42 * MBY;
constexpr unsigned SX_ST = 0;
constexpr unsigned SX_ROW = 512 * 1024;
constexpr unsigned SX_CAND = SX_ROW + 256 * 1024;
constexpr unsigned SX_GP = SX_CAND + 32 * 1024;
constexpr unsigned SX_YP = SX_GP + 224 * 1024;
constexpr unsigned SX_LAYER = SX_YP + 224 * 1024;
constexpr unsigned SX_QP = 2 * SX_LAYER;
constexpr unsigned SX_BYTES = SX_QP + 512 * 1024;
static_assert(SX_BYTES <= 6 * 256 * 128 * 16, "sentinel fill");
constexpr unsigned WS_BYTES = 48 * MBY, OOB = 0x80000000u;
constexpr unsigned SENT = 0xFFFFFFFFu;

constexpr int NS = 9, DD = 3, SLOTB = 12352, HALFB = 6176;
constexpr int L_XCH = 122880;
constexpr int L_SL = L_XCH + 8192;
constexpr int L_CAND = L_XCH + 32768;
constexpr int L_AB = L_XCH + 34816;
constexpr int L_FULL = 159744, L_FREE = L_FULL + 512;
constexpr int L_CS = L_FREE + 1024, L_PATCH = L_CS + 768;
constexpr int L_GU = L_PATCH + 768, L_TAG = L_GU + 256, L_WR = L_TAG + 256;
constexpr int L_X2F = L_WR + 128, L_X2P = L_X2F + 128;
constexpr int L_W = L_X2P + 128;
constexpr int LDS_BYTES = 163840;
static_assert(L_W + 128 <= LDS_BYTES && NS * SLOTB + 256 <= L_XCH && L_AB + 2048 <= L_FULL, "LDS map");

DI int fresh_lane() { int l; asm volatile("v_mbcnt_lo_u32_b32 %0, -1, 0\n\tv_mbcnt_hi_u32_b32 %0, -1, %0" : "=v"(l)); return l; }
#define LANE_VARS const int lane = fresh_lane(), r32 = lane & 31, hi = lane >> 5, tq = (lane & 15) >> 2, tp = lane & 3, tblk = (lane >> 4) & 1, \
  wrow = 4 * hi + tq, wsw = 4 * ((wrow >> 1) & 1), wch = 2 * tblk + (tp >> 1), c24 = r32 < 24 ? r32 : 23; \
  (void)r32; (void)hi; (void)wsw; (void)wch; (void)c24
DI int fresh_s(int v) { asm volatile("" : "+s"(v)); return v; }
#define PHASE_VARS const int tm = fresh_s(tm_), tn = fresh_s(tn_), wid = fresh_s(wid_), tile = tm * 4 + wid, hh = wid & 1, kh = wid >> 1; (void)tile; (void)hh; (void)kh; LANE_VARS
DI int kpos(int tm, int tn, int i) { return i == 0 ? tn : (tn + 1 + (1 * (3 - (tn & 3)) + i - 1) % 15) & 15; }
DI float bf2f(u16 v) { return __uint_as_float(((unsigned)v) << 16); }
DI unsigned pack2(float lo, float hi) { f32x2 v = {lo, hi}; return __builtin_bit_cast(unsigned, __builtin_convertvector(v, bf16x2)); }
DI float rbf(float f) { return __uint_as_float(pack2(f, 0.f) << 16); }
DI float lo16(unsigned u) { return __uint_as_float(u << 16); }
DI float hi16(unsigned u) { return __uint_as_float(u & 0xffff0000u); }
DI float opq(float r) { asm("" : "+v"(r)); return r; }
DI float fadd(float a, float b) { return opq(a + b); }
DI float fmul(float a, float b) { return opq(a * b); }
DI float ffma(float a, float b, float c) { return opq(__builtin_fmaf(a, b, c)); }
DI float U2F(unsigned u) { return __uint_as_float(u); }
DI float half_swap_sum(float v) {
  auto r = __builtin_amdgcn_permlane32_swap(__float_as_uint(v), __float_as_uint(v), false, false);
  return fadd(U2F(r[0]), U2F(r[1]));
}
DI float half_swap_max(float v) {
  auto r = __builtin_amdgcn_permlane32_swap(__float_as_uint(v), __float_as_uint(v), false, false);
  return fmaxf(__uint_as_float(r[0]), __uint_as_float(r[1]));
}
template <int CTRL> DI unsigned dpp(unsigned v) { return (unsigned)__builtin_amdgcn_update_dpp(0, (int)v, CTRL, 0xf, 0xf, false); }
DI float row_swap_sum(float v) {
  auto r = __builtin_amdgcn_permlane16_swap(__float_as_uint(v), __float_as_uint(v), false, false);
  return fadd(U2F(r[0]), U2F(r[1]));
}
DI float half_sum(float v) {
  v = fadd(v, U2F(dpp<0xB1>(__float_as_uint(v)))); v = fadd(v, U2F(dpp<0x4E>(__float_as_uint(v))));
  v = fadd(v, U2F(dpp<0x141>(__float_as_uint(v)))); v = fadd(v, U2F(dpp<0x140>(__float_as_uint(v))));
  return row_swap_sum(v);
}
DI float wave_sum(float v) { return half_swap_sum(half_sum(v)); }
DI unsigned half_max_u32(unsigned v) {
  v = max(v, dpp<0xB1>(v)); v = max(v, dpp<0x4E>(v)); v = max(v, dpp<0x141>(v)); v = max(v, dpp<0x140>(v));
  auto r = __builtin_amdgcn_permlane16_swap(v, v, false, false);
  return max(r[0], r[1]);
}
DI unsigned wave_max_u32(unsigned v) {
  v = half_max_u32(v);
  auto r = __builtin_amdgcn_permlane32_swap(v, v, false, false);
  return max(r[0], r[1]);
}
DI u32x4 fo_swap(u32x4 v) {
  auto a = __builtin_amdgcn_permlane32_swap(v[0], v[2], false, false);
  auto b = __builtin_amdgcn_permlane32_swap(v[1], v[3], false, false);
  u32x4 o; o[0] = a[0]; o[1] = b[0]; o[2] = a[1]; o[3] = b[1];
  return o;
}
DI void row_swap(u32x4& a, u32x4& b) {
#pragma unroll
  for (int e = 0; e < 4; ++e) { auto r = __builtin_amdgcn_permlane16_swap(a[e], b[e], false, false); a[e] = r[0]; b[e] = r[1]; }
}
DI void glds4(const void* sb, unsigned vo, unsigned lds_dst) {
  unsigned keep;
  asm volatile("s_mov_b32 %0, m0\n\ts_mov_b32 m0, %3\n\ts_nop 0\n\tglobal_load_lds_dword %1, %2\n\ts_mov_b32 m0, %0"
               : "=&s"(keep) : "v"(vo), "s"(sb), "s"(lds_dst) : "memory");
}
#define GL_ "global_load_lds_dwordx4 "
DI void glds_v4(const void* sb, const unsigned (&v)[4], unsigned m0v) {
  unsigned keep;
  asm volatile("s_mov_b32 %0, m0\n\ts_mov_b32 m0, %6\n\ts_nop 0\n\t" GL_ "%1, %5\n\t" GL_ "%2, %5 offset:1024\n\t" GL_ "%3, %5 offset:2048\n\t"
               GL_ "%4, %5 offset:3072\n\ts_mov_b32 m0, %0" : "=&s"(keep) : "v"(v[0]), "v"(v[1]), "v"(v[2]), "v"(v[3]), "s"(sb), "s"(m0v) : "memory");
}
#define GP_ "\n\ts_sleep %8\n\ts_waitcnt vmcnt(%7)\n\t"
template <int TH, int SL> DI void glds_v4t(const void* sb, const unsigned (&v)[4], unsigned m0v) {
  unsigned keep;
  asm volatile("s_mov_b32 %0, m0\n\ts_mov_b32 m0, %6\n\ts_nop 0\n\t" GL_ "%1, %5" GP_ GL_ "%2, %5 offset:1024" GP_ GL_ "%3, %5 offset:2048" GP_ GL_ "%4, %5 offset:3072" GP_ "s_mov_b32 m0, %0"
               : "=&s"(keep) : "v"(v[0]), "v"(v[1]), "v"(v[2]), "v"(v[3]), "s"(sb), "s"(m0v), "n"(TH), "n"(SL) : "memory");
}
DI void glds_v3(const void* sb, const unsigned (&v)[3], unsigned m0v) {
  unsigned keep;
  asm volatile("s_mov_b32 %0, m0\n\ts_mov_b32 m0, %5\n\ts_nop 0\n\t" GL_ "%1, %4\n\t" GL_ "%2, %4 offset:1024\n\t" GL_ "%3, %4 offset:2048\n\t"
               "s_mov_b32 m0, %0" : "=&s"(keep) : "v"(v[0]), "v"(v[1]), "v"(v[2]), "s"(sb), "s"(m0v) : "memory");
}
DI float dot2(unsigned a, unsigned b, float c) {
  return __builtin_amdgcn_fdot2_f32_bf16(__builtin_bit_cast(bf16x2, a), __builtin_bit_cast(bf16x2, b), c, false);
}
#define CODE_ADDR(sym, out) do { unsigned long long pc_; unsigned off_; \
  asm volatile("s_getpc_b64 %0\n1:\n\ts_mov_b32 %1, " #sym "-1b" : "=s"(pc_), "=s"(off_)); out = (const char*)(pc_ + (long long)(int)off_); } while (0)
DI void st16(void* p, u32x4 v) { asm volatile("global_store_dwordx4 %0, %1, off sc0 sc1\n\ts_nop 1" :: "v"(p), "v"(v) : "memory"); }
DI void ln_scalars(float mean, float var, float& mb, float& den) { mb = rbf(mean); den = rbf(__builtin_amdgcn_sqrtf(rbf(rbf(var) + 1e-5f))); }
DI unsigned ln_pair(unsigned v, float mb, float rd) {
  const unsigned c = pack2(lo16(v) - mb, hi16(v) - mb);
  return pack2(lo16(c) * rd, hi16(c) * rd);
}
DI unsigned ln_pair_s(unsigned v, float nmb, float rd) {
  const unsigned c = pack2(fadd(lo16(v), nmb), fadd(hi16(v), nmb));
  return pack2(fmul(lo16(c), rd), fmul(hi16(c), rd));
}
template <int N, int M> DI f32x4 sum4(const u32x4 (&v)[M]) {
  f32x4 t;
#pragma unroll
  for (int e = 0; e < 4; ++e) {
    t[e] = U2F(v[0][e]);
#pragma unroll
    for (int i = 1; i < N; ++i) t[e] = fadd(t[e], U2F(v[i][e]));
  }
  return t;
}
#define PERM_LO(x, y) __builtin_amdgcn_perm(y, x, 0x05040100u)
#define PERM_HI(x, y) __builtin_amdgcn_perm(y, x, 0x07060302u)
constexpr unsigned SPIN_CAP = 1u << 15;
DI unsigned nz(unsigned v) { return min(v, SENT - 1u); }
DI unsigned nzf(float v) { return nz(__float_as_uint(v)); }
DI unsigned mx4(unsigned m, const u32x4& v) { return max(max(max(m, v[0]), v[1]), max(v[2], v[3])); }
template <int N> DI bool none_sent(const u32x4 (&v)[N]) {
  unsigned m = 0u;
#pragma unroll
  for (int i = 0; i < N; ++i) m = mx4(m, v[i]);
  return m != SENT;
}
typedef __amdgpu_buffer_rsrc_t rsrc_t;
DI u32x4 ldg(rsrc_t rs, unsigned off) { return __builtin_amdgcn_raw_buffer_load_b128(rs, (int)off, 0, 16); }
DI void stg(rsrc_t rs, unsigned off, u32x4 v) { __builtin_amdgcn_raw_buffer_store_b128(v, rs, (int)off, 0, 16); }
DI void poll_pause(bool slow) { if (slow) __builtin_amdgcn_s_sleep(4); asm volatile("s_sleep 1" ::: "memory"); }
template <int N, class LD, class OKF> DI void poll_all(u32x4 (&out)[N], bool slow, LD ld, OKF okf) {
  for (unsigned spins = 0;; ++spins) {
    ld(out);
    SBAR();
    if (__all(okf(out)) || spins > SPIN_CAP) break;
    poll_pause(slow);
  }
}
DI void lds_set(unsigned addr, unsigned v) { asm volatile("s_waitcnt lgkmcnt(0)\n\tds_write_b32 %0, %1" :: "v"(addr), "v"(v) : "memory"); }
template <int SLEEP = 1> DI void lds_wait_ge(unsigned addr, unsigned need) {
  unsigned spin = 0x100000, v;
  asm volatile("1:\n\tds_read_b32 %0, %2\n\ts_sub_u32 %1, %1, 1\n\ts_waitcnt lgkmcnt(0)\n\ts_cbranch_scc1 2f\n\tv_cmp_gt_u32 vcc, %3, %0\n\ts_cbranch_vccz 2f\n\ts_sleep %4\n\ts_branch 1b\n2:"
               : "=&v"(v), "+s"(spin) : "v"(addr), "s"(need), "n"(SLEEP) : "vcc", "scc", "memory");
}
DI s16x8 tr2(const char* p, int second) {
  s16x4 lo = __builtin_amdgcn_ds_read_tr16_b64_v4i16(LDS_PTR(s16x4, p)), hi = __builtin_amdgcn_ds_read_tr16_b64_v4i16(LDS_PTR(s16x4, p + second));
  return __builtin_shufflevector(lo, hi, 0, 1, 2, 3, 4, 5, 6, 7);
}
DI u32x16 cat4(const u32x4& a, const u32x4& b, const u32x4& c, const u32x4& d) {
  const u32x8 x = __builtin_shufflevector(a, b, 0, 1, 2, 3, 4, 5, 6, 7), y = __builtin_shufflevector(c, d, 0, 1, 2, 3, 4, 5, 6, 7);
  return __builtin_shufflevector(x, y, 0, 1, 2, 3, 4, 5, 6, 7, 8, 9, 10, 11, 12, 13, 14, 15);
}

DI unsigned stats_dst(unsigned sx, int tile, int tn, int lane) { return lane < 32 ? sx + SX_ST + tile * 8192 + tn * 512 + lane * 16 : OOB; }
DI void row_stats_sum(rsrc_t rs, unsigned sx, int tile, int lane, float& S, float& Q, float& Dd) {
  const unsigned exb = sx + SX_ST + tile * 8192 + (lane & 31) * 16;
  u32x4 pv[8];
  poll_all<8>(pv, false, [&](u32x4 (&v)[8]) {
#pragma unroll
    for (int i = 0; i < 8; ++i) v[i] = ldg(rs, exb + ((lane >> 5) * 8 + i) * 512);
  }, [&](const u32x4 (&v)[8]) { return none_sent(v); });
  S = 0.f; Q = 0.f; Dd = 0.f;
#pragma unroll
  for (int i = 0; i < 8; ++i) { S += __uint_as_float(pv[i][0]); Q += __uint_as_float(pv[i][1]); Dd += __uint_as_float(pv[i][2]); }
}
DI void row_stats_fin(rsrc_t rs, unsigned kdst, char* smem, int epoch, int tile, int lane, float S, float Q, float Dd, float wsum, const u32x4 (&x1p)[4],
                      float& mean, float& var, float& mb, float& rd) {
  S = half_swap_sum(S); Q = half_swap_sum(Q); Dd = half_swap_sum(Dd);
  mean = S * (1.0f / 1024.0f); var = fmaxf(Q * (1.0f / 1024.0f) - mean * mean, 0.f);
  float den;
  ln_scalars(mean, var, mb, den);
  rd = RCP(den);
  const unsigned rb = pack2((Dd - mb * wsum) * rd, 0.f) & 0xffffu;
  const unsigned mine = (((rb & 0x8000u) ? (~rb & 0xffffu) : (rb | 0x8000u)) << 16) | (unsigned)(2047 - (tile * 32 + (lane & 31)));
  const bool best = half_max_u32(mine) == mine;
  __builtin_amdgcn_raw_buffer_store_b32(mine, rs, (best && lane < 32) ? kdst : OOB, 0, 16);
  if (best) {
    char* c = smem + L_CAND + (tile & 3) * 144;
#pragma unroll
    for (int i = 0; i < 4; ++i) *reinterpret_cast<u32x4*>(c + (2 * i + (lane >> 5)) * 16) = x1p[i];
    *reinterpret_cast<u32x4*>(c + 128) = u32x4{mine, __float_as_uint(mb), __float_as_uint(rd), 0u};
  }
  lds_set((unsigned)(uintptr_t)smem + L_W + 32 + (tile & 3) * 4, epoch);
}

DI void attn_safe(rsrc_t rs, unsigned kvoff, const u32x8& q, const u32x4& ones, f32x16& o) {
  const s16x8 q0 = __builtin_bit_cast(s16x8, __builtin_shufflevector(q, q, 0, 1, 2, 3)), q1 = __builtin_bit_cast(s16x8, __builtin_shufflevector(q, q, 4, 5, 6, 7));
  f32x4 l = {0.f, 0.f, 0.f, 0.f};
#pragma unroll
  for (int e = 0; e < 16; ++e) o[e] = 0.f;
  float m = -INFINITY;
#pragma unroll 1
  for (int kt = 0; kt < 64; ++kt) {
    const unsigned p = kvoff + kt * 4096;
    const u32x4 k0 = ldg(rs, p), k1 = ldg(rs, p + 1024), v0 = ldg(rs, p + 2048), v1 = ldg(rs, p + 3072);
    f32x16 s = {};
    s = MFMA32(__builtin_bit_cast(s16x8, k0), q0, s);
    s = MFMA32(__builtin_bit_cast(s16x8, k1), q1, s);
    float mt = fmaxf(s[0], s[1]);
#pragma unroll
    for (int e = 2; e < 16; ++e) mt = fmaxf(mt, s[e]);
    const float mn = fmaxf(m, half_swap_max(mt));
    const float alpha = EX2(m - mn);
    m = mn;
#pragma unroll
    for (int e = 0; e < 16; ++e) { s[e] = EX2(s[e] - mn); o[e] *= alpha; }
    l[0] *= alpha;
    const bf16x16 pa = __builtin_bit_cast(bf16x16, __builtin_shufflevector(s, s, 0, 1, 2, 3, 4, 5, 6, 7));
    const bf16x16 pb = __builtin_bit_cast(bf16x16, __builtin_shufflevector(s, s, 8, 9, 10, 11, 12, 13, 14, 15));
    o = __builtin_amdgcn_smfmac_f32_32x32x32_bf16(__builtin_bit_cast(bf16x8, v0), pa, o, 0xdddd, 0, 0);
    l = __builtin_amdgcn_smfmac_f32_16x16x64_bf16(__builtin_bit_cast(bf16x8, ones), pa, l, 0xdddd, 0, 0);
    o = __builtin_amdgcn_smfmac_f32_32x32x32_bf16(__builtin_bit_cast(bf16x8, v1), pb, o, 0xdddd, 0, 0);
    l = __builtin_amdgcn_smfmac_f32_16x16x64_bf16(__builtin_bit_cast(bf16x8, ones), pb, l, 0xdddd, 0, 0);
  }
  o *= 1.0f / l[0];
}

extern "C" __global__ __launch_bounds__(512, 2) void mega_kernel(const u16* __restrict__ x0, const u16* __restrict__ wqkv, const u16* __restrict__ wo,
                                                              const u16* __restrict__ w_r, const u16* __restrict__ wg, const u16* __restrict__ wu,
                                                              const u16* __restrict__ wd, char* ws, u16* __restrict__ out) {
  __shared__ __attribute__((aligned(16))) char smem[LDS_BYTES];
  const int wid = __builtin_amdgcn_readfirstlane(threadIdx.x >> 6);
  STAMP_INIT;
  STAMPV(0);
  const int bid = blockIdx.x, xcd = bid & 7, loc = bid >> 3;
  const int tm = 8 * (xcd >> 2) + (loc >> 2), tn = 4 * (xcd & 3) + (loc & 3);
  const unsigned lbase = (unsigned)(uintptr_t)smem;
  const rsrc_t rs = __builtin_amdgcn_make_buffer_rsrc(ws, 0, (int)WS_BYTES, 0x00020000);
  volatile __attribute__((address_space(3))) unsigned* words = LDS_PTR(volatile unsigned, smem + L_W);
  u32x4 v[4];
  if (wid < 4)
    asm volatile("global_load_dwordx4 %0, %4, %5\n\tglobal_load_dwordx4 %1, %4, %5 offset:512\n\tglobal_load_dwordx4 %2, %4, %5 offset:1024\n\tglobal_load_dwordx4 %3, %4, %5 offset:1536"
                 : "=&v"(v[0]), "=&v"(v[1]), "=&v"(v[2]), "=&v"(v[3])
                 : "v"((threadIdx.x >> 5) * (D_ * 2) + (threadIdx.x & 31) * 16), "s"(x0 + (size_t)(tm * 128 + tn * 8) * D_) : "memory");
  else {
    asm volatile("" : "=v"(v[0]), "=v"(v[1]), "=v"(v[2]), "=v"(v[3]));
    if (wid == 6) {
      const char *c0, *c1;
      CODE_ADDR(mega_kernel, c0); CODE_ADDR(end_marker, c1);
      const unsigned o = (loc * 64 + (threadIdx.x & 63)) * 32;
      glds4(c0, (long long)o + 4 <= c1 - c0 ? o : 0u, lbase + NS * SLOTB);
    }
  }
  STAMPV(48);
  *reinterpret_cast<unsigned*>(smem + L_FULL + threadIdx.x * 4) = 0u;
  words[threadIdx.x & 31] = 0u;
  BAR_LGKM();
  if (wid < 4) {
    const int tid = threadIdx.x, lane = tid & 63, r32 = lane & 31, row = 2 * wid + (lane >> 5);
    char* sm = smem + L_XCH;
    unsigned dst = OFF_HF + (tm * 4 + (tn >> 2)) * TS + (tn & 3) * 128 + (tid >> 3) * 512 + (tid & 7) * 16;
    asm volatile("s_waitcnt vmcnt(0)" : "+v"(v[0]), "+v"(v[1]), "+v"(v[2]), "+v"(v[3]), "+v"(dst) :: "memory");
    STAMPV(49);
    float s = 0.f, s2 = 0.f;
#pragma unroll
    for (int i = 0; i < 4; ++i)
#pragma unroll
      for (int e = 0; e < 4; e += 2) { s = dot2(v[i][e], 0x3f803f80u, s); s2 = dot2(v[i][e + 1], 0x3f803f80u, s2); }
    const float mean = half_sum(s + s2) * (1.0f / 1024.0f);
    f32x2 m2 = {0.f, 0.f};
#pragma unroll
    for (int i = 0; i < 4; ++i)
#pragma unroll
      for (int e = 0; e < 4; ++e) { const f32x2 d = f32x2{lo16(v[i][e]), hi16(v[i][e])} - mean; m2 += d * d; }
    float mb, den;
    ln_scalars(mean, half_sum(m2[0] + m2[1]) * (1.0f / 1024.0f), mb, den);
    const float rd = 1.0f / den;
#pragma unroll
    for (int i = 0; i < 4; ++i) {
      char* p = sm + ((16 * i + (r32 >> 1)) * 2) * 144 + row * 16 + (r32 & 1) * 8;
      *reinterpret_cast<u32x2*>(p) = u32x2{ln_pair(v[i][0], mb, rd), ln_pair(v[i][1], mb, rd)};
      *reinterpret_cast<u32x2*>(p + 144) = u32x2{ln_pair(v[i][2], mb, rd), ln_pair(v[i][3], mb, rd)};
    }
    STAMPV(37);
    lds_set(lbase + L_W + 64 + 4 * wid, 1);
    lds_wait_ge<0>(lbase + L_W + 64 + 4 * (lane & 3), 1);
    STAMPV(50);
    u32x4 t[4];
#pragma unroll
    for (int i = 0; i < 4; ++i) t[i] = *reinterpret_cast<const u32x4*>(sm + (i * 32 + (tid >> 3)) * 144 + (tid & 7) * 16);
    SBAR();
#pragma unroll
    for (int i = 0; i < 4; ++i) stg(rs, dst + i * 16384, t[i]);
    STAMPV(51);
    DRAIN();
    lds_set(lbase + L_W + 64 + 4 * wid, 2);
    STAMPV(38);
  }
  const int lw = wid & 1;
  const char* wb = reinterpret_cast<const char*>(wqkv + (size_t)(16 * lw) * (3 * D_) + tn * 64);
  auto stage_lanes = [](unsigned (&wv)[3]) {
    const int lane = fresh_lane();
#pragma unroll
    for (int i = 0; i < 3; ++i) {
      const int li = lane + 64 * i, k = li / 24, cp = li % 24, c = cp ^ (4 * ((k >> 1) & 1));
      wv[i] = (unsigned)(k * (3 * D_ * 2) + (c >> 3) * (D_ * 2) + (c & 7) * 16 - i * 1024);
    }
  };
  auto stage = [&](const unsigned (&wv)[3], int s, int slot) {
    const char* p = wb + (size_t)(2 * kpos(tm, tn, s >> 1) + (s & 1)) * (32 * 3 * D_ * 2);
    glds_v3(p, wv, lbase + slot * SLOTB + lw * HALFB);
    glds_v3(p + 8 * (3 * D_ * 2), wv, lbase + slot * SLOTB + lw * HALFB + 3072);
  };
  auto exchange = [&](const u32x2& tag, unsigned line0, int me, bool bar = false) {
    const int lane = fresh_lane();
    __builtin_amdgcn_raw_buffer_store_b64(tag, rs, lane == 0 ? line0 + me * 128 : OOB, 0, 16);
    if (bar) BARRIER();
    JIT();
    unsigned po = lane < 16 ? line0 + lane * 128 : OOB;
    const u32x4 srd = {(unsigned)(uintptr_t)ws, (unsigned)((uintptr_t)ws >> 32) & 0xffffu, WS_BYTES, 0x00020000u};
    for (unsigned spins = 0;; ++spins) {
      u32x2 v = {0u, 0u};
      asm volatile("buffer_atomic_or_x2 %0, %1, %2, 0 offen sc0 sc1\n\ts_waitcnt vmcnt(0)" : "+v"(v) : "v"(po), "s"(srd) : "memory");
      po = ((v[0] == tag[0]) & (v[1] == tag[1])) ? OOB : po;
      if (__all(po == OOB) || spins > SPIN_CAP) break;
    }
  };
  if ((wid >> 1) == 2) {
    unsigned wv[3];
    stage_lanes(wv);
    const int r = tm & 7;
#pragma unroll 1
    for (int i = 0; i < NS; ++i) {
      const int s = i == 0 ? r : i <= r ? i - 1 : i;
      stage(wv, s, s);
      const int j = i - 1, full = j < 1 ? (j == 0 && r == 0) : j < r ? j : j + 1;
      asm volatile("s_waitcnt vmcnt(%2)\n\tds_write_b32 %0, %1" :: "v"(lbase + L_FULL + lw * 256), "v"(full), "n"(6 * 1) : "memory");
    }
    asm volatile("s_waitcnt vmcnt(0)\n\tds_write_b32 %0, %1" :: "v"(lbase + L_FULL + lw * 256), "v"(NS) : "memory");
    STAMPV(1);
  }
  if (wid == 7) {
    u32x2 tag = __builtin_amdgcn_raw_buffer_load_b64(rs, FL_EPOCH, 0, 16);
    tag[0] += 1u; tag[1] += 0x9E3779B9u;
    *reinterpret_cast<u32x2*>(smem + L_TAG) = tag;
    lds_wait_ge<0>(lbase + L_W + 64 + 4 * (fresh_lane() & 3), 2);
    exchange(tag, FL_X + tm * 2048, tn);
    lds_set(lbase + L_W + 24, 1);
  }
  if (wid >= 4) {
    auto wo_rows = [&](int g0, int n, int gs) {
      const int lane = fresh_lane();
      unsigned wov[4];
#pragma unroll
      for (int i = 0; i < 4; ++i) wov[i] = (unsigned)((8 * i + (lane >> 3)) * (D_ * 2) + ((lane & 7) ^ (4 * ((lane >> 4) & 1))) * 16 - i * 1024);
#pragma unroll 1
      for (int i = 0, k = (0 * (tm & 7)) % n; i < n; ++i, k = k + 1 == n ? 0 : k + 1) {
        const int g = g0 + gs * k;
        glds_v4t<3, 1>(reinterpret_cast<const char*>(wo) + (size_t)((((tn >> 2) + 1) * 256 + 32 * g) & 1023) * (D_ * 2) + tn * 128, wov, lbase + g * 4096);
      }
    };
    if (wid < 6) {
      const int lane = fresh_lane();
      unsigned wv[3];
      stage_lanes(wv);
      __builtin_amdgcn_s_setprio(3);
      const unsigned fra = lbase + L_FREE + (lane & 3) * 256, fla = lbase + L_FULL + lw * 256 + lane * 4;
      u32x4 pre[NS * 6];
#pragma unroll
      for (int i = 0; i < NS * 6; ++i) pre[i] = *reinterpret_cast<const u32x4*>(smem + lw * HALFB + lane * 16 + (i / 6) * SLOTB + (i % 6) * 1024);
#pragma unroll
      for (int i = 0; i < NS * 6; ++i) PIN(pre[i]);
#pragma unroll 1
      for (int L = 0; L < 2; ++L) {
        const int s0 = NS, lead = L ? 8 : NS - 1;
        int slot = 0;
#pragma unroll 1
        for (int s = NS; s < 32; ++s) {
          lds_wait_ge(fra, 32 * L + s - lead + 1);
          stage(wv, s, slot);
          JIT();
          slot = (slot == NS - 1) ? 0 : slot + 1;
          if (s >= s0 + DD) asm volatile("s_waitcnt vmcnt(%0)\n\tds_write_b32 %1, %2" :: "n"(DD * 6), "v"(fla), "v"(32 * L + s - DD + 1) : "memory");
        }
#pragma unroll
        for (int d = DD - 1; d >= 0; --d) asm volatile("s_waitcnt vmcnt(%0)\n\tds_write_b32 %1, %2" :: "n"(d * 6), "v"(fla), "v"(32 * L + 32 - d) : "memory");
        u32x2 tag = *reinterpret_cast<const u32x2*>(smem + L_TAG);
        PIN(tag);
        if (lw == 0) {
          lds_wait_ge(lbase + L_W + 96 + 4 * (lane & 1), 1);
          lds_wait_ge<0>(lbase + L_W + 80 + 4 * (lane & 3), 2 * L + 1);
          exchange(tag, FL_KV + (L * 16 + tn) * 2048, tm, true);
          lds_wait_ge(fra, 32 * L + 32);
          lds_set(lbase + L_W + 16, L + 1);
        } else { BAR_LGKM(); lds_wait_ge<8>(lbase + L_W + 16, L + 1); }
        __builtin_amdgcn_s_sleep(24);
        wo_rows(lw, 15, 2);
        STAMPV(7 + 16 * L);
        DRAIN();
        const unsigned fo = FL_O + (L * 16 + tm) * 2048;
        if (lw) {
          lds_set(lbase + L_W + 112, 2 * L + 1);
          lds_wait_ge<4>(lbase + L_W + 48 + 4 * (lane & 3), L + 1);
          lds_wait_ge<0>(lbase + L_W + 80 + 4 * (lane & 3), 2 * L + 2);
          __builtin_amdgcn_raw_buffer_store_b64(tag, rs, lane == 0 ? fo + tn * 128 : OOB, 0, 16);
          JIT();
          wo_rows(30, 2, 1);
          DRAIN();
          lds_set(lbase + L_W + 112, 2 * L + 2);
        } else {
          lds_wait_ge<4>(lbase + L_W + 112, 2 * L + 1);
          lds_wait_ge<4>(lbase + L_W + 48 + 4 * (lane & 3), L + 1);
          unsigned po = lane < 16 ? fo + ((lane + 4 + (tn & 12)) & 15) * 128 : OOB;
          const u32x4 srd = {(unsigned)(uintptr_t)ws, (unsigned)((uintptr_t)ws >> 32) & 0xffffu, WS_BYTES, 0x00020000u};
          for (unsigned spins = 0;; ++spins) {
            u32x2 v = {0u, 0u};
            asm volatile("buffer_atomic_or_x2 %0, %1, %2, 0 offen sc0 sc1\n\ts_waitcnt vmcnt(0)" : "+v"(v) : "v"(po), "s"(srd) : "memory");
            po = ((v[0] == tag[0]) & (v[1] == tag[1])) ? OOB : po;
            unsigned m = (unsigned)__ballot(po == OOB);
            m &= m >> 1; m &= m >> 2;
            m = (m & 1u) | ((m >> 3) & 2u) | ((m >> 6) & 4u) | ((m >> 9) & 8u);
            if (m == 15u || spins > SPIN_CAP) break;
            words[5 + 2 * L] = words[28] == 2u * L + 2u ? m : m & 7u;
          }
          words[5 + 2 * L] = words[28] == 2u * L + 2u ? 15u : 7u;
          lds_wait_ge<0>(lbase + L_W + 112, 2 * L + 2);
          words[5 + 2 * L] = 15u;
          __builtin_amdgcn_raw_buffer_store_b64(tag, rs, (bid == 0 && lane == 0) ? FL_EPOCH : OOB, 0, 16);
        }
        BAR_ALL();
        if (L == 0) {
          char* d = smem + lw * HALFB + fresh_lane() * 16;
#pragma unroll
          for (int s = 0; s < NS; ++s) {
#pragma unroll
            for (int i = 0; i < 6; ++i) *reinterpret_cast<u32x4*>(d + s * SLOTB + i * 1024) = pre[s * 6 + i];
            lds_set(fla, 32 + s + 1);
          }
          STAMPV(2);
          lds_wait_ge<2>(lbase + L_W + 24, 2);
        }
      }
      STAMP_END;
      return;
    }
    const int a = wid - 6;
    const bool ffn = loc < 28;
    const int grp = xcd * 7 + (ffn ? loc >> 2 : 0), qa = (loc & 3) * 2 + a;
    const int kr = loc, cg = xcd;
    const unsigned wa = lbase + L_W;
    lds_wait_ge<2>(wa + 24, 1);
    __builtin_amdgcn_s_sleep(16);
    auto transpose8 = [](u32x4* v) {
      u32x4 t[8];
#pragma unroll
      for (int j = 0; j < 8; ++j)
#pragma unroll
        for (int e = 0; e < 4; ++e) t[j][e] = (j & 1) ? PERM_HI(v[2 * e][j >> 1], v[2 * e + 1][j >> 1]) : PERM_LO(v[2 * e][j >> 1], v[2 * e + 1][j >> 1]);
#pragma unroll
      for (int j = 0; j < 8; ++j) { v[j] = t[j]; PIN(v[j]); }
    };
    u32x4 gf[32];
    u32x4 wdf[16];
    auto wait_go = [&](int L) {
      __builtin_amdgcn_s_setprio(1);
      lds_wait_ge<8>(lbase + L_W + 16, L + 1);
      __builtin_amdgcn_s_sleep(24);
    };
    auto load_wd = [&](unsigned dv) {
      const rsrc_t rd = __builtin_amdgcn_make_buffer_rsrc(const_cast<u16*>(wd) + (size_t)grp * 64 * D_ + 128 * qa, 0, 64 * D_ * 2 - 256 * qa, 0x00020000);
#pragma unroll
      for (int i = 0; i < 16; ++i) {
        wdf[i] = __builtin_amdgcn_raw_buffer_load_b128(rd, dv, (32 * (i >> 3) + (i & 7)) * (D_ * 2), 0);
        asm volatile("s_sleep 2\n\ts_waitcnt vmcnt(2)" ::: "memory");
      }
    };
    {
      LANE_VARS;
      if (a == 0) {
        const u32x4 w0 = *reinterpret_cast<const u32x4*>(w_r + lane * 8), w1 = *reinterpret_cast<const u32x4*>(w_r + 512 + lane * 8);
        float t = 0.f;
#pragma unroll
        for (int e = 0; e < 4; ++e) t = fadd(fadd(t, fadd(lo16(w0[e]), hi16(w0[e]))), fadd(lo16(w1[e]), hi16(w1[e])));
        words[0] = __float_as_uint(wave_sum(t));
        if ((lane >> 3) == (tn & 7)) *reinterpret_cast<u32x4*>(smem + L_WR + (lane & 7) * 16) = tn < 8 ? w0 : w1;
      }
      const u16* p = wqkv + (size_t)(64 * tm + 32 * a + hi) * (3 * D_) + (c24 >> 3) * D_ + tn * 64 + (c24 & 7) * 8;
      float cs[8] = {0.f, 0.f, 0.f, 0.f, 0.f, 0.f, 0.f, 0.f};
      u32x4 v[16];
#pragma unroll
      for (int i = 0; i < 16; ++i) v[i] = *reinterpret_cast<const u32x4*>(p + (size_t)i * (2 * 3 * D_));
      SBAR();
      const u32x4 sv = {SENT, SENT, SENT, SENT};
#pragma unroll
      for (int i = 0; i < 6; ++i) {
        const unsigned o = ((i * 256 + bid) * 128 + a * 64 + lane) * 16;
        stg(rs, o < SX_BYTES ? OFF_SX + o : OOB, sv);
      }

#pragma unroll
      for (int i = 0; i < 16; ++i)
#pragma unroll
        for (int e = 0; e < 4; ++e) { cs[2 * e] = fadd(cs[2 * e], lo16(v[i][e])); cs[2 * e + 1] = fadd(cs[2 * e + 1], hi16(v[i][e])); }
      u32x4 o;
#pragma unroll
      for (int e = 0; e < 4; ++e) { const float x = half_swap_sum(cs[e]), y = half_swap_sum(cs[4 + e]); o[e] = __float_as_uint(hi ? y : x); }
      stg(rs, r32 < 24 ? OFF_CS + (tn * 32 + tm * 2 + a) * 768 + r32 * 32 + hi * 16 : OOB, o);
    }
    {
      DRAIN();
      lds_set(wa + 96 + 4 * a, 1);
      BAR_LGKM();
      LANE_VARS;
      const char* gp = reinterpret_cast<const char*>(((lane & 8) ? wu : wg) + (size_t)(128 * qa + 16 * hi + 4 * tblk) * F_ + 64 * grp + (lane & 7) * 8);
      unsigned dv = (lane >> 4) * (8 * D_ * 2) + (lane & 15) * 16;
      PIN(gp); PIN(dv);
      unsigned sv[3];
#pragma unroll
      for (int i = 0; i < 3; ++i) { const int li = lane + 64 * i, k = li / 24, c = li % 24; sv[i] = (unsigned)(k * (3 * D_ * 2) + (c >> 3) * (D_ * 2) + (c & 7) * 16 - i * 1024); PIN(sv[i]); }
      wait_go(0);
#pragma unroll
      for (int i = 0; i < 32; ++i) {
        gf[i] = *reinterpret_cast<const u32x4*>(gp + (size_t)(32 * (i >> 3) + 8 * ((i >> 2) & 1) + (i & 3)) * (F_ * 2));
        asm volatile("s_sleep 2\n\ts_waitcnt vmcnt(2)" ::: "memory");
      }
      load_wd(dv);
      STAMPV(7);
      if (1) __builtin_amdgcn_s_setprio(0);
      lds_wait_ge<4>(wa + 80 + 4 * (lane & 3), 2);
      __builtin_amdgcn_s_setprio(0);
#pragma unroll
      for (int g = 0; g < 4; ++g) glds_v3(wqkv + (size_t)(32 * kr + 8 * g) * (3 * D_) + (2 * cg + a) * 64, sv, lbase + L_SL + a * 12288 + g * 3072);
      transpose8(gf); transpose8(gf + 8); transpose8(gf + 16); transpose8(gf + 24);
      transpose8(wdf); transpose8(wdf + 8);
      {
        LANE_VARS;
        f32x4 t[2];
#pragma unroll
        for (int c = 0; c < 2; ++c) {
          u32x4 v[8];
#pragma unroll
          for (int i = 0; i < 8; ++i) v[i] = ldg(rs, OFF_CS + (tn * 32 + 16 * c + 2 * i + hi) * 768 + a * 384 + c24 * 16);
          SBAR();
          t[c] = sum4<8>(v);
          SBAR();
        }
#pragma unroll
        for (int e = 0; e < 4; ++e) t[0][e] = half_swap_sum(fadd(t[0][e], t[1][e]));
        if (lane < 24) *reinterpret_cast<f32x4*>(smem + L_CS + a * 384 + lane * 16) = t[0];
      }
      u32x2 tag = *reinterpret_cast<const u32x2*>(smem + L_TAG);
      PIN(tag);
      BAR_ALL();
      if (a) {
        lds_wait_ge<0>(wa + 64 + 4 * (lane & 3), 3);
        exchange(tag, FL_X + (16 + tm) * 2048, tn);
        lds_set(wa + 24, 2);
        STAMPV(12);
      }
    }
#pragma unroll 1
    for (int L = fresh_s(0);; ++L) {
      const unsigned sx = OFF_SX + L * SX_LAYER;
      STAMPV(1 + 16 * L);
      u32x4 hf[4];
      unsigned key;
      { LANE_VARS;
      lds_wait_ge<0>(wa + 32 + 4 * tp, L + 1);
      if (a == 0) {
        const char* cp = smem + L_CAND + (r32 >> 3) * 144;
        const u32x4 c = *reinterpret_cast<const u32x4*>(cp + (lane & 7) * 16), rec = *reinterpret_cast<const u32x4*>(cp + 128);
        u32x4 o;
#pragma unroll
        for (int e = 0; e < 4; ++e) { unsigned h = ln_pair_s(c[e], -U2F(rec[1]), U2F(rec[2])); PIN(h); o[e] = nz(hi ? c[e] : h); }
        stg(rs, sx + SX_ROW + hi * 131072 + (tm * 4 + (r32 >> 3)) * 2048 + tn * 128 + (lane & 7) * 16, o);
        STAMPV(8 + 16 * L);
        lds_wait_ge<0>(wa + 104, L + 1);
        key = __builtin_amdgcn_readfirstlane(words[27]);
      } else {
        const u32x4 srd = {(unsigned)(uintptr_t)ws, (unsigned)((uintptr_t)ws >> 32) & 0xffffu, WS_BYTES, 0x00020000u};
        unsigned po = sx + SX_CAND + (tn * 16 + (lane >> 2)) * 128 + tp * 4;
        key = 0u;
        for (unsigned spins = 0;; ++spins) {
          unsigned v = 0u;
          asm volatile("buffer_atomic_or %0, %1, %2, 0 offen sc0 sc1\n\ts_waitcnt vmcnt(0)" : "+v"(v) : "v"(po), "s"(srd) : "memory");
          key = v == SENT ? key : max(key, v);
          po = v == SENT ? po : OOB;
          if (__all(po == OOB) || spins > SPIN_CAP) break;
        }
        key = __builtin_amdgcn_readfirstlane(wave_max_u32(key));
        words[27] = key;
        words[3] = 2047u - (key & 2047u);
        lds_set(wa + 104, L + 1);
        STAMPV(13 + 16 * L);
      }
      poll_all<4>(hf, false, [&](u32x4 (&t)[4]) {
#pragma unroll
        for (int i = 0; i < 4; ++i) t[i] = ldg(rs, sx + SX_ROW + ((2047 - (int)(key & 2047u)) >> 5) * 2048 + (16 * qa + 4 * i + (lane >> 4)) * 16);
      }, [&](const u32x4 (&t)[4]) { return none_sent(t); });
      if (L == 1) lds_set(wa + 4 + 4 * a, 2);
      }
      STAMPV(2 + 16 * L);
      f32x4 d[8];
      const f32x4 zero = {0.f, 0.f, 0.f, 0.f};
      SBAR();
      __builtin_amdgcn_s_setprio(3);
#pragma unroll
      for (int t = 0; t < 32; ++t) d[t & 7] = MFMA16(__builtin_bit_cast(s16x8, hf[t >> 3]), __builtin_bit_cast(s16x8, gf[t]), t < 8 ? zero : d[t & 7]);
      SBAR();
      __builtin_amdgcn_s_setprio(0);
      LANE_VARS;
      const int idx = 2047 - (int)(key & 2047u), win = idx >> 7;
      const bool owner = win == tm;
      auto pick2 = [&](const f32x4 (&d)[8]) {
        u32x2 o;
#pragma unroll
        for (int e = 0; e < 2; ++e) {
          const float x = tblk ? d[2 + e][0] : d[e][0], y = tblk ? d[6 + e][0] : d[4 + e][0];
          o[e] = nzf(hi ? y : x);
        }
        return o;
      };
      if (ffn) {
        const unsigned ppos = (lane & 15) * 32 + (lane >> 4) * 8;
        __builtin_amdgcn_raw_buffer_store_b64(pick2(d), rs, sx + SX_GP + (grp * 8 + qa) * 512 + ppos, 0, 16);
        STAMPV(9 + 16 * L);
        __builtin_amdgcn_s_sleep(16);
        u32x2 gu[8];
        for (unsigned spins = 0;; ++spins) {
#pragma unroll
          for (int i = 0; i < 8; ++i) gu[i] = __builtin_amdgcn_raw_buffer_load_b64(rs, sx + SX_GP + (grp * 8 + i) * 512 + lane * 8, 0, 16);
          SBAR();
          unsigned m = 0u;
#pragma unroll
          for (int i = 0; i < 8; ++i) m = max(max(m, gu[i][0]), gu[i][1]);
          if (__all(m != SENT) || spins > SPIN_CAP) break;
          poll_pause(L == 0);
        }
        STAMPV(10 + 16 * L);
        float t0 = opq(U2F(gu[0][0])), t1 = opq(U2F(gu[0][1]));
#pragma unroll
        for (int i = 1; i < 8; ++i) { t0 = fadd(t0, U2F(gu[i][0])); t1 = fadd(t1, U2F(gu[i][1])); }
        auto sw = __builtin_amdgcn_permlane32_swap(__float_as_uint(t0), __float_as_uint(t1), false, false);
        const float gv = rbf(U2F(sw[0])), uv = rbf(U2F(sw[1]));
        reinterpret_cast<u16*>(smem + L_GU + a * 128)[2 * r32 + hi] = (u16)pack2(fmul(rbf(fmul(gv, RCP(fadd(1.0f, __expf(-gv))))), uv), 0.f);
        asm volatile("s_waitcnt lgkmcnt(0)" ::: "memory");
        const s16x8 a0 = *reinterpret_cast<const s16x8*>(smem + L_GU + a * 128 + (lane >> 4) * 16), a1 = *reinterpret_cast<const s16x8*>(smem + L_GU + a * 128 + 64 + (lane >> 4) * 16);
        SBAR();
        __builtin_amdgcn_s_setprio(3);
#pragma unroll
        for (int t = 0; t < 16; ++t) d[t & 7] = MFMA16(t < 8 ? a0 : a1, __builtin_bit_cast(s16x8, wdf[t]), t < 8 ? zero : d[t & 7]);
        SBAR();
        __builtin_amdgcn_s_setprio(0);
        __builtin_amdgcn_raw_buffer_store_b64(pick2(d), rs, sx + SX_YP + grp * 4096 + qa * 512 + ppos, 0, 16);
      }
      STAMPV(3 + 16 * L);
      if (L == 1 && (cg != (kr & 7) || a)) { STAMP_END; __builtin_amdgcn_endpgm(); }
      s16x8 wpf[12];
#pragma unroll
      for (int t = 0; t < 12; ++t) wpf[t] = tr2(smem + L_SL + a * 12288 + (16 * hi + 4 * tblk + tq) * 384 + t * 32 + tp * 8, 8 * 384);
#pragma unroll
      for (int t = 0; t < 12; ++t) PIN(wpf[t]);
      if (L == 0 && !owner) lds_set(wa + 4 + 4 * a, 1);
      __builtin_amdgcn_s_sleep(9);
      if (L == 0 && !ffn) __builtin_amdgcn_s_sleep(64);
      const unsigned rk = key >> 16;
      float rsig = rbf(RCP(fadd(1.0f, __expf(-lo16((rk & 0x8000u) ? (rk & 0x7fffu) : (~rk & 0xffffu))))));
      PIN(rsig);
      const int n = 4 * (lane & 7) + 2 * tblk + hi;
      const int fo = ((n >> 4) * 2 + ((n >> 2) & 1)) * 8 + ((n >> 3) & 1) * 4 + (n & 3);
      unsigned odst = (L == 1 && !(lane & 8)) ? (idx * D_ + kr * 32 + n) * 2 : OOB;
      PIN(odst);
      u32x4 pv[8];
      poll_all<8>(pv, L == 0, [&](u32x4 (&t)[8]) {
#pragma unroll
        for (int i = 0; i < 7; ++i) t[i] = ldg(rs, sx + SX_YP + (8 * i + (lane >> 3)) * 4096 + kr * 128 + (lane & 7) * 16);
        t[7][0] = t[7][2] = __builtin_amdgcn_raw_buffer_load_b32(rs, sx + SX_ROW + (idx >> 5) * 2048 + kr * 64 + (fo >> 1) * 4, 0, 16);
        t[7][1] = t[7][3] = __builtin_amdgcn_raw_buffer_load_b32(rs, sx + SX_ROW + 131072 + (idx >> 5) * 2048 + kr * 64 + (fo >> 1) * 4, 0, 16);
      }, [&](const u32x4 (&t)[8]) { return none_sent(t); });
      const float hv = (fo & 1) ? hi16(pv[7][0]) : lo16(pv[7][0]), xv = (fo & 1) ? hi16(pv[7][1]) : lo16(pv[7][1]);
      STAMPV(4 + 16 * L);
      float ys;
      {
        const f32x4 t = sum4<7>(pv);
        auto p = __builtin_amdgcn_permlane32_swap(__float_as_uint(t[0]), __float_as_uint(t[1]), false, false);
        auto q = __builtin_amdgcn_permlane32_swap(__float_as_uint(t[2]), __float_as_uint(t[3]), false, false);
        auto r = __builtin_amdgcn_permlane16_swap(__float_as_uint(fadd(U2F(p[0]), U2F(p[1]))), __float_as_uint(fadd(U2F(q[0]), U2F(q[1]))), false, false);
        ys = fadd(U2F(r[0]), U2F(r[1]));
        ys = fadd(ys, U2F(dpp<0x128>(__float_as_uint(ys))));
      }
      const unsigned ob = pack2(fadd(xv, rbf(fadd(hv, rbf(fmul(rbf(ys), rsig))))), 0.f) & 0xffffu;
      __builtin_amdgcn_raw_buffer_store_b16((u16)ob, __builtin_amdgcn_make_buffer_rsrc(out, 0, 2048 * D_ * 2, 0x00020000), odst, 0, 0);
      if (L == 1) {
        STAMPV(5 + 16 * L);
        STAMP_END;
        __builtin_amdgcn_endpgm();
      }
      reinterpret_cast<u16*>(smem + L_X2F + a * 64)[fo] = (u16)ob;
      asm volatile("s_waitcnt lgkmcnt(0)" ::: "memory");
      {
        const s16x8 xa = *reinterpret_cast<const s16x8*>(smem + L_X2F + a * 64 + (lane >> 4) * 16);
        const u32x4 xf = *reinterpret_cast<const u32x4*>(smem + L_X2F + a * 64 + tp * 16);
        f32x4 z[12];
        SBAR();
        __builtin_amdgcn_s_setprio(3);
#pragma unroll
        for (int t = 0; t < 12; ++t) z[t] = MFMA16(xa, wpf[t], zero);
        SBAR();
        __builtin_amdgcn_s_setprio(0);
        const float x2 = lo16(ob), s1 = fmul(wave_sum(x2), 0.5f), s2 = fmul(wave_sum(fmul(x2, x2)), 0.5f);
        u32x4 o;
#pragma unroll
        for (int e = 0; e < 4; ++e) {
          const unsigned sp = lane < 52 ? (e == 0 ? __float_as_uint(s1) : __float_as_uint(s2)) : xf[e];
          const unsigned x = __float_as_uint(tblk ? z[4 + e][0] : z[e][0]), y = tblk ? sp : __float_as_uint(z[8 + e][0]);
          o[e] = nz(hi ? y : x);
        }
        const unsigned pos = lane < 24 ? lane * 16 : lane < 48 ? lane * 16 + 128 : lane < 52 ? (lane & 1) * 512 + 384 : 400 + tp * 16;
        stg(rs, lane < 56 ? OFF_SX + SX_QP + ((2 * cg + a) * 32 + kr) * 1024 + pos : OOB, o);
      }
      STAMPV(5 + 16 * L);
      if (owner) {
        __builtin_amdgcn_s_sleep(13);
        const int c25 = r32 < 25 ? r32 : 24, pl = 24 * a + c24, col = 64 * (pl >> 4) + (pl & 15);
        const int pfo = (col & ~15) + ((col >> 2) & 1) * 16 + ((col >> 3) & 1) * 8 + (col & 3);
        float cs[4];
#pragma unroll
        for (int e = 0; e < 4; ++e) { cs[e] = reinterpret_cast<const float*>(smem + L_CS)[col + 16 * e]; PIN(cs[e]); }
        u32x4 qv[17];
        poll_all<17>(qv, true, [&](u32x4 (&t)[17]) {
#pragma unroll
          for (int i = 0; i < 16; ++i) t[i] = ldg(rs, OFF_SX + SX_QP + (tn * 32 + 2 * i + hi) * 1024 + a * 512 + c25 * 16);
          t[16] = ldg(rs, OFF_SX + SX_QP + (tn * 34 + hi) * 1024 + 400 + tp * 16);
        }, [&](const u32x4 (&t)[17]) { return none_sent(t); });
        *reinterpret_cast<u32x4*>(smem + L_X2P + hi * 64 + tp * 16) = qv[16];
        f32x4 t = sum4<16>(qv);
#pragma unroll
        for (int e = 0; e < 4; ++e) t[e] = half_swap_sum(t[e]);
        const float mean = fmul(U2F(__builtin_amdgcn_readlane(__float_as_uint(t[0]), 24)), 1.0f / 1024.0f);
        const float var = fmaxf(ffma(-mean, mean, fmul(U2F(__builtin_amdgcn_readlane(__float_as_uint(t[1]), 24)), 1.0f / 1024.0f)), 0.f);
        float mb, den;
        ln_scalars(mean, var, mb, den);
        const float rd = RCP(den);
        if (lane < 24) {
#pragma unroll
          for (int e = 0; e < 4; ++e) reinterpret_cast<u16*>(smem + L_PATCH)[pfo + 32 * (e >> 1) + 4 * (e & 1)] = (u16)pack2(fmul(ffma(-mb, cs[e], t[e]), pl < 16 ? fmul(rd, QS) : rd), 0.f);
        }
        lds_set(wa + 4 + 4 * a, L + 1);
      }
      STAMPV(6 + 16 * L);
      BAR_ALL();
      unsigned dv = fresh_lane();
      dv = (dv >> 4) * (8 * D_ * 2) + (dv & 15) * 16;
      PIN(dv);
      wait_go(1);
      load_wd(dv);
      STAMPV(23);
      if (1) __builtin_amdgcn_s_setprio(0);
      lds_wait_ge<4>(wa + 80 + 4 * (fresh_lane() & 3), 4);
      __builtin_amdgcn_s_setprio(0);
      transpose8(wdf); transpose8(wdf + 8);
      BARRIER();
    }
  }
  const int tm_ = tm, tn_ = tn, wid_ = wid;
#define KSKIP "{s49}"(tn * 4096), "{s50}"(((tn + 1) & 15) * 4096)
  const u32x4 srd = {(unsigned)(uintptr_t)ws, (unsigned)((uintptr_t)ws >> 32) & 0xffffu, WS_BYTES, 0x00020000u};
  u32x4 x1p[4];
  asm volatile("" : "=v"(x1p[0]), "=v"(x1p[1]), "=v"(x1p[2]), "=v"(x1p[3]));
#pragma unroll 1
  for (int L = 0; L < 2; ++L) {
    const unsigned sx = OFF_SX + L * SX_LAYER;
    u32x8 q[4];
    u32x16 kva, kvb;
    f32x16 oz[5];
    {
      f32x16 acc[6];
      u32x16 stA, stB, xr0, xr1, xr2, xr3;
      u32x4 stC;
      asm volatile("" : "={v[112:127]}"(stB), "={v[128:131]}"(stC));
      { PHASE_VARS;
      stB[8] = (L ? OFF_X1 : OFF_HF) + tile * TS + tblk * 1024 + (hi * 32 + (lane & 15)) * 16;
      stB[9] = lbase + tblk * HALFB + wrow * 384 + (((4 * hh + (tp >> 1)) ^ wsw) << 4) + 8 * (tp & 1);
      stB[10] = lbase + tblk * HALFB + wrow * 384 + (((4 * (hh ^ 1) + (tp >> 1)) ^ wsw) << 4) + 8 * (tp & 1);
      stB[13] = lbase + L_FULL + (lane & 1) * 256;
      stB[15] = lbase + L_FREE + wid * 256 + lane * 4;
      stC[0] = 32 * L + 1; }
      unsigned soff, slot;
      if (L == 0) {
        lds_wait_ge<0>(lbase + L_W + 24, 1);
        STAMPV(39);
        soff = tn * 4096;
        asm volatile(
          "v_add_u32 v131, s44, v120\n\t"
          "buffer_load_dwordx4 v[132:135], v131, s[40:43], 0 offen offset:0 sc1\n\t"
          "buffer_load_dwordx4 v[136:139], v131, s[40:43], 0 offen offset:256 sc1\n\t"
          "buffer_load_dwordx4 v[140:143], v131, s[40:43], 0 offen offset:2048 sc1\n\t"
          "buffer_load_dwordx4 v[144:147], v131, s[40:43], 0 offen offset:2304 sc1\n\t"
          "s_mov_b32 s44, s51\n\t"
          "v_add_u32 v131, s44, v120\n\t"
          "buffer_load_dwordx4 v[148:151], v131, s[40:43], 0 offen offset:0 sc1\n\t"
          "buffer_load_dwordx4 v[152:155], v131, s[40:43], 0 offen offset:256 sc1\n\t"
          "buffer_load_dwordx4 v[156:159], v131, s[40:43], 0 offen offset:2048 sc1\n\t"
          "buffer_load_dwordx4 v[160:163], v131, s[40:43], 0 offen offset:2304 sc1\n\t"
          "s_addk_i32 s44, 0x1000\n\t"
          "s_bitset0_b32 s44, 16\n\t"
          "s_cmp_eq_u32 s44, s49\n\t"
          "s_cselect_b32 s44, s50, s44\n\t"
          "v_add_u32 v131, s44, v120\n\t"
          "buffer_load_dwordx4 v[164:167], v131, s[40:43], 0 offen offset:0 sc1\n\t"
          "buffer_load_dwordx4 v[168:171], v131, s[40:43], 0 offen offset:256 sc1\n\t"
          "buffer_load_dwordx4 v[172:175], v131, s[40:43], 0 offen offset:2048 sc1\n\t"
          "buffer_load_dwordx4 v[176:179], v131, s[40:43], 0 offen offset:2304 sc1\n\t"
          "s_addk_i32 s44, 0x1000\n\t"
          "s_bitset0_b32 s44, 16\n\t"
          "s_cmp_eq_u32 s44, s49\n\t"
          "s_cselect_b32 s44, s50, s44\n\t"
          "v_add_u32 v131, s44, v120\n\t"
          "buffer_load_dwordx4 v[180:183], v131, s[40:43], 0 offen offset:0 sc1\n\t"
          "buffer_load_dwordx4 v[184:187], v131, s[40:43], 0 offen offset:256 sc1\n\t"
          "buffer_load_dwordx4 v[188:191], v131, s[40:43], 0 offen offset:2048 sc1\n\t"
          "buffer_load_dwordx4 v[192:195], v131, s[40:43], 0 offen offset:2304 sc1\n\t"
          : "={v[132:147]}"(xr0), "={v[148:163]}"(xr1), "={v[164:179]}"(xr2), "={v[180:195]}"(xr3), "+{s44}"(soff), "+{v[128:131]}"(stC) : "{v[112:127]}"(stB), "{s[40:43]}"(srd), KSKIP, "{s51}"(kpos(tm, tn, 1) * 4096) : "scc", "memory");
      } else {
        u32x4 t[4] = {x1p[0], x1p[1], x1p[2], x1p[3]};
        row_swap(t[0], t[1]); row_swap(t[2], t[3]);
        xr0 = cat4(t[0], t[1], t[2], t[3]);
        asm volatile("" : "={v[148:163]}"(xr1), "={v[164:179]}"(xr2), "={v[180:195]}"(xr3));
      }
      STAMPV(1 + 16 * L);
      asm volatile(
        "v_mov_b32 v123, v121\n\t"
        "v_mov_b32 v124, v122\n\t"
        "s_mov_b64 vcc, -1\n\t"
        "s_cbranch_vccnz 10f\n\t"
        "11:\n\t"
        "s_movk_i32 s46, 12352\n\t"
        "s_mov_b32 s47, 111168\n\t"
        "ds_read_b64_tr_b16 v[96:97], v123 offset:256\n\t"
        "ds_read_b64_tr_b16 v[98:99], v123 offset:3328\n\t"
        "ds_read_b64_tr_b16 v[100:101], v123 offset:288\n\t"
        "ds_read_b64_tr_b16 v[102:103], v123 offset:3360\n\t"
        "ds_read_b64_tr_b16 v[104:105], v124 offset:256\n\t"
        "ds_read_b64_tr_b16 v[106:107], v124 offset:3328\n\t"
        "ds_write_b32 v127, v128\n\t"
        "ds_read_b64_tr_b16 v[108:109], v124 offset:288\n\t"
        "ds_read_b64_tr_b16 v[110:111], v124 offset:3360\n\t"
        "ds_read_b64_tr_b16 v[112:113], v123 offset:128\n\t"
        "ds_read_b64_tr_b16 v[114:115], v123 offset:3200\n\t"
        "v_add_u32 v128, 1, v128\n\t"
        "s_waitcnt vmcnt(14) lgkmcnt(7)\n\t"
        "v_mfma_f32_16x16x32_bf16 v[64:67], v[132:135], v[96:99], 0\n\t"
        "ds_read_b64_tr_b16 v[116:117], v123 offset:160\n\t"
        "v_mfma_f32_16x16x32_bf16 v[72:75], v[136:139], v[96:99], 0\n\t"
        "ds_read_b64_tr_b16 v[118:119], v123 offset:3232\n\t"
        "v_mfma_f32_16x16x32_bf16 v[68:71], v[132:135], v[100:103], 0\n\t"
        "ds_read_b64_tr_b16 v[96:97], v124 offset:128\n\t"
        "v_mfma_f32_16x16x32_bf16 v[76:79], v[136:139], v[100:103], 0\n\t"
        "ds_read_b64_tr_b16 v[98:99], v124 offset:3200\n\t"
        "s_waitcnt lgkmcnt(6)\n\t"
        "v_mfma_f32_16x16x32_bf16 v[80:83], v[132:135], v[104:107], 0\n\t"
        "ds_read_b64_tr_b16 v[100:101], v124 offset:160\n\t"
        "v_mfma_f32_16x16x32_bf16 v[88:91], v[136:139], v[104:107], 0\n\t"
        "ds_read_b64_tr_b16 v[102:103], v124 offset:3232\n\t"
        "v_mfma_f32_16x16x32_bf16 v[84:87], v[132:135], v[108:111], 0\n\t"
        "ds_read_b64_tr_b16 v[104:105], v123 offset:0\n\t"
        "ds_read_b32 v126, v125\n\t"
        "v_mfma_f32_16x16x32_bf16 v[92:95], v[136:139], v[108:111], 0\n\t"
        "ds_read_b64_tr_b16 v[106:107], v123 offset:3072\n\t"
        "s_waitcnt lgkmcnt(7)\n\t"
        "v_mfma_f32_16x16x32_bf16 v[32:35], v[112:115], v[132:135], 0\n\t"
        "ds_read_b64_tr_b16 v[108:109], v123 offset:32\n\t"
        "v_add_u32 v129, s46, v121\n\t"
        "v_mfma_f32_16x16x32_bf16 v[40:43], v[112:115], v[136:139], 0\n\t"
        "ds_read_b64_tr_b16 v[110:111], v123 offset:3104\n\t"
        "v_add_u32 v130, s46, v122\n\t"
        "v_mfma_f32_16x16x32_bf16 v[36:39], v[116:119], v[132:135], 0\n\t"
        "ds_read_b64_tr_b16 v[112:113], v124 offset:0\n\t"
        "v_mfma_f32_16x16x32_bf16 v[44:47], v[116:119], v[136:139], 0\n\t"
        "ds_read_b64_tr_b16 v[114:115], v124 offset:3072\n\t"
        "s_waitcnt lgkmcnt(5)\n\t"
        "v_mfma_f32_16x16x32_bf16 v[48:51], v[96:99], v[132:135], 0\n\t"
        "ds_read_b64_tr_b16 v[116:117], v124 offset:32\n\t"
        "v_cmp_gt_u32 vcc, v128, v126\n\t"
        "v_mfma_f32_16x16x32_bf16 v[56:59], v[96:99], v[136:139], 0\n\t"
        "ds_read_b64_tr_b16 v[118:119], v124 offset:3104\n\t"
        "s_cbranch_vccnz 12f\n\t"
        "13:\n\t"
        "v_mfma_f32_16x16x32_bf16 v[52:55], v[100:103], v[132:135], 0\n\t"
        "ds_read_b64_tr_b16 v[96:97], v129 offset:256\n\t"
        "s_addk_i32 s46, 12352\n\t"
        "v_mfma_f32_16x16x32_bf16 v[60:63], v[100:103], v[136:139], 0\n\t"
        "ds_read_b64_tr_b16 v[98:99], v129 offset:3328\n\t"
        "s_waitcnt lgkmcnt(6)\n\t"
        "v_mfma_f32_16x16x32_bf16 v[0:3], v[104:107], v[132:135], 0\n\t"
        "ds_read_b64_tr_b16 v[100:101], v129 offset:288\n\t"
        "s_cmp_eq_u32 s46, s47\n\t"
        "v_mfma_f32_16x16x32_bf16 v[8:11], v[104:107], v[136:139], 0\n\t"
        "ds_read_b64_tr_b16 v[102:103], v129 offset:3360\n\t"
        "s_cselect_b32 s46, 0, s46\n\t"
        "v_mfma_f32_16x16x32_bf16 v[4:7], v[108:111], v[132:135], 0\n\t"
        "ds_read_b64_tr_b16 v[104:105], v130 offset:256\n\t"
        "v_mfma_f32_16x16x32_bf16 v[12:15], v[108:111], v[136:139], 0\n\t"
        "ds_read_b64_tr_b16 v[106:107], v130 offset:3328\n\t"
        "s_waitcnt lgkmcnt(6)\n\t"
        "v_mfma_f32_16x16x32_bf16 v[16:19], v[112:115], v[132:135], 0\n\t"
        "ds_write_b32 v127, v128\n\t"
        "ds_read_b64_tr_b16 v[108:109], v130 offset:288\n\t"
        "v_mfma_f32_16x16x32_bf16 v[24:27], v[112:115], v[136:139], 0\n\t"
        "ds_read_b64_tr_b16 v[110:111], v130 offset:3360\n\t"
        "v_add_u32 v128, 1, v128\n\t"
        "v_mfma_f32_16x16x32_bf16 v[20:23], v[116:119], v[132:135], 0\n\t"
        "ds_read_b64_tr_b16 v[112:113], v129 offset:128\n\t"
        "v_mfma_f32_16x16x32_bf16 v[28:31], v[116:119], v[136:139], 0\n\t"
        "ds_read_b64_tr_b16 v[114:115], v129 offset:3200\n\t"
        "s_waitcnt vmcnt(12) lgkmcnt(7)\n\t"
        "v_mfma_f32_16x16x32_bf16 v[64:67], v[140:143], v[96:99], v[64:67]\n\t"
        "ds_read_b64_tr_b16 v[116:117], v129 offset:160\n\t"
        "v_mfma_f32_16x16x32_bf16 v[72:75], v[144:147], v[96:99], v[72:75]\n\t"
        "ds_read_b64_tr_b16 v[118:119], v129 offset:3232\n\t"
        "v_mfma_f32_16x16x32_bf16 v[68:71], v[140:143], v[100:103], v[68:71]\n\t"
        "ds_read_b64_tr_b16 v[96:97], v130 offset:128\n\t"
        "v_mfma_f32_16x16x32_bf16 v[76:79], v[144:147], v[100:103], v[76:79]\n\t"
        "ds_read_b64_tr_b16 v[98:99], v130 offset:3200\n\t"
        "s_waitcnt lgkmcnt(6)\n\t"
        "v_mfma_f32_16x16x32_bf16 v[80:83], v[140:143], v[104:107], v[80:83]\n\t"
        "ds_read_b64_tr_b16 v[100:101], v130 offset:160\n\t"
        "v_mfma_f32_16x16x32_bf16 v[88:91], v[144:147], v[104:107], v[88:91]\n\t"
        "ds_read_b64_tr_b16 v[102:103], v130 offset:3232\n\t"
        "v_mfma_f32_16x16x32_bf16 v[84:87], v[140:143], v[108:111], v[84:87]\n\t"
        "ds_read_b64_tr_b16 v[104:105], v129 offset:0\n\t"
        "ds_read_b32 v126, v125\n\t"
        "v_mfma_f32_16x16x32_bf16 v[92:95], v[144:147], v[108:111], v[92:95]\n\t"
        "ds_read_b64_tr_b16 v[106:107], v129 offset:3072\n\t"
        "s_waitcnt lgkmcnt(7)\n\t"
        "v_mfma_f32_16x16x32_bf16 v[32:35], v[112:115], v[140:143], v[32:35]\n\t"
        "ds_read_b64_tr_b16 v[108:109], v129 offset:32\n\t"
        "v_add_u32 v123, s46, v121\n\t"
        "v_mfma_f32_16x16x32_bf16 v[40:43], v[112:115], v[144:147], v[40:43]\n\t"
        "ds_read_b64_tr_b16 v[110:111], v129 offset:3104\n\t"
        "v_add_u32 v124, s46, v122\n\t"
        "v_mfma_f32_16x16x32_bf16 v[36:39], v[116:119], v[140:143], v[36:39]\n\t"
        "ds_read_b64_tr_b16 v[112:113], v130 offset:0\n\t"
        "v_mfma_f32_16x16x32_bf16 v[44:47], v[116:119], v[144:147], v[44:47]\n\t"
        "ds_read_b64_tr_b16 v[114:115], v130 offset:3072\n\t"
        "s_waitcnt lgkmcnt(5)\n\t"
        "v_mfma_f32_16x16x32_bf16 v[48:51], v[96:99], v[140:143], v[48:51]\n\t"
        "ds_read_b64_tr_b16 v[116:117], v130 offset:32\n\t"
        "v_cmp_gt_u32 vcc, v128, v126\n\t"
        "v_mfma_f32_16x16x32_bf16 v[56:59], v[96:99], v[144:147], v[56:59]\n\t"
        "ds_read_b64_tr_b16 v[118:119], v130 offset:3104\n\t"
        "s_cbranch_vccnz 14f\n\t"
        "15:\n\t"
        "v_mfma_f32_16x16x32_bf16 v[52:55], v[100:103], v[140:143], v[52:55]\n\t"
        "ds_read_b64_tr_b16 v[96:97], v123 offset:256\n\t"
        "s_addk_i32 s46, 12352\n\t"
        "v_mfma_f32_16x16x32_bf16 v[60:63], v[100:103], v[144:147], v[60:63]\n\t"
        "ds_read_b64_tr_b16 v[98:99], v123 offset:3328\n\t"
        "s_waitcnt lgkmcnt(6)\n\t"
        "v_mfma_f32_16x16x32_bf16 v[0:3], v[104:107], v[140:143], v[0:3]\n\t"
        "ds_read_b64_tr_b16 v[100:101], v123 offset:288\n\t"
        "s_cmp_eq_u32 s46, s47\n\t"
        "v_mfma_f32_16x16x32_bf16 v[8:11], v[104:107], v[144:147], v[8:11]\n\t"
        "ds_read_b64_tr_b16 v[102:103], v123 offset:3360\n\t"
        "s_cselect_b32 s46, 0, s46\n\t"
        "v_mfma_f32_16x16x32_bf16 v[4:7], v[108:111], v[140:143], v[4:7]\n\t"
        "ds_read_b64_tr_b16 v[104:105], v124 offset:256\n\t"
        "v_mfma_f32_16x16x32_bf16 v[12:15], v[108:111], v[144:147], v[12:15]\n\t"
        "ds_read_b64_tr_b16 v[106:107], v124 offset:3328\n\t"
        "s_waitcnt lgkmcnt(6)\n\t"
        "v_mfma_f32_16x16x32_bf16 v[16:19], v[112:115], v[140:143], v[16:19]\n\t"
        "ds_write_b32 v127, v128\n\t"
        "ds_read_b64_tr_b16 v[108:109], v124 offset:288\n\t"
        "v_mfma_f32_16x16x32_bf16 v[24:27], v[112:115], v[144:147], v[24:27]\n\t"
        "ds_read_b64_tr_b16 v[110:111], v124 offset:3360\n\t"
        "v_add_u32 v128, 1, v128\n\t"
        "v_mfma_f32_16x16x32_bf16 v[20:23], v[116:119], v[140:143], v[20:23]\n\t"
        "ds_read_b64_tr_b16 v[112:113], v123 offset:128\n\t"
        "v_mfma_f32_16x16x32_bf16 v[28:31], v[116:119], v[144:147], v[28:31]\n\t"
        "ds_read_b64_tr_b16 v[114:115], v123 offset:3200\n\t"
        "s_waitcnt lgkmcnt(0)\n\t"
        "s_nop 7\n\t"
        "s_branch 9f\n\t"
        "10:\n\t"
        "s_mov_b32 s48, 0x10000\n\t"
        "8:\n\t"
        "ds_read_b32 v126, v125\n\t"
        "s_sub_u32 s48, s48, 1\n\t"
        "s_waitcnt lgkmcnt(0)\n\t"
        "s_cbranch_scc1 11b\n\t"
        "v_cmp_gt_u32 vcc, v128, v126\n\t"
        "s_cbranch_vccnz 8b\n\t"
        "s_branch 11b\n\t"
        "12:\n\t"
        "s_mov_b32 s48, 0x10000\n\t"
        "8:\n\t"
        "ds_read_b32 v126, v125\n\t"
        "s_sub_u32 s48, s48, 1\n\t"
        "s_waitcnt lgkmcnt(0)\n\t"
        "s_cbranch_scc1 13b\n\t"
        "v_cmp_gt_u32 vcc, v128, v126\n\t"
        "s_cbranch_vccnz 8b\n\t"
        "s_branch 13b\n\t"
        "14:\n\t"
        "s_mov_b32 s48, 0x10000\n\t"
        "8:\n\t"
        "ds_read_b32 v126, v125\n\t"
        "s_sub_u32 s48, s48, 1\n\t"
        "s_waitcnt lgkmcnt(0)\n\t"
        "s_cbranch_scc1 15b\n\t"
        "v_cmp_gt_u32 vcc, v128, v126\n\t"
        "s_cbranch_vccnz 8b\n\t"
        "s_branch 15b\n\t"
        "9:\n\t"
        : "={v[0:15]}"(acc[0]), "={v[16:31]}"(acc[1]), "={v[32:47]}"(acc[2]), "={v[48:63]}"(acc[3]), "={v[64:79]}"(acc[4]), "={v[80:95]}"(acc[5]),
          "={v[96:111]}"(stA), "+{v[112:127]}"(stB), "+{v[128:131]}"(stC), "={s46}"(slot)
        : "{v[132:147]}"(xr0) : "s47", "s48", "vcc", "scc", "memory");
      STAMPV(2 + 16 * L);
      if (L == 1) {
        PHASE_VARS;
        float S, Q, Dd;
        row_stats_sum(rs, OFF_SX, tile, lane, S, Q, Dd);
        STAMPV(36);
        lds_wait_ge<0>(lbase + L_W + 24, 2);
        soff = kpos(tm, tn, 1) * 4096;
        asm volatile(
          "v_add_u32 v131, s44, v120\n\t"
          "buffer_load_dwordx4 v[148:151], v131, s[40:43], 0 offen offset:0 sc1\n\t"
          "buffer_load_dwordx4 v[152:155], v131, s[40:43], 0 offen offset:256 sc1\n\t"
          "buffer_load_dwordx4 v[156:159], v131, s[40:43], 0 offen offset:2048 sc1\n\t"
          "buffer_load_dwordx4 v[160:163], v131, s[40:43], 0 offen offset:2304 sc1\n\t"
          : "={v[148:163]}"(xr1), "={v[164:179]}"(xr2), "={v[180:195]}"(xr3), "+{s44}"(soff), "+v"(S), "+v"(Q), "+v"(Dd), "+{v[128:131]}"(stC) : "{v[112:127]}"(stB), "{s[40:43]}"(srd), KSKIP, "{s51}"(kpos(tm, tn, 1) * 4096) : "scc", "memory");
        float mean, var, mb1, rd1, mb2, den2;
        row_stats_fin(rs, OFF_SX + SX_CAND + (tn * 16 + tm) * 128 + wid * 4, smem, 1, tile, lane, S, Q, Dd, U2F(words[0]), x1p, mean, var, mb1, rd1);
        ln_scalars(mean + (mean - mb1) * rd1, var * (1.0f + rd1) * (1.0f + rd1), mb2, den2);
        const float rd2 = RCP(den2);
        const float al = (1.0f + rd1) * rd2;
        const float be = -(mb1 * rd1 + mb2) * RCP(1.0f + rd1);
        reinterpret_cast<float*>(smem + L_AB)[wid * 64 + lane] = hi ? be : al;
        reinterpret_cast<float*>(smem + L_AB)[256 + wid * 64 + lane] = hi ? 1.0f + rd1 : -mb1 * rd1;
        soff = kpos(tm, tn, 1 + 0) * 4096;
        asm volatile(
          "s_nop 0\n\t"
          "v_add_u32 v131, s44, v120\n\t"
          "s_addk_i32 s44, 0x1000\n\t"
          "s_bitset0_b32 s44, 16\n\t"
          "s_cmp_eq_u32 s44, s49\n\t"
          "s_cselect_b32 s44, s50, s44\n\t"
          "v_add_u32 v131, s44, v120\n\t"
          "buffer_load_dwordx4 v[164:167], v131, s[40:43], 0 offen offset:0 sc1\n\t"
          "buffer_load_dwordx4 v[168:171], v131, s[40:43], 0 offen offset:256 sc1\n\t"
          "buffer_load_dwordx4 v[172:175], v131, s[40:43], 0 offen offset:2048 sc1\n\t"
          "buffer_load_dwordx4 v[176:179], v131, s[40:43], 0 offen offset:2304 sc1\n\t"
          "s_addk_i32 s44, 0x1000\n\t"
          "s_bitset0_b32 s44, 16\n\t"
          "s_cmp_eq_u32 s44, s49\n\t"
          "s_cselect_b32 s44, s50, s44\n\t"
          "v_add_u32 v131, s44, v120\n\t"
          "buffer_load_dwordx4 v[180:183], v131, s[40:43], 0 offen offset:0 sc1\n\t"
          "buffer_load_dwordx4 v[184:187], v131, s[40:43], 0 offen offset:256 sc1\n\t"
          "buffer_load_dwordx4 v[188:191], v131, s[40:43], 0 offen offset:2048 sc1\n\t"
          "buffer_load_dwordx4 v[192:195], v131, s[40:43], 0 offen offset:2304 sc1\n\t"
          : "+{v[148:163]}"(xr1), "+{v[164:179]}"(xr2), "+{v[180:195]}"(xr3), "+{s44}"(soff), "+{v[128:131]}"(stC) : "{v[112:127]}"(stB), "{s[40:43]}"(srd), KSKIP, "{s51}"(kpos(tm, tn, 1) * 4096) : "scc", "memory");
        const float* ab = reinterpret_cast<const float*>(smem + L_AB) + wid * 64 + 32;
        const float bt[2] = {ab[lane & 15], ab[16 + (lane & 15)]};
        f32x4 cs[4][2], bv[2];
        float cv[2][2];
#pragma unroll
        for (int i = 0; i < 2; ++i) {
          bv[i] = *reinterpret_cast<const f32x4*>(ab + 16 * i + 4 * (lane >> 4));
#pragma unroll
          for (int j = 0; j < 6; ++j) {
            const float* p = reinterpret_cast<const float*>(smem + L_CS) + (j >> 1) * 64 + ((j & 1) ^ hh) * 32 + 16 * i;
            if (j < 4) cs[j][i] = *reinterpret_cast<const f32x4*>(p + 4 * (lane >> 4)); else cv[j - 4][i] = p[lane & 15];
          }
        }
        SBAR();
#pragma unroll
        for (int j = 0; j < 6; ++j)
#pragma unroll
          for (int e = 0; e < 16; ++e) acc[j][e] += j < 4 ? bt[e >> 3] * cs[j & 3][(e >> 2) & 1][e & 3] : bv[e >> 3][e & 3] * cv[j & 1][(e >> 2) & 1];
        SBAR();
      }
      u32x4 ep;
      f32x2 al2;
      { PHASE_VARS;
        float* ab = reinterpret_cast<float*>(smem + L_AB) + wid * 64;
        if (L == 0) ab[lane] = 1.0f;
        al2 = f32x2{ab[lane & 15], ab[16 + (lane & 15)]};
        const unsigned dst = OFF_KV + L * (10 * MBY) + tn * 2 * HS + tile * 4096 + lane * 16;
        ep = u32x4{0u, dst + hh * HS, dst + (hh ^ 1) * HS, lbase + L_AB + wid * 256 + (lane >> 4) * 16}; }
      STAMPV(3 + 16 * L);
      soff = kpos(tm, tn, 1 + 2) * 4096;
      asm volatile(
        "s_mov_b32 s47, 111168\n\t"
        "v_add_u32 v131, s44, v120\n\t"
        "s_addk_i32 s44, 0x1000\n\t"
        "s_bitset0_b32 s44, 16\n\t"
        "s_cmp_eq_u32 s44, s49\n\t"
        "s_cselect_b32 s44, s50, s44\n\t"
        "v_add_u32 v131, s44, v120\n\t"
        "buffer_load_dwordx4 v[132:135], v131, s[40:43], 0 offen offset:0 sc1\n\t"
        "buffer_load_dwordx4 v[136:139], v131, s[40:43], 0 offen offset:256 sc1\n\t"
        "s_mov_b32 s45, 2\n\t"
        "1:\n\t"
        "s_waitcnt vmcnt(12) lgkmcnt(7)\n\t"
        "v_mfma_f32_16x16x32_bf16 v[64:67], v[148:151], v[96:99], v[64:67]\n\t"
        "ds_read_b64_tr_b16 v[116:117], v123 offset:160\n\t"
        "v_mfma_f32_16x16x32_bf16 v[72:75], v[152:155], v[96:99], v[72:75]\n\t"
        "ds_read_b64_tr_b16 v[118:119], v123 offset:3232\n\t"
        "v_mfma_f32_16x16x32_bf16 v[68:71], v[148:151], v[100:103], v[68:71]\n\t"
        "ds_read_b64_tr_b16 v[96:97], v124 offset:128\n\t"
        "v_mfma_f32_16x16x32_bf16 v[76:79], v[152:155], v[100:103], v[76:79]\n\t"
        "ds_read_b64_tr_b16 v[98:99], v124 offset:3200\n\t"
        "s_waitcnt lgkmcnt(6)\n\t"
        "v_mfma_f32_16x16x32_bf16 v[80:83], v[148:151], v[104:107], v[80:83]\n\t"
        "ds_read_b64_tr_b16 v[100:101], v124 offset:160\n\t"
        "v_mfma_f32_16x16x32_bf16 v[88:91], v[152:155], v[104:107], v[88:91]\n\t"
        "ds_read_b64_tr_b16 v[102:103], v124 offset:3232\n\t"
        "v_mfma_f32_16x16x32_bf16 v[84:87], v[148:151], v[108:111], v[84:87]\n\t"
        "ds_read_b64_tr_b16 v[104:105], v123 offset:0\n\t"
        "ds_read_b32 v126, v125\n\t"
        "v_mfma_f32_16x16x32_bf16 v[92:95], v[152:155], v[108:111], v[92:95]\n\t"
        "ds_read_b64_tr_b16 v[106:107], v123 offset:3072\n\t"
        "s_waitcnt lgkmcnt(7)\n\t"
        "v_mfma_f32_16x16x32_bf16 v[32:35], v[112:115], v[148:151], v[32:35]\n\t"
        "ds_read_b64_tr_b16 v[108:109], v123 offset:32\n\t"
        "v_add_u32 v129, s46, v121\n\t"
        "v_mfma_f32_16x16x32_bf16 v[40:43], v[112:115], v[152:155], v[40:43]\n\t"
        "ds_read_b64_tr_b16 v[110:111], v123 offset:3104\n\t"
        "v_add_u32 v130, s46, v122\n\t"
        "v_mfma_f32_16x16x32_bf16 v[36:39], v[116:119], v[148:151], v[36:39]\n\t"
        "ds_read_b64_tr_b16 v[112:113], v124 offset:0\n\t"
        "buffer_load_dwordx4 v[140:143], v131, s[40:43], 0 offen offset:2048 sc1\n\t"
        "v_mfma_f32_16x16x32_bf16 v[44:47], v[116:119], v[152:155], v[44:47]\n\t"
        "ds_read_b64_tr_b16 v[114:115], v124 offset:3072\n\t"
        "s_waitcnt lgkmcnt(5)\n\t"
        "v_mfma_f32_16x16x32_bf16 v[48:51], v[96:99], v[148:151], v[48:51]\n\t"
        "ds_read_b64_tr_b16 v[116:117], v124 offset:32\n\t"
        "v_cmp_gt_u32 vcc, v128, v126\n\t"
        "v_mfma_f32_16x16x32_bf16 v[56:59], v[96:99], v[152:155], v[56:59]\n\t"
        "ds_read_b64_tr_b16 v[118:119], v124 offset:3104\n\t"
        "s_cbranch_vccnz 16f\n\t"
        "17:\n\t"
        "v_mfma_f32_16x16x32_bf16 v[52:55], v[100:103], v[148:151], v[52:55]\n\t"
        "ds_read_b64_tr_b16 v[96:97], v129 offset:256\n\t"
        "s_addk_i32 s46, 12352\n\t"
        "v_mfma_f32_16x16x32_bf16 v[60:63], v[100:103], v[152:155], v[60:63]\n\t"
        "ds_read_b64_tr_b16 v[98:99], v129 offset:3328\n\t"
        "s_waitcnt lgkmcnt(6)\n\t"
        "v_mfma_f32_16x16x32_bf16 v[0:3], v[104:107], v[148:151], v[0:3]\n\t"
        "ds_read_b64_tr_b16 v[100:101], v129 offset:288\n\t"
        "s_cmp_eq_u32 s46, s47\n\t"
        "v_mfma_f32_16x16x32_bf16 v[8:11], v[104:107], v[152:155], v[8:11]\n\t"
        "ds_read_b64_tr_b16 v[102:103], v129 offset:3360\n\t"
        "s_cselect_b32 s46, 0, s46\n\t"
        "v_mfma_f32_16x16x32_bf16 v[4:7], v[108:111], v[148:151], v[4:7]\n\t"
        "ds_read_b64_tr_b16 v[104:105], v130 offset:256\n\t"
        "buffer_load_dwordx4 v[144:147], v131, s[40:43], 0 offen offset:2304 sc1\n\t"
        "v_mfma_f32_16x16x32_bf16 v[12:15], v[108:111], v[152:155], v[12:15]\n\t"
        "ds_read_b64_tr_b16 v[106:107], v130 offset:3328\n\t"
        "s_waitcnt lgkmcnt(6)\n\t"
        "v_mfma_f32_16x16x32_bf16 v[16:19], v[112:115], v[148:151], v[16:19]\n\t"
        "ds_write_b32 v127, v128\n\t"
        "ds_read_b64_tr_b16 v[108:109], v130 offset:288\n\t"
        "v_mfma_f32_16x16x32_bf16 v[24:27], v[112:115], v[152:155], v[24:27]\n\t"
        "ds_read_b64_tr_b16 v[110:111], v130 offset:3360\n\t"
        "v_add_u32 v128, 1, v128\n\t"
        "v_mfma_f32_16x16x32_bf16 v[20:23], v[116:119], v[148:151], v[20:23]\n\t"
        "ds_read_b64_tr_b16 v[112:113], v129 offset:128\n\t"
        "v_mfma_f32_16x16x32_bf16 v[28:31], v[116:119], v[152:155], v[28:31]\n\t"
        "ds_read_b64_tr_b16 v[114:115], v129 offset:3200\n\t"
        "s_waitcnt vmcnt(12) lgkmcnt(7)\n\t"
        "v_mfma_f32_16x16x32_bf16 v[64:67], v[156:159], v[96:99], v[64:67]\n\t"
        "ds_read_b64_tr_b16 v[116:117], v129 offset:160\n\t"
        "s_addk_i32 s44, 0x1000\n\t"
        "v_mfma_f32_16x16x32_bf16 v[72:75], v[160:163], v[96:99], v[72:75]\n\t"
        "ds_read_b64_tr_b16 v[118:119], v129 offset:3232\n\t"
        "s_bitset0_b32 s44, 16\n\t"
        "v_mfma_f32_16x16x32_bf16 v[68:71], v[156:159], v[100:103], v[68:71]\n\t"
        "ds_read_b64_tr_b16 v[96:97], v130 offset:128\n\t"
        "s_cmp_eq_u32 s44, s49\n\t"
        "v_mfma_f32_16x16x32_bf16 v[76:79], v[160:163], v[100:103], v[76:79]\n\t"
        "ds_read_b64_tr_b16 v[98:99], v130 offset:3200\n\t"
        "s_waitcnt lgkmcnt(6)\n\t"
        "v_mfma_f32_16x16x32_bf16 v[80:83], v[156:159], v[104:107], v[80:83]\n\t"
        "ds_read_b64_tr_b16 v[100:101], v130 offset:160\n\t"
        "s_cselect_b32 s44, s50, s44\n\t"
        "v_mfma_f32_16x16x32_bf16 v[88:91], v[160:163], v[104:107], v[88:91]\n\t"
        "ds_read_b64_tr_b16 v[102:103], v130 offset:3232\n\t"
        "v_add_u32 v131, s44, v120\n\t"
        "v_mfma_f32_16x16x32_bf16 v[84:87], v[156:159], v[108:111], v[84:87]\n\t"
        "ds_read_b64_tr_b16 v[104:105], v129 offset:0\n\t"
        "ds_read_b32 v126, v125\n\t"
        "v_mfma_f32_16x16x32_bf16 v[92:95], v[160:163], v[108:111], v[92:95]\n\t"
        "ds_read_b64_tr_b16 v[106:107], v129 offset:3072\n\t"
        "s_waitcnt lgkmcnt(7)\n\t"
        "v_mfma_f32_16x16x32_bf16 v[32:35], v[112:115], v[156:159], v[32:35]\n\t"
        "ds_read_b64_tr_b16 v[108:109], v129 offset:32\n\t"
        "v_add_u32 v123, s46, v121\n\t"
        "v_mfma_f32_16x16x32_bf16 v[40:43], v[112:115], v[160:163], v[40:43]\n\t"
        "ds_read_b64_tr_b16 v[110:111], v129 offset:3104\n\t"
        "v_add_u32 v124, s46, v122\n\t"
        "v_mfma_f32_16x16x32_bf16 v[36:39], v[116:119], v[156:159], v[36:39]\n\t"
        "ds_read_b64_tr_b16 v[112:113], v130 offset:0\n\t"
        "buffer_load_dwordx4 v[148:151], v131, s[40:43], 0 offen offset:0 sc1\n\t"
        "v_mfma_f32_16x16x32_bf16 v[44:47], v[116:119], v[160:163], v[44:47]\n\t"
        "ds_read_b64_tr_b16 v[114:115], v130 offset:3072\n\t"
        "s_waitcnt lgkmcnt(5)\n\t"
        "v_mfma_f32_16x16x32_bf16 v[48:51], v[96:99], v[156:159], v[48:51]\n\t"
        "ds_read_b64_tr_b16 v[116:117], v130 offset:32\n\t"
        "v_cmp_gt_u32 vcc, v128, v126\n\t"
        "v_mfma_f32_16x16x32_bf16 v[56:59], v[96:99], v[160:163], v[56:59]\n\t"
        "ds_read_b64_tr_b16 v[118:119], v130 offset:3104\n\t"
        "s_cbranch_vccnz 18f\n\t"
        "19:\n\t"
        "v_mfma_f32_16x16x32_bf16 v[52:55], v[100:103], v[156:159], v[52:55]\n\t"
        "ds_read_b64_tr_b16 v[96:97], v123 offset:256\n\t"
        "s_addk_i32 s46, 12352\n\t"
        "v_mfma_f32_16x16x32_bf16 v[60:63], v[100:103], v[160:163], v[60:63]\n\t"
        "ds_read_b64_tr_b16 v[98:99], v123 offset:3328\n\t"
        "s_waitcnt lgkmcnt(6)\n\t"
        "v_mfma_f32_16x16x32_bf16 v[0:3], v[104:107], v[156:159], v[0:3]\n\t"
        "ds_read_b64_tr_b16 v[100:101], v123 offset:288\n\t"
        "s_cmp_eq_u32 s46, s47\n\t"
        "v_mfma_f32_16x16x32_bf16 v[8:11], v[104:107], v[160:163], v[8:11]\n\t"
        "ds_read_b64_tr_b16 v[102:103], v123 offset:3360\n\t"
        "s_cselect_b32 s46, 0, s46\n\t"
        "v_mfma_f32_16x16x32_bf16 v[4:7], v[108:111], v[156:159], v[4:7]\n\t"
        "ds_read_b64_tr_b16 v[104:105], v124 offset:256\n\t"
        "buffer_load_dwordx4 v[152:155], v131, s[40:43], 0 offen offset:256 sc1\n\t"
        "v_mfma_f32_16x16x32_bf16 v[12:15], v[108:111], v[160:163], v[12:15]\n\t"
        "ds_read_b64_tr_b16 v[106:107], v124 offset:3328\n\t"
        "s_waitcnt lgkmcnt(6)\n\t"
        "v_mfma_f32_16x16x32_bf16 v[16:19], v[112:115], v[156:159], v[16:19]\n\t"
        "ds_write_b32 v127, v128\n\t"
        "ds_read_b64_tr_b16 v[108:109], v124 offset:288\n\t"
        "v_mfma_f32_16x16x32_bf16 v[24:27], v[112:115], v[160:163], v[24:27]\n\t"
        "ds_read_b64_tr_b16 v[110:111], v124 offset:3360\n\t"
        "v_add_u32 v128, 1, v128\n\t"
        "v_mfma_f32_16x16x32_bf16 v[20:23], v[116:119], v[156:159], v[20:23]\n\t"
        "ds_read_b64_tr_b16 v[112:113], v123 offset:128\n\t"
        "v_mfma_f32_16x16x32_bf16 v[28:31], v[116:119], v[160:163], v[28:31]\n\t"
        "ds_read_b64_tr_b16 v[114:115], v123 offset:3200\n\t"
        "s_waitcnt vmcnt(12) lgkmcnt(7)\n\t"
        "v_mfma_f32_16x16x32_bf16 v[64:67], v[164:167], v[96:99], v[64:67]\n\t"
        "ds_read_b64_tr_b16 v[116:117], v123 offset:160\n\t"
        "v_mfma_f32_16x16x32_bf16 v[72:75], v[168:171], v[96:99], v[72:75]\n\t"
        "ds_read_b64_tr_b16 v[118:119], v123 offset:3232\n\t"
        "v_mfma_f32_16x16x32_bf16 v[68:71], v[164:167], v[100:103], v[68:71]\n\t"
        "ds_read_b64_tr_b16 v[96:97], v124 offset:128\n\t"
        "v_mfma_f32_16x16x32_bf16 v[76:79], v[168:171], v[100:103], v[76:79]\n\t"
        "ds_read_b64_tr_b16 v[98:99], v124 offset:3200\n\t"
        "s_waitcnt lgkmcnt(6)\n\t"
        "v_mfma_f32_16x16x32_bf16 v[80:83], v[164:167], v[104:107], v[80:83]\n\t"
        "ds_read_b64_tr_b16 v[100:101], v124 offset:160\n\t"
        "v_mfma_f32_16x16x32_bf16 v[88:91], v[168:171], v[104:107], v[88:91]\n\t"
        "ds_read_b64_tr_b16 v[102:103], v124 offset:3232\n\t"
        "v_mfma_f32_16x16x32_bf16 v[84:87], v[164:167], v[108:111], v[84:87]\n\t"
        "ds_read_b64_tr_b16 v[104:105], v123 offset:0\n\t"
        "ds_read_b32 v126, v125\n\t"
        "v_mfma_f32_16x16x32_bf16 v[92:95], v[168:171], v[108:111], v[92:95]\n\t"
        "ds_read_b64_tr_b16 v[106:107], v123 offset:3072\n\t"
        "s_waitcnt lgkmcnt(7)\n\t"
        "v_mfma_f32_16x16x32_bf16 v[32:35], v[112:115], v[164:167], v[32:35]\n\t"
        "ds_read_b64_tr_b16 v[108:109], v123 offset:32\n\t"
        "v_add_u32 v129, s46, v121\n\t"
        "v_mfma_f32_16x16x32_bf16 v[40:43], v[112:115], v[168:171], v[40:43]\n\t"
        "ds_read_b64_tr_b16 v[110:111], v123 offset:3104\n\t"
        "v_add_u32 v130, s46, v122\n\t"
        "v_mfma_f32_16x16x32_bf16 v[36:39], v[116:119], v[164:167], v[36:39]\n\t"
        "ds_read_b64_tr_b16 v[112:113], v124 offset:0\n\t"
        "buffer_load_dwordx4 v[156:159], v131, s[40:43], 0 offen offset:2048 sc1\n\t"
        "v_mfma_f32_16x16x32_bf16 v[44:47], v[116:119], v[168:171], v[44:47]\n\t"
        "ds_read_b64_tr_b16 v[114:115], v124 offset:3072\n\t"
        "s_waitcnt lgkmcnt(5)\n\t"
        "v_mfma_f32_16x16x32_bf16 v[48:51], v[96:99], v[164:167], v[48:51]\n\t"
        "ds_read_b64_tr_b16 v[116:117], v124 offset:32\n\t"
        "v_cmp_gt_u32 vcc, v128, v126\n\t"
        "v_mfma_f32_16x16x32_bf16 v[56:59], v[96:99], v[168:171], v[56:59]\n\t"
        "ds_read_b64_tr_b16 v[118:119], v124 offset:3104\n\t"
        "s_cbranch_vccnz 20f\n\t"
        "21:\n\t"
        "v_mfma_f32_16x16x32_bf16 v[52:55], v[100:103], v[164:167], v[52:55]\n\t"
        "ds_read_b64_tr_b16 v[96:97], v129 offset:256\n\t"
        "s_addk_i32 s46, 12352\n\t"
        "v_mfma_f32_16x16x32_bf16 v[60:63], v[100:103], v[168:171], v[60:63]\n\t"
        "ds_read_b64_tr_b16 v[98:99], v129 offset:3328\n\t"
        "s_waitcnt lgkmcnt(6)\n\t"
        "v_mfma_f32_16x16x32_bf16 v[0:3], v[104:107], v[164:167], v[0:3]\n\t"
        "ds_read_b64_tr_b16 v[100:101], v129 offset:288\n\t"
        "s_cmp_eq_u32 s46, s47\n\t"
        "v_mfma_f32_16x16x32_bf16 v[8:11], v[104:107], v[168:171], v[8:11]\n\t"
        "ds_read_b64_tr_b16 v[102:103], v129 offset:3360\n\t"
        "s_cselect_b32 s46, 0, s46\n\t"
        "v_mfma_f32_16x16x32_bf16 v[4:7], v[108:111], v[164:167], v[4:7]\n\t"
        "ds_read_b64_tr_b16 v[104:105], v130 offset:256\n\t"
        "buffer_load_dwordx4 v[160:163], v131, s[40:43], 0 offen offset:2304 sc1\n\t"
        "v_mfma_f32_16x16x32_bf16 v[12:15], v[108:111], v[168:171], v[12:15]\n\t"
        "ds_read_b64_tr_b16 v[106:107], v130 offset:3328\n\t"
        "s_waitcnt lgkmcnt(6)\n\t"
        "v_mfma_f32_16x16x32_bf16 v[16:19], v[112:115], v[164:167], v[16:19]\n\t"
        "ds_write_b32 v127, v128\n\t"
        "ds_read_b64_tr_b16 v[108:109], v130 offset:288\n\t"
        "v_mfma_f32_16x16x32_bf16 v[24:27], v[112:115], v[168:171], v[24:27]\n\t"
        "ds_read_b64_tr_b16 v[110:111], v130 offset:3360\n\t"
        "v_add_u32 v128, 1, v128\n\t"
        "v_mfma_f32_16x16x32_bf16 v[20:23], v[116:119], v[164:167], v[20:23]\n\t"
        "ds_read_b64_tr_b16 v[112:113], v129 offset:128\n\t"
        "v_mfma_f32_16x16x32_bf16 v[28:31], v[116:119], v[168:171], v[28:31]\n\t"
        "ds_read_b64_tr_b16 v[114:115], v129 offset:3200\n\t"
        "s_waitcnt vmcnt(12) lgkmcnt(7)\n\t"
        "v_mfma_f32_16x16x32_bf16 v[64:67], v[172:175], v[96:99], v[64:67]\n\t"
        "ds_read_b64_tr_b16 v[116:117], v129 offset:160\n\t"
        "s_addk_i32 s44, 0x1000\n\t"
        "v_mfma_f32_16x16x32_bf16 v[72:75], v[176:179], v[96:99], v[72:75]\n\t"
        "ds_read_b64_tr_b16 v[118:119], v129 offset:3232\n\t"
        "s_bitset0_b32 s44, 16\n\t"
        "v_mfma_f32_16x16x32_bf16 v[68:71], v[172:175], v[100:103], v[68:71]\n\t"
        "ds_read_b64_tr_b16 v[96:97], v130 offset:128\n\t"
        "s_cmp_eq_u32 s44, s49\n\t"
        "v_mfma_f32_16x16x32_bf16 v[76:79], v[176:179], v[100:103], v[76:79]\n\t"
        "ds_read_b64_tr_b16 v[98:99], v130 offset:3200\n\t"
        "s_waitcnt lgkmcnt(6)\n\t"
        "v_mfma_f32_16x16x32_bf16 v[80:83], v[172:175], v[104:107], v[80:83]\n\t"
        "ds_read_b64_tr_b16 v[100:101], v130 offset:160\n\t"
        "s_cselect_b32 s44, s50, s44\n\t"
        "v_mfma_f32_16x16x32_bf16 v[88:91], v[176:179], v[104:107], v[88:91]\n\t"
        "ds_read_b64_tr_b16 v[102:103], v130 offset:3232\n\t"
        "v_add_u32 v131, s44, v120\n\t"
        "v_mfma_f32_16x16x32_bf16 v[84:87], v[172:175], v[108:111], v[84:87]\n\t"
        "ds_read_b64_tr_b16 v[104:105], v129 offset:0\n\t"
        "ds_read_b32 v126, v125\n\t"
        "v_mfma_f32_16x16x32_bf16 v[92:95], v[176:179], v[108:111], v[92:95]\n\t"
        "ds_read_b64_tr_b16 v[106:107], v129 offset:3072\n\t"
        "s_waitcnt lgkmcnt(7)\n\t"
        "v_mfma_f32_16x16x32_bf16 v[32:35], v[112:115], v[172:175], v[32:35]\n\t"
        "ds_read_b64_tr_b16 v[108:109], v129 offset:32\n\t"
        "v_add_u32 v123, s46, v121\n\t"
        "v_mfma_f32_16x16x32_bf16 v[40:43], v[112:115], v[176:179], v[40:43]\n\t"
        "ds_read_b64_tr_b16 v[110:111], v129 offset:3104\n\t"
        "v_add_u32 v124, s46, v122\n\t"
        "v_mfma_f32_16x16x32_bf16 v[36:39], v[116:119], v[172:175], v[36:39]\n\t"
        "ds_read_b64_tr_b16 v[112:113], v130 offset:0\n\t"
        "buffer_load_dwordx4 v[164:167], v131, s[40:43], 0 offen offset:0 sc1\n\t"
        "v_mfma_f32_16x16x32_bf16 v[44:47], v[116:119], v[176:179], v[44:47]\n\t"
        "ds_read_b64_tr_b16 v[114:115], v130 offset:3072\n\t"
        "s_waitcnt lgkmcnt(5)\n\t"
        "v_mfma_f32_16x16x32_bf16 v[48:51], v[96:99], v[172:175], v[48:51]\n\t"
        "ds_read_b64_tr_b16 v[116:117], v130 offset:32\n\t"
        "v_cmp_gt_u32 vcc, v128, v126\n\t"
        "v_mfma_f32_16x16x32_bf16 v[56:59], v[96:99], v[176:179], v[56:59]\n\t"
        "ds_read_b64_tr_b16 v[118:119], v130 offset:3104\n\t"
        "s_cbranch_vccnz 22f\n\t"
        "23:\n\t"
        "v_mfma_f32_16x16x32_bf16 v[52:55], v[100:103], v[172:175], v[52:55]\n\t"
        "ds_read_b64_tr_b16 v[96:97], v123 offset:256\n\t"
        "s_addk_i32 s46, 12352\n\t"
        "v_mfma_f32_16x16x32_bf16 v[60:63], v[100:103], v[176:179], v[60:63]\n\t"
        "ds_read_b64_tr_b16 v[98:99], v123 offset:3328\n\t"
        "s_waitcnt lgkmcnt(6)\n\t"
        "v_mfma_f32_16x16x32_bf16 v[0:3], v[104:107], v[172:175], v[0:3]\n\t"
        "ds_read_b64_tr_b16 v[100:101], v123 offset:288\n\t"
        "s_cmp_eq_u32 s46, s47\n\t"
        "v_mfma_f32_16x16x32_bf16 v[8:11], v[104:107], v[176:179], v[8:11]\n\t"
        "ds_read_b64_tr_b16 v[102:103], v123 offset:3360\n\t"
        "s_cselect_b32 s46, 0, s46\n\t"
        "v_mfma_f32_16x16x32_bf16 v[4:7], v[108:111], v[172:175], v[4:7]\n\t"
        "ds_read_b64_tr_b16 v[104:105], v124 offset:256\n\t"
        "buffer_load_dwordx4 v[168:171], v131, s[40:43], 0 offen offset:256 sc1\n\t"
        "v_mfma_f32_16x16x32_bf16 v[12:15], v[108:111], v[176:179], v[12:15]\n\t"
        "ds_read_b64_tr_b16 v[106:107], v124 offset:3328\n\t"
        "s_waitcnt lgkmcnt(6)\n\t"
        "v_mfma_f32_16x16x32_bf16 v[16:19], v[112:115], v[172:175], v[16:19]\n\t"
        "ds_write_b32 v127, v128\n\t"
        "ds_read_b64_tr_b16 v[108:109], v124 offset:288\n\t"
        "v_mfma_f32_16x16x32_bf16 v[24:27], v[112:115], v[176:179], v[24:27]\n\t"
        "ds_read_b64_tr_b16 v[110:111], v124 offset:3360\n\t"
        "v_add_u32 v128, 1, v128\n\t"
        "v_mfma_f32_16x16x32_bf16 v[20:23], v[116:119], v[172:175], v[20:23]\n\t"
        "ds_read_b64_tr_b16 v[112:113], v123 offset:128\n\t"
        "v_mfma_f32_16x16x32_bf16 v[28:31], v[116:119], v[176:179], v[28:31]\n\t"
        "ds_read_b64_tr_b16 v[114:115], v123 offset:3200\n\t"
        "s_waitcnt vmcnt(12) lgkmcnt(7)\n\t"
        "v_mfma_f32_16x16x32_bf16 v[64:67], v[180:183], v[96:99], v[64:67]\n\t"
        "ds_read_b64_tr_b16 v[116:117], v123 offset:160\n\t"
        "v_mfma_f32_16x16x32_bf16 v[72:75], v[184:187], v[96:99], v[72:75]\n\t"
        "ds_read_b64_tr_b16 v[118:119], v123 offset:3232\n\t"
        "v_mfma_f32_16x16x32_bf16 v[68:71], v[180:183], v[100:103], v[68:71]\n\t"
        "ds_read_b64_tr_b16 v[96:97], v124 offset:128\n\t"
        "v_mfma_f32_16x16x32_bf16 v[76:79], v[184:187], v[100:103], v[76:79]\n\t"
        "ds_read_b64_tr_b16 v[98:99], v124 offset:3200\n\t"
        "s_waitcnt lgkmcnt(6)\n\t"
        "v_mfma_f32_16x16x32_bf16 v[80:83], v[180:183], v[104:107], v[80:83]\n\t"
        "ds_read_b64_tr_b16 v[100:101], v124 offset:160\n\t"
        "v_mfma_f32_16x16x32_bf16 v[88:91], v[184:187], v[104:107], v[88:91]\n\t"
        "ds_read_b64_tr_b16 v[102:103], v124 offset:3232\n\t"
        "v_mfma_f32_16x16x32_bf16 v[84:87], v[180:183], v[108:111], v[84:87]\n\t"
        "ds_read_b64_tr_b16 v[104:105], v123 offset:0\n\t"
        "ds_read_b32 v126, v125\n\t"
        "v_mfma_f32_16x16x32_bf16 v[92:95], v[184:187], v[108:111], v[92:95]\n\t"
        "ds_read_b64_tr_b16 v[106:107], v123 offset:3072\n\t"
        "s_waitcnt lgkmcnt(7)\n\t"
        "v_mfma_f32_16x16x32_bf16 v[32:35], v[112:115], v[180:183], v[32:35]\n\t"
        "ds_read_b64_tr_b16 v[108:109], v123 offset:32\n\t"
        "v_add_u32 v129, s46, v121\n\t"
        "v_mfma_f32_16x16x32_bf16 v[40:43], v[112:115], v[184:187], v[40:43]\n\t"
        "ds_read_b64_tr_b16 v[110:111], v123 offset:3104\n\t"
        "v_add_u32 v130, s46, v122\n\t"
        "v_mfma_f32_16x16x32_bf16 v[36:39], v[116:119], v[180:183], v[36:39]\n\t"
        "ds_read_b64_tr_b16 v[112:113], v124 offset:0\n\t"
        "buffer_load_dwordx4 v[172:175], v131, s[40:43], 0 offen offset:2048 sc1\n\t"
        "v_mfma_f32_16x16x32_bf16 v[44:47], v[116:119], v[184:187], v[44:47]\n\t"
        "ds_read_b64_tr_b16 v[114:115], v124 offset:3072\n\t"
        "s_waitcnt lgkmcnt(5)\n\t"
        "v_mfma_f32_16x16x32_bf16 v[48:51], v[96:99], v[180:183], v[48:51]\n\t"
        "ds_read_b64_tr_b16 v[116:117], v124 offset:32\n\t"
        "v_cmp_gt_u32 vcc, v128, v126\n\t"
        "v_mfma_f32_16x16x32_bf16 v[56:59], v[96:99], v[184:187], v[56:59]\n\t"
        "ds_read_b64_tr_b16 v[118:119], v124 offset:3104\n\t"
        "s_cbranch_vccnz 24f\n\t"
        "25:\n\t"
        "v_mfma_f32_16x16x32_bf16 v[52:55], v[100:103], v[180:183], v[52:55]\n\t"
        "ds_read_b64_tr_b16 v[96:97], v129 offset:256\n\t"
        "s_addk_i32 s46, 12352\n\t"
        "v_mfma_f32_16x16x32_bf16 v[60:63], v[100:103], v[184:187], v[60:63]\n\t"
        "ds_read_b64_tr_b16 v[98:99], v129 offset:3328\n\t"
        "s_waitcnt lgkmcnt(6)\n\t"
        "v_mfma_f32_16x16x32_bf16 v[0:3], v[104:107], v[180:183], v[0:3]\n\t"
        "ds_read_b64_tr_b16 v[100:101], v129 offset:288\n\t"
        "s_cmp_eq_u32 s46, s47\n\t"
        "v_mfma_f32_16x16x32_bf16 v[8:11], v[104:107], v[184:187], v[8:11]\n\t"
        "ds_read_b64_tr_b16 v[102:103], v129 offset:3360\n\t"
        "s_cselect_b32 s46, 0, s46\n\t"
        "v_mfma_f32_16x16x32_bf16 v[4:7], v[108:111], v[180:183], v[4:7]\n\t"
        "ds_read_b64_tr_b16 v[104:105], v130 offset:256\n\t"
        "buffer_load_dwordx4 v[176:179], v131, s[40:43], 0 offen offset:2304 sc1\n\t"
        "v_mfma_f32_16x16x32_bf16 v[12:15], v[108:111], v[184:187], v[12:15]\n\t"
        "ds_read_b64_tr_b16 v[106:107], v130 offset:3328\n\t"
        "s_waitcnt lgkmcnt(6)\n\t"
        "v_mfma_f32_16x16x32_bf16 v[16:19], v[112:115], v[180:183], v[16:19]\n\t"
        "ds_write_b32 v127, v128\n\t"
        "ds_read_b64_tr_b16 v[108:109], v130 offset:288\n\t"
        "v_mfma_f32_16x16x32_bf16 v[24:27], v[112:115], v[184:187], v[24:27]\n\t"
        "ds_read_b64_tr_b16 v[110:111], v130 offset:3360\n\t"
        "v_add_u32 v128, 1, v128\n\t"
        "v_mfma_f32_16x16x32_bf16 v[20:23], v[116:119], v[180:183], v[20:23]\n\t"
        "ds_read_b64_tr_b16 v[112:113], v129 offset:128\n\t"
        "v_mfma_f32_16x16x32_bf16 v[28:31], v[116:119], v[184:187], v[28:31]\n\t"
        "ds_read_b64_tr_b16 v[114:115], v129 offset:3200\n\t"
        "s_waitcnt vmcnt(12) lgkmcnt(7)\n\t"
        "v_mfma_f32_16x16x32_bf16 v[64:67], v[188:191], v[96:99], v[64:67]\n\t"
        "ds_read_b64_tr_b16 v[116:117], v129 offset:160\n\t"
        "s_addk_i32 s44, 0x1000\n\t"
        "v_mfma_f32_16x16x32_bf16 v[72:75], v[192:195], v[96:99], v[72:75]\n\t"
        "ds_read_b64_tr_b16 v[118:119], v129 offset:3232\n\t"
        "s_bitset0_b32 s44, 16\n\t"
        "v_mfma_f32_16x16x32_bf16 v[68:71], v[188:191], v[100:103], v[68:71]\n\t"
        "ds_read_b64_tr_b16 v[96:97], v130 offset:128\n\t"
        "s_cmp_eq_u32 s44, s49\n\t"
        "v_mfma_f32_16x16x32_bf16 v[76:79], v[192:195], v[100:103], v[76:79]\n\t"
        "ds_read_b64_tr_b16 v[98:99], v130 offset:3200\n\t"
        "s_waitcnt lgkmcnt(6)\n\t"
        "v_mfma_f32_16x16x32_bf16 v[80:83], v[188:191], v[104:107], v[80:83]\n\t"
        "ds_read_b64_tr_b16 v[100:101], v130 offset:160\n\t"
        "s_cselect_b32 s44, s50, s44\n\t"
        "v_mfma_f32_16x16x32_bf16 v[88:91], v[192:195], v[104:107], v[88:91]\n\t"
        "ds_read_b64_tr_b16 v[102:103], v130 offset:3232\n\t"
        "v_add_u32 v131, s44, v120\n\t"
        "v_mfma_f32_16x16x32_bf16 v[84:87], v[188:191], v[108:111], v[84:87]\n\t"
        "ds_read_b64_tr_b16 v[104:105], v129 offset:0\n\t"
        "ds_read_b32 v126, v125\n\t"
        "v_mfma_f32_16x16x32_bf16 v[92:95], v[192:195], v[108:111], v[92:95]\n\t"
        "ds_read_b64_tr_b16 v[106:107], v129 offset:3072\n\t"
        "s_waitcnt lgkmcnt(7)\n\t"
        "v_mfma_f32_16x16x32_bf16 v[32:35], v[112:115], v[188:191], v[32:35]\n\t"
        "ds_read_b64_tr_b16 v[108:109], v129 offset:32\n\t"
        "v_add_u32 v123, s46, v121\n\t"
        "v_mfma_f32_16x16x32_bf16 v[40:43], v[112:115], v[192:195], v[40:43]\n\t"
        "ds_read_b64_tr_b16 v[110:111], v129 offset:3104\n\t"
        "v_add_u32 v124, s46, v122\n\t"
        "v_mfma_f32_16x16x32_bf16 v[36:39], v[116:119], v[188:191], v[36:39]\n\t"
        "ds_read_b64_tr_b16 v[112:113], v130 offset:0\n\t"
        "buffer_load_dwordx4 v[180:183], v131, s[40:43], 0 offen offset:0 sc1\n\t"
        "v_mfma_f32_16x16x32_bf16 v[44:47], v[116:119], v[192:195], v[44:47]\n\t"
        "ds_read_b64_tr_b16 v[114:115], v130 offset:3072\n\t"
        "s_waitcnt lgkmcnt(5)\n\t"
        "v_mfma_f32_16x16x32_bf16 v[48:51], v[96:99], v[188:191], v[48:51]\n\t"
        "ds_read_b64_tr_b16 v[116:117], v130 offset:32\n\t"
        "v_cmp_gt_u32 vcc, v128, v126\n\t"
        "v_mfma_f32_16x16x32_bf16 v[56:59], v[96:99], v[192:195], v[56:59]\n\t"
        "ds_read_b64_tr_b16 v[118:119], v130 offset:3104\n\t"
        "s_cbranch_vccnz 26f\n\t"
        "27:\n\t"
        "v_mfma_f32_16x16x32_bf16 v[52:55], v[100:103], v[188:191], v[52:55]\n\t"
        "ds_read_b64_tr_b16 v[96:97], v123 offset:256\n\t"
        "s_addk_i32 s46, 12352\n\t"
        "v_mfma_f32_16x16x32_bf16 v[60:63], v[100:103], v[192:195], v[60:63]\n\t"
        "ds_read_b64_tr_b16 v[98:99], v123 offset:3328\n\t"
        "s_waitcnt lgkmcnt(6)\n\t"
        "v_mfma_f32_16x16x32_bf16 v[0:3], v[104:107], v[188:191], v[0:3]\n\t"
        "ds_read_b64_tr_b16 v[100:101], v123 offset:288\n\t"
        "s_cmp_eq_u32 s46, s47\n\t"
        "v_mfma_f32_16x16x32_bf16 v[8:11], v[104:107], v[192:195], v[8:11]\n\t"
        "ds_read_b64_tr_b16 v[102:103], v123 offset:3360\n\t"
        "s_cselect_b32 s46, 0, s46\n\t"
        "v_mfma_f32_16x16x32_bf16 v[4:7], v[108:111], v[188:191], v[4:7]\n\t"
        "ds_read_b64_tr_b16 v[104:105], v124 offset:256\n\t"
        "buffer_load_dwordx4 v[184:187], v131, s[40:43], 0 offen offset:256 sc1\n\t"
        "v_mfma_f32_16x16x32_bf16 v[12:15], v[108:111], v[192:195], v[12:15]\n\t"
        "ds_read_b64_tr_b16 v[106:107], v124 offset:3328\n\t"
        "s_waitcnt lgkmcnt(6)\n\t"
        "v_mfma_f32_16x16x32_bf16 v[16:19], v[112:115], v[188:191], v[16:19]\n\t"
        "ds_write_b32 v127, v128\n\t"
        "ds_read_b64_tr_b16 v[108:109], v124 offset:288\n\t"
        "v_mfma_f32_16x16x32_bf16 v[24:27], v[112:115], v[192:195], v[24:27]\n\t"
        "ds_read_b64_tr_b16 v[110:111], v124 offset:3360\n\t"
        "v_add_u32 v128, 1, v128\n\t"
        "v_mfma_f32_16x16x32_bf16 v[20:23], v[116:119], v[188:191], v[20:23]\n\t"
        "ds_read_b64_tr_b16 v[112:113], v123 offset:128\n\t"
        "v_mfma_f32_16x16x32_bf16 v[28:31], v[116:119], v[192:195], v[28:31]\n\t"
        "ds_read_b64_tr_b16 v[114:115], v123 offset:3200\n\t"
        "s_waitcnt vmcnt(12) lgkmcnt(7)\n\t"
        "v_mfma_f32_16x16x32_bf16 v[64:67], v[132:135], v[96:99], v[64:67]\n\t"
        "ds_read_b64_tr_b16 v[116:117], v123 offset:160\n\t"
        "v_mfma_f32_16x16x32_bf16 v[72:75], v[136:139], v[96:99], v[72:75]\n\t"
        "ds_read_b64_tr_b16 v[118:119], v123 offset:3232\n\t"
        "v_mfma_f32_16x16x32_bf16 v[68:71], v[132:135], v[100:103], v[68:71]\n\t"
        "ds_read_b64_tr_b16 v[96:97], v124 offset:128\n\t"
        "v_mfma_f32_16x16x32_bf16 v[76:79], v[136:139], v[100:103], v[76:79]\n\t"
        "ds_read_b64_tr_b16 v[98:99], v124 offset:3200\n\t"
        "s_waitcnt lgkmcnt(6)\n\t"
        "v_mfma_f32_16x16x32_bf16 v[80:83], v[132:135], v[104:107], v[80:83]\n\t"
        "ds_read_b64_tr_b16 v[100:101], v124 offset:160\n\t"
        "v_mfma_f32_16x16x32_bf16 v[88:91], v[136:139], v[104:107], v[88:91]\n\t"
        "ds_read_b64_tr_b16 v[102:103], v124 offset:3232\n\t"
        "v_mfma_f32_16x16x32_bf16 v[84:87], v[132:135], v[108:111], v[84:87]\n\t"
        "ds_read_b64_tr_b16 v[104:105], v123 offset:0\n\t"
        "ds_read_b32 v126, v125\n\t"
        "v_mfma_f32_16x16x32_bf16 v[92:95], v[136:139], v[108:111], v[92:95]\n\t"
        "ds_read_b64_tr_b16 v[106:107], v123 offset:3072\n\t"
        "s_waitcnt lgkmcnt(7)\n\t"
        "v_mfma_f32_16x16x32_bf16 v[32:35], v[112:115], v[132:135], v[32:35]\n\t"
        "ds_read_b64_tr_b16 v[108:109], v123 offset:32\n\t"
        "v_add_u32 v129, s46, v121\n\t"
        "v_mfma_f32_16x16x32_bf16 v[40:43], v[112:115], v[136:139], v[40:43]\n\t"
        "ds_read_b64_tr_b16 v[110:111], v123 offset:3104\n\t"
        "v_add_u32 v130, s46, v122\n\t"
        "v_mfma_f32_16x16x32_bf16 v[36:39], v[116:119], v[132:135], v[36:39]\n\t"
        "ds_read_b64_tr_b16 v[112:113], v124 offset:0\n\t"
        "buffer_load_dwordx4 v[188:191], v131, s[40:43], 0 offen offset:2048 sc1\n\t"
        "v_mfma_f32_16x16x32_bf16 v[44:47], v[116:119], v[136:139], v[44:47]\n\t"
        "ds_read_b64_tr_b16 v[114:115], v124 offset:3072\n\t"
        "s_waitcnt lgkmcnt(5)\n\t"
        "v_mfma_f32_16x16x32_bf16 v[48:51], v[96:99], v[132:135], v[48:51]\n\t"
        "ds_read_b64_tr_b16 v[116:117], v124 offset:32\n\t"
        "v_cmp_gt_u32 vcc, v128, v126\n\t"
        "v_mfma_f32_16x16x32_bf16 v[56:59], v[96:99], v[136:139], v[56:59]\n\t"
        "ds_read_b64_tr_b16 v[118:119], v124 offset:3104\n\t"
        "s_cbranch_vccnz 28f\n\t"
        "29:\n\t"
        "v_mfma_f32_16x16x32_bf16 v[52:55], v[100:103], v[132:135], v[52:55]\n\t"
        "ds_read_b64_tr_b16 v[96:97], v129 offset:256\n\t"
        "s_addk_i32 s46, 12352\n\t"
        "v_mfma_f32_16x16x32_bf16 v[60:63], v[100:103], v[136:139], v[60:63]\n\t"
        "ds_read_b64_tr_b16 v[98:99], v129 offset:3328\n\t"
        "s_waitcnt lgkmcnt(6)\n\t"
        "v_mfma_f32_16x16x32_bf16 v[0:3], v[104:107], v[132:135], v[0:3]\n\t"
        "ds_read_b64_tr_b16 v[100:101], v129 offset:288\n\t"
        "s_cmp_eq_u32 s46, s47\n\t"
        "v_mfma_f32_16x16x32_bf16 v[8:11], v[104:107], v[136:139], v[8:11]\n\t"
        "ds_read_b64_tr_b16 v[102:103], v129 offset:3360\n\t"
        "s_cselect_b32 s46, 0, s46\n\t"
        "v_mfma_f32_16x16x32_bf16 v[4:7], v[108:111], v[132:135], v[4:7]\n\t"
        "ds_read_b64_tr_b16 v[104:105], v130 offset:256\n\t"
        "buffer_load_dwordx4 v[192:195], v131, s[40:43], 0 offen offset:2304 sc1\n\t"
        "v_mfma_f32_16x16x32_bf16 v[12:15], v[108:111], v[136:139], v[12:15]\n\t"
        "ds_read_b64_tr_b16 v[106:107], v130 offset:3328\n\t"
        "s_waitcnt lgkmcnt(6)\n\t"
        "v_mfma_f32_16x16x32_bf16 v[16:19], v[112:115], v[132:135], v[16:19]\n\t"
        "ds_write_b32 v127, v128\n\t"
        "ds_read_b64_tr_b16 v[108:109], v130 offset:288\n\t"
        "v_mfma_f32_16x16x32_bf16 v[24:27], v[112:115], v[136:139], v[24:27]\n\t"
        "ds_read_b64_tr_b16 v[110:111], v130 offset:3360\n\t"
        "v_add_u32 v128, 1, v128\n\t"
        "v_mfma_f32_16x16x32_bf16 v[20:23], v[116:119], v[132:135], v[20:23]\n\t"
        "ds_read_b64_tr_b16 v[112:113], v129 offset:128\n\t"
        "v_mfma_f32_16x16x32_bf16 v[28:31], v[116:119], v[136:139], v[28:31]\n\t"
        "ds_read_b64_tr_b16 v[114:115], v129 offset:3200\n\t"
        "s_waitcnt vmcnt(12) lgkmcnt(7)\n\t"
        "v_mfma_f32_16x16x32_bf16 v[64:67], v[140:143], v[96:99], v[64:67]\n\t"
        "ds_read_b64_tr_b16 v[116:117], v129 offset:160\n\t"
        "s_addk_i32 s44, 0x1000\n\t"
        "v_mfma_f32_16x16x32_bf16 v[72:75], v[144:147], v[96:99], v[72:75]\n\t"
        "ds_read_b64_tr_b16 v[118:119], v129 offset:3232\n\t"
        "s_bitset0_b32 s44, 16\n\t"
        "v_mfma_f32_16x16x32_bf16 v[68:71], v[140:143], v[100:103], v[68:71]\n\t"
        "ds_read_b64_tr_b16 v[96:97], v130 offset:128\n\t"
        "s_cmp_eq_u32 s44, s49\n\t"
        "v_mfma_f32_16x16x32_bf16 v[76:79], v[144:147], v[100:103], v[76:79]\n\t"
        "ds_read_b64_tr_b16 v[98:99], v130 offset:3200\n\t"
        "s_waitcnt lgkmcnt(6)\n\t"
        "v_mfma_f32_16x16x32_bf16 v[80:83], v[140:143], v[104:107], v[80:83]\n\t"
        "ds_read_b64_tr_b16 v[100:101], v130 offset:160\n\t"
        "s_cselect_b32 s44, s50, s44\n\t"
        "v_mfma_f32_16x16x32_bf16 v[88:91], v[144:147], v[104:107], v[88:91]\n\t"
        "ds_read_b64_tr_b16 v[102:103], v130 offset:3232\n\t"
        "v_add_u32 v131, s44, v120\n\t"
        "v_mfma_f32_16x16x32_bf16 v[84:87], v[140:143], v[108:111], v[84:87]\n\t"
        "ds_read_b64_tr_b16 v[104:105], v129 offset:0\n\t"
        "ds_read_b32 v126, v125\n\t"
        "v_mfma_f32_16x16x32_bf16 v[92:95], v[144:147], v[108:111], v[92:95]\n\t"
        "ds_read_b64_tr_b16 v[106:107], v129 offset:3072\n\t"
        "s_waitcnt lgkmcnt(7)\n\t"
        "v_mfma_f32_16x16x32_bf16 v[32:35], v[112:115], v[140:143], v[32:35]\n\t"
        "ds_read_b64_tr_b16 v[108:109], v129 offset:32\n\t"
        "v_add_u32 v123, s46, v121\n\t"
        "v_mfma_f32_16x16x32_bf16 v[40:43], v[112:115], v[144:147], v[40:43]\n\t"
        "ds_read_b64_tr_b16 v[110:111], v129 offset:3104\n\t"
        "v_add_u32 v124, s46, v122\n\t"
        "v_mfma_f32_16x16x32_bf16 v[36:39], v[116:119], v[140:143], v[36:39]\n\t"
        "ds_read_b64_tr_b16 v[112:113], v130 offset:0\n\t"
        "buffer_load_dwordx4 v[132:135], v131, s[40:43], 0 offen offset:0 sc1\n\t"
        "v_mfma_f32_16x16x32_bf16 v[44:47], v[116:119], v[144:147], v[44:47]\n\t"
        "ds_read_b64_tr_b16 v[114:115], v130 offset:3072\n\t"
        "s_waitcnt lgkmcnt(5)\n\t"
        "v_mfma_f32_16x16x32_bf16 v[48:51], v[96:99], v[140:143], v[48:51]\n\t"
        "ds_read_b64_tr_b16 v[116:117], v130 offset:32\n\t"
        "v_cmp_gt_u32 vcc, v128, v126\n\t"
        "v_mfma_f32_16x16x32_bf16 v[56:59], v[96:99], v[144:147], v[56:59]\n\t"
        "ds_read_b64_tr_b16 v[118:119], v130 offset:3104\n\t"
        "s_cbranch_vccnz 30f\n\t"
        "31:\n\t"
        "v_mfma_f32_16x16x32_bf16 v[52:55], v[100:103], v[140:143], v[52:55]\n\t"
        "ds_read_b64_tr_b16 v[96:97], v123 offset:256\n\t"
        "s_addk_i32 s46, 12352\n\t"
        "v_mfma_f32_16x16x32_bf16 v[60:63], v[100:103], v[144:147], v[60:63]\n\t"
        "ds_read_b64_tr_b16 v[98:99], v123 offset:3328\n\t"
        "s_waitcnt lgkmcnt(6)\n\t"
        "v_mfma_f32_16x16x32_bf16 v[0:3], v[104:107], v[140:143], v[0:3]\n\t"
        "ds_read_b64_tr_b16 v[100:101], v123 offset:288\n\t"
        "s_cmp_eq_u32 s46, s47\n\t"
        "v_mfma_f32_16x16x32_bf16 v[8:11], v[104:107], v[144:147], v[8:11]\n\t"
        "ds_read_b64_tr_b16 v[102:103], v123 offset:3360\n\t"
        "s_cselect_b32 s46, 0, s46\n\t"
        "v_mfma_f32_16x16x32_bf16 v[4:7], v[108:111], v[140:143], v[4:7]\n\t"
        "ds_read_b64_tr_b16 v[104:105], v124 offset:256\n\t"
        "buffer_load_dwordx4 v[136:139], v131, s[40:43], 0 offen offset:256 sc1\n\t"
        "v_mfma_f32_16x16x32_bf16 v[12:15], v[108:111], v[144:147], v[12:15]\n\t"
        "ds_read_b64_tr_b16 v[106:107], v124 offset:3328\n\t"
        "s_waitcnt lgkmcnt(6)\n\t"
        "v_mfma_f32_16x16x32_bf16 v[16:19], v[112:115], v[140:143], v[16:19]\n\t"
        "ds_write_b32 v127, v128\n\t"
        "ds_read_b64_tr_b16 v[108:109], v124 offset:288\n\t"
        "v_mfma_f32_16x16x32_bf16 v[24:27], v[112:115], v[144:147], v[24:27]\n\t"
        "ds_read_b64_tr_b16 v[110:111], v124 offset:3360\n\t"
        "v_add_u32 v128, 1, v128\n\t"
        "v_mfma_f32_16x16x32_bf16 v[20:23], v[116:119], v[140:143], v[20:23]\n\t"
        "ds_read_b64_tr_b16 v[112:113], v123 offset:128\n\t"
        "s_sub_u32 s45, s45, 1\n\t"
        "v_mfma_f32_16x16x32_bf16 v[28:31], v[116:119], v[144:147], v[28:31]\n\t"
        "ds_read_b64_tr_b16 v[114:115], v123 offset:3200\n\t"
        "s_cbranch_scc0 1b\n\t"
        "ds_read_b32 v126, v125\n\t"
        "v_add_u32 v128, 4, v128\n\t"
        "s_waitcnt vmcnt(12) lgkmcnt(0)\n\t"
        "v_cmp_gt_u32 vcc, v128, v126\n\t"
        "v_mfma_f32_16x16x32_bf16 v[64:67], v[148:151], v[96:99], v[64:67]\n\t"
        "s_cbranch_vccnz 64f\n\t"
        "65:\n\t"
        "ds_read_b64_tr_b16 v[116:117], v121 offset:256\n\t"
        "v_mfma_f32_16x16x32_bf16 v[72:75], v[152:155], v[96:99], v[72:75]\n\t"
        "ds_read_b64_tr_b16 v[118:119], v121 offset:3328\n\t"
        "v_mfma_f32_16x16x32_bf16 v[68:71], v[148:151], v[100:103], v[68:71]\n\t"
        "ds_read_b64_tr_b16 v[96:97], v121 offset:288\n\t"
        "v_mfma_f32_16x16x32_bf16 v[76:79], v[152:155], v[100:103], v[76:79]\n\t"
        "ds_read_b64_tr_b16 v[98:99], v121 offset:3360\n\t"
        "s_waitcnt lgkmcnt(7)\n\t"
        "v_mfma_f32_16x16x32_bf16 v[80:83], v[148:151], v[104:107], v[80:83]\n\t"
        "ds_read_b64_tr_b16 v[100:101], v122 offset:256\n\t"
        "v_mfma_f32_16x16x32_bf16 v[88:91], v[152:155], v[104:107], v[88:91]\n\t"
        "ds_read_b64_tr_b16 v[102:103], v122 offset:3328\n\t"
        "v_mfma_f32_16x16x32_bf16 v[84:87], v[148:151], v[108:111], v[84:87]\n\t"
        "ds_read_b64_tr_b16 v[104:105], v122 offset:288\n\t"
        "v_mfma_f32_16x16x32_bf16 v[92:95], v[152:155], v[108:111], v[92:95]\n\t"
        "ds_read_b64_tr_b16 v[106:107], v122 offset:3360\n\t"
        "s_waitcnt vmcnt(10) lgkmcnt(6)\n\t"
        "v_mfma_f32_16x16x32_bf16 v[32:35], v[112:115], v[148:151], v[32:35]\n\t"
        "ds_read_b64_tr_b16 v[108:109], v121 offset:12608\n\t"
        "v_mfma_f32_16x16x32_bf16 v[40:43], v[112:115], v[152:155], v[40:43]\n\t"
        "ds_read_b64_tr_b16 v[110:111], v121 offset:15680\n\t"
        "v_mfma_f32_16x16x32_bf16 v[64:67], v[156:159], v[116:119], v[64:67]\n\t"
        "ds_read_b64_tr_b16 v[112:113], v121 offset:12640\n\t"
        "v_mfma_f32_16x16x32_bf16 v[72:75], v[160:163], v[116:119], v[72:75]\n\t"
        "ds_read_b64_tr_b16 v[114:115], v121 offset:15712\n\t"
        "s_waitcnt lgkmcnt(6)\n\t"
        "v_mfma_f32_16x16x32_bf16 v[68:71], v[156:159], v[96:99], v[68:71]\n\t"
        "ds_read_b64_tr_b16 v[116:117], v122 offset:12608\n\t"
        "ds_read_b128 v[196:199], v215 offset:0\n\t"
        "v_mfma_f32_16x16x32_bf16 v[76:79], v[160:163], v[96:99], v[76:79]\n\t"
        "ds_read_b64_tr_b16 v[118:119], v122 offset:15680\n\t"
        "v_mfma_f32_16x16x32_bf16 v[80:83], v[156:159], v[100:103], v[80:83]\n\t"
        "ds_read_b64_tr_b16 v[96:97], v122 offset:12640\n\t"
        "v_mfma_f32_16x16x32_bf16 v[88:91], v[160:163], v[100:103], v[88:91]\n\t"
        "ds_read_b64_tr_b16 v[98:99], v122 offset:15712\n\t"
        "s_waitcnt vmcnt(8) lgkmcnt(7)\n\t"
        "v_mfma_f32_16x16x32_bf16 v[84:87], v[156:159], v[104:107], v[84:87]\n\t"
        "ds_read_b64_tr_b16 v[100:101], v121 offset:24960\n\t"
        "ds_read_b128 v[200:203], v215 offset:64\n\t"
        "v_mfma_f32_16x16x32_bf16 v[92:95], v[160:163], v[104:107], v[92:95]\n\t"
        "ds_read_b64_tr_b16 v[102:103], v121 offset:28032\n\t"
        "v_mfma_f32_16x16x32_bf16 v[64:67], v[164:167], v[108:111], v[64:67]\n\t"
        "ds_read_b64_tr_b16 v[104:105], v121 offset:24992\n\t"
        "v_mfma_f32_16x16x32_bf16 v[72:75], v[168:171], v[108:111], v[72:75]\n\t"
        "ds_read_b64_tr_b16 v[106:107], v121 offset:28064\n\t"
        "s_waitcnt lgkmcnt(7)\n\t"
        "v_mfma_f32_16x16x32_bf16 v[68:71], v[164:167], v[112:115], v[68:71]\n\t"
        "ds_read_b64_tr_b16 v[108:109], v122 offset:24960\n\t"
        "v_mfma_f32_16x16x32_bf16 v[76:79], v[168:171], v[112:115], v[76:79]\n\t"
        "ds_read_b64_tr_b16 v[110:111], v122 offset:28032\n\t"
        "v_mfma_f32_16x16x32_bf16 v[80:83], v[164:167], v[116:119], v[80:83]\n\t"
        "ds_read_b64_tr_b16 v[112:113], v122 offset:24992\n\t"
        "v_mfma_f32_16x16x32_bf16 v[88:91], v[168:171], v[116:119], v[88:91]\n\t"
        "ds_read_b64_tr_b16 v[114:115], v122 offset:28064\n\t"
        "s_waitcnt vmcnt(6) lgkmcnt(6)\n\t"
        "v_mfma_f32_16x16x32_bf16 v[84:87], v[164:167], v[96:99], v[84:87]\n\t"
        "ds_read_b64_tr_b16 v[116:117], v121 offset:37312\n\t"
        "v_mfma_f32_16x16x32_bf16 v[92:95], v[168:171], v[96:99], v[92:95]\n\t"
        "ds_read_b64_tr_b16 v[118:119], v121 offset:40384\n\t"
        "v_mfma_f32_16x16x32_bf16 v[64:67], v[172:175], v[100:103], v[64:67]\n\t"
        "ds_read_b64_tr_b16 v[96:97], v121 offset:37344\n\t"
        "v_mfma_f32_16x16x32_bf16 v[72:75], v[176:179], v[100:103], v[72:75]\n\t"
        "ds_read_b64_tr_b16 v[98:99], v121 offset:40416\n\t"
        "s_waitcnt lgkmcnt(6)\n\t"
        "v_mfma_f32_16x16x32_bf16 v[68:71], v[172:175], v[104:107], v[68:71]\n\t"
        "ds_read_b64_tr_b16 v[100:101], v122 offset:37312\n\t"
        "v_mfma_f32_16x16x32_bf16 v[76:79], v[176:179], v[104:107], v[76:79]\n\t"
        "ds_read_b64_tr_b16 v[102:103], v122 offset:40384\n\t"
        "v_mfma_f32_16x16x32_bf16 v[80:83], v[172:175], v[108:111], v[80:83]\n\t"
        "ds_read_b64_tr_b16 v[104:105], v122 offset:37344\n\t"
        "v_mfma_f32_16x16x32_bf16 v[88:91], v[176:179], v[108:111], v[88:91]\n\t"
        "ds_read_b64_tr_b16 v[106:107], v122 offset:40416\n\t"
        "s_waitcnt vmcnt(4) lgkmcnt(6)\n\t"
        "v_mfma_f32_16x16x32_bf16 v[84:87], v[172:175], v[112:115], v[84:87]\n\t"
        "ds_read_b64_tr_b16 v[108:109], v121 offset:49664\n\t"
        "v_mfma_f32_16x16x32_bf16 v[92:95], v[176:179], v[112:115], v[92:95]\n\t"
        "ds_read_b64_tr_b16 v[110:111], v121 offset:52736\n\t"
        "v_mfma_f32_16x16x32_bf16 v[64:67], v[180:183], v[116:119], v[64:67]\n\t"
        "ds_read_b64_tr_b16 v[112:113], v121 offset:49696\n\t"
        "v_mfma_f32_16x16x32_bf16 v[72:75], v[184:187], v[116:119], v[72:75]\n\t"
        "ds_read_b64_tr_b16 v[114:115], v121 offset:52768\n\t"
        "s_waitcnt lgkmcnt(6)\n\t"
        "v_mfma_f32_16x16x32_bf16 v[68:71], v[180:183], v[96:99], v[68:71]\n\t"
        "ds_read_b64_tr_b16 v[116:117], v122 offset:49664\n\t"
        "v_mfma_f32_16x16x32_bf16 v[76:79], v[184:187], v[96:99], v[76:79]\n\t"
        "ds_read_b64_tr_b16 v[118:119], v122 offset:52736\n\t"
        "v_mfma_f32_16x16x32_bf16 v[80:83], v[180:183], v[100:103], v[80:83]\n\t"
        "ds_read_b64_tr_b16 v[96:97], v122 offset:49696\n\t"
        "v_mfma_f32_16x16x32_bf16 v[88:91], v[184:187], v[100:103], v[88:91]\n\t"
        "ds_read_b64_tr_b16 v[98:99], v122 offset:52768\n\t"
        "s_waitcnt vmcnt(0) lgkmcnt(6)\n\t"
        "v_mfma_f32_16x16x32_bf16 v[84:87], v[180:183], v[104:107], v[84:87]\n\t"
        "ds_read_b64_tr_b16 v[100:101], v123 offset:160\n\t"
        "v_mfma_f32_16x16x32_bf16 v[92:95], v[184:187], v[104:107], v[92:95]\n\t"
        "ds_read_b64_tr_b16 v[102:103], v123 offset:3232\n\t"
        "v_mfma_f32_16x16x32_bf16 v[64:67], v[188:191], v[108:111], v[64:67]\n\t"
        "ds_read_b64_tr_b16 v[104:105], v124 offset:128\n\t"
        "v_mfma_f32_16x16x32_bf16 v[72:75], v[192:195], v[108:111], v[72:75]\n\t"
        "ds_read_b64_tr_b16 v[106:107], v124 offset:3200\n\t"
        "s_waitcnt lgkmcnt(6)\n\t"
        "v_mfma_f32_16x16x32_bf16 v[68:71], v[188:191], v[112:115], v[68:71]\n\t"
        "ds_read_b64_tr_b16 v[108:109], v124 offset:160\n\t"
        "v_mfma_f32_16x16x32_bf16 v[76:79], v[192:195], v[112:115], v[76:79]\n\t"
        "ds_read_b64_tr_b16 v[110:111], v124 offset:3232\n\t"
        "v_mfma_f32_16x16x32_bf16 v[80:83], v[188:191], v[116:119], v[80:83]\n\t"
        "ds_read_b64_tr_b16 v[112:113], v121 offset:128\n\t"
        "v_mfma_f32_16x16x32_bf16 v[88:91], v[192:195], v[116:119], v[88:91]\n\t"
        "ds_read_b64_tr_b16 v[114:115], v121 offset:3200\n\t"
        "s_waitcnt lgkmcnt(6)\n\t"
        "v_mfma_f32_16x16x32_bf16 v[84:87], v[188:191], v[96:99], v[84:87]\n\t"
        "ds_read_b64_tr_b16 v[116:117], v121 offset:160\n\t"
        "v_mfma_f32_16x16x32_bf16 v[92:95], v[192:195], v[96:99], v[92:95]\n\t"
        "ds_read_b64_tr_b16 v[118:119], v121 offset:3232\n\t"
        "v_mfma_f32_16x16x32_bf16 v[36:39], v[100:103], v[148:151], v[36:39]\n\t"
        "ds_read_b64_tr_b16 v[96:97], v122 offset:128\n\t"
        "v_mfma_f32_16x16x32_bf16 v[44:47], v[100:103], v[152:155], v[44:47]\n\t"
        "ds_read_b64_tr_b16 v[98:99], v122 offset:3200\n\t"
        "s_waitcnt lgkmcnt(6)\n\t"
        "v_mfma_f32_16x16x32_bf16 v[48:51], v[104:107], v[148:151], v[48:51]\n\t"
        "ds_read_b64_tr_b16 v[100:101], v122 offset:160\n\t"
        "v_mul_f32 v64, v196, v64\n\t"
        "v_mfma_f32_16x16x32_bf16 v[56:59], v[104:107], v[152:155], v[56:59]\n\t"
        "ds_read_b64_tr_b16 v[102:103], v122 offset:3232\n\t"
        "v_mul_f32 v65, v197, v65\n\t"
        "v_mfma_f32_16x16x32_bf16 v[52:55], v[108:111], v[148:151], v[52:55]\n\t"
        "ds_read_b64_tr_b16 v[104:105], v121 offset:12480\n\t"
        "v_mul_f32 v66, v198, v66\n\t"
        "v_mul_f32 v67, v199, v67\n\t"
        "v_mfma_f32_16x16x32_bf16 v[60:63], v[108:111], v[152:155], v[60:63]\n\t"
        "ds_read_b64_tr_b16 v[106:107], v121 offset:15552\n\t"
        "v_mul_f32 v68, v196, v68\n\t"
        "s_waitcnt lgkmcnt(6)\n\t"
        "v_mfma_f32_16x16x32_bf16 v[32:35], v[112:115], v[156:159], v[32:35]\n\t"
        "ds_read_b64_tr_b16 v[108:109], v121 offset:12512\n\t"
        "v_mul_f32 v69, v197, v69\n\t"
        "v_mfma_f32_16x16x32_bf16 v[40:43], v[112:115], v[160:163], v[40:43]\n\t"
        "ds_read_b64_tr_b16 v[110:111], v121 offset:15584\n\t"
        "v_mul_f32 v70, v198, v70\n\t"
        "v_mfma_f32_16x16x32_bf16 v[36:39], v[116:119], v[156:159], v[36:39]\n\t"
        "ds_read_b64_tr_b16 v[112:113], v122 offset:12480\n\t"
        "v_mul_f32 v71, v199, v71\n\t"
        "v_cvt_pk_bf16_f32 v64, v64, v65\n\t"
        "v_mfma_f32_16x16x32_bf16 v[44:47], v[116:119], v[160:163], v[44:47]\n\t"
        "ds_read_b64_tr_b16 v[114:115], v122 offset:15552\n\t"
        "v_cvt_pk_bf16_f32 v65, v66, v67\n\t"
        "s_waitcnt lgkmcnt(6)\n\t"
        "v_mfma_f32_16x16x32_bf16 v[48:51], v[96:99], v[156:159], v[48:51]\n\t"
        "ds_read_b64_tr_b16 v[116:117], v122 offset:12512\n\t"
        "v_cvt_pk_bf16_f32 v66, v68, v69\n\t"
        "v_mfma_f32_16x16x32_bf16 v[56:59], v[96:99], v[160:163], v[56:59]\n\t"
        "ds_read_b64_tr_b16 v[118:119], v122 offset:15584\n\t"
        "v_cvt_pk_bf16_f32 v67, v70, v71\n\t"
        "v_mfma_f32_16x16x32_bf16 v[52:55], v[100:103], v[156:159], v[52:55]\n\t"
        "ds_read_b64_tr_b16 v[96:97], v121 offset:24832\n\t"
        "v_mul_f32 v72, v200, v72\n\t"
        "v_mul_f32 v73, v201, v73\n\t"
        "v_mfma_f32_16x16x32_bf16 v[60:63], v[100:103], v[160:163], v[60:63]\n\t"
        "ds_read_b64_tr_b16 v[98:99], v121 offset:27904\n\t"
        "v_mul_f32 v74, v202, v74\n\t"
        "s_waitcnt lgkmcnt(6)\n\t"
        "v_mfma_f32_16x16x32_bf16 v[32:35], v[104:107], v[164:167], v[32:35]\n\t"
        "ds_read_b64_tr_b16 v[100:101], v121 offset:24864\n\t"
        "v_mul_f32 v75, v203, v75\n\t"
        "v_mfma_f32_16x16x32_bf16 v[40:43], v[104:107], v[168:171], v[40:43]\n\t"
        "ds_read_b64_tr_b16 v[102:103], v121 offset:27936\n\t"
        "v_mul_f32 v76, v200, v76\n\t"
        "v_mfma_f32_16x16x32_bf16 v[36:39], v[108:111], v[164:167], v[36:39]\n\t"
        "ds_read_b64_tr_b16 v[104:105], v122 offset:24832\n\t"
        "v_mul_f32 v77, v201, v77\n\t"
        "v_mul_f32 v78, v202, v78\n\t"
        "v_mfma_f32_16x16x32_bf16 v[44:47], v[108:111], v[168:171], v[44:47]\n\t"
        "ds_read_b64_tr_b16 v[106:107], v122 offset:27904\n\t"
        "v_mul_f32 v79, v203, v79\n\t"
        "s_waitcnt lgkmcnt(6)\n\t"
        "v_mfma_f32_16x16x32_bf16 v[48:51], v[112:115], v[164:167], v[48:51]\n\t"
        "ds_read_b64_tr_b16 v[108:109], v122 offset:24864\n\t"
        "v_cvt_pk_bf16_f32 v68, v72, v73\n\t"
        "v_mfma_f32_16x16x32_bf16 v[56:59], v[112:115], v[168:171], v[56:59]\n\t"
        "ds_read_b64_tr_b16 v[110:111], v122 offset:27936\n\t"
        "v_cvt_pk_bf16_f32 v69, v74, v75\n\t"
        "v_mfma_f32_16x16x32_bf16 v[52:55], v[116:119], v[164:167], v[52:55]\n\t"
        "ds_read_b64_tr_b16 v[112:113], v121 offset:37184\n\t"
        "v_cvt_pk_bf16_f32 v70, v76, v77\n\t"
        "v_cvt_pk_bf16_f32 v71, v78, v79\n\t"
        "v_mfma_f32_16x16x32_bf16 v[60:63], v[116:119], v[168:171], v[60:63]\n\t"
        "ds_read_b64_tr_b16 v[114:115], v121 offset:40256\n\t"
        "v_permlane16_swap_b32 v64, v66\n\t"
        "s_waitcnt lgkmcnt(6)\n\t"
        "v_mfma_f32_16x16x32_bf16 v[32:35], v[96:99], v[172:175], v[32:35]\n\t"
        "ds_read_b64_tr_b16 v[116:117], v121 offset:37216\n\t"
        "v_permlane16_swap_b32 v65, v67\n\t"
        "v_mfma_f32_16x16x32_bf16 v[40:43], v[96:99], v[176:179], v[40:43]\n\t"
        "ds_read_b64_tr_b16 v[118:119], v121 offset:40288\n\t"
        "v_permlane16_swap_b32 v68, v70\n\t"
        "v_mfma_f32_16x16x32_bf16 v[36:39], v[100:103], v[172:175], v[36:39]\n\t"
        "ds_read_b64_tr_b16 v[96:97], v122 offset:37184\n\t"
        "v_permlane16_swap_b32 v69, v71\n\t"
        "buffer_store_dwordx4 v[64:67], v213, s[40:43], 0 offen offset:2048 sc1\n\t"
        "v_mfma_f32_16x16x32_bf16 v[44:47], v[100:103], v[176:179], v[44:47]\n\t"
        "ds_read_b64_tr_b16 v[98:99], v122 offset:40256\n\t"
        "buffer_store_dwordx4 v[68:71], v213, s[40:43], 0 offen offset:3072 sc1\n\t"
        "s_waitcnt lgkmcnt(6)\n\t"
        "v_mfma_f32_16x16x32_bf16 v[48:51], v[104:107], v[172:175], v[48:51]\n\t"
        "ds_read_b64_tr_b16 v[100:101], v122 offset:37216\n\t"
        "v_mul_f32 v80, v196, v80\n\t"
        "v_mfma_f32_16x16x32_bf16 v[56:59], v[104:107], v[176:179], v[56:59]\n\t"
        "ds_read_b64_tr_b16 v[102:103], v122 offset:40288\n\t"
        "v_mul_f32 v81, v197, v81\n\t"
        "v_mfma_f32_16x16x32_bf16 v[52:55], v[108:111], v[172:175], v[52:55]\n\t"
        "ds_read_b64_tr_b16 v[104:105], v121 offset:49536\n\t"
        "v_mul_f32 v82, v198, v82\n\t"
        "v_mul_f32 v83, v199, v83\n\t"
        "v_mfma_f32_16x16x32_bf16 v[60:63], v[108:111], v[176:179], v[60:63]\n\t"
        "ds_read_b64_tr_b16 v[106:107], v121 offset:52608\n\t"
        "v_mul_f32 v84, v196, v84\n\t"
        "s_waitcnt lgkmcnt(6)\n\t"
        "v_mfma_f32_16x16x32_bf16 v[32:35], v[112:115], v[180:183], v[32:35]\n\t"
        "ds_read_b64_tr_b16 v[108:109], v121 offset:49568\n\t"
        "v_mul_f32 v85, v197, v85\n\t"
        "v_mfma_f32_16x16x32_bf16 v[40:43], v[112:115], v[184:187], v[40:43]\n\t"
        "ds_read_b64_tr_b16 v[110:111], v121 offset:52640\n\t"
        "v_mul_f32 v86, v198, v86\n\t"
        "v_mfma_f32_16x16x32_bf16 v[36:39], v[116:119], v[180:183], v[36:39]\n\t"
        "ds_read_b64_tr_b16 v[112:113], v122 offset:49536\n\t"
        "v_mul_f32 v87, v199, v87\n\t"
        "v_cvt_pk_bf16_f32 v80, v80, v81\n\t"
        "v_mfma_f32_16x16x32_bf16 v[44:47], v[116:119], v[184:187], v[44:47]\n\t"
        "ds_read_b64_tr_b16 v[114:115], v122 offset:52608\n\t"
        "v_cvt_pk_bf16_f32 v81, v82, v83\n\t"
        "s_waitcnt lgkmcnt(6)\n\t"
        "v_mfma_f32_16x16x32_bf16 v[48:51], v[96:99], v[180:183], v[48:51]\n\t"
        "ds_read_b64_tr_b16 v[116:117], v122 offset:49568\n\t"
        "v_cvt_pk_bf16_f32 v82, v84, v85\n\t"
        "v_mfma_f32_16x16x32_bf16 v[56:59], v[96:99], v[184:187], v[56:59]\n\t"
        "ds_read_b64_tr_b16 v[118:119], v122 offset:52640\n\t"
        "v_cvt_pk_bf16_f32 v83, v86, v87\n\t"
        "v_mfma_f32_16x16x32_bf16 v[52:55], v[100:103], v[180:183], v[52:55]\n\t"
        "ds_read_b64_tr_b16 v[96:97], v123 offset:0\n\t"
        "v_mul_f32 v88, v200, v88\n\t"
        "v_mul_f32 v89, v201, v89\n\t"
        "v_mfma_f32_16x16x32_bf16 v[60:63], v[100:103], v[184:187], v[60:63]\n\t"
        "ds_read_b64_tr_b16 v[98:99], v123 offset:3072\n\t"
        "v_mul_f32 v90, v202, v90\n\t"
        "s_waitcnt lgkmcnt(6)\n\t"
        "v_mfma_f32_16x16x32_bf16 v[32:35], v[104:107], v[188:191], v[32:35]\n\t"
        "ds_read_b64_tr_b16 v[100:101], v123 offset:32\n\t"
        "v_mul_f32 v91, v203, v91\n\t"
        "v_mfma_f32_16x16x32_bf16 v[40:43], v[104:107], v[192:195], v[40:43]\n\t"
        "ds_read_b64_tr_b16 v[102:103], v123 offset:3104\n\t"
        "v_mul_f32 v92, v200, v92\n\t"
        "v_mfma_f32_16x16x32_bf16 v[36:39], v[108:111], v[188:191], v[36:39]\n\t"
        "ds_read_b64_tr_b16 v[104:105], v124 offset:0\n\t"
        "v_mul_f32 v93, v201, v93\n\t"
        "v_mul_f32 v94, v202, v94\n\t"
        "v_mfma_f32_16x16x32_bf16 v[44:47], v[108:111], v[192:195], v[44:47]\n\t"
        "ds_read_b64_tr_b16 v[106:107], v124 offset:3072\n\t"
        "v_mul_f32 v95, v203, v95\n\t"
        "s_waitcnt lgkmcnt(6)\n\t"
        "v_mfma_f32_16x16x32_bf16 v[48:51], v[112:115], v[188:191], v[48:51]\n\t"
        "ds_read_b64_tr_b16 v[108:109], v124 offset:32\n\t"
        "v_cvt_pk_bf16_f32 v84, v88, v89\n\t"
        "v_mfma_f32_16x16x32_bf16 v[56:59], v[112:115], v[192:195], v[56:59]\n\t"
        "ds_read_b64_tr_b16 v[110:111], v124 offset:3104\n\t"
        "v_cvt_pk_bf16_f32 v85, v90, v91\n\t"
        "v_mfma_f32_16x16x32_bf16 v[52:55], v[116:119], v[188:191], v[52:55]\n\t"
        "ds_read_b64_tr_b16 v[112:113], v121 offset:0\n\t"
        "v_cvt_pk_bf16_f32 v86, v92, v93\n\t"
        "v_cvt_pk_bf16_f32 v87, v94, v95\n\t"
        "v_mfma_f32_16x16x32_bf16 v[60:63], v[116:119], v[192:195], v[60:63]\n\t"
        "ds_read_b64_tr_b16 v[114:115], v121 offset:3072\n\t"
        "v_permlane16_swap_b32 v80, v82\n\t"
        "s_waitcnt lgkmcnt(6)\n\t"
        "v_mfma_f32_16x16x32_bf16 v[0:3], v[96:99], v[148:151], v[0:3]\n\t"
        "ds_read_b64_tr_b16 v[116:117], v121 offset:32\n\t"
        "v_permlane16_swap_b32 v81, v83\n\t"
        "v_mfma_f32_16x16x32_bf16 v[8:11], v[96:99], v[152:155], v[8:11]\n\t"
        "ds_read_b64_tr_b16 v[118:119], v121 offset:3104\n\t"
        "v_permlane16_swap_b32 v84, v86\n\t"
        "v_mfma_f32_16x16x32_bf16 v[4:7], v[100:103], v[148:151], v[4:7]\n\t"
        "ds_read_b64_tr_b16 v[96:97], v122 offset:0\n\t"
        "v_permlane16_swap_b32 v85, v87\n\t"
        "buffer_store_dwordx4 v[80:83], v214, s[40:43], 0 offen offset:2048 sc1\n\t"
        "v_mfma_f32_16x16x32_bf16 v[12:15], v[100:103], v[152:155], v[12:15]\n\t"
        "ds_read_b64_tr_b16 v[98:99], v122 offset:3072\n\t"
        "buffer_store_dwordx4 v[84:87], v214, s[40:43], 0 offen offset:3072 sc1\n\t"
        "s_waitcnt lgkmcnt(6)\n\t"
        "v_mfma_f32_16x16x32_bf16 v[16:19], v[104:107], v[148:151], v[16:19]\n\t"
        "ds_read_b64_tr_b16 v[100:101], v122 offset:32\n\t"
        "v_mul_f32 v32, v210, v32\n\t"
        "v_mfma_f32_16x16x32_bf16 v[24:27], v[104:107], v[152:155], v[24:27]\n\t"
        "ds_read_b64_tr_b16 v[102:103], v122 offset:3104\n\t"
        "v_mul_f32 v33, v210, v33\n\t"
        "v_mfma_f32_16x16x32_bf16 v[20:23], v[108:111], v[148:151], v[20:23]\n\t"
        "ds_read_b64_tr_b16 v[104:105], v121 offset:12352\n\t"
        "v_mul_f32 v34, v210, v34\n\t"
        "v_mul_f32 v35, v210, v35\n\t"
        "v_mfma_f32_16x16x32_bf16 v[28:31], v[108:111], v[152:155], v[28:31]\n\t"
        "ds_read_b64_tr_b16 v[106:107], v121 offset:15424\n\t"
        "v_mul_f32 v36, v210, v36\n\t"
        "s_waitcnt lgkmcnt(6)\n\t"
        "v_mfma_f32_16x16x32_bf16 v[0:3], v[112:115], v[156:159], v[0:3]\n\t"
        "ds_read_b64_tr_b16 v[108:109], v121 offset:12384\n\t"
        "v_mul_f32 v37, v210, v37\n\t"
        "v_mfma_f32_16x16x32_bf16 v[8:11], v[112:115], v[160:163], v[8:11]\n\t"
        "ds_read_b64_tr_b16 v[110:111], v121 offset:15456\n\t"
        "v_mul_f32 v38, v210, v38\n\t"
        "v_mfma_f32_16x16x32_bf16 v[4:7], v[116:119], v[156:159], v[4:7]\n\t"
        "ds_read_b64_tr_b16 v[112:113], v122 offset:12352\n\t"
        "v_mul_f32 v39, v210, v39\n\t"
        "v_cvt_pk_bf16_f32 v32, v32, v33\n\t"
        "v_mfma_f32_16x16x32_bf16 v[12:15], v[116:119], v[160:163], v[12:15]\n\t"
        "ds_read_b64_tr_b16 v[114:115], v122 offset:15424\n\t"
        "v_cvt_pk_bf16_f32 v33, v34, v35\n\t"
        "s_waitcnt lgkmcnt(6)\n\t"
        "v_mfma_f32_16x16x32_bf16 v[16:19], v[96:99], v[156:159], v[16:19]\n\t"
        "ds_read_b64_tr_b16 v[116:117], v122 offset:12384\n\t"
        "v_cvt_pk_bf16_f32 v34, v36, v37\n\t"
        "v_mfma_f32_16x16x32_bf16 v[24:27], v[96:99], v[160:163], v[24:27]\n\t"
        "ds_read_b64_tr_b16 v[118:119], v122 offset:15456\n\t"
        "v_cvt_pk_bf16_f32 v35, v38, v39\n\t"
        "v_mfma_f32_16x16x32_bf16 v[20:23], v[100:103], v[156:159], v[20:23]\n\t"
        "ds_read_b64_tr_b16 v[96:97], v121 offset:24704\n\t"
        "v_mul_f32 v40, v211, v40\n\t"
        "v_mul_f32 v41, v211, v41\n\t"
        "v_mfma_f32_16x16x32_bf16 v[28:31], v[100:103], v[160:163], v[28:31]\n\t"
        "ds_read_b64_tr_b16 v[98:99], v121 offset:27776\n\t"
        "v_mul_f32 v42, v211, v42\n\t"
        "s_waitcnt lgkmcnt(6)\n\t"
        "v_mfma_f32_16x16x32_bf16 v[0:3], v[104:107], v[164:167], v[0:3]\n\t"
        "ds_read_b64_tr_b16 v[100:101], v121 offset:24736\n\t"
        "v_mul_f32 v43, v211, v43\n\t"
        "v_mfma_f32_16x16x32_bf16 v[8:11], v[104:107], v[168:171], v[8:11]\n\t"
        "ds_read_b64_tr_b16 v[102:103], v121 offset:27808\n\t"
        "v_mul_f32 v44, v211, v44\n\t"
        "v_mfma_f32_16x16x32_bf16 v[4:7], v[108:111], v[164:167], v[4:7]\n\t"
        "ds_read_b64_tr_b16 v[104:105], v122 offset:24704\n\t"
        "v_mul_f32 v45, v211, v45\n\t"
        "v_mul_f32 v46, v211, v46\n\t"
        "v_mfma_f32_16x16x32_bf16 v[12:15], v[108:111], v[168:171], v[12:15]\n\t"
        "ds_read_b64_tr_b16 v[106:107], v122 offset:27776\n\t"
        "v_mul_f32 v47, v211, v47\n\t"
        "s_waitcnt lgkmcnt(6)\n\t"
        "v_mfma_f32_16x16x32_bf16 v[16:19], v[112:115], v[164:167], v[16:19]\n\t"
        "ds_read_b64_tr_b16 v[108:109], v122 offset:24736\n\t"
        "v_cvt_pk_bf16_f32 v36, v40, v41\n\t"
        "v_mfma_f32_16x16x32_bf16 v[24:27], v[112:115], v[168:171], v[24:27]\n\t"
        "ds_read_b64_tr_b16 v[110:111], v122 offset:27808\n\t"
        "v_cvt_pk_bf16_f32 v37, v42, v43\n\t"
        "v_mfma_f32_16x16x32_bf16 v[20:23], v[116:119], v[164:167], v[20:23]\n\t"
        "ds_read_b64_tr_b16 v[112:113], v121 offset:37056\n\t"
        "v_cvt_pk_bf16_f32 v38, v44, v45\n\t"
        "v_cvt_pk_bf16_f32 v39, v46, v47\n\t"
        "v_mfma_f32_16x16x32_bf16 v[28:31], v[116:119], v[168:171], v[28:31]\n\t"
        "ds_read_b64_tr_b16 v[114:115], v121 offset:40128\n\t"
        "v_permlane16_swap_b32 v32, v36\n\t"
        "s_waitcnt lgkmcnt(6)\n\t"
        "v_mfma_f32_16x16x32_bf16 v[0:3], v[96:99], v[172:175], v[0:3]\n\t"
        "ds_read_b64_tr_b16 v[116:117], v121 offset:37088\n\t"
        "v_permlane16_swap_b32 v33, v37\n\t"
        "v_mfma_f32_16x16x32_bf16 v[8:11], v[96:99], v[176:179], v[8:11]\n\t"
        "ds_read_b64_tr_b16 v[118:119], v121 offset:40160\n\t"
        "v_permlane16_swap_b32 v34, v38\n\t"
        "v_mfma_f32_16x16x32_bf16 v[4:7], v[100:103], v[172:175], v[4:7]\n\t"
        "ds_read_b64_tr_b16 v[96:97], v122 offset:37056\n\t"
        "v_permlane16_swap_b32 v35, v39\n\t"
        "buffer_store_dwordx4 v[32:35], v213, s[40:43], 0 offen offset:0 sc1\n\t"
        "v_mfma_f32_16x16x32_bf16 v[12:15], v[100:103], v[176:179], v[12:15]\n\t"
        "ds_read_b64_tr_b16 v[98:99], v122 offset:40128\n\t"
        "buffer_store_dwordx4 v[36:39], v213, s[40:43], 0 offen offset:1024 sc1\n\t"
        "s_waitcnt lgkmcnt(6)\n\t"
        "v_mfma_f32_16x16x32_bf16 v[16:19], v[104:107], v[172:175], v[16:19]\n\t"
        "ds_read_b64_tr_b16 v[100:101], v122 offset:37088\n\t"
        "v_mul_f32 v48, v210, v48\n\t"
        "v_mfma_f32_16x16x32_bf16 v[24:27], v[104:107], v[176:179], v[24:27]\n\t"
        "ds_read_b64_tr_b16 v[102:103], v122 offset:40160\n\t"
        "v_mul_f32 v49, v210, v49\n\t"
        "v_mfma_f32_16x16x32_bf16 v[20:23], v[108:111], v[172:175], v[20:23]\n\t"
        "ds_read_b64_tr_b16 v[104:105], v121 offset:49408\n\t"
        "v_mul_f32 v50, v210, v50\n\t"
        "v_mul_f32 v51, v210, v51\n\t"
        "v_mfma_f32_16x16x32_bf16 v[28:31], v[108:111], v[176:179], v[28:31]\n\t"
        "ds_read_b64_tr_b16 v[106:107], v121 offset:52480\n\t"
        "v_mul_f32 v52, v210, v52\n\t"
        "s_waitcnt lgkmcnt(6)\n\t"
        "v_mfma_f32_16x16x32_bf16 v[0:3], v[112:115], v[180:183], v[0:3]\n\t"
        "ds_read_b64_tr_b16 v[108:109], v121 offset:49440\n\t"
        "v_mul_f32 v53, v210, v53\n\t"
        "v_mfma_f32_16x16x32_bf16 v[8:11], v[112:115], v[184:187], v[8:11]\n\t"
        "ds_read_b64_tr_b16 v[110:111], v121 offset:52512\n\t"
        "v_mul_f32 v54, v210, v54\n\t"
        "v_mfma_f32_16x16x32_bf16 v[4:7], v[116:119], v[180:183], v[4:7]\n\t"
        "ds_read_b64_tr_b16 v[112:113], v122 offset:49408\n\t"
        "v_mul_f32 v55, v210, v55\n\t"
        "v_cvt_pk_bf16_f32 v48, v48, v49\n\t"
        "v_mfma_f32_16x16x32_bf16 v[12:15], v[116:119], v[184:187], v[12:15]\n\t"
        "ds_read_b64_tr_b16 v[114:115], v122 offset:52480\n\t"
        "v_cvt_pk_bf16_f32 v49, v50, v51\n\t"
        "s_waitcnt lgkmcnt(6)\n\t"
        "v_mfma_f32_16x16x32_bf16 v[16:19], v[96:99], v[180:183], v[16:19]\n\t"
        "ds_read_b64_tr_b16 v[116:117], v122 offset:49440\n\t"
        "v_cvt_pk_bf16_f32 v50, v52, v53\n\t"
        "v_mfma_f32_16x16x32_bf16 v[24:27], v[96:99], v[184:187], v[24:27]\n\t"
        "ds_read_b64_tr_b16 v[118:119], v122 offset:52512\n\t"
        "v_cvt_pk_bf16_f32 v51, v54, v55\n\t"
        "v_mfma_f32_16x16x32_bf16 v[20:23], v[100:103], v[180:183], v[20:23]\n\t"
        "v_mul_f32 v56, v211, v56\n\t"
        "v_mul_f32 v57, v211, v57\n\t"
        "v_mul_f32 v58, v211, v58\n\t"
        "v_mfma_f32_16x16x32_bf16 v[28:31], v[100:103], v[184:187], v[28:31]\n\t"
        "v_mul_f32 v59, v211, v59\n\t"
        "v_mul_f32 v60, v211, v60\n\t"
        "s_waitcnt lgkmcnt(4)\n\t"
        "v_mfma_f32_16x16x32_bf16 v[0:3], v[104:107], v[188:191], v[0:3]\n\t"
        "v_mul_f32 v61, v211, v61\n\t"
        "v_mul_f32 v62, v211, v62\n\t"
        "v_mfma_f32_16x16x32_bf16 v[8:11], v[104:107], v[192:195], v[8:11]\n\t"
        "v_mul_f32 v63, v211, v63\n\t"
        "v_cvt_pk_bf16_f32 v52, v56, v57\n\t"
        "v_mfma_f32_16x16x32_bf16 v[4:7], v[108:111], v[188:191], v[4:7]\n\t"
        "v_cvt_pk_bf16_f32 v53, v58, v59\n\t"
        "v_cvt_pk_bf16_f32 v54, v60, v61\n\t"
        "v_cvt_pk_bf16_f32 v55, v62, v63\n\t"
        "v_mfma_f32_16x16x32_bf16 v[12:15], v[108:111], v[192:195], v[12:15]\n\t"
        "v_permlane16_swap_b32 v48, v52\n\t"
        "v_permlane16_swap_b32 v49, v53\n\t"
        "s_waitcnt lgkmcnt(0)\n\t"
        "v_mfma_f32_16x16x32_bf16 v[16:19], v[112:115], v[188:191], v[16:19]\n\t"
        "v_permlane16_swap_b32 v50, v54\n\t"
        "v_permlane16_swap_b32 v51, v55\n\t"
        "v_mfma_f32_16x16x32_bf16 v[24:27], v[112:115], v[192:195], v[24:27]\n\t"
        "buffer_store_dwordx4 v[48:51], v214, s[40:43], 0 offen offset:0 sc1\n\t"
        "buffer_store_dwordx4 v[52:55], v214, s[40:43], 0 offen offset:1024 sc1\n\t"
        "v_mfma_f32_16x16x32_bf16 v[20:23], v[116:119], v[188:191], v[20:23]\n\t"
        "v_mfma_f32_16x16x32_bf16 v[28:31], v[116:119], v[192:195], v[28:31]\n\t"
        "s_nop 7\n\t"
        "s_branch 9f\n\t"
        "16:\n\t"
        "s_mov_b32 s48, 0x10000\n\t"
        "8:\n\t"
        "ds_read_b32 v126, v125\n\t"
        "s_sub_u32 s48, s48, 1\n\t"
        "s_waitcnt lgkmcnt(0)\n\t"
        "s_cbranch_scc1 17b\n\t"
        "v_cmp_gt_u32 vcc, v128, v126\n\t"
        "s_cbranch_vccnz 8b\n\t"
        "s_branch 17b\n\t"
        "18:\n\t"
        "s_mov_b32 s48, 0x10000\n\t"
        "8:\n\t"
        "ds_read_b32 v126, v125\n\t"
        "s_sub_u32 s48, s48, 1\n\t"
        "s_waitcnt lgkmcnt(0)\n\t"
        "s_cbranch_scc1 19b\n\t"
        "v_cmp_gt_u32 vcc, v128, v126\n\t"
        "s_cbranch_vccnz 8b\n\t"
        "s_branch 19b\n\t"
        "20:\n\t"
        "s_mov_b32 s48, 0x10000\n\t"
        "8:\n\t"
        "ds_read_b32 v126, v125\n\t"
        "s_sub_u32 s48, s48, 1\n\t"
        "s_waitcnt lgkmcnt(0)\n\t"
        "s_cbranch_scc1 21b\n\t"
        "v_cmp_gt_u32 vcc, v128, v126\n\t"
        "s_cbranch_vccnz 8b\n\t"
        "s_branch 21b\n\t"
        "22:\n\t"
        "s_mov_b32 s48, 0x10000\n\t"
        "8:\n\t"
        "ds_read_b32 v126, v125\n\t"
        "s_sub_u32 s48, s48, 1\n\t"
        "s_waitcnt lgkmcnt(0)\n\t"
        "s_cbranch_scc1 23b\n\t"
        "v_cmp_gt_u32 vcc, v128, v126\n\t"
        "s_cbranch_vccnz 8b\n\t"
        "s_branch 23b\n\t"
        "24:\n\t"
        "s_mov_b32 s48, 0x10000\n\t"
        "8:\n\t"
        "ds_read_b32 v126, v125\n\t"
        "s_sub_u32 s48, s48, 1\n\t"
        "s_waitcnt lgkmcnt(0)\n\t"
        "s_cbranch_scc1 25b\n\t"
        "v_cmp_gt_u32 vcc, v128, v126\n\t"
        "s_cbranch_vccnz 8b\n\t"
        "s_branch 25b\n\t"
        "26:\n\t"
        "s_mov_b32 s48, 0x10000\n\t"
        "8:\n\t"
        "ds_read_b32 v126, v125\n\t"
        "s_sub_u32 s48, s48, 1\n\t"
        "s_waitcnt lgkmcnt(0)\n\t"
        "s_cbranch_scc1 27b\n\t"
        "v_cmp_gt_u32 vcc, v128, v126\n\t"
        "s_cbranch_vccnz 8b\n\t"
        "s_branch 27b\n\t"
        "28:\n\t"
        "s_mov_b32 s48, 0x10000\n\t"
        "8:\n\t"
        "ds_read_b32 v126, v125\n\t"
        "s_sub_u32 s48, s48, 1\n\t"
        "s_waitcnt lgkmcnt(0)\n\t"
        "s_cbranch_scc1 29b\n\t"
        "v_cmp_gt_u32 vcc, v128, v126\n\t"
        "s_cbranch_vccnz 8b\n\t"
        "s_branch 29b\n\t"
        "30:\n\t"
        "s_mov_b32 s48, 0x10000\n\t"
        "8:\n\t"
        "ds_read_b32 v126, v125\n\t"
        "s_sub_u32 s48, s48, 1\n\t"
        "s_waitcnt lgkmcnt(0)\n\t"
        "s_cbranch_scc1 31b\n\t"
        "v_cmp_gt_u32 vcc, v128, v126\n\t"
        "s_cbranch_vccnz 8b\n\t"
        "s_branch 31b\n\t"
        "64:\n\t"
        "s_mov_b32 s48, 0x10000\n\t"
        "8:\n\t"
        "ds_read_b32 v126, v125\n\t"
        "s_sub_u32 s48, s48, 1\n\t"
        "s_waitcnt lgkmcnt(0)\n\t"
        "s_cbranch_scc1 65b\n\t"
        "v_cmp_gt_u32 vcc, v128, v126\n\t"
        "s_cbranch_vccnz 8b\n\t"
        "s_branch 65b\n\t"
        "9:\n\t"
        : "+{v[0:15]}"(acc[0]), "+{v[16:31]}"(acc[1]), "+{v[32:47]}"(acc[2]), "+{v[48:63]}"(acc[3]), "+{v[64:79]}"(acc[4]), "+{v[80:95]}"(acc[5]),
          "+{v[96:111]}"(stA), "+{v[112:127]}"(stB), "+{v[128:131]}"(stC), "+{v[148:163]}"(xr1), "+{v[164:179]}"(xr2), "+{v[180:195]}"(xr3),
          "+{s44}"(soff), "+{s46}"(slot)
        : "{s[40:43]}"(srd), KSKIP, "{v[212:215]}"(ep), "{v[210:211]}"(al2) : "v196", "v197", "v198", "v199", "v200", "v201", "v202", "v203", "v132", "v133", "v134", "v135", "v136", "v137", "v138", "v139", "v140", "v141", "v142", "v143", "v144", "v145", "v146", "v147", "s45", "s47", "s48", "vcc", "scc", "memory");
      STAMPV(4 + 16 * L);
      PHASE_VARS;
      u32x4 kv[4][2];
#pragma unroll
      for (int j = 0; j < 4; ++j)
#pragma unroll
        for (int e = 0; e < 8; ++e) kv[j][e >> 2][e & 3] = __float_as_uint(acc[2 + j][e]);
      u32x4 qo[2][2];
      auto drained = [&]() { asm volatile("s_waitcnt vmcnt(0)\n\tds_write_b32 %0, %1" :: "v"(lbase + L_W + 80 + 4 * wid), "v"(2 * L + 1) : "memory"); };
      if (L == 0) {
#pragma unroll
        for (int e = 0; e < 16; ++e) {
          if (e == 8) { SBAR(); drained(); SBAR(); }
          const unsigned w = pack2(acc[e >> 3][2 * (e & 7)], acc[e >> 3][2 * (e & 7) + 1]);
          qo[e >> 3][(e >> 2) & 1][e & 3] = pack2(lo16(w) * QS, hi16(w) * QS);
        }
        row_swap(qo[0][0], qo[0][1]); row_swap(qo[1][0], qo[1][1]);
      } else {
        const f32x2 aq = al2 * QS;
#pragma unroll
        for (int e = 0; e < 16; ++e) qo[e >> 3][(e >> 2) & 1][e & 3] = pack2(acc[e >> 3][2 * (e & 7)] * aq[(e >> 2) & 1], acc[e >> 3][2 * (e & 7) + 1] * aq[(e >> 2) & 1]);
        row_swap(qo[0][0], qo[0][1]); row_swap(qo[1][0], qo[1][1]);
        SBAR();
        lds_wait_ge<0>(lbase + L_W + 4 + 4 * (lane & 1), 1);
        const int idx = (int)__builtin_amdgcn_readfirstlane(words[3]);
        if ((idx >> 5) == tile) {
          const int rel = (idx & 31) - 8 * hi, dfo = ((r32 >> 2) & 1) * 16 + ((r32 >> 3) & 1) * 8 + (r32 >> 4) * 4 + (r32 & 3);
          unsigned pv[2];
#pragma unroll
          for (int j = 0; j < 2; ++j) pv[j] = reinterpret_cast<const u16*>(smem + L_PATCH)[128 + (j ^ hh) * 32 + dfo] * 0x10001u;
          DRAIN();
          if (r32 == (idx & 31)) {
#pragma unroll
            for (int i = 0; i < 4; ++i) {
              const char* p = smem + L_PATCH + ((i >> 1) ^ hh) * 64 + (i & 1) * 32 + hi * 16;
              qo[i >> 1][i & 1] = *reinterpret_cast<const u32x4*>(p);
              kv[i >> 1][i & 1] = *reinterpret_cast<const u32x4*>(p + 128);
              x1p[i] = *reinterpret_cast<const u32x4*>(smem + L_X2P + (i * 2 + hi) * 16);
            }
            reinterpret_cast<float*>(smem + L_AB)[256 + wid * 64 + lane] = hi ? 1.0f : 0.0f;
#pragma unroll
            for (int i = 0; i < 4; ++i) stg(rs, ep[1 + (i >> 1)] + (i & 1) * 1024, kv[i >> 1][i & 1]);
          }
#pragma unroll
          for (int i = 0; i < 4; ++i) {
#pragma unroll
            for (int e = 0; e < 4; ++e) {
              const int t0 = 16 * (i & 1) + 2 * e;
              const unsigned mask = rel == t0 ? 0xffffu : rel == t0 + 1 ? 0xffff0000u : 0u;
              kv[2 + (i >> 1)][i & 1][e] = (kv[2 + (i >> 1)][i & 1][e] & ~mask) | (pv[i >> 1] & mask);
            }
            stg(rs, (rel >> 3) == 2 * (i & 1) ? ep[1 + (i >> 1)] + 2048 + (i & 1) * 1024 : OOB, kv[2 + (i >> 1)][i & 1]);
          }
        }
        drained();
      }
      SBAR();
#pragma unroll
      for (int j = 0; j < 2; ++j) {
        char* p = smem + L_XCH + ((j ^ hh) * 4 + wid) * 2048 + lane * 16;
        *reinterpret_cast<u32x4*>(p) = qo[j][0]; *reinterpret_cast<u32x4*>(p + 1024) = qo[j][1];
        p = smem + L_XCH + 16384 + wid * 4096 + j * 2048 + lane * 16;
        *reinterpret_cast<u32x4*>(p) = kv[2 * j + 1][0]; *reinterpret_cast<u32x4*>(p + 1024) = kv[2 * j + 1][1];
      }
      STAMPV(41 + 4 * L);
      BAR_LGKM();
      STAMPV(43 + 4 * L);
      kva = cat4(kv[0][0], kv[0][1], kv[2][0], kv[2][1]);
      const u32x4* pk = reinterpret_cast<const u32x4*>(smem + L_XCH + 16384 + (wid ^ 1) * 4096 + lane * 16);
#pragma unroll
      for (int u = 0; u < 4; ++u) {
        const u32x4* p = reinterpret_cast<const u32x4*>(smem + L_XCH + (hh * 4 + ((2 * kh + 2 + u) & 3)) * 2048 + lane * 16);
        q[u] = __builtin_shufflevector(p[0], p[64], 0, 1, 2, 3, 4, 5, 6, 7);
      }
      kvb = cat4(pk[0], pk[64], pk[128], pk[192]);
      SBAR();
      asm volatile(
        "v_mov_b64 v[0:1], 0\n\t"
        "v_mov_b64 v[2:3], 0\n\t"
        "v_mov_b64 v[4:5], 0\n\t"
        "v_mov_b64 v[6:7], 0\n\t"
        "v_mov_b64 v[8:9], 0\n\t"
        "v_mov_b64 v[10:11], 0\n\t"
        "v_mov_b64 v[12:13], 0\n\t"
        "v_mov_b64 v[14:15], 0\n\t"
        "v_mov_b64 v[16:17], 0\n\t"
        "v_mov_b64 v[18:19], 0\n\t"
        "v_mov_b64 v[20:21], 0\n\t"
        "v_mov_b64 v[22:23], 0\n\t"
        "v_mov_b64 v[24:25], 0\n\t"
        "v_mov_b64 v[26:27], 0\n\t"
        "v_mov_b64 v[28:29], 0\n\t"
        "v_mov_b64 v[30:31], 0\n\t"
        "v_mov_b64 v[32:33], 0\n\t"
        "v_mov_b64 v[34:35], 0\n\t"
        "v_mov_b64 v[36:37], 0\n\t"
        "v_mov_b64 v[38:39], 0\n\t"
        "v_mov_b64 v[40:41], 0\n\t"
        "v_mov_b64 v[42:43], 0\n\t"
        "v_mov_b64 v[44:45], 0\n\t"
        "v_mov_b64 v[46:47], 0\n\t"
        "v_mov_b64 v[48:49], 0\n\t"
        "v_mov_b64 v[50:51], 0\n\t"
        "v_mov_b64 v[52:53], 0\n\t"
        "v_mov_b64 v[54:55], 0\n\t"
        "v_mov_b64 v[56:57], 0\n\t"
        "v_mov_b64 v[58:59], 0\n\t"
        "v_mov_b64 v[60:61], 0\n\t"
        "v_mov_b64 v[62:63], 0\n\t"
        "v_mov_b64 v[128:129], 0\n\t"
        "v_mov_b64 v[130:131], 0\n\t"
        "v_mov_b64 v[132:133], 0\n\t"
        "v_mov_b64 v[134:135], 0\n\t"
        "v_mov_b64 v[136:137], 0\n\t"
        "v_mov_b64 v[138:139], 0\n\t"
        "v_mov_b64 v[140:141], 0\n\t"
        "v_mov_b64 v[142:143], 0\n\t"
        : "={v[0:15]}"(oz[0]), "={v[16:31]}"(oz[1]), "={v[32:47]}"(oz[2]), "={v[48:63]}"(oz[3]), "={v[128:143]}"(oz[4]));
    }
    STAMPV(5 + 16 * L);
    {
      u32x4 ones;
      unsigned kvoff;
      u32x8 fv, cv;
      asm volatile("" : "={v[184:191]}"(fv), "={v[216:223]}"(cv));
      { PHASE_VARS;
        const unsigned v = (lane & 3) == 0 ? 0x3F803F80u : 0u; const bool up = (lane & 4) != 0; ones[0] = ones[1] = up ? 0u : v; ones[2] = ones[3] = up ? v : 0u;
        kvoff = OFF_KV + L * (10 * MBY) + (tn * 2 + hh) * HS + kh * 8192 + lane * 16;
        fv[1] = lbase + L_FREE + wid * 256 + lane * 4; fv[2] = 32 * L + 32;
        cv[0] = lbase + L_XCH + wid * 8192 + lane * 16; cv[1] = lbase + L_XCH + (wid ^ 2) * 8192 + lane * 16;
        cv[2] = lbase + L_XCH + 32768 + wid * 512 + lane * 4; cv[3] = lbase + L_XCH + 32768 + (wid ^ 2) * 512 + lane * 4;
        cv[4] = lbase + L_W + 48 + wid * 4; cv[5] = lbase + L_W + 48 + (wid ^ 2) * 4; cv[6] = L + 1;
        cv[7] = ((tile * 32 + r32) * D_ + tn * 64 + hi * 8) * 2; }
      f32x16 o0 = oz[0], o1 = oz[1], o2 = oz[2], o3 = oz[3], lv = oz[4];
      unsigned soff = (tm | 7) * 16384;
      STAMP_DECL;
      asm volatile(
        "s_nop 1\n\t"
        "v_mfma_f32_32x32x16_bf16 v[96:111], v[152:155], v[64:67], 0\n\t"
        "v_mfma_f32_32x32x16_bf16 v[96:111], v[156:159], v[68:71], v[96:111]\n\t"
        "s_mov_b32 s46, 0\n\t"
        "s_mov_b64 s[54:55], s[42:43]\n\t"
        "ds_write_b32 v185, v186\n\t"
        "s_nop 8\n\t"
        "v_exp_f32 v96, v96\n\t"
        "v_exp_f32 v97, v97\n\t"
        "v_exp_f32 v98, v98\n\t"
        "v_exp_f32 v99, v99\n\t"
        "v_exp_f32 v100, v100\n\t"
        "v_exp_f32 v101, v101\n\t"
        "v_exp_f32 v102, v102\n\t"
        "v_mfma_f32_32x32x16_bf16 v[112:127], v[152:155], v[72:75], 0\n\t"
        "v_exp_f32 v103, v103\n\t"
        "v_exp_f32 v104, v104\n\t"
        "v_exp_f32 v105, v105\n\t"
        "v_mfma_f32_32x32x16_bf16 v[112:127], v[156:159], v[76:79], v[112:127]\n\t"
        "v_exp_f32 v106, v106\n\t"
        "v_exp_f32 v107, v107\n\t"
        "v_exp_f32 v108, v108\n\t"
        "v_smfmac_f32_32x32x32_bf16 v[0:15], v[160:163], v[96:103], v148\n\t"
        "v_exp_f32 v109, v109\n\t"
        "v_exp_f32 v110, v110\n\t"
        "v_exp_f32 v111, v111\n\t"
        "v_smfmac_f32_16x16x64_bf16 v[128:131], v[144:147], v[96:103], v148\n\t"
        "v_exp_f32 v112, v112\n\t"
        "v_exp_f32 v113, v113\n\t"
        "v_smfmac_f32_32x32x32_bf16 v[0:15], v[164:167], v[104:111], v148\n\t"
        "v_exp_f32 v114, v114\n\t"
        "v_exp_f32 v115, v115\n\t"
        "v_exp_f32 v116, v116\n\t"
        "v_smfmac_f32_16x16x64_bf16 v[128:131], v[144:147], v[104:111], v148\n\t"
        "v_exp_f32 v117, v117\n\t"
        "v_exp_f32 v118, v118\n\t"
        "v_mfma_f32_32x32x16_bf16 v[96:111], v[152:155], v[80:83], 0\n\t"
        "v_exp_f32 v119, v119\n\t"
        "v_exp_f32 v120, v120\n\t"
        "v_exp_f32 v121, v121\n\t"
        "v_mfma_f32_32x32x16_bf16 v[96:111], v[156:159], v[84:87], v[96:111]\n\t"
        "ds_read_b32 v151, v224\n\t"
        "v_exp_f32 v122, v122\n\t"
        "v_exp_f32 v123, v123\n\t"
        "v_exp_f32 v124, v124\n\t"
        "v_smfmac_f32_32x32x32_bf16 v[16:31], v[160:163], v[112:119], v148\n\t"
        "v_exp_f32 v125, v125\n\t"
        "v_exp_f32 v126, v126\n\t"
        "v_exp_f32 v127, v127\n\t"
        "v_smfmac_f32_16x16x64_bf16 v[132:135], v[144:147], v[112:119], v148\n\t"
        "s_cmp_lg_u32 s46, 0\n\t"
        "s_cbranch_scc1 25f\n\t"
        "s_waitcnt lgkmcnt(0)\n\t"
        "v_cmp_ne_u32 vcc, v222, v151\n\t"
        "s_cbranch_vccz 24f\n\t"
        "25:\n\t"
        "v_exp_f32 v96, v96\n\t"
        "v_exp_f32 v97, v97\n\t"
        "v_smfmac_f32_32x32x32_bf16 v[16:31], v[164:167], v[120:127], v148\n\t"
        "v_exp_f32 v98, v98\n\t"
        "v_exp_f32 v99, v99\n\t"
        "v_exp_f32 v100, v100\n\t"
        "v_smfmac_f32_16x16x64_bf16 v[132:135], v[144:147], v[120:127], v148\n\t"
        "v_exp_f32 v101, v101\n\t"
        "v_exp_f32 v102, v102\n\t"
        "v_mfma_f32_32x32x16_bf16 v[112:127], v[152:155], v[88:91], 0\n\t"
        "v_exp_f32 v103, v103\n\t"
        "v_exp_f32 v104, v104\n\t"
        "v_exp_f32 v105, v105\n\t"
        "v_mfma_f32_32x32x16_bf16 v[112:127], v[156:159], v[92:95], v[112:127]\n\t"
        "ds_read_b32 v151, v224\n\t"
        "v_exp_f32 v106, v106\n\t"
        "v_exp_f32 v107, v107\n\t"
        "v_exp_f32 v108, v108\n\t"
        "v_smfmac_f32_32x32x32_bf16 v[32:47], v[160:163], v[96:103], v148\n\t"
        "v_exp_f32 v109, v109\n\t"
        "v_exp_f32 v110, v110\n\t"
        "v_exp_f32 v111, v111\n\t"
        "v_smfmac_f32_16x16x64_bf16 v[136:139], v[144:147], v[96:103], v148\n\t"
        "s_cmp_lg_u32 s46, 0\n\t"
        "s_cbranch_scc1 27f\n\t"
        "s_waitcnt lgkmcnt(0)\n\t"
        "v_cmp_ne_u32 vcc, v222, v151\n\t"
        "s_cbranch_vccz 26f\n\t"
        "27:\n\t"
        "v_exp_f32 v112, v112\n\t"
        "v_exp_f32 v113, v113\n\t"
        "v_smfmac_f32_32x32x32_bf16 v[32:47], v[164:167], v[104:111], v148\n\t"
        "v_exp_f32 v114, v114\n\t"
        "v_exp_f32 v115, v115\n\t"
        "v_exp_f32 v116, v116\n\t"
        "v_smfmac_f32_16x16x64_bf16 v[136:139], v[144:147], v[104:111], v148\n\t"
        "v_exp_f32 v117, v117\n\t"
        "v_exp_f32 v118, v118\n\t"
        "v_mfma_f32_32x32x16_bf16 v[96:111], v[168:171], v[64:67], 0\n\t"
        "v_exp_f32 v119, v119\n\t"
        "v_exp_f32 v120, v120\n\t"
        "v_exp_f32 v121, v121\n\t"
        "v_mfma_f32_32x32x16_bf16 v[96:111], v[172:175], v[68:71], v[96:111]\n\t"
        "ds_read_b32 v151, v224\n\t"
        "v_exp_f32 v122, v122\n\t"
        "v_exp_f32 v123, v123\n\t"
        "v_exp_f32 v124, v124\n\t"
        "v_smfmac_f32_32x32x32_bf16 v[48:63], v[160:163], v[112:119], v148\n\t"
        "v_exp_f32 v125, v125\n\t"
        "v_exp_f32 v126, v126\n\t"
        "v_exp_f32 v127, v127\n\t"
        "v_smfmac_f32_16x16x64_bf16 v[140:143], v[144:147], v[112:119], v148\n\t"
        "s_cmp_lg_u32 s46, 0\n\t"
        "s_cbranch_scc1 29f\n\t"
        "s_waitcnt lgkmcnt(0)\n\t"
        "v_cmp_ne_u32 vcc, v222, v151\n\t"
        "s_cbranch_vccz 28f\n\t"
        "29:\n\t"
        "v_exp_f32 v96, v96\n\t"
        "v_exp_f32 v97, v97\n\t"
        "v_smfmac_f32_32x32x32_bf16 v[48:63], v[164:167], v[120:127], v148\n\t"
        "v_exp_f32 v98, v98\n\t"
        "v_exp_f32 v99, v99\n\t"
        "v_exp_f32 v100, v100\n\t"
        "v_smfmac_f32_16x16x64_bf16 v[140:143], v[144:147], v[120:127], v148\n\t"
        "v_exp_f32 v101, v101\n\t"
        "v_exp_f32 v102, v102\n\t"
        "v_mfma_f32_32x32x16_bf16 v[112:127], v[168:171], v[72:75], 0\n\t"
        "v_exp_f32 v103, v103\n\t"
        "v_exp_f32 v104, v104\n\t"
        "v_exp_f32 v105, v105\n\t"
        "v_mfma_f32_32x32x16_bf16 v[112:127], v[172:175], v[76:79], v[112:127]\n\t"
        "ds_read_b32 v151, v224\n\t"
        "v_exp_f32 v106, v106\n\t"
        "v_exp_f32 v107, v107\n\t"
        "v_exp_f32 v108, v108\n\t"
        "v_smfmac_f32_32x32x32_bf16 v[0:15], v[176:179], v[96:103], v148\n\t"
        "v_exp_f32 v109, v109\n\t"
        "v_exp_f32 v110, v110\n\t"
        "v_exp_f32 v111, v111\n\t"
        "v_smfmac_f32_16x16x64_bf16 v[128:131], v[144:147], v[96:103], v148\n\t"
        "s_cmp_lg_u32 s46, 0\n\t"
        "s_cbranch_scc1 31f\n\t"
        "s_waitcnt lgkmcnt(0)\n\t"
        "v_cmp_ne_u32 vcc, v222, v151\n\t"
        "s_cbranch_vccz 30f\n\t"
        "31:\n\t"
        "v_exp_f32 v112, v112\n\t"
        "v_exp_f32 v113, v113\n\t"
        "v_smfmac_f32_32x32x32_bf16 v[0:15], v[180:183], v[104:111], v148\n\t"
        "v_exp_f32 v114, v114\n\t"
        "v_exp_f32 v115, v115\n\t"
        "v_exp_f32 v116, v116\n\t"
        "v_smfmac_f32_16x16x64_bf16 v[128:131], v[144:147], v[104:111], v148\n\t"
        "v_exp_f32 v117, v117\n\t"
        "v_exp_f32 v118, v118\n\t"
        "v_mfma_f32_32x32x16_bf16 v[96:111], v[168:171], v[80:83], 0\n\t"
        "v_exp_f32 v119, v119\n\t"
        "v_exp_f32 v120, v120\n\t"
        "v_exp_f32 v121, v121\n\t"
        "v_mfma_f32_32x32x16_bf16 v[96:111], v[172:175], v[84:87], v[96:111]\n\t"
        "ds_read_b32 v151, v224\n\t"
        "v_exp_f32 v122, v122\n\t"
        "v_exp_f32 v123, v123\n\t"
        "v_exp_f32 v124, v124\n\t"
        "v_smfmac_f32_32x32x32_bf16 v[16:31], v[176:179], v[112:119], v148\n\t"
        "v_exp_f32 v125, v125\n\t"
        "v_exp_f32 v126, v126\n\t"
        "v_exp_f32 v127, v127\n\t"
        "v_smfmac_f32_16x16x64_bf16 v[132:135], v[144:147], v[112:119], v148\n\t"
        "s_cmp_lg_u32 s46, 0\n\t"
        "s_cbranch_scc1 33f\n\t"
        "s_waitcnt lgkmcnt(0)\n\t"
        "v_cmp_ne_u32 vcc, v222, v151\n\t"
        "s_cbranch_vccz 32f\n\t"
        "33:\n\t"
        "v_exp_f32 v96, v96\n\t"
        "v_exp_f32 v97, v97\n\t"
        "v_smfmac_f32_32x32x32_bf16 v[16:31], v[180:183], v[120:127], v148\n\t"
        "v_exp_f32 v98, v98\n\t"
        "v_exp_f32 v99, v99\n\t"
        "v_exp_f32 v100, v100\n\t"
        "v_smfmac_f32_16x16x64_bf16 v[132:135], v[144:147], v[120:127], v148\n\t"
        "v_exp_f32 v101, v101\n\t"
        "v_exp_f32 v102, v102\n\t"
        "v_mfma_f32_32x32x16_bf16 v[112:127], v[168:171], v[88:91], 0\n\t"
        "v_exp_f32 v103, v103\n\t"
        "v_exp_f32 v104, v104\n\t"
        "v_exp_f32 v105, v105\n\t"
        "v_mfma_f32_32x32x16_bf16 v[112:127], v[172:175], v[92:95], v[112:127]\n\t"
        "v_exp_f32 v106, v106\n\t"
        "v_exp_f32 v107, v107\n\t"
        "v_exp_f32 v108, v108\n\t"
        "v_smfmac_f32_32x32x32_bf16 v[32:47], v[176:179], v[96:103], v148\n\t"
        "v_exp_f32 v109, v109\n\t"
        "v_exp_f32 v110, v110\n\t"
        "v_exp_f32 v111, v111\n\t"
        "v_smfmac_f32_16x16x64_bf16 v[136:139], v[144:147], v[96:103], v148\n\t"
        "s_cmp_lg_u32 s46, 0\n\t"
        "s_cbranch_scc1 5f\n\t"
        "s_mov_b32 s45, 0x200000\n\t"
        "2:\n\t"
        "ds_read_b32 v151, v224\n\t"
        "s_sub_u32 s45, s45, 1\n\t"
        "s_waitcnt lgkmcnt(0)\n\t"
        "s_cbranch_scc1 3f\n\t"
        "v_cmp_ne_u32 vcc, v222, v151\n\t"
        "s_cbranch_vccnz 2b\n\t"
        "3:\n\t"
        "s_add_u32 s44, s44, 0x4000\n\t"
        "s_and_b32 s44, s44, 0x3ffff\n\t"
        "s_cmp_eq_u32 s44, s49\n\t"
        "s_cselect_b32 s44, s50, s44\n\t"
        "s_add_u32 s52, s40, s44\n\t"
        "s_addc_u32 s53, s41, 0\n\t"
        "buffer_load_dwordx4 v[184:187], v149, s[52:55], 0 offen offset:0 sc1\n\t"
        "buffer_load_dwordx4 v[188:191], v149, s[52:55], 0 offen offset:1024 sc1\n\t"
        "buffer_load_dwordx4 v[192:195], v149, s[52:55], 0 offen offset:2048 sc1\n\t"
        "buffer_load_dwordx4 v[196:199], v149, s[52:55], 0 offen offset:3072 sc1\n\t"
        "buffer_load_dwordx4 v[200:203], v150, s[52:55], 0 offen offset:0 sc1\n\t"
        "buffer_load_dwordx4 v[204:207], v150, s[52:55], 0 offen offset:1024 sc1\n\t"
        "buffer_load_dwordx4 v[208:211], v150, s[52:55], 0 offen offset:2048 sc1\n\t"
        "buffer_load_dwordx4 v[212:215], v150, s[52:55], 0 offen offset:3072 sc1\n\t"
        "5:\n\t"
        "s_add_u32 s44, s44, 0x4000\n\t"
        "s_and_b32 s44, s44, 0x3ffff\n\t"
        "s_cmp_eq_u32 s44, s49\n\t"
        "s_cselect_b32 s44, s50, s44\n\t"
        "s_add_u32 s52, s40, s44\n\t"
        "s_addc_u32 s53, s41, 0\n\t"
        "buffer_load_dwordx4 v[152:155], v149, s[52:55], 0 offen offset:0 sc1\n\t"
        "buffer_load_dwordx4 v[156:159], v149, s[52:55], 0 offen offset:1024 sc1\n\t"
        "buffer_load_dwordx4 v[160:163], v149, s[52:55], 0 offen offset:2048 sc1\n\t"
        "buffer_load_dwordx4 v[164:167], v149, s[52:55], 0 offen offset:3072 sc1\n\t"
        "s_mov_b32 s45, 6\n\t"
        "1:\n\t"
        "v_exp_f32 v112, v112\n\t"
        "v_exp_f32 v113, v113\n\t"
        "v_smfmac_f32_32x32x32_bf16 v[32:47], v[180:183], v[104:111], v148\n\t"
        "v_exp_f32 v114, v114\n\t"
        "v_exp_f32 v115, v115\n\t"
        "v_exp_f32 v116, v116\n\t"
        "v_smfmac_f32_16x16x64_bf16 v[136:139], v[144:147], v[104:111], v148\n\t"
        "s_waitcnt vmcnt(8)\n\t"
        "v_exp_f32 v117, v117\n\t"
        "v_exp_f32 v118, v118\n\t"
        "v_mfma_f32_32x32x16_bf16 v[96:111], v[184:187], v[64:67], 0\n\t"
        "v_exp_f32 v119, v119\n\t"
        "v_exp_f32 v120, v120\n\t"
        "v_exp_f32 v121, v121\n\t"
        "v_mfma_f32_32x32x16_bf16 v[96:111], v[188:191], v[68:71], v[96:111]\n\t"
        "v_exp_f32 v122, v122\n\t"
        "v_exp_f32 v123, v123\n\t"
        "v_exp_f32 v124, v124\n\t"
        "v_smfmac_f32_32x32x32_bf16 v[48:63], v[176:179], v[112:119], v148\n\t"
        "v_exp_f32 v125, v125\n\t"
        "v_exp_f32 v126, v126\n\t"
        "v_exp_f32 v127, v127\n\t"
        "v_smfmac_f32_16x16x64_bf16 v[140:143], v[144:147], v[112:119], v148\n\t"
        "buffer_load_dwordx4 v[168:171], v150, s[52:55], 0 offen offset:0 sc1\n\t"
        "v_exp_f32 v96, v96\n\t"
        "v_exp_f32 v97, v97\n\t"
        "v_smfmac_f32_32x32x32_bf16 v[48:63], v[180:183], v[120:127], v148\n\t"
        "v_exp_f32 v98, v98\n\t"
        "v_exp_f32 v99, v99\n\t"
        "v_exp_f32 v100, v100\n\t"
        "v_smfmac_f32_16x16x64_bf16 v[140:143], v[144:147], v[120:127], v148\n\t"
        "v_exp_f32 v101, v101\n\t"
        "v_exp_f32 v102, v102\n\t"
        "v_mfma_f32_32x32x16_bf16 v[112:127], v[184:187], v[72:75], 0\n\t"
        "v_exp_f32 v103, v103\n\t"
        "v_exp_f32 v104, v104\n\t"
        "v_exp_f32 v105, v105\n\t"
        "v_mfma_f32_32x32x16_bf16 v[112:127], v[188:191], v[76:79], v[112:127]\n\t"
        "v_exp_f32 v106, v106\n\t"
        "v_exp_f32 v107, v107\n\t"
        "v_exp_f32 v108, v108\n\t"
        "v_smfmac_f32_32x32x32_bf16 v[0:15], v[192:195], v[96:103], v148\n\t"
        "v_exp_f32 v109, v109\n\t"
        "v_exp_f32 v110, v110\n\t"
        "v_exp_f32 v111, v111\n\t"
        "v_smfmac_f32_16x16x64_bf16 v[128:131], v[144:147], v[96:103], v148\n\t"
        "buffer_load_dwordx4 v[172:175], v150, s[52:55], 0 offen offset:1024 sc1\n\t"
        "v_exp_f32 v112, v112\n\t"
        "v_exp_f32 v113, v113\n\t"
        "v_smfmac_f32_32x32x32_bf16 v[0:15], v[196:199], v[104:111], v148\n\t"
        "v_exp_f32 v114, v114\n\t"
        "v_exp_f32 v115, v115\n\t"
        "v_exp_f32 v116, v116\n\t"
        "v_smfmac_f32_16x16x64_bf16 v[128:131], v[144:147], v[104:111], v148\n\t"
        "v_exp_f32 v117, v117\n\t"
        "v_exp_f32 v118, v118\n\t"
        "v_mfma_f32_32x32x16_bf16 v[96:111], v[184:187], v[80:83], 0\n\t"
        "v_exp_f32 v119, v119\n\t"
        "v_exp_f32 v120, v120\n\t"
        "v_exp_f32 v121, v121\n\t"
        "v_mfma_f32_32x32x16_bf16 v[96:111], v[188:191], v[84:87], v[96:111]\n\t"
        "v_exp_f32 v122, v122\n\t"
        "v_exp_f32 v123, v123\n\t"
        "v_exp_f32 v124, v124\n\t"
        "v_smfmac_f32_32x32x32_bf16 v[16:31], v[192:195], v[112:119], v148\n\t"
        "v_exp_f32 v125, v125\n\t"
        "v_exp_f32 v126, v126\n\t"
        "v_exp_f32 v127, v127\n\t"
        "v_smfmac_f32_16x16x64_bf16 v[132:135], v[144:147], v[112:119], v148\n\t"
        "buffer_load_dwordx4 v[176:179], v150, s[52:55], 0 offen offset:2048 sc1\n\t"
        "v_exp_f32 v96, v96\n\t"
        "v_exp_f32 v97, v97\n\t"
        "v_smfmac_f32_32x32x32_bf16 v[16:31], v[196:199], v[120:127], v148\n\t"
        "v_exp_f32 v98, v98\n\t"
        "v_exp_f32 v99, v99\n\t"
        "v_exp_f32 v100, v100\n\t"
        "v_smfmac_f32_16x16x64_bf16 v[132:135], v[144:147], v[120:127], v148\n\t"
        "v_exp_f32 v101, v101\n\t"
        "v_exp_f32 v102, v102\n\t"
        "v_mfma_f32_32x32x16_bf16 v[112:127], v[184:187], v[88:91], 0\n\t"
        "v_exp_f32 v103, v103\n\t"
        "v_exp_f32 v104, v104\n\t"
        "v_exp_f32 v105, v105\n\t"
        "v_mfma_f32_32x32x16_bf16 v[112:127], v[188:191], v[92:95], v[112:127]\n\t"
        "v_exp_f32 v106, v106\n\t"
        "v_exp_f32 v107, v107\n\t"
        "v_exp_f32 v108, v108\n\t"
        "v_smfmac_f32_32x32x32_bf16 v[32:47], v[192:195], v[96:103], v148\n\t"
        "v_exp_f32 v109, v109\n\t"
        "v_exp_f32 v110, v110\n\t"
        "v_exp_f32 v111, v111\n\t"
        "v_smfmac_f32_16x16x64_bf16 v[136:139], v[144:147], v[96:103], v148\n\t"
        "buffer_load_dwordx4 v[180:183], v150, s[52:55], 0 offen offset:3072 sc1\n\t"
        "v_exp_f32 v112, v112\n\t"
        "v_exp_f32 v113, v113\n\t"
        "v_smfmac_f32_32x32x32_bf16 v[32:47], v[196:199], v[104:111], v148\n\t"
        "s_add_u32 s44, s44, 0x4000\n\t"
        "s_and_b32 s44, s44, 0x3ffff\n\t"
        "v_exp_f32 v114, v114\n\t"
        "v_exp_f32 v115, v115\n\t"
        "v_exp_f32 v116, v116\n\t"
        "v_smfmac_f32_16x16x64_bf16 v[136:139], v[144:147], v[104:111], v148\n\t"
        "s_waitcnt vmcnt(8)\n\t"
        "v_exp_f32 v117, v117\n\t"
        "v_exp_f32 v118, v118\n\t"
        "v_mfma_f32_32x32x16_bf16 v[96:111], v[200:203], v[64:67], 0\n\t"
        "s_cmp_eq_u32 s44, s49\n\t"
        "v_exp_f32 v119, v119\n\t"
        "v_exp_f32 v120, v120\n\t"
        "v_exp_f32 v121, v121\n\t"
        "v_mfma_f32_32x32x16_bf16 v[96:111], v[204:207], v[68:71], v[96:111]\n\t"
        "s_cselect_b32 s44, s50, s44\n\t"
        "v_exp_f32 v122, v122\n\t"
        "v_exp_f32 v123, v123\n\t"
        "v_exp_f32 v124, v124\n\t"
        "v_smfmac_f32_32x32x32_bf16 v[48:63], v[192:195], v[112:119], v148\n\t"
        "s_add_u32 s52, s40, s44\n\t"
        "s_addc_u32 s53, s41, 0\n\t"
        "v_exp_f32 v125, v125\n\t"
        "v_exp_f32 v126, v126\n\t"
        "v_exp_f32 v127, v127\n\t"
        "v_smfmac_f32_16x16x64_bf16 v[140:143], v[144:147], v[112:119], v148\n\t"
        "buffer_load_dwordx4 v[184:187], v149, s[52:55], 0 offen offset:0 sc1\n\t"
        "v_exp_f32 v96, v96\n\t"
        "v_exp_f32 v97, v97\n\t"
        "v_smfmac_f32_32x32x32_bf16 v[48:63], v[196:199], v[120:127], v148\n\t"
        "v_exp_f32 v98, v98\n\t"
        "v_exp_f32 v99, v99\n\t"
        "v_exp_f32 v100, v100\n\t"
        "v_smfmac_f32_16x16x64_bf16 v[140:143], v[144:147], v[120:127], v148\n\t"
        "v_exp_f32 v101, v101\n\t"
        "v_exp_f32 v102, v102\n\t"
        "v_mfma_f32_32x32x16_bf16 v[112:127], v[200:203], v[72:75], 0\n\t"
        "v_exp_f32 v103, v103\n\t"
        "v_exp_f32 v104, v104\n\t"
        "v_exp_f32 v105, v105\n\t"
        "v_mfma_f32_32x32x16_bf16 v[112:127], v[204:207], v[76:79], v[112:127]\n\t"
        "v_exp_f32 v106, v106\n\t"
        "v_exp_f32 v107, v107\n\t"
        "v_exp_f32 v108, v108\n\t"
        "v_smfmac_f32_32x32x32_bf16 v[0:15], v[208:211], v[96:103], v148\n\t"
        "v_exp_f32 v109, v109\n\t"
        "v_exp_f32 v110, v110\n\t"
        "v_exp_f32 v111, v111\n\t"
        "v_smfmac_f32_16x16x64_bf16 v[128:131], v[144:147], v[96:103], v148\n\t"
        "buffer_load_dwordx4 v[188:191], v149, s[52:55], 0 offen offset:1024 sc1\n\t"
        "v_exp_f32 v112, v112\n\t"
        "v_exp_f32 v113, v113\n\t"
        "v_smfmac_f32_32x32x32_bf16 v[0:15], v[212:215], v[104:111], v148\n\t"
        "v_exp_f32 v114, v114\n\t"
        "v_exp_f32 v115, v115\n\t"
        "v_exp_f32 v116, v116\n\t"
        "v_smfmac_f32_16x16x64_bf16 v[128:131], v[144:147], v[104:111], v148\n\t"
        "v_exp_f32 v117, v117\n\t"
        "v_exp_f32 v118, v118\n\t"
        "v_mfma_f32_32x32x16_bf16 v[96:111], v[200:203], v[80:83], 0\n\t"
        "v_exp_f32 v119, v119\n\t"
        "v_exp_f32 v120, v120\n\t"
        "v_exp_f32 v121, v121\n\t"
        "v_mfma_f32_32x32x16_bf16 v[96:111], v[204:207], v[84:87], v[96:111]\n\t"
        "v_exp_f32 v122, v122\n\t"
        "v_exp_f32 v123, v123\n\t"
        "v_exp_f32 v124, v124\n\t"
        "v_smfmac_f32_32x32x32_bf16 v[16:31], v[208:211], v[112:119], v148\n\t"
        "v_exp_f32 v125, v125\n\t"
        "v_exp_f32 v126, v126\n\t"
        "v_exp_f32 v127, v127\n\t"
        "v_smfmac_f32_16x16x64_bf16 v[132:135], v[144:147], v[112:119], v148\n\t"
        "buffer_load_dwordx4 v[192:195], v149, s[52:55], 0 offen offset:2048 sc1\n\t"
        "v_exp_f32 v96, v96\n\t"
        "v_exp_f32 v97, v97\n\t"
        "v_smfmac_f32_32x32x32_bf16 v[16:31], v[212:215], v[120:127], v148\n\t"
        "v_exp_f32 v98, v98\n\t"
        "v_exp_f32 v99, v99\n\t"
        "v_exp_f32 v100, v100\n\t"
        "v_smfmac_f32_16x16x64_bf16 v[132:135], v[144:147], v[120:127], v148\n\t"
        "v_exp_f32 v101, v101\n\t"
        "v_exp_f32 v102, v102\n\t"
        "v_mfma_f32_32x32x16_bf16 v[112:127], v[200:203], v[88:91], 0\n\t"
        "v_exp_f32 v103, v103\n\t"
        "v_exp_f32 v104, v104\n\t"
        "v_exp_f32 v105, v105\n\t"
        "v_mfma_f32_32x32x16_bf16 v[112:127], v[204:207], v[92:95], v[112:127]\n\t"
        "v_exp_f32 v106, v106\n\t"
        "v_exp_f32 v107, v107\n\t"
        "v_exp_f32 v108, v108\n\t"
        "v_smfmac_f32_32x32x32_bf16 v[32:47], v[208:211], v[96:103], v148\n\t"
        "v_exp_f32 v109, v109\n\t"
        "v_exp_f32 v110, v110\n\t"
        "v_exp_f32 v111, v111\n\t"
        "v_smfmac_f32_16x16x64_bf16 v[136:139], v[144:147], v[96:103], v148\n\t"
        "buffer_load_dwordx4 v[196:199], v149, s[52:55], 0 offen offset:3072 sc1\n\t"
        "v_exp_f32 v112, v112\n\t"
        "v_exp_f32 v113, v113\n\t"
        "v_smfmac_f32_32x32x32_bf16 v[32:47], v[212:215], v[104:111], v148\n\t"
        "v_exp_f32 v114, v114\n\t"
        "v_exp_f32 v115, v115\n\t"
        "v_exp_f32 v116, v116\n\t"
        "v_smfmac_f32_16x16x64_bf16 v[136:139], v[144:147], v[104:111], v148\n\t"
        "s_waitcnt vmcnt(8)\n\t"
        "v_exp_f32 v117, v117\n\t"
        "v_exp_f32 v118, v118\n\t"
        "v_mfma_f32_32x32x16_bf16 v[96:111], v[152:155], v[64:67], 0\n\t"
        "v_exp_f32 v119, v119\n\t"
        "v_exp_f32 v120, v120\n\t"
        "v_exp_f32 v121, v121\n\t"
        "v_mfma_f32_32x32x16_bf16 v[96:111], v[156:159], v[68:71], v[96:111]\n\t"
        "v_exp_f32 v122, v122\n\t"
        "v_exp_f32 v123, v123\n\t"
        "v_exp_f32 v124, v124\n\t"
        "v_smfmac_f32_32x32x32_bf16 v[48:63], v[208:211], v[112:119], v148\n\t"
        "v_exp_f32 v125, v125\n\t"
        "v_exp_f32 v126, v126\n\t"
        "v_exp_f32 v127, v127\n\t"
        "v_smfmac_f32_16x16x64_bf16 v[140:143], v[144:147], v[112:119], v148\n\t"
        "buffer_load_dwordx4 v[200:203], v150, s[52:55], 0 offen offset:0 sc1\n\t"
        "v_exp_f32 v96, v96\n\t"
        "v_exp_f32 v97, v97\n\t"
        "v_smfmac_f32_32x32x32_bf16 v[48:63], v[212:215], v[120:127], v148\n\t"
        "v_exp_f32 v98, v98\n\t"
        "v_exp_f32 v99, v99\n\t"
        "v_exp_f32 v100, v100\n\t"
        "v_smfmac_f32_16x16x64_bf16 v[140:143], v[144:147], v[120:127], v148\n\t"
        "v_exp_f32 v101, v101\n\t"
        "v_exp_f32 v102, v102\n\t"
        "v_mfma_f32_32x32x16_bf16 v[112:127], v[152:155], v[72:75], 0\n\t"
        "v_exp_f32 v103, v103\n\t"
        "v_exp_f32 v104, v104\n\t"
        "v_exp_f32 v105, v105\n\t"
        "v_mfma_f32_32x32x16_bf16 v[112:127], v[156:159], v[76:79], v[112:127]\n\t"
        "v_exp_f32 v106, v106\n\t"
        "v_exp_f32 v107, v107\n\t"
        "v_exp_f32 v108, v108\n\t"
        "v_smfmac_f32_32x32x32_bf16 v[0:15], v[160:163], v[96:103], v148\n\t"
        "v_exp_f32 v109, v109\n\t"
        "v_exp_f32 v110, v110\n\t"
        "v_exp_f32 v111, v111\n\t"
        "v_smfmac_f32_16x16x64_bf16 v[128:131], v[144:147], v[96:103], v148\n\t"
        "buffer_load_dwordx4 v[204:207], v150, s[52:55], 0 offen offset:1024 sc1\n\t"
        "v_exp_f32 v112, v112\n\t"
        "v_exp_f32 v113, v113\n\t"
        "v_smfmac_f32_32x32x32_bf16 v[0:15], v[164:167], v[104:111], v148\n\t"
        "v_exp_f32 v114, v114\n\t"
        "v_exp_f32 v115, v115\n\t"
        "v_exp_f32 v116, v116\n\t"
        "v_smfmac_f32_16x16x64_bf16 v[128:131], v[144:147], v[104:111], v148\n\t"
        "v_exp_f32 v117, v117\n\t"
        "v_exp_f32 v118, v118\n\t"
        "v_mfma_f32_32x32x16_bf16 v[96:111], v[152:155], v[80:83], 0\n\t"
        "v_exp_f32 v119, v119\n\t"
        "v_exp_f32 v120, v120\n\t"
        "v_exp_f32 v121, v121\n\t"
        "v_mfma_f32_32x32x16_bf16 v[96:111], v[156:159], v[84:87], v[96:111]\n\t"
        "v_exp_f32 v122, v122\n\t"
        "v_exp_f32 v123, v123\n\t"
        "v_exp_f32 v124, v124\n\t"
        "v_smfmac_f32_32x32x32_bf16 v[16:31], v[160:163], v[112:119], v148\n\t"
        "v_exp_f32 v125, v125\n\t"
        "v_exp_f32 v126, v126\n\t"
        "v_exp_f32 v127, v127\n\t"
        "v_smfmac_f32_16x16x64_bf16 v[132:135], v[144:147], v[112:119], v148\n\t"
        "buffer_load_dwordx4 v[208:211], v150, s[52:55], 0 offen offset:2048 sc1\n\t"
        "v_exp_f32 v96, v96\n\t"
        "v_exp_f32 v97, v97\n\t"
        "v_smfmac_f32_32x32x32_bf16 v[16:31], v[164:167], v[120:127], v148\n\t"
        "v_exp_f32 v98, v98\n\t"
        "v_exp_f32 v99, v99\n\t"
        "v_exp_f32 v100, v100\n\t"
        "v_smfmac_f32_16x16x64_bf16 v[132:135], v[144:147], v[120:127], v148\n\t"
        "v_exp_f32 v101, v101\n\t"
        "v_exp_f32 v102, v102\n\t"
        "v_mfma_f32_32x32x16_bf16 v[112:127], v[152:155], v[88:91], 0\n\t"
        "v_exp_f32 v103, v103\n\t"
        "v_exp_f32 v104, v104\n\t"
        "v_exp_f32 v105, v105\n\t"
        "v_mfma_f32_32x32x16_bf16 v[112:127], v[156:159], v[92:95], v[112:127]\n\t"
        "v_exp_f32 v106, v106\n\t"
        "v_exp_f32 v107, v107\n\t"
        "v_exp_f32 v108, v108\n\t"
        "v_smfmac_f32_32x32x32_bf16 v[32:47], v[160:163], v[96:103], v148\n\t"
        "v_exp_f32 v109, v109\n\t"
        "v_exp_f32 v110, v110\n\t"
        "v_exp_f32 v111, v111\n\t"
        "v_smfmac_f32_16x16x64_bf16 v[136:139], v[144:147], v[96:103], v148\n\t"
        "buffer_load_dwordx4 v[212:215], v150, s[52:55], 0 offen offset:3072 sc1\n\t"
        "v_exp_f32 v112, v112\n\t"
        "v_exp_f32 v113, v113\n\t"
        "v_smfmac_f32_32x32x32_bf16 v[32:47], v[164:167], v[104:111], v148\n\t"
        "s_add_u32 s44, s44, 0x4000\n\t"
        "s_and_b32 s44, s44, 0x3ffff\n\t"
        "v_exp_f32 v114, v114\n\t"
        "v_exp_f32 v115, v115\n\t"
        "v_exp_f32 v116, v116\n\t"
        "v_smfmac_f32_16x16x64_bf16 v[136:139], v[144:147], v[104:111], v148\n\t"
        "s_waitcnt vmcnt(8)\n\t"
        "v_exp_f32 v117, v117\n\t"
        "v_exp_f32 v118, v118\n\t"
        "v_mfma_f32_32x32x16_bf16 v[96:111], v[168:171], v[64:67], 0\n\t"
        "s_cmp_eq_u32 s44, s49\n\t"
        "v_exp_f32 v119, v119\n\t"
        "v_exp_f32 v120, v120\n\t"
        "v_exp_f32 v121, v121\n\t"
        "v_mfma_f32_32x32x16_bf16 v[96:111], v[172:175], v[68:71], v[96:111]\n\t"
        "s_cselect_b32 s44, s50, s44\n\t"
        "v_exp_f32 v122, v122\n\t"
        "v_exp_f32 v123, v123\n\t"
        "v_exp_f32 v124, v124\n\t"
        "v_smfmac_f32_32x32x32_bf16 v[48:63], v[160:163], v[112:119], v148\n\t"
        "s_add_u32 s52, s40, s44\n\t"
        "s_addc_u32 s53, s41, 0\n\t"
        "v_exp_f32 v125, v125\n\t"
        "v_exp_f32 v126, v126\n\t"
        "v_exp_f32 v127, v127\n\t"
        "v_smfmac_f32_16x16x64_bf16 v[140:143], v[144:147], v[112:119], v148\n\t"
        "buffer_load_dwordx4 v[152:155], v149, s[52:55], 0 offen offset:0 sc1\n\t"
        "v_exp_f32 v96, v96\n\t"
        "v_exp_f32 v97, v97\n\t"
        "v_smfmac_f32_32x32x32_bf16 v[48:63], v[164:167], v[120:127], v148\n\t"
        "v_exp_f32 v98, v98\n\t"
        "v_exp_f32 v99, v99\n\t"
        "v_exp_f32 v100, v100\n\t"
        "v_smfmac_f32_16x16x64_bf16 v[140:143], v[144:147], v[120:127], v148\n\t"
        "v_exp_f32 v101, v101\n\t"
        "v_exp_f32 v102, v102\n\t"
        "v_mfma_f32_32x32x16_bf16 v[112:127], v[168:171], v[72:75], 0\n\t"
        "v_exp_f32 v103, v103\n\t"
        "v_exp_f32 v104, v104\n\t"
        "v_exp_f32 v105, v105\n\t"
        "v_mfma_f32_32x32x16_bf16 v[112:127], v[172:175], v[76:79], v[112:127]\n\t"
        "v_exp_f32 v106, v106\n\t"
        "v_exp_f32 v107, v107\n\t"
        "v_exp_f32 v108, v108\n\t"
        "v_smfmac_f32_32x32x32_bf16 v[0:15], v[176:179], v[96:103], v148\n\t"
        "v_exp_f32 v109, v109\n\t"
        "v_exp_f32 v110, v110\n\t"
        "v_exp_f32 v111, v111\n\t"
        "v_smfmac_f32_16x16x64_bf16 v[128:131], v[144:147], v[96:103], v148\n\t"
        "buffer_load_dwordx4 v[156:159], v149, s[52:55], 0 offen offset:1024 sc1\n\t"
        "v_exp_f32 v112, v112\n\t"
        "v_exp_f32 v113, v113\n\t"
        "v_smfmac_f32_32x32x32_bf16 v[0:15], v[180:183], v[104:111], v148\n\t"
        "v_exp_f32 v114, v114\n\t"
        "v_exp_f32 v115, v115\n\t"
        "v_exp_f32 v116, v116\n\t"
        "v_smfmac_f32_16x16x64_bf16 v[128:131], v[144:147], v[104:111], v148\n\t"
        "v_exp_f32 v117, v117\n\t"
        "v_exp_f32 v118, v118\n\t"
        "v_mfma_f32_32x32x16_bf16 v[96:111], v[168:171], v[80:83], 0\n\t"
        "v_exp_f32 v119, v119\n\t"
        "v_exp_f32 v120, v120\n\t"
        "v_exp_f32 v121, v121\n\t"
        "v_mfma_f32_32x32x16_bf16 v[96:111], v[172:175], v[84:87], v[96:111]\n\t"
        "v_exp_f32 v122, v122\n\t"
        "v_exp_f32 v123, v123\n\t"
        "v_exp_f32 v124, v124\n\t"
        "v_smfmac_f32_32x32x32_bf16 v[16:31], v[176:179], v[112:119], v148\n\t"
        "v_exp_f32 v125, v125\n\t"
        "v_exp_f32 v126, v126\n\t"
        "v_exp_f32 v127, v127\n\t"
        "v_smfmac_f32_16x16x64_bf16 v[132:135], v[144:147], v[112:119], v148\n\t"
        "buffer_load_dwordx4 v[160:163], v149, s[52:55], 0 offen offset:2048 sc1\n\t"
        "v_exp_f32 v96, v96\n\t"
        "v_exp_f32 v97, v97\n\t"
        "v_smfmac_f32_32x32x32_bf16 v[16:31], v[180:183], v[120:127], v148\n\t"
        "v_exp_f32 v98, v98\n\t"
        "v_exp_f32 v99, v99\n\t"
        "v_exp_f32 v100, v100\n\t"
        "v_smfmac_f32_16x16x64_bf16 v[132:135], v[144:147], v[120:127], v148\n\t"
        "v_exp_f32 v101, v101\n\t"
        "v_exp_f32 v102, v102\n\t"
        "v_mfma_f32_32x32x16_bf16 v[112:127], v[168:171], v[88:91], 0\n\t"
        "v_exp_f32 v103, v103\n\t"
        "v_exp_f32 v104, v104\n\t"
        "v_exp_f32 v105, v105\n\t"
        "v_mfma_f32_32x32x16_bf16 v[112:127], v[172:175], v[92:95], v[112:127]\n\t"
        "v_exp_f32 v106, v106\n\t"
        "v_exp_f32 v107, v107\n\t"
        "v_exp_f32 v108, v108\n\t"
        "v_smfmac_f32_32x32x32_bf16 v[32:47], v[176:179], v[96:103], v148\n\t"
        "s_sub_u32 s45, s45, 1\n\t"
        "v_exp_f32 v109, v109\n\t"
        "v_exp_f32 v110, v110\n\t"
        "v_exp_f32 v111, v111\n\t"
        "v_smfmac_f32_16x16x64_bf16 v[136:139], v[144:147], v[96:103], v148\n\t"
        "buffer_load_dwordx4 v[164:167], v149, s[52:55], 0 offen offset:3072 sc1\n\t"
        "s_cbranch_scc0 1b\n\t"
        "v_exp_f32 v112, v112\n\t"
        "v_exp_f32 v113, v113\n\t"
        "v_smfmac_f32_32x32x32_bf16 v[32:47], v[180:183], v[104:111], v148\n\t"
        "v_exp_f32 v114, v114\n\t"
        "v_exp_f32 v115, v115\n\t"
        "v_exp_f32 v116, v116\n\t"
        "v_smfmac_f32_16x16x64_bf16 v[136:139], v[144:147], v[104:111], v148\n\t"
        "s_waitcnt vmcnt(8)\n\t"
        "v_exp_f32 v117, v117\n\t"
        "v_exp_f32 v118, v118\n\t"
        "v_mfma_f32_32x32x16_bf16 v[96:111], v[184:187], v[64:67], 0\n\t"
        "v_exp_f32 v119, v119\n\t"
        "v_exp_f32 v120, v120\n\t"
        "v_exp_f32 v121, v121\n\t"
        "v_mfma_f32_32x32x16_bf16 v[96:111], v[188:191], v[68:71], v[96:111]\n\t"
        "v_exp_f32 v122, v122\n\t"
        "v_exp_f32 v123, v123\n\t"
        "v_exp_f32 v124, v124\n\t"
        "v_smfmac_f32_32x32x32_bf16 v[48:63], v[176:179], v[112:119], v148\n\t"
        "v_exp_f32 v125, v125\n\t"
        "v_exp_f32 v126, v126\n\t"
        "v_exp_f32 v127, v127\n\t"
        "v_smfmac_f32_16x16x64_bf16 v[140:143], v[144:147], v[112:119], v148\n\t"
        "v_exp_f32 v96, v96\n\t"
        "v_exp_f32 v97, v97\n\t"
        "v_smfmac_f32_32x32x32_bf16 v[48:63], v[180:183], v[120:127], v148\n\t"
        "v_exp_f32 v98, v98\n\t"
        "v_exp_f32 v99, v99\n\t"
        "v_exp_f32 v100, v100\n\t"
        "v_smfmac_f32_16x16x64_bf16 v[140:143], v[144:147], v[120:127], v148\n\t"
        "v_exp_f32 v101, v101\n\t"
        "v_exp_f32 v102, v102\n\t"
        "v_mfma_f32_32x32x16_bf16 v[112:127], v[184:187], v[72:75], 0\n\t"
        "v_exp_f32 v103, v103\n\t"
        "v_exp_f32 v104, v104\n\t"
        "v_exp_f32 v105, v105\n\t"
        "v_mfma_f32_32x32x16_bf16 v[112:127], v[188:191], v[76:79], v[112:127]\n\t"
        "v_exp_f32 v106, v106\n\t"
        "v_exp_f32 v107, v107\n\t"
        "v_exp_f32 v108, v108\n\t"
        "v_smfmac_f32_32x32x32_bf16 v[0:15], v[192:195], v[96:103], v148\n\t"
        "v_exp_f32 v109, v109\n\t"
        "v_exp_f32 v110, v110\n\t"
        "v_exp_f32 v111, v111\n\t"
        "v_smfmac_f32_16x16x64_bf16 v[128:131], v[144:147], v[96:103], v148\n\t"
        "v_exp_f32 v112, v112\n\t"
        "v_exp_f32 v113, v113\n\t"
        "v_smfmac_f32_32x32x32_bf16 v[0:15], v[196:199], v[104:111], v148\n\t"
        "v_exp_f32 v114, v114\n\t"
        "v_exp_f32 v115, v115\n\t"
        "v_exp_f32 v116, v116\n\t"
        "v_smfmac_f32_16x16x64_bf16 v[128:131], v[144:147], v[104:111], v148\n\t"
        "s_waitcnt vmcnt(4)\n\t"
        "v_exp_f32 v117, v117\n\t"
        "v_exp_f32 v118, v118\n\t"
        "v_mfma_f32_32x32x16_bf16 v[96:111], v[200:203], v[64:67], 0\n\t"
        "v_exp_f32 v119, v119\n\t"
        "v_exp_f32 v120, v120\n\t"
        "v_exp_f32 v121, v121\n\t"
        "v_mfma_f32_32x32x16_bf16 v[96:111], v[204:207], v[68:71], v[96:111]\n\t"
        "v_exp_f32 v122, v122\n\t"
        "v_exp_f32 v123, v123\n\t"
        "v_exp_f32 v124, v124\n\t"
        "v_smfmac_f32_32x32x32_bf16 v[16:31], v[192:195], v[112:119], v148\n\t"
        "v_exp_f32 v125, v125\n\t"
        "v_exp_f32 v126, v126\n\t"
        "v_exp_f32 v127, v127\n\t"
        "v_smfmac_f32_16x16x64_bf16 v[132:135], v[144:147], v[112:119], v148\n\t"
        "s_cmp_lg_u32 s47, 0\n\t"
        "s_cbranch_scc1 4f\n\t"
        "global_load_dwordx4 %[r0], v223, %[xb] offset:0\n\t"
        "global_load_dwordx4 %[r1], v223, %[xb] offset:32\n\t"
        "global_load_dwordx4 %[r2], v223, %[xb] offset:64\n\t"
        "global_load_dwordx4 %[r3], v223, %[xb] offset:96\n\t"
        "4:\n\t"
        "v_exp_f32 v96, v96\n\t"
        "v_exp_f32 v97, v97\n\t"
        "v_smfmac_f32_32x32x32_bf16 v[16:31], v[196:199], v[120:127], v148\n\t"
        "v_exp_f32 v98, v98\n\t"
        "v_exp_f32 v99, v99\n\t"
        "v_exp_f32 v100, v100\n\t"
        "v_smfmac_f32_16x16x64_bf16 v[132:135], v[144:147], v[120:127], v148\n\t"
        "v_exp_f32 v101, v101\n\t"
        "v_exp_f32 v102, v102\n\t"
        "v_mfma_f32_32x32x16_bf16 v[112:127], v[200:203], v[72:75], 0\n\t"
        "v_exp_f32 v103, v103\n\t"
        "v_exp_f32 v104, v104\n\t"
        "v_exp_f32 v105, v105\n\t"
        "v_mfma_f32_32x32x16_bf16 v[112:127], v[204:207], v[76:79], v[112:127]\n\t"
        "v_exp_f32 v106, v106\n\t"
        "v_exp_f32 v107, v107\n\t"
        "v_exp_f32 v108, v108\n\t"
        "v_smfmac_f32_32x32x32_bf16 v[0:15], v[208:211], v[96:103], v148\n\t"
        "v_exp_f32 v109, v109\n\t"
        "v_exp_f32 v110, v110\n\t"
        "v_exp_f32 v111, v111\n\t"
        "v_smfmac_f32_16x16x64_bf16 v[128:131], v[144:147], v[96:103], v148\n\t"
        "v_exp_f32 v112, v112\n\t"
        "v_exp_f32 v113, v113\n\t"
        "v_smfmac_f32_32x32x32_bf16 v[0:15], v[212:215], v[104:111], v148\n\t"
        "v_exp_f32 v114, v114\n\t"
        "v_exp_f32 v115, v115\n\t"
        "v_exp_f32 v116, v116\n\t"
        "v_smfmac_f32_16x16x64_bf16 v[128:131], v[144:147], v[104:111], v148\n\t"
        "v_exp_f32 v117, v117\n\t"
        "v_exp_f32 v118, v118\n\t"
        "v_mfma_f32_32x32x16_bf16 v[96:111], v[184:187], v[80:83], 0\n\t"
        "v_exp_f32 v119, v119\n\t"
        "v_exp_f32 v120, v120\n\t"
        "v_exp_f32 v121, v121\n\t"
        "v_mfma_f32_32x32x16_bf16 v[96:111], v[188:191], v[84:87], v[96:111]\n\t"
        "ds_write_b128 v216, v[0:3] offset:0\n\t"
        "ds_write_b128 v216, v[4:7] offset:1024\n\t"
        "v_exp_f32 v122, v122\n\t"
        "v_exp_f32 v123, v123\n\t"
        "v_exp_f32 v124, v124\n\t"
        "v_smfmac_f32_32x32x32_bf16 v[16:31], v[208:211], v[112:119], v148\n\t"
        "ds_write_b128 v216, v[8:11] offset:2048\n\t"
        "ds_write_b128 v216, v[12:15] offset:3072\n\t"
        "v_exp_f32 v125, v125\n\t"
        "v_exp_f32 v126, v126\n\t"
        "v_exp_f32 v127, v127\n\t"
        "v_smfmac_f32_16x16x64_bf16 v[132:135], v[144:147], v[112:119], v148\n\t"
        "ds_write_b32 v218, v128 offset:0\n\t"
        "v_exp_f32 v96, v96\n\t"
        "v_exp_f32 v97, v97\n\t"
        "v_smfmac_f32_32x32x32_bf16 v[16:31], v[212:215], v[120:127], v148\n\t"
        "v_exp_f32 v98, v98\n\t"
        "v_exp_f32 v99, v99\n\t"
        "v_exp_f32 v100, v100\n\t"
        "v_smfmac_f32_16x16x64_bf16 v[132:135], v[144:147], v[120:127], v148\n\t"
        "v_exp_f32 v101, v101\n\t"
        "v_exp_f32 v102, v102\n\t"
        "v_mfma_f32_32x32x16_bf16 v[112:127], v[184:187], v[88:91], 0\n\t"
        "v_exp_f32 v103, v103\n\t"
        "v_exp_f32 v104, v104\n\t"
        "v_exp_f32 v105, v105\n\t"
        "v_mfma_f32_32x32x16_bf16 v[112:127], v[188:191], v[92:95], v[112:127]\n\t"
        "ds_write_b128 v216, v[16:19] offset:4096\n\t"
        "ds_write_b128 v216, v[20:23] offset:5120\n\t"
        "v_exp_f32 v106, v106\n\t"
        "v_exp_f32 v107, v107\n\t"
        "v_exp_f32 v108, v108\n\t"
        "v_smfmac_f32_32x32x32_bf16 v[32:47], v[192:195], v[96:103], v148\n\t"
        "ds_write_b128 v216, v[24:27] offset:6144\n\t"
        "ds_write_b128 v216, v[28:31] offset:7168\n\t"
        "v_exp_f32 v109, v109\n\t"
        "v_exp_f32 v110, v110\n\t"
        "v_exp_f32 v111, v111\n\t"
        "v_smfmac_f32_16x16x64_bf16 v[136:139], v[144:147], v[96:103], v148\n\t"
        "ds_write_b32 v218, v132 offset:256\n\t"
        "ds_write_b32 v220, v222\n\t"
        "v_exp_f32 v112, v112\n\t"
        "v_exp_f32 v113, v113\n\t"
        "v_smfmac_f32_32x32x32_bf16 v[32:47], v[196:199], v[104:111], v148\n\t"
        "v_exp_f32 v114, v114\n\t"
        "v_exp_f32 v115, v115\n\t"
        "v_exp_f32 v116, v116\n\t"
        "v_smfmac_f32_16x16x64_bf16 v[136:139], v[144:147], v[104:111], v148\n\t"
        "v_exp_f32 v117, v117\n\t"
        "v_exp_f32 v118, v118\n\t"
        "v_mfma_f32_32x32x16_bf16 v[96:111], v[200:203], v[80:83], 0\n\t"
        "v_exp_f32 v119, v119\n\t"
        "v_exp_f32 v120, v120\n\t"
        "v_exp_f32 v121, v121\n\t"
        "v_mfma_f32_32x32x16_bf16 v[96:111], v[204:207], v[84:87], v[96:111]\n\t"
        "v_exp_f32 v122, v122\n\t"
        "v_exp_f32 v123, v123\n\t"
        "v_exp_f32 v124, v124\n\t"
        "v_smfmac_f32_32x32x32_bf16 v[48:63], v[192:195], v[112:119], v148\n\t"
        "v_exp_f32 v125, v125\n\t"
        "v_exp_f32 v126, v126\n\t"
        "v_exp_f32 v127, v127\n\t"
        "v_smfmac_f32_16x16x64_bf16 v[140:143], v[144:147], v[112:119], v148\n\t"
        "v_exp_f32 v96, v96\n\t"
        "v_exp_f32 v97, v97\n\t"
        "v_smfmac_f32_32x32x32_bf16 v[48:63], v[196:199], v[120:127], v148\n\t"
        "v_exp_f32 v98, v98\n\t"
        "v_exp_f32 v99, v99\n\t"
        "v_exp_f32 v100, v100\n\t"
        "v_smfmac_f32_16x16x64_bf16 v[140:143], v[144:147], v[120:127], v148\n\t"
        "v_exp_f32 v101, v101\n\t"
        "v_exp_f32 v102, v102\n\t"
        "v_mfma_f32_32x32x16_bf16 v[112:127], v[200:203], v[88:91], 0\n\t"
        "v_exp_f32 v103, v103\n\t"
        "v_exp_f32 v104, v104\n\t"
        "v_exp_f32 v105, v105\n\t"
        "v_mfma_f32_32x32x16_bf16 v[112:127], v[204:207], v[92:95], v[112:127]\n\t"
        "ds_read_b32 v151, v221\n\t"
        "v_exp_f32 v106, v106\n\t"
        "v_exp_f32 v107, v107\n\t"
        "v_exp_f32 v108, v108\n\t"
        "v_smfmac_f32_32x32x32_bf16 v[32:47], v[208:211], v[96:103], v148\n\t"
        "v_exp_f32 v109, v109\n\t"
        "v_exp_f32 v110, v110\n\t"
        "v_exp_f32 v111, v111\n\t"
        "v_smfmac_f32_16x16x64_bf16 v[136:139], v[144:147], v[96:103], v148\n\t"
        "s_waitcnt lgkmcnt(0)\n\t"
        "v_cmp_ne_u32 vcc, v222, v151\n\t"
        "s_cbranch_vccnz 40f\n\t"
        "41:\n\t"
        "v_exp_f32 v112, v112\n\t"
        "v_exp_f32 v113, v113\n\t"
        "v_smfmac_f32_32x32x32_bf16 v[32:47], v[212:215], v[104:111], v148\n\t"
        "ds_read_b128 v[0:3], v217 offset:0\n\t"
        "ds_read_b128 v[4:7], v217 offset:1024\n\t"
        "ds_read_b128 v[8:11], v217 offset:2048\n\t"
        "ds_read_b128 v[12:15], v217 offset:3072\n\t"
        "v_exp_f32 v114, v114\n\t"
        "v_exp_f32 v115, v115\n\t"
        "v_exp_f32 v116, v116\n\t"
        "v_smfmac_f32_16x16x64_bf16 v[136:139], v[144:147], v[104:111], v148\n\t"
        "ds_read_b32 v128, v219 offset:0\n\t"
        "ds_read_b128 v[16:19], v217 offset:4096\n\t"
        "ds_read_b128 v[20:23], v217 offset:5120\n\t"
        "v_exp_f32 v117, v117\n\t"
        "v_exp_f32 v118, v118\n\t"
        "v_exp_f32 v119, v119\n\t"
        "v_exp_f32 v120, v120\n\t"
        "v_exp_f32 v121, v121\n\t"
        "v_exp_f32 v122, v122\n\t"
        "v_exp_f32 v123, v123\n\t"
        "v_exp_f32 v124, v124\n\t"
        "v_smfmac_f32_32x32x32_bf16 v[48:63], v[208:211], v[112:119], v148\n\t"
        "ds_read_b128 v[24:27], v217 offset:6144\n\t"
        "ds_read_b128 v[28:31], v217 offset:7168\n\t"
        "ds_read_b32 v132, v219 offset:256\n\t"
        "v_exp_f32 v125, v125\n\t"
        "v_exp_f32 v126, v126\n\t"
        "v_exp_f32 v127, v127\n\t"
        "v_smfmac_f32_16x16x64_bf16 v[140:143], v[144:147], v[112:119], v148\n\t"
        "s_nop 1\n\t"
        "v_smfmac_f32_32x32x32_bf16 v[48:63], v[212:215], v[120:127], v148\n\t"
        "v_smfmac_f32_16x16x64_bf16 v[140:143], v[144:147], v[120:127], v148\n\t"
        "s_waitcnt vmcnt(0) lgkmcnt(0)\n\t"
        "s_nop 6\n\t"
        "s_branch 9f\n\t"
        "24:\n\t"
        "s_add_u32 s44, s44, 0x4000\n\t"
        "s_and_b32 s44, s44, 0x3ffff\n\t"
        "s_cmp_eq_u32 s44, s49\n\t"
        "s_cselect_b32 s44, s50, s44\n\t"
        "s_add_u32 s52, s40, s44\n\t"
        "s_addc_u32 s53, s41, 0\n\t"
        "buffer_load_dwordx4 v[184:187], v149, s[52:55], 0 offen offset:0 sc1\n\t"
        "buffer_load_dwordx4 v[188:191], v149, s[52:55], 0 offen offset:1024 sc1\n\t"
        "buffer_load_dwordx4 v[192:195], v149, s[52:55], 0 offen offset:2048 sc1\n\t"
        "buffer_load_dwordx4 v[196:199], v149, s[52:55], 0 offen offset:3072 sc1\n\t"
        "buffer_load_dwordx4 v[200:203], v150, s[52:55], 0 offen offset:0 sc1\n\t"
        "buffer_load_dwordx4 v[204:207], v150, s[52:55], 0 offen offset:1024 sc1\n\t"
        "buffer_load_dwordx4 v[208:211], v150, s[52:55], 0 offen offset:2048 sc1\n\t"
        "buffer_load_dwordx4 v[212:215], v150, s[52:55], 0 offen offset:3072 sc1\n\t"
        "s_mov_b32 s46, 1\n\t"
        "s_branch 25b\n\t"
        "26:\n\t"
        "s_add_u32 s44, s44, 0x4000\n\t"
        "s_and_b32 s44, s44, 0x3ffff\n\t"
        "s_cmp_eq_u32 s44, s49\n\t"
        "s_cselect_b32 s44, s50, s44\n\t"
        "s_add_u32 s52, s40, s44\n\t"
        "s_addc_u32 s53, s41, 0\n\t"
        "buffer_load_dwordx4 v[184:187], v149, s[52:55], 0 offen offset:0 sc1\n\t"
        "buffer_load_dwordx4 v[188:191], v149, s[52:55], 0 offen offset:1024 sc1\n\t"
        "buffer_load_dwordx4 v[192:195], v149, s[52:55], 0 offen offset:2048 sc1\n\t"
        "buffer_load_dwordx4 v[196:199], v149, s[52:55], 0 offen offset:3072 sc1\n\t"
        "buffer_load_dwordx4 v[200:203], v150, s[52:55], 0 offen offset:0 sc1\n\t"
        "buffer_load_dwordx4 v[204:207], v150, s[52:55], 0 offen offset:1024 sc1\n\t"
        "buffer_load_dwordx4 v[208:211], v150, s[52:55], 0 offen offset:2048 sc1\n\t"
        "buffer_load_dwordx4 v[212:215], v150, s[52:55], 0 offen offset:3072 sc1\n\t"
        "s_mov_b32 s46, 1\n\t"
        "s_branch 27b\n\t"
        "28:\n\t"
        "s_add_u32 s44, s44, 0x4000\n\t"
        "s_and_b32 s44, s44, 0x3ffff\n\t"
        "s_cmp_eq_u32 s44, s49\n\t"
        "s_cselect_b32 s44, s50, s44\n\t"
        "s_add_u32 s52, s40, s44\n\t"
        "s_addc_u32 s53, s41, 0\n\t"
        "buffer_load_dwordx4 v[184:187], v149, s[52:55], 0 offen offset:0 sc1\n\t"
        "buffer_load_dwordx4 v[188:191], v149, s[52:55], 0 offen offset:1024 sc1\n\t"
        "buffer_load_dwordx4 v[192:195], v149, s[52:55], 0 offen offset:2048 sc1\n\t"
        "buffer_load_dwordx4 v[196:199], v149, s[52:55], 0 offen offset:3072 sc1\n\t"
        "buffer_load_dwordx4 v[200:203], v150, s[52:55], 0 offen offset:0 sc1\n\t"
        "buffer_load_dwordx4 v[204:207], v150, s[52:55], 0 offen offset:1024 sc1\n\t"
        "buffer_load_dwordx4 v[208:211], v150, s[52:55], 0 offen offset:2048 sc1\n\t"
        "buffer_load_dwordx4 v[212:215], v150, s[52:55], 0 offen offset:3072 sc1\n\t"
        "s_mov_b32 s46, 1\n\t"
        "s_branch 29b\n\t"
        "30:\n\t"
        "s_add_u32 s44, s44, 0x4000\n\t"
        "s_and_b32 s44, s44, 0x3ffff\n\t"
        "s_cmp_eq_u32 s44, s49\n\t"
        "s_cselect_b32 s44, s50, s44\n\t"
        "s_add_u32 s52, s40, s44\n\t"
        "s_addc_u32 s53, s41, 0\n\t"
        "buffer_load_dwordx4 v[184:187], v149, s[52:55], 0 offen offset:0 sc1\n\t"
        "buffer_load_dwordx4 v[188:191], v149, s[52:55], 0 offen offset:1024 sc1\n\t"
        "buffer_load_dwordx4 v[192:195], v149, s[52:55], 0 offen offset:2048 sc1\n\t"
        "buffer_load_dwordx4 v[196:199], v149, s[52:55], 0 offen offset:3072 sc1\n\t"
        "buffer_load_dwordx4 v[200:203], v150, s[52:55], 0 offen offset:0 sc1\n\t"
        "buffer_load_dwordx4 v[204:207], v150, s[52:55], 0 offen offset:1024 sc1\n\t"
        "buffer_load_dwordx4 v[208:211], v150, s[52:55], 0 offen offset:2048 sc1\n\t"
        "buffer_load_dwordx4 v[212:215], v150, s[52:55], 0 offen offset:3072 sc1\n\t"
        "s_mov_b32 s46, 1\n\t"
        "s_branch 31b\n\t"
        "32:\n\t"
        "s_add_u32 s44, s44, 0x4000\n\t"
        "s_and_b32 s44, s44, 0x3ffff\n\t"
        "s_cmp_eq_u32 s44, s49\n\t"
        "s_cselect_b32 s44, s50, s44\n\t"
        "s_add_u32 s52, s40, s44\n\t"
        "s_addc_u32 s53, s41, 0\n\t"
        "buffer_load_dwordx4 v[184:187], v149, s[52:55], 0 offen offset:0 sc1\n\t"
        "buffer_load_dwordx4 v[188:191], v149, s[52:55], 0 offen offset:1024 sc1\n\t"
        "buffer_load_dwordx4 v[192:195], v149, s[52:55], 0 offen offset:2048 sc1\n\t"
        "buffer_load_dwordx4 v[196:199], v149, s[52:55], 0 offen offset:3072 sc1\n\t"
        "buffer_load_dwordx4 v[200:203], v150, s[52:55], 0 offen offset:0 sc1\n\t"
        "buffer_load_dwordx4 v[204:207], v150, s[52:55], 0 offen offset:1024 sc1\n\t"
        "buffer_load_dwordx4 v[208:211], v150, s[52:55], 0 offen offset:2048 sc1\n\t"
        "buffer_load_dwordx4 v[212:215], v150, s[52:55], 0 offen offset:3072 sc1\n\t"
        "s_mov_b32 s46, 1\n\t"
        "s_branch 33b\n\t"
        "40:\n\t"
        "s_mov_b32 s45, 0x100000\n\t"
        "42:\n\t"
        "ds_read_b32 v151, v221\n\t"
        "s_sub_u32 s45, s45, 1\n\t"
        "s_waitcnt lgkmcnt(0)\n\t"
        "s_cbranch_scc1 41b\n\t"
        "v_cmp_ne_u32 vcc, v222, v151\n\t"
        "s_cbranch_vccnz 42b\n\t"
        "s_branch 41b\n\t"
        "9:\n\t"
        : "+{v[0:15]}"(o0), "+{v[16:31]}"(o1), "+{v[32:47]}"(o2), "+{v[48:63]}"(o3), "+{v[128:143]}"(lv), "+{s44}"(soff), "+{v[152:167]}"(kva), "+{v[168:183]}"(kvb), "+{v[184:191]}"(fv),
          [r0] "+v"(x1p[0]), [r1] "+v"(x1p[1]), [r2] "+v"(x1p[2]), [r3] "+v"(x1p[3]) STAMP_OUTS
        : "{v[64:71]}"(q[0]), "{v[72:79]}"(q[1]), "{v[80:87]}"(q[2]), "{v[88:95]}"(q[3]), "{v[144:147]}"(ones), "{v148}"(0xddddu), "{v149}"(kvoff), "{v150}"(kvoff + 4096),
          "{v[216:223]}"(cv), "{v224}"(lbase + L_W + 16), "{s[40:43]}"(srd), "{s47}"(L), "{s49}"(tm * 16384), "{s50}"(((tm + 1) & 15) * 16384), [xb] "s"(x0)
        : "v96", "v97", "v98", "v99", "v100", "v101", "v102", "v103", "v104", "v105", "v106", "v107", "v108", "v109", "v110", "v111", "v112", "v113", "v114", "v115", "v116", "v117", "v118", "v119", "v120", "v121", "v122", "v123", "v124", "v125", "v126", "v127", "v151", "v192", "v193", "v194", "v195", "v196", "v197", "v198", "v199", "v200", "v201", "v202", "v203", "v204", "v205", "v206", "v207", "v208", "v209", "v210", "v211", "v212", "v213", "v214", "v215", "s45", "s46", "s52", "s53", "s54", "s55", "vcc", "scc", "memory");
      STAMP_PUT(6 + 16 * L);
      STAMPV(9 + 16 * L);
      PHASE_VARS;
      const unsigned dst = OFF_OF + L * (5 * MBY) + (tm * 4 + 2 * kh) * TS + (tn * 2 + hh) * 2048 + lane * 16;
      auto put = [&](int u, const f32x16& o) {
#pragma unroll
        for (int c = 0; c < 2; ++c) {
          u32x4 w;
#pragma unroll
          for (int e = 0; e < 4; ++e) w[e] = pack2(o[8 * c + 2 * e], o[8 * c + 2 * e + 1]);
          stg(rs, dst + u * TS + c * 1024, w);
        }
      };
      const float la = lv[8] + lv[0], lb = lv[12] + lv[4];
      o2 = (o2 + o0) * RCP(la); put(0, o2);
      SBAR();
      o3 = (o3 + o1) * RCP(lb); put(1, o3);
      SBAR();
      {
        const f32x16 t = o2 + o3;
        float sum = 0.f;
#pragma unroll
        for (int e = 0; e < 16; e += 4) sum += (t[e] + t[e + 1]) + (t[e + 2] + t[e + 3]);
        if (__any(!(fabsf(sum) < 1e37f) | !(fmaxf(la, lb) < 1e27f) | !(fminf(la, lb) > 1e-9f) | !(la + lb == la + lb))) {
          DRAIN();
          attn_safe(rs, kvoff - kh * 8192, q[2], ones, o2); put(0, o2);
          attn_safe(rs, kvoff - kh * 8192, q[3], ones, o3); put(1, o3);
        }
      }
    }
    STAMPV(10 + 16 * L);
    {
      f32x16 acc[8];
      u32x16 wrc;
      u32x16 fl0, fl1, fl2;
      STAMP_DECL;
      { PHASE_VARS;
      float r0 = 0.f, r1 = 1.f;
      if (L == 0) {
#pragma unroll
        for (int i = 0; i < 4; ++i) x1p[i] = fo_swap(x1p[i]);
      } else {
        r0 = reinterpret_cast<const float*>(smem + L_AB)[256 + wid * 64 + r32]; r1 = reinterpret_cast<const float*>(smem + L_AB)[256 + wid * 64 + 32 + r32];
      }
#pragma unroll
      for (int e = 0; e < 16; ++e) { acc[e >> 3][2 * (e & 7)] = lo16(x1p[e >> 2][e & 3]) * r1 + r0; acc[e >> 3][2 * (e & 7) + 1] = hi16(x1p[e >> 2][e & 3]) * r1 + r0; }
      PIN(acc[0]); PIN(acc[1]);
      asm volatile("s_waitcnt vmcnt(0)\n\tds_write_b32 %0, %1" :: "v"(lbase + L_W + 80 + 4 * wid), "v"(2 * L + 2) : "memory");
      STAMPV(11 + 16 * L);
      const unsigned a0 = lbase + wrow * 128 + ((wch ^ wsw) << 4) + 8 * (tp & 1), a1 = lbase + wrow * 128 + (((4 + wch) ^ wsw) << 4) + 8 * (tp & 1);
      asm volatile(
        "s_mov_b32 s46, 0\n\t"
        "ostart_%=:\n\t"
        "s_mov_b32 s48, 0x200000\n\t"
        "2:\n\t"
        "ds_read_b32 v233, v232\n\t"
        "s_sub_u32 s48, s48, 1\n\t"
        "s_waitcnt lgkmcnt(0)\n\t"
        "s_cselect_b32 s47, 15, 0\n\t"
        "v_readfirstlane_b32 s51, v233\n\t"
        "s_nop 3\n\t"
        "s_or_b32 s51, s51, s47\n\t"
        "s_andn2_b32 s47, s51, s46\n\t"
        "s_cbranch_scc0 2b\n\t"
        "s_andn2_b32 s51, s51, s46\n\t"
        "s_ff1_i32_b32 s47, s51\n\t"
        "s_add_u32 s45, s47, s49\n\t"
        "s_and_b32 s45, s45, 3\n\t"
        "s_lshl_b32 s45, s45, 14\n\t"
        "s_and_b32 s51, s47, 0x80000000\n\t"
        "v_or_b32 v225, s51, v224\n\t"
        "s_and_b32 s51, s47, 3\n\t"
        "s_lshl_b32 s51, s51, 15\n\t"
        "v_add_u32 v230, s51, v226\n\t"
        "v_add_u32 v231, s51, v227\n\t"
        "buffer_load_dwordx4 v[160:163], v225, s[40:43], s45 offen offset:0 sc1\n\t"
        "buffer_load_dwordx4 v[164:167], v225, s[40:43], s45 offen offset:1024 sc1\n\t"
        "buffer_load_dwordx4 v[168:171], v225, s[40:43], s45 offen offset:2048 sc1\n\t"
        "buffer_load_dwordx4 v[172:175], v225, s[40:43], s45 offen offset:3072 sc1\n\t"
        "s_add_u32 s45, s45, 0x1000\n\t"
        "buffer_load_dwordx4 v[176:179], v225, s[40:43], s45 offen offset:0 sc1\n\t"
        "buffer_load_dwordx4 v[180:183], v225, s[40:43], s45 offen offset:1024 sc1\n\t"
        "buffer_load_dwordx4 v[184:187], v225, s[40:43], s45 offen offset:2048 sc1\n\t"
        "buffer_load_dwordx4 v[188:191], v225, s[40:43], s45 offen offset:3072 sc1\n\t"
        "s_add_u32 s45, s45, 0x1000\n\t"
        "buffer_load_dwordx4 v[192:195], v225, s[40:43], s45 offen offset:0 sc1\n\t"
        "buffer_load_dwordx4 v[196:199], v225, s[40:43], s45 offen offset:1024 sc1\n\t"
        "buffer_load_dwordx4 v[200:203], v225, s[40:43], s45 offen offset:2048 sc1\n\t"
        "buffer_load_dwordx4 v[204:207], v225, s[40:43], s45 offen offset:3072 sc1\n\t"
        "ds_read_b64_tr_b16 v[128:129], v230 offset:0\n\t"
        "ds_read_b64_tr_b16 v[130:131], v230 offset:1024\n\t"
        "ds_read_b64_tr_b16 v[132:133], v231 offset:0\n\t"
        "ds_read_b64_tr_b16 v[134:135], v231 offset:1024\n\t"
        "ds_read_b64_tr_b16 v[136:137], v230 offset:2048\n\t"
        "ds_read_b64_tr_b16 v[138:139], v230 offset:3072\n\t"
        "ds_read_b64_tr_b16 v[140:141], v231 offset:2048\n\t"
        "ds_read_b64_tr_b16 v[142:143], v231 offset:3072\n\t"
        "ds_read_b64_tr_b16 v[144:145], v230 offset:4096\n\t"
        "ds_read_b64_tr_b16 v[146:147], v230 offset:5120\n\t"
        "ds_read_b64_tr_b16 v[148:149], v231 offset:4096\n\t"
        "ds_read_b64_tr_b16 v[150:151], v231 offset:5120\n\t"
        "ds_read_b64_tr_b16 v[152:153], v230 offset:6144\n\t"
        "ds_read_b64_tr_b16 v[154:155], v230 offset:7168\n\t"
        "ds_read_b64_tr_b16 v[156:157], v231 offset:6144\n\t"
        "ds_read_b64_tr_b16 v[158:159], v231 offset:7168\n\t"
        "s_mov_b32 s44, s45\n\t"
        "s_cmp_eq_u32 s47, 0\n\t"
        "s_cbranch_scc1 oseg0_%=\n\t"
        "s_cmp_eq_u32 s47, 1\n\t"
        "s_cbranch_scc1 oseg1_%=\n\t"
        "s_cmp_eq_u32 s47, 2\n\t"
        "s_cbranch_scc1 oseg2_%=\n\t"
        "s_cmp_eq_u32 s47, 3\n\t"
        "s_cbranch_scc1 oseg3_%=\n\t"
        "oseg0_%=:\n\t"
        "s_or_b32 s46, s46, 1\n\t"
        "s_waitcnt vmcnt(11) lgkmcnt(14)\n\t"
        "v_mfma_f32_32x32x16_bf16 v[0:15], v[128:131], v[160:163], v[0:15]\n\t"
        "ds_read_b64_tr_b16 v[128:129], v226 offset:8192\n\t"
        "ds_read_b64_tr_b16 v[130:131], v226 offset:9216\n\t"
        "s_waitcnt lgkmcnt(14)\n\t"
        "v_mfma_f32_32x32x16_bf16 v[16:31], v[132:135], v[160:163], v[16:31]\n\t"
        "ds_read_b64_tr_b16 v[132:133], v227 offset:8192\n\t"
        "ds_read_b64_tr_b16 v[134:135], v227 offset:9216\n\t"
        "s_add_u32 s44, s44, 0x1000\n\t"
        "buffer_load_dwordx4 v[208:211], v224, s[40:43], s44 offen offset:0 sc1\n\t"
        "s_waitcnt vmcnt(11) lgkmcnt(14)\n\t"
        "v_mfma_f32_32x32x16_bf16 v[0:15], v[136:139], v[164:167], v[0:15]\n\t"
        "ds_read_b32 v233, v232\n\t"
        "ds_read_b64_tr_b16 v[136:137], v226 offset:10240\n\t"
        "ds_read_b64_tr_b16 v[138:139], v226 offset:11264\n\t"
        "s_waitcnt lgkmcnt(14)\n\t"
        "v_mfma_f32_32x32x16_bf16 v[16:31], v[140:143], v[164:167], v[16:31]\n\t"
        "ds_read_b64_tr_b16 v[140:141], v227 offset:10240\n\t"
        "ds_read_b64_tr_b16 v[142:143], v227 offset:11264\n\t"
        "buffer_load_dwordx4 v[212:215], v224, s[40:43], s44 offen offset:1024 sc1\n\t"
        "s_waitcnt vmcnt(11) lgkmcnt(14)\n\t"
        "v_mfma_f32_32x32x16_bf16 v[0:15], v[144:147], v[168:171], v[0:15]\n\t"
        "ds_read_b64_tr_b16 v[144:145], v226 offset:12288\n\t"
        "ds_read_b64_tr_b16 v[146:147], v226 offset:13312\n\t"
        "s_waitcnt lgkmcnt(14)\n\t"
        "v_mfma_f32_32x32x16_bf16 v[16:31], v[148:151], v[168:171], v[16:31]\n\t"
        "ds_read_b64_tr_b16 v[148:149], v227 offset:12288\n\t"
        "ds_read_b64_tr_b16 v[150:151], v227 offset:13312\n\t"
        "buffer_load_dwordx4 v[216:219], v224, s[40:43], s44 offen offset:2048 sc1\n\t"
        "s_waitcnt vmcnt(11) lgkmcnt(8)\n\t"
        "v_mfma_f32_32x32x16_bf16 v[0:15], v[152:155], v[172:175], v[0:15]\n\t"
        "v_readfirstlane_b32 s51, v233\n\t"
        "ds_read_b64_tr_b16 v[152:153], v226 offset:14336\n\t"
        "ds_read_b64_tr_b16 v[154:155], v226 offset:15360\n\t"
        "s_waitcnt lgkmcnt(14)\n\t"
        "v_mfma_f32_32x32x16_bf16 v[16:31], v[156:159], v[172:175], v[16:31]\n\t"
        "s_andn2_b32 s51, s51, s46\n\t"
        "s_ff1_i32_b32 s47, s51\n\t"
        "s_add_u32 s45, s47, s49\n\t"
        "s_and_b32 s45, s45, 3\n\t"
        "s_lshl_b32 s45, s45, 14\n\t"
        "ds_read_b64_tr_b16 v[156:157], v227 offset:14336\n\t"
        "ds_read_b64_tr_b16 v[158:159], v227 offset:15360\n\t"
        "buffer_load_dwordx4 v[220:223], v224, s[40:43], s44 offen offset:3072 sc1\n\t"
        "s_waitcnt vmcnt(11) lgkmcnt(14)\n\t"
        "v_mfma_f32_32x32x16_bf16 v[0:15], v[128:131], v[176:179], v[0:15]\n\t"
        "s_and_b32 s51, s47, 0x80000000\n\t"
        "v_or_b32 v225, s51, v224\n\t"
        "s_and_b32 s51, s47, 3\n\t"
        "s_lshl_b32 s51, s51, 15\n\t"
        "v_add_u32 v230, s51, v226\n\t"
        "v_add_u32 v231, s51, v227\n\t"
        "ds_read_b64_tr_b16 v[128:129], v226 offset:16384\n\t"
        "ds_read_b64_tr_b16 v[130:131], v226 offset:17408\n\t"
        "s_waitcnt lgkmcnt(14)\n\t"
        "v_mfma_f32_32x32x16_bf16 v[16:31], v[132:135], v[176:179], v[16:31]\n\t"
        "ds_read_b64_tr_b16 v[132:133], v227 offset:16384\n\t"
        "ds_read_b64_tr_b16 v[134:135], v227 offset:17408\n\t"
        "buffer_load_dwordx4 v[160:163], v225, s[40:43], s45 offen offset:0 sc1\n\t"
        "s_waitcnt vmcnt(11) lgkmcnt(14)\n\t"
        "v_mfma_f32_32x32x16_bf16 v[0:15], v[136:139], v[180:183], v[0:15]\n\t"
        "ds_read_b64_tr_b16 v[136:137], v226 offset:18432\n\t"
        "ds_read_b64_tr_b16 v[138:139], v226 offset:19456\n\t"
        "s_waitcnt lgkmcnt(14)\n\t"
        "v_mfma_f32_32x32x16_bf16 v[16:31], v[140:143], v[180:183], v[16:31]\n\t"
        "ds_read_b64_tr_b16 v[140:141], v227 offset:18432\n\t"
        "ds_read_b64_tr_b16 v[142:143], v227 offset:19456\n\t"
        "buffer_load_dwordx4 v[164:167], v225, s[40:43], s45 offen offset:1024 sc1\n\t"
        "s_waitcnt vmcnt(11) lgkmcnt(14)\n\t"
        "v_mfma_f32_32x32x16_bf16 v[0:15], v[144:147], v[184:187], v[0:15]\n\t"
        "ds_read_b64_tr_b16 v[144:145], v226 offset:20480\n\t"
        "ds_read_b64_tr_b16 v[146:147], v226 offset:21504\n\t"
        "s_waitcnt lgkmcnt(14)\n\t"
        "v_mfma_f32_32x32x16_bf16 v[16:31], v[148:151], v[184:187], v[16:31]\n\t"
        "ds_read_b64_tr_b16 v[148:149], v227 offset:20480\n\t"
        "ds_read_b64_tr_b16 v[150:151], v227 offset:21504\n\t"
        "buffer_load_dwordx4 v[168:171], v225, s[40:43], s45 offen offset:2048 sc1\n\t"
        "s_waitcnt vmcnt(11) lgkmcnt(14)\n\t"
        "v_mfma_f32_32x32x16_bf16 v[0:15], v[152:155], v[188:191], v[0:15]\n\t"
        "ds_read_b64_tr_b16 v[152:153], v226 offset:22528\n\t"
        "ds_read_b64_tr_b16 v[154:155], v226 offset:23552\n\t"
        "s_waitcnt lgkmcnt(14)\n\t"
        "v_mfma_f32_32x32x16_bf16 v[16:31], v[156:159], v[188:191], v[16:31]\n\t"
        "ds_read_b64_tr_b16 v[156:157], v227 offset:22528\n\t"
        "ds_read_b64_tr_b16 v[158:159], v227 offset:23552\n\t"
        "buffer_load_dwordx4 v[172:175], v225, s[40:43], s45 offen offset:3072 sc1\n\t"
        "s_waitcnt vmcnt(11) lgkmcnt(14)\n\t"
        "v_mfma_f32_32x32x16_bf16 v[0:15], v[128:131], v[192:195], v[0:15]\n\t"
        "ds_read_b64_tr_b16 v[128:129], v226 offset:24576\n\t"
        "ds_read_b64_tr_b16 v[130:131], v226 offset:25600\n\t"
        "s_waitcnt lgkmcnt(14)\n\t"
        "v_mfma_f32_32x32x16_bf16 v[16:31], v[132:135], v[192:195], v[16:31]\n\t"
        "ds_read_b64_tr_b16 v[132:133], v227 offset:24576\n\t"
        "ds_read_b64_tr_b16 v[134:135], v227 offset:25600\n\t"
        "s_add_u32 s45, s45, 0x1000\n\t"
        "buffer_load_dwordx4 v[176:179], v225, s[40:43], s45 offen offset:0 sc1\n\t"
        "s_waitcnt vmcnt(11) lgkmcnt(14)\n\t"
        "v_mfma_f32_32x32x16_bf16 v[0:15], v[136:139], v[196:199], v[0:15]\n\t"
        "ds_read_b64_tr_b16 v[136:137], v226 offset:26624\n\t"
        "ds_read_b64_tr_b16 v[138:139], v226 offset:27648\n\t"
        "s_waitcnt lgkmcnt(14)\n\t"
        "v_mfma_f32_32x32x16_bf16 v[16:31], v[140:143], v[196:199], v[16:31]\n\t"
        "ds_read_b64_tr_b16 v[140:141], v227 offset:26624\n\t"
        "ds_read_b64_tr_b16 v[142:143], v227 offset:27648\n\t"
        "buffer_load_dwordx4 v[180:183], v225, s[40:43], s45 offen offset:1024 sc1\n\t"
        "s_waitcnt vmcnt(11) lgkmcnt(14)\n\t"
        "v_mfma_f32_32x32x16_bf16 v[0:15], v[144:147], v[200:203], v[0:15]\n\t"
        "ds_read_b64_tr_b16 v[144:145], v226 offset:28672\n\t"
        "ds_read_b64_tr_b16 v[146:147], v226 offset:29696\n\t"
        "s_waitcnt lgkmcnt(14)\n\t"
        "v_mfma_f32_32x32x16_bf16 v[16:31], v[148:151], v[200:203], v[16:31]\n\t"
        "ds_read_b64_tr_b16 v[148:149], v227 offset:28672\n\t"
        "ds_read_b64_tr_b16 v[150:151], v227 offset:29696\n\t"
        "buffer_load_dwordx4 v[184:187], v225, s[40:43], s45 offen offset:2048 sc1\n\t"
        "s_waitcnt vmcnt(11) lgkmcnt(14)\n\t"
        "v_mfma_f32_32x32x16_bf16 v[0:15], v[152:155], v[204:207], v[0:15]\n\t"
        "ds_read_b64_tr_b16 v[152:153], v226 offset:30720\n\t"
        "ds_read_b64_tr_b16 v[154:155], v226 offset:31744\n\t"
        "s_waitcnt lgkmcnt(14)\n\t"
        "v_mfma_f32_32x32x16_bf16 v[16:31], v[156:159], v[204:207], v[16:31]\n\t"
        "ds_read_b64_tr_b16 v[156:157], v227 offset:30720\n\t"
        "ds_read_b64_tr_b16 v[158:159], v227 offset:31744\n\t"
        "buffer_load_dwordx4 v[188:191], v225, s[40:43], s45 offen offset:3072 sc1\n\t"
        "s_waitcnt vmcnt(11) lgkmcnt(14)\n\t"
        "v_mfma_f32_32x32x16_bf16 v[0:15], v[128:131], v[208:211], v[0:15]\n\t"
        "ds_read_b64_tr_b16 v[128:129], v230 offset:0\n\t"
        "ds_read_b64_tr_b16 v[130:131], v230 offset:1024\n\t"
        "s_waitcnt lgkmcnt(14)\n\t"
        "v_mfma_f32_32x32x16_bf16 v[16:31], v[132:135], v[208:211], v[16:31]\n\t"
        "ds_read_b64_tr_b16 v[132:133], v231 offset:0\n\t"
        "ds_read_b64_tr_b16 v[134:135], v231 offset:1024\n\t"
        "s_add_u32 s45, s45, 0x1000\n\t"
        "buffer_load_dwordx4 v[192:195], v225, s[40:43], s45 offen offset:0 sc1\n\t"
        "s_waitcnt vmcnt(11) lgkmcnt(14)\n\t"
        "v_mfma_f32_32x32x16_bf16 v[0:15], v[136:139], v[212:215], v[0:15]\n\t"
        "ds_read_b64_tr_b16 v[136:137], v230 offset:2048\n\t"
        "ds_read_b64_tr_b16 v[138:139], v230 offset:3072\n\t"
        "s_waitcnt lgkmcnt(14)\n\t"
        "v_mfma_f32_32x32x16_bf16 v[16:31], v[140:143], v[212:215], v[16:31]\n\t"
        "ds_read_b64_tr_b16 v[140:141], v231 offset:2048\n\t"
        "ds_read_b64_tr_b16 v[142:143], v231 offset:3072\n\t"
        "buffer_load_dwordx4 v[196:199], v225, s[40:43], s45 offen offset:1024 sc1\n\t"
        "s_waitcnt vmcnt(11) lgkmcnt(14)\n\t"
        "v_mfma_f32_32x32x16_bf16 v[0:15], v[144:147], v[216:219], v[0:15]\n\t"
        "ds_read_b64_tr_b16 v[144:145], v230 offset:4096\n\t"
        "ds_read_b64_tr_b16 v[146:147], v230 offset:5120\n\t"
        "s_waitcnt lgkmcnt(14)\n\t"
        "v_mfma_f32_32x32x16_bf16 v[16:31], v[148:151], v[216:219], v[16:31]\n\t"
        "ds_read_b64_tr_b16 v[148:149], v231 offset:4096\n\t"
        "ds_read_b64_tr_b16 v[150:151], v231 offset:5120\n\t"
        "buffer_load_dwordx4 v[200:203], v225, s[40:43], s45 offen offset:2048 sc1\n\t"
        "s_waitcnt vmcnt(11) lgkmcnt(14)\n\t"
        "v_mfma_f32_32x32x16_bf16 v[0:15], v[152:155], v[220:223], v[0:15]\n\t"
        "ds_read_b64_tr_b16 v[152:153], v230 offset:6144\n\t"
        "ds_read_b64_tr_b16 v[154:155], v230 offset:7168\n\t"
        "s_waitcnt lgkmcnt(14)\n\t"
        "v_mfma_f32_32x32x16_bf16 v[16:31], v[156:159], v[220:223], v[16:31]\n\t"
        "ds_read_b64_tr_b16 v[156:157], v231 offset:6144\n\t"
        "ds_read_b64_tr_b16 v[158:159], v231 offset:7168\n\t"
        "buffer_load_dwordx4 v[204:207], v225, s[40:43], s45 offen offset:3072 sc1\n\t"
        "s_mov_b32 s44, s45\n\t"
        "s_cmp_eq_u32 s47, 0\n\t"
        "s_cbranch_scc1 oseg0_%=\n\t"
        "s_cmp_eq_u32 s47, 1\n\t"
        "s_cbranch_scc1 oseg1_%=\n\t"
        "s_cmp_eq_u32 s47, 2\n\t"
        "s_cbranch_scc1 oseg2_%=\n\t"
        "s_cmp_eq_u32 s47, 3\n\t"
        "s_cbranch_scc1 oseg3_%=\n\t"
        "s_cmp_lg_u32 s46, 15\n\t"
        "s_cbranch_scc1 ostart_%=\n\t"
        "s_branch oend_%=\n\t"
        "oseg1_%=:\n\t"
        "s_or_b32 s46, s46, 2\n\t"
        "s_waitcnt vmcnt(11) lgkmcnt(14)\n\t"
        "v_mfma_f32_32x32x16_bf16 v[32:47], v[128:131], v[160:163], 0\n\t"
        "ds_read_b64_tr_b16 v[128:129], v226 offset:40960\n\t"
        "ds_read_b64_tr_b16 v[130:131], v226 offset:41984\n\t"
        "s_waitcnt lgkmcnt(14)\n\t"
        "v_mfma_f32_32x32x16_bf16 v[48:63], v[132:135], v[160:163], 0\n\t"
        "ds_read_b64_tr_b16 v[132:133], v227 offset:40960\n\t"
        "ds_read_b64_tr_b16 v[134:135], v227 offset:41984\n\t"
        "s_add_u32 s44, s44, 0x1000\n\t"
        "buffer_load_dwordx4 v[208:211], v224, s[40:43], s44 offen offset:0 sc1\n\t"
        "s_waitcnt vmcnt(11) lgkmcnt(14)\n\t"
        "v_mfma_f32_32x32x16_bf16 v[32:47], v[136:139], v[164:167], v[32:47]\n\t"
        "ds_read_b32 v233, v232\n\t"
        "ds_read_b64_tr_b16 v[136:137], v226 offset:43008\n\t"
        "ds_read_b64_tr_b16 v[138:139], v226 offset:44032\n\t"
        "s_waitcnt lgkmcnt(14)\n\t"
        "v_mfma_f32_32x32x16_bf16 v[48:63], v[140:143], v[164:167], v[48:63]\n\t"
        "ds_read_b64_tr_b16 v[140:141], v227 offset:43008\n\t"
        "ds_read_b64_tr_b16 v[142:143], v227 offset:44032\n\t"
        "buffer_load_dwordx4 v[212:215], v224, s[40:43], s44 offen offset:1024 sc1\n\t"
        "s_waitcnt vmcnt(11) lgkmcnt(14)\n\t"
        "v_mfma_f32_32x32x16_bf16 v[32:47], v[144:147], v[168:171], v[32:47]\n\t"
        "ds_read_b64_tr_b16 v[144:145], v226 offset:45056\n\t"
        "ds_read_b64_tr_b16 v[146:147], v226 offset:46080\n\t"
        "s_waitcnt lgkmcnt(14)\n\t"
        "v_mfma_f32_32x32x16_bf16 v[48:63], v[148:151], v[168:171], v[48:63]\n\t"
        "ds_read_b64_tr_b16 v[148:149], v227 offset:45056\n\t"
        "ds_read_b64_tr_b16 v[150:151], v227 offset:46080\n\t"
        "buffer_load_dwordx4 v[216:219], v224, s[40:43], s44 offen offset:2048 sc1\n\t"
        "s_waitcnt vmcnt(11) lgkmcnt(8)\n\t"
        "v_mfma_f32_32x32x16_bf16 v[32:47], v[152:155], v[172:175], v[32:47]\n\t"
        "v_readfirstlane_b32 s51, v233\n\t"
        "ds_read_b64_tr_b16 v[152:153], v226 offset:47104\n\t"
        "ds_read_b64_tr_b16 v[154:155], v226 offset:48128\n\t"
        "s_waitcnt lgkmcnt(14)\n\t"
        "v_mfma_f32_32x32x16_bf16 v[48:63], v[156:159], v[172:175], v[48:63]\n\t"
        "s_andn2_b32 s51, s51, s46\n\t"
        "s_ff1_i32_b32 s47, s51\n\t"
        "s_add_u32 s45, s47, s49\n\t"
        "s_and_b32 s45, s45, 3\n\t"
        "s_lshl_b32 s45, s45, 14\n\t"
        "ds_read_b64_tr_b16 v[156:157], v227 offset:47104\n\t"
        "ds_read_b64_tr_b16 v[158:159], v227 offset:48128\n\t"
        "buffer_load_dwordx4 v[220:223], v224, s[40:43], s44 offen offset:3072 sc1\n\t"
        "s_waitcnt vmcnt(11) lgkmcnt(14)\n\t"
        "v_mfma_f32_32x32x16_bf16 v[32:47], v[128:131], v[176:179], v[32:47]\n\t"
        "s_and_b32 s51, s47, 0x80000000\n\t"
        "v_or_b32 v225, s51, v224\n\t"
        "s_and_b32 s51, s47, 3\n\t"
        "s_lshl_b32 s51, s51, 15\n\t"
        "v_add_u32 v230, s51, v226\n\t"
        "v_add_u32 v231, s51, v227\n\t"
        "ds_read_b64_tr_b16 v[128:129], v226 offset:49152\n\t"
        "ds_read_b64_tr_b16 v[130:131], v226 offset:50176\n\t"
        "s_waitcnt lgkmcnt(14)\n\t"
        "v_mfma_f32_32x32x16_bf16 v[48:63], v[132:135], v[176:179], v[48:63]\n\t"
        "ds_read_b64_tr_b16 v[132:133], v227 offset:49152\n\t"
        "ds_read_b64_tr_b16 v[134:135], v227 offset:50176\n\t"
        "buffer_load_dwordx4 v[160:163], v225, s[40:43], s45 offen offset:0 sc1\n\t"
        "s_waitcnt vmcnt(11) lgkmcnt(14)\n\t"
        "v_mfma_f32_32x32x16_bf16 v[32:47], v[136:139], v[180:183], v[32:47]\n\t"
        "ds_read_b64_tr_b16 v[136:137], v226 offset:51200\n\t"
        "ds_read_b64_tr_b16 v[138:139], v226 offset:52224\n\t"
        "s_waitcnt lgkmcnt(14)\n\t"
        "v_mfma_f32_32x32x16_bf16 v[48:63], v[140:143], v[180:183], v[48:63]\n\t"
        "ds_read_b64_tr_b16 v[140:141], v227 offset:51200\n\t"
        "ds_read_b64_tr_b16 v[142:143], v227 offset:52224\n\t"
        "buffer_load_dwordx4 v[164:167], v225, s[40:43], s45 offen offset:1024 sc1\n\t"
        "s_waitcnt vmcnt(11) lgkmcnt(14)\n\t"
        "v_mfma_f32_32x32x16_bf16 v[32:47], v[144:147], v[184:187], v[32:47]\n\t"
        "ds_read_b64_tr_b16 v[144:145], v226 offset:53248\n\t"
        "ds_read_b64_tr_b16 v[146:147], v226 offset:54272\n\t"
        "s_waitcnt lgkmcnt(14)\n\t"
        "v_mfma_f32_32x32x16_bf16 v[48:63], v[148:151], v[184:187], v[48:63]\n\t"
        "ds_read_b64_tr_b16 v[148:149], v227 offset:53248\n\t"
        "ds_read_b64_tr_b16 v[150:151], v227 offset:54272\n\t"
        "buffer_load_dwordx4 v[168:171], v225, s[40:43], s45 offen offset:2048 sc1\n\t"
        "s_waitcnt vmcnt(11) lgkmcnt(14)\n\t"
        "v_mfma_f32_32x32x16_bf16 v[32:47], v[152:155], v[188:191], v[32:47]\n\t"
        "ds_read_b64_tr_b16 v[152:153], v226 offset:55296\n\t"
        "ds_read_b64_tr_b16 v[154:155], v226 offset:56320\n\t"
        "s_waitcnt lgkmcnt(14)\n\t"
        "v_mfma_f32_32x32x16_bf16 v[48:63], v[156:159], v[188:191], v[48:63]\n\t"
        "ds_read_b64_tr_b16 v[156:157], v227 offset:55296\n\t"
        "ds_read_b64_tr_b16 v[158:159], v227 offset:56320\n\t"
        "buffer_load_dwordx4 v[172:175], v225, s[40:43], s45 offen offset:3072 sc1\n\t"
        "s_waitcnt vmcnt(11) lgkmcnt(14)\n\t"
        "v_mfma_f32_32x32x16_bf16 v[32:47], v[128:131], v[192:195], v[32:47]\n\t"
        "ds_read_b64_tr_b16 v[128:129], v226 offset:57344\n\t"
        "ds_read_b64_tr_b16 v[130:131], v226 offset:58368\n\t"
        "s_waitcnt lgkmcnt(14)\n\t"
        "v_mfma_f32_32x32x16_bf16 v[48:63], v[132:135], v[192:195], v[48:63]\n\t"
        "ds_read_b64_tr_b16 v[132:133], v227 offset:57344\n\t"
        "ds_read_b64_tr_b16 v[134:135], v227 offset:58368\n\t"
        "s_add_u32 s45, s45, 0x1000\n\t"
        "buffer_load_dwordx4 v[176:179], v225, s[40:43], s45 offen offset:0 sc1\n\t"
        "s_waitcnt vmcnt(11) lgkmcnt(14)\n\t"
        "v_mfma_f32_32x32x16_bf16 v[32:47], v[136:139], v[196:199], v[32:47]\n\t"
        "ds_read_b64_tr_b16 v[136:137], v226 offset:59392\n\t"
        "ds_read_b64_tr_b16 v[138:139], v226 offset:60416\n\t"
        "s_waitcnt lgkmcnt(14)\n\t"
        "v_mfma_f32_32x32x16_bf16 v[48:63], v[140:143], v[196:199], v[48:63]\n\t"
        "ds_read_b64_tr_b16 v[140:141], v227 offset:59392\n\t"
        "ds_read_b64_tr_b16 v[142:143], v227 offset:60416\n\t"
        "buffer_load_dwordx4 v[180:183], v225, s[40:43], s45 offen offset:1024 sc1\n\t"
        "s_waitcnt vmcnt(11) lgkmcnt(14)\n\t"
        "v_mfma_f32_32x32x16_bf16 v[32:47], v[144:147], v[200:203], v[32:47]\n\t"
        "ds_read_b64_tr_b16 v[144:145], v226 offset:61440\n\t"
        "ds_read_b64_tr_b16 v[146:147], v226 offset:62464\n\t"
        "s_waitcnt lgkmcnt(14)\n\t"
        "v_mfma_f32_32x32x16_bf16 v[48:63], v[148:151], v[200:203], v[48:63]\n\t"
        "ds_read_b64_tr_b16 v[148:149], v227 offset:61440\n\t"
        "ds_read_b64_tr_b16 v[150:151], v227 offset:62464\n\t"
        "buffer_load_dwordx4 v[184:187], v225, s[40:43], s45 offen offset:2048 sc1\n\t"
        "s_waitcnt vmcnt(11) lgkmcnt(14)\n\t"
        "v_mfma_f32_32x32x16_bf16 v[32:47], v[152:155], v[204:207], v[32:47]\n\t"
        "ds_read_b64_tr_b16 v[152:153], v226 offset:63488\n\t"
        "ds_read_b64_tr_b16 v[154:155], v226 offset:64512\n\t"
        "s_waitcnt lgkmcnt(14)\n\t"
        "v_mfma_f32_32x32x16_bf16 v[48:63], v[156:159], v[204:207], v[48:63]\n\t"
        "ds_read_b64_tr_b16 v[156:157], v227 offset:63488\n\t"
        "ds_read_b64_tr_b16 v[158:159], v227 offset:64512\n\t"
        "buffer_load_dwordx4 v[188:191], v225, s[40:43], s45 offen offset:3072 sc1\n\t"
        "s_waitcnt vmcnt(11) lgkmcnt(14)\n\t"
        "v_mfma_f32_32x32x16_bf16 v[32:47], v[128:131], v[208:211], v[32:47]\n\t"
        "ds_read_b64_tr_b16 v[128:129], v230 offset:0\n\t"
        "ds_read_b64_tr_b16 v[130:131], v230 offset:1024\n\t"
        "s_waitcnt lgkmcnt(14)\n\t"
        "v_mfma_f32_32x32x16_bf16 v[48:63], v[132:135], v[208:211], v[48:63]\n\t"
        "ds_read_b64_tr_b16 v[132:133], v231 offset:0\n\t"
        "ds_read_b64_tr_b16 v[134:135], v231 offset:1024\n\t"
        "s_add_u32 s45, s45, 0x1000\n\t"
        "buffer_load_dwordx4 v[192:195], v225, s[40:43], s45 offen offset:0 sc1\n\t"
        "s_waitcnt vmcnt(11) lgkmcnt(14)\n\t"
        "v_mfma_f32_32x32x16_bf16 v[32:47], v[136:139], v[212:215], v[32:47]\n\t"
        "ds_read_b64_tr_b16 v[136:137], v230 offset:2048\n\t"
        "ds_read_b64_tr_b16 v[138:139], v230 offset:3072\n\t"
        "s_waitcnt lgkmcnt(14)\n\t"
        "v_mfma_f32_32x32x16_bf16 v[48:63], v[140:143], v[212:215], v[48:63]\n\t"
        "ds_read_b64_tr_b16 v[140:141], v231 offset:2048\n\t"
        "ds_read_b64_tr_b16 v[142:143], v231 offset:3072\n\t"
        "buffer_load_dwordx4 v[196:199], v225, s[40:43], s45 offen offset:1024 sc1\n\t"
        "s_waitcnt vmcnt(11) lgkmcnt(14)\n\t"
        "v_mfma_f32_32x32x16_bf16 v[32:47], v[144:147], v[216:219], v[32:47]\n\t"
        "ds_read_b64_tr_b16 v[144:145], v230 offset:4096\n\t"
        "ds_read_b64_tr_b16 v[146:147], v230 offset:5120\n\t"
        "s_waitcnt lgkmcnt(14)\n\t"
        "v_mfma_f32_32x32x16_bf16 v[48:63], v[148:151], v[216:219], v[48:63]\n\t"
        "ds_read_b64_tr_b16 v[148:149], v231 offset:4096\n\t"
        "ds_read_b64_tr_b16 v[150:151], v231 offset:5120\n\t"
        "buffer_load_dwordx4 v[200:203], v225, s[40:43], s45 offen offset:2048 sc1\n\t"
        "s_waitcnt vmcnt(11) lgkmcnt(14)\n\t"
        "v_mfma_f32_32x32x16_bf16 v[32:47], v[152:155], v[220:223], v[32:47]\n\t"
        "ds_read_b64_tr_b16 v[152:153], v230 offset:6144\n\t"
        "ds_read_b64_tr_b16 v[154:155], v230 offset:7168\n\t"
        "s_waitcnt lgkmcnt(14)\n\t"
        "v_mfma_f32_32x32x16_bf16 v[48:63], v[156:159], v[220:223], v[48:63]\n\t"
        "ds_read_b64_tr_b16 v[156:157], v231 offset:6144\n\t"
        "ds_read_b64_tr_b16 v[158:159], v231 offset:7168\n\t"
        "buffer_load_dwordx4 v[204:207], v225, s[40:43], s45 offen offset:3072 sc1\n\t"
        "s_mov_b32 s44, s45\n\t"
        "s_cmp_eq_u32 s47, 0\n\t"
        "s_cbranch_scc1 oseg0_%=\n\t"
        "s_cmp_eq_u32 s47, 1\n\t"
        "s_cbranch_scc1 oseg1_%=\n\t"
        "s_cmp_eq_u32 s47, 2\n\t"
        "s_cbranch_scc1 oseg2_%=\n\t"
        "s_cmp_eq_u32 s47, 3\n\t"
        "s_cbranch_scc1 oseg3_%=\n\t"
        "s_cmp_lg_u32 s46, 15\n\t"
        "s_cbranch_scc1 ostart_%=\n\t"
        "s_branch oend_%=\n\t"
        "oseg2_%=:\n\t"
        "s_or_b32 s46, s46, 4\n\t"
        "s_waitcnt vmcnt(11) lgkmcnt(14)\n\t"
        "v_mfma_f32_32x32x16_bf16 v[64:79], v[128:131], v[160:163], 0\n\t"
        "ds_read_b64_tr_b16 v[128:129], v228 offset:8192\n\t"
        "ds_read_b64_tr_b16 v[130:131], v228 offset:9216\n\t"
        "s_waitcnt lgkmcnt(14)\n\t"
        "v_mfma_f32_32x32x16_bf16 v[80:95], v[132:135], v[160:163], 0\n\t"
        "ds_read_b64_tr_b16 v[132:133], v229 offset:8192\n\t"
        "ds_read_b64_tr_b16 v[134:135], v229 offset:9216\n\t"
        "s_add_u32 s44, s44, 0x1000\n\t"
        "buffer_load_dwordx4 v[208:211], v224, s[40:43], s44 offen offset:0 sc1\n\t"
        "s_waitcnt vmcnt(11) lgkmcnt(14)\n\t"
        "v_mfma_f32_32x32x16_bf16 v[64:79], v[136:139], v[164:167], v[64:79]\n\t"
        "ds_read_b32 v233, v232\n\t"
        "ds_read_b64_tr_b16 v[136:137], v228 offset:10240\n\t"
        "ds_read_b64_tr_b16 v[138:139], v228 offset:11264\n\t"
        "s_waitcnt lgkmcnt(14)\n\t"
        "v_mfma_f32_32x32x16_bf16 v[80:95], v[140:143], v[164:167], v[80:95]\n\t"
        "ds_read_b64_tr_b16 v[140:141], v229 offset:10240\n\t"
        "ds_read_b64_tr_b16 v[142:143], v229 offset:11264\n\t"
        "buffer_load_dwordx4 v[212:215], v224, s[40:43], s44 offen offset:1024 sc1\n\t"
        "s_waitcnt vmcnt(11) lgkmcnt(14)\n\t"
        "v_mfma_f32_32x32x16_bf16 v[64:79], v[144:147], v[168:171], v[64:79]\n\t"
        "ds_read_b64_tr_b16 v[144:145], v228 offset:12288\n\t"
        "ds_read_b64_tr_b16 v[146:147], v228 offset:13312\n\t"
        "s_waitcnt lgkmcnt(14)\n\t"
        "v_mfma_f32_32x32x16_bf16 v[80:95], v[148:151], v[168:171], v[80:95]\n\t"
        "ds_read_b64_tr_b16 v[148:149], v229 offset:12288\n\t"
        "ds_read_b64_tr_b16 v[150:151], v229 offset:13312\n\t"
        "buffer_load_dwordx4 v[216:219], v224, s[40:43], s44 offen offset:2048 sc1\n\t"
        "s_waitcnt vmcnt(11) lgkmcnt(8)\n\t"
        "v_mfma_f32_32x32x16_bf16 v[64:79], v[152:155], v[172:175], v[64:79]\n\t"
        "v_readfirstlane_b32 s51, v233\n\t"
        "ds_read_b64_tr_b16 v[152:153], v228 offset:14336\n\t"
        "ds_read_b64_tr_b16 v[154:155], v228 offset:15360\n\t"
        "s_waitcnt lgkmcnt(14)\n\t"
        "v_mfma_f32_32x32x16_bf16 v[80:95], v[156:159], v[172:175], v[80:95]\n\t"
        "s_andn2_b32 s51, s51, s46\n\t"
        "s_ff1_i32_b32 s47, s51\n\t"
        "s_add_u32 s45, s47, s49\n\t"
        "s_and_b32 s45, s45, 3\n\t"
        "s_lshl_b32 s45, s45, 14\n\t"
        "ds_read_b64_tr_b16 v[156:157], v229 offset:14336\n\t"
        "ds_read_b64_tr_b16 v[158:159], v229 offset:15360\n\t"
        "buffer_load_dwordx4 v[220:223], v224, s[40:43], s44 offen offset:3072 sc1\n\t"
        "s_waitcnt vmcnt(11) lgkmcnt(14)\n\t"
        "v_mfma_f32_32x32x16_bf16 v[64:79], v[128:131], v[176:179], v[64:79]\n\t"
        "s_and_b32 s51, s47, 0x80000000\n\t"
        "v_or_b32 v225, s51, v224\n\t"
        "s_and_b32 s51, s47, 3\n\t"
        "s_lshl_b32 s51, s51, 15\n\t"
        "v_add_u32 v230, s51, v226\n\t"
        "v_add_u32 v231, s51, v227\n\t"
        "ds_read_b64_tr_b16 v[128:129], v228 offset:16384\n\t"
        "ds_read_b64_tr_b16 v[130:131], v228 offset:17408\n\t"
        "s_waitcnt lgkmcnt(14)\n\t"
        "v_mfma_f32_32x32x16_bf16 v[80:95], v[132:135], v[176:179], v[80:95]\n\t"
        "ds_read_b64_tr_b16 v[132:133], v229 offset:16384\n\t"
        "ds_read_b64_tr_b16 v[134:135], v229 offset:17408\n\t"
        "buffer_load_dwordx4 v[160:163], v225, s[40:43], s45 offen offset:0 sc1\n\t"
        "s_waitcnt vmcnt(11) lgkmcnt(14)\n\t"
        "v_mfma_f32_32x32x16_bf16 v[64:79], v[136:139], v[180:183], v[64:79]\n\t"
        "ds_read_b64_tr_b16 v[136:137], v228 offset:18432\n\t"
        "ds_read_b64_tr_b16 v[138:139], v228 offset:19456\n\t"
        "s_waitcnt lgkmcnt(14)\n\t"
        "v_mfma_f32_32x32x16_bf16 v[80:95], v[140:143], v[180:183], v[80:95]\n\t"
        "ds_read_b64_tr_b16 v[140:141], v229 offset:18432\n\t"
        "ds_read_b64_tr_b16 v[142:143], v229 offset:19456\n\t"
        "buffer_load_dwordx4 v[164:167], v225, s[40:43], s45 offen offset:1024 sc1\n\t"
        "s_waitcnt vmcnt(11) lgkmcnt(14)\n\t"
        "v_mfma_f32_32x32x16_bf16 v[64:79], v[144:147], v[184:187], v[64:79]\n\t"
        "ds_read_b64_tr_b16 v[144:145], v228 offset:20480\n\t"
        "ds_read_b64_tr_b16 v[146:147], v228 offset:21504\n\t"
        "s_waitcnt lgkmcnt(14)\n\t"
        "v_mfma_f32_32x32x16_bf16 v[80:95], v[148:151], v[184:187], v[80:95]\n\t"
        "ds_read_b64_tr_b16 v[148:149], v229 offset:20480\n\t"
        "ds_read_b64_tr_b16 v[150:151], v229 offset:21504\n\t"
        "buffer_load_dwordx4 v[168:171], v225, s[40:43], s45 offen offset:2048 sc1\n\t"
        "s_waitcnt vmcnt(11) lgkmcnt(14)\n\t"
        "v_mfma_f32_32x32x16_bf16 v[64:79], v[152:155], v[188:191], v[64:79]\n\t"
        "ds_read_b64_tr_b16 v[152:153], v228 offset:22528\n\t"
        "ds_read_b64_tr_b16 v[154:155], v228 offset:23552\n\t"
        "s_waitcnt lgkmcnt(14)\n\t"
        "v_mfma_f32_32x32x16_bf16 v[80:95], v[156:159], v[188:191], v[80:95]\n\t"
        "ds_read_b64_tr_b16 v[156:157], v229 offset:22528\n\t"
        "ds_read_b64_tr_b16 v[158:159], v229 offset:23552\n\t"
        "buffer_load_dwordx4 v[172:175], v225, s[40:43], s45 offen offset:3072 sc1\n\t"
        "s_waitcnt vmcnt(11) lgkmcnt(14)\n\t"
        "v_mfma_f32_32x32x16_bf16 v[64:79], v[128:131], v[192:195], v[64:79]\n\t"
        "ds_read_b64_tr_b16 v[128:129], v228 offset:24576\n\t"
        "ds_read_b64_tr_b16 v[130:131], v228 offset:25600\n\t"
        "s_waitcnt lgkmcnt(14)\n\t"
        "v_mfma_f32_32x32x16_bf16 v[80:95], v[132:135], v[192:195], v[80:95]\n\t"
        "ds_read_b64_tr_b16 v[132:133], v229 offset:24576\n\t"
        "ds_read_b64_tr_b16 v[134:135], v229 offset:25600\n\t"
        "s_add_u32 s45, s45, 0x1000\n\t"
        "buffer_load_dwordx4 v[176:179], v225, s[40:43], s45 offen offset:0 sc1\n\t"
        "s_waitcnt vmcnt(11) lgkmcnt(14)\n\t"
        "v_mfma_f32_32x32x16_bf16 v[64:79], v[136:139], v[196:199], v[64:79]\n\t"
        "ds_read_b64_tr_b16 v[136:137], v228 offset:26624\n\t"
        "ds_read_b64_tr_b16 v[138:139], v228 offset:27648\n\t"
        "s_waitcnt lgkmcnt(14)\n\t"
        "v_mfma_f32_32x32x16_bf16 v[80:95], v[140:143], v[196:199], v[80:95]\n\t"
        "ds_read_b64_tr_b16 v[140:141], v229 offset:26624\n\t"
        "ds_read_b64_tr_b16 v[142:143], v229 offset:27648\n\t"
        "buffer_load_dwordx4 v[180:183], v225, s[40:43], s45 offen offset:1024 sc1\n\t"
        "s_waitcnt vmcnt(11) lgkmcnt(14)\n\t"
        "v_mfma_f32_32x32x16_bf16 v[64:79], v[144:147], v[200:203], v[64:79]\n\t"
        "ds_read_b64_tr_b16 v[144:145], v228 offset:28672\n\t"
        "ds_read_b64_tr_b16 v[146:147], v228 offset:29696\n\t"
        "s_waitcnt lgkmcnt(14)\n\t"
        "v_mfma_f32_32x32x16_bf16 v[80:95], v[148:151], v[200:203], v[80:95]\n\t"
        "ds_read_b64_tr_b16 v[148:149], v229 offset:28672\n\t"
        "ds_read_b64_tr_b16 v[150:151], v229 offset:29696\n\t"
        "buffer_load_dwordx4 v[184:187], v225, s[40:43], s45 offen offset:2048 sc1\n\t"
        "s_waitcnt vmcnt(11) lgkmcnt(14)\n\t"
        "v_mfma_f32_32x32x16_bf16 v[64:79], v[152:155], v[204:207], v[64:79]\n\t"
        "ds_read_b64_tr_b16 v[152:153], v228 offset:30720\n\t"
        "ds_read_b64_tr_b16 v[154:155], v228 offset:31744\n\t"
        "s_waitcnt lgkmcnt(14)\n\t"
        "v_mfma_f32_32x32x16_bf16 v[80:95], v[156:159], v[204:207], v[80:95]\n\t"
        "ds_read_b64_tr_b16 v[156:157], v229 offset:30720\n\t"
        "ds_read_b64_tr_b16 v[158:159], v229 offset:31744\n\t"
        "buffer_load_dwordx4 v[188:191], v225, s[40:43], s45 offen offset:3072 sc1\n\t"
        "s_waitcnt vmcnt(11) lgkmcnt(14)\n\t"
        "v_mfma_f32_32x32x16_bf16 v[64:79], v[128:131], v[208:211], v[64:79]\n\t"
        "ds_read_b64_tr_b16 v[128:129], v230 offset:0\n\t"
        "ds_read_b64_tr_b16 v[130:131], v230 offset:1024\n\t"
        "s_waitcnt lgkmcnt(14)\n\t"
        "v_mfma_f32_32x32x16_bf16 v[80:95], v[132:135], v[208:211], v[80:95]\n\t"
        "ds_read_b64_tr_b16 v[132:133], v231 offset:0\n\t"
        "ds_read_b64_tr_b16 v[134:135], v231 offset:1024\n\t"
        "s_add_u32 s45, s45, 0x1000\n\t"
        "buffer_load_dwordx4 v[192:195], v225, s[40:43], s45 offen offset:0 sc1\n\t"
        "s_waitcnt vmcnt(11) lgkmcnt(14)\n\t"
        "v_mfma_f32_32x32x16_bf16 v[64:79], v[136:139], v[212:215], v[64:79]\n\t"
        "ds_read_b64_tr_b16 v[136:137], v230 offset:2048\n\t"
        "ds_read_b64_tr_b16 v[138:139], v230 offset:3072\n\t"
        "s_waitcnt lgkmcnt(14)\n\t"
        "v_mfma_f32_32x32x16_bf16 v[80:95], v[140:143], v[212:215], v[80:95]\n\t"
        "ds_read_b64_tr_b16 v[140:141], v231 offset:2048\n\t"
        "ds_read_b64_tr_b16 v[142:143], v231 offset:3072\n\t"
        "buffer_load_dwordx4 v[196:199], v225, s[40:43], s45 offen offset:1024 sc1\n\t"
        "s_waitcnt vmcnt(11) lgkmcnt(14)\n\t"
        "v_mfma_f32_32x32x16_bf16 v[64:79], v[144:147], v[216:219], v[64:79]\n\t"
        "ds_read_b64_tr_b16 v[144:145], v230 offset:4096\n\t"
        "ds_read_b64_tr_b16 v[146:147], v230 offset:5120\n\t"
        "s_waitcnt lgkmcnt(14)\n\t"
        "v_mfma_f32_32x32x16_bf16 v[80:95], v[148:151], v[216:219], v[80:95]\n\t"
        "ds_read_b64_tr_b16 v[148:149], v231 offset:4096\n\t"
        "ds_read_b64_tr_b16 v[150:151], v231 offset:5120\n\t"
        "buffer_load_dwordx4 v[200:203], v225, s[40:43], s45 offen offset:2048 sc1\n\t"
        "s_waitcnt vmcnt(11) lgkmcnt(14)\n\t"
        "v_mfma_f32_32x32x16_bf16 v[64:79], v[152:155], v[220:223], v[64:79]\n\t"
        "ds_read_b64_tr_b16 v[152:153], v230 offset:6144\n\t"
        "ds_read_b64_tr_b16 v[154:155], v230 offset:7168\n\t"
        "s_waitcnt lgkmcnt(14)\n\t"
        "v_mfma_f32_32x32x16_bf16 v[80:95], v[156:159], v[220:223], v[80:95]\n\t"
        "ds_read_b64_tr_b16 v[156:157], v231 offset:6144\n\t"
        "ds_read_b64_tr_b16 v[158:159], v231 offset:7168\n\t"
        "buffer_load_dwordx4 v[204:207], v225, s[40:43], s45 offen offset:3072 sc1\n\t"
        "s_mov_b32 s44, s45\n\t"
        "s_cmp_eq_u32 s47, 0\n\t"
        "s_cbranch_scc1 oseg0_%=\n\t"
        "s_cmp_eq_u32 s47, 1\n\t"
        "s_cbranch_scc1 oseg1_%=\n\t"
        "s_cmp_eq_u32 s47, 2\n\t"
        "s_cbranch_scc1 oseg2_%=\n\t"
        "s_cmp_eq_u32 s47, 3\n\t"
        "s_cbranch_scc1 oseg3_%=\n\t"
        "s_cmp_lg_u32 s46, 15\n\t"
        "s_cbranch_scc1 ostart_%=\n\t"
        "s_branch oend_%=\n\t"
        "oseg3_%=:\n\t"
        "s_or_b32 s46, s46, 8\n\t"
        "s_waitcnt vmcnt(11) lgkmcnt(14)\n\t"
        "v_mfma_f32_32x32x16_bf16 v[96:111], v[128:131], v[160:163], 0\n\t"
        "ds_read_b64_tr_b16 v[128:129], v228 offset:40960\n\t"
        "ds_read_b64_tr_b16 v[130:131], v228 offset:41984\n\t"
        "s_waitcnt lgkmcnt(14)\n\t"
        "v_mfma_f32_32x32x16_bf16 v[112:127], v[132:135], v[160:163], 0\n\t"
        "ds_read_b64_tr_b16 v[132:133], v229 offset:40960\n\t"
        "ds_read_b64_tr_b16 v[134:135], v229 offset:41984\n\t"
        "s_add_u32 s44, s44, 0x1000\n\t"
        "buffer_load_dwordx4 v[208:211], v224, s[40:43], s44 offen offset:0 sc1\n\t"
        "s_waitcnt vmcnt(11) lgkmcnt(14)\n\t"
        "v_mfma_f32_32x32x16_bf16 v[96:111], v[136:139], v[164:167], v[96:111]\n\t"
        "ds_read_b32 v233, v232\n\t"
        "ds_read_b64_tr_b16 v[136:137], v228 offset:43008\n\t"
        "ds_read_b64_tr_b16 v[138:139], v228 offset:44032\n\t"
        "s_waitcnt lgkmcnt(14)\n\t"
        "v_mfma_f32_32x32x16_bf16 v[112:127], v[140:143], v[164:167], v[112:127]\n\t"
        "ds_read_b64_tr_b16 v[140:141], v229 offset:43008\n\t"
        "ds_read_b64_tr_b16 v[142:143], v229 offset:44032\n\t"
        "buffer_load_dwordx4 v[212:215], v224, s[40:43], s44 offen offset:1024 sc1\n\t"
        "s_waitcnt vmcnt(11) lgkmcnt(14)\n\t"
        "v_mfma_f32_32x32x16_bf16 v[96:111], v[144:147], v[168:171], v[96:111]\n\t"
        "ds_read_b64_tr_b16 v[144:145], v228 offset:45056\n\t"
        "ds_read_b64_tr_b16 v[146:147], v228 offset:46080\n\t"
        "s_waitcnt lgkmcnt(14)\n\t"
        "v_mfma_f32_32x32x16_bf16 v[112:127], v[148:151], v[168:171], v[112:127]\n\t"
        "ds_read_b64_tr_b16 v[148:149], v229 offset:45056\n\t"
        "ds_read_b64_tr_b16 v[150:151], v229 offset:46080\n\t"
        "buffer_load_dwordx4 v[216:219], v224, s[40:43], s44 offen offset:2048 sc1\n\t"
        "s_waitcnt vmcnt(11) lgkmcnt(8)\n\t"
        "v_mfma_f32_32x32x16_bf16 v[96:111], v[152:155], v[172:175], v[96:111]\n\t"
        "v_readfirstlane_b32 s51, v233\n\t"
        "ds_read_b64_tr_b16 v[152:153], v228 offset:47104\n\t"
        "ds_read_b64_tr_b16 v[154:155], v228 offset:48128\n\t"
        "s_waitcnt lgkmcnt(14)\n\t"
        "v_mfma_f32_32x32x16_bf16 v[112:127], v[156:159], v[172:175], v[112:127]\n\t"
        "s_andn2_b32 s51, s51, s46\n\t"
        "s_ff1_i32_b32 s47, s51\n\t"
        "s_add_u32 s45, s47, s49\n\t"
        "s_and_b32 s45, s45, 3\n\t"
        "s_lshl_b32 s45, s45, 14\n\t"
        "ds_read_b64_tr_b16 v[156:157], v229 offset:47104\n\t"
        "ds_read_b64_tr_b16 v[158:159], v229 offset:48128\n\t"
        "buffer_load_dwordx4 v[220:223], v224, s[40:43], s44 offen offset:3072 sc1\n\t"
        "s_waitcnt vmcnt(11) lgkmcnt(14)\n\t"
        "v_mfma_f32_32x32x16_bf16 v[96:111], v[128:131], v[176:179], v[96:111]\n\t"
        "s_and_b32 s51, s47, 0x80000000\n\t"
        "v_or_b32 v225, s51, v224\n\t"
        "s_and_b32 s51, s47, 3\n\t"
        "s_lshl_b32 s51, s51, 15\n\t"
        "v_add_u32 v230, s51, v226\n\t"
        "v_add_u32 v231, s51, v227\n\t"
        "ds_read_b64_tr_b16 v[128:129], v228 offset:49152\n\t"
        "ds_read_b64_tr_b16 v[130:131], v228 offset:50176\n\t"
        "s_waitcnt lgkmcnt(14)\n\t"
        "v_mfma_f32_32x32x16_bf16 v[112:127], v[132:135], v[176:179], v[112:127]\n\t"
        "ds_read_b64_tr_b16 v[132:133], v229 offset:49152\n\t"
        "ds_read_b64_tr_b16 v[134:135], v229 offset:50176\n\t"
        "buffer_load_dwordx4 v[160:163], v225, s[40:43], s45 offen offset:0 sc1\n\t"
        "s_waitcnt vmcnt(11) lgkmcnt(14)\n\t"
        "v_mfma_f32_32x32x16_bf16 v[96:111], v[136:139], v[180:183], v[96:111]\n\t"
        "ds_read_b64_tr_b16 v[136:137], v228 offset:51200\n\t"
        "ds_read_b64_tr_b16 v[138:139], v228 offset:52224\n\t"
        "s_waitcnt lgkmcnt(14)\n\t"
        "v_mfma_f32_32x32x16_bf16 v[112:127], v[140:143], v[180:183], v[112:127]\n\t"
        "ds_read_b64_tr_b16 v[140:141], v229 offset:51200\n\t"
        "ds_read_b64_tr_b16 v[142:143], v229 offset:52224\n\t"
        "buffer_load_dwordx4 v[164:167], v225, s[40:43], s45 offen offset:1024 sc1\n\t"
        "s_waitcnt vmcnt(11) lgkmcnt(14)\n\t"
        "v_mfma_f32_32x32x16_bf16 v[96:111], v[144:147], v[184:187], v[96:111]\n\t"
        "ds_read_b64_tr_b16 v[144:145], v228 offset:53248\n\t"
        "ds_read_b64_tr_b16 v[146:147], v228 offset:54272\n\t"
        "s_waitcnt lgkmcnt(14)\n\t"
        "v_mfma_f32_32x32x16_bf16 v[112:127], v[148:151], v[184:187], v[112:127]\n\t"
        "ds_read_b64_tr_b16 v[148:149], v229 offset:53248\n\t"
        "ds_read_b64_tr_b16 v[150:151], v229 offset:54272\n\t"
        "buffer_load_dwordx4 v[168:171], v225, s[40:43], s45 offen offset:2048 sc1\n\t"
        "s_waitcnt vmcnt(11) lgkmcnt(14)\n\t"
        "v_mfma_f32_32x32x16_bf16 v[96:111], v[152:155], v[188:191], v[96:111]\n\t"
        "ds_read_b64_tr_b16 v[152:153], v228 offset:55296\n\t"
        "ds_read_b64_tr_b16 v[154:155], v228 offset:56320\n\t"
        "s_waitcnt lgkmcnt(14)\n\t"
        "v_mfma_f32_32x32x16_bf16 v[112:127], v[156:159], v[188:191], v[112:127]\n\t"
        "ds_read_b64_tr_b16 v[156:157], v229 offset:55296\n\t"
        "ds_read_b64_tr_b16 v[158:159], v229 offset:56320\n\t"
        "buffer_load_dwordx4 v[172:175], v225, s[40:43], s45 offen offset:3072 sc1\n\t"
        "s_waitcnt vmcnt(11) lgkmcnt(14)\n\t"
        "v_mfma_f32_32x32x16_bf16 v[96:111], v[128:131], v[192:195], v[96:111]\n\t"
        "ds_read_b64_tr_b16 v[128:129], v228 offset:57344\n\t"
        "ds_read_b64_tr_b16 v[130:131], v228 offset:58368\n\t"
        "s_waitcnt lgkmcnt(14)\n\t"
        "v_mfma_f32_32x32x16_bf16 v[112:127], v[132:135], v[192:195], v[112:127]\n\t"
        "ds_read_b64_tr_b16 v[132:133], v229 offset:57344\n\t"
        "ds_read_b64_tr_b16 v[134:135], v229 offset:58368\n\t"
        "s_add_u32 s45, s45, 0x1000\n\t"
        "buffer_load_dwordx4 v[176:179], v225, s[40:43], s45 offen offset:0 sc1\n\t"
        "s_waitcnt vmcnt(11) lgkmcnt(14)\n\t"
        "v_mfma_f32_32x32x16_bf16 v[96:111], v[136:139], v[196:199], v[96:111]\n\t"
        "ds_read_b64_tr_b16 v[136:137], v228 offset:59392\n\t"
        "ds_read_b64_tr_b16 v[138:139], v228 offset:60416\n\t"
        "s_waitcnt lgkmcnt(14)\n\t"
        "v_mfma_f32_32x32x16_bf16 v[112:127], v[140:143], v[196:199], v[112:127]\n\t"
        "ds_read_b64_tr_b16 v[140:141], v229 offset:59392\n\t"
        "ds_read_b64_tr_b16 v[142:143], v229 offset:60416\n\t"
        "buffer_load_dwordx4 v[180:183], v225, s[40:43], s45 offen offset:1024 sc1\n\t"
        "s_waitcnt vmcnt(11) lgkmcnt(14)\n\t"
        "v_mfma_f32_32x32x16_bf16 v[96:111], v[144:147], v[200:203], v[96:111]\n\t"
        "ds_read_b64_tr_b16 v[144:145], v228 offset:61440\n\t"
        "ds_read_b64_tr_b16 v[146:147], v228 offset:62464\n\t"
        "s_waitcnt lgkmcnt(14)\n\t"
        "v_mfma_f32_32x32x16_bf16 v[112:127], v[148:151], v[200:203], v[112:127]\n\t"
        "ds_read_b64_tr_b16 v[148:149], v229 offset:61440\n\t"
        "ds_read_b64_tr_b16 v[150:151], v229 offset:62464\n\t"
        "buffer_load_dwordx4 v[184:187], v225, s[40:43], s45 offen offset:2048 sc1\n\t"
        "s_waitcnt vmcnt(11) lgkmcnt(14)\n\t"
        "v_mfma_f32_32x32x16_bf16 v[96:111], v[152:155], v[204:207], v[96:111]\n\t"
        "ds_read_b64_tr_b16 v[152:153], v228 offset:63488\n\t"
        "ds_read_b64_tr_b16 v[154:155], v228 offset:64512\n\t"
        "s_waitcnt lgkmcnt(14)\n\t"
        "v_mfma_f32_32x32x16_bf16 v[112:127], v[156:159], v[204:207], v[112:127]\n\t"
        "ds_read_b64_tr_b16 v[156:157], v229 offset:63488\n\t"
        "ds_read_b64_tr_b16 v[158:159], v229 offset:64512\n\t"
        "buffer_load_dwordx4 v[188:191], v225, s[40:43], s45 offen offset:3072 sc1\n\t"
        "s_waitcnt vmcnt(11) lgkmcnt(14)\n\t"
        "v_mfma_f32_32x32x16_bf16 v[96:111], v[128:131], v[208:211], v[96:111]\n\t"
        "ds_read_b64_tr_b16 v[128:129], v230 offset:0\n\t"
        "ds_read_b64_tr_b16 v[130:131], v230 offset:1024\n\t"
        "s_waitcnt lgkmcnt(14)\n\t"
        "v_mfma_f32_32x32x16_bf16 v[112:127], v[132:135], v[208:211], v[112:127]\n\t"
        "ds_read_b64_tr_b16 v[132:133], v231 offset:0\n\t"
        "ds_read_b64_tr_b16 v[134:135], v231 offset:1024\n\t"
        "s_add_u32 s45, s45, 0x1000\n\t"
        "buffer_load_dwordx4 v[192:195], v225, s[40:43], s45 offen offset:0 sc1\n\t"
        "s_waitcnt vmcnt(11) lgkmcnt(14)\n\t"
        "v_mfma_f32_32x32x16_bf16 v[96:111], v[136:139], v[212:215], v[96:111]\n\t"
        "ds_read_b64_tr_b16 v[136:137], v230 offset:2048\n\t"
        "ds_read_b64_tr_b16 v[138:139], v230 offset:3072\n\t"
        "s_waitcnt lgkmcnt(14)\n\t"
        "v_mfma_f32_32x32x16_bf16 v[112:127], v[140:143], v[212:215], v[112:127]\n\t"
        "ds_read_b64_tr_b16 v[140:141], v231 offset:2048\n\t"
        "ds_read_b64_tr_b16 v[142:143], v231 offset:3072\n\t"
        "buffer_load_dwordx4 v[196:199], v225, s[40:43], s45 offen offset:1024 sc1\n\t"
        "s_waitcnt vmcnt(11) lgkmcnt(14)\n\t"
        "v_mfma_f32_32x32x16_bf16 v[96:111], v[144:147], v[216:219], v[96:111]\n\t"
        "ds_read_b64_tr_b16 v[144:145], v230 offset:4096\n\t"
        "ds_read_b64_tr_b16 v[146:147], v230 offset:5120\n\t"
        "s_waitcnt lgkmcnt(14)\n\t"
        "v_mfma_f32_32x32x16_bf16 v[112:127], v[148:151], v[216:219], v[112:127]\n\t"
        "ds_read_b64_tr_b16 v[148:149], v231 offset:4096\n\t"
        "ds_read_b64_tr_b16 v[150:151], v231 offset:5120\n\t"
        "buffer_load_dwordx4 v[200:203], v225, s[40:43], s45 offen offset:2048 sc1\n\t"
        "s_waitcnt vmcnt(11) lgkmcnt(14)\n\t"
        "v_mfma_f32_32x32x16_bf16 v[96:111], v[152:155], v[220:223], v[96:111]\n\t"
        "ds_read_b64_tr_b16 v[152:153], v230 offset:6144\n\t"
        "ds_read_b64_tr_b16 v[154:155], v230 offset:7168\n\t"
        "s_waitcnt lgkmcnt(14)\n\t"
        "v_mfma_f32_32x32x16_bf16 v[112:127], v[156:159], v[220:223], v[112:127]\n\t"
        "ds_read_b64_tr_b16 v[156:157], v231 offset:6144\n\t"
        "ds_read_b64_tr_b16 v[158:159], v231 offset:7168\n\t"
        "buffer_load_dwordx4 v[204:207], v225, s[40:43], s45 offen offset:3072 sc1\n\t"
        "s_mov_b32 s44, s45\n\t"
        "s_cmp_eq_u32 s47, 0\n\t"
        "s_cbranch_scc1 oseg0_%=\n\t"
        "s_cmp_eq_u32 s47, 1\n\t"
        "s_cbranch_scc1 oseg1_%=\n\t"
        "s_cmp_eq_u32 s47, 2\n\t"
        "s_cbranch_scc1 oseg2_%=\n\t"
        "s_cmp_eq_u32 s47, 3\n\t"
        "s_cbranch_scc1 oseg3_%=\n\t"
        "s_cmp_lg_u32 s46, 15\n\t"
        "s_cbranch_scc1 ostart_%=\n\t"
        "s_branch oend_%=\n\t"
        "oend_%=:\n\t"
        "ds_read_b64 v[128:129], v234 offset:0\n\t"
        "ds_read_b64 v[130:131], v234 offset:16\n\t"
        "ds_read_b64 v[132:133], v234 offset:32\n\t"
        "ds_read_b64 v[134:135], v234 offset:48\n\t"
        "ds_read_b64 v[136:137], v234 offset:64\n\t"
        "ds_read_b64 v[138:139], v234 offset:80\n\t"
        "ds_read_b64 v[140:141], v234 offset:96\n\t"
        "ds_read_b64 v[142:143], v234 offset:112\n\t"
        "s_waitcnt lgkmcnt(0)\n\t"
        "s_barrier\n\t"
        : "+{v[0:15]}"(acc[0]), "+{v[16:31]}"(acc[1]), "={v[32:47]}"(acc[2]), "={v[48:63]}"(acc[3]), "={v[64:79]}"(acc[4]), "={v[80:95]}"(acc[5]),
          "={v[96:111]}"(acc[6]), "={v[112:127]}"(acc[7]), "={v[128:143]}"(wrc),
          "={v[160:175]}"(fl0), "={v[176:191]}"(fl1), "={v[192:207]}"(fl2) STAMP_OUTS
        : "{v224}"(OFF_OF + L * (5 * MBY) + tile * TS + lane * 16), "{v226}"(a0), "{v227}"(a1), "{v228}"(a0 + 65536), "{v229}"(a1 + 65536),
          "{v232}"(lbase + L_W + 20 + 8 * L), "{v234}"(lbase + L_WR + hi * 8), "{s49}"((tn >> 2) + 1), "{s[40:43]}"(srd)
        : "v144", "v145", "v146", "v147", "v148", "v149", "v150", "v151", "v152", "v153", "v154", "v155", "v156", "v157", "v158", "v159", "v208", "v209", "v210", "v211", "v212", "v213", "v214", "v215", "v216", "v217", "v218", "v219", "v220", "v221", "v222", "v223", "v225", "v230", "v231", "v233", "s44", "s45", "s46", "s47", "s48", "s51", "vcc", "scc", "memory");
      }
      PHASE_VARS;
      STAMP_PUT(12 + 16 * L);
      STAMPV(15 + 16 * L);
      float s = 0.f, sq = 0.f, d = 0.f;
      const unsigned xdst = OFF_X1 + tile * TS + tn * 4096 + lane * 16;
#pragma unroll
      for (int i = 0; i < 4; ++i) {
#pragma unroll
        for (int e = 0; e < 4; ++e) {
          const int j = i >> 1, k = 8 * (i & 1) + 2 * e;
          x1p[i][e] = pack2((acc[j][k] + acc[2 + j][k]) + (acc[4 + j][k] + acc[6 + j][k]), (acc[j][k + 1] + acc[2 + j][k + 1]) + (acc[4 + j][k + 1] + acc[6 + j][k + 1]));
        }
        if (L == 0) stg(rs, xdst + i * 1024, x1p[i]);
        SBAR();
      }
#pragma unroll
      for (int g = 0; g < 8; ++g)
#pragma unroll
        for (int e = 0; e < 2; ++e) {
          const unsigned o = x1p[g >> 1][2 * (g & 1) + e];
          s = dot2(o, 0x3f803f80u, s); sq = dot2(o, o, sq); d = dot2(o, wrc[2 * g + e], d);
        }
      const u32x4 pst = {nzf(half_swap_sum(s)), nzf(half_swap_sum(sq)), nzf(half_swap_sum(d)), 0u};
      asm volatile("buffer_store_dwordx4 %0, %1, %2, 0 offen sc1\n\ts_waitcnt vmcnt(1)\n\tds_write_b32 %3, %4"
                   :: "v"(pst), "v"(stats_dst(sx, tile, tn, lane)), "s"(srd), "v"(lbase + L_W + 64 + 4 * wid), "v"(3),
                      "{v[160:175]}"(fl0), "{v[176:191]}"(fl1), "{v[192:207]}"(fl2) : "memory");
    }
    STAMPV(16 + 16 * L);
  }
  {
    PHASE_VARS;
    float S, Q, Dd, mean, var, mb1, rd1;
    __builtin_amdgcn_s_sleep(13);
    row_stats_sum(rs, OFF_SX + SX_LAYER, tile, lane, S, Q, Dd);
    STAMPV(33);
    row_stats_fin(rs, OFF_SX + SX_LAYER + SX_CAND + (tn * 16 + tm) * 128 + wid * 4, smem, 2, tile, lane, S, Q, Dd, U2F(words[0]), x1p, mean, var, mb1, rd1);
    STAMPV(34);
    u32x4 xo[4];
#pragma unroll
    for (int i = 0; i < 4; ++i) {
#pragma unroll
      for (int e = 0; e < 4; ++e) {
        const unsigned h2 = ln_pair(x1p[i][e], mb1, rd1);
        xo[i][e] = pack2(lo16(x1p[i][e]) + lo16(h2), hi16(x1p[i][e]) + hi16(h2));
      }
      *reinterpret_cast<u32x4*>(smem + wid * 4096 + r32 * 128 + ((2 * i + hi) ^ (r32 & 7)) * 16) = fo_swap(xo[i]);
    }
    asm volatile("s_waitcnt lgkmcnt(0)" ::: "memory");
#pragma unroll
    for (int i = 0; i < 4; ++i) { xo[i] = *reinterpret_cast<const u32x4*>(smem + wid * 4096 + i * 1024 + (lane >> 3) * 128 + ((lane ^ (lane >> 3)) & 7) * 16); PIN(xo[i]); }
    u16* dst = out + (size_t)(tile * 32 + (lane >> 3)) * D_ + tn * 64 + (lane & 7) * 8;
    lds_wait_ge(lbase + L_W + 4 + 4 * (lane & 1), 2);
    const int rel = (int)words[3] - tile * 32 - (lane >> 3);
#pragma unroll
    for (int i = 0; i < 4; ++i)
      if (rel != 8 * i) st16(dst + (size_t)i * 8 * D_, xo[i]);
    STAMPV(35);
    STAMP_END;
  }
}

extern "C" __global__ void end_marker() {}

extern "C" void kernel_launch(void* const* d_in, const int* in_sizes, int n_in, void* d_out, int out_size,
                              void* d_ws, size_t ws_size, hipStream_t stream) {
  char* ws = (char*)d_ws;
  mega_kernel<<<256, 512, 0, stream>>>((const u16*)d_in[0], (const u16*)d_in[1], (const u16*)d_in[2], (const u16*)d_in[4], (const u16*)d_in[5],
                                       (const u16*)d_in[6], (const u16*)d_in[7], ws, (u16*)d_out);
}
```

```cpp
#include <hip/hip_runtime.h>
#include <hip/hip_bf16.h>
#include <stdint.h>

typedef unsigned short u16;
typedef short s16x4 __attribute__((ext_vector_type(4)));
typedef short s16x8 __attribute__((ext_vector_type(8)));
typedef float f32x16 __attribute__((ext_vector_type(16)));
typedef float f32x8 __attribute__((ext_vector_type(8)));
typedef float f32x4 __attribute__((ext_vector_type(4)));
typedef float f32x2 __attribute__((ext_vector_type(2)));
typedef unsigned int u32x16 __attribute__((ext_vector_type(16)));
typedef unsigned int u32x8 __attribute__((ext_vector_type(8)));
typedef unsigned int u32x4 __attribute__((ext_vector_type(4)));
typedef unsigned int u32x2 __attribute__((ext_vector_type(2)));
typedef __bf16 bf16x2 __attribute__((ext_vector_type(2)));
typedef __bf16 bf16x8 __attribute__((ext_vector_type(8)));
typedef __bf16 bf16x16 __attribute__((ext_vector_type(16)));

#define DI __device__ __forceinline__
#define LDS_PTR(T, p) ((__attribute__((address_space(3))) T*)(p))
#define BARRIER() asm volatile("s_barrier" ::: "memory")
#define BAR_LGKM() asm volatile("s_waitcnt lgkmcnt(0)\n\ts_barrier" ::: "memory")
#define BAR_ALL() asm volatile("s_waitcnt vmcnt(0) lgkmcnt(0)\n\ts_barrier" ::: "memory")
#define DRAIN() asm volatile("s_waitcnt vmcnt(0)" ::: "memory")
#define MFMA32(a, b, c) __builtin_amdgcn_mfma_f32_32x32x16_bf16(a, b, c, 0, 0, 0)
#define MFMA16(a, b, c) __builtin_amdgcn_mfma_f32_16x16x32_bf16(a, b, c, 0, 0, 0)
#define PIN(x) asm volatile("" : "+v"(x))
#define EX2(x) __builtin_amdgcn_exp2f(x)
#define RCP(x) __builtin_amdgcn_rcpf(x)
#define SBAR() __builtin_amdgcn_sched_barrier(0)

#define STAMP_INIT
#define STAMPV(i)
#define STAMP_END
#define STAMP_DECL
#define STAMP_OUTS
#define STAMP_PUT(i)
#define JIT()
constexpr int D_ = 1024, F_ = 3584;
constexpr float QS = 0.17677669529663687f * 1.4426950408889634f;

constexpr unsigned MBY = 1u << 20;
constexpr unsigned TS = 65536 + 4096, HS = 262144 + 4096;
constexpr unsigned OFF_HF = 0, OFF_X1 = 5 * MBY;
constexpr unsigned OFF_OF = 10 * MBY;
constexpr unsigned OFF_KV = 20 * MBY;
constexpr unsigned OFF_FL = 40 * MBY;
constexpr unsigned FL_EPOCH = OFF_FL;
constexpr unsigned FL_KV = OFF_FL + 4096;
constexpr unsigned FL_O = FL_KV + 65536;
constexpr unsigned FL_X = FL_O + 65536;
constexpr unsigned OFF_CS = 41 * MBY;
constexpr unsigned OFF_SX = 42 * MBY;
constexpr unsigned SX_ST = 0;
constexpr unsigned SX_ROW = 512 * 1024;
constexpr unsigned SX_CAND = SX_ROW + 256 * 1024;
constexpr unsigned SX_GP = SX_CAND + 32 * 1024;
constexpr unsigned SX_YP = SX_GP + 224 * 1024;
constexpr unsigned SX_LAYER = SX_YP + 224 * 1024;
constexpr unsigned SX_QP = 2 * SX_LAYER;
constexpr unsigned SX_BYTES = SX_QP + 512 * 1024;
static_assert(SX_BYTES <= 6 * 256 * 128 * 16, "sentinel fill");
constexpr unsigned WS_BYTES = 48 * MBY, OOB = 0x80000000u;
constexpr unsigned SENT = 0xFFFFFFFFu;

constexpr int NS = 9, DD = 3, SLOTB = 12352, HALFB = 6176;
constexpr int L_XCH = 122880;
constexpr int L_SL = L_XCH + 8192;
constexpr int L_CAND = L_XCH + 32768;
constexpr int L_AB = L_XCH + 34816;
constexpr int L_FULL = 159744, L_FREE = L_FULL + 512;
constexpr int L_CS = L_FREE + 1024, L_PATCH = L_CS + 768;
constexpr int L_GU = L_PATCH + 768, L_TAG = L_GU + 256, L_WR = L_TAG + 256;
constexpr int L_X2F = L_WR + 128, L_X2P = L_X2F + 128;
constexpr int L_W = L_X2P + 128;
constexpr int LDS_BYTES = 163840;
static_assert(L_W + 128 <= LDS_BYTES && NS * SLOTB + 256 <= L_XCH && L_AB + 2048 <= L_FULL, "LDS map");

DI int fresh_lane() { int l; asm volatile("v_mbcnt_lo_u32_b32 %0, -1, 0\n\tv_mbcnt_hi_u32_b32 %0, -1, %0" : "=v"(l)); return l; }
#define LANE_VARS const int lane = fresh_lane(), r32 = lane & 31, hi = lane >> 5, tq = (lane & 15) >> 2, tp = lane & 3, tblk = (lane >> 4) & 1, \
  wrow = 4 * hi + tq, wsw = 4 * ((wrow >> 1) & 1), wch = 2 * tblk + (tp >> 1), c24 = r32 < 24 ? r32 : 23; \
  (void)r32; (void)hi; (void)wsw; (void)wch; (void)c24
DI int fresh_s(int v) { asm volatile("" : "+s"(v)); return v; }
#define PHASE_VARS const int tm = fresh_s(tm_), tn = fresh_s(tn_), wid = fresh_s(wid_), tile = tm * 4 + wid, hh = wid & 1, kh = wid >> 1; (void)tile; (void)hh; (void)kh; LANE_VARS
DI int kpos(int tm, int tn, int i) { return i == 0 ? tn : (tn + 1 + (1 * (3 - (tn & 3)) + i - 1) % 15) & 15; }
DI float bf2f(u16 v) { return __uint_as_float(((unsigned)v) << 16); }
DI unsigned pack2(float lo, float hi) { f32x2 v = {lo, hi}; return __builtin_bit_cast(unsigned, __builtin_convertvector(v, bf16x2)); }
DI float rbf(float f) { return __uint_as_float(pack2(f, 0.f) << 16); }
DI float lo16(unsigned u) { return __uint_as_float(u << 16); }
DI float hi16(unsigned u) { return __uint_as_float(u & 0xffff0000u); }
DI float opq(float r) { asm("" : "+v"(r)); return r; }
DI float fadd(float a, float b) { return opq(a + b); }
DI float fmul(float a, float b) { return opq(a * b); }
DI float ffma(float a, float b, float c) { return opq(__builtin_fmaf(a, b, c)); }
DI float U2F(unsigned u) { return __uint_as_float(u); }
DI float half_swap_sum(float v) {
  auto r = __builtin_amdgcn_permlane32_swap(__float_as_uint(v), __float_as_uint(v), false, false);
  return fadd(U2F(r[0]), U2F(r[1]));
}
DI float half_swap_max(float v) {
  auto r = __builtin_amdgcn_permlane32_swap(__float_as_uint(v), __float_as_uint(v), false, false);
  return fmaxf(__uint_as_float(r[0]), __uint_as_float(r[1]));
}
template <int CTRL> DI unsigned dpp(unsigned v) { return (unsigned)__builtin_amdgcn_update_dpp(0, (int)v, CTRL, 0xf, 0xf, false); }
DI float row_swap_sum(float v) {
  auto r = __builtin_amdgcn_permlane16_swap(__float_as_uint(v), __float_as_uint(v), false, false);
  return fadd(U2F(r[0]), U2F(r[1]));
}
DI float half_sum(float v) {
  v = fadd(v, U2F(dpp<0xB1>(__float_as_uint(v)))); v = fadd(v, U2F(dpp<0x4E>(__float_as_uint(v))));
  v = fadd(v, U2F(dpp<0x141>(__float_as_uint(v)))); v = fadd(v, U2F(dpp<0x140>(__float_as_uint(v))));
  return row_swap_sum(v);
}
DI float wave_sum(float v) { return half_swap_sum(half_sum(v)); }
DI unsigned half_max_u32(unsigned v) {
  v = max(v, dpp<0xB1>(v)); v = max(v, dpp<0x4E>(v)); v = max(v, dpp<0x141>(v)); v = max(v, dpp<0x140>(v));
  auto r = __builtin_amdgcn_permlane16_swap(v, v, false, false);
  return max(r[0], r[1]);
}
DI unsigned wave_max_u32(unsigned v) {
  v = half_max_u32(v);
  auto r = __builtin_amdgcn_permlane32_swap(v, v, false, false);
  return max(r[0], r[1]);
}
DI u32x4 fo_swap(u32x4 v) {
  auto a = __builtin_amdgcn_permlane32_swap(v[0], v[2], false, false);
  auto b = __builtin_amdgcn_permlane32_swap(v[1], v[3], false, false);
  u32x4 o; o[0] = a[0]; o[1] = b[0]; o[2] = a[1]; o[3] = b[1];
  return o;
}
DI void row_swap(u32x4& a, u32x4& b) {
#pragma unroll
  for (int e = 0; e < 4; ++e) { auto r = __builtin_amdgcn_permlane16_swap(a[e], b[e], false, false); a[e] = r[0]; b[e] = r[1]; }
}
DI void glds4(const void* sb, unsigned vo, unsigned lds_dst) {
  unsigned keep;
  asm volatile("s_mov_b32 %0, m0\n\ts_mov_b32 m0, %3\n\ts_nop 0\n\tglobal_load_lds_dword %1, %2\n\ts_mov_b32 m0, %0"
               : "=&s"(keep) : "v"(vo), "s"(sb), "s"(lds_dst) : "memory");
}
#define GL_ "global_load_lds_dwordx4 "
DI void glds_v4(const void* sb, const unsigned (&v)[4], unsigned m0v) {
  unsigned keep;
  asm volatile("s_mov_b32 %0, m0\n\ts_mov_b32 m0, %6\n\ts_nop 0\n\t" GL_ "%1, %5\n\t" GL_ "%2, %5 offset:1024\n\t" GL_ "%3, %5 offset:2048\n\t"
               GL_ "%4, %5 offset:3072\n\ts_mov_b32 m0, %0" : "=&s"(keep) : "v"(v[0]), "v"(v[1]), "v"(v[2]), "v"(v[3]), "s"(sb), "s"(m0v) : "memory");
}
#define GP_ "\n\ts_sleep %8\n\ts_waitcnt vmcnt(%7)\n\t"
template <int TH, int SL> DI void glds_v4t(const void* sb, const unsigned (&v)[4], unsigned m0v) {
  unsigned keep;
  asm volatile("s_mov_b32 %0, m0\n\ts_mov_b32 m0, %6\n\ts_nop 0\n\t" GL_ "%1, %5" GP_ GL_ "%2, %5 offset:1024" GP_ GL_ "%3, %5 offset:2048" GP_ GL_ "%4, %5 offset:3072" GP_ "s_mov_b32 m0, %0"
               : "=&s"(keep) : "v"(v[0]), "v"(v[1]), "v"(v[2]), "v"(v[3]), "s"(sb), "s"(m0v), "n"(TH), "n"(SL) : "memory");
}
DI void glds_v3(const void* sb, const unsigned (&v)[3], unsigned m0v) {
  unsigned keep;
  asm volatile("s_mov_b32 %0, m0\n\ts_mov_b32 m0, %5\n\ts_nop 0\n\t" GL_ "%1, %4\n\t" GL_ "%2, %4 offset:1024\n\t" GL_ "%3, %4 offset:2048\n\t"
               "s_mov_b32 m0, %0" : "=&s"(keep) : "v"(v[0]), "v"(v[1]), "v"(v[2]), "s"(sb), "s"(m0v) : "memory");
}
DI float dot2(unsigned a, unsigned b, float c) {
  return __builtin_amdgcn_fdot2_f32_bf16(__builtin_bit_cast(bf16x2, a), __builtin_bit_cast(bf16x2, b), c, false);
}
#define CODE_ADDR(sym, out) do { unsigned long long pc_; unsigned off_; \
  asm volatile("s_getpc_b64 %0\n1:\n\ts_mov_b32 %1, " #sym "-1b" : "=s"(pc_), "=s"(off_)); out = (const char*)(pc_ + (long long)(int)off_); } while (0)
DI void st16(void* p, u32x4 v) { asm volatile("global_store_dwordx4 %0, %1, off sc0 sc1\n\ts_nop 1" :: "v"(p), "v"(v) : "memory"); }
DI void ln_scalars(float mean, float var, float& mb, float& den) { mb = rbf(mean); den = rbf(__builtin_amdgcn_sqrtf(rbf(rbf(var) + 1e-5f))); }
DI unsigned ln_pair(unsigned v, float mb, float rd) {
  const unsigned c = pack2(lo16(v) - mb, hi16(v) - mb);
  return pack2(lo16(c) * rd, hi16(c) * rd);
}
DI unsigned ln_pair_s(unsigned v, float nmb, float rd) {
  const unsigned c = pack2(fadd(lo16(v), nmb), fadd(hi16(v), nmb));
  return pack2(fmul(lo16(c), rd), fmul(hi16(c), rd));
}
template <int N, int M> DI f32x4 sum4(const u32x4 (&v)[M]) {
  f32x4 t;
#pragma unroll
  for (int e = 0; e < 4; ++e) {
    t[e] = U2F(v[0][e]);
#pragma unroll
    for (int i = 1; i < N; ++i) t[e] = fadd(t[e], U2F(v[i][e]));
  }
  return t;
}
#define PERM_LO(x, y) __builtin_amdgcn_perm(y, x, 0x05040100u)
#define PERM_HI(x, y) __builtin_amdgcn_perm(y, x, 0x07060302u)
constexpr unsigned SPIN_CAP = 1u << 15;
DI unsigned nz(unsigned v) { return min(v, SENT - 1u); }
DI unsigned nzf(float v) { return nz(__float_as_uint(v)); }
DI unsigned mx4(unsigned m, const u32x4& v) { return max(max(max(m, v[0]), v[1]), max(v[2], v[3])); }
template <int N> DI bool none_sent(const u32x4 (&v)[N]) {
  unsigned m = 0u;
#pragma unroll
  for (int i = 0; i < N; ++i) m = mx4(m, v[i]);
  return m != SENT;
}
typedef __amdgpu_buffer_rsrc_t rsrc_t;
DI u32x4 ldg(rsrc_t rs, unsigned off) { return __builtin_amdgcn_raw_buffer_load_b128(rs, (int)off, 0, 16); }
DI void stg(rsrc_t rs, unsigned off, u32x4 v) { __builtin_amdgcn_raw_buffer_store_b128(v, rs, (int)off, 0, 16); }
DI void poll_pause(bool slow) { if (slow) __builtin_amdgcn_s_sleep(4); asm volatile("s_sleep 1" ::: "memory"); }
template <int N, class LD, class OKF> DI void poll_all(u32x4 (&out)[N], bool slow, LD ld, OKF okf) {
  for (unsigned spins = 0;; ++spins) {
    ld(out);
    SBAR();
    if (__all(okf(out)) || spins > SPIN_CAP) break;
    poll_pause(slow);
  }
}
DI void lds_set(unsigned addr, unsigned v) { asm volatile("s_waitcnt lgkmcnt(0)\n\tds_write_b32 %0, %1" :: "v"(addr), "v"(v) : "memory"); }
template <int SLEEP = 1> DI void lds_wait_ge(unsigned addr, unsigned need) {
  unsigned spin = 0x100000, v;
  asm volatile("1:\n\tds_read_b32 %0, %2\n\ts_sub_u32 %1, %1, 1\n\ts_waitcnt lgkmcnt(0)\n\ts_cbranch_scc1 2f\n\tv_cmp_gt_u32 vcc, %3, %0\n\ts_cbranch_vccz 2f\n\ts_sleep %4\n\ts_branch 1b\n2:"
               : "=&v"(v), "+&s"(spin) : "v"(addr), "s"(need), "n"(SLEEP) : "vcc", "scc", "memory");
}
DI s16x8 tr2(const char* p, int second) {
  s16x4 lo = __builtin_amdgcn_ds_read_tr16_b64_v4i16(LDS_PTR(s16x4, p)), hi = __builtin_amdgcn_ds_read_tr16_b64_v4i16(LDS_PTR(s16x4, p + second));
  return __builtin_shufflevector(lo, hi, 0, 1, 2, 3, 4, 5, 6, 7);
}
DI u32x16 cat4(const u32x4& a, const u32x4& b, const u32x4& c, const u32x4& d) {
  const u32x8 x = __builtin_shufflevector(a, b, 0, 1, 2, 3, 4, 5, 6, 7), y = __builtin_shufflevector(c, d, 0, 1, 2, 3, 4, 5, 6, 7);
  return __builtin_shufflevector(x, y, 0, 1, 2, 3, 4, 5, 6, 7, 8, 9, 10, 11, 12, 13, 14, 15);
}

DI unsigned stats_dst(unsigned sx, int tile, int tn, int lane) { return lane < 32 ? sx + SX_ST + tile * 8192 + tn * 512 + lane * 16 : OOB; }
DI void row_stats_sum(rsrc_t rs, unsigned sx, int tile, int lane, float& S, float& Q, float& Dd) {
  const unsigned exb = sx + SX_ST + tile * 8192 + (lane & 31) * 16;
  u32x4 pv[8];
  poll_all<8>(pv, false, [&](u32x4 (&v)[8]) {
#pragma unroll
    for (int i = 0; i < 8; ++i) v[i] = ldg(rs, exb + ((lane >> 5) * 8 + i) * 512);
  }, [&](const u32x4 (&v)[8]) { return none_sent(v); });
  S = 0.f; Q = 0.f; Dd = 0.f;
#pragma unroll
  for (int i = 0; i < 8; ++i) { S += __uint_as_float(pv[i][0]); Q += __uint_as_float(pv[i][1]); Dd += __uint_as_float(pv[i][2]); }
}
DI void row_stats_fin(rsrc_t rs, unsigned kdst, char* smem, int epoch, int tile, int lane, float S, float Q, float Dd, float wsum, const u32x4 (&x1p)[4],
                      float& mean, float& var, float& mb, float& rd) {
  S = half_swap_sum(S); Q = half_swap_sum(Q); Dd = half_swap_sum(Dd);
  mean = S * (1.0f / 1024.0f); var = fmaxf(Q * (1.0f / 1024.0f) - mean * mean, 0.f);
  float den;
  ln_scalars(mean, var, mb, den);
  rd = RCP(den);
  const unsigned rb = pack2((Dd - mb * wsum) * rd, 0.f) & 0xffffu;
  const unsigned mine = (((rb & 0x8000u) ? (~rb & 0xffffu) : (rb | 0x8000u)) << 16) | (unsigned)(2047 - (tile * 32 + (lane & 31)));
  const bool best = half_max_u32(mine) == mine;
  __builtin_amdgcn_raw_buffer_store_b32(mine, rs, (best && lane < 32) ? kdst : OOB, 0, 16);
  if (best) {
    char* c = smem + L_CAND + (tile & 3) * 144;
#pragma unroll
    for (int i = 0; i < 4; ++i) *reinterpret_cast<u32x4*>(c + (2 * i + (lane >> 5)) * 16) = x1p[i];
    *reinterpret_cast<u32x4*>(c + 128) = u32x4{mine, __float_as_uint(mb), __float_as_uint(rd), 0u};
  }
  lds_set((unsigned)(uintptr_t)smem + L_W + 32 + (tile & 3) * 4, epoch);
}

DI void attn_safe(rsrc_t rs, unsigned kvoff, const u32x8& q, const u32x4& ones, f32x16& o) {
  const s16x8 q0 = __builtin_bit_cast(s16x8, __builtin_shufflevector(q, q, 0, 1, 2, 3)), q1 = __builtin_bit_cast(s16x8, __builtin_shufflevector(q, q, 4, 5, 6, 7));
  f32x4 l = {0.f, 0.f, 0.f, 0.f};
#pragma unroll
  for (int e = 0; e < 16; ++e) o[e] = 0.f;
  float m = -INFINITY;
#pragma unroll 1
  for (int kt = 0; kt < 64; ++kt) {
    const unsigned p = kvoff + kt * 4096;
    const u32x4 k0 = ldg(rs, p), k1 = ldg(rs, p + 1024), v0 = ldg(rs, p + 2048), v1 = ldg(rs, p + 3072);
    f32x16 s = {};
    s = MFMA32(__builtin_bit_cast(s16x8, k0), q0, s);
    s = MFMA32(__builtin_bit_cast(s16x8, k1), q1, s);
    float mt = fmaxf(s[0], s[1]);
#pragma unroll
    for (int e = 2; e < 16; ++e) mt = fmaxf(mt, s[e]);
    const float mn = fmaxf(m, half_swap_max(mt));
    const float alpha = EX2(m - mn);
    m = mn;
#pragma unroll
    for (int e = 0; e < 16; ++e) { s[e] = EX2(s[e] - mn); o[e] *= alpha; }
    l[0] *= alpha;
    const bf16x16 pa = __builtin_bit_cast(bf16x16, __builtin_shufflevector(s, s, 0, 1, 2, 3, 4, 5, 6, 7));
    const bf16x16 pb = __builtin_bit_cast(bf16x16, __builtin_shufflevector(s, s, 8, 9, 10, 11, 12, 13, 14, 15));
    o = __builtin_amdgcn_smfmac_f32_32x32x32_bf16(__builtin_bit_cast(bf16x8, v0), pa, o, 0xdddd, 0, 0);
    l = __builtin_amdgcn_smfmac_f32_16x16x64_bf16(__builtin_bit_cast(bf16x8, ones), pa, l, 0xdddd, 0, 0);
    o = __builtin_amdgcn_smfmac_f32_32x32x32_bf16(__builtin_bit_cast(bf16x8, v1), pb, o, 0xdddd, 0, 0);
    l = __builtin_amdgcn_smfmac_f32_16x16x64_bf16(__builtin_bit_cast(bf16x8, ones), pb, l, 0xdddd, 0, 0);
  }
  o *= 1.0f / l[0];
}

extern "C" __global__ __launch_bounds__(512, 2) void mega_kernel(const u16* __restrict__ x0, const u16* __restrict__ wqkv, const u16* __restrict__ wo,
                                                              const u16* __restrict__ w_r, const u16* __restrict__ wg, const u16* __restrict__ wu,
                                                              const u16* __restrict__ wd, char* ws, u16* __restrict__ out) {
  __shared__ __attribute__((aligned(16))) char smem[LDS_BYTES];
  const int wid = __builtin_amdgcn_readfirstlane(threadIdx.x >> 6);
  STAMP_INIT;
  STAMPV(0);
  const int bid = blockIdx.x, xcd = bid & 7, loc = bid >> 3;
  const int tm = 8 * (xcd >> 2) + (loc >> 2), tn = 4 * (xcd & 3) + (loc & 3);
  const unsigned lbase = (unsigned)(uintptr_t)smem;
  const rsrc_t rs = __builtin_amdgcn_make_buffer_rsrc(ws, 0, (int)WS_BYTES, 0x00020000);
  volatile __attribute__((address_space(3))) unsigned* words = LDS_PTR(volatile unsigned, smem + L_W);
  u32x4 v[4];
  if (wid < 4)
    asm volatile("global_load_dwordx4 %0, %4, %5\n\tglobal_load_dwordx4 %1, %4, %5 offset:512\n\tglobal_load_dwordx4 %2, %4, %5 offset:1024\n\tglobal_load_dwordx4 %3, %4, %5 offset:1536"
                 : "=&v"(v[0]), "=&v"(v[1]), "=&v"(v[2]), "=&v"(v[3])
                 : "v"((threadIdx.x >> 5) * (D_ * 2) + (threadIdx.x & 31) * 16), "s"(x0 + (size_t)(tm * 128 + tn * 8) * D_) : "memory");
  else {
    asm volatile("" : "=v"(v[0]), "=v"(v[1]), "=v"(v[2]), "=v"(v[3]));
    if (wid == 6) {
      const char *c0, *c1;
      CODE_ADDR(mega_kernel, c0); CODE_ADDR(end_marker, c1);
      const unsigned o = (loc * 64 + (threadIdx.x & 63)) * 32;
      glds4(c0, (long long)o + 4 <= c1 - c0 ? o : 0u, lbase + NS * SLOTB);
    }
  }
  STAMPV(48);
  *reinterpret_cast<unsigned*>(smem + L_FULL + threadIdx.x * 4) = 0u;
  words[threadIdx.x & 31] = 0u;
  BAR_LGKM();
  if (wid < 4) {
    const int tid = threadIdx.x, lane = tid & 63, r32 = lane & 31, row = 2 * wid + (lane >> 5);
    char* sm = smem + L_XCH;
    unsigned dst = OFF_HF + (tm * 4 + (tn >> 2)) * TS + (tn & 3) * 128 + (tid >> 3) * 512 + (tid & 7) * 16;
    asm volatile("s_waitcnt vmcnt(0)" : "+v"(v[0]), "+v"(v[1]), "+v"(v[2]), "+v"(v[3]), "+v"(dst) :: "memory");
    STAMPV(49);
    float s = 0.f, s2 = 0.f;
#pragma unroll
    for (int i = 0; i < 4; ++i)
#pragma unroll
      for (int e = 0; e < 4; e += 2) { s = dot2(v[i][e], 0x3f803f80u, s); s2 = dot2(v[i][e + 1], 0x3f803f80u, s2); }
    const float mean = half_sum(s + s2) * (1.0f / 1024.0f);
    f32x2 m2 = {0.f, 0.f};
#pragma unroll
    for (int i = 0; i < 4; ++i)
#pragma unroll
      for (int e = 0; e < 4; ++e) { const f32x2 d = f32x2{lo16(v[i][e]), hi16(v[i][e])} - mean; m2 += d * d; }
    float mb, den;
    ln_scalars(mean, half_sum(m2[0] + m2[1]) * (1.0f / 1024.0f), mb, den);
    const float rd = 1.0f / den;
#pragma unroll
    for (int i = 0; i < 4; ++i) {
      char* p = sm + ((16 * i + (r32 >> 1)) * 2) * 144 + row * 16 + (r32 & 1) * 8;
      *reinterpret_cast<u32x2*>(p) = u32x2{ln_pair(v[i][0], mb, rd), ln_pair(v[i][1], mb, rd)};
      *reinterpret_cast<u32x2*>(p + 144) = u32x2{ln_pair(v[i][2], mb, rd), ln_pair(v[i][3], mb, rd)};
    }
    STAMPV(37);
    lds_set(lbase + L_W + 64 + 4 * wid, 1);
    lds_wait_ge<0>(lbase + L_W + 64 + 4 * (lane & 3), 1);
    STAMPV(50);
    u32x4 t[4];
#pragma unroll
    for (int i = 0; i < 4; ++i) t[i] = *reinterpret_cast<const u32x4*>(sm + (i * 32 + (tid >> 3)) * 144 + (tid & 7) * 16);
    SBAR();
#pragma unroll
    for (int i = 0; i < 4; ++i) stg(rs, dst + i * 16384, t[i]);
    STAMPV(51);
    DRAIN();
    lds_set(lbase + L_W + 64 + 4 * wid, 2);
    STAMPV(38);
  }
  const int lw = wid & 1;
  const char* wb = reinterpret_cast<const char*>(wqkv + (size_t)(16 * lw) * (3 * D_) + tn * 64);
  auto stage_lanes = [](unsigned (&wv)[3]) {
    const int lane = fresh_lane();
#pragma unroll
    for (int i = 0; i < 3; ++i) {
      const int li = lane + 64 * i, k = li / 24, cp = li % 24, c = cp ^ (4 * ((k >> 1) & 1));
      wv[i] = (unsigned)(k * (3 * D_ * 2) + (c >> 3) * (D_ * 2) + (c & 7) * 16 - i * 1024);
    }
  };
  auto stage = [&](const unsigned (&wv)[3], int s, int slot) {
    const char* p = wb + (size_t)(2 * kpos(tm, tn, s >> 1) + (s & 1)) * (32 * 3 * D_ * 2);
    glds_v3(p, wv, lbase + slot * SLOTB + lw * HALFB);
    glds_v3(p + 8 * (3 * D_ * 2), wv, lbase + slot * SLOTB + lw * HALFB + 3072);
  };
  auto exchange = [&](const u32x2& tag, unsigned line0, int me, bool bar = false) {
    const int lane = fresh_lane();
    __builtin_amdgcn_raw_buffer_store_b64(tag, rs, lane == 0 ? line0 + me * 128 : OOB, 0, 16);
    if (bar) BARRIER();
    JIT();
    unsigned po = lane < 16 ? line0 + lane * 128 : OOB;
    const u32x4 srd = {(unsigned)(uintptr_t)ws, (unsigned)((uintptr_t)ws >> 32) & 0xffffu, WS_BYTES, 0x00020000u};
    for (unsigned spins = 0;; ++spins) {
      u32x2 v = {0u, 0u};
      asm volatile("buffer_atomic_or_x2 %0, %1, %2, 0 offen sc0 sc1\n\ts_waitcnt vmcnt(0)" : "+v"(v) : "v"(po), "s"(srd) : "memory");
      po = ((v[0] == tag[0]) & (v[1] == tag[1])) ? OOB : po;
      if (__all(po == OOB) || spins > SPIN_CAP) break;
    }
  };
  if ((wid >> 1) == 2) {
    unsigned wv[3];
    stage_lanes(wv);
    const int r = tm & 7;
#pragma unroll 1
    for (int i = 0; i < NS; ++i) {
      const int s = i == 0 ? r : i <= r ? i - 1 : i;
      stage(wv, s, s);
      const int j = i - 1, full = j < 1 ? (j == 0 && r == 0) : j < r ? j : j + 1;
      asm volatile("s_waitcnt vmcnt(%2)\n\tds_write_b32 %0, %1" :: "v"(lbase + L_FULL + lw * 256), "v"(full), "n"(6 * 1) : "memory");
    }
    asm volatile("s_waitcnt vmcnt(0)\n\tds_write_b32 %0, %1" :: "v"(lbase + L_FULL + lw * 256), "v"(NS) : "memory");
    STAMPV(1);
  }
  if (wid == 7) {
    u32x2 tag = __builtin_amdgcn_raw_buffer_load_b64(rs, FL_EPOCH, 0, 16);
    tag[0] += 1u; tag[1] += 0x9E3779B9u;
    *reinterpret_cast<u32x2*>(smem + L_TAG) = tag;
    lds_wait_ge<0>(lbase + L_W + 64 + 4 * (fresh_lane() & 3), 2);
    exchange(tag, FL_X + tm * 2048, tn);
    lds_set(lbase + L_W + 24, 1);
  }
  if (wid >= 4) {
    auto wo_rows = [&](int g0, int n, int gs) {
      const int lane = fresh_lane();
      unsigned wov[4];
#pragma unroll
      for (int i = 0; i < 4; ++i) wov[i] = (unsigned)((8 * i + (lane >> 3)) * (D_ * 2) + ((lane & 7) ^ (4 * ((lane >> 4) & 1))) * 16 - i * 1024);
#pragma unroll 1
      for (int i = 0, k = (0 * (tm & 7)) % n; i < n; ++i, k = k + 1 == n ? 0 : k + 1) {
        const int g = g0 + gs * k;
        glds_v4t<3, 1>(reinterpret_cast<const char*>(wo) + (size_t)((((tn >> 2) + 1) * 256 + 32 * g) & 1023) * (D_ * 2) + tn * 128, wov, lbase + g * 4096);
      }
    };
    if (wid < 6) {
      const int lane = fresh_lane();
      unsigned wv[3];
      stage_lanes(wv);
      __builtin_amdgcn_s_setprio(3);
      const unsigned fra = lbase + L_FREE + (lane & 3) * 256, fla = lbase + L_FULL + lw * 256 + lane * 4;
      u32x4 pre[NS * 6];
#pragma unroll
      for (int i = 0; i < NS * 6; ++i) pre[i] = *reinterpret_cast<const u32x4*>(smem + lw * HALFB + lane * 16 + (i / 6) * SLOTB + (i % 6) * 1024);
#pragma unroll
      for (int i = 0; i < NS * 6; ++i) PIN(pre[i]);
#pragma unroll 1
      for (int L = 0; L < 2; ++L) {
        {
          constexpr unsigned STG = 32 * 3 * D_ * 2;
          unsigned off = (2 * kpos(tm, tn, NS >> 1) + (NS & 1)) * STG, dst = lbase + lw * HALFB, need = 32 * L + 3, cnt = 31 - NS, pub = 32 * L + NS - DD, keep, spin, t0, t1;
          asm volatile(
            "s_mov_b32 %[keep], m0\n\ts_branch 3f\n"
            "1:\n\ts_cbranch_vccnz 3f\n"
            "2:\n\ts_add_u32 s56, %[blo], %[off]\n\ts_addc_u32 s57, %[bhi], 0\n\ts_mov_b32 m0, %[dst]\n\ts_add_u32 s58, s56, %[half]\n\t"
            GL_ "%[w0], s[56:57]\n\ts_nop 7\n\t" GL_ "%[w1], s[56:57] offset:1024\n\ts_nop 7\n\t" GL_ "%[w2], s[56:57] offset:2048\n\ts_nop 7\n\t"
            "s_addc_u32 s59, s57, 0\n\ts_add_u32 m0, %[dst], 3072\n\ts_add_u32 %[off], %[off], %[stg]\n\t"
            GL_ "%[w0], s[58:59]\n\ts_nop 7\n\t" GL_ "%[w1], s[58:59] offset:1024\n\ts_nop 7\n\t" GL_ "%[w2], s[58:59] offset:2048\n\ts_nop 7\n\t"
            "ds_read_b32 %[t0], %[fra]\n\t"
            "s_cmp_eq_u32 %[off], %[wrap]\n\ts_cselect_b32 %[off], 0, %[off]\n\ts_cmp_eq_u32 %[off], %[own]\n\ts_cselect_b32 %[off], %[own2], %[off]\n\t"
            "s_add_u32 %[dst], %[dst], %[slot]\n\ts_cmp_eq_u32 %[dst], %[dend]\n\ts_cselect_b32 %[dst], %[dbeg], %[dst]\n\t"
            "v_add_u32 %[pub], 1, %[pub]\n\ts_add_u32 %[need], %[need], 1\n\tv_max_u32 %[t1], %[pmin], %[pub]\n\t"
            "s_waitcnt vmcnt(%[vm]) lgkmcnt(0)\n\tds_write_b32 %[fla], %[t1]\n\tv_cmp_gt_u32 vcc, %[need], %[t0]\n\t"
            "s_sub_u32 %[cnt], %[cnt], 1\n\ts_cbranch_scc0 1b\n\ts_branch 5f\n"
            "3:\n\ts_mov_b32 %[spin], 0x100000\n"
            "4:\n\tds_read_b32 %[t0], %[fra]\n\ts_sub_u32 %[spin], %[spin], 1\n\ts_waitcnt lgkmcnt(0)\n\ts_cbranch_scc1 2b\n\t"
            "v_cmp_gt_u32 vcc, %[need], %[t0]\n\ts_cbranch_vccz 2b\n\ts_sleep 1\n\ts_branch 4b\n"
            "5:\n\ts_mov_b32 m0, %[keep]"
            : [off] "+&s"(off), [dst] "+&s"(dst), [need] "+&s"(need), [cnt] "+&s"(cnt), [pub] "+&v"(pub), [keep] "=&s"(keep), [spin] "=&s"(spin), [t0] "=&v"(t0), [t1] "=&v"(t1)
            : [blo] "s"((unsigned)(uintptr_t)wb), [bhi] "s"((unsigned)((uintptr_t)wb >> 32)), [half] "s"(STG / 4), [stg] "s"(STG), [wrap] "s"(32 * STG), [own] "s"(2 * tn * STG),
              [own2] "s"(2 * ((tn + 1) & 15) * STG), [slot] "s"(SLOTB), [dbeg] "s"(lbase + lw * HALFB), [dend] "s"(lbase + lw * HALFB + NS * SLOTB), [pmin] "s"(32 * L + NS),
              [w0] "v"(wv[0]), [w1] "v"(wv[1]), [w2] "v"(wv[2]), [fra] "v"(fra), [fla] "v"(fla), [vm] "n"(DD * 6)
            : "s56", "s57", "s58", "s59", "vcc", "scc", "memory");
        }
#pragma unroll
        for (int d = DD - 1; d >= 0; --d) asm volatile("s_waitcnt vmcnt(%0)\n\tds_write_b32 %1, %2" :: "n"(d * 6), "v"(fla), "v"(32 * L + 32 - d) : "memory");
        u32x2 tag = *reinterpret_cast<const u32x2*>(smem + L_TAG);
        PIN(tag);
        if (lw == 0) {
          lds_wait_ge(lbase + L_W + 96 + 4 * (lane & 1), 1);
          lds_wait_ge<0>(lbase + L_W + 80 + 4 * (lane & 3), 2 * L + 1);
          exchange(tag, FL_KV + (L * 16 + tn) * 2048, tm, true);
          lds_wait_ge(fra, 32 * L + 32);
          lds_set(lbase + L_W + 16, L + 1);
        } else { BAR_LGKM(); lds_wait_ge<8>(lbase + L_W + 16, L + 1); }
        __builtin_amdgcn_s_sleep(24);
        wo_rows(lw, 15, 2);
        STAMPV(7 + 16 * L);
        DRAIN();
        const unsigned fo = FL_O + (L * 16 + tm) * 2048;
        if (lw) {
          lds_set(lbase + L_W + 112, 2 * L + 1);
          lds_wait_ge<4>(lbase + L_W + 48 + 4 * (lane & 3), L + 1);
          lds_wait_ge<0>(lbase + L_W + 80 + 4 * (lane & 3), 2 * L + 2);
          __builtin_amdgcn_raw_buffer_store_b64(tag, rs, lane == 0 ? fo + tn * 128 : OOB, 0, 16);
          JIT();
          wo_rows(30, 2, 1);
          DRAIN();
          lds_set(lbase + L_W + 112, 2 * L + 2);
        } else {
          lds_wait_ge<4>(lbase + L_W + 112, 2 * L + 1);
          lds_wait_ge<4>(lbase + L_W + 48 + 4 * (lane & 3), L + 1);
          unsigned po = lane < 16 ? fo + ((lane + 4 + (tn & 12)) & 15) * 128 : OOB;
          const u32x4 srd = {(unsigned)(uintptr_t)ws, (unsigned)((uintptr_t)ws >> 32) & 0xffffu, WS_BYTES, 0x00020000u};
          for (unsigned spins = 0;; ++spins) {
            u32x2 v = {0u, 0u};
            asm volatile("buffer_atomic_or_x2 %0, %1, %2, 0 offen sc0 sc1\n\ts_waitcnt vmcnt(0)" : "+v"(v) : "v"(po), "s"(srd) : "memory");
            po = ((v[0] == tag[0]) & (v[1] == tag[1])) ? OOB : po;
            unsigned m = (unsigned)__ballot(po == OOB);
            m &= m >> 1; m &= m >> 2;
            m = (m & 1u) | ((m >> 3) & 2u) | ((m >> 6) & 4u) | ((m >> 9) & 8u);
            if (m == 15u || spins > SPIN_CAP) break;
            words[5 + 2 * L] = words[28] == 2u * L + 2u ? m : m & 7u;
          }
          words[5 + 2 * L] = words[28] == 2u * L + 2u ? 15u : 7u;
          lds_wait_ge<0>(lbase + L_W + 112, 2 * L + 2);
          words[5 + 2 * L] = 15u;
          __builtin_amdgcn_raw_buffer_store_b64(tag, rs, (bid == 0 && lane == 0) ? FL_EPOCH : OOB, 0, 16);
        }
        BAR_ALL();
        if (L == 0) {
          char* d = smem + lw * HALFB + fresh_lane() * 16;
#pragma unroll
          for (int s = 0; s < NS; ++s) {
#pragma unroll
            for (int i = 0; i < 6; ++i) *reinterpret_cast<u32x4*>(d + s * SLOTB + i * 1024) = pre[s * 6 + i];
            lds_set(fla, 32 + s + 1);
          }
          STAMPV(2);
          lds_wait_ge<2>(lbase + L_W + 24, 2);
        }
      }
      STAMP_END;
      return;
    }
    const int a = wid - 6;
    const bool ffn = loc < 28;
    const int grp = xcd * 7 + (ffn ? loc >> 2 : 0), qa = (loc & 3) * 2 + a;
    const int kr = loc, cg = xcd;
    const unsigned wa = lbase + L_W;
    lds_wait_ge<2>(wa + 24, 1);
    __builtin_amdgcn_s_sleep(16);
    auto transpose8 = [](u32x4* v) {
      u32x4 t[8];
#pragma unroll
      for (int j = 0; j < 8; ++j)
#pragma unroll
        for (int e = 0; e < 4; ++e) t[j][e] = (j & 1) ? PERM_HI(v[2 * e][j >> 1], v[2 * e + 1][j >> 1]) : PERM_LO(v[2 * e][j >> 1], v[2 * e + 1][j >> 1]);
#pragma unroll
      for (int j = 0; j < 8; ++j) { v[j] = t[j]; PIN(v[j]); }
    };
    u32x4 gf[32];
    u32x4 wdf[16];
    auto wait_go = [&](int L) {
      __builtin_amdgcn_s_setprio(1);
      lds_wait_ge<8>(lbase + L_W + 16, L + 1);
      __builtin_amdgcn_s_sleep(24);
    };
    auto load_wd = [&](unsigned dv) {
      const rsrc_t rd = __builtin_amdgcn_make_buffer_rsrc(const_cast<u16*>(wd) + (size_t)grp * 64 * D_ + 128 * qa, 0, 64 * D_ * 2 - 256 * qa, 0x00020000);
#pragma unroll
      for (int i = 0; i < 16; ++i) {
        wdf[i] = __builtin_amdgcn_raw_buffer_load_b128(rd, dv, (32 * (i >> 3) + (i & 7)) * (D_ * 2), 0);
        asm volatile("s_sleep 2\n\ts_waitcnt vmcnt(2)" ::: "memory");
      }
    };
    {
      LANE_VARS;
      if (a == 0) {
        const u32x4 w0 = *reinterpret_cast<const u32x4*>(w_r + lane * 8), w1 = *reinterpret_cast<const u32x4*>(w_r + 512 + lane * 8);
        float t = 0.f;
#pragma unroll
        for (int e = 0; e < 4; ++e) t = fadd(fadd(t, fadd(lo16(w0[e]), hi16(w0[e]))), fadd(lo16(w1[e]), hi16(w1[e])));
        words[0] = __float_as_uint(wave_sum(t));
        if ((lane >> 3) == (tn & 7)) *reinterpret_cast<u32x4*>(smem + L_WR + (lane & 7) * 16) = tn < 8 ? w0 : w1;
      }
      const u16* p = wqkv + (size_t)(64 * tm + 32 * a + hi) * (3 * D_) + (c24 >> 3) * D_ + tn * 64 + (c24 & 7) * 8;
      float cs[8] = {0.f, 0.f, 0.f, 0.f, 0.f, 0.f, 0.f, 0.f};
      u32x4 v[16];
#pragma unroll
      for (int i = 0; i < 16; ++i) v[i] = *reinterpret_cast<const u32x4*>(p + (size_t)i * (2 * 3 * D_));
      SBAR();
      const u32x4 sv = {SENT, SENT, SENT, SENT};
#pragma unroll
      for (int i = 0; i < 6; ++i) {
        const unsigned o = ((i * 256 + bid) * 128 + a * 64 + lane) * 16;
        stg(rs, o < SX_BYTES ? OFF_SX + o : OOB, sv);
      }

#pragma unroll
      for (int i = 0; i < 16; ++i)
#pragma unroll
        for (int e = 0; e < 4; ++e) { cs[2 * e] = fadd(cs[2 * e], lo16(v[i][e])); cs[2 * e + 1] = fadd(cs[2 * e + 1], hi16(v[i][e])); }
      u32x4 o;
#pragma unroll
      for (int e = 0; e < 4; ++e) { const float x = half_swap_sum(cs[e]), y = half_swap_sum(cs[4 + e]); o[e] = __float_as_uint(hi ? y : x); }
      stg(rs, r32 < 24 ? OFF_CS + (tn * 32 + tm * 2 + a) * 768 + r32 * 32 + hi * 16 : OOB, o);
    }
    {
      DRAIN();
      lds_set(wa + 96 + 4 * a, 1);
      BAR_LGKM();
      LANE_VARS;
      const char* gp = reinterpret_cast<const char*>(((lane & 8) ? wu : wg) + (size_t)(128 * qa + 16 * hi + 4 * tblk) * F_ + 64 * grp + (lane & 7) * 8);
      unsigned dv = (lane >> 4) * (8 * D_ * 2) + (lane & 15) * 16;
      PIN(gp); PIN(dv);
      unsigned sv[3];
#pragma unroll
      for (int i = 0; i < 3; ++i) { const int li = lane + 64 * i, k = li / 24, c = li % 24; sv[i] = (unsigned)(k * (3 * D_ * 2) + (c >> 3) * (D_ * 2) + (c & 7) * 16 - i * 1024); PIN(sv[i]); }
      wait_go(0);
#pragma unroll
      for (int i = 0; i < 32; ++i) {
        gf[i] = *reinterpret_cast<const u32x4*>(gp + (size_t)(32 * (i >> 3) + 8 * ((i >> 2) & 1) + (i & 3)) * (F_ * 2));
        asm volatile("s_sleep 2\n\ts_waitcnt vmcnt(2)" ::: "memory");
      }
      load_wd(dv);
      STAMPV(7);
      if (1) __builtin_amdgcn_s_setprio(0);
      lds_wait_ge<4>(wa + 80 + 4 * (lane & 3), 2);
      __builtin_amdgcn_s_setprio(0);
#pragma unroll
      for (int g = 0; g < 4; ++g) glds_v3(wqkv + (size_t)(32 * kr + 8 * g) * (3 * D_) + (2 * cg + a) * 64, sv, lbase + L_SL + a * 12288 + g * 3072);
      transpose8(gf); transpose8(gf + 8); transpose8(gf + 16); transpose8(gf + 24);
      transpose8(wdf); transpose8(wdf + 8);
      {
        LANE_VARS;
        f32x4 t[2];
#pragma unroll
        for (int c = 0; c < 2; ++c) {
          u32x4 v[8];
#pragma unroll
          for (int i = 0; i < 8; ++i) v[i] = ldg(rs, OFF_CS + (tn * 32 + 16 * c + 2 * i + hi) * 768 + a * 384 + c24 * 16);
          SBAR();
          t[c] = sum4<8>(v);
          SBAR();
        }
#pragma unroll
        for (int e = 0; e < 4; ++e) t[0][e] = half_swap_sum(fadd(t[0][e], t[1][e]));
        if (lane < 24) *reinterpret_cast<f32x4*>(smem + L_CS + a * 384 + lane * 16) = t[0];
      }
      u32x2 tag = *reinterpret_cast<const u32x2*>(smem + L_TAG);
      PIN(tag);
      BAR_ALL();
      if (a) {
        lds_wait_ge<0>(wa + 64 + 4 * (lane & 3), 3);
        exchange(tag, FL_X + (16 + tm) * 2048, tn);
        lds_set(wa + 24, 2);
        STAMPV(12);
      }
    }
#pragma unroll 1
    for (int L = fresh_s(0);; ++L) {
      const unsigned sx = OFF_SX + L * SX_LAYER;
      STAMPV(1 + 16 * L);
      __builtin_amdgcn_s_setprio(1);
      u32x4 hf[4];
      unsigned key;
      { LANE_VARS;
      lds_wait_ge<0>(wa + 32 + 4 * tp, L + 1);
      if (a == 0) {
        const char* cp = smem + L_CAND + (r32 >> 3) * 144;
        const u32x4 c = *reinterpret_cast<const u32x4*>(cp + (lane & 7) * 16), rec = *reinterpret_cast<const u32x4*>(cp + 128);
        u32x4 o;
#pragma unroll
        for (int e = 0; e < 4; ++e) { unsigned h = ln_pair_s(c[e], -U2F(rec[1]), U2F(rec[2])); PIN(h); o[e] = nz(hi ? c[e] : h); }
        stg(rs, sx + SX_ROW + hi * 131072 + (tm * 4 + (r32 >> 3)) * 2048 + tn * 128 + (lane & 7) * 16, o);
        STAMPV(8 + 16 * L);
        lds_wait_ge<0>(wa + 104, L + 1);
        key = __builtin_amdgcn_readfirstlane(words[27]);
      } else {
        const u32x4 srd = {(unsigned)(uintptr_t)ws, (unsigned)((uintptr_t)ws >> 32) & 0xffffu, WS_BYTES, 0x00020000u};
        unsigned po = sx + SX_CAND + (tn * 16 + (lane >> 2)) * 128 + tp * 4;
        key = 0u;
        for (unsigned spins = 0;; ++spins) {
          unsigned v = 0u;
          asm volatile("buffer_atomic_or %0, %1, %2, 0 offen sc0 sc1\n\ts_waitcnt vmcnt(0)" : "+v"(v) : "v"(po), "s"(srd) : "memory");
          key = v == SENT ? key : max(key, v);
          po = v == SENT ? po : OOB;
          if (__all(po == OOB) || spins > SPIN_CAP) break;
        }
        key = __builtin_amdgcn_readfirstlane(wave_max_u32(key));
        words[27] = key;
        words[3] = 2047u - (key & 2047u);
        lds_set(wa + 104, L + 1);
        STAMPV(13 + 16 * L);
      }
      poll_all<4>(hf, false, [&](u32x4 (&t)[4]) {
#pragma unroll
        for (int i = 0; i < 4; ++i) t[i] = ldg(rs, sx + SX_ROW + ((2047 - (int)(key & 2047u)) >> 5) * 2048 + (16 * qa + 4 * i + (lane >> 4)) * 16);
      }, [&](const u32x4 (&t)[4]) { return none_sent(t); });
      if (L == 1) lds_set(wa + 4 + 4 * a, 2);
      }
      STAMPV(2 + 16 * L);
      f32x4 d[8];
      const f32x4 zero = {0.f, 0.f, 0.f, 0.f};
      SBAR();
      __builtin_amdgcn_s_setprio(3);
#pragma unroll
      for (int t = 0; t < 32; ++t) d[t & 7] = MFMA16(__builtin_bit_cast(s16x8, hf[t >> 3]), __builtin_bit_cast(s16x8, gf[t]), t < 8 ? zero : d[t & 7]);
      SBAR();
      __builtin_amdgcn_s_setprio(1);
      LANE_VARS;
      const int idx = 2047 - (int)(key & 2047u), win = idx >> 7;
      const bool owner = win == tm;
      auto pick2 = [&](const f32x4 (&d)[8]) {
        u32x2 o;
#pragma unroll
        for (int e = 0; e < 2; ++e) {
          const float x = tblk ? d[2 + e][0] : d[e][0], y = tblk ? d[6 + e][0] : d[4 + e][0];
          o[e] = nzf(hi ? y : x);
        }
        return o;
      };
      if (ffn) {
        const unsigned ppos = (lane & 15) * 32 + (lane >> 4) * 8;
        __builtin_amdgcn_raw_buffer_store_b64(pick2(d), rs, sx + SX_GP + (grp * 8 + qa) * 512 + ppos, 0, 16);
        STAMPV(9 + 16 * L);
        __builtin_amdgcn_s_sleep(16);
        u32x2 gu[8];
        for (unsigned spins = 0;; ++spins) {
#pragma unroll
          for (int i = 0; i < 8; ++i) gu[i] = __builtin_amdgcn_raw_buffer_load_b64(rs, sx + SX_GP + (grp * 8 + i) * 512 + lane * 8, 0, 16);
          SBAR();
          unsigned m = 0u;
#pragma unroll
          for (int i = 0; i < 8; ++i) m = max(max(m, gu[i][0]), gu[i][1]);
          if (__all(m != SENT) || spins > SPIN_CAP) break;
          poll_pause(L == 0);
        }
        STAMPV(10 + 16 * L);
        float t0 = opq(U2F(gu[0][0])), t1 = opq(U2F(gu[0][1]));
#pragma unroll
        for (int i = 1; i < 8; ++i) { t0 = fadd(t0, U2F(gu[i][0])); t1 = fadd(t1, U2F(gu[i][1])); }
        auto sw = __builtin_amdgcn_permlane32_swap(__float_as_uint(t0), __float_as_uint(t1), false, false);
        const float gv = rbf(U2F(sw[0])), uv = rbf(U2F(sw[1]));
        reinterpret_cast<u16*>(smem + L_GU + a * 128)[2 * r32 + hi] = (u16)pack2(fmul(rbf(fmul(gv, RCP(fadd(1.0f, __expf(-gv))))), uv), 0.f);
        asm volatile("s_waitcnt lgkmcnt(0)" ::: "memory");
        const s16x8 a0 = *reinterpret_cast<const s16x8*>(smem + L_GU + a * 128 + (lane >> 4) * 16), a1 = *reinterpret_cast<const s16x8*>(smem + L_GU + a * 128 + 64 + (lane >> 4) * 16);
        SBAR();
        __builtin_amdgcn_s_setprio(3);
#pragma unroll
        for (int t = 0; t < 16; ++t) d[t & 7] = MFMA16(t < 8 ? a0 : a1, __builtin_bit_cast(s16x8, wdf[t]), t < 8 ? zero : d[t & 7]);
        SBAR();
        __builtin_amdgcn_s_setprio(1);
        __builtin_amdgcn_raw_buffer_store_b64(pick2(d), rs, sx + SX_YP + grp * 4096 + qa * 512 + ppos, 0, 16);
      }
      STAMPV(3 + 16 * L);
      if (L == 1 && (cg != (kr & 7) || a)) { STAMP_END; __builtin_amdgcn_endpgm(); }
      s16x8 wpf[12];
#pragma unroll
      for (int t = 0; t < 12; ++t) wpf[t] = tr2(smem + L_SL + a * 12288 + (16 * hi + 4 * tblk + tq) * 384 + t * 32 + tp * 8, 8 * 384);
#pragma unroll
      for (int t = 0; t < 12; ++t) PIN(wpf[t]);
      if (L == 0 && !owner) lds_set(wa + 4 + 4 * a, 1);
      __builtin_amdgcn_s_sleep(9);
      if (L == 0 && !ffn) __builtin_amdgcn_s_sleep(64);
      const unsigned rk = key >> 16;
      float rsig = rbf(RCP(fadd(1.0f, __expf(-lo16((rk & 0x8000u) ? (rk & 0x7fffu) : (~rk & 0xffffu))))));
      PIN(rsig);
      const int n = 4 * (lane & 7) + 2 * tblk + hi;
      const int fo = ((n >> 4) * 2 + ((n >> 2) & 1)) * 8 + ((n >> 3) & 1) * 4 + (n & 3);
      unsigned odst = (L == 1 && !(lane & 8)) ? (idx * D_ + kr * 32 + n) * 2 : OOB;
      PIN(odst);
      u32x4 pv[8];
      poll_all<8>(pv, L == 0, [&](u32x4 (&t)[8]) {
#pragma unroll
        for (int i = 0; i < 7; ++i) t[i] = ldg(rs, sx + SX_YP + (8 * i + (lane >> 3)) * 4096 + kr * 128 + (lane & 7) * 16);
        t[7][0] = t[7][2] = __builtin_amdgcn_raw_buffer_load_b32(rs, sx + SX_ROW + (idx >> 5) * 2048 + kr * 64 + (fo >> 1) * 4, 0, 16);
        t[7][1] = t[7][3] = __builtin_amdgcn_raw_buffer_load_b32(rs, sx + SX_ROW + 131072 + (idx >> 5) * 2048 + kr * 64 + (fo >> 1) * 4, 0, 16);
      }, [&](const u32x4 (&t)[8]) { return none_sent(t); });
      const float hv = (fo & 1) ? hi16(pv[7][0]) : lo16(pv[7][0]), xv = (fo & 1) ? hi16(pv[7][1]) : lo16(pv[7][1]);
      STAMPV(4 + 16 * L);
      float ys;
      {
        const f32x4 t = sum4<7>(pv);
        auto p = __builtin_amdgcn_permlane32_swap(__float_as_uint(t[0]), __float_as_uint(t[1]), false, false);
        auto q = __builtin_amdgcn_permlane32_swap(__float_as_uint(t[2]), __float_as_uint(t[3]), false, false);
        auto r = __builtin_amdgcn_permlane16_swap(__float_as_uint(fadd(U2F(p[0]), U2F(p[1]))), __float_as_uint(fadd(U2F(q[0]), U2F(q[1]))), false, false);
        ys = fadd(U2F(r[0]), U2F(r[1]));
        ys = fadd(ys, U2F(dpp<0x128>(__float_as_uint(ys))));
      }
      const unsigned ob = pack2(fadd(xv, rbf(fadd(hv, rbf(fmul(rbf(ys), rsig))))), 0.f) & 0xffffu;
      __builtin_amdgcn_raw_buffer_store_b16((u16)ob, __builtin_amdgcn_make_buffer_rsrc(out, 0, 2048 * D_ * 2, 0x00020000), odst, 0, 0);
      if (L == 1) {
        STAMPV(5 + 16 * L);
        STAMP_END;
        __builtin_amdgcn_endpgm();
      }
      reinterpret_cast<u16*>(smem + L_X2F + a * 64)[fo] = (u16)ob;
      asm volatile("s_waitcnt lgkmcnt(0)" ::: "memory");
      {
        const s16x8 xa = *reinterpret_cast<const s16x8*>(smem + L_X2F + a * 64 + (lane >> 4) * 16);
        const u32x4 xf = *reinterpret_cast<const u32x4*>(smem + L_X2F + a * 64 + tp * 16);
        f32x4 z[12];
        SBAR();
        __builtin_amdgcn_s_setprio(3);
#pragma unroll
        for (int t = 0; t < 12; ++t) z[t] = MFMA16(xa, wpf[t], zero);
        SBAR();
        __builtin_amdgcn_s_setprio(1);
        const float x2 = lo16(ob), s1 = fmul(wave_sum(x2), 0.5f), s2 = fmul(wave_sum(fmul(x2, x2)), 0.5f);
        u32x4 o;
#pragma unroll
        for (int e = 0; e < 4; ++e) {
          const unsigned sp = lane < 52 ? (e == 0 ? __float_as_uint(s1) : __float_as_uint(s2)) : xf[e];
          const unsigned x = __float_as_uint(tblk ? z[4 + e][0] : z[e][0]), y = tblk ? sp : __float_as_uint(z[8 + e][0]);
          o[e] = nz(hi ? y : x);
        }
        const unsigned pos = lane < 24 ? lane * 16 : lane < 48 ? lane * 16 + 128 : lane < 52 ? (lane & 1) * 512 + 384 : 400 + tp * 16;
        stg(rs, lane < 56 ? OFF_SX + SX_QP + ((2 * cg + a) * 32 + kr) * 1024 + pos : OOB, o);
      }
      STAMPV(5 + 16 * L);
      if (owner) {
        __builtin_amdgcn_s_sleep(13);
        const int c25 = r32 < 25 ? r32 : 24, pl = 24 * a + c24, col = 64 * (pl >> 4) + (pl & 15);
        const int pfo = (col & ~15) + ((col >> 2) & 1) * 16 + ((col >> 3) & 1) * 8 + (col & 3);
        float cs[4];
#pragma unroll
        for (int e = 0; e < 4; ++e) { cs[e] = reinterpret_cast<const float*>(smem + L_CS)[col + 16 * e]; PIN(cs[e]); }
        u32x4 qv[17];
        poll_all<17>(qv, true, [&](u32x4 (&t)[17]) {
#pragma unroll
          for (int i = 0; i < 16; ++i) t[i] = ldg(rs, OFF_SX + SX_QP + (tn * 32 + 2 * i + hi) * 1024 + a * 512 + c25 * 16);
          t[16] = ldg(rs, OFF_SX + SX_QP + (tn * 34 + hi) * 1024 + 400 + tp * 16);
        }, [&](const u32x4 (&t)[17]) { return none_sent(t); });
        *reinterpret_cast<u32x4*>(smem + L_X2P + hi * 64 + tp * 16) = qv[16];
        f32x4 t = sum4<16>(qv);
#pragma unroll
        for (int e = 0; e < 4; ++e) t[e] = half_swap_sum(t[e]);
        const float mean = fmul(U2F(__builtin_amdgcn_readlane(__float_as_uint(t[0]), 24)), 1.0f / 1024.0f);
        const float var = fmaxf(ffma(-mean, mean, fmul(U2F(__builtin_amdgcn_readlane(__float_as_uint(t[1]), 24)), 1.0f / 1024.0f)), 0.f);
        float mb, den;
        ln_scalars(mean, var, mb, den);
        const float rd = RCP(den);
        if (lane < 24) {
#pragma unroll
          for (int e = 0; e < 4; ++e) reinterpret_cast<u16*>(smem + L_PATCH)[pfo + 32 * (e >> 1) + 4 * (e & 1)] = (u16)pack2(fmul(ffma(-mb, cs[e], t[e]), pl < 16 ? fmul(rd, QS) : rd), 0.f);
        }
        lds_set(wa + 4 + 4 * a, L + 1);
      }
      STAMPV(6 + 16 * L);
      BAR_ALL();
      unsigned dv = fresh_lane();
      dv = (dv >> 4) * (8 * D_ * 2) + (dv & 15) * 16;
      PIN(dv);
      wait_go(1);
      load_wd(dv);
      STAMPV(23);
      if (1) __builtin_amdgcn_s_setprio(0);
      lds_wait_ge<4>(wa + 80 + 4 * (fresh_lane() & 3), 4);
      __builtin_amdgcn_s_setprio(0);
      transpose8(wdf); transpose8(wdf + 8);
      BARRIER();
    }
  }
  const int tm_ = tm, tn_ = tn, wid_ = wid;
#define KSKIP "{s49}"(tn * 4096), "{s50}"(((tn + 1) & 15) * 4096)
  const u32x4 srd = {(unsigned)(uintptr_t)ws, (unsigned)((uintptr_t)ws >> 32) & 0xffffu, WS_BYTES, 0x00020000u};
  u32x4 x1p[4];
  asm volatile("" : "=v"(x1p[0]), "=v"(x1p[1]), "=v"(x1p[2]), "=v"(x1p[3]));
#pragma unroll 1
  for (int L = 0; L < 2; ++L) {
    const unsigned sx = OFF_SX + L * SX_LAYER;
    u32x8 q[4];
    u32x16 kva, kvb;
    f32x16 oz[5];
    {
      f32x16 acc[6];
      u32x16 stA, stB, xr0, xr1, xr2, xr3;
      u32x4 stC;
      asm volatile("" : "={v[112:127]}"(stB), "={v[128:131]}"(stC));
      { PHASE_VARS;
      stB[8] = (L ? OFF_X1 : OFF_HF) + tile * TS + tblk * 1024 + (hi * 32 + (lane & 15)) * 16;
      stB[9] = lbase + tblk * HALFB + wrow * 384 + (((4 * hh + (tp >> 1)) ^ wsw) << 4) + 8 * (tp & 1);
      stB[10] = lbase + tblk * HALFB + wrow * 384 + (((4 * (hh ^ 1) + (tp >> 1)) ^ wsw) << 4) + 8 * (tp & 1);
      stB[13] = lbase + L_FULL + (lane & 1) * 256;
      stB[15] = lbase + L_FREE + wid * 256 + lane * 4;
      stC[0] = 32 * L + 2; }
      unsigned soff, slot;
      if (L == 0) {
        lds_wait_ge<0>(lbase + L_W + 24, 1);
        STAMPV(39);
        soff = tn * 4096;
        asm volatile(
          "v_add_u32 v131, s44, v120\n\t"
          "buffer_load_dwordx4 v[132:135], v131, s[40:43], 0 offen offset:0 sc1\n\t"
          "buffer_load_dwordx4 v[136:139], v131, s[40:43], 0 offen offset:256 sc1\n\t"
          "buffer_load_dwordx4 v[140:143], v131, s[40:43], 0 offen offset:2048 sc1\n\t"
          "buffer_load_dwordx4 v[144:147], v131, s[40:43], 0 offen offset:2304 sc1\n\t"
          "s_mov_b32 s44, s51\n\t"
          "v_add_u32 v131, s44, v120\n\t"
          "buffer_load_dwordx4 v[148:151], v131, s[40:43], 0 offen offset:0 sc1\n\t"
          "buffer_load_dwordx4 v[152:155], v131, s[40:43], 0 offen offset:256 sc1\n\t"
          "buffer_load_dwordx4 v[156:159], v131, s[40:43], 0 offen offset:2048 sc1\n\t"
          "buffer_load_dwordx4 v[160:163], v131, s[40:43], 0 offen offset:2304 sc1\n\t"
          "s_addk_i32 s44, 0x1000\n\t"
          "s_bitset0_b32 s44, 16\n\t"
          "s_cmp_eq_u32 s44, s49\n\t"
          "s_cselect_b32 s44, s50, s44\n\t"
          "v_add_u32 v131, s44, v120\n\t"
          "buffer_load_dwordx4 v[164:167], v131, s[40:43], 0 offen offset:0 sc1\n\t"
          "buffer_load_dwordx4 v[168:171], v131, s[40:43], 0 offen offset:256 sc1\n\t"
          "buffer_load_dwordx4 v[172:175], v131, s[40:43], 0 offen offset:2048 sc1\n\t"
          "buffer_load_dwordx4 v[176:179], v131, s[40:43], 0 offen offset:2304 sc1\n\t"
          "s_addk_i32 s44, 0x1000\n\t"
          "s_bitset0_b32 s44, 16\n\t"
          "s_cmp_eq_u32 s44, s49\n\t"
          "s_cselect_b32 s44, s50, s44\n\t"
          "v_add_u32 v131, s44, v120\n\t"
          "buffer_load_dwordx4 v[180:183], v131, s[40:43], 0 offen offset:0 sc1\n\t"
          "buffer_load_dwordx4 v[184:187], v131, s[40:43], 0 offen offset:256 sc1\n\t"
          "buffer_load_dwordx4 v[188:191], v131, s[40:43], 0 offen offset:2048 sc1\n\t"
          "buffer_load_dwordx4 v[192:195], v131, s[40:43], 0 offen offset:2304 sc1\n\t"
          : "={v[132:147]}"(xr0), "={v[148:163]}"(xr1), "={v[164:179]}"(xr2), "={v[180:195]}"(xr3), "+{s44}"(soff), "+{v[128:131]}"(stC) : "{v[112:127]}"(stB), "{s[40:43]}"(srd), KSKIP, "{s51}"(kpos(tm, tn, 1) * 4096) : "scc", "memory");
      } else {
        u32x4 t[4] = {x1p[0], x1p[1], x1p[2], x1p[3]};
        row_swap(t[0], t[1]); row_swap(t[2], t[3]);
        xr0 = cat4(t[0], t[1], t[2], t[3]);
        asm volatile("" : "={v[148:163]}"(xr1), "={v[164:179]}"(xr2), "={v[180:195]}"(xr3));
      }
      STAMPV(1 + 16 * L);
      asm volatile(
        "v_mov_b32 v123, v121\n\t"
        "v_mov_b32 v124, v122\n\t"
        "s_mov_b64 vcc, -1\n\t"
        "s_cbranch_vccnz 10f\n\t"
        "11:\n\t"
        "s_movk_i32 s46, 12352\n\t"
        "s_mov_b32 s47, 111168\n\t"
        "ds_read_b64_tr_b16 v[96:97], v123 offset:256\n\t"
        "ds_read_b64_tr_b16 v[98:99], v123 offset:3328\n\t"
        "ds_read_b64_tr_b16 v[100:101], v123 offset:288\n\t"
        "ds_read_b64_tr_b16 v[102:103], v123 offset:3360\n\t"
        "ds_read_b64_tr_b16 v[104:105], v124 offset:256\n\t"
        "ds_read_b64_tr_b16 v[106:107], v124 offset:3328\n\t"
        "ds_write_b32 v127, v128\n\t"
        "ds_read_b64_tr_b16 v[108:109], v124 offset:288\n\t"
        "ds_read_b64_tr_b16 v[110:111], v124 offset:3360\n\t"
        "ds_read_b64_tr_b16 v[112:113], v123 offset:128\n\t"
        "ds_read_b64_tr_b16 v[114:115], v123 offset:3200\n\t"
        "v_add_u32 v128, 2, v128\n\t"
        "s_waitcnt vmcnt(14) lgkmcnt(7)\n\t"
        "v_mfma_f32_16x16x32_bf16 v[64:67], v[132:135], v[96:99], 0\n\t"
        "ds_read_b64_tr_b16 v[116:117], v123 offset:160\n\t"
        "v_mfma_f32_16x16x32_bf16 v[72:75], v[136:139], v[96:99], 0\n\t"
        "ds_read_b64_tr_b16 v[118:119], v123 offset:3232\n\t"
        "v_mfma_f32_16x16x32_bf16 v[68:71], v[132:135], v[100:103], 0\n\t"
        "ds_read_b64_tr_b16 v[96:97], v124 offset:128\n\t"
        "v_mfma_f32_16x16x32_bf16 v[76:79], v[136:139], v[100:103], 0\n\t"
        "ds_read_b64_tr_b16 v[98:99], v124 offset:3200\n\t"
        "s_waitcnt lgkmcnt(6)\n\t"
        "v_mfma_f32_16x16x32_bf16 v[80:83], v[132:135], v[104:107], 0\n\t"
        "ds_read_b64_tr_b16 v[100:101], v124 offset:160\n\t"
        "v_mfma_f32_16x16x32_bf16 v[88:91], v[136:139], v[104:107], 0\n\t"
        "ds_read_b64_tr_b16 v[102:103], v124 offset:3232\n\t"
        "v_mfma_f32_16x16x32_bf16 v[84:87], v[132:135], v[108:111], 0\n\t"
        "ds_read_b64_tr_b16 v[104:105], v123 offset:0\n\t"
        "v_add_u32 v129, s46, v121\n\t"
        "v_mfma_f32_16x16x32_bf16 v[92:95], v[136:139], v[108:111], 0\n\t"
        "ds_read_b64_tr_b16 v[106:107], v123 offset:3072\n\t"
        "s_waitcnt lgkmcnt(6)\n\t"
        "v_mfma_f32_16x16x32_bf16 v[32:35], v[112:115], v[132:135], 0\n\t"
        "ds_read_b64_tr_b16 v[108:109], v123 offset:32\n\t"
        "v_mfma_f32_16x16x32_bf16 v[40:43], v[112:115], v[136:139], 0\n\t"
        "ds_read_b64_tr_b16 v[110:111], v123 offset:3104\n\t"
        "v_add_u32 v130, s46, v122\n\t"
        "v_mfma_f32_16x16x32_bf16 v[36:39], v[116:119], v[132:135], 0\n\t"
        "ds_read_b64_tr_b16 v[112:113], v124 offset:0\n\t"
        "v_mfma_f32_16x16x32_bf16 v[44:47], v[116:119], v[136:139], 0\n\t"
        "ds_read_b64_tr_b16 v[114:115], v124 offset:3072\n\t"
        "s_waitcnt lgkmcnt(6)\n\t"
        "v_mfma_f32_16x16x32_bf16 v[48:51], v[96:99], v[132:135], 0\n\t"
        "ds_read_b64_tr_b16 v[116:117], v124 offset:32\n\t"
        "v_mfma_f32_16x16x32_bf16 v[56:59], v[96:99], v[136:139], 0\n\t"
        "ds_read_b64_tr_b16 v[118:119], v124 offset:3104\n\t"
        "v_mfma_f32_16x16x32_bf16 v[52:55], v[100:103], v[132:135], 0\n\t"
        "ds_read_b64_tr_b16 v[96:97], v129 offset:256\n\t"
        "s_addk_i32 s46, 12352\n\t"
        "v_mfma_f32_16x16x32_bf16 v[60:63], v[100:103], v[136:139], 0\n\t"
        "ds_read_b64_tr_b16 v[98:99], v129 offset:3328\n\t"
        "s_waitcnt lgkmcnt(6)\n\t"
        "v_mfma_f32_16x16x32_bf16 v[0:3], v[104:107], v[132:135], 0\n\t"
        "ds_read_b64_tr_b16 v[100:101], v129 offset:288\n\t"
        "s_cmp_eq_u32 s46, s47\n\t"
        "v_mfma_f32_16x16x32_bf16 v[8:11], v[104:107], v[136:139], 0\n\t"
        "ds_read_b64_tr_b16 v[102:103], v129 offset:3360\n\t"
        "s_cselect_b32 s46, 0, s46\n\t"
        "v_mfma_f32_16x16x32_bf16 v[4:7], v[108:111], v[132:135], 0\n\t"
        "ds_read_b64_tr_b16 v[104:105], v130 offset:256\n\t"
        "v_mfma_f32_16x16x32_bf16 v[12:15], v[108:111], v[136:139], 0\n\t"
        "ds_read_b64_tr_b16 v[106:107], v130 offset:3328\n\t"
        "s_waitcnt lgkmcnt(6)\n\t"
        "v_mfma_f32_16x16x32_bf16 v[16:19], v[112:115], v[132:135], 0\n\t"
        "ds_read_b64_tr_b16 v[108:109], v130 offset:288\n\t"
        "v_mfma_f32_16x16x32_bf16 v[24:27], v[112:115], v[136:139], 0\n\t"
        "ds_read_b64_tr_b16 v[110:111], v130 offset:3360\n\t"
        "v_mfma_f32_16x16x32_bf16 v[20:23], v[116:119], v[132:135], 0\n\t"
        "ds_read_b64_tr_b16 v[112:113], v129 offset:128\n\t"
        "v_mfma_f32_16x16x32_bf16 v[28:31], v[116:119], v[136:139], 0\n\t"
        "ds_read_b64_tr_b16 v[114:115], v129 offset:3200\n\t"
        "s_waitcnt vmcnt(12) lgkmcnt(6)\n\t"
        "v_mfma_f32_16x16x32_bf16 v[64:67], v[140:143], v[96:99], v[64:67]\n\t"
        "ds_read_b64_tr_b16 v[116:117], v129 offset:160\n\t"
        "v_mfma_f32_16x16x32_bf16 v[72:75], v[144:147], v[96:99], v[72:75]\n\t"
        "ds_read_b64_tr_b16 v[118:119], v129 offset:3232\n\t"
        "v_mfma_f32_16x16x32_bf16 v[68:71], v[140:143], v[100:103], v[68:71]\n\t"
        "ds_read_b64_tr_b16 v[96:97], v130 offset:128\n\t"
        "ds_read_b32 v126, v125\n\t"
        "v_mfma_f32_16x16x32_bf16 v[76:79], v[144:147], v[100:103], v[76:79]\n\t"
        "ds_read_b64_tr_b16 v[98:99], v130 offset:3200\n\t"
        "s_waitcnt lgkmcnt(7)\n\t"
        "v_mfma_f32_16x16x32_bf16 v[80:83], v[140:143], v[104:107], v[80:83]\n\t"
        "ds_read_b64_tr_b16 v[100:101], v130 offset:160\n\t"
        "v_mfma_f32_16x16x32_bf16 v[88:91], v[144:147], v[104:107], v[88:91]\n\t"
        "ds_read_b64_tr_b16 v[102:103], v130 offset:3232\n\t"
        "v_mfma_f32_16x16x32_bf16 v[84:87], v[140:143], v[108:111], v[84:87]\n\t"
        "ds_read_b64_tr_b16 v[104:105], v129 offset:0\n\t"
        "v_add_u32 v123, s46, v121\n\t"
        "v_mfma_f32_16x16x32_bf16 v[92:95], v[144:147], v[108:111], v[92:95]\n\t"
        "ds_read_b64_tr_b16 v[106:107], v129 offset:3072\n\t"
        "s_waitcnt lgkmcnt(5)\n\t"
        "v_mfma_f32_16x16x32_bf16 v[32:35], v[112:115], v[140:143], v[32:35]\n\t"
        "ds_read_b64_tr_b16 v[108:109], v129 offset:32\n\t"
        "v_cmp_gt_u32 vcc, v128, v126\n\t"
        "v_mfma_f32_16x16x32_bf16 v[40:43], v[112:115], v[144:147], v[40:43]\n\t"
        "ds_read_b64_tr_b16 v[110:111], v129 offset:3104\n\t"
        "v_add_u32 v124, s46, v122\n\t"
        "v_mfma_f32_16x16x32_bf16 v[36:39], v[116:119], v[140:143], v[36:39]\n\t"
        "ds_read_b64_tr_b16 v[112:113], v130 offset:0\n\t"
        "v_mfma_f32_16x16x32_bf16 v[44:47], v[116:119], v[144:147], v[44:47]\n\t"
        "ds_read_b64_tr_b16 v[114:115], v130 offset:3072\n\t"
        "s_waitcnt lgkmcnt(6)\n\t"
        "v_mfma_f32_16x16x32_bf16 v[48:51], v[96:99], v[140:143], v[48:51]\n\t"
        "ds_read_b64_tr_b16 v[116:117], v130 offset:32\n\t"
        "v_mfma_f32_16x16x32_bf16 v[56:59], v[96:99], v[144:147], v[56:59]\n\t"
        "ds_read_b64_tr_b16 v[118:119], v130 offset:3104\n\t"
        "s_cbranch_vccnz 12f\n\t"
        "13:\n\t"
        "v_mfma_f32_16x16x32_bf16 v[52:55], v[100:103], v[140:143], v[52:55]\n\t"
        "ds_read_b64_tr_b16 v[96:97], v123 offset:256\n\t"
        "s_addk_i32 s46, 12352\n\t"
        "v_mfma_f32_16x16x32_bf16 v[60:63], v[100:103], v[144:147], v[60:63]\n\t"
        "ds_read_b64_tr_b16 v[98:99], v123 offset:3328\n\t"
        "s_waitcnt lgkmcnt(6)\n\t"
        "v_mfma_f32_16x16x32_bf16 v[0:3], v[104:107], v[140:143], v[0:3]\n\t"
        "ds_read_b64_tr_b16 v[100:101], v123 offset:288\n\t"
        "s_cmp_eq_u32 s46, s47\n\t"
        "v_mfma_f32_16x16x32_bf16 v[8:11], v[104:107], v[144:147], v[8:11]\n\t"
        "ds_read_b64_tr_b16 v[102:103], v123 offset:3360\n\t"
        "s_cselect_b32 s46, 0, s46\n\t"
        "v_mfma_f32_16x16x32_bf16 v[4:7], v[108:111], v[140:143], v[4:7]\n\t"
        "ds_read_b64_tr_b16 v[104:105], v124 offset:256\n\t"
        "v_mfma_f32_16x16x32_bf16 v[12:15], v[108:111], v[144:147], v[12:15]\n\t"
        "ds_read_b64_tr_b16 v[106:107], v124 offset:3328\n\t"
        "s_waitcnt lgkmcnt(6)\n\t"
        "v_mfma_f32_16x16x32_bf16 v[16:19], v[112:115], v[140:143], v[16:19]\n\t"
        "ds_write_b32 v127, v128\n\t"
        "ds_read_b64_tr_b16 v[108:109], v124 offset:288\n\t"
        "v_mfma_f32_16x16x32_bf16 v[24:27], v[112:115], v[144:147], v[24:27]\n\t"
        "ds_read_b64_tr_b16 v[110:111], v124 offset:3360\n\t"
        "v_add_u32 v128, 2, v128\n\t"
        "v_mfma_f32_16x16x32_bf16 v[20:23], v[116:119], v[140:143], v[20:23]\n\t"
        "ds_read_b64_tr_b16 v[112:113], v123 offset:128\n\t"
        "v_mfma_f32_16x16x32_bf16 v[28:31], v[116:119], v[144:147], v[28:31]\n\t"
        "ds_read_b64_tr_b16 v[114:115], v123 offset:3200\n\t"
        "s_waitcnt lgkmcnt(0)\n\t"
        "s_nop 7\n\t"
        "s_branch 9f\n\t"
        "10:\n\t"
        "s_mov_b32 s48, 0x10000\n\t"
        "8:\n\t"
        "ds_read_b32 v126, v125\n\t"
        "s_sub_u32 s48, s48, 1\n\t"
        "s_waitcnt lgkmcnt(0)\n\t"
        "s_cbranch_scc1 11b\n\t"
        "v_cmp_gt_u32 vcc, v128, v126\n\t"
        "s_cbranch_vccnz 8b\n\t"
        "s_branch 11b\n\t"
        "12:\n\t"
        "s_mov_b32 s48, 0x10000\n\t"
        "8:\n\t"
        "ds_read_b32 v126, v125\n\t"
        "s_sub_u32 s48, s48, 1\n\t"
        "s_waitcnt lgkmcnt(0)\n\t"
        "s_cbranch_scc1 13b\n\t"
        "v_cmp_gt_u32 vcc, v128, v126\n\t"
        "s_cbranch_vccnz 8b\n\t"
        "s_branch 13b\n\t"
        "9:\n\t"
        : "={v[0:15]}"(acc[0]), "={v[16:31]}"(acc[1]), "={v[32:47]}"(acc[2]), "={v[48:63]}"(acc[3]), "={v[64:79]}"(acc[4]), "={v[80:95]}"(acc[5]),
          "={v[96:111]}"(stA), "+{v[112:127]}"(stB), "+{v[128:131]}"(stC), "={s46}"(slot)
        : "{v[132:147]}"(xr0) : "s47", "s48", "vcc", "scc", "memory");
      STAMPV(2 + 16 * L);
      if (L == 1) {
        PHASE_VARS;
        float S, Q, Dd;
        row_stats_sum(rs, OFF_SX, tile, lane, S, Q, Dd);
        STAMPV(36);
        lds_wait_ge<0>(lbase + L_W + 24, 2);
        soff = kpos(tm, tn, 1) * 4096;
        asm volatile(
          "v_add_u32 v131, s44, v120\n\t"
          "buffer_load_dwordx4 v[148:151], v131, s[40:43], 0 offen offset:0 sc1\n\t"
          "buffer_load_dwordx4 v[152:155], v131, s[40:43], 0 offen offset:256 sc1\n\t"
          "buffer_load_dwordx4 v[156:159], v131, s[40:43], 0 offen offset:2048 sc1\n\t"
          "buffer_load_dwordx4 v[160:163], v131, s[40:43], 0 offen offset:2304 sc1\n\t"
          : "={v[148:163]}"(xr1), "={v[164:179]}"(xr2), "={v[180:195]}"(xr3), "+{s44}"(soff), "+v"(S), "+v"(Q), "+v"(Dd), "+{v[128:131]}"(stC) : "{v[112:127]}"(stB), "{s[40:43]}"(srd), KSKIP, "{s51}"(kpos(tm, tn, 1) * 4096) : "scc", "memory");
        float mean, var, mb1, rd1, mb2, den2;
        row_stats_fin(rs, OFF_SX + SX_CAND + (tn * 16 + tm) * 128 + wid * 4, smem, 1, tile, lane, S, Q, Dd, U2F(words[0]), x1p, mean, var, mb1, rd1);
        ln_scalars(mean + (mean - mb1) * rd1, var * (1.0f + rd1) * (1.0f + rd1), mb2, den2);
        const float rd2 = RCP(den2);
        const float al = (1.0f + rd1) * rd2;
        const float be = -(mb1 * rd1 + mb2) * RCP(1.0f + rd1);
        reinterpret_cast<float*>(smem + L_AB)[wid * 64 + lane] = hi ? be : al;
        reinterpret_cast<float*>(smem + L_AB)[256 + wid * 64 + lane] = hi ? 1.0f + rd1 : -mb1 * rd1;
        soff = kpos(tm, tn, 1 + 0) * 4096;
        asm volatile(
          "s_nop 0\n\t"
          "v_add_u32 v131, s44, v120\n\t"
          "s_addk_i32 s44, 0x1000\n\t"
          "s_bitset0_b32 s44, 16\n\t"
          "s_cmp_eq_u32 s44, s49\n\t"
          "s_cselect_b32 s44, s50, s44\n\t"
          "v_add_u32 v131, s44, v120\n\t"
          "buffer_load_dwordx4 v[164:167], v131, s[40:43], 0 offen offset:0 sc1\n\t"
          "buffer_load_dwordx4 v[168:171], v131, s[40:43], 0 offen offset:256 sc1\n\t"
          "buffer_load_dwordx4 v[172:175], v131, s[40:43], 0 offen offset:2048 sc1\n\t"
          "buffer_load_dwordx4 v[176:179], v131, s[40:43], 0 offen offset:2304 sc1\n\t"
          "s_addk_i32 s44, 0x1000\n\t"
          "s_bitset0_b32 s44, 16\n\t"
          "s_cmp_eq_u32 s44, s49\n\t"
          "s_cselect_b32 s44, s50, s44\n\t"
          "v_add_u32 v131, s44, v120\n\t"
          "buffer_load_dwordx4 v[180:183], v131, s[40:43], 0 offen offset:0 sc1\n\t"
          "buffer_load_dwordx4 v[184:187], v131, s[40:43], 0 offen offset:256 sc1\n\t"
          "buffer_load_dwordx4 v[188:191], v131, s[40:43], 0 offen offset:2048 sc1\n\t"
          "buffer_load_dwordx4 v[192:195], v131, s[40:43], 0 offen offset:2304 sc1\n\t"
          : "+{v[148:163]}"(xr1), "+{v[164:179]}"(xr2), "+{v[180:195]}"(xr3), "+{s44}"(soff), "+{v[128:131]}"(stC) : "{v[112:127]}"(stB), "{s[40:43]}"(srd), KSKIP, "{s51}"(kpos(tm, tn, 1) * 4096) : "scc", "memory");
        const float* ab = reinterpret_cast<const float*>(smem + L_AB) + wid * 64 + 32;
        const float bt[2] = {ab[lane & 15], ab[16 + (lane & 15)]};
#pragma unroll
        for (int jj = 0; jj < 2; ++jj) {
          f32x4 cs[2][2], bv[2];
          float cv[2];
#pragma unroll
          for (int i = 0; i < 2; ++i) {
            const float* p = reinterpret_cast<const float*>(smem + L_CS) + (jj ^ hh) * 32 + 16 * i;
            bv[i] = *reinterpret_cast<const f32x4*>(ab + 16 * i + 4 * (lane >> 4));
            cs[0][i] = *reinterpret_cast<const f32x4*>(p + 4 * (lane >> 4)); cs[1][i] = *reinterpret_cast<const f32x4*>(p + 64 + 4 * (lane >> 4));
            cv[i] = p[128 + (lane & 15)];
          }
          SBAR();
#pragma unroll
          for (int j = 0; j < 3; ++j)
#pragma unroll
            for (int e = 0; e < 16; ++e) acc[2 * j + jj][e] += j < 2 ? bt[e >> 3] * cs[j & 1][(e >> 2) & 1][e & 3] : bv[e >> 3][e & 3] * cv[(e >> 2) & 1];
          SBAR();
        }
      }
      unsigned ep[3];
      f32x2 al2;
      { PHASE_VARS;
        float* ab = reinterpret_cast<float*>(smem + L_AB) + wid * 64;
        if (L == 0) ab[lane] = 1.0f;
        al2 = f32x2{ab[lane & 15], ab[16 + (lane & 15)]};
        const unsigned dst = OFF_KV + L * (10 * MBY) + tn * 2 * HS + tile * 4096 + lane * 16;
        ep[0] = dst + hh * HS; ep[1] = dst + (hh ^ 1) * HS; ep[2] = lbase + L_AB + wid * 256 + (lane >> 4) * 16; }
      STAMPV(3 + 16 * L);
      soff = kpos(tm, tn, 1 + 2) * 4096;
      asm volatile(
        "s_mov_b32 s47, 111168\n\t"
        "v_add_u32 v131, s44, v120\n\t"
        "s_mov_b32 s45, 2\n\t"
        "1:\n\t"
        "s_waitcnt vmcnt(10) lgkmcnt(7)\n\t"
        "v_mfma_f32_16x16x32_bf16 v[64:67], v[148:151], v[96:99], v[64:67]\n\t"
        "ds_read_b64_tr_b16 v[116:117], v123 offset:160\n\t"
        "s_addk_i32 s44, 0x1000\n\t"
        "v_mfma_f32_16x16x32_bf16 v[72:75], v[152:155], v[96:99], v[72:75]\n\t"
        "ds_read_b64_tr_b16 v[118:119], v123 offset:3232\n\t"
        "s_bitset0_b32 s44, 16\n\t"
        "v_mfma_f32_16x16x32_bf16 v[68:71], v[148:151], v[100:103], v[68:71]\n\t"
        "ds_read_b64_tr_b16 v[96:97], v124 offset:128\n\t"
        "s_cmp_eq_u32 s44, s49\n\t"
        "v_mfma_f32_16x16x32_bf16 v[76:79], v[152:155], v[100:103], v[76:79]\n\t"
        "ds_read_b64_tr_b16 v[98:99], v124 offset:3200\n\t"
        "s_waitcnt lgkmcnt(6)\n\t"
        "v_mfma_f32_16x16x32_bf16 v[80:83], v[148:151], v[104:107], v[80:83]\n\t"
        "ds_read_b64_tr_b16 v[100:101], v124 offset:160\n\t"
        "s_cselect_b32 s44, s50, s44\n\t"
        "v_mfma_f32_16x16x32_bf16 v[88:91], v[152:155], v[104:107], v[88:91]\n\t"
        "ds_read_b64_tr_b16 v[102:103], v124 offset:3232\n\t"
        "v_add_u32 v131, s44, v120\n\t"
        "v_mfma_f32_16x16x32_bf16 v[84:87], v[148:151], v[108:111], v[84:87]\n\t"
        "ds_read_b64_tr_b16 v[104:105], v123 offset:0\n\t"
        "v_add_u32 v129, s46, v121\n\t"
        "v_mfma_f32_16x16x32_bf16 v[92:95], v[152:155], v[108:111], v[92:95]\n\t"
        "ds_read_b64_tr_b16 v[106:107], v123 offset:3072\n\t"
        "s_waitcnt lgkmcnt(6)\n\t"
        "v_mfma_f32_16x16x32_bf16 v[32:35], v[112:115], v[148:151], v[32:35]\n\t"
        "ds_read_b64_tr_b16 v[108:109], v123 offset:32\n\t"
        "v_mfma_f32_16x16x32_bf16 v[40:43], v[112:115], v[152:155], v[40:43]\n\t"
        "ds_read_b64_tr_b16 v[110:111], v123 offset:3104\n\t"
        "v_add_u32 v130, s46, v122\n\t"
        "v_mfma_f32_16x16x32_bf16 v[36:39], v[116:119], v[148:151], v[36:39]\n\t"
        "ds_read_b64_tr_b16 v[112:113], v124 offset:0\n\t"
        "buffer_load_dwordx4 v[132:135], v131, s[40:43], 0 offen offset:0 sc1\n\t"
        "v_mfma_f32_16x16x32_bf16 v[44:47], v[116:119], v[152:155], v[44:47]\n\t"
        "ds_read_b64_tr_b16 v[114:115], v124 offset:3072\n\t"
        "s_waitcnt lgkmcnt(6)\n\t"
        "v_mfma_f32_16x16x32_bf16 v[48:51], v[96:99], v[148:151], v[48:51]\n\t"
        "ds_read_b64_tr_b16 v[116:117], v124 offset:32\n\t"
        "v_mfma_f32_16x16x32_bf16 v[56:59], v[96:99], v[152:155], v[56:59]\n\t"
        "ds_read_b64_tr_b16 v[118:119], v124 offset:3104\n\t"
        "v_mfma_f32_16x16x32_bf16 v[52:55], v[100:103], v[148:151], v[52:55]\n\t"
        "ds_read_b64_tr_b16 v[96:97], v129 offset:256\n\t"
        "s_addk_i32 s46, 12352\n\t"
        "v_mfma_f32_16x16x32_bf16 v[60:63], v[100:103], v[152:155], v[60:63]\n\t"
        "ds_read_b64_tr_b16 v[98:99], v129 offset:3328\n\t"
        "s_waitcnt lgkmcnt(6)\n\t"
        "v_mfma_f32_16x16x32_bf16 v[0:3], v[104:107], v[148:151], v[0:3]\n\t"
        "ds_read_b64_tr_b16 v[100:101], v129 offset:288\n\t"
        "s_cmp_eq_u32 s46, s47\n\t"
        "v_mfma_f32_16x16x32_bf16 v[8:11], v[104:107], v[152:155], v[8:11]\n\t"
        "ds_read_b64_tr_b16 v[102:103], v129 offset:3360\n\t"
        "s_cselect_b32 s46, 0, s46\n\t"
        "v_mfma_f32_16x16x32_bf16 v[4:7], v[108:111], v[148:151], v[4:7]\n\t"
        "ds_read_b64_tr_b16 v[104:105], v130 offset:256\n\t"
        "buffer_load_dwordx4 v[136:139], v131, s[40:43], 0 offen offset:256 sc1\n\t"
        "v_mfma_f32_16x16x32_bf16 v[12:15], v[108:111], v[152:155], v[12:15]\n\t"
        "ds_read_b64_tr_b16 v[106:107], v130 offset:3328\n\t"
        "s_waitcnt lgkmcnt(6)\n\t"
        "v_mfma_f32_16x16x32_bf16 v[16:19], v[112:115], v[148:151], v[16:19]\n\t"
        "ds_read_b64_tr_b16 v[108:109], v130 offset:288\n\t"
        "v_mfma_f32_16x16x32_bf16 v[24:27], v[112:115], v[152:155], v[24:27]\n\t"
        "ds_read_b64_tr_b16 v[110:111], v130 offset:3360\n\t"
        "v_mfma_f32_16x16x32_bf16 v[20:23], v[116:119], v[148:151], v[20:23]\n\t"
        "ds_read_b64_tr_b16 v[112:113], v129 offset:128\n\t"
        "v_mfma_f32_16x16x32_bf16 v[28:31], v[116:119], v[152:155], v[28:31]\n\t"
        "ds_read_b64_tr_b16 v[114:115], v129 offset:3200\n\t"
        "s_waitcnt vmcnt(10) lgkmcnt(6)\n\t"
        "v_mfma_f32_16x16x32_bf16 v[64:67], v[156:159], v[96:99], v[64:67]\n\t"
        "ds_read_b64_tr_b16 v[116:117], v129 offset:160\n\t"
        "v_mfma_f32_16x16x32_bf16 v[72:75], v[160:163], v[96:99], v[72:75]\n\t"
        "ds_read_b64_tr_b16 v[118:119], v129 offset:3232\n\t"
        "v_mfma_f32_16x16x32_bf16 v[68:71], v[156:159], v[100:103], v[68:71]\n\t"
        "ds_read_b64_tr_b16 v[96:97], v130 offset:128\n\t"
        "ds_read_b32 v126, v125\n\t"
        "v_mfma_f32_16x16x32_bf16 v[76:79], v[160:163], v[100:103], v[76:79]\n\t"
        "ds_read_b64_tr_b16 v[98:99], v130 offset:3200\n\t"
        "s_waitcnt lgkmcnt(7)\n\t"
        "v_mfma_f32_16x16x32_bf16 v[80:83], v[156:159], v[104:107], v[80:83]\n\t"
        "ds_read_b64_tr_b16 v[100:101], v130 offset:160\n\t"
        "v_mfma_f32_16x16x32_bf16 v[88:91], v[160:163], v[104:107], v[88:91]\n\t"
        "ds_read_b64_tr_b16 v[102:103], v130 offset:3232\n\t"
        "v_mfma_f32_16x16x32_bf16 v[84:87], v[156:159], v[108:111], v[84:87]\n\t"
        "ds_read_b64_tr_b16 v[104:105], v129 offset:0\n\t"
        "v_add_u32 v123, s46, v121\n\t"
        "v_mfma_f32_16x16x32_bf16 v[92:95], v[160:163], v[108:111], v[92:95]\n\t"
        "ds_read_b64_tr_b16 v[106:107], v129 offset:3072\n\t"
        "s_waitcnt lgkmcnt(5)\n\t"
        "v_mfma_f32_16x16x32_bf16 v[32:35], v[112:115], v[156:159], v[32:35]\n\t"
        "ds_read_b64_tr_b16 v[108:109], v129 offset:32\n\t"
        "v_cmp_gt_u32 vcc, v128, v126\n\t"
        "v_mfma_f32_16x16x32_bf16 v[40:43], v[112:115], v[160:163], v[40:43]\n\t"
        "ds_read_b64_tr_b16 v[110:111], v129 offset:3104\n\t"
        "v_add_u32 v124, s46, v122\n\t"
        "v_mfma_f32_16x16x32_bf16 v[36:39], v[116:119], v[156:159], v[36:39]\n\t"
        "ds_read_b64_tr_b16 v[112:113], v130 offset:0\n\t"
        "buffer_load_dwordx4 v[140:143], v131, s[40:43], 0 offen offset:2048 sc1\n\t"
        "v_mfma_f32_16x16x32_bf16 v[44:47], v[116:119], v[160:163], v[44:47]\n\t"
        "ds_read_b64_tr_b16 v[114:115], v130 offset:3072\n\t"
        "s_waitcnt lgkmcnt(6)\n\t"
        "v_mfma_f32_16x16x32_bf16 v[48:51], v[96:99], v[156:159], v[48:51]\n\t"
        "ds_read_b64_tr_b16 v[116:117], v130 offset:32\n\t"
        "v_mfma_f32_16x16x32_bf16 v[56:59], v[96:99], v[160:163], v[56:59]\n\t"
        "ds_read_b64_tr_b16 v[118:119], v130 offset:3104\n\t"
        "s_cbranch_vccnz 14f\n\t"
        "15:\n\t"
        "v_mfma_f32_16x16x32_bf16 v[52:55], v[100:103], v[156:159], v[52:55]\n\t"
        "ds_read_b64_tr_b16 v[96:97], v123 offset:256\n\t"
        "s_addk_i32 s46, 12352\n\t"
        "v_mfma_f32_16x16x32_bf16 v[60:63], v[100:103], v[160:163], v[60:63]\n\t"
        "ds_read_b64_tr_b16 v[98:99], v123 offset:3328\n\t"
        "s_waitcnt lgkmcnt(6)\n\t"
        "v_mfma_f32_16x16x32_bf16 v[0:3], v[104:107], v[156:159], v[0:3]\n\t"
        "ds_read_b64_tr_b16 v[100:101], v123 offset:288\n\t"
        "s_cmp_eq_u32 s46, s47\n\t"
        "v_mfma_f32_16x16x32_bf16 v[8:11], v[104:107], v[160:163], v[8:11]\n\t"
        "ds_read_b64_tr_b16 v[102:103], v123 offset:3360\n\t"
        "s_cselect_b32 s46, 0, s46\n\t"
        "v_mfma_f32_16x16x32_bf16 v[4:7], v[108:111], v[156:159], v[4:7]\n\t"
        "ds_read_b64_tr_b16 v[104:105], v124 offset:256\n\t"
        "buffer_load_dwordx4 v[144:147], v131, s[40:43], 0 offen offset:2304 sc1\n\t"
        "v_mfma_f32_16x16x32_bf16 v[12:15], v[108:111], v[160:163], v[12:15]\n\t"
        "ds_read_b64_tr_b16 v[106:107], v124 offset:3328\n\t"
        "s_waitcnt lgkmcnt(6)\n\t"
        "v_mfma_f32_16x16x32_bf16 v[16:19], v[112:115], v[156:159], v[16:19]\n\t"
        "ds_write_b32 v127, v128\n\t"
        "ds_read_b64_tr_b16 v[108:109], v124 offset:288\n\t"
        "v_mfma_f32_16x16x32_bf16 v[24:27], v[112:115], v[160:163], v[24:27]\n\t"
        "ds_read_b64_tr_b16 v[110:111], v124 offset:3360\n\t"
        "v_add_u32 v128, 2, v128\n\t"
        "v_mfma_f32_16x16x32_bf16 v[20:23], v[116:119], v[156:159], v[20:23]\n\t"
        "ds_read_b64_tr_b16 v[112:113], v123 offset:128\n\t"
        "v_mfma_f32_16x16x32_bf16 v[28:31], v[116:119], v[160:163], v[28:31]\n\t"
        "ds_read_b64_tr_b16 v[114:115], v123 offset:3200\n\t"
        "s_waitcnt vmcnt(10) lgkmcnt(7)\n\t"
        "v_mfma_f32_16x16x32_bf16 v[64:67], v[164:167], v[96:99], v[64:67]\n\t"
        "ds_read_b64_tr_b16 v[116:117], v123 offset:160\n\t"
        "s_addk_i32 s44, 0x1000\n\t"
        "v_mfma_f32_16x16x32_bf16 v[72:75], v[168:171], v[96:99], v[72:75]\n\t"
        "ds_read_b64_tr_b16 v[118:119], v123 offset:3232\n\t"
        "s_bitset0_b32 s44, 16\n\t"
        "v_mfma_f32_16x16x32_bf16 v[68:71], v[164:167], v[100:103], v[68:71]\n\t"
        "ds_read_b64_tr_b16 v[96:97], v124 offset:128\n\t"
        "s_cmp_eq_u32 s44, s49\n\t"
        "v_mfma_f32_16x16x32_bf16 v[76:79], v[168:171], v[100:103], v[76:79]\n\t"
        "ds_read_b64_tr_b16 v[98:99], v124 offset:3200\n\t"
        "s_waitcnt lgkmcnt(6)\n\t"
        "v_mfma_f32_16x16x32_bf16 v[80:83], v[164:167], v[104:107], v[80:83]\n\t"
        "ds_read_b64_tr_b16 v[100:101], v124 offset:160\n\t"
        "s_cselect_b32 s44, s50, s44\n\t"
        "v_mfma_f32_16x16x32_bf16 v[88:91], v[168:171], v[104:107], v[88:91]\n\t"
        "ds_read_b64_tr_b16 v[102:103], v124 offset:3232\n\t"
        "v_add_u32 v131, s44, v120\n\t"
        "v_mfma_f32_16x16x32_bf16 v[84:87], v[164:167], v[108:111], v[84:87]\n\t"
        "ds_read_b64_tr_b16 v[104:105], v123 offset:0\n\t"
        "v_add_u32 v129, s46, v121\n\t"
        "v_mfma_f32_16x16x32_bf16 v[92:95], v[168:171], v[108:111], v[92:95]\n\t"
        "ds_read_b64_tr_b16 v[106:107], v123 offset:3072\n\t"
        "s_waitcnt lgkmcnt(6)\n\t"
        "v_mfma_f32_16x16x32_bf16 v[32:35], v[112:115], v[164:167], v[32:35]\n\t"
        "ds_read_b64_tr_b16 v[108:109], v123 offset:32\n\t"
        "v_mfma_f32_16x16x32_bf16 v[40:43], v[112:115], v[168:171], v[40:43]\n\t"
        "ds_read_b64_tr_b16 v[110:111], v123 offset:3104\n\t"
        "v_add_u32 v130, s46, v122\n\t"
        "v_mfma_f32_16x16x32_bf16 v[36:39], v[116:119], v[164:167], v[36:39]\n\t"
        "ds_read_b64_tr_b16 v[112:113], v124 offset:0\n\t"
        "buffer_load_dwordx4 v[148:151], v131, s[40:43], 0 offen offset:0 sc1\n\t"
        "v_mfma_f32_16x16x32_bf16 v[44:47], v[116:119], v[168:171], v[44:47]\n\t"
        "ds_read_b64_tr_b16 v[114:115], v124 offset:3072\n\t"
        "s_waitcnt lgkmcnt(6)\n\t"
        "v_mfma_f32_16x16x32_bf16 v[48:51], v[96:99], v[164:167], v[48:51]\n\t"
        "ds_read_b64_tr_b16 v[116:117], v124 offset:32\n\t"
        "v_mfma_f32_16x16x32_bf16 v[56:59], v[96:99], v[168:171], v[56:59]\n\t"
        "ds_read_b64_tr_b16 v[118:119], v124 offset:3104\n\t"
        "v_mfma_f32_16x16x32_bf16 v[52:55], v[100:103], v[164:167], v[52:55]\n\t"
        "ds_read_b64_tr_b16 v[96:97], v129 offset:256\n\t"
        "s_addk_i32 s46, 12352\n\t"
        "v_mfma_f32_16x16x32_bf16 v[60:63], v[100:103], v[168:171], v[60:63]\n\t"
        "ds_read_b64_tr_b16 v[98:99], v129 offset:3328\n\t"
        "s_waitcnt lgkmcnt(6)\n\t"
        "v_mfma_f32_16x16x32_bf16 v[0:3], v[104:107], v[164:167], v[0:3]\n\t"
        "ds_read_b64_tr_b16 v[100:101], v129 offset:288\n\t"
        "s_cmp_eq_u32 s46, s47\n\t"
        "v_mfma_f32_16x16x32_bf16 v[8:11], v[104:107], v[168:171], v[8:11]\n\t"
        "ds_read_b64_tr_b16 v[102:103], v129 offset:3360\n\t"
        "s_cselect_b32 s46, 0, s46\n\t"
        "v_mfma_f32_16x16x32_bf16 v[4:7], v[108:111], v[164:167], v[4:7]\n\t"
        "ds_read_b64_tr_b16 v[104:105], v130 offset:256\n\t"
        "buffer_load_dwordx4 v[152:155], v131, s[40:43], 0 offen offset:256 sc1\n\t"
        "v_mfma_f32_16x16x32_bf16 v[12:15], v[108:111], v[168:171], v[12:15]\n\t"
        "ds_read_b64_tr_b16 v[106:107], v130 offset:3328\n\t"
        "s_waitcnt lgkmcnt(6)\n\t"
        "v_mfma_f32_16x16x32_bf16 v[16:19], v[112:115], v[164:167], v[16:19]\n\t"
        "ds_read_b64_tr_b16 v[108:109], v130 offset:288\n\t"
        "v_mfma_f32_16x16x32_bf16 v[24:27], v[112:115], v[168:171], v[24:27]\n\t"
        "ds_read_b64_tr_b16 v[110:111], v130 offset:3360\n\t"
        "v_mfma_f32_16x16x32_bf16 v[20:23], v[116:119], v[164:167], v[20:23]\n\t"
        "ds_read_b64_tr_b16 v[112:113], v129 offset:128\n\t"
        "v_mfma_f32_16x16x32_bf16 v[28:31], v[116:119], v[168:171], v[28:31]\n\t"
        "ds_read_b64_tr_b16 v[114:115], v129 offset:3200\n\t"
        "s_waitcnt vmcnt(10) lgkmcnt(6)\n\t"
        "v_mfma_f32_16x16x32_bf16 v[64:67], v[172:175], v[96:99], v[64:67]\n\t"
        "ds_read_b64_tr_b16 v[116:117], v129 offset:160\n\t"
        "v_mfma_f32_16x16x32_bf16 v[72:75], v[176:179], v[96:99], v[72:75]\n\t"
        "ds_read_b64_tr_b16 v[118:119], v129 offset:3232\n\t"
        "v_mfma_f32_16x16x32_bf16 v[68:71], v[172:175], v[100:103], v[68:71]\n\t"
        "ds_read_b64_tr_b16 v[96:97], v130 offset:128\n\t"
        "ds_read_b32 v126, v125\n\t"
        "v_mfma_f32_16x16x32_bf16 v[76:79], v[176:179], v[100:103], v[76:79]\n\t"
        "ds_read_b64_tr_b16 v[98:99], v130 offset:3200\n\t"
        "s_waitcnt lgkmcnt(7)\n\t"
        "v_mfma_f32_16x16x32_bf16 v[80:83], v[172:175], v[104:107], v[80:83]\n\t"
        "ds_read_b64_tr_b16 v[100:101], v130 offset:160\n\t"
        "v_mfma_f32_16x16x32_bf16 v[88:91], v[176:179], v[104:107], v[88:91]\n\t"
        "ds_read_b64_tr_b16 v[102:103], v130 offset:3232\n\t"
        "v_mfma_f32_16x16x32_bf16 v[84:87], v[172:175], v[108:111], v[84:87]\n\t"
        "ds_read_b64_tr_b16 v[104:105], v129 offset:0\n\t"
        "v_add_u32 v123, s46, v121\n\t"
        "v_mfma_f32_16x16x32_bf16 v[92:95], v[176:179], v[108:111], v[92:95]\n\t"
        "ds_read_b64_tr_b16 v[106:107], v129 offset:3072\n\t"
        "s_waitcnt lgkmcnt(5)\n\t"
        "v_mfma_f32_16x16x32_bf16 v[32:35], v[112:115], v[172:175], v[32:35]\n\t"
        "ds_read_b64_tr_b16 v[108:109], v129 offset:32\n\t"
        "v_cmp_gt_u32 vcc, v128, v126\n\t"
        "v_mfma_f32_16x16x32_bf16 v[40:43], v[112:115], v[176:179], v[40:43]\n\t"
        "ds_read_b64_tr_b16 v[110:111], v129 offset:3104\n\t"
        "v_add_u32 v124, s46, v122\n\t"
        "v_mfma_f32_16x16x32_bf16 v[36:39], v[116:119], v[172:175], v[36:39]\n\t"
        "ds_read_b64_tr_b16 v[112:113], v130 offset:0\n\t"
        "buffer_load_dwordx4 v[156:159], v131, s[40:43], 0 offen offset:2048 sc1\n\t"
        "v_mfma_f32_16x16x32_bf16 v[44:47], v[116:119], v[176:179], v[44:47]\n\t"
        "ds_read_b64_tr_b16 v[114:115], v130 offset:3072\n\t"
        "s_waitcnt lgkmcnt(6)\n\t"
        "v_mfma_f32_16x16x32_bf16 v[48:51], v[96:99], v[172:175], v[48:51]\n\t"
        "ds_read_b64_tr_b16 v[116:117], v130 offset:32\n\t"
        "v_mfma_f32_16x16x32_bf16 v[56:59], v[96:99], v[176:179], v[56:59]\n\t"
        "ds_read_b64_tr_b16 v[118:119], v130 offset:3104\n\t"
        "s_cbranch_vccnz 16f\n\t"
        "17:\n\t"
        "v_mfma_f32_16x16x32_bf16 v[52:55], v[100:103], v[172:175], v[52:55]\n\t"
        "ds_read_b64_tr_b16 v[96:97], v123 offset:256\n\t"
        "s_addk_i32 s46, 12352\n\t"
        "v_mfma_f32_16x16x32_bf16 v[60:63], v[100:103], v[176:179], v[60:63]\n\t"
        "ds_read_b64_tr_b16 v[98:99], v123 offset:3328\n\t"
        "s_waitcnt lgkmcnt(6)\n\t"
        "v_mfma_f32_16x16x32_bf16 v[0:3], v[104:107], v[172:175], v[0:3]\n\t"
        "ds_read_b64_tr_b16 v[100:101], v123 offset:288\n\t"
        "s_cmp_eq_u32 s46, s47\n\t"
        "v_mfma_f32_16x16x32_bf16 v[8:11], v[104:107], v[176:179], v[8:11]\n\t"
        "ds_read_b64_tr_b16 v[102:103], v123 offset:3360\n\t"
        "s_cselect_b32 s46, 0, s46\n\t"
        "v_mfma_f32_16x16x32_bf16 v[4:7], v[108:111], v[172:175], v[4:7]\n\t"
        "ds_read_b64_tr_b16 v[104:105], v124 offset:256\n\t"
        "buffer_load_dwordx4 v[160:163], v131, s[40:43], 0 offen offset:2304 sc1\n\t"
        "v_mfma_f32_16x16x32_bf16 v[12:15], v[108:111], v[176:179], v[12:15]\n\t"
        "ds_read_b64_tr_b16 v[106:107], v124 offset:3328\n\t"
        "s_waitcnt lgkmcnt(6)\n\t"
        "v_mfma_f32_16x16x32_bf16 v[16:19], v[112:115], v[172:175], v[16:19]\n\t"
        "ds_write_b32 v127, v128\n\t"
        "ds_read_b64_tr_b16 v[108:109], v124 offset:288\n\t"
        "v_mfma_f32_16x16x32_bf16 v[24:27], v[112:115], v[176:179], v[24:27]\n\t"
        "ds_read_b64_tr_b16 v[110:111], v124 offset:3360\n\t"
        "v_add_u32 v128, 2, v128\n\t"
        "v_mfma_f32_16x16x32_bf16 v[20:23], v[116:119], v[172:175], v[20:23]\n\t"
        "ds_read_b64_tr_b16 v[112:113], v123 offset:128\n\t"
        "v_mfma_f32_16x16x32_bf16 v[28:31], v[116:119], v[176:179], v[28:31]\n\t"
        "ds_read_b64_tr_b16 v[114:115], v123 offset:3200\n\t"
        "s_waitcnt vmcnt(10) lgkmcnt(7)\n\t"
        "v_mfma_f32_16x16x32_bf16 v[64:67], v[180:183], v[96:99], v[64:67]\n\t"
        "ds_read_b64_tr_b16 v[116:117], v123 offset:160\n\t"
        "s_addk_i32 s44, 0x1000\n\t"
        "v_mfma_f32_16x16x32_bf16 v[72:75], v[184:187], v[96:99], v[72:75]\n\t"
        "ds_read_b64_tr_b16 v[118:119], v123 offset:3232\n\t"
        "s_bitset0_b32 s44, 16\n\t"
        "v_mfma_f32_16x16x32_bf16 v[68:71], v[180:183], v[100:103], v[68:71]\n\t"
        "ds_read_b64_tr_b16 v[96:97], v124 offset:128\n\t"
        "s_cmp_eq_u32 s44, s49\n\t"
        "v_mfma_f32_16x16x32_bf16 v[76:79], v[184:187], v[100:103], v[76:79]\n\t"
        "ds_read_b64_tr_b16 v[98:99], v124 offset:3200\n\t"
        "s_waitcnt lgkmcnt(6)\n\t"
        "v_mfma_f32_16x16x32_bf16 v[80:83], v[180:183], v[104:107], v[80:83]\n\t"
        "ds_read_b64_tr_b16 v[100:101], v124 offset:160\n\t"
        "s_cselect_b32 s44, s50, s44\n\t"
        "v_mfma_f32_16x16x32_bf16 v[88:91], v[184:187], v[104:107], v[88:91]\n\t"
        "ds_read_b64_tr_b16 v[102:103], v124 offset:3232\n\t"
        "v_add_u32 v131, s44, v120\n\t"
        "v_mfma_f32_16x16x32_bf16 v[84:87], v[180:183], v[108:111], v[84:87]\n\t"
        "ds_read_b64_tr_b16 v[104:105], v123 offset:0\n\t"
        "v_add_u32 v129, s46, v121\n\t"
        "v_mfma_f32_16x16x32_bf16 v[92:95], v[184:187], v[108:111], v[92:95]\n\t"
        "ds_read_b64_tr_b16 v[106:107], v123 offset:3072\n\t"
        "s_waitcnt lgkmcnt(6)\n\t"
        "v_mfma_f32_16x16x32_bf16 v[32:35], v[112:115], v[180:183], v[32:35]\n\t"
        "ds_read_b64_tr_b16 v[108:109], v123 offset:32\n\t"
        "v_mfma_f32_16x16x32_bf16 v[40:43], v[112:115], v[184:187], v[40:43]\n\t"
        "ds_read_b64_tr_b16 v[110:111], v123 offset:3104\n\t"
        "v_add_u32 v130, s46, v122\n\t"
        "v_mfma_f32_16x16x32_bf16 v[36:39], v[116:119], v[180:183], v[36:39]\n\t"
        "ds_read_b64_tr_b16 v[112:113], v124 offset:0\n\t"
        "buffer_load_dwordx4 v[164:167], v131, s[40:43], 0 offen offset:0 sc1\n\t"
        "v_mfma_f32_16x16x32_bf16 v[44:47], v[116:119], v[184:187], v[44:47]\n\t"
        "ds_read_b64_tr_b16 v[114:115], v124 offset:3072\n\t"
        "s_waitcnt lgkmcnt(6)\n\t"
        "v_mfma_f32_16x16x32_bf16 v[48:51], v[96:99], v[180:183], v[48:51]\n\t"
        "ds_read_b64_tr_b16 v[116:117], v124 offset:32\n\t"
        "v_mfma_f32_16x16x32_bf16 v[56:59], v[96:99], v[184:187], v[56:59]\n\t"
        "ds_read_b64_tr_b16 v[118:119], v124 offset:3104\n\t"
        "v_mfma_f32_16x16x32_bf16 v[52:55], v[100:103], v[180:183], v[52:55]\n\t"
        "ds_read_b64_tr_b16 v[96:97], v129 offset:256\n\t"
        "s_addk_i32 s46, 12352\n\t"
        "v_mfma_f32_16x16x32_bf16 v[60:63], v[100:103], v[184:187], v[60:63]\n\t"
        "ds_read_b64_tr_b16 v[98:99], v129 offset:3328\n\t"
        "s_waitcnt lgkmcnt(6)\n\t"
        "v_mfma_f32_16x16x32_bf16 v[0:3], v[104:107], v[180:183], v[0:3]\n\t"
        "ds_read_b64_tr_b16 v[100:101], v129 offset:288\n\t"
        "s_cmp_eq_u32 s46, s47\n\t"
        "v_mfma_f32_16x16x32_bf16 v[8:11], v[104:107], v[184:187], v[8:11]\n\t"
        "ds_read_b64_tr_b16 v[102:103], v129 offset:3360\n\t"
        "s_cselect_b32 s46, 0, s46\n\t"
        "v_mfma_f32_16x16x32_bf16 v[4:7], v[108:111], v[180:183], v[4:7]\n\t"
        "ds_read_b64_tr_b16 v[104:105], v130 offset:256\n\t"
        "buffer_load_dwordx4 v[168:171], v131, s[40:43], 0 offen offset:256 sc1\n\t"
        "v_mfma_f32_16x16x32_bf16 v[12:15], v[108:111], v[184:187], v[12:15]\n\t"
        "ds_read_b64_tr_b16 v[106:107], v130 offset:3328\n\t"
        "s_waitcnt lgkmcnt(6)\n\t"
        "v_mfma_f32_16x16x32_bf16 v[16:19], v[112:115], v[180:183], v[16:19]\n\t"
        "ds_read_b64_tr_b16 v[108:109], v130 offset:288\n\t"
        "v_mfma_f32_16x16x32_bf16 v[24:27], v[112:115], v[184:187], v[24:27]\n\t"
        "ds_read_b64_tr_b16 v[110:111], v130 offset:3360\n\t"
        "v_mfma_f32_16x16x32_bf16 v[20:23], v[116:119], v[180:183], v[20:23]\n\t"
        "ds_read_b64_tr_b16 v[112:113], v129 offset:128\n\t"
        "v_mfma_f32_16x16x32_bf16 v[28:31], v[116:119], v[184:187], v[28:31]\n\t"
        "ds_read_b64_tr_b16 v[114:115], v129 offset:3200\n\t"
        "s_waitcnt vmcnt(10) lgkmcnt(6)\n\t"
        "v_mfma_f32_16x16x32_bf16 v[64:67], v[188:191], v[96:99], v[64:67]\n\t"
        "ds_read_b64_tr_b16 v[116:117], v129 offset:160\n\t"
        "v_mfma_f32_16x16x32_bf16 v[72:75], v[192:195], v[96:99], v[72:75]\n\t"
        "ds_read_b64_tr_b16 v[118:119], v129 offset:3232\n\t"
        "v_mfma_f32_16x16x32_bf16 v[68:71], v[188:191], v[100:103], v[68:71]\n\t"
        "ds_read_b64_tr_b16 v[96:97], v130 offset:128\n\t"
        "ds_read_b32 v126, v125\n\t"
        "v_mfma_f32_16x16x32_bf16 v[76:79], v[192:195], v[100:103], v[76:79]\n\t"
        "ds_read_b64_tr_b16 v[98:99], v130 offset:3200\n\t"
        "s_waitcnt lgkmcnt(7)\n\t"
        "v_mfma_f32_16x16x32_bf16 v[80:83], v[188:191], v[104:107], v[80:83]\n\t"
        "ds_read_b64_tr_b16 v[100:101], v130 offset:160\n\t"
        "v_mfma_f32_16x16x32_bf16 v[88:91], v[192:195], v[104:107], v[88:91]\n\t"
        "ds_read_b64_tr_b16 v[102:103], v130 offset:3232\n\t"
        "v_mfma_f32_16x16x32_bf16 v[84:87], v[188:191], v[108:111], v[84:87]\n\t"
        "ds_read_b64_tr_b16 v[104:105], v129 offset:0\n\t"
        "v_add_u32 v123, s46, v121\n\t"
        "v_mfma_f32_16x16x32_bf16 v[92:95], v[192:195], v[108:111], v[92:95]\n\t"
        "ds_read_b64_tr_b16 v[106:107], v129 offset:3072\n\t"
        "s_waitcnt lgkmcnt(5)\n\t"
        "v_mfma_f32_16x16x32_bf16 v[32:35], v[112:115], v[188:191], v[32:35]\n\t"
        "ds_read_b64_tr_b16 v[108:109], v129 offset:32\n\t"
        "v_cmp_gt_u32 vcc, v128, v126\n\t"
        "v_mfma_f32_16x16x32_bf16 v[40:43], v[112:115], v[192:195], v[40:43]\n\t"
        "ds_read_b64_tr_b16 v[110:111], v129 offset:3104\n\t"
        "v_add_u32 v124, s46, v122\n\t"
        "v_mfma_f32_16x16x32_bf16 v[36:39], v[116:119], v[188:191], v[36:39]\n\t"
        "ds_read_b64_tr_b16 v[112:113], v130 offset:0\n\t"
        "buffer_load_dwordx4 v[172:175], v131, s[40:43], 0 offen offset:2048 sc1\n\t"
        "v_mfma_f32_16x16x32_bf16 v[44:47], v[116:119], v[192:195], v[44:47]\n\t"
        "ds_read_b64_tr_b16 v[114:115], v130 offset:3072\n\t"
        "s_waitcnt lgkmcnt(6)\n\t"
        "v_mfma_f32_16x16x32_bf16 v[48:51], v[96:99], v[188:191], v[48:51]\n\t"
        "ds_read_b64_tr_b16 v[116:117], v130 offset:32\n\t"
        "v_mfma_f32_16x16x32_bf16 v[56:59], v[96:99], v[192:195], v[56:59]\n\t"
        "ds_read_b64_tr_b16 v[118:119], v130 offset:3104\n\t"
        "s_cbranch_vccnz 18f\n\t"
        "19:\n\t"
        "v_mfma_f32_16x16x32_bf16 v[52:55], v[100:103], v[188:191], v[52:55]\n\t"
        "ds_read_b64_tr_b16 v[96:97], v123 offset:256\n\t"
        "s_addk_i32 s46, 12352\n\t"
        "v_mfma_f32_16x16x32_bf16 v[60:63], v[100:103], v[192:195], v[60:63]\n\t"
        "ds_read_b64_tr_b16 v[98:99], v123 offset:3328\n\t"
        "s_waitcnt lgkmcnt(6)\n\t"
        "v_mfma_f32_16x16x32_bf16 v[0:3], v[104:107], v[188:191], v[0:3]\n\t"
        "ds_read_b64_tr_b16 v[100:101], v123 offset:288\n\t"
        "s_cmp_eq_u32 s46, s47\n\t"
        "v_mfma_f32_16x16x32_bf16 v[8:11], v[104:107], v[192:195], v[8:11]\n\t"
        "ds_read_b64_tr_b16 v[102:103], v123 offset:3360\n\t"
        "s_cselect_b32 s46, 0, s46\n\t"
        "v_mfma_f32_16x16x32_bf16 v[4:7], v[108:111], v[188:191], v[4:7]\n\t"
        "ds_read_b64_tr_b16 v[104:105], v124 offset:256\n\t"
        "buffer_load_dwordx4 v[176:179], v131, s[40:43], 0 offen offset:2304 sc1\n\t"
        "v_mfma_f32_16x16x32_bf16 v[12:15], v[108:111], v[192:195], v[12:15]\n\t"
        "ds_read_b64_tr_b16 v[106:107], v124 offset:3328\n\t"
        "s_waitcnt lgkmcnt(6)\n\t"
        "v_mfma_f32_16x16x32_bf16 v[16:19], v[112:115], v[188:191], v[16:19]\n\t"
        "ds_write_b32 v127, v128\n\t"
        "ds_read_b64_tr_b16 v[108:109], v124 offset:288\n\t"
        "v_mfma_f32_16x16x32_bf16 v[24:27], v[112:115], v[192:195], v[24:27]\n\t"
        "ds_read_b64_tr_b16 v[110:111], v124 offset:3360\n\t"
        "v_add_u32 v128, 2, v128\n\t"
        "v_mfma_f32_16x16x32_bf16 v[20:23], v[116:119], v[188:191], v[20:23]\n\t"
        "ds_read_b64_tr_b16 v[112:113], v123 offset:128\n\t"
        "v_mfma_f32_16x16x32_bf16 v[28:31], v[116:119], v[192:195], v[28:31]\n\t"
        "ds_read_b64_tr_b16 v[114:115], v123 offset:3200\n\t"
        "s_waitcnt vmcnt(10) lgkmcnt(7)\n\t"
        "v_mfma_f32_16x16x32_bf16 v[64:67], v[132:135], v[96:99], v[64:67]\n\t"
        "ds_read_b64_tr_b16 v[116:117], v123 offset:160\n\t"
        "s_addk_i32 s44, 0x1000\n\t"
        "v_mfma_f32_16x16x32_bf16 v[72:75], v[136:139], v[96:99], v[72:75]\n\t"
        "ds_read_b64_tr_b16 v[118:119], v123 offset:3232\n\t"
        "s_bitset0_b32 s44, 16\n\t"
        "v_mfma_f32_16x16x32_bf16 v[68:71], v[132:135], v[100:103], v[68:71]\n\t"
        "ds_read_b64_tr_b16 v[96:97], v124 offset:128\n\t"
        "s_cmp_eq_u32 s44, s49\n\t"
        "v_mfma_f32_16x16x32_bf16 v[76:79], v[136:139], v[100:103], v[76:79]\n\t"
        "ds_read_b64_tr_b16 v[98:99], v124 offset:3200\n\t"
        "s_waitcnt lgkmcnt(6)\n\t"
        "v_mfma_f32_16x16x32_bf16 v[80:83], v[132:135], v[104:107], v[80:83]\n\t"
        "ds_read_b64_tr_b16 v[100:101], v124 offset:160\n\t"
        "s_cselect_b32 s44, s50, s44\n\t"
        "v_mfma_f32_16x16x32_bf16 v[88:91], v[136:139], v[104:107], v[88:91]\n\t"
        "ds_read_b64_tr_b16 v[102:103], v124 offset:3232\n\t"
        "v_add_u32 v131, s44, v120\n\t"
        "v_mfma_f32_16x16x32_bf16 v[84:87], v[132:135], v[108:111], v[84:87]\n\t"
        "ds_read_b64_tr_b16 v[104:105], v123 offset:0\n\t"
        "v_add_u32 v129, s46, v121\n\t"
        "v_mfma_f32_16x16x32_bf16 v[92:95], v[136:139], v[108:111], v[92:95]\n\t"
        "ds_read_b64_tr_b16 v[106:107], v123 offset:3072\n\t"
        "s_waitcnt lgkmcnt(6)\n\t"
        "v_mfma_f32_16x16x32_bf16 v[32:35], v[112:115], v[132:135], v[32:35]\n\t"
        "ds_read_b64_tr_b16 v[108:109], v123 offset:32\n\t"
        "v_mfma_f32_16x16x32_bf16 v[40:43], v[112:115], v[136:139], v[40:43]\n\t"
        "ds_read_b64_tr_b16 v[110:111], v123 offset:3104\n\t"
        "v_add_u32 v130, s46, v122\n\t"
        "v_mfma_f32_16x16x32_bf16 v[36:39], v[116:119], v[132:135], v[36:39]\n\t"
        "ds_read_b64_tr_b16 v[112:113], v124 offset:0\n\t"
        "buffer_load_dwordx4 v[180:183], v131, s[40:43], 0 offen offset:0 sc1\n\t"
        "v_mfma_f32_16x16x32_bf16 v[44:47], v[116:119], v[136:139], v[44:47]\n\t"
        "ds_read_b64_tr_b16 v[114:115], v124 offset:3072\n\t"
        "s_waitcnt lgkmcnt(6)\n\t"
        "v_mfma_f32_16x16x32_bf16 v[48:51], v[96:99], v[132:135], v[48:51]\n\t"
        "ds_read_b64_tr_b16 v[116:117], v124 offset:32\n\t"
        "v_mfma_f32_16x16x32_bf16 v[56:59], v[96:99], v[136:139], v[56:59]\n\t"
        "ds_read_b64_tr_b16 v[118:119], v124 offset:3104\n\t"
        "v_mfma_f32_16x16x32_bf16 v[52:55], v[100:103], v[132:135], v[52:55]\n\t"
        "ds_read_b64_tr_b16 v[96:97], v129 offset:256\n\t"
        "s_addk_i32 s46, 12352\n\t"
        "v_mfma_f32_16x16x32_bf16 v[60:63], v[100:103], v[136:139], v[60:63]\n\t"
        "ds_read_b64_tr_b16 v[98:99], v129 offset:3328\n\t"
        "s_waitcnt lgkmcnt(6)\n\t"
        "v_mfma_f32_16x16x32_bf16 v[0:3], v[104:107], v[132:135], v[0:3]\n\t"
        "ds_read_b64_tr_b16 v[100:101], v129 offset:288\n\t"
        "s_cmp_eq_u32 s46, s47\n\t"
        "v_mfma_f32_16x16x32_bf16 v[8:11], v[104:107], v[136:139], v[8:11]\n\t"
        "ds_read_b64_tr_b16 v[102:103], v129 offset:3360\n\t"
        "s_cselect_b32 s46, 0, s46\n\t"
        "v_mfma_f32_16x16x32_bf16 v[4:7], v[108:111], v[132:135], v[4:7]\n\t"
        "ds_read_b64_tr_b16 v[104:105], v130 offset:256\n\t"
        "buffer_load_dwordx4 v[184:187], v131, s[40:43], 0 offen offset:256 sc1\n\t"
        "v_mfma_f32_16x16x32_bf16 v[12:15], v[108:111], v[136:139], v[12:15]\n\t"
        "ds_read_b64_tr_b16 v[106:107], v130 offset:3328\n\t"
        "s_waitcnt lgkmcnt(6)\n\t"
        "v_mfma_f32_16x16x32_bf16 v[16:19], v[112:115], v[132:135], v[16:19]\n\t"
        "ds_read_b64_tr_b16 v[108:109], v130 offset:288\n\t"
        "v_mfma_f32_16x16x32_bf16 v[24:27], v[112:115], v[136:139], v[24:27]\n\t"
        "ds_read_b64_tr_b16 v[110:111], v130 offset:3360\n\t"
        "v_mfma_f32_16x16x32_bf16 v[20:23], v[116:119], v[132:135], v[20:23]\n\t"
        "ds_read_b64_tr_b16 v[112:113], v129 offset:128\n\t"
        "v_mfma_f32_16x16x32_bf16 v[28:31], v[116:119], v[136:139], v[28:31]\n\t"
        "ds_read_b64_tr_b16 v[114:115], v129 offset:3200\n\t"
        "s_waitcnt vmcnt(10) lgkmcnt(6)\n\t"
        "v_mfma_f32_16x16x32_bf16 v[64:67], v[140:143], v[96:99], v[64:67]\n\t"
        "ds_read_b64_tr_b16 v[116:117], v129 offset:160\n\t"
        "v_mfma_f32_16x16x32_bf16 v[72:75], v[144:147], v[96:99], v[72:75]\n\t"
        "ds_read_b64_tr_b16 v[118:119], v129 offset:3232\n\t"
        "v_mfma_f32_16x16x32_bf16 v[68:71], v[140:143], v[100:103], v[68:71]\n\t"
        "ds_read_b64_tr_b16 v[96:97], v130 offset:128\n\t"
        "ds_read_b32 v126, v125\n\t"
        "v_mfma_f32_16x16x32_bf16 v[76:79], v[144:147], v[100:103], v[76:79]\n\t"
        "ds_read_b64_tr_b16 v[98:99], v130 offset:3200\n\t"
        "s_waitcnt lgkmcnt(7)\n\t"
        "v_mfma_f32_16x16x32_bf16 v[80:83], v[140:143], v[104:107], v[80:83]\n\t"
        "ds_read_b64_tr_b16 v[100:101], v130 offset:160\n\t"
        "v_mfma_f32_16x16x32_bf16 v[88:91], v[144:147], v[104:107], v[88:91]\n\t"
        "ds_read_b64_tr_b16 v[102:103], v130 offset:3232\n\t"
        "v_mfma_f32_16x16x32_bf16 v[84:87], v[140:143], v[108:111], v[84:87]\n\t"
        "ds_read_b64_tr_b16 v[104:105], v129 offset:0\n\t"
        "v_add_u32 v123, s46, v121\n\t"
        "v_mfma_f32_16x16x32_bf16 v[92:95], v[144:147], v[108:111], v[92:95]\n\t"
        "ds_read_b64_tr_b16 v[106:107], v129 offset:3072\n\t"
        "s_waitcnt lgkmcnt(5)\n\t"
        "v_mfma_f32_16x16x32_bf16 v[32:35], v[112:115], v[140:143], v[32:35]\n\t"
        "ds_read_b64_tr_b16 v[108:109], v129 offset:32\n\t"
        "v_cmp_gt_u32 vcc, v128, v126\n\t"
        "v_mfma_f32_16x16x32_bf16 v[40:43], v[112:115], v[144:147], v[40:43]\n\t"
        "ds_read_b64_tr_b16 v[110:111], v129 offset:3104\n\t"
        "v_add_u32 v124, s46, v122\n\t"
        "v_mfma_f32_16x16x32_bf16 v[36:39], v[116:119], v[140:143], v[36:39]\n\t"
        "ds_read_b64_tr_b16 v[112:113], v130 offset:0\n\t"
        "buffer_load_dwordx4 v[188:191], v131, s[40:43], 0 offen offset:2048 sc1\n\t"
        "v_mfma_f32_16x16x32_bf16 v[44:47], v[116:119], v[144:147], v[44:47]\n\t"
        "ds_read_b64_tr_b16 v[114:115], v130 offset:3072\n\t"
        "s_waitcnt lgkmcnt(6)\n\t"
        "v_mfma_f32_16x16x32_bf16 v[48:51], v[96:99], v[140:143], v[48:51]\n\t"
        "ds_read_b64_tr_b16 v[116:117], v130 offset:32\n\t"
        "v_mfma_f32_16x16x32_bf16 v[56:59], v[96:99], v[144:147], v[56:59]\n\t"
        "ds_read_b64_tr_b16 v[118:119], v130 offset:3104\n\t"
        "s_cbranch_vccnz 20f\n\t"
        "21:\n\t"
        "v_mfma_f32_16x16x32_bf16 v[52:55], v[100:103], v[140:143], v[52:55]\n\t"
        "ds_read_b64_tr_b16 v[96:97], v123 offset:256\n\t"
        "s_addk_i32 s46, 12352\n\t"
        "v_mfma_f32_16x16x32_bf16 v[60:63], v[100:103], v[144:147], v[60:63]\n\t"
        "ds_read_b64_tr_b16 v[98:99], v123 offset:3328\n\t"
        "s_waitcnt lgkmcnt(6)\n\t"
        "v_mfma_f32_16x16x32_bf16 v[0:3], v[104:107], v[140:143], v[0:3]\n\t"
        "ds_read_b64_tr_b16 v[100:101], v123 offset:288\n\t"
        "s_cmp_eq_u32 s46, s47\n\t"
        "v_mfma_f32_16x16x32_bf16 v[8:11], v[104:107], v[144:147], v[8:11]\n\t"
        "ds_read_b64_tr_b16 v[102:103], v123 offset:3360\n\t"
        "s_cselect_b32 s46, 0, s46\n\t"
        "v_mfma_f32_16x16x32_bf16 v[4:7], v[108:111], v[140:143], v[4:7]\n\t"
        "ds_read_b64_tr_b16 v[104:105], v124 offset:256\n\t"
        "buffer_load_dwordx4 v[192:195], v131, s[40:43], 0 offen offset:2304 sc1\n\t"
        "v_mfma_f32_16x16x32_bf16 v[12:15], v[108:111], v[144:147], v[12:15]\n\t"
        "ds_read_b64_tr_b16 v[106:107], v124 offset:3328\n\t"
        "s_waitcnt lgkmcnt(6)\n\t"
        "v_mfma_f32_16x16x32_bf16 v[16:19], v[112:115], v[140:143], v[16:19]\n\t"
        "ds_write_b32 v127, v128\n\t"
        "ds_read_b64_tr_b16 v[108:109], v124 offset:288\n\t"
        "v_mfma_f32_16x16x32_bf16 v[24:27], v[112:115], v[144:147], v[24:27]\n\t"
        "ds_read_b64_tr_b16 v[110:111], v124 offset:3360\n\t"
        "v_add_u32 v128, 2, v128\n\t"
        "v_mfma_f32_16x16x32_bf16 v[20:23], v[116:119], v[140:143], v[20:23]\n\t"
        "ds_read_b64_tr_b16 v[112:113], v123 offset:128\n\t"
        "s_sub_u32 s45, s45, 1\n\t"
        "v_mfma_f32_16x16x32_bf16 v[28:31], v[116:119], v[144:147], v[28:31]\n\t"
        "ds_read_b64_tr_b16 v[114:115], v123 offset:3200\n\t"
        "s_cbranch_scc0 1b\n\t"
        "s_waitcnt vmcnt(10) lgkmcnt(7)\n\t"
        "v_mfma_f32_16x16x32_bf16 v[64:67], v[148:151], v[96:99], v[64:67]\n\t"
        "ds_read_b64_tr_b16 v[116:117], v121 offset:256\n\t"
        "ds_read_b32 v126, v125\n\t"
        "v_mfma_f32_16x16x32_bf16 v[72:75], v[152:155], v[96:99], v[72:75]\n\t"
        "ds_read_b64_tr_b16 v[118:119], v121 offset:3328\n\t"
        "v_add_u32 v128, 2, v128\n\t"
        "v_mfma_f32_16x16x32_bf16 v[68:71], v[148:151], v[100:103], v[68:71]\n\t"
        "ds_read_b64_tr_b16 v[96:97], v121 offset:288\n\t"
        "v_mfma_f32_16x16x32_bf16 v[76:79], v[152:155], v[100:103], v[76:79]\n\t"
        "ds_read_b64_tr_b16 v[98:99], v121 offset:3360\n\t"
        "s_waitcnt lgkmcnt(3)\n\t"
        "v_mfma_f32_16x16x32_bf16 v[80:83], v[148:151], v[104:107], v[80:83]\n\t"
        "ds_read_b64_tr_b16 v[100:101], v122 offset:256\n\t"
        "v_cmp_gt_u32 vcc, v128, v126\n\t"
        "v_mfma_f32_16x16x32_bf16 v[88:91], v[152:155], v[104:107], v[88:91]\n\t"
        "ds_read_b64_tr_b16 v[102:103], v122 offset:3328\n\t"
        "v_mfma_f32_16x16x32_bf16 v[84:87], v[148:151], v[108:111], v[84:87]\n\t"
        "ds_read_b64_tr_b16 v[104:105], v122 offset:288\n\t"
        "v_mfma_f32_16x16x32_bf16 v[92:95], v[152:155], v[108:111], v[92:95]\n\t"
        "ds_read_b64_tr_b16 v[106:107], v122 offset:3360\n\t"
        "s_cbranch_vccnz 38f\n\t"
        "39:\n\t"
        "s_waitcnt vmcnt(8) lgkmcnt(6)\n\t"
        "v_mfma_f32_16x16x32_bf16 v[32:35], v[112:115], v[148:151], v[32:35]\n\t"
        "ds_read_b64_tr_b16 v[108:109], v121 offset:12608\n\t"
        "v_mfma_f32_16x16x32_bf16 v[40:43], v[112:115], v[152:155], v[40:43]\n\t"
        "ds_read_b64_tr_b16 v[110:111], v121 offset:15680\n\t"
        "v_mfma_f32_16x16x32_bf16 v[64:67], v[156:159], v[116:119], v[64:67]\n\t"
        "ds_read_b64_tr_b16 v[112:113], v121 offset:12640\n\t"
        "v_mfma_f32_16x16x32_bf16 v[72:75], v[160:163], v[116:119], v[72:75]\n\t"
        "ds_read_b64_tr_b16 v[114:115], v121 offset:15712\n\t"
        "s_waitcnt lgkmcnt(6)\n\t"
        "v_mfma_f32_16x16x32_bf16 v[68:71], v[156:159], v[96:99], v[68:71]\n\t"
        "ds_read_b64_tr_b16 v[116:117], v122 offset:12608\n\t"
        "ds_read_b128 v[196:199], v215 offset:0\n\t"
        "v_mfma_f32_16x16x32_bf16 v[76:79], v[160:163], v[96:99], v[76:79]\n\t"
        "ds_read_b64_tr_b16 v[118:119], v122 offset:15680\n\t"
        "v_mfma_f32_16x16x32_bf16 v[80:83], v[156:159], v[100:103], v[80:83]\n\t"
        "ds_read_b64_tr_b16 v[96:97], v122 offset:12640\n\t"
        "v_mfma_f32_16x16x32_bf16 v[88:91], v[160:163], v[100:103], v[88:91]\n\t"
        "ds_read_b64_tr_b16 v[98:99], v122 offset:15712\n\t"
        "s_waitcnt vmcnt(6) lgkmcnt(7)\n\t"
        "v_mfma_f32_16x16x32_bf16 v[84:87], v[156:159], v[104:107], v[84:87]\n\t"
        "ds_read_b64_tr_b16 v[100:101], v121 offset:24960\n\t"
        "ds_read_b128 v[200:203], v215 offset:64\n\t"
        "v_mfma_f32_16x16x32_bf16 v[92:95], v[160:163], v[104:107], v[92:95]\n\t"
        "ds_read_b64_tr_b16 v[102:103], v121 offset:28032\n\t"
        "v_mfma_f32_16x16x32_bf16 v[64:67], v[164:167], v[108:111], v[64:67]\n\t"
        "ds_read_b64_tr_b16 v[104:105], v121 offset:24992\n\t"
        "v_mfma_f32_16x16x32_bf16 v[72:75], v[168:171], v[108:111], v[72:75]\n\t"
        "ds_read_b64_tr_b16 v[106:107], v121 offset:28064\n\t"
        "s_waitcnt lgkmcnt(7)\n\t"
        "v_mfma_f32_16x16x32_bf16 v[68:71], v[164:167], v[112:115], v[68:71]\n\t"
        "ds_read_b64_tr_b16 v[108:109], v122 offset:24960\n\t"
        "v_mfma_f32_16x16x32_bf16 v[76:79], v[168:171], v[112:115], v[76:79]\n\t"
        "ds_read_b64_tr_b16 v[110:111], v122 offset:28032\n\t"
        "v_mfma_f32_16x16x32_bf16 v[80:83], v[164:167], v[116:119], v[80:83]\n\t"
        "ds_read_b64_tr_b16 v[112:113], v122 offset:24992\n\t"
        "v_mfma_f32_16x16x32_bf16 v[88:91], v[168:171], v[116:119], v[88:91]\n\t"
        "ds_read_b64_tr_b16 v[114:115], v122 offset:28064\n\t"
        "s_waitcnt vmcnt(4) lgkmcnt(6)\n\t"
        "v_mfma_f32_16x16x32_bf16 v[84:87], v[164:167], v[96:99], v[84:87]\n\t"
        "ds_read_b64_tr_b16 v[116:117], v121 offset:37312\n\t"
        "v_mfma_f32_16x16x32_bf16 v[92:95], v[168:171], v[96:99], v[92:95]\n\t"
        "ds_read_b64_tr_b16 v[118:119], v121 offset:40384\n\t"
        "v_mfma_f32_16x16x32_bf16 v[64:67], v[172:175], v[100:103], v[64:67]\n\t"
        "ds_read_b64_tr_b16 v[96:97], v121 offset:37344\n\t"
        "v_mfma_f32_16x16x32_bf16 v[72:75], v[176:179], v[100:103], v[72:75]\n\t"
        "ds_read_b64_tr_b16 v[98:99], v121 offset:40416\n\t"
        "s_waitcnt lgkmcnt(6)\n\t"
        "v_mfma_f32_16x16x32_bf16 v[68:71], v[172:175], v[104:107], v[68:71]\n\t"
        "ds_read_b64_tr_b16 v[100:101], v122 offset:37312\n\t"
        "v_mfma_f32_16x16x32_bf16 v[76:79], v[176:179], v[104:107], v[76:79]\n\t"
        "ds_read_b64_tr_b16 v[102:103], v122 offset:40384\n\t"
        "v_mfma_f32_16x16x32_bf16 v[80:83], v[172:175], v[108:111], v[80:83]\n\t"
        "ds_read_b64_tr_b16 v[104:105], v122 offset:37344\n\t"
        "v_mfma_f32_16x16x32_bf16 v[88:91], v[176:179], v[108:111], v[88:91]\n\t"
        "ds_read_b64_tr_b16 v[106:107], v122 offset:40416\n\t"
        "s_waitcnt vmcnt(2) lgkmcnt(6)\n\t"
        "v_mfma_f32_16x16x32_bf16 v[84:87], v[172:175], v[112:115], v[84:87]\n\t"
        "ds_read_b64_tr_b16 v[108:109], v121 offset:49664\n\t"
        "v_mfma_f32_16x16x32_bf16 v[92:95], v[176:179], v[112:115], v[92:95]\n\t"
        "ds_read_b64_tr_b16 v[110:111], v121 offset:52736\n\t"
        "v_mfma_f32_16x16x32_bf16 v[64:67], v[180:183], v[116:119], v[64:67]\n\t"
        "ds_read_b64_tr_b16 v[112:113], v121 offset:49696\n\t"
        "v_mfma_f32_16x16x32_bf16 v[72:75], v[184:187], v[116:119], v[72:75]\n\t"
        "ds_read_b64_tr_b16 v[114:115], v121 offset:52768\n\t"
        "s_waitcnt lgkmcnt(6)\n\t"
        "v_mfma_f32_16x16x32_bf16 v[68:71], v[180:183], v[96:99], v[68:71]\n\t"
        "ds_read_b64_tr_b16 v[116:117], v122 offset:49664\n\t"
        "v_mfma_f32_16x16x32_bf16 v[76:79], v[184:187], v[96:99], v[76:79]\n\t"
        "ds_read_b64_tr_b16 v[118:119], v122 offset:52736\n\t"
        "v_mfma_f32_16x16x32_bf16 v[80:83], v[180:183], v[100:103], v[80:83]\n\t"
        "ds_read_b64_tr_b16 v[96:97], v122 offset:49696\n\t"
        "v_mfma_f32_16x16x32_bf16 v[88:91], v[184:187], v[100:103], v[88:91]\n\t"
        "ds_read_b64_tr_b16 v[98:99], v122 offset:52768\n\t"
        "s_waitcnt vmcnt(0) lgkmcnt(6)\n\t"
        "v_mfma_f32_16x16x32_bf16 v[84:87], v[180:183], v[104:107], v[84:87]\n\t"
        "ds_read_b64_tr_b16 v[100:101], v123 offset:160\n\t"
        "v_mfma_f32_16x16x32_bf16 v[92:95], v[184:187], v[104:107], v[92:95]\n\t"
        "ds_read_b64_tr_b16 v[102:103], v123 offset:3232\n\t"
        "v_mfma_f32_16x16x32_bf16 v[64:67], v[188:191], v[108:111], v[64:67]\n\t"
        "ds_read_b64_tr_b16 v[104:105], v124 offset:128\n\t"
        "v_mfma_f32_16x16x32_bf16 v[72:75], v[192:195], v[108:111], v[72:75]\n\t"
        "ds_read_b64_tr_b16 v[106:107], v124 offset:3200\n\t"
        "s_waitcnt lgkmcnt(6)\n\t"
        "v_mfma_f32_16x16x32_bf16 v[68:71], v[188:191], v[112:115], v[68:71]\n\t"
        "ds_read_b64_tr_b16 v[108:109], v124 offset:160\n\t"
        "v_mfma_f32_16x16x32_bf16 v[76:79], v[192:195], v[112:115], v[76:79]\n\t"
        "ds_read_b64_tr_b16 v[110:111], v124 offset:3232\n\t"
        "v_mfma_f32_16x16x32_bf16 v[80:83], v[188:191], v[116:119], v[80:83]\n\t"
        "ds_read_b64_tr_b16 v[112:113], v121 offset:128\n\t"
        "v_mfma_f32_16x16x32_bf16 v[88:91], v[192:195], v[116:119], v[88:91]\n\t"
        "ds_read_b64_tr_b16 v[114:115], v121 offset:3200\n\t"
        "s_waitcnt lgkmcnt(6)\n\t"
        "v_mfma_f32_16x16x32_bf16 v[84:87], v[188:191], v[96:99], v[84:87]\n\t"
        "ds_read_b64_tr_b16 v[116:117], v121 offset:160\n\t"
        "v_mfma_f32_16x16x32_bf16 v[92:95], v[192:195], v[96:99], v[92:95]\n\t"
        "ds_read_b64_tr_b16 v[118:119], v121 offset:3232\n\t"
        "v_mfma_f32_16x16x32_bf16 v[36:39], v[100:103], v[148:151], v[36:39]\n\t"
        "ds_read_b64_tr_b16 v[96:97], v122 offset:128\n\t"
        "v_mfma_f32_16x16x32_bf16 v[44:47], v[100:103], v[152:155], v[44:47]\n\t"
        "ds_read_b64_tr_b16 v[98:99], v122 offset:3200\n\t"
        "s_waitcnt lgkmcnt(6)\n\t"
        "v_mfma_f32_16x16x32_bf16 v[48:51], v[104:107], v[148:151], v[48:51]\n\t"
        "ds_read_b64_tr_b16 v[100:101], v122 offset:160\n\t"
        "v_mul_f32 v64, v196, v64\n\t"
        "v_mul_f32 v65, v197, v65\n\t"
        "v_mfma_f32_16x16x32_bf16 v[56:59], v[104:107], v[152:155], v[56:59]\n\t"
        "ds_read_b64_tr_b16 v[102:103], v122 offset:3232\n\t"
        "v_mul_f32 v66, v198, v66\n\t"
        "v_mul_f32 v67, v199, v67\n\t"
        "v_mfma_f32_16x16x32_bf16 v[52:55], v[108:111], v[148:151], v[52:55]\n\t"
        "ds_read_b64_tr_b16 v[104:105], v121 offset:12480\n\t"
        "v_mul_f32 v68, v196, v68\n\t"
        "v_mul_f32 v69, v197, v69\n\t"
        "v_mul_f32 v70, v198, v70\n\t"
        "v_mfma_f32_16x16x32_bf16 v[60:63], v[108:111], v[152:155], v[60:63]\n\t"
        "ds_read_b64_tr_b16 v[106:107], v121 offset:15552\n\t"
        "v_mul_f32 v71, v199, v71\n\t"
        "v_cvt_pk_bf16_f32 v64, v64, v65\n\t"
        "s_waitcnt lgkmcnt(6)\n\t"
        "v_mfma_f32_16x16x32_bf16 v[32:35], v[112:115], v[156:159], v[32:35]\n\t"
        "ds_read_b64_tr_b16 v[108:109], v121 offset:12512\n\t"
        "v_cvt_pk_bf16_f32 v65, v66, v67\n\t"
        "v_cvt_pk_bf16_f32 v66, v68, v69\n\t"
        "v_mfma_f32_16x16x32_bf16 v[40:43], v[112:115], v[160:163], v[40:43]\n\t"
        "ds_read_b64_tr_b16 v[110:111], v121 offset:15584\n\t"
        "v_cvt_pk_bf16_f32 v67, v70, v71\n\t"
        "v_mul_f32 v72, v200, v72\n\t"
        "v_mfma_f32_16x16x32_bf16 v[36:39], v[116:119], v[156:159], v[36:39]\n\t"
        "ds_read_b64_tr_b16 v[112:113], v122 offset:12480\n\t"
        "v_mul_f32 v73, v201, v73\n\t"
        "v_mul_f32 v74, v202, v74\n\t"
        "v_mul_f32 v75, v203, v75\n\t"
        "v_mfma_f32_16x16x32_bf16 v[44:47], v[116:119], v[160:163], v[44:47]\n\t"
        "ds_read_b64_tr_b16 v[114:115], v122 offset:15552\n\t"
        "v_mul_f32 v76, v200, v76\n\t"
        "v_mul_f32 v77, v201, v77\n\t"
        "s_waitcnt lgkmcnt(6)\n\t"
        "v_mfma_f32_16x16x32_bf16 v[48:51], v[96:99], v[156:159], v[48:51]\n\t"
        "ds_read_b64_tr_b16 v[116:117], v122 offset:12512\n\t"
        "v_mul_f32 v78, v202, v78\n\t"
        "v_mul_f32 v79, v203, v79\n\t"
        "v_mfma_f32_16x16x32_bf16 v[56:59], v[96:99], v[160:163], v[56:59]\n\t"
        "ds_read_b64_tr_b16 v[118:119], v122 offset:15584\n\t"
        "v_cvt_pk_bf16_f32 v68, v72, v73\n\t"
        "v_cvt_pk_bf16_f32 v69, v74, v75\n\t"
        "v_mfma_f32_16x16x32_bf16 v[52:55], v[100:103], v[156:159], v[52:55]\n\t"
        "ds_read_b64_tr_b16 v[96:97], v121 offset:24832\n\t"
        "v_cvt_pk_bf16_f32 v70, v76, v77\n\t"
        "v_cvt_pk_bf16_f32 v71, v78, v79\n\t"
        "v_permlane16_swap_b32 v64, v66\n\t"
        "v_mfma_f32_16x16x32_bf16 v[60:63], v[100:103], v[160:163], v[60:63]\n\t"
        "ds_read_b64_tr_b16 v[98:99], v121 offset:27904\n\t"
        "v_permlane16_swap_b32 v65, v67\n\t"
        "v_permlane16_swap_b32 v68, v70\n\t"
        "s_waitcnt lgkmcnt(6)\n\t"
        "v_mfma_f32_16x16x32_bf16 v[32:35], v[104:107], v[164:167], v[32:35]\n\t"
        "ds_read_b64_tr_b16 v[100:101], v121 offset:24864\n\t"
        "v_permlane16_swap_b32 v69, v71\n\t"
        "buffer_store_dwordx4 v[64:67], v213, s[40:43], 0 offen offset:2048 sc1\n\t"
        "v_mfma_f32_16x16x32_bf16 v[40:43], v[104:107], v[168:171], v[40:43]\n\t"
        "ds_read_b64_tr_b16 v[102:103], v121 offset:27936\n\t"
        "buffer_store_dwordx4 v[68:71], v213, s[40:43], 0 offen offset:3072 sc1\n\t"
        "v_mul_f32 v80, v196, v80\n\t"
        "v_mfma_f32_16x16x32_bf16 v[36:39], v[108:111], v[164:167], v[36:39]\n\t"
        "ds_read_b64_tr_b16 v[104:105], v122 offset:24832\n\t"
        "v_mul_f32 v81, v197, v81\n\t"
        "v_mul_f32 v82, v198, v82\n\t"
        "v_mul_f32 v83, v199, v83\n\t"
        "v_mfma_f32_16x16x32_bf16 v[44:47], v[108:111], v[168:171], v[44:47]\n\t"
        "ds_read_b64_tr_b16 v[106:107], v122 offset:27904\n\t"
        "v_mul_f32 v84, v196, v84\n\t"
        "v_mul_f32 v85, v197, v85\n\t"
        "s_waitcnt lgkmcnt(6)\n\t"
        "v_mfma_f32_16x16x32_bf16 v[48:51], v[112:115], v[164:167], v[48:51]\n\t"
        "ds_read_b64_tr_b16 v[108:109], v122 offset:24864\n\t"
        "v_mul_f32 v86, v198, v86\n\t"
        "v_mul_f32 v87, v199, v87\n\t"
        "v_mfma_f32_16x16x32_bf16 v[56:59], v[112:115], v[168:171], v[56:59]\n\t"
        "ds_read_b64_tr_b16 v[110:111], v122 offset:27936\n\t"
        "v_cvt_pk_bf16_f32 v80, v80, v81\n\t"
        "v_cvt_pk_bf16_f32 v81, v82, v83\n\t"
        "v_mfma_f32_16x16x32_bf16 v[52:55], v[116:119], v[164:167], v[52:55]\n\t"
        "ds_read_b64_tr_b16 v[112:113], v121 offset:37184\n\t"
        "v_cvt_pk_bf16_f32 v82, v84, v85\n\t"
        "v_cvt_pk_bf16_f32 v83, v86, v87\n\t"
        "v_mul_f32 v88, v200, v88\n\t"
        "v_mfma_f32_16x16x32_bf16 v[60:63], v[116:119], v[168:171], v[60:63]\n\t"
        "ds_read_b64_tr_b16 v[114:115], v121 offset:40256\n\t"
        "v_mul_f32 v89, v201, v89\n\t"
        "v_mul_f32 v90, v202, v90\n\t"
        "s_waitcnt lgkmcnt(6)\n\t"
        "v_mfma_f32_16x16x32_bf16 v[32:35], v[96:99], v[172:175], v[32:35]\n\t"
        "ds_read_b64_tr_b16 v[116:117], v121 offset:37216\n\t"
        "v_mul_f32 v91, v203, v91\n\t"
        "v_mul_f32 v92, v200, v92\n\t"
        "v_mfma_f32_16x16x32_bf16 v[40:43], v[96:99], v[176:179], v[40:43]\n\t"
        "ds_read_b64_tr_b16 v[118:119], v121 offset:40288\n\t"
        "v_mul_f32 v93, v201, v93\n\t"
        "v_mul_f32 v94, v202, v94\n\t"
        "v_mfma_f32_16x16x32_bf16 v[36:39], v[100:103], v[172:175], v[36:39]\n\t"
        "ds_read_b64_tr_b16 v[96:97], v122 offset:37184\n\t"
        "v_mul_f32 v95, v203, v95\n\t"
        "v_cvt_pk_bf16_f32 v84, v88, v89\n\t"
        "v_cvt_pk_bf16_f32 v85, v90, v91\n\t"
        "v_mfma_f32_16x16x32_bf16 v[44:47], v[100:103], v[176:179], v[44:47]\n\t"
        "ds_read_b64_tr_b16 v[98:99], v122 offset:40256\n\t"
        "v_cvt_pk_bf16_f32 v86, v92, v93\n\t"
        "v_cvt_pk_bf16_f32 v87, v94, v95\n\t"
        "s_waitcnt lgkmcnt(6)\n\t"
        "v_mfma_f32_16x16x32_bf16 v[48:51], v[104:107], v[172:175], v[48:51]\n\t"
        "ds_read_b64_tr_b16 v[100:101], v122 offset:37216\n\t"
        "v_permlane16_swap_b32 v80, v82\n\t"
        "v_permlane16_swap_b32 v81, v83\n\t"
        "v_mfma_f32_16x16x32_bf16 v[56:59], v[104:107], v[176:179], v[56:59]\n\t"
        "ds_read_b64_tr_b16 v[102:103], v122 offset:40288\n\t"
        "v_permlane16_swap_b32 v84, v86\n\t"
        "v_permlane16_swap_b32 v85, v87\n\t"
        "v_mfma_f32_16x16x32_bf16 v[52:55], v[108:111], v[172:175], v[52:55]\n\t"
        "ds_read_b64_tr_b16 v[104:105], v121 offset:49536\n\t"
        "buffer_store_dwordx4 v[80:83], v214, s[40:43], 0 offen offset:2048 sc1\n\t"
        "buffer_store_dwordx4 v[84:87], v214, s[40:43], 0 offen offset:3072 sc1\n\t"
        "v_mfma_f32_16x16x32_bf16 v[60:63], v[108:111], v[176:179], v[60:63]\n\t"
        "ds_read_b64_tr_b16 v[106:107], v121 offset:52608\n\t"
        "s_waitcnt lgkmcnt(6)\n\t"
        "v_mfma_f32_16x16x32_bf16 v[32:35], v[112:115], v[180:183], v[32:35]\n\t"
        "ds_read_b64_tr_b16 v[108:109], v121 offset:49568\n\t"
        "v_mfma_f32_16x16x32_bf16 v[40:43], v[112:115], v[184:187], v[40:43]\n\t"
        "ds_read_b64_tr_b16 v[110:111], v121 offset:52640\n\t"
        "v_mfma_f32_16x16x32_bf16 v[36:39], v[116:119], v[180:183], v[36:39]\n\t"
        "ds_read_b64_tr_b16 v[112:113], v122 offset:49536\n\t"
        "v_mfma_f32_16x16x32_bf16 v[44:47], v[116:119], v[184:187], v[44:47]\n\t"
        "ds_read_b64_tr_b16 v[114:115], v122 offset:52608\n\t"
        "s_waitcnt lgkmcnt(6)\n\t"
        "v_mfma_f32_16x16x32_bf16 v[48:51], v[96:99], v[180:183], v[48:51]\n\t"
        "ds_read_b64_tr_b16 v[116:117], v122 offset:49568\n\t"
        "v_mfma_f32_16x16x32_bf16 v[56:59], v[96:99], v[184:187], v[56:59]\n\t"
        "ds_read_b64_tr_b16 v[118:119], v122 offset:52640\n\t"
        "v_mfma_f32_16x16x32_bf16 v[52:55], v[100:103], v[180:183], v[52:55]\n\t"
        "ds_read_b64_tr_b16 v[96:97], v123 offset:0\n\t"
        "v_mfma_f32_16x16x32_bf16 v[60:63], v[100:103], v[184:187], v[60:63]\n\t"
        "ds_read_b64_tr_b16 v[98:99], v123 offset:3072\n\t"
        "s_waitcnt lgkmcnt(6)\n\t"
        "v_mfma_f32_16x16x32_bf16 v[32:35], v[104:107], v[188:191], v[32:35]\n\t"
        "ds_read_b64_tr_b16 v[100:101], v123 offset:32\n\t"
        "v_mfma_f32_16x16x32_bf16 v[40:43], v[104:107], v[192:195], v[40:43]\n\t"
        "ds_read_b64_tr_b16 v[102:103], v123 offset:3104\n\t"
        "v_mfma_f32_16x16x32_bf16 v[36:39], v[108:111], v[188:191], v[36:39]\n\t"
        "ds_read_b64_tr_b16 v[104:105], v124 offset:0\n\t"
        "v_mfma_f32_16x16x32_bf16 v[44:47], v[108:111], v[192:195], v[44:47]\n\t"
        "ds_read_b64_tr_b16 v[106:107], v124 offset:3072\n\t"
        "s_waitcnt lgkmcnt(6)\n\t"
        "v_mfma_f32_16x16x32_bf16 v[48:51], v[112:115], v[188:191], v[48:51]\n\t"
        "ds_read_b64_tr_b16 v[108:109], v124 offset:32\n\t"
        "v_mfma_f32_16x16x32_bf16 v[56:59], v[112:115], v[192:195], v[56:59]\n\t"
        "ds_read_b64_tr_b16 v[110:111], v124 offset:3104\n\t"
        "v_mfma_f32_16x16x32_bf16 v[52:55], v[116:119], v[188:191], v[52:55]\n\t"
        "ds_read_b64_tr_b16 v[112:113], v121 offset:0\n\t"
        "v_mfma_f32_16x16x32_bf16 v[60:63], v[116:119], v[192:195], v[60:63]\n\t"
        "ds_read_b64_tr_b16 v[114:115], v121 offset:3072\n\t"
        "s_waitcnt lgkmcnt(6)\n\t"
        "v_mfma_f32_16x16x32_bf16 v[0:3], v[96:99], v[148:151], v[0:3]\n\t"
        "ds_read_b64_tr_b16 v[116:117], v121 offset:32\n\t"
        "v_mfma_f32_16x16x32_bf16 v[8:11], v[96:99], v[152:155], v[8:11]\n\t"
        "ds_read_b64_tr_b16 v[118:119], v121 offset:3104\n\t"
        "v_mfma_f32_16x16x32_bf16 v[4:7], v[100:103], v[148:151], v[4:7]\n\t"
        "ds_read_b64_tr_b16 v[96:97], v122 offset:0\n\t"
        "v_mul_f32 v32, v210, v32\n\t"
        "v_mul_f32 v33, v210, v33\n\t"
        "v_mul_f32 v34, v210, v34\n\t"
        "v_mfma_f32_16x16x32_bf16 v[12:15], v[100:103], v[152:155], v[12:15]\n\t"
        "ds_read_b64_tr_b16 v[98:99], v122 offset:3072\n\t"
        "v_mul_f32 v35, v210, v35\n\t"
        "v_mul_f32 v36, v210, v36\n\t"
        "s_waitcnt lgkmcnt(6)\n\t"
        "v_mfma_f32_16x16x32_bf16 v[16:19], v[104:107], v[148:151], v[16:19]\n\t"
        "ds_read_b64_tr_b16 v[100:101], v122 offset:32\n\t"
        "v_mul_f32 v37, v210, v37\n\t"
        "v_mul_f32 v38, v210, v38\n\t"
        "v_mfma_f32_16x16x32_bf16 v[24:27], v[104:107], v[152:155], v[24:27]\n\t"
        "ds_read_b64_tr_b16 v[102:103], v122 offset:3104\n\t"
        "v_mul_f32 v39, v210, v39\n\t"
        "v_cvt_pk_bf16_f32 v32, v32, v33\n\t"
        "v_mfma_f32_16x16x32_bf16 v[20:23], v[108:111], v[148:151], v[20:23]\n\t"
        "ds_read_b64_tr_b16 v[104:105], v121 offset:12352\n\t"
        "v_cvt_pk_bf16_f32 v33, v34, v35\n\t"
        "v_cvt_pk_bf16_f32 v34, v36, v37\n\t"
        "v_cvt_pk_bf16_f32 v35, v38, v39\n\t"
        "v_mfma_f32_16x16x32_bf16 v[28:31], v[108:111], v[152:155], v[28:31]\n\t"
        "ds_read_b64_tr_b16 v[106:107], v121 offset:15424\n\t"
        "v_mul_f32 v40, v211, v40\n\t"
        "v_mul_f32 v41, v211, v41\n\t"
        "s_waitcnt lgkmcnt(6)\n\t"
        "v_mfma_f32_16x16x32_bf16 v[0:3], v[112:115], v[156:159], v[0:3]\n\t"
        "ds_read_b64_tr_b16 v[108:109], v121 offset:12384\n\t"
        "v_mul_f32 v42, v211, v42\n\t"
        "v_mul_f32 v43, v211, v43\n\t"
        "v_mfma_f32_16x16x32_bf16 v[8:11], v[112:115], v[160:163], v[8:11]\n\t"
        "ds_read_b64_tr_b16 v[110:111], v121 offset:15456\n\t"
        "v_mul_f32 v44, v211, v44\n\t"
        "v_mul_f32 v45, v211, v45\n\t"
        "v_mfma_f32_16x16x32_bf16 v[4:7], v[116:119], v[156:159], v[4:7]\n\t"
        "ds_read_b64_tr_b16 v[112:113], v122 offset:12352\n\t"
        "v_mul_f32 v46, v211, v46\n\t"
        "v_mul_f32 v47, v211, v47\n\t"
        "v_cvt_pk_bf16_f32 v36, v40, v41\n\t"
        "v_mfma_f32_16x16x32_bf16 v[12:15], v[116:119], v[160:163], v[12:15]\n\t"
        "ds_read_b64_tr_b16 v[114:115], v122 offset:15424\n\t"
        "v_cvt_pk_bf16_f32 v37, v42, v43\n\t"
        "v_cvt_pk_bf16_f32 v38, v44, v45\n\t"
        "s_waitcnt lgkmcnt(6)\n\t"
        "v_mfma_f32_16x16x32_bf16 v[16:19], v[96:99], v[156:159], v[16:19]\n\t"
        "ds_read_b64_tr_b16 v[116:117], v122 offset:12384\n\t"
        "v_cvt_pk_bf16_f32 v39, v46, v47\n\t"
        "v_permlane16_swap_b32 v32, v36\n\t"
        "v_mfma_f32_16x16x32_bf16 v[24:27], v[96:99], v[160:163], v[24:27]\n\t"
        "ds_read_b64_tr_b16 v[118:119], v122 offset:15456\n\t"
        "v_permlane16_swap_b32 v33, v37\n\t"
        "v_permlane16_swap_b32 v34, v38\n\t"
        "v_mfma_f32_16x16x32_bf16 v[20:23], v[100:103], v[156:159], v[20:23]\n\t"
        "ds_read_b64_tr_b16 v[96:97], v121 offset:24704\n\t"
        "v_permlane16_swap_b32 v35, v39\n\t"
        "buffer_store_dwordx4 v[32:35], v213, s[40:43], 0 offen offset:0 sc1\n\t"
        "buffer_store_dwordx4 v[36:39], v213, s[40:43], 0 offen offset:1024 sc1\n\t"
        "v_mfma_f32_16x16x32_bf16 v[28:31], v[100:103], v[160:163], v[28:31]\n\t"
        "ds_read_b64_tr_b16 v[98:99], v121 offset:27776\n\t"
        "v_mul_f32 v48, v210, v48\n\t"
        "v_mul_f32 v49, v210, v49\n\t"
        "s_waitcnt lgkmcnt(6)\n\t"
        "v_mfma_f32_16x16x32_bf16 v[0:3], v[104:107], v[164:167], v[0:3]\n\t"
        "ds_read_b64_tr_b16 v[100:101], v121 offset:24736\n\t"
        "v_mul_f32 v50, v210, v50\n\t"
        "v_mul_f32 v51, v210, v51\n\t"
        "v_mfma_f32_16x16x32_bf16 v[8:11], v[104:107], v[168:171], v[8:11]\n\t"
        "ds_read_b64_tr_b16 v[102:103], v121 offset:27808\n\t"
        "v_mul_f32 v52, v210, v52\n\t"
        "v_mul_f32 v53, v210, v53\n\t"
        "v_mfma_f32_16x16x32_bf16 v[4:7], v[108:111], v[164:167], v[4:7]\n\t"
        "ds_read_b64_tr_b16 v[104:105], v122 offset:24704\n\t"
        "v_mul_f32 v54, v210, v54\n\t"
        "v_mul_f32 v55, v210, v55\n\t"
        "v_cvt_pk_bf16_f32 v48, v48, v49\n\t"
        "v_mfma_f32_16x16x32_bf16 v[12:15], v[108:111], v[168:171], v[12:15]\n\t"
        "ds_read_b64_tr_b16 v[106:107], v122 offset:27776\n\t"
        "v_cvt_pk_bf16_f32 v49, v50, v51\n\t"
        "v_cvt_pk_bf16_f32 v50, v52, v53\n\t"
        "s_waitcnt lgkmcnt(6)\n\t"
        "v_mfma_f32_16x16x32_bf16 v[16:19], v[112:115], v[164:167], v[16:19]\n\t"
        "ds_read_b64_tr_b16 v[108:109], v122 offset:24736\n\t"
        "v_cvt_pk_bf16_f32 v51, v54, v55\n\t"
        "v_mul_f32 v56, v211, v56\n\t"
        "v_mfma_f32_16x16x32_bf16 v[24:27], v[112:115], v[168:171], v[24:27]\n\t"
        "ds_read_b64_tr_b16 v[110:111], v122 offset:27808\n\t"
        "v_mul_f32 v57, v211, v57\n\t"
        "v_mul_f32 v58, v211, v58\n\t"
        "v_mfma_f32_16x16x32_bf16 v[20:23], v[116:119], v[164:167], v[20:23]\n\t"
        "ds_read_b64_tr_b16 v[112:113], v121 offset:37056\n\t"
        "v_mul_f32 v59, v211, v59\n\t"
        "v_mul_f32 v60, v211, v60\n\t"
        "v_mul_f32 v61, v211, v61\n\t"
        "v_mfma_f32_16x16x32_bf16 v[28:31], v[116:119], v[168:171], v[28:31]\n\t"
        "ds_read_b64_tr_b16 v[114:115], v121 offset:40128\n\t"
        "v_mul_f32 v62, v211, v62\n\t"
        "v_mul_f32 v63, v211, v63\n\t"
        "s_waitcnt lgkmcnt(6)\n\t"
        "v_mfma_f32_16x16x32_bf16 v[0:3], v[96:99], v[172:175], v[0:3]\n\t"
        "ds_read_b64_tr_b16 v[116:117], v121 offset:37088\n\t"
        "v_cvt_pk_bf16_f32 v52, v56, v57\n\t"
        "v_cvt_pk_bf16_f32 v53, v58, v59\n\t"
        "v_mfma_f32_16x16x32_bf16 v[8:11], v[96:99], v[176:179], v[8:11]\n\t"
        "ds_read_b64_tr_b16 v[118:119], v121 offset:40160\n\t"
        "v_cvt_pk_bf16_f32 v54, v60, v61\n\t"
        "v_cvt_pk_bf16_f32 v55, v62, v63\n\t"
        "v_mfma_f32_16x16x32_bf16 v[4:7], v[100:103], v[172:175], v[4:7]\n\t"
        "ds_read_b64_tr_b16 v[96:97], v122 offset:37056\n\t"
        "v_permlane16_swap_b32 v48, v52\n\t"
        "v_permlane16_swap_b32 v49, v53\n\t"
        "v_permlane16_swap_b32 v50, v54\n\t"
        "v_mfma_f32_16x16x32_bf16 v[12:15], v[100:103], v[176:179], v[12:15]\n\t"
        "ds_read_b64_tr_b16 v[98:99], v122 offset:40128\n\t"
        "v_permlane16_swap_b32 v51, v55\n\t"
        "buffer_store_dwordx4 v[48:51], v214, s[40:43], 0 offen offset:0 sc1\n\t"
        "s_waitcnt lgkmcnt(6)\n\t"
        "v_mfma_f32_16x16x32_bf16 v[16:19], v[104:107], v[172:175], v[16:19]\n\t"
        "ds_read_b64_tr_b16 v[100:101], v122 offset:37088\n\t"
        "buffer_store_dwordx4 v[52:55], v214, s[40:43], 0 offen offset:1024 sc1\n\t"
        "v_mfma_f32_16x16x32_bf16 v[24:27], v[104:107], v[176:179], v[24:27]\n\t"
        "ds_read_b64_tr_b16 v[102:103], v122 offset:40160\n\t"
        "v_mfma_f32_16x16x32_bf16 v[20:23], v[108:111], v[172:175], v[20:23]\n\t"
        "ds_read_b64_tr_b16 v[104:105], v121 offset:49408\n\t"
        "v_mfma_f32_16x16x32_bf16 v[28:31], v[108:111], v[176:179], v[28:31]\n\t"
        "ds_read_b64_tr_b16 v[106:107], v121 offset:52480\n\t"
        "s_waitcnt lgkmcnt(6)\n\t"
        "v_mfma_f32_16x16x32_bf16 v[0:3], v[112:115], v[180:183], v[0:3]\n\t"
        "ds_read_b64_tr_b16 v[108:109], v121 offset:49440\n\t"
        "v_mfma_f32_16x16x32_bf16 v[8:11], v[112:115], v[184:187], v[8:11]\n\t"
        "ds_read_b64_tr_b16 v[110:111], v121 offset:52512\n\t"
        "v_mfma_f32_16x16x32_bf16 v[4:7], v[116:119], v[180:183], v[4:7]\n\t"
        "ds_read_b64_tr_b16 v[112:113], v122 offset:49408\n\t"
        "v_mfma_f32_16x16x32_bf16 v[12:15], v[116:119], v[184:187], v[12:15]\n\t"
        "ds_read_b64_tr_b16 v[114:115], v122 offset:52480\n\t"
        "s_waitcnt lgkmcnt(6)\n\t"
        "v_mfma_f32_16x16x32_bf16 v[16:19], v[96:99], v[180:183], v[16:19]\n\t"
        "ds_read_b64_tr_b16 v[116:117], v122 offset:49440\n\t"
        "v_mfma_f32_16x16x32_bf16 v[24:27], v[96:99], v[184:187], v[24:27]\n\t"
        "ds_read_b64_tr_b16 v[118:119], v122 offset:52512\n\t"
        "v_mfma_f32_16x16x32_bf16 v[20:23], v[100:103], v[180:183], v[20:23]\n\t"
        "v_mfma_f32_16x16x32_bf16 v[28:31], v[100:103], v[184:187], v[28:31]\n\t"
        "s_waitcnt lgkmcnt(4)\n\t"
        "v_mfma_f32_16x16x32_bf16 v[0:3], v[104:107], v[188:191], v[0:3]\n\t"
        "v_mfma_f32_16x16x32_bf16 v[8:11], v[104:107], v[192:195], v[8:11]\n\t"
        "v_mfma_f32_16x16x32_bf16 v[4:7], v[108:111], v[188:191], v[4:7]\n\t"
        "v_mfma_f32_16x16x32_bf16 v[12:15], v[108:111], v[192:195], v[12:15]\n\t"
        "s_waitcnt lgkmcnt(0)\n\t"
        "v_mfma_f32_16x16x32_bf16 v[16:19], v[112:115], v[188:191], v[16:19]\n\t"
        "v_mfma_f32_16x16x32_bf16 v[24:27], v[112:115], v[192:195], v[24:27]\n\t"
        "v_mfma_f32_16x16x32_bf16 v[20:23], v[116:119], v[188:191], v[20:23]\n\t"
        "v_mfma_f32_16x16x32_bf16 v[28:31], v[116:119], v[192:195], v[28:31]\n\t"
        "s_nop 7\n\t"
        "s_branch 9f\n\t"
        "14:\n\t"
        "s_mov_b32 s48, 0x10000\n\t"
        "8:\n\t"
        "ds_read_b32 v126, v125\n\t"
        "s_sub_u32 s48, s48, 1\n\t"
        "s_waitcnt lgkmcnt(0)\n\t"
        "s_cbranch_scc1 15b\n\t"
        "v_cmp_gt_u32 vcc, v128, v126\n\t"
        "s_cbranch_vccnz 8b\n\t"
        "s_branch 15b\n\t"
        "16:\n\t"
        "s_mov_b32 s48, 0x10000\n\t"
        "8:\n\t"
        "ds_read_b32 v126, v125\n\t"
        "s_sub_u32 s48, s48, 1\n\t"
        "s_waitcnt lgkmcnt(0)\n\t"
        "s_cbranch_scc1 17b\n\t"
        "v_cmp_gt_u32 vcc, v128, v126\n\t"
        "s_cbranch_vccnz 8b\n\t"
        "s_branch 17b\n\t"
        "18:\n\t"
        "s_mov_b32 s48, 0x10000\n\t"
        "8:\n\t"
        "ds_read_b32 v126, v125\n\t"
        "s_sub_u32 s48, s48, 1\n\t"
        "s_waitcnt lgkmcnt(0)\n\t"
        "s_cbranch_scc1 19b\n\t"
        "v_cmp_gt_u32 vcc, v128, v126\n\t"
        "s_cbranch_vccnz 8b\n\t"
        "s_branch 19b\n\t"
        "20:\n\t"
        "s_mov_b32 s48, 0x10000\n\t"
        "8:\n\t"
        "ds_read_b32 v126, v125\n\t"
        "s_sub_u32 s48, s48, 1\n\t"
        "s_waitcnt lgkmcnt(0)\n\t"
        "s_cbranch_scc1 21b\n\t"
        "v_cmp_gt_u32 vcc, v128, v126\n\t"
        "s_cbranch_vccnz 8b\n\t"
        "s_branch 21b\n\t"
        "38:\n\t"
        "s_mov_b32 s48, 0x10000\n\t"
        "8:\n\t"
        "ds_read_b32 v126, v125\n\t"
        "s_sub_u32 s48, s48, 1\n\t"
        "s_waitcnt lgkmcnt(0)\n\t"
        "s_cbranch_scc1 39b\n\t"
        "v_cmp_gt_u32 vcc, v128, v126\n\t"
        "s_cbranch_vccnz 8b\n\t"
        "s_branch 39b\n\t"
        "9:\n\t"
        : "+{v[0:15]}"(acc[0]), "+{v[16:31]}"(acc[1]), "+{v[32:47]}"(acc[2]), "+{v[48:63]}"(acc[3]), "+{v[64:79]}"(acc[4]), "+{v[80:95]}"(acc[5]),
          "+{v[96:111]}"(stA), "+{v[112:127]}"(stB), "+{v[128:131]}"(stC), "+{v[148:163]}"(xr1), "+{v[164:179]}"(xr2), "+{v[180:195]}"(xr3),
          "+{s44}"(soff), "+{s46}"(slot)
        : "{s[40:43]}"(srd), KSKIP, "{v213}"(ep[0]), "{v214}"(ep[1]), "{v215}"(ep[2]), "{v[210:211]}"(al2) : "v196", "v197", "v198", "v199", "v200", "v201", "v202", "v203", "v132", "v133", "v134", "v135", "v136", "v137", "v138", "v139", "v140", "v141", "v142", "v143", "v144", "v145", "v146", "v147", "s45", "s47", "s48", "vcc", "scc", "memory");
      STAMPV(4 + 16 * L);
      PHASE_VARS;
      u32x4 kv[4][2];
#pragma unroll
      for (int j = 0; j < 4; ++j)
#pragma unroll
        for (int e = 0; e < 8; ++e) kv[j][e >> 2][e & 3] = __float_as_uint(acc[2 + j][e]);
      u32x4 qo[2][2];
      auto drained = [&]() { asm volatile("s_waitcnt vmcnt(0)\n\tds_write_b32 %0, %1" :: "v"(lbase + L_W + 80 + 4 * wid), "v"(2 * L + 1) : "memory"); };
      if (L == 0) {
#pragma unroll
        for (int e = 0; e < 16; ++e) {
          if (e == 8) { SBAR(); drained(); SBAR(); }
          const unsigned w = pack2(acc[e >> 3][2 * (e & 7)], acc[e >> 3][2 * (e & 7) + 1]);
          qo[e >> 3][(e >> 2) & 1][e & 3] = pack2(lo16(w) * QS, hi16(w) * QS);
        }
        row_swap(qo[0][0], qo[0][1]); row_swap(qo[1][0], qo[1][1]);
      } else {
        const f32x2 aq = al2 * QS;
#pragma unroll
        for (int e = 0; e < 16; ++e) qo[e >> 3][(e >> 2) & 1][e & 3] = pack2(acc[e >> 3][2 * (e & 7)] * aq[(e >> 2) & 1], acc[e >> 3][2 * (e & 7) + 1] * aq[(e >> 2) & 1]);
        row_swap(qo[0][0], qo[0][1]); row_swap(qo[1][0], qo[1][1]);
        SBAR();
        lds_wait_ge<0>(lbase + L_W + 4 + 4 * (lane & 1), 1);
        const int idx = (int)__builtin_amdgcn_readfirstlane(words[3]);
        if ((idx >> 5) == tile) {
          const int rel = (idx & 31) - 8 * hi, dfo = ((r32 >> 2) & 1) * 16 + ((r32 >> 3) & 1) * 8 + (r32 >> 4) * 4 + (r32 & 3);
          unsigned pv[2];
#pragma unroll
          for (int j = 0; j < 2; ++j) pv[j] = reinterpret_cast<const u16*>(smem + L_PATCH)[128 + (j ^ hh) * 32 + dfo] * 0x10001u;
          DRAIN();
          if (r32 == (idx & 31)) {
#pragma unroll
            for (int i = 0; i < 4; ++i) {
              const char* p = smem + L_PATCH + ((i >> 1) ^ hh) * 64 + (i & 1) * 32 + hi * 16;
              qo[i >> 1][i & 1] = *reinterpret_cast<const u32x4*>(p);
              kv[i >> 1][i & 1] = *reinterpret_cast<const u32x4*>(p + 128);
              x1p[i] = *reinterpret_cast<const u32x4*>(smem + L_X2P + (i * 2 + hi) * 16);
            }
            reinterpret_cast<float*>(smem + L_AB)[256 + wid * 64 + lane] = hi ? 1.0f : 0.0f;
#pragma unroll
            for (int i = 0; i < 4; ++i) stg(rs, ep[i >> 1] + (i & 1) * 1024, kv[i >> 1][i & 1]);
          }
#pragma unroll
          for (int i = 0; i < 4; ++i) {
#pragma unroll
            for (int e = 0; e < 4; ++e) {
              const int t0 = 16 * (i & 1) + 2 * e;
              const unsigned mask = rel == t0 ? 0xffffu : rel == t0 + 1 ? 0xffff0000u : 0u;
              kv[2 + (i >> 1)][i & 1][e] = (kv[2 + (i >> 1)][i & 1][e] & ~mask) | (pv[i >> 1] & mask);
            }
            stg(rs, (rel >> 3) == 2 * (i & 1) ? ep[i >> 1] + 2048 + (i & 1) * 1024 : OOB, kv[2 + (i >> 1)][i & 1]);
          }
        }
        if (8 < 16) drained();
      }
      SBAR();
#pragma unroll
      for (int j = 0; j < 2; ++j) {
        char* p = smem + L_XCH + ((j ^ hh) * 4 + wid) * 2048 + lane * 16;
        *reinterpret_cast<u32x4*>(p) = qo[j][0]; *reinterpret_cast<u32x4*>(p + 1024) = qo[j][1];
        p = smem + L_XCH + 16384 + wid * 4096 + j * 2048 + lane * 16;
        *reinterpret_cast<u32x4*>(p) = kv[2 * j + 1][0]; *reinterpret_cast<u32x4*>(p + 1024) = kv[2 * j + 1][1];
      }
      if (8 >= 16) drained();
      STAMPV(41 + 4 * L);
      BAR_LGKM();
      STAMPV(43 + 4 * L);
      kva = cat4(kv[0][0], kv[0][1], kv[2][0], kv[2][1]);
      const u32x4* pk = reinterpret_cast<const u32x4*>(smem + L_XCH + 16384 + (wid ^ 1) * 4096 + lane * 16);
#pragma unroll
      for (int u = 0; u < 4; ++u) {
        const u32x4* p = reinterpret_cast<const u32x4*>(smem + L_XCH + (hh * 4 + ((2 * kh + 2 + u) & 3)) * 2048 + lane * 16);
        q[u] = __builtin_shufflevector(p[0], p[64], 0, 1, 2, 3, 4, 5, 6, 7);
      }
      kvb = cat4(pk[0], pk[64], pk[128], pk[192]);
      SBAR();
      asm volatile(
        "v_mov_b64 v[0:1], 0\n\t"
        "v_mov_b64 v[2:3], 0\n\t"
        "v_mov_b64 v[4:5], 0\n\t"
        "v_mov_b64 v[6:7], 0\n\t"
        "v_mov_b64 v[8:9], 0\n\t"
        "v_mov_b64 v[10:11], 0\n\t"
        "v_mov_b64 v[12:13], 0\n\t"
        "v_mov_b64 v[14:15], 0\n\t"
        "v_mov_b64 v[16:17], 0\n\t"
        "v_mov_b64 v[18:19], 0\n\t"
        "v_mov_b64 v[20:21], 0\n\t"
        "v_mov_b64 v[22:23], 0\n\t"
        "v_mov_b64 v[24:25], 0\n\t"
        "v_mov_b64 v[26:27], 0\n\t"
        "v_mov_b64 v[28:29], 0\n\t"
        "v_mov_b64 v[30:31], 0\n\t"
        "v_mov_b64 v[32:33], 0\n\t"
        "v_mov_b64 v[34:35], 0\n\t"
        "v_mov_b64 v[36:37], 0\n\t"
        "v_mov_b64 v[38:39], 0\n\t"
        "v_mov_b64 v[40:41], 0\n\t"
        "v_mov_b64 v[42:43], 0\n\t"
        "v_mov_b64 v[44:45], 0\n\t"
        "v_mov_b64 v[46:47], 0\n\t"
        "v_mov_b64 v[48:49], 0\n\t"
        "v_mov_b64 v[50:51], 0\n\t"
        "v_mov_b64 v[52:53], 0\n\t"
        "v_mov_b64 v[54:55], 0\n\t"
        "v_mov_b64 v[56:57], 0\n\t"
        "v_mov_b64 v[58:59], 0\n\t"
        "v_mov_b64 v[60:61], 0\n\t"
        "v_mov_b64 v[62:63], 0\n\t"
        "v_mov_b64 v[128:129], 0\n\t"
        "v_mov_b64 v[130:131], 0\n\t"
        "v_mov_b64 v[132:133], 0\n\t"
        "v_mov_b64 v[134:135], 0\n\t"
        "v_mov_b64 v[136:137], 0\n\t"
        "v_mov_b64 v[138:139], 0\n\t"
        "v_mov_b64 v[140:141], 0\n\t"
        "v_mov_b64 v[142:143], 0\n\t"
        : "={v[0:15]}"(oz[0]), "={v[16:31]}"(oz[1]), "={v[32:47]}"(oz[2]), "={v[48:63]}"(oz[3]), "={v[128:143]}"(oz[4]));
    }
    STAMPV(5 + 16 * L);
    {
      u32x4 ones;
      unsigned kvoff;
      u32x8 fv, cv;
      asm volatile("" : "={v[184:191]}"(fv), "={v[216:223]}"(cv));
      { PHASE_VARS;
        const unsigned v = (lane & 3) == 0 ? 0x3F803F80u : 0u; const bool up = (lane & 4) != 0; ones[0] = ones[1] = up ? 0u : v; ones[2] = ones[3] = up ? v : 0u;
        kvoff = OFF_KV + L * (10 * MBY) + (tn * 2 + hh) * HS + kh * 8192 + lane * 16;
        fv[1] = lbase + L_FREE + wid * 256 + lane * 4; fv[2] = 32 * L + 32;
        cv[0] = lbase + L_XCH + wid * 8192 + lane * 16; cv[1] = lbase + L_XCH + (wid ^ 2) * 8192 + lane * 16;
        cv[2] = lbase + L_XCH + 32768 + wid * 512 + lane * 4; cv[3] = lbase + L_XCH + 32768 + (wid ^ 2) * 512 + lane * 4;
        cv[4] = lbase + L_W + 48 + wid * 4; cv[5] = lbase + L_W + 48 + (wid ^ 2) * 4; cv[6] = L + 1;
        cv[7] = ((tile * 32 + r32) * D_ + tn * 64 + hi * 8) * 2; }
      f32x16 o0 = oz[0], o1 = oz[1], o2 = oz[2], o3 = oz[3], lv = oz[4];
      unsigned soff = (tm | 7) * 16384;
      STAMP_DECL;
      asm volatile(
        "s_nop 1\n\t"
        "v_mfma_f32_32x32x16_bf16 v[96:111], v[152:155], v[64:67], 0\n\t"
        "v_mfma_f32_32x32x16_bf16 v[96:111], v[156:159], v[68:71], v[96:111]\n\t"
        "s_mov_b32 s46, 0\n\t"
        "s_mov_b64 s[54:55], s[42:43]\n\t"
        "ds_write_b32 v185, v186\n\t"
        "s_nop 8\n\t"
        "v_exp_f32 v96, v96\n\t"
        "v_exp_f32 v97, v97\n\t"
        "v_exp_f32 v98, v98\n\t"
        "v_exp_f32 v99, v99\n\t"
        "v_exp_f32 v100, v100\n\t"
        "v_exp_f32 v101, v101\n\t"
        "v_exp_f32 v102, v102\n\t"
        "v_mfma_f32_32x32x16_bf16 v[112:127], v[152:155], v[72:75], 0\n\t"
        "v_exp_f32 v103, v103\n\t"
        "v_exp_f32 v104, v104\n\t"
        "v_exp_f32 v105, v105\n\t"
        "v_mfma_f32_32x32x16_bf16 v[112:127], v[156:159], v[76:79], v[112:127]\n\t"
        "v_exp_f32 v106, v106\n\t"
        "v_exp_f32 v107, v107\n\t"
        "v_exp_f32 v108, v108\n\t"
        "v_smfmac_f32_32x32x32_bf16 v[0:15], v[160:163], v[96:103], v148\n\t"
        "v_exp_f32 v109, v109\n\t"
        "v_exp_f32 v110, v110\n\t"
        "v_exp_f32 v111, v111\n\t"
        "v_smfmac_f32_16x16x64_bf16 v[128:131], v[144:147], v[96:103], v148\n\t"
        "v_exp_f32 v112, v112\n\t"
        "v_exp_f32 v113, v113\n\t"
        "v_smfmac_f32_32x32x32_bf16 v[0:15], v[164:167], v[104:111], v148\n\t"
        "v_exp_f32 v114, v114\n\t"
        "v_exp_f32 v115, v115\n\t"
        "v_exp_f32 v116, v116\n\t"
        "v_smfmac_f32_16x16x64_bf16 v[128:131], v[144:147], v[104:111], v148\n\t"
        "v_exp_f32 v117, v117\n\t"
        "v_exp_f32 v118, v118\n\t"
        "v_mfma_f32_32x32x16_bf16 v[96:111], v[152:155], v[80:83], 0\n\t"
        "v_exp_f32 v119, v119\n\t"
        "v_exp_f32 v120, v120\n\t"
        "v_exp_f32 v121, v121\n\t"
        "v_mfma_f32_32x32x16_bf16 v[96:111], v[156:159], v[84:87], v[96:111]\n\t"
        "ds_read_b32 v151, v224\n\t"
        "v_exp_f32 v122, v122\n\t"
        "v_exp_f32 v123, v123\n\t"
        "v_exp_f32 v124, v124\n\t"
        "v_smfmac_f32_32x32x32_bf16 v[16:31], v[160:163], v[112:119], v148\n\t"
        "v_exp_f32 v125, v125\n\t"
        "v_exp_f32 v126, v126\n\t"
        "v_exp_f32 v127, v127\n\t"
        "v_smfmac_f32_16x16x64_bf16 v[132:135], v[144:147], v[112:119], v148\n\t"
        "s_cmp_lg_u32 s46, 0\n\t"
        "s_cbranch_scc1 25f\n\t"
        "s_waitcnt lgkmcnt(0)\n\t"
        "v_cmp_ne_u32 vcc, v222, v151\n\t"
        "s_cbranch_vccz 24f\n\t"
        "25:\n\t"
        "v_exp_f32 v96, v96\n\t"
        "v_exp_f32 v97, v97\n\t"
        "v_smfmac_f32_32x32x32_bf16 v[16:31], v[164:167], v[120:127], v148\n\t"
        "v_exp_f32 v98, v98\n\t"
        "v_exp_f32 v99, v99\n\t"
        "v_exp_f32 v100, v100\n\t"
        "v_smfmac_f32_16x16x64_bf16 v[132:135], v[144:147], v[120:127], v148\n\t"
        "v_exp_f32 v101, v101\n\t"
        "v_exp_f32 v102, v102\n\t"
        "v_mfma_f32_32x32x16_bf16 v[112:127], v[152:155], v[88:91], 0\n\t"
        "v_exp_f32 v103, v103\n\t"
        "v_exp_f32 v104, v104\n\t"
        "v_exp_f32 v105, v105\n\t"
        "v_mfma_f32_32x32x16_bf16 v[112:127], v[156:159], v[92:95], v[112:127]\n\t"
        "ds_read_b32 v151, v224\n\t"
        "v_exp_f32 v106, v106\n\t"
        "v_exp_f32 v107, v107\n\t"
        "v_exp_f32 v108, v108\n\t"
        "v_smfmac_f32_32x32x32_bf16 v[32:47], v[160:163], v[96:103], v148\n\t"
        "v_exp_f32 v109, v109\n\t"
        "v_exp_f32 v110, v110\n\t"
        "v_exp_f32 v111, v111\n\t"
        "v_smfmac_f32_16x16x64_bf16 v[136:139], v[144:147], v[96:103], v148\n\t"
        "s_cmp_lg_u32 s46, 0\n\t"
        "s_cbranch_scc1 27f\n\t"
        "s_waitcnt lgkmcnt(0)\n\t"
        "v_cmp_ne_u32 vcc, v222, v151\n\t"
        "s_cbranch_vccz 26f\n\t"
        "27:\n\t"
        "v_exp_f32 v112, v112\n\t"
        "v_exp_f32 v113, v113\n\t"
        "v_smfmac_f32_32x32x32_bf16 v[32:47], v[164:167], v[104:111], v148\n\t"
        "v_exp_f32 v114, v114\n\t"
        "v_exp_f32 v115, v115\n\t"
        "v_exp_f32 v116, v116\n\t"
        "v_smfmac_f32_16x16x64_bf16 v[136:139], v[144:147], v[104:111], v148\n\t"
        "v_exp_f32 v117, v117\n\t"
        "v_exp_f32 v118, v118\n\t"
        "v_mfma_f32_32x32x16_bf16 v[96:111], v[168:171], v[64:67], 0\n\t"
        "v_exp_f32 v119, v119\n\t"
        "v_exp_f32 v120, v120\n\t"
        "v_exp_f32 v121, v121\n\t"
        "v_mfma_f32_32x32x16_bf16 v[96:111], v[172:175], v[68:71], v[96:111]\n\t"
        "ds_read_b32 v151, v224\n\t"
        "v_exp_f32 v122, v122\n\t"
        "v_exp_f32 v123, v123\n\t"
        "v_exp_f32 v124, v124\n\t"
        "v_smfmac_f32_32x32x32_bf16 v[48:63], v[160:163], v[112:119], v148\n\t"
        "v_exp_f32 v125, v125\n\t"
        "v_exp_f32 v126, v126\n\t"
        "v_exp_f32 v127, v127\n\t"
        "v_smfmac_f32_16x16x64_bf16 v[140:143], v[144:147], v[112:119], v148\n\t"
        "s_cmp_lg_u32 s46, 0\n\t"
        "s_cbranch_scc1 29f\n\t"
        "s_waitcnt lgkmcnt(0)\n\t"
        "v_cmp_ne_u32 vcc, v222, v151\n\t"
        "s_cbranch_vccz 28f\n\t"
        "29:\n\t"
        "v_exp_f32 v96, v96\n\t"
        "v_exp_f32 v97, v97\n\t"
        "v_smfmac_f32_32x32x32_bf16 v[48:63], v[164:167], v[120:127], v148\n\t"
        "v_exp_f32 v98, v98\n\t"
        "v_exp_f32 v99, v99\n\t"
        "v_exp_f32 v100, v100\n\t"
        "v_smfmac_f32_16x16x64_bf16 v[140:143], v[144:147], v[120:127], v148\n\t"
        "v_exp_f32 v101, v101\n\t"
        "v_exp_f32 v102, v102\n\t"
        "v_mfma_f32_32x32x16_bf16 v[112:127], v[168:171], v[72:75], 0\n\t"
        "v_exp_f32 v103, v103\n\t"
        "v_exp_f32 v104, v104\n\t"
        "v_exp_f32 v105, v105\n\t"
        "v_mfma_f32_32x32x16_bf16 v[112:127], v[172:175], v[76:79], v[112:127]\n\t"
        "ds_read_b32 v151, v224\n\t"
        "v_exp_f32 v106, v106\n\t"
        "v_exp_f32 v107, v107\n\t"
        "v_exp_f32 v108, v108\n\t"
        "v_smfmac_f32_32x32x32_bf16 v[0:15], v[176:179], v[96:103], v148\n\t"
        "v_exp_f32 v109, v109\n\t"
        "v_exp_f32 v110, v110\n\t"
        "v_exp_f32 v111, v111\n\t"
        "v_smfmac_f32_16x16x64_bf16 v[128:131], v[144:147], v[96:103], v148\n\t"
        "s_cmp_lg_u32 s46, 0\n\t"
        "s_cbranch_scc1 31f\n\t"
        "s_waitcnt lgkmcnt(0)\n\t"
        "v_cmp_ne_u32 vcc, v222, v151\n\t"
        "s_cbranch_vccz 30f\n\t"
        "31:\n\t"
        "v_exp_f32 v112, v112\n\t"
        "v_exp_f32 v113, v113\n\t"
        "v_smfmac_f32_32x32x32_bf16 v[0:15], v[180:183], v[104:111], v148\n\t"
        "v_exp_f32 v114, v114\n\t"
        "v_exp_f32 v115, v115\n\t"
        "v_exp_f32 v116, v116\n\t"
        "v_smfmac_f32_16x16x64_bf16 v[128:131], v[144:147], v[104:111], v148\n\t"
        "v_exp_f32 v117, v117\n\t"
        "v_exp_f32 v118, v118\n\t"
        "v_mfma_f32_32x32x16_bf16 v[96:111], v[168:171], v[80:83], 0\n\t"
        "v_exp_f32 v119, v119\n\t"
        "v_exp_f32 v120, v120\n\t"
        "v_exp_f32 v121, v121\n\t"
        "v_mfma_f32_32x32x16_bf16 v[96:111], v[172:175], v[84:87], v[96:111]\n\t"
        "ds_read_b32 v151, v224\n\t"
        "v_exp_f32 v122, v122\n\t"
        "v_exp_f32 v123, v123\n\t"
        "v_exp_f32 v124, v124\n\t"
        "v_smfmac_f32_32x32x32_bf16 v[16:31], v[176:179], v[112:119], v148\n\t"
        "v_exp_f32 v125, v125\n\t"
        "v_exp_f32 v126, v126\n\t"
        "v_exp_f32 v127, v127\n\t"
        "v_smfmac_f32_16x16x64_bf16 v[132:135], v[144:147], v[112:119], v148\n\t"
        "s_cmp_lg_u32 s46, 0\n\t"
        "s_cbranch_scc1 33f\n\t"
        "s_waitcnt lgkmcnt(0)\n\t"
        "v_cmp_ne_u32 vcc, v222, v151\n\t"
        "s_cbranch_vccz 32f\n\t"
        "33:\n\t"
        "v_exp_f32 v96, v96\n\t"
        "v_exp_f32 v97, v97\n\t"
        "v_smfmac_f32_32x32x32_bf16 v[16:31], v[180:183], v[120:127], v148\n\t"
        "v_exp_f32 v98, v98\n\t"
        "v_exp_f32 v99, v99\n\t"
        "v_exp_f32 v100, v100\n\t"
        "v_smfmac_f32_16x16x64_bf16 v[132:135], v[144:147], v[120:127], v148\n\t"
        "v_exp_f32 v101, v101\n\t"
        "v_exp_f32 v102, v102\n\t"
        "v_mfma_f32_32x32x16_bf16 v[112:127], v[168:171], v[88:91], 0\n\t"
        "v_exp_f32 v103, v103\n\t"
        "v_exp_f32 v104, v104\n\t"
        "v_exp_f32 v105, v105\n\t"
        "v_mfma_f32_32x32x16_bf16 v[112:127], v[172:175], v[92:95], v[112:127]\n\t"
        "v_exp_f32 v106, v106\n\t"
        "v_exp_f32 v107, v107\n\t"
        "v_exp_f32 v108, v108\n\t"
        "v_smfmac_f32_32x32x32_bf16 v[32:47], v[176:179], v[96:103], v148\n\t"
        "v_exp_f32 v109, v109\n\t"
        "v_exp_f32 v110, v110\n\t"
        "v_exp_f32 v111, v111\n\t"
        "v_smfmac_f32_16x16x64_bf16 v[136:139], v[144:147], v[96:103], v148\n\t"
        "s_cmp_lg_u32 s46, 0\n\t"
        "s_cbranch_scc1 5f\n\t"
        "s_mov_b32 s45, 0x200000\n\t"
        "2:\n\t"
        "ds_read_b32 v151, v224\n\t"
        "s_sub_u32 s45, s45, 1\n\t"
        "s_waitcnt lgkmcnt(0)\n\t"
        "s_cbranch_scc1 3f\n\t"
        "v_cmp_ne_u32 vcc, v222, v151\n\t"
        "s_cbranch_vccnz 2b\n\t"
        "3:\n\t"
        "s_add_u32 s44, s44, 0x4000\n\t"
        "s_and_b32 s44, s44, 0x3ffff\n\t"
        "s_cmp_eq_u32 s44, s49\n\t"
        "s_cselect_b32 s44, s50, s44\n\t"
        "s_add_u32 s52, s40, s44\n\t"
        "s_addc_u32 s53, s41, 0\n\t"
        "buffer_load_dwordx4 v[184:187], v149, s[52:55], 0 offen offset:0 sc1\n\t"
        "buffer_load_dwordx4 v[188:191], v149, s[52:55], 0 offen offset:1024 sc1\n\t"
        "buffer_load_dwordx4 v[192:195], v149, s[52:55], 0 offen offset:2048 sc1\n\t"
        "buffer_load_dwordx4 v[196:199], v149, s[52:55], 0 offen offset:3072 sc1\n\t"
        "buffer_load_dwordx4 v[200:203], v150, s[52:55], 0 offen offset:0 sc1\n\t"
        "buffer_load_dwordx4 v[204:207], v150, s[52:55], 0 offen offset:1024 sc1\n\t"
        "buffer_load_dwordx4 v[208:211], v150, s[52:55], 0 offen offset:2048 sc1\n\t"
        "buffer_load_dwordx4 v[212:215], v150, s[52:55], 0 offen offset:3072 sc1\n\t"
        "5:\n\t"
        "s_add_u32 s44, s44, 0x4000\n\t"
        "s_and_b32 s44, s44, 0x3ffff\n\t"
        "s_cmp_eq_u32 s44, s49\n\t"
        "s_cselect_b32 s44, s50, s44\n\t"
        "s_add_u32 s52, s40, s44\n\t"
        "s_addc_u32 s53, s41, 0\n\t"
        "buffer_load_dwordx4 v[152:155], v149, s[52:55], 0 offen offset:0 sc1\n\t"
        "buffer_load_dwordx4 v[156:159], v149, s[52:55], 0 offen offset:1024 sc1\n\t"
        "buffer_load_dwordx4 v[160:163], v149, s[52:55], 0 offen offset:2048 sc1\n\t"
        "buffer_load_dwordx4 v[164:167], v149, s[52:55], 0 offen offset:3072 sc1\n\t"
        "s_mov_b32 s45, 6\n\t"
        "1:\n\t"
        "v_exp_f32 v112, v112\n\t"
        "v_exp_f32 v113, v113\n\t"
        "v_smfmac_f32_32x32x32_bf16 v[32:47], v[180:183], v[104:111], v148\n\t"
        "v_exp_f32 v114, v114\n\t"
        "v_exp_f32 v115, v115\n\t"
        "v_exp_f32 v116, v116\n\t"
        "v_smfmac_f32_16x16x64_bf16 v[136:139], v[144:147], v[104:111], v148\n\t"
        "s_waitcnt vmcnt(8)\n\t"
        "v_exp_f32 v117, v117\n\t"
        "v_exp_f32 v118, v118\n\t"
        "v_mfma_f32_32x32x16_bf16 v[96:111], v[184:187], v[64:67], 0\n\t"
        "v_exp_f32 v119, v119\n\t"
        "v_exp_f32 v120, v120\n\t"
        "v_exp_f32 v121, v121\n\t"
        "v_mfma_f32_32x32x16_bf16 v[96:111], v[188:191], v[68:71], v[96:111]\n\t"
        "v_exp_f32 v122, v122\n\t"
        "v_exp_f32 v123, v123\n\t"
        "v_exp_f32 v124, v124\n\t"
        "v_smfmac_f32_32x32x32_bf16 v[48:63], v[176:179], v[112:119], v148\n\t"
        "v_exp_f32 v125, v125\n\t"
        "v_exp_f32 v126, v126\n\t"
        "v_exp_f32 v127, v127\n\t"
        "v_smfmac_f32_16x16x64_bf16 v[140:143], v[144:147], v[112:119], v148\n\t"
        "buffer_load_dwordx4 v[168:171], v150, s[52:55], 0 offen offset:0 sc1\n\t"
        "v_exp_f32 v96, v96\n\t"
        "v_exp_f32 v97, v97\n\t"
        "v_smfmac_f32_32x32x32_bf16 v[48:63], v[180:183], v[120:127], v148\n\t"
        "v_exp_f32 v98, v98\n\t"
        "v_exp_f32 v99, v99\n\t"
        "v_exp_f32 v100, v100\n\t"
        "v_smfmac_f32_16x16x64_bf16 v[140:143], v[144:147], v[120:127], v148\n\t"
        "v_exp_f32 v101, v101\n\t"
        "v_exp_f32 v102, v102\n\t"
        "v_mfma_f32_32x32x16_bf16 v[112:127], v[184:187], v[72:75], 0\n\t"
        "v_exp_f32 v103, v103\n\t"
        "v_exp_f32 v104, v104\n\t"
        "v_exp_f32 v105, v105\n\t"
        "v_mfma_f32_32x32x16_bf16 v[112:127], v[188:191], v[76:79], v[112:127]\n\t"
        "v_exp_f32 v106, v106\n\t"
        "v_exp_f32 v107, v107\n\t"
        "v_exp_f32 v108, v108\n\t"
        "v_smfmac_f32_32x32x32_bf16 v[0:15], v[192:195], v[96:103], v148\n\t"
        "v_exp_f32 v109, v109\n\t"
        "v_exp_f32 v110, v110\n\t"
        "v_exp_f32 v111, v111\n\t"
        "v_smfmac_f32_16x16x64_bf16 v[128:131], v[144:147], v[96:103], v148\n\t"
        "buffer_load_dwordx4 v[172:175], v150, s[52:55], 0 offen offset:1024 sc1\n\t"
        "v_exp_f32 v112, v112\n\t"
        "v_exp_f32 v113, v113\n\t"
        "v_smfmac_f32_32x32x32_bf16 v[0:15], v[196:199], v[104:111], v148\n\t"
        "v_exp_f32 v114, v114\n\t"
        "v_exp_f32 v115, v115\n\t"
        "v_exp_f32 v116, v116\n\t"
        "v_smfmac_f32_16x16x64_bf16 v[128:131], v[144:147], v[104:111], v148\n\t"
        "v_exp_f32 v117, v117\n\t"
        "v_exp_f32 v118, v118\n\t"
        "v_mfma_f32_32x32x16_bf16 v[96:111], v[184:187], v[80:83], 0\n\t"
        "v_exp_f32 v119, v119\n\t"
        "v_exp_f32 v120, v120\n\t"
        "v_exp_f32 v121, v121\n\t"
        "v_mfma_f32_32x32x16_bf16 v[96:111], v[188:191], v[84:87], v[96:111]\n\t"
        "v_exp_f32 v122, v122\n\t"
        "v_exp_f32 v123, v123\n\t"
        "v_exp_f32 v124, v124\n\t"
        "v_smfmac_f32_32x32x32_bf16 v[16:31], v[192:195], v[112:119], v148\n\t"
        "v_exp_f32 v125, v125\n\t"
        "v_exp_f32 v126, v126\n\t"
        "v_exp_f32 v127, v127\n\t"
        "v_smfmac_f32_16x16x64_bf16 v[132:135], v[144:147], v[112:119], v148\n\t"
        "buffer_load_dwordx4 v[176:179], v150, s[52:55], 0 offen offset:2048 sc1\n\t"
        "v_exp_f32 v96, v96\n\t"
        "v_exp_f32 v97, v97\n\t"
        "v_smfmac_f32_32x32x32_bf16 v[16:31], v[196:199], v[120:127], v148\n\t"
        "v_exp_f32 v98, v98\n\t"
        "v_exp_f32 v99, v99\n\t"
        "v_exp_f32 v100, v100\n\t"
        "v_smfmac_f32_16x16x64_bf16 v[132:135], v[144:147], v[120:127], v148\n\t"
        "v_exp_f32 v101, v101\n\t"
        "v_exp_f32 v102, v102\n\t"
        "v_mfma_f32_32x32x16_bf16 v[112:127], v[184:187], v[88:91], 0\n\t"
        "v_exp_f32 v103, v103\n\t"
        "v_exp_f32 v104, v104\n\t"
        "v_exp_f32 v105, v105\n\t"
        "v_mfma_f32_32x32x16_bf16 v[112:127], v[188:191], v[92:95], v[112:127]\n\t"
        "v_exp_f32 v106, v106\n\t"
        "v_exp_f32 v107, v107\n\t"
        "v_exp_f32 v108, v108\n\t"
        "v_smfmac_f32_32x32x32_bf16 v[32:47], v[192:195], v[96:103], v148\n\t"
        "v_exp_f32 v109, v109\n\t"
        "v_exp_f32 v110, v110\n\t"
        "v_exp_f32 v111, v111\n\t"
        "v_smfmac_f32_16x16x64_bf16 v[136:139], v[144:147], v[96:103], v148\n\t"
        "buffer_load_dwordx4 v[180:183], v150, s[52:55], 0 offen offset:3072 sc1\n\t"
        "v_exp_f32 v112, v112\n\t"
        "v_exp_f32 v113, v113\n\t"
        "v_smfmac_f32_32x32x32_bf16 v[32:47], v[196:199], v[104:111], v148\n\t"
        "s_add_u32 s44, s44, 0x4000\n\t"
        "s_and_b32 s44, s44, 0x3ffff\n\t"
        "v_exp_f32 v114, v114\n\t"
        "v_exp_f32 v115, v115\n\t"
        "v_exp_f32 v116, v116\n\t"
        "v_smfmac_f32_16x16x64_bf16 v[136:139], v[144:147], v[104:111], v148\n\t"
        "s_waitcnt vmcnt(8)\n\t"
        "v_exp_f32 v117, v117\n\t"
        "v_exp_f32 v118, v118\n\t"
        "v_mfma_f32_32x32x16_bf16 v[96:111], v[200:203], v[64:67], 0\n\t"
        "s_cmp_eq_u32 s44, s49\n\t"
        "v_exp_f32 v119, v119\n\t"
        "v_exp_f32 v120, v120\n\t"
        "v_exp_f32 v121, v121\n\t"
        "v_mfma_f32_32x32x16_bf16 v[96:111], v[204:207], v[68:71], v[96:111]\n\t"
        "s_cselect_b32 s44, s50, s44\n\t"
        "v_exp_f32 v122, v122\n\t"
        "v_exp_f32 v123, v123\n\t"
        "v_exp_f32 v124, v124\n\t"
        "v_smfmac_f32_32x32x32_bf16 v[48:63], v[192:195], v[112:119], v148\n\t"
        "s_add_u32 s52, s40, s44\n\t"
        "s_addc_u32 s53, s41, 0\n\t"
        "v_exp_f32 v125, v125\n\t"
        "v_exp_f32 v126, v126\n\t"
        "v_exp_f32 v127, v127\n\t"
        "v_smfmac_f32_16x16x64_bf16 v[140:143], v[144:147], v[112:119], v148\n\t"
        "buffer_load_dwordx4 v[184:187], v149, s[52:55], 0 offen offset:0 sc1\n\t"
        "v_exp_f32 v96, v96\n\t"
        "v_exp_f32 v97, v97\n\t"
        "v_smfmac_f32_32x32x32_bf16 v[48:63], v[196:199], v[120:127], v148\n\t"
        "v_exp_f32 v98, v98\n\t"
        "v_exp_f32 v99, v99\n\t"
        "v_exp_f32 v100, v100\n\t"
        "v_smfmac_f32_16x16x64_bf16 v[140:143], v[144:147], v[120:127], v148\n\t"
        "v_exp_f32 v101, v101\n\t"
        "v_exp_f32 v102, v102\n\t"
        "v_mfma_f32_32x32x16_bf16 v[112:127], v[200:203], v[72:75], 0\n\t"
        "v_exp_f32 v103, v103\n\t"
        "v_exp_f32 v104, v104\n\t"
        "v_exp_f32 v105, v105\n\t"
        "v_mfma_f32_32x32x16_bf16 v[112:127], v[204:207], v[76:79], v[112:127]\n\t"
        "v_exp_f32 v106, v106\n\t"
        "v_exp_f32 v107, v107\n\t"
        "v_exp_f32 v108, v108\n\t"
        "v_smfmac_f32_32x32x32_bf16 v[0:15], v[208:211], v[96:103], v148\n\t"
        "v_exp_f32 v109, v109\n\t"
        "v_exp_f32 v110, v110\n\t"
        "v_exp_f32 v111, v111\n\t"
        "v_smfmac_f32_16x16x64_bf16 v[128:131], v[144:147], v[96:103], v148\n\t"
        "buffer_load_dwordx4 v[188:191], v149, s[52:55], 0 offen offset:1024 sc1\n\t"
        "v_exp_f32 v112, v112\n\t"
        "v_exp_f32 v113, v113\n\t"
        "v_smfmac_f32_32x32x32_bf16 v[0:15], v[212:215], v[104:111], v148\n\t"
        "v_exp_f32 v114, v114\n\t"
        "v_exp_f32 v115, v115\n\t"
        "v_exp_f32 v116, v116\n\t"
        "v_smfmac_f32_16x16x64_bf16 v[128:131], v[144:147], v[104:111], v148\n\t"
        "v_exp_f32 v117, v117\n\t"
        "v_exp_f32 v118, v118\n\t"
        "v_mfma_f32_32x32x16_bf16 v[96:111], v[200:203], v[80:83], 0\n\t"
        "v_exp_f32 v119, v119\n\t"
        "v_exp_f32 v120, v120\n\t"
        "v_exp_f32 v121, v121\n\t"
        "v_mfma_f32_32x32x16_bf16 v[96:111], v[204:207], v[84:87], v[96:111]\n\t"
        "v_exp_f32 v122, v122\n\t"
        "v_exp_f32 v123, v123\n\t"
        "v_exp_f32 v124, v124\n\t"
        "v_smfmac_f32_32x32x32_bf16 v[16:31], v[208:211], v[112:119], v148\n\t"
        "v_exp_f32 v125, v125\n\t"
        "v_exp_f32 v126, v126\n\t"
        "v_exp_f32 v127, v127\n\t"
        "v_smfmac_f32_16x16x64_bf16 v[132:135], v[144:147], v[112:119], v148\n\t"
        "buffer_load_dwordx4 v[192:195], v149, s[52:55], 0 offen offset:2048 sc1\n\t"
        "v_exp_f32 v96, v96\n\t"
        "v_exp_f32 v97, v97\n\t"
        "v_smfmac_f32_32x32x32_bf16 v[16:31], v[212:215], v[120:127], v148\n\t"
        "v_exp_f32 v98, v98\n\t"
        "v_exp_f32 v99, v99\n\t"
        "v_exp_f32 v100, v100\n\t"
        "v_smfmac_f32_16x16x64_bf16 v[132:135], v[144:147], v[120:127], v148\n\t"
        "v_exp_f32 v101, v101\n\t"
        "v_exp_f32 v102, v102\n\t"
        "v_mfma_f32_32x32x16_bf16 v[112:127], v[200:203], v[88:91], 0\n\t"
        "v_exp_f32 v103, v103\n\t"
        "v_exp_f32 v104, v104\n\t"
        "v_exp_f32 v105, v105\n\t"
        "v_mfma_f32_32x32x16_bf16 v[112:127], v[204:207], v[92:95], v[112:127]\n\t"
        "v_exp_f32 v106, v106\n\t"
        "v_exp_f32 v107, v107\n\t"
        "v_exp_f32 v108, v108\n\t"
        "v_smfmac_f32_32x32x32_bf16 v[32:47], v[208:211], v[96:103], v148\n\t"
        "v_exp_f32 v109, v109\n\t"
        "v_exp_f32 v110, v110\n\t"
        "v_exp_f32 v111, v111\n\t"
        "v_smfmac_f32_16x16x64_bf16 v[136:139], v[144:147], v[96:103], v148\n\t"
        "buffer_load_dwordx4 v[196:199], v149, s[52:55], 0 offen offset:3072 sc1\n\t"
        "v_exp_f32 v112, v112\n\t"
        "v_exp_f32 v113, v113\n\t"
        "v_smfmac_f32_32x32x32_bf16 v[32:47], v[212:215], v[104:111], v148\n\t"
        "v_exp_f32 v114, v114\n\t"
        "v_exp_f32 v115, v115\n\t"
        "v_exp_f32 v116, v116\n\t"
        "v_smfmac_f32_16x16x64_bf16 v[136:139], v[144:147], v[104:111], v148\n\t"
        "s_waitcnt vmcnt(8)\n\t"
        "v_exp_f32 v117, v117\n\t"
        "v_exp_f32 v118, v118\n\t"
        "v_mfma_f32_32x32x16_bf16 v[96:111], v[152:155], v[64:67], 0\n\t"
        "v_exp_f32 v119, v119\n\t"
        "v_exp_f32 v120, v120\n\t"
        "v_exp_f32 v121, v121\n\t"
        "v_mfma_f32_32x32x16_bf16 v[96:111], v[156:159], v[68:71], v[96:111]\n\t"
        "v_exp_f32 v122, v122\n\t"
        "v_exp_f32 v123, v123\n\t"
        "v_exp_f32 v124, v124\n\t"
        "v_smfmac_f32_32x32x32_bf16 v[48:63], v[208:211], v[112:119], v148\n\t"
        "v_exp_f32 v125, v125\n\t"
        "v_exp_f32 v126, v126\n\t"
        "v_exp_f32 v127, v127\n\t"
        "v_smfmac_f32_16x16x64_bf16 v[140:143], v[144:147], v[112:119], v148\n\t"
        "buffer_load_dwordx4 v[200:203], v150, s[52:55], 0 offen offset:0 sc1\n\t"
        "v_exp_f32 v96, v96\n\t"
        "v_exp_f32 v97, v97\n\t"
        "v_smfmac_f32_32x32x32_bf16 v[48:63], v[212:215], v[120:127], v148\n\t"
        "v_exp_f32 v98, v98\n\t"
        "v_exp_f32 v99, v99\n\t"
        "v_exp_f32 v100, v100\n\t"
        "v_smfmac_f32_16x16x64_bf16 v[140:143], v[144:147], v[120:127], v148\n\t"
        "v_exp_f32 v101, v101\n\t"
        "v_exp_f32 v102, v102\n\t"
        "v_mfma_f32_32x32x16_bf16 v[112:127], v[152:155], v[72:75], 0\n\t"
        "v_exp_f32 v103, v103\n\t"
        "v_exp_f32 v104, v104\n\t"
        "v_exp_f32 v105, v105\n\t"
        "v_mfma_f32_32x32x16_bf16 v[112:127], v[156:159], v[76:79], v[112:127]\n\t"
        "v_exp_f32 v106, v106\n\t"
        "v_exp_f32 v107, v107\n\t"
        "v_exp_f32 v108, v108\n\t"
        "v_smfmac_f32_32x32x32_bf16 v[0:15], v[160:163], v[96:103], v148\n\t"
        "v_exp_f32 v109, v109\n\t"
        "v_exp_f32 v110, v110\n\t"
        "v_exp_f32 v111, v111\n\t"
        "v_smfmac_f32_16x16x64_bf16 v[128:131], v[144:147], v[96:103], v148\n\t"
        "buffer_load_dwordx4 v[204:207], v150, s[52:55], 0 offen offset:1024 sc1\n\t"
        "v_exp_f32 v112, v112\n\t"
        "v_exp_f32 v113, v113\n\t"
        "v_smfmac_f32_32x32x32_bf16 v[0:15], v[164:167], v[104:111], v148\n\t"
        "v_exp_f32 v114, v114\n\t"
        "v_exp_f32 v115, v115\n\t"
        "v_exp_f32 v116, v116\n\t"
        "v_smfmac_f32_16x16x64_bf16 v[128:131], v[144:147], v[104:111], v148\n\t"
        "v_exp_f32 v117, v117\n\t"
        "v_exp_f32 v118, v118\n\t"
        "v_mfma_f32_32x32x16_bf16 v[96:111], v[152:155], v[80:83], 0\n\t"
        "v_exp_f32 v119, v119\n\t"
        "v_exp_f32 v120, v120\n\t"
        "v_exp_f32 v121, v121\n\t"
        "v_mfma_f32_32x32x16_bf16 v[96:111], v[156:159], v[84:87], v[96:111]\n\t"
        "v_exp_f32 v122, v122\n\t"
        "v_exp_f32 v123, v123\n\t"
        "v_exp_f32 v124, v124\n\t"
        "v_smfmac_f32_32x32x32_bf16 v[16:31], v[160:163], v[112:119], v148\n\t"
        "v_exp_f32 v125, v125\n\t"
        "v_exp_f32 v126, v126\n\t"
        "v_exp_f32 v127, v127\n\t"
        "v_smfmac_f32_16x16x64_bf16 v[132:135], v[144:147], v[112:119], v148\n\t"
        "buffer_load_dwordx4 v[208:211], v150, s[52:55], 0 offen offset:2048 sc1\n\t"
        "v_exp_f32 v96, v96\n\t"
        "v_exp_f32 v97, v97\n\t"
        "v_smfmac_f32_32x32x32_bf16 v[16:31], v[164:167], v[120:127], v148\n\t"
        "v_exp_f32 v98, v98\n\t"
        "v_exp_f32 v99, v99\n\t"
        "v_exp_f32 v100, v100\n\t"
        "v_smfmac_f32_16x16x64_bf16 v[132:135], v[144:147], v[120:127], v148\n\t"
        "v_exp_f32 v101, v101\n\t"
        "v_exp_f32 v102, v102\n\t"
        "v_mfma_f32_32x32x16_bf16 v[112:127], v[152:155], v[88:91], 0\n\t"
        "v_exp_f32 v103, v103\n\t"
        "v_exp_f32 v104, v104\n\t"
        "v_exp_f32 v105, v105\n\t"
        "v_mfma_f32_32x32x16_bf16 v[112:127], v[156:159], v[92:95], v[112:127]\n\t"
        "v_exp_f32 v106, v106\n\t"
        "v_exp_f32 v107, v107\n\t"
        "v_exp_f32 v108, v108\n\t"
        "v_smfmac_f32_32x32x32_bf16 v[32:47], v[160:163], v[96:103], v148\n\t"
        "v_exp_f32 v109, v109\n\t"
        "v_exp_f32 v110, v110\n\t"
        "v_exp_f32 v111, v111\n\t"
        "v_smfmac_f32_16x16x64_bf16 v[136:139], v[144:147], v[96:103], v148\n\t"
        "buffer_load_dwordx4 v[212:215], v150, s[52:55], 0 offen offset:3072 sc1\n\t"
        "v_exp_f32 v112, v112\n\t"
        "v_exp_f32 v113, v113\n\t"
        "v_smfmac_f32_32x32x32_bf16 v[32:47], v[164:167], v[104:111], v148\n\t"
        "s_add_u32 s44, s44, 0x4000\n\t"
        "s_and_b32 s44, s44, 0x3ffff\n\t"
        "v_exp_f32 v114, v114\n\t"
        "v_exp_f32 v115, v115\n\t"
        "v_exp_f32 v116, v116\n\t"
        "v_smfmac_f32_16x16x64_bf16 v[136:139], v[144:147], v[104:111], v148\n\t"
        "s_waitcnt vmcnt(8)\n\t"
        "v_exp_f32 v117, v117\n\t"
        "v_exp_f32 v118, v118\n\t"
        "v_mfma_f32_32x32x16_bf16 v[96:111], v[168:171], v[64:67], 0\n\t"
        "s_cmp_eq_u32 s44, s49\n\t"
        "v_exp_f32 v119, v119\n\t"
        "v_exp_f32 v120, v120\n\t"
        "v_exp_f32 v121, v121\n\t"
        "v_mfma_f32_32x32x16_bf16 v[96:111], v[172:175], v[68:71], v[96:111]\n\t"
        "s_cselect_b32 s44, s50, s44\n\t"
        "v_exp_f32 v122, v122\n\t"
        "v_exp_f32 v123, v123\n\t"
        "v_exp_f32 v124, v124\n\t"
        "v_smfmac_f32_32x32x32_bf16 v[48:63], v[160:163], v[112:119], v148\n\t"
        "s_add_u32 s52, s40, s44\n\t"
        "s_addc_u32 s53, s41, 0\n\t"
        "v_exp_f32 v125, v125\n\t"
        "v_exp_f32 v126, v126\n\t"
        "v_exp_f32 v127, v127\n\t"
        "v_smfmac_f32_16x16x64_bf16 v[140:143], v[144:147], v[112:119], v148\n\t"
        "buffer_load_dwordx4 v[152:155], v149, s[52:55], 0 offen offset:0 sc1\n\t"
        "v_exp_f32 v96, v96\n\t"
        "v_exp_f32 v97, v97\n\t"
        "v_smfmac_f32_32x32x32_bf16 v[48:63], v[164:167], v[120:127], v148\n\t"
        "v_exp_f32 v98, v98\n\t"
        "v_exp_f32 v99, v99\n\t"
        "v_exp_f32 v100, v100\n\t"
        "v_smfmac_f32_16x16x64_bf16 v[140:143], v[144:147], v[120:127], v148\n\t"
        "v_exp_f32 v101, v101\n\t"
        "v_exp_f32 v102, v102\n\t"
        "v_mfma_f32_32x32x16_bf16 v[112:127], v[168:171], v[72:75], 0\n\t"
        "v_exp_f32 v103, v103\n\t"
        "v_exp_f32 v104, v104\n\t"
        "v_exp_f32 v105, v105\n\t"
        "v_mfma_f32_32x32x16_bf16 v[112:127], v[172:175], v[76:79], v[112:127]\n\t"
        "v_exp_f32 v106, v106\n\t"
        "v_exp_f32 v107, v107\n\t"
        "v_exp_f32 v108, v108\n\t"
        "v_smfmac_f32_32x32x32_bf16 v[0:15], v[176:179], v[96:103], v148\n\t"
        "v_exp_f32 v109, v109\n\t"
        "v_exp_f32 v110, v110\n\t"
        "v_exp_f32 v111, v111\n\t"
        "v_smfmac_f32_16x16x64_bf16 v[128:131], v[144:147], v[96:103], v148\n\t"
        "buffer_load_dwordx4 v[156:159], v149, s[52:55], 0 offen offset:1024 sc1\n\t"
        "v_exp_f32 v112, v112\n\t"
        "v_exp_f32 v113, v113\n\t"
        "v_smfmac_f32_32x32x32_bf16 v[0:15], v[180:183], v[104:111], v148\n\t"
        "v_exp_f32 v114, v114\n\t"
        "v_exp_f32 v115, v115\n\t"
        "v_exp_f32 v116, v116\n\t"
        "v_smfmac_f32_16x16x64_bf16 v[128:131], v[144:147], v[104:111], v148\n\t"
        "v_exp_f32 v117, v117\n\t"
        "v_exp_f32 v118, v118\n\t"
        "v_mfma_f32_32x32x16_bf16 v[96:111], v[168:171], v[80:83], 0\n\t"
        "v_exp_f32 v119, v119\n\t"
        "v_exp_f32 v120, v120\n\t"
        "v_exp_f32 v121, v121\n\t"
        "v_mfma_f32_32x32x16_bf16 v[96:111], v[172:175], v[84:87], v[96:111]\n\t"
        "v_exp_f32 v122, v122\n\t"
        "v_exp_f32 v123, v123\n\t"
        "v_exp_f32 v124, v124\n\t"
        "v_smfmac_f32_32x32x32_bf16 v[16:31], v[176:179], v[112:119], v148\n\t"
        "v_exp_f32 v125, v125\n\t"
        "v_exp_f32 v126, v126\n\t"
        "v_exp_f32 v127, v127\n\t"
        "v_smfmac_f32_16x16x64_bf16 v[132:135], v[144:147], v[112:119], v148\n\t"
        "buffer_load_dwordx4 v[160:163], v149, s[52:55], 0 offen offset:2048 sc1\n\t"
        "v_exp_f32 v96, v96\n\t"
        "v_exp_f32 v97, v97\n\t"
        "v_smfmac_f32_32x32x32_bf16 v[16:31], v[180:183], v[120:127], v148\n\t"
        "v_exp_f32 v98, v98\n\t"
        "v_exp_f32 v99, v99\n\t"
        "v_exp_f32 v100, v100\n\t"
        "v_smfmac_f32_16x16x64_bf16 v[132:135], v[144:147], v[120:127], v148\n\t"
        "v_exp_f32 v101, v101\n\t"
        "v_exp_f32 v102, v102\n\t"
        "v_mfma_f32_32x32x16_bf16 v[112:127], v[168:171], v[88:91], 0\n\t"
        "v_exp_f32 v103, v103\n\t"
        "v_exp_f32 v104, v104\n\t"
        "v_exp_f32 v105, v105\n\t"
        "v_mfma_f32_32x32x16_bf16 v[112:127], v[172:175], v[92:95], v[112:127]\n\t"
        "v_exp_f32 v106, v106\n\t"
        "v_exp_f32 v107, v107\n\t"
        "v_exp_f32 v108, v108\n\t"
        "v_smfmac_f32_32x32x32_bf16 v[32:47], v[176:179], v[96:103], v148\n\t"
        "s_sub_u32 s45, s45, 1\n\t"
        "v_exp_f32 v109, v109\n\t"
        "v_exp_f32 v110, v110\n\t"
        "v_exp_f32 v111, v111\n\t"
        "v_smfmac_f32_16x16x64_bf16 v[136:139], v[144:147], v[96:103], v148\n\t"
        "buffer_load_dwordx4 v[164:167], v149, s[52:55], 0 offen offset:3072 sc1\n\t"
        "s_cbranch_scc0 1b\n\t"
        "v_exp_f32 v112, v112\n\t"
        "v_exp_f32 v113, v113\n\t"
        "v_smfmac_f32_32x32x32_bf16 v[32:47], v[180:183], v[104:111], v148\n\t"
        "v_exp_f32 v114, v114\n\t"
        "v_exp_f32 v115, v115\n\t"
        "v_exp_f32 v116, v116\n\t"
        "v_smfmac_f32_16x16x64_bf16 v[136:139], v[144:147], v[104:111], v148\n\t"
        "s_waitcnt vmcnt(8)\n\t"
        "v_exp_f32 v117, v117\n\t"
        "v_exp_f32 v118, v118\n\t"
        "v_mfma_f32_32x32x16_bf16 v[96:111], v[184:187], v[64:67], 0\n\t"
        "v_exp_f32 v119, v119\n\t"
        "v_exp_f32 v120, v120\n\t"
        "v_exp_f32 v121, v121\n\t"
        "v_mfma_f32_32x32x16_bf16 v[96:111], v[188:191], v[68:71], v[96:111]\n\t"
        "v_exp_f32 v122, v122\n\t"
        "v_exp_f32 v123, v123\n\t"
        "v_exp_f32 v124, v124\n\t"
        "v_smfmac_f32_32x32x32_bf16 v[48:63], v[176:179], v[112:119], v148\n\t"
        "v_exp_f32 v125, v125\n\t"
        "v_exp_f32 v126, v126\n\t"
        "v_exp_f32 v127, v127\n\t"
        "v_smfmac_f32_16x16x64_bf16 v[140:143], v[144:147], v[112:119], v148\n\t"
        "v_exp_f32 v96, v96\n\t"
        "v_exp_f32 v97, v97\n\t"
        "v_smfmac_f32_32x32x32_bf16 v[48:63], v[180:183], v[120:127], v148\n\t"
        "v_exp_f32 v98, v98\n\t"
        "v_exp_f32 v99, v99\n\t"
        "v_exp_f32 v100, v100\n\t"
        "v_smfmac_f32_16x16x64_bf16 v[140:143], v[144:147], v[120:127], v148\n\t"
        "v_exp_f32 v101, v101\n\t"
        "v_exp_f32 v102, v102\n\t"
        "v_mfma_f32_32x32x16_bf16 v[112:127], v[184:187], v[72:75], 0\n\t"
        "v_exp_f32 v103, v103\n\t"
        "v_exp_f32 v104, v104\n\t"
        "v_exp_f32 v105, v105\n\t"
        "v_mfma_f32_32x32x16_bf16 v[112:127], v[188:191], v[76:79], v[112:127]\n\t"
        "v_exp_f32 v106, v106\n\t"
        "v_exp_f32 v107, v107\n\t"
        "v_exp_f32 v108, v108\n\t"
        "v_smfmac_f32_32x32x32_bf16 v[0:15], v[192:195], v[96:103], v148\n\t"
        "v_exp_f32 v109, v109\n\t"
        "v_exp_f32 v110, v110\n\t"
        "v_exp_f32 v111, v111\n\t"
        "v_smfmac_f32_16x16x64_bf16 v[128:131], v[144:147], v[96:103], v148\n\t"
        "v_exp_f32 v112, v112\n\t"
        "v_exp_f32 v113, v113\n\t"
        "v_smfmac_f32_32x32x32_bf16 v[0:15], v[196:199], v[104:111], v148\n\t"
        "v_exp_f32 v114, v114\n\t"
        "v_exp_f32 v115, v115\n\t"
        "v_exp_f32 v116, v116\n\t"
        "v_smfmac_f32_16x16x64_bf16 v[128:131], v[144:147], v[104:111], v148\n\t"
        "s_waitcnt vmcnt(4)\n\t"
        "v_exp_f32 v117, v117\n\t"
        "v_exp_f32 v118, v118\n\t"
        "v_mfma_f32_32x32x16_bf16 v[96:111], v[200:203], v[64:67], 0\n\t"
        "v_exp_f32 v119, v119\n\t"
        "v_exp_f32 v120, v120\n\t"
        "v_exp_f32 v121, v121\n\t"
        "v_mfma_f32_32x32x16_bf16 v[96:111], v[204:207], v[68:71], v[96:111]\n\t"
        "v_exp_f32 v122, v122\n\t"
        "v_exp_f32 v123, v123\n\t"
        "v_exp_f32 v124, v124\n\t"
        "v_smfmac_f32_32x32x32_bf16 v[16:31], v[192:195], v[112:119], v148\n\t"
        "v_exp_f32 v125, v125\n\t"
        "v_exp_f32 v126, v126\n\t"
        "v_exp_f32 v127, v127\n\t"
        "v_smfmac_f32_16x16x64_bf16 v[132:135], v[144:147], v[112:119], v148\n\t"
        "s_cmp_lg_u32 s47, 0\n\t"
        "s_cbranch_scc1 4f\n\t"
        "global_load_dwordx4 %[r0], v223, %[xb] offset:0\n\t"
        "global_load_dwordx4 %[r1], v223, %[xb] offset:32\n\t"
        "global_load_dwordx4 %[r2], v223, %[xb] offset:64\n\t"
        "global_load_dwordx4 %[r3], v223, %[xb] offset:96\n\t"
        "4:\n\t"
        "v_exp_f32 v96, v96\n\t"
        "v_exp_f32 v97, v97\n\t"
        "v_smfmac_f32_32x32x32_bf16 v[16:31], v[196:199], v[120:127], v148\n\t"
        "v_exp_f32 v98, v98\n\t"
        "v_exp_f32 v99, v99\n\t"
        "v_exp_f32 v100, v100\n\t"
        "v_smfmac_f32_16x16x64_bf16 v[132:135], v[144:147], v[120:127], v148\n\t"
        "v_exp_f32 v101, v101\n\t"
        "v_exp_f32 v102, v102\n\t"
        "v_mfma_f32_32x32x16_bf16 v[112:127], v[200:203], v[72:75], 0\n\t"
        "v_exp_f32 v103, v103\n\t"
        "v_exp_f32 v104, v104\n\t"
        "v_exp_f32 v105, v105\n\t"
        "v_mfma_f32_32x32x16_bf16 v[112:127], v[204:207], v[76:79], v[112:127]\n\t"
        "v_exp_f32 v106, v106\n\t"
        "v_exp_f32 v107, v107\n\t"
        "v_exp_f32 v108, v108\n\t"
        "v_smfmac_f32_32x32x32_bf16 v[0:15], v[208:211], v[96:103], v148\n\t"
        "v_exp_f32 v109, v109\n\t"
        "v_exp_f32 v110, v110\n\t"
        "v_exp_f32 v111, v111\n\t"
        "v_smfmac_f32_16x16x64_bf16 v[128:131], v[144:147], v[96:103], v148\n\t"
        "v_exp_f32 v112, v112\n\t"
        "v_exp_f32 v113, v113\n\t"
        "v_smfmac_f32_32x32x32_bf16 v[0:15], v[212:215], v[104:111], v148\n\t"
        "v_exp_f32 v114, v114\n\t"
        "v_exp_f32 v115, v115\n\t"
        "v_exp_f32 v116, v116\n\t"
        "v_smfmac_f32_16x16x64_bf16 v[128:131], v[144:147], v[104:111], v148\n\t"
        "v_exp_f32 v117, v117\n\t"
        "v_exp_f32 v118, v118\n\t"
        "v_mfma_f32_32x32x16_bf16 v[96:111], v[184:187], v[80:83], 0\n\t"
        "v_exp_f32 v119, v119\n\t"
        "v_exp_f32 v120, v120\n\t"
        "v_exp_f32 v121, v121\n\t"
        "v_mfma_f32_32x32x16_bf16 v[96:111], v[188:191], v[84:87], v[96:111]\n\t"
        "ds_write_b128 v216, v[0:3] offset:0\n\t"
        "ds_write_b128 v216, v[4:7] offset:1024\n\t"
        "v_exp_f32 v122, v122\n\t"
        "v_exp_f32 v123, v123\n\t"
        "v_exp_f32 v124, v124\n\t"
        "v_smfmac_f32_32x32x32_bf16 v[16:31], v[208:211], v[112:119], v148\n\t"
        "ds_write_b128 v216, v[8:11] offset:2048\n\t"
        "ds_write_b128 v216, v[12:15] offset:3072\n\t"
        "v_exp_f32 v125, v125\n\t"
        "v_exp_f32 v126, v126\n\t"
        "v_exp_f32 v127, v127\n\t"
        "v_smfmac_f32_16x16x64_bf16 v[132:135], v[144:147], v[112:119], v148\n\t"
        "ds_write_b32 v218, v128 offset:0\n\t"
        "v_exp_f32 v96, v96\n\t"
        "v_exp_f32 v97, v97\n\t"
        "v_smfmac_f32_32x32x32_bf16 v[16:31], v[212:215], v[120:127], v148\n\t"
        "v_exp_f32 v98, v98\n\t"
        "v_exp_f32 v99, v99\n\t"
        "v_exp_f32 v100, v100\n\t"
        "v_smfmac_f32_16x16x64_bf16 v[132:135], v[144:147], v[120:127], v148\n\t"
        "v_exp_f32 v101, v101\n\t"
        "v_exp_f32 v102, v102\n\t"
        "v_mfma_f32_32x32x16_bf16 v[112:127], v[184:187], v[88:91], 0\n\t"
        "v_exp_f32 v103, v103\n\t"
        "v_exp_f32 v104, v104\n\t"
        "v_exp_f32 v105, v105\n\t"
        "v_mfma_f32_32x32x16_bf16 v[112:127], v[188:191], v[92:95], v[112:127]\n\t"
        "ds_write_b128 v216, v[16:19] offset:4096\n\t"
        "ds_write_b128 v216, v[20:23] offset:5120\n\t"
        "v_exp_f32 v106, v106\n\t"
        "v_exp_f32 v107, v107\n\t"
        "v_exp_f32 v108, v108\n\t"
        "v_smfmac_f32_32x32x32_bf16 v[32:47], v[192:195], v[96:103], v148\n\t"
        "ds_write_b128 v216, v[24:27] offset:6144\n\t"
        "ds_write_b128 v216, v[28:31] offset:7168\n\t"
        "v_exp_f32 v109, v109\n\t"
        "v_exp_f32 v110, v110\n\t"
        "v_exp_f32 v111, v111\n\t"
        "v_smfmac_f32_16x16x64_bf16 v[136:139], v[144:147], v[96:103], v148\n\t"
        "ds_write_b32 v218, v132 offset:256\n\t"
        "ds_write_b32 v220, v222\n\t"
        "v_exp_f32 v112, v112\n\t"
        "v_exp_f32 v113, v113\n\t"
        "v_smfmac_f32_32x32x32_bf16 v[32:47], v[196:199], v[104:111], v148\n\t"
        "v_exp_f32 v114, v114\n\t"
        "v_exp_f32 v115, v115\n\t"
        "v_exp_f32 v116, v116\n\t"
        "v_smfmac_f32_16x16x64_bf16 v[136:139], v[144:147], v[104:111], v148\n\t"
        "v_exp_f32 v117, v117\n\t"
        "v_exp_f32 v118, v118\n\t"
        "v_mfma_f32_32x32x16_bf16 v[96:111], v[200:203], v[80:83], 0\n\t"
        "v_exp_f32 v119, v119\n\t"
        "v_exp_f32 v120, v120\n\t"
        "v_exp_f32 v121, v121\n\t"
        "v_mfma_f32_32x32x16_bf16 v[96:111], v[204:207], v[84:87], v[96:111]\n\t"
        "v_exp_f32 v122, v122\n\t"
        "v_exp_f32 v123, v123\n\t"
        "v_exp_f32 v124, v124\n\t"
        "v_smfmac_f32_32x32x32_bf16 v[48:63], v[192:195], v[112:119], v148\n\t"
        "v_exp_f32 v125, v125\n\t"
        "v_exp_f32 v126, v126\n\t"
        "v_exp_f32 v127, v127\n\t"
        "v_smfmac_f32_16x16x64_bf16 v[140:143], v[144:147], v[112:119], v148\n\t"
        "v_exp_f32 v96, v96\n\t"
        "v_exp_f32 v97, v97\n\t"
        "v_smfmac_f32_32x32x32_bf16 v[48:63], v[196:199], v[120:127], v148\n\t"
        "v_exp_f32 v98, v98\n\t"
        "v_exp_f32 v99, v99\n\t"
        "v_exp_f32 v100, v100\n\t"
        "v_smfmac_f32_16x16x64_bf16 v[140:143], v[144:147], v[120:127], v148\n\t"
        "v_exp_f32 v101, v101\n\t"
        "v_exp_f32 v102, v102\n\t"
        "v_mfma_f32_32x32x16_bf16 v[112:127], v[200:203], v[88:91], 0\n\t"
        "v_exp_f32 v103, v103\n\t"
        "v_exp_f32 v104, v104\n\t"
        "v_exp_f32 v105, v105\n\t"
        "v_mfma_f32_32x32x16_bf16 v[112:127], v[204:207], v[92:95], v[112:127]\n\t"
        "ds_read_b32 v151, v221\n\t"
        "v_exp_f32 v106, v106\n\t"
        "v_exp_f32 v107, v107\n\t"
        "v_exp_f32 v108, v108\n\t"
        "v_smfmac_f32_32x32x32_bf16 v[32:47], v[208:211], v[96:103], v148\n\t"
        "v_exp_f32 v109, v109\n\t"
        "v_exp_f32 v110, v110\n\t"
        "v_exp_f32 v111, v111\n\t"
        "v_smfmac_f32_16x16x64_bf16 v[136:139], v[144:147], v[96:103], v148\n\t"
        "s_waitcnt lgkmcnt(0)\n\t"
        "v_cmp_ne_u32 vcc, v222, v151\n\t"
        "s_cbranch_vccnz 40f\n\t"
        "41:\n\t"
        "v_exp_f32 v112, v112\n\t"
        "v_exp_f32 v113, v113\n\t"
        "v_smfmac_f32_32x32x32_bf16 v[32:47], v[212:215], v[104:111], v148\n\t"
        "ds_read_b128 v[0:3], v217 offset:0\n\t"
        "ds_read_b128 v[4:7], v217 offset:1024\n\t"
        "ds_read_b128 v[8:11], v217 offset:2048\n\t"
        "ds_read_b128 v[12:15], v217 offset:3072\n\t"
        "v_exp_f32 v114, v114\n\t"
        "v_exp_f32 v115, v115\n\t"
        "v_exp_f32 v116, v116\n\t"
        "v_smfmac_f32_16x16x64_bf16 v[136:139], v[144:147], v[104:111], v148\n\t"
        "ds_read_b32 v128, v219 offset:0\n\t"
        "ds_read_b128 v[16:19], v217 offset:4096\n\t"
        "ds_read_b128 v[20:23], v217 offset:5120\n\t"
        "v_exp_f32 v117, v117\n\t"
        "v_exp_f32 v118, v118\n\t"
        "v_exp_f32 v119, v119\n\t"
        "v_exp_f32 v120, v120\n\t"
        "v_exp_f32 v121, v121\n\t"
        "v_exp_f32 v122, v122\n\t"
        "v_exp_f32 v123, v123\n\t"
        "v_exp_f32 v124, v124\n\t"
        "v_smfmac_f32_32x32x32_bf16 v[48:63], v[208:211], v[112:119], v148\n\t"
        "ds_read_b128 v[24:27], v217 offset:6144\n\t"
        "ds_read_b128 v[28:31], v217 offset:7168\n\t"
        "ds_read_b32 v132, v219 offset:256\n\t"
        "v_exp_f32 v125, v125\n\t"
        "v_exp_f32 v126, v126\n\t"
        "v_exp_f32 v127, v127\n\t"
        "v_smfmac_f32_16x16x64_bf16 v[140:143], v[144:147], v[112:119], v148\n\t"
        "s_nop 1\n\t"
        "v_smfmac_f32_32x32x32_bf16 v[48:63], v[212:215], v[120:127], v148\n\t"
        "v_smfmac_f32_16x16x64_bf16 v[140:143], v[144:147], v[120:127], v148\n\t"
        "s_waitcnt vmcnt(0) lgkmcnt(0)\n\t"
        "s_nop 6\n\t"
        "s_branch 9f\n\t"
        "24:\n\t"
        "s_add_u32 s44, s44, 0x4000\n\t"
        "s_and_b32 s44, s44, 0x3ffff\n\t"
        "s_cmp_eq_u32 s44, s49\n\t"
        "s_cselect_b32 s44, s50, s44\n\t"
        "s_add_u32 s52, s40, s44\n\t"
        "s_addc_u32 s53, s41, 0\n\t"
        "buffer_load_dwordx4 v[184:187], v149, s[52:55], 0 offen offset:0 sc1\n\t"
        "buffer_load_dwordx4 v[188:191], v149, s[52:55], 0 offen offset:1024 sc1\n\t"
        "buffer_load_dwordx4 v[192:195], v149, s[52:55], 0 offen offset:2048 sc1\n\t"
        "buffer_load_dwordx4 v[196:199], v149, s[52:55], 0 offen offset:3072 sc1\n\t"
        "buffer_load_dwordx4 v[200:203], v150, s[52:55], 0 offen offset:0 sc1\n\t"
        "buffer_load_dwordx4 v[204:207], v150, s[52:55], 0 offen offset:1024 sc1\n\t"
        "buffer_load_dwordx4 v[208:211], v150, s[52:55], 0 offen offset:2048 sc1\n\t"
        "buffer_load_dwordx4 v[212:215], v150, s[52:55], 0 offen offset:3072 sc1\n\t"
        "s_mov_b32 s46, 1\n\t"
        "s_branch 25b\n\t"
        "26:\n\t"
        "s_add_u32 s44, s44, 0x4000\n\t"
        "s_and_b32 s44, s44, 0x3ffff\n\t"
        "s_cmp_eq_u32 s44, s49\n\t"
        "s_cselect_b32 s44, s50, s44\n\t"
        "s_add_u32 s52, s40, s44\n\t"
        "s_addc_u32 s53, s41, 0\n\t"
        "buffer_load_dwordx4 v[184:187], v149, s[52:55], 0 offen offset:0 sc1\n\t"
        "buffer_load_dwordx4 v[188:191], v149, s[52:55], 0 offen offset:1024 sc1\n\t"
        "buffer_load_dwordx4 v[192:195], v149, s[52:55], 0 offen offset:2048 sc1\n\t"
        "buffer_load_dwordx4 v[196:199], v149, s[52:55], 0 offen offset:3072 sc1\n\t"
        "buffer_load_dwordx4 v[200:203], v150, s[52:55], 0 offen offset:0 sc1\n\t"
        "buffer_load_dwordx4 v[204:207], v150, s[52:55], 0 offen offset:1024 sc1\n\t"
        "buffer_load_dwordx4 v[208:211], v150, s[52:55], 0 offen offset:2048 sc1\n\t"
        "buffer_load_dwordx4 v[212:215], v150, s[52:55], 0 offen offset:3072 sc1\n\t"
        "s_mov_b32 s46, 1\n\t"
        "s_branch 27b\n\t"
        "28:\n\t"
        "s_add_u32 s44, s44, 0x4000\n\t"
        "s_and_b32 s44, s44, 0x3ffff\n\t"
        "s_cmp_eq_u32 s44, s49\n\t"
        "s_cselect_b32 s44, s50, s44\n\t"
        "s_add_u32 s52, s40, s44\n\t"
        "s_addc_u32 s53, s41, 0\n\t"
        "buffer_load_dwordx4 v[184:187], v149, s[52:55], 0 offen offset:0 sc1\n\t"
        "buffer_load_dwordx4 v[188:191], v149, s[52:55], 0 offen offset:1024 sc1\n\t"
        "buffer_load_dwordx4 v[192:195], v149, s[52:55], 0 offen offset:2048 sc1\n\t"
        "buffer_load_dwordx4 v[196:199], v149, s[52:55], 0 offen offset:3072 sc1\n\t"
        "buffer_load_dwordx4 v[200:203], v150, s[52:55], 0 offen offset:0 sc1\n\t"
        "buffer_load_dwordx4 v[204:207], v150, s[52:55], 0 offen offset:1024 sc1\n\t"
        "buffer_load_dwordx4 v[208:211], v150, s[52:55], 0 offen offset:2048 sc1\n\t"
        "buffer_load_dwordx4 v[212:215], v150, s[52:55], 0 offen offset:3072 sc1\n\t"
        "s_mov_b32 s46, 1\n\t"
        "s_branch 29b\n\t"
        "30:\n\t"
        "s_add_u32 s44, s44, 0x4000\n\t"
        "s_and_b32 s44, s44, 0x3ffff\n\t"
        "s_cmp_eq_u32 s44, s49\n\t"
        "s_cselect_b32 s44, s50, s44\n\t"
        "s_add_u32 s52, s40, s44\n\t"
        "s_addc_u32 s53, s41, 0\n\t"
        "buffer_load_dwordx4 v[184:187], v149, s[52:55], 0 offen offset:0 sc1\n\t"
        "buffer_load_dwordx4 v[188:191], v149, s[52:55], 0 offen offset:1024 sc1\n\t"
        "buffer_load_dwordx4 v[192:195], v149, s[52:55], 0 offen offset:2048 sc1\n\t"
        "buffer_load_dwordx4 v[196:199], v149, s[52:55], 0 offen offset:3072 sc1\n\t"
        "buffer_load_dwordx4 v[200:203], v150, s[52:55], 0 offen offset:0 sc1\n\t"
        "buffer_load_dwordx4 v[204:207], v150, s[52:55], 0 offen offset:1024 sc1\n\t"
        "buffer_load_dwordx4 v[208:211], v150, s[52:55], 0 offen offset:2048 sc1\n\t"
        "buffer_load_dwordx4 v[212:215], v150, s[52:55], 0 offen offset:3072 sc1\n\t"
        "s_mov_b32 s46, 1\n\t"
        "s_branch 31b\n\t"
        "32:\n\t"
        "s_add_u32 s44, s44, 0x4000\n\t"
        "s_and_b32 s44, s44, 0x3ffff\n\t"
        "s_cmp_eq_u32 s44, s49\n\t"
        "s_cselect_b32 s44, s50, s44\n\t"
        "s_add_u32 s52, s40, s44\n\t"
        "s_addc_u32 s53, s41, 0\n\t"
        "buffer_load_dwordx4 v[184:187], v149, s[52:55], 0 offen offset:0 sc1\n\t"
        "buffer_load_dwordx4 v[188:191], v149, s[52:55], 0 offen offset:1024 sc1\n\t"
        "buffer_load_dwordx4 v[192:195], v149, s[52:55], 0 offen offset:2048 sc1\n\t"
        "buffer_load_dwordx4 v[196:199], v149, s[52:55], 0 offen offset:3072 sc1\n\t"
        "buffer_load_dwordx4 v[200:203], v150, s[52:55], 0 offen offset:0 sc1\n\t"
        "buffer_load_dwordx4 v[204:207], v150, s[52:55], 0 offen offset:1024 sc1\n\t"
        "buffer_load_dwordx4 v[208:211], v150, s[52:55], 0 offen offset:2048 sc1\n\t"
        "buffer_load_dwordx4 v[212:215], v150, s[52:55], 0 offen offset:3072 sc1\n\t"
        "s_mov_b32 s46, 1\n\t"
        "s_branch 33b\n\t"
        "40:\n\t"
        "s_mov_b32 s45, 0x100000\n\t"
        "42:\n\t"
        "ds_read_b32 v151, v221\n\t"
        "s_sub_u32 s45, s45, 1\n\t"
        "s_waitcnt lgkmcnt(0)\n\t"
        "s_cbranch_scc1 41b\n\t"
        "v_cmp_ne_u32 vcc, v222, v151\n\t"
        "s_cbranch_vccnz 42b\n\t"
        "s_branch 41b\n\t"
        "9:\n\t"
        : "+{v[0:15]}"(o0), "+{v[16:31]}"(o1), "+{v[32:47]}"(o2), "+{v[48:63]}"(o3), "+{v[128:143]}"(lv), "+{s44}"(soff), "+{v[152:167]}"(kva), "+{v[168:183]}"(kvb), "+{v[184:191]}"(fv),
          [r0] "+v"(x1p[0]), [r1] "+v"(x1p[1]), [r2] "+v"(x1p[2]), [r3] "+v"(x1p[3]) STAMP_OUTS
        : "{v[64:71]}"(q[0]), "{v[72:79]}"(q[1]), "{v[80:87]}"(q[2]), "{v[88:95]}"(q[3]), "{v[144:147]}"(ones), "{v148}"(0xddddu), "{v149}"(kvoff), "{v150}"(kvoff + 4096),
          "{v[216:223]}"(cv), "{v224}"(lbase + L_W + 16), "{s[40:43]}"(srd), "{s47}"(L), "{s49}"(tm * 16384), "{s50}"(((tm + 1) & 15) * 16384), [xb] "s"(x0)
        : "v96", "v97", "v98", "v99", "v100", "v101", "v102", "v103", "v104", "v105", "v106", "v107", "v108", "v109", "v110", "v111", "v112", "v113", "v114", "v115", "v116", "v117", "v118", "v119", "v120", "v121", "v122", "v123", "v124", "v125", "v126", "v127", "v151", "v192", "v193", "v194", "v195", "v196", "v197", "v198", "v199", "v200", "v201", "v202", "v203", "v204", "v205", "v206", "v207", "v208", "v209", "v210", "v211", "v212", "v213", "v214", "v215", "s45", "s46", "s52", "s53", "s54", "s55", "vcc", "scc", "memory");
      STAMP_PUT(6 + 16 * L);
      STAMPV(9 + 16 * L);
      PHASE_VARS;
      const unsigned dst = OFF_OF + L * (5 * MBY) + (tm * 4 + 2 * kh) * TS + (tn * 2 + hh) * 2048 + lane * 16;
      auto put = [&](int u, const f32x16& o) {
#pragma unroll
        for (int c = 0; c < 2; ++c) {
          u32x4 w;
#pragma unroll
          for (int e = 0; e < 4; ++e) w[e] = pack2(o[8 * c + 2 * e], o[8 * c + 2 * e + 1]);
          stg(rs, dst + u * TS + c * 1024, w);
        }
      };
      const float la = lv[8] + lv[0], lb = lv[12] + lv[4];
      o2 = (o2 + o0) * RCP(la); put(0, o2);
      SBAR();
      o3 = (o3 + o1) * RCP(lb); put(1, o3);
      SBAR();
      {
        const f32x16 t = o2 + o3;
        float sum = 0.f;
#pragma unroll
        for (int e = 0; e < 16; e += 4) sum += (t[e] + t[e + 1]) + (t[e + 2] + t[e + 3]);
        if (__any(!(fabsf(sum) < 1e37f) | !(fmaxf(la, lb) < 1e27f) | !(fminf(la, lb) > 1e-9f) | !(la + lb == la + lb))) {
          DRAIN();
          attn_safe(rs, kvoff - kh * 8192, q[2], ones, o2); put(0, o2);
          attn_safe(rs, kvoff - kh * 8192, q[3], ones, o3); put(1, o3);
        }
      }
    }
    STAMPV(10 + 16 * L);
    {
      f32x16 acc[8];
      u32x16 wrc;
      u32x16 fl0, fl1, fl2;
      STAMP_DECL;
      { PHASE_VARS;
      float r0 = 0.f, r1 = 1.f;
      if (L == 0) {
#pragma unroll
        for (int i = 0; i < 4; ++i) x1p[i] = fo_swap(x1p[i]);
      } else {
        r0 = reinterpret_cast<const float*>(smem + L_AB)[256 + wid * 64 + r32]; r1 = reinterpret_cast<const float*>(smem + L_AB)[256 + wid * 64 + 32 + r32];
      }
#pragma unroll
      for (int e = 0; e < 16; ++e) { acc[e >> 3][2 * (e & 7)] = lo16(x1p[e >> 2][e & 3]) * r1 + r0; acc[e >> 3][2 * (e & 7) + 1] = hi16(x1p[e >> 2][e & 3]) * r1 + r0; }
      PIN(acc[0]); PIN(acc[1]);
      asm volatile("s_waitcnt vmcnt(0)\n\tds_write_b32 %0, %1" :: "v"(lbase + L_W + 80 + 4 * wid), "v"(2 * L + 2) : "memory");
      STAMPV(11 + 16 * L);
      const unsigned a0 = lbase + wrow * 128 + ((wch ^ wsw) << 4) + 8 * (tp & 1), a1 = lbase + wrow * 128 + (((4 + wch) ^ wsw) << 4) + 8 * (tp & 1);
      asm volatile(
        "s_mov_b32 s46, 0\n\t"
        "ostart_%=:\n\t"
        "s_mov_b32 s48, 0x200000\n\t"
        "2:\n\t"
        "ds_read_b32 v233, v232\n\t"
        "s_sub_u32 s48, s48, 1\n\t"
        "s_waitcnt lgkmcnt(0)\n\t"
        "s_cselect_b32 s47, 15, 0\n\t"
        "v_readfirstlane_b32 s51, v233\n\t"
        "s_nop 3\n\t"
        "s_or_b32 s51, s51, s47\n\t"
        "s_andn2_b32 s47, s51, s46\n\t"
        "s_cbranch_scc0 2b\n\t"
        "s_andn2_b32 s51, s51, s46\n\t"
        "s_ff1_i32_b32 s47, s51\n\t"
        "s_add_u32 s45, s47, s49\n\t"
        "s_and_b32 s45, s45, 3\n\t"
        "s_lshl_b32 s45, s45, 14\n\t"
        "s_and_b32 s51, s47, 0x80000000\n\t"
        "v_or_b32 v225, s51, v224\n\t"
        "s_and_b32 s51, s47, 3\n\t"
        "s_lshl_b32 s51, s51, 15\n\t"
        "v_add_u32 v230, s51, v226\n\t"
        "v_add_u32 v231, s51, v227\n\t"
        "buffer_load_dwordx4 v[160:163], v225, s[40:43], s45 offen offset:0 sc1\n\t"
        "buffer_load_dwordx4 v[164:167], v225, s[40:43], s45 offen offset:1024 sc1\n\t"
        "buffer_load_dwordx4 v[168:171], v225, s[40:43], s45 offen offset:2048 sc1\n\t"
        "buffer_load_dwordx4 v[172:175], v225, s[40:43], s45 offen offset:3072 sc1\n\t"
        "s_add_u32 s45, s45, 0x1000\n\t"
        "buffer_load_dwordx4 v[176:179], v225, s[40:43], s45 offen offset:0 sc1\n\t"
        "buffer_load_dwordx4 v[180:183], v225, s[40:43], s45 offen offset:1024 sc1\n\t"
        "buffer_load_dwordx4 v[184:187], v225, s[40:43], s45 offen offset:2048 sc1\n\t"
        "buffer_load_dwordx4 v[188:191], v225, s[40:43], s45 offen offset:3072 sc1\n\t"
        "s_add_u32 s45, s45, 0x1000\n\t"
        "buffer_load_dwordx4 v[192:195], v225, s[40:43], s45 offen offset:0 sc1\n\t"
        "buffer_load_dwordx4 v[196:199], v225, s[40:43], s45 offen offset:1024 sc1\n\t"
        "buffer_load_dwordx4 v[200:203], v225, s[40:43], s45 offen offset:2048 sc1\n\t"
        "buffer_load_dwordx4 v[204:207], v225, s[40:43], s45 offen offset:3072 sc1\n\t"
        "ds_read_b64_tr_b16 v[128:129], v230 offset:0\n\t"
        "ds_read_b64_tr_b16 v[130:131], v230 offset:1024\n\t"
        "ds_read_b64_tr_b16 v[132:133], v231 offset:0\n\t"
        "ds_read_b64_tr_b16 v[134:135], v231 offset:1024\n\t"
        "ds_read_b64_tr_b16 v[136:137], v230 offset:2048\n\t"
        "ds_read_b64_tr_b16 v[138:139], v230 offset:3072\n\t"
        "ds_read_b64_tr_b16 v[140:141], v231 offset:2048\n\t"
        "ds_read_b64_tr_b16 v[142:143], v231 offset:3072\n\t"
        "ds_read_b64_tr_b16 v[144:145], v230 offset:4096\n\t"
        "ds_read_b64_tr_b16 v[146:147], v230 offset:5120\n\t"
        "ds_read_b64_tr_b16 v[148:149], v231 offset:4096\n\t"
        "ds_read_b64_tr_b16 v[150:151], v231 offset:5120\n\t"
        "ds_read_b64_tr_b16 v[152:153], v230 offset:6144\n\t"
        "ds_read_b64_tr_b16 v[154:155], v230 offset:7168\n\t"
        "ds_read_b64_tr_b16 v[156:157], v231 offset:6144\n\t"
        "ds_read_b64_tr_b16 v[158:159], v231 offset:7168\n\t"
        "s_mov_b32 s44, s45\n\t"
        "s_cmp_eq_u32 s47, 0\n\t"
        "s_cbranch_scc1 oseg0_%=\n\t"
        "s_cmp_eq_u32 s47, 1\n\t"
        "s_cbranch_scc1 oseg1_%=\n\t"
        "s_cmp_eq_u32 s47, 2\n\t"
        "s_cbranch_scc1 oseg2_%=\n\t"
        "s_cmp_eq_u32 s47, 3\n\t"
        "s_cbranch_scc1 oseg3_%=\n\t"
        "oseg0_%=:\n\t"
        "s_or_b32 s46, s46, 1\n\t"
        "s_waitcnt vmcnt(11) lgkmcnt(14)\n\t"
        "v_mfma_f32_32x32x16_bf16 v[0:15], v[128:131], v[160:163], v[0:15]\n\t"
        "ds_read_b64_tr_b16 v[128:129], v226 offset:8192\n\t"
        "ds_read_b64_tr_b16 v[130:131], v226 offset:9216\n\t"
        "s_waitcnt lgkmcnt(14)\n\t"
        "v_mfma_f32_32x32x16_bf16 v[16:31], v[132:135], v[160:163], v[16:31]\n\t"
        "ds_read_b64_tr_b16 v[132:133], v227 offset:8192\n\t"
        "ds_read_b64_tr_b16 v[134:135], v227 offset:9216\n\t"
        "s_add_u32 s44, s44, 0x1000\n\t"
        "buffer_load_dwordx4 v[208:211], v224, s[40:43], s44 offen offset:0 sc1\n\t"
        "s_waitcnt vmcnt(11) lgkmcnt(14)\n\t"
        "v_mfma_f32_32x32x16_bf16 v[0:15], v[136:139], v[164:167], v[0:15]\n\t"
        "ds_read_b32 v233, v232\n\t"
        "ds_read_b64_tr_b16 v[136:137], v226 offset:10240\n\t"
        "ds_read_b64_tr_b16 v[138:139], v226 offset:11264\n\t"
        "s_waitcnt lgkmcnt(14)\n\t"
        "v_mfma_f32_32x32x16_bf16 v[16:31], v[140:143], v[164:167], v[16:31]\n\t"
        "ds_read_b64_tr_b16 v[140:141], v227 offset:10240\n\t"
        "ds_read_b64_tr_b16 v[142:143], v227 offset:11264\n\t"
        "buffer_load_dwordx4 v[212:215], v224, s[40:43], s44 offen offset:1024 sc1\n\t"
        "s_waitcnt vmcnt(11) lgkmcnt(14)\n\t"
        "v_mfma_f32_32x32x16_bf16 v[0:15], v[144:147], v[168:171], v[0:15]\n\t"
        "ds_read_b64_tr_b16 v[144:145], v226 offset:12288\n\t"
        "ds_read_b64_tr_b16 v[146:147], v226 offset:13312\n\t"
        "s_waitcnt lgkmcnt(14)\n\t"
        "v_mfma_f32_32x32x16_bf16 v[16:31], v[148:151], v[168:171], v[16:31]\n\t"
        "ds_read_b64_tr_b16 v[148:149], v227 offset:12288\n\t"
        "ds_read_b64_tr_b16 v[150:151], v227 offset:13312\n\t"
        "buffer_load_dwordx4 v[216:219], v224, s[40:43], s44 offen offset:2048 sc1\n\t"
        "s_waitcnt vmcnt(11) lgkmcnt(8)\n\t"
        "v_mfma_f32_32x32x16_bf16 v[0:15], v[152:155], v[172:175], v[0:15]\n\t"
        "v_readfirstlane_b32 s51, v233\n\t"
        "ds_read_b64_tr_b16 v[152:153], v226 offset:14336\n\t"
        "ds_read_b64_tr_b16 v[154:155], v226 offset:15360\n\t"
        "s_waitcnt lgkmcnt(14)\n\t"
        "v_mfma_f32_32x32x16_bf16 v[16:31], v[156:159], v[172:175], v[16:31]\n\t"
        "s_andn2_b32 s51, s51, s46\n\t"
        "s_ff1_i32_b32 s47, s51\n\t"
        "s_add_u32 s45, s47, s49\n\t"
        "s_and_b32 s45, s45, 3\n\t"
        "s_lshl_b32 s45, s45, 14\n\t"
        "ds_read_b64_tr_b16 v[156:157], v227 offset:14336\n\t"
        "ds_read_b64_tr_b16 v[158:159], v227 offset:15360\n\t"
        "buffer_load_dwordx4 v[220:223], v224, s[40:43], s44 offen offset:3072 sc1\n\t"
        "s_waitcnt vmcnt(11) lgkmcnt(14)\n\t"
        "v_mfma_f32_32x32x16_bf16 v[0:15], v[128:131], v[176:179], v[0:15]\n\t"
        "s_and_b32 s51, s47, 0x80000000\n\t"
        "v_or_b32 v225, s51, v224\n\t"
        "s_and_b32 s51, s47, 3\n\t"
        "s_lshl_b32 s51, s51, 15\n\t"
        "v_add_u32 v230, s51, v226\n\t"
        "v_add_u32 v231, s51, v227\n\t"
        "ds_read_b64_tr_b16 v[128:129], v226 offset:16384\n\t"
        "ds_read_b64_tr_b16 v[130:131], v226 offset:17408\n\t"
        "s_waitcnt lgkmcnt(14)\n\t"
        "v_mfma_f32_32x32x16_bf16 v[16:31], v[132:135], v[176:179], v[16:31]\n\t"
        "ds_read_b64_tr_b16 v[132:133], v227 offset:16384\n\t"
        "ds_read_b64_tr_b16 v[134:135], v227 offset:17408\n\t"
        "buffer_load_dwordx4 v[160:163], v225, s[40:43], s45 offen offset:0 sc1\n\t"
        "s_waitcnt vmcnt(11) lgkmcnt(14)\n\t"
        "v_mfma_f32_32x32x16_bf16 v[0:15], v[136:139], v[180:183], v[0:15]\n\t"
        "ds_read_b64_tr_b16 v[136:137], v226 offset:18432\n\t"
        "ds_read_b64_tr_b16 v[138:139], v226 offset:19456\n\t"
        "s_waitcnt lgkmcnt(14)\n\t"
        "v_mfma_f32_32x32x16_bf16 v[16:31], v[140:143], v[180:183], v[16:31]\n\t"
        "ds_read_b64_tr_b16 v[140:141], v227 offset:18432\n\t"
        "ds_read_b64_tr_b16 v[142:143], v227 offset:19456\n\t"
        "buffer_load_dwordx4 v[164:167], v225, s[40:43], s45 offen offset:1024 sc1\n\t"
        "s_waitcnt vmcnt(11) lgkmcnt(14)\n\t"
        "v_mfma_f32_32x32x16_bf16 v[0:15], v[144:147], v[184:187], v[0:15]\n\t"
        "ds_read_b64_tr_b16 v[144:145], v226 offset:20480\n\t"
        "ds_read_b64_tr_b16 v[146:147], v226 offset:21504\n\t"
        "s_waitcnt lgkmcnt(14)\n\t"
        "v_mfma_f32_32x32x16_bf16 v[16:31], v[148:151], v[184:187], v[16:31]\n\t"
        "ds_read_b64_tr_b16 v[148:149], v227 offset:20480\n\t"
        "ds_read_b64_tr_b16 v[150:151], v227 offset:21504\n\t"
        "buffer_load_dwordx4 v[168:171], v225, s[40:43], s45 offen offset:2048 sc1\n\t"
        "s_waitcnt vmcnt(11) lgkmcnt(14)\n\t"
        "v_mfma_f32_32x32x16_bf16 v[0:15], v[152:155], v[188:191], v[0:15]\n\t"
        "ds_read_b64_tr_b16 v[152:153], v226 offset:22528\n\t"
        "ds_read_b64_tr_b16 v[154:155], v226 offset:23552\n\t"
        "s_waitcnt lgkmcnt(14)\n\t"
        "v_mfma_f32_32x32x16_bf16 v[16:31], v[156:159], v[188:191], v[16:31]\n\t"
        "ds_read_b64_tr_b16 v[156:157], v227 offset:22528\n\t"
        "ds_read_b64_tr_b16 v[158:159], v227 offset:23552\n\t"
        "buffer_load_dwordx4 v[172:175], v225, s[40:43], s45 offen offset:3072 sc1\n\t"
        "s_waitcnt vmcnt(11) lgkmcnt(14)\n\t"
        "v_mfma_f32_32x32x16_bf16 v[0:15], v[128:131], v[192:195], v[0:15]\n\t"
        "ds_read_b64_tr_b16 v[128:129], v226 offset:24576\n\t"
        "ds_read_b64_tr_b16 v[130:131], v226 offset:25600\n\t"
        "s_waitcnt lgkmcnt(14)\n\t"
        "v_mfma_f32_32x32x16_bf16 v[16:31], v[132:135], v[192:195], v[16:31]\n\t"
        "ds_read_b64_tr_b16 v[132:133], v227 offset:24576\n\t"
        "ds_read_b64_tr_b16 v[134:135], v227 offset:25600\n\t"
        "s_add_u32 s45, s45, 0x1000\n\t"
        "buffer_load_dwordx4 v[176:179], v225, s[40:43], s45 offen offset:0 sc1\n\t"
        "s_waitcnt vmcnt(11) lgkmcnt(14)\n\t"
        "v_mfma_f32_32x32x16_bf16 v[0:15], v[136:139], v[196:199], v[0:15]\n\t"
        "ds_read_b64_tr_b16 v[136:137], v226 offset:26624\n\t"
        "ds_read_b64_tr_b16 v[138:139], v226 offset:27648\n\t"
        "s_waitcnt lgkmcnt(14)\n\t"
        "v_mfma_f32_32x32x16_bf16 v[16:31], v[140:143], v[196:199], v[16:31]\n\t"
        "ds_read_b64_tr_b16 v[140:141], v227 offset:26624\n\t"
        "ds_read_b64_tr_b16 v[142:143], v227 offset:27648\n\t"
        "buffer_load_dwordx4 v[180:183], v225, s[40:43], s45 offen offset:1024 sc1\n\t"
        "s_waitcnt vmcnt(11) lgkmcnt(14)\n\t"
        "v_mfma_f32_32x32x16_bf16 v[0:15], v[144:147], v[200:203], v[0:15]\n\t"
        "ds_read_b64_tr_b16 v[144:145], v226 offset:28672\n\t"
        "ds_read_b64_tr_b16 v[146:147], v226 offset:29696\n\t"
        "s_waitcnt lgkmcnt(14)\n\t"
        "v_mfma_f32_32x32x16_bf16 v[16:31], v[148:151], v[200:203], v[16:31]\n\t"
        "ds_read_b64_tr_b16 v[148:149], v227 offset:28672\n\t"
        "ds_read_b64_tr_b16 v[150:151], v227 offset:29696\n\t"
        "buffer_load_dwordx4 v[184:187], v225, s[40:43], s45 offen offset:2048 sc1\n\t"
        "s_waitcnt vmcnt(11) lgkmcnt(14)\n\t"
        "v_mfma_f32_32x32x16_bf16 v[0:15], v[152:155], v[204:207], v[0:15]\n\t"
        "ds_read_b64_tr_b16 v[152:153], v226 offset:30720\n\t"
        "ds_read_b64_tr_b16 v[154:155], v226 offset:31744\n\t"
        "s_waitcnt lgkmcnt(14)\n\t"
        "v_mfma_f32_32x32x16_bf16 v[16:31], v[156:159], v[204:207], v[16:31]\n\t"
        "ds_read_b64_tr_b16 v[156:157], v227 offset:30720\n\t"
        "ds_read_b64_tr_b16 v[158:159], v227 offset:31744\n\t"
        "buffer_load_dwordx4 v[188:191], v225, s[40:43], s45 offen offset:3072 sc1\n\t"
        "s_waitcnt vmcnt(11) lgkmcnt(14)\n\t"
        "v_mfma_f32_32x32x16_bf16 v[0:15], v[128:131], v[208:211], v[0:15]\n\t"
        "ds_read_b64_tr_b16 v[128:129], v230 offset:0\n\t"
        "ds_read_b64_tr_b16 v[130:131], v230 offset:1024\n\t"
        "s_waitcnt lgkmcnt(14)\n\t"
        "v_mfma_f32_32x32x16_bf16 v[16:31], v[132:135], v[208:211], v[16:31]\n\t"
        "ds_read_b64_tr_b16 v[132:133], v231 offset:0\n\t"
        "ds_read_b64_tr_b16 v[134:135], v231 offset:1024\n\t"
        "s_add_u32 s45, s45, 0x1000\n\t"
        "buffer_load_dwordx4 v[192:195], v225, s[40:43], s45 offen offset:0 sc1\n\t"
        "s_waitcnt vmcnt(11) lgkmcnt(14)\n\t"
        "v_mfma_f32_32x32x16_bf16 v[0:15], v[136:139], v[212:215], v[0:15]\n\t"
        "ds_read_b64_tr_b16 v[136:137], v230 offset:2048\n\t"
        "ds_read_b64_tr_b16 v[138:139], v230 offset:3072\n\t"
        "s_waitcnt lgkmcnt(14)\n\t"
        "v_mfma_f32_32x32x16_bf16 v[16:31], v[140:143], v[212:215], v[16:31]\n\t"
        "ds_read_b64_tr_b16 v[140:141], v231 offset:2048\n\t"
        "ds_read_b64_tr_b16 v[142:143], v231 offset:3072\n\t"
        "buffer_load_dwordx4 v[196:199], v225, s[40:43], s45 offen offset:1024 sc1\n\t"
        "s_waitcnt vmcnt(11) lgkmcnt(14)\n\t"
        "v_mfma_f32_32x32x16_bf16 v[0:15], v[144:147], v[216:219], v[0:15]\n\t"
        "ds_read_b64_tr_b16 v[144:145], v230 offset:4096\n\t"
        "ds_read_b64_tr_b16 v[146:147], v230 offset:5120\n\t"
        "s_waitcnt lgkmcnt(14)\n\t"
        "v_mfma_f32_32x32x16_bf16 v[16:31], v[148:151], v[216:219], v[16:31]\n\t"
        "ds_read_b64_tr_b16 v[148:149], v231 offset:4096\n\t"
        "ds_read_b64_tr_b16 v[150:151], v231 offset:5120\n\t"
        "buffer_load_dwordx4 v[200:203], v225, s[40:43], s45 offen offset:2048 sc1\n\t"
        "s_waitcnt vmcnt(11) lgkmcnt(14)\n\t"
        "v_mfma_f32_32x32x16_bf16 v[0:15], v[152:155], v[220:223], v[0:15]\n\t"
        "ds_read_b64_tr_b16 v[152:153], v230 offset:6144\n\t"
        "ds_read_b64_tr_b16 v[154:155], v230 offset:7168\n\t"
        "s_waitcnt lgkmcnt(14)\n\t"
        "v_mfma_f32_32x32x16_bf16 v[16:31], v[156:159], v[220:223], v[16:31]\n\t"
        "ds_read_b64_tr_b16 v[156:157], v231 offset:6144\n\t"
        "ds_read_b64_tr_b16 v[158:159], v231 offset:7168\n\t"
        "buffer_load_dwordx4 v[204:207], v225, s[40:43], s45 offen offset:3072 sc1\n\t"
        "s_mov_b32 s44, s45\n\t"
        "s_cmp_eq_u32 s47, 0\n\t"
        "s_cbranch_scc1 oseg0_%=\n\t"
        "s_cmp_eq_u32 s47, 1\n\t"
        "s_cbranch_scc1 oseg1_%=\n\t"
        "s_cmp_eq_u32 s47, 2\n\t"
        "s_cbranch_scc1 oseg2_%=\n\t"
        "s_cmp_eq_u32 s47, 3\n\t"
        "s_cbranch_scc1 oseg3_%=\n\t"
        "s_cmp_lg_u32 s46, 15\n\t"
        "s_cbranch_scc1 ostart_%=\n\t"
        "s_branch oend_%=\n\t"
        "oseg1_%=:\n\t"
        "s_or_b32 s46, s46, 2\n\t"
        "s_waitcnt vmcnt(11) lgkmcnt(14)\n\t"
        "v_mfma_f32_32x32x16_bf16 v[32:47], v[128:131], v[160:163], 0\n\t"
        "ds_read_b64_tr_b16 v[128:129], v226 offset:40960\n\t"
        "ds_read_b64_tr_b16 v[130:131], v226 offset:41984\n\t"
        "s_waitcnt lgkmcnt(14)\n\t"
        "v_mfma_f32_32x32x16_bf16 v[48:63], v[132:135], v[160:163], 0\n\t"
        "ds_read_b64_tr_b16 v[132:133], v227 offset:40960\n\t"
        "ds_read_b64_tr_b16 v[134:135], v227 offset:41984\n\t"
        "s_add_u32 s44, s44, 0x1000\n\t"
        "buffer_load_dwordx4 v[208:211], v224, s[40:43], s44 offen offset:0 sc1\n\t"
        "s_waitcnt vmcnt(11) lgkmcnt(14)\n\t"
        "v_mfma_f32_32x32x16_bf16 v[32:47], v[136:139], v[164:167], v[32:47]\n\t"
        "ds_read_b32 v233, v232\n\t"
        "ds_read_b64_tr_b16 v[136:137], v226 offset:43008\n\t"
        "ds_read_b64_tr_b16 v[138:139], v226 offset:44032\n\t"
        "s_waitcnt lgkmcnt(14)\n\t"
        "v_mfma_f32_32x32x16_bf16 v[48:63], v[140:143], v[164:167], v[48:63]\n\t"
        "ds_read_b64_tr_b16 v[140:141], v227 offset:43008\n\t"
        "ds_read_b64_tr_b16 v[142:143], v227 offset:44032\n\t"
        "buffer_load_dwordx4 v[212:215], v224, s[40:43], s44 offen offset:1024 sc1\n\t"
        "s_waitcnt vmcnt(11) lgkmcnt(14)\n\t"
        "v_mfma_f32_32x32x16_bf16 v[32:47], v[144:147], v[168:171], v[32:47]\n\t"
        "ds_read_b64_tr_b16 v[144:145], v226 offset:45056\n\t"
        "ds_read_b64_tr_b16 v[146:147], v226 offset:46080\n\t"
        "s_waitcnt lgkmcnt(14)\n\t"
        "v_mfma_f32_32x32x16_bf16 v[48:63], v[148:151], v[168:171], v[48:63]\n\t"
        "ds_read_b64_tr_b16 v[148:149], v227 offset:45056\n\t"
        "ds_read_b64_tr_b16 v[150:151], v227 offset:46080\n\t"
        "buffer_load_dwordx4 v[216:219], v224, s[40:43], s44 offen offset:2048 sc1\n\t"
        "s_waitcnt vmcnt(11) lgkmcnt(8)\n\t"
        "v_mfma_f32_32x32x16_bf16 v[32:47], v[152:155], v[172:175], v[32:47]\n\t"
        "v_readfirstlane_b32 s51, v233\n\t"
        "ds_read_b64_tr_b16 v[152:153], v226 offset:47104\n\t"
        "ds_read_b64_tr_b16 v[154:155], v226 offset:48128\n\t"
        "s_waitcnt lgkmcnt(14)\n\t"
        "v_mfma_f32_32x32x16_bf16 v[48:63], v[156:159], v[172:175], v[48:63]\n\t"
        "s_andn2_b32 s51, s51, s46\n\t"
        "s_ff1_i32_b32 s47, s51\n\t"
        "s_add_u32 s45, s47, s49\n\t"
        "s_and_b32 s45, s45, 3\n\t"
        "s_lshl_b32 s45, s45, 14\n\t"
        "ds_read_b64_tr_b16 v[156:157], v227 offset:47104\n\t"
        "ds_read_b64_tr_b16 v[158:159], v227 offset:48128\n\t"
        "buffer_load_dwordx4 v[220:223], v224, s[40:43], s44 offen offset:3072 sc1\n\t"
        "s_waitcnt vmcnt(11) lgkmcnt(14)\n\t"
        "v_mfma_f32_32x32x16_bf16 v[32:47], v[128:131], v[176:179], v[32:47]\n\t"
        "s_and_b32 s51, s47, 0x80000000\n\t"
        "v_or_b32 v225, s51, v224\n\t"
        "s_and_b32 s51, s47, 3\n\t"
        "s_lshl_b32 s51, s51, 15\n\t"
        "v_add_u32 v230, s51, v226\n\t"
        "v_add_u32 v231, s51, v227\n\t"
        "ds_read_b64_tr_b16 v[128:129], v226 offset:49152\n\t"
        "ds_read_b64_tr_b16 v[130:131], v226 offset:50176\n\t"
        "s_waitcnt lgkmcnt(14)\n\t"
        "v_mfma_f32_32x32x16_bf16 v[48:63], v[132:135], v[176:179], v[48:63]\n\t"
        "ds_read_b64_tr_b16 v[132:133], v227 offset:49152\n\t"
        "ds_read_b64_tr_b16 v[134:135], v227 offset:50176\n\t"
        "buffer_load_dwordx4 v[160:163], v225, s[40:43], s45 offen offset:0 sc1\n\t"
        "s_waitcnt vmcnt(11) lgkmcnt(14)\n\t"
        "v_mfma_f32_32x32x16_bf16 v[32:47], v[136:139], v[180:183], v[32:47]\n\t"
        "ds_read_b64_tr_b16 v[136:137], v226 offset:51200\n\t"
        "ds_read_b64_tr_b16 v[138:139], v226 offset:52224\n\t"
        "s_waitcnt lgkmcnt(14)\n\t"
        "v_mfma_f32_32x32x16_bf16 v[48:63], v[140:143], v[180:183], v[48:63]\n\t"
        "ds_read_b64_tr_b16 v[140:141], v227 offset:51200\n\t"
        "ds_read_b64_tr_b16 v[142:143], v227 offset:52224\n\t"
        "buffer_load_dwordx4 v[164:167], v225, s[40:43], s45 offen offset:1024 sc1\n\t"
        "s_waitcnt vmcnt(11) lgkmcnt(14)\n\t"
        "v_mfma_f32_32x32x16_bf16 v[32:47], v[144:147], v[184:187], v[32:47]\n\t"
        "ds_read_b64_tr_b16 v[144:145], v226 offset:53248\n\t"
        "ds_read_b64_tr_b16 v[146:147], v226 offset:54272\n\t"
        "s_waitcnt lgkmcnt(14)\n\t"
        "v_mfma_f32_32x32x16_bf16 v[48:63], v[148:151], v[184:187], v[48:63]\n\t"
        "ds_read_b64_tr_b16 v[148:149], v227 offset:53248\n\t"
        "ds_read_b64_tr_b16 v[150:151], v227 offset:54272\n\t"
        "buffer_load_dwordx4 v[168:171], v225, s[40:43], s45 offen offset:2048 sc1\n\t"
        "s_waitcnt vmcnt(11) lgkmcnt(14)\n\t"
        "v_mfma_f32_32x32x16_bf16 v[32:47], v[152:155], v[188:191], v[32:47]\n\t"
        "ds_read_b64_tr_b16 v[152:153], v226 offset:55296\n\t"
        "ds_read_b64_tr_b16 v[154:155], v226 offset:56320\n\t"
        "s_waitcnt lgkmcnt(14)\n\t"
        "v_mfma_f32_32x32x16_bf16 v[48:63], v[156:159], v[188:191], v[48:63]\n\t"
        "ds_read_b64_tr_b16 v[156:157], v227 offset:55296\n\t"
        "ds_read_b64_tr_b16 v[158:159], v227 offset:56320\n\t"
        "buffer_load_dwordx4 v[172:175], v225, s[40:43], s45 offen offset:3072 sc1\n\t"
        "s_waitcnt vmcnt(11) lgkmcnt(14)\n\t"
        "v_mfma_f32_32x32x16_bf16 v[32:47], v[128:131], v[192:195], v[32:47]\n\t"
        "ds_read_b64_tr_b16 v[128:129], v226 offset:57344\n\t"
        "ds_read_b64_tr_b16 v[130:131], v226 offset:58368\n\t"
        "s_waitcnt lgkmcnt(14)\n\t"
        "v_mfma_f32_32x32x16_bf16 v[48:63], v[132:135], v[192:195], v[48:63]\n\t"
        "ds_read_b64_tr_b16 v[132:133], v227 offset:57344\n\t"
        "ds_read_b64_tr_b16 v[134:135], v227 offset:58368\n\t"
        "s_add_u32 s45, s45, 0x1000\n\t"
        "buffer_load_dwordx4 v[176:179], v225, s[40:43], s45 offen offset:0 sc1\n\t"
        "s_waitcnt vmcnt(11) lgkmcnt(14)\n\t"
        "v_mfma_f32_32x32x16_bf16 v[32:47], v[136:139], v[196:199], v[32:47]\n\t"
        "ds_read_b64_tr_b16 v[136:137], v226 offset:59392\n\t"
        "ds_read_b64_tr_b16 v[138:139], v226 offset:60416\n\t"
        "s_waitcnt lgkmcnt(14)\n\t"
        "v_mfma_f32_32x32x16_bf16 v[48:63], v[140:143], v[196:199], v[48:63]\n\t"
        "ds_read_b64_tr_b16 v[140:141], v227 offset:59392\n\t"
        "ds_read_b64_tr_b16 v[142:143], v227 offset:60416\n\t"
        "buffer_load_dwordx4 v[180:183], v225, s[40:43], s45 offen offset:1024 sc1\n\t"
        "s_waitcnt vmcnt(11) lgkmcnt(14)\n\t"
        "v_mfma_f32_32x32x16_bf16 v[32:47], v[144:147], v[200:203], v[32:47]\n\t"
        "ds_read_b64_tr_b16 v[144:145], v226 offset:61440\n\t"
        "ds_read_b64_tr_b16 v[146:147], v226 offset:62464\n\t"
        "s_waitcnt lgkmcnt(14)\n\t"
        "v_mfma_f32_32x32x16_bf16 v[48:63], v[148:151], v[200:203], v[48:63]\n\t"
        "ds_read_b64_tr_b16 v[148:149], v227 offset:61440\n\t"
        "ds_read_b64_tr_b16 v[150:151], v227 offset:62464\n\t"
        "buffer_load_dwordx4 v[184:187], v225, s[40:43], s45 offen offset:2048 sc1\n\t"
        "s_waitcnt vmcnt(11) lgkmcnt(14)\n\t"
        "v_mfma_f32_32x32x16_bf16 v[32:47], v[152:155], v[204:207], v[32:47]\n\t"
        "ds_read_b64_tr_b16 v[152:153], v226 offset:63488\n\t"
        "ds_read_b64_tr_b16 v[154:155], v226 offset:64512\n\t"
        "s_waitcnt lgkmcnt(14)\n\t"
        "v_mfma_f32_32x32x16_bf16 v[48:63], v[156:159], v[204:207], v[48:63]\n\t"
        "ds_read_b64_tr_b16 v[156:157], v227 offset:63488\n\t"
        "ds_read_b64_tr_b16 v[158:159], v227 offset:64512\n\t"
        "buffer_load_dwordx4 v[188:191], v225, s[40:43], s45 offen offset:3072 sc1\n\t"
        "s_waitcnt vmcnt(11) lgkmcnt(14)\n\t"
        "v_mfma_f32_32x32x16_bf16 v[32:47], v[128:131], v[208:211], v[32:47]\n\t"
        "ds_read_b64_tr_b16 v[128:129], v230 offset:0\n\t"
        "ds_read_b64_tr_b16 v[130:131], v230 offset:1024\n\t"
        "s_waitcnt lgkmcnt(14)\n\t"
        "v_mfma_f32_32x32x16_bf16 v[48:63], v[132:135], v[208:211], v[48:63]\n\t"
        "ds_read_b64_tr_b16 v[132:133], v231 offset:0\n\t"
        "ds_read_b64_tr_b16 v[134:135], v231 offset:1024\n\t"
        "s_add_u32 s45, s45, 0x1000\n\t"
        "buffer_load_dwordx4 v[192:195], v225, s[40:43], s45 offen offset:0 sc1\n\t"
        "s_waitcnt vmcnt(11) lgkmcnt(14)\n\t"
        "v_mfma_f32_32x32x16_bf16 v[32:47], v[136:139], v[212:215], v[32:47]\n\t"
        "ds_read_b64_tr_b16 v[136:137], v230 offset:2048\n\t"
        "ds_read_b64_tr_b16 v[138:139], v230 offset:3072\n\t"
        "s_waitcnt lgkmcnt(14)\n\t"
        "v_mfma_f32_32x32x16_bf16 v[48:63], v[140:143], v[212:215], v[48:63]\n\t"
        "ds_read_b64_tr_b16 v[140:141], v231 offset:2048\n\t"
        "ds_read_b64_tr_b16 v[142:143], v231 offset:3072\n\t"
        "buffer_load_dwordx4 v[196:199], v225, s[40:43], s45 offen offset:1024 sc1\n\t"
        "s_waitcnt vmcnt(11) lgkmcnt(14)\n\t"
        "v_mfma_f32_32x32x16_bf16 v[32:47], v[144:147], v[216:219], v[32:47]\n\t"
        "ds_read_b64_tr_b16 v[144:145], v230 offset:4096\n\t"
        "ds_read_b64_tr_b16 v[146:147], v230 offset:5120\n\t"
        "s_waitcnt lgkmcnt(14)\n\t"
        "v_mfma_f32_32x32x16_bf16 v[48:63], v[148:151], v[216:219], v[48:63]\n\t"
        "ds_read_b64_tr_b16 v[148:149], v231 offset:4096\n\t"
        "ds_read_b64_tr_b16 v[150:151], v231 offset:5120\n\t"
        "buffer_load_dwordx4 v[200:203], v225, s[40:43], s45 offen offset:2048 sc1\n\t"
        "s_waitcnt vmcnt(11) lgkmcnt(14)\n\t"
        "v_mfma_f32_32x32x16_bf16 v[32:47], v[152:155], v[220:223], v[32:47]\n\t"
        "ds_read_b64_tr_b16 v[152:153], v230 offset:6144\n\t"
        "ds_read_b64_tr_b16 v[154:155], v230 offset:7168\n\t"
        "s_waitcnt lgkmcnt(14)\n\t"
        "v_mfma_f32_32x32x16_bf16 v[48:63], v[156:159], v[220:223], v[48:63]\n\t"
        "ds_read_b64_tr_b16 v[156:157], v231 offset:6144\n\t"
        "ds_read_b64_tr_b16 v[158:159], v231 offset:7168\n\t"
        "buffer_load_dwordx4 v[204:207], v225, s[40:43], s45 offen offset:3072 sc1\n\t"
        "s_mov_b32 s44, s45\n\t"
        "s_cmp_eq_u32 s47, 0\n\t"
        "s_cbranch_scc1 oseg0_%=\n\t"
        "s_cmp_eq_u32 s47, 1\n\t"
        "s_cbranch_scc1 oseg1_%=\n\t"
        "s_cmp_eq_u32 s47, 2\n\t"
        "s_cbranch_scc1 oseg2_%=\n\t"
        "s_cmp_eq_u32 s47, 3\n\t"
        "s_cbranch_scc1 oseg3_%=\n\t"
        "s_cmp_lg_u32 s46, 15\n\t"
        "s_cbranch_scc1 ostart_%=\n\t"
        "s_branch oend_%=\n\t"
        "oseg2_%=:\n\t"
        "s_or_b32 s46, s46, 4\n\t"
        "s_waitcnt vmcnt(11) lgkmcnt(14)\n\t"
        "v_mfma_f32_32x32x16_bf16 v[64:79], v[128:131], v[160:163], 0\n\t"
        "ds_read_b64_tr_b16 v[128:129], v228 offset:8192\n\t"
        "ds_read_b64_tr_b16 v[130:131], v228 offset:9216\n\t"
        "s_waitcnt lgkmcnt(14)\n\t"
        "v_mfma_f32_32x32x16_bf16 v[80:95], v[132:135], v[160:163], 0\n\t"
        "ds_read_b64_tr_b16 v[132:133], v229 offset:8192\n\t"
        "ds_read_b64_tr_b16 v[134:135], v229 offset:9216\n\t"
        "s_add_u32 s44, s44, 0x1000\n\t"
        "buffer_load_dwordx4 v[208:211], v224, s[40:43], s44 offen offset:0 sc1\n\t"
        "s_waitcnt vmcnt(11) lgkmcnt(14)\n\t"
        "v_mfma_f32_32x32x16_bf16 v[64:79], v[136:139], v[164:167], v[64:79]\n\t"
        "ds_read_b32 v233, v232\n\t"
        "ds_read_b64_tr_b16 v[136:137], v228 offset:10240\n\t"
        "ds_read_b64_tr_b16 v[138:139], v228 offset:11264\n\t"
        "s_waitcnt lgkmcnt(14)\n\t"
        "v_mfma_f32_32x32x16_bf16 v[80:95], v[140:143], v[164:167], v[80:95]\n\t"
        "ds_read_b64_tr_b16 v[140:141], v229 offset:10240\n\t"
        "ds_read_b64_tr_b16 v[142:143], v229 offset:11264\n\t"
        "buffer_load_dwordx4 v[212:215], v224, s[40:43], s44 offen offset:1024 sc1\n\t"
        "s_waitcnt vmcnt(11) lgkmcnt(14)\n\t"
        "v_mfma_f32_32x32x16_bf16 v[64:79], v[144:147], v[168:171], v[64:79]\n\t"
        "ds_read_b64_tr_b16 v[144:145], v228 offset:12288\n\t"
        "ds_read_b64_tr_b16 v[146:147], v228 offset:13312\n\t"
        "s_waitcnt lgkmcnt(14)\n\t"
        "v_mfma_f32_32x32x16_bf16 v[80:95], v[148:151], v[168:171], v[80:95]\n\t"
        "ds_read_b64_tr_b16 v[148:149], v229 offset:12288\n\t"
        "ds_read_b64_tr_b16 v[150:151], v229 offset:13312\n\t"
        "buffer_load_dwordx4 v[216:219], v224, s[40:43], s44 offen offset:2048 sc1\n\t"
        "s_waitcnt vmcnt(11) lgkmcnt(8)\n\t"
        "v_mfma_f32_32x32x16_bf16 v[64:79], v[152:155], v[172:175], v[64:79]\n\t"
        "v_readfirstlane_b32 s51, v233\n\t"
        "ds_read_b64_tr_b16 v[152:153], v228 offset:14336\n\t"
        "ds_read_b64_tr_b16 v[154:155], v228 offset:15360\n\t"
        "s_waitcnt lgkmcnt(14)\n\t"
        "v_mfma_f32_32x32x16_bf16 v[80:95], v[156:159], v[172:175], v[80:95]\n\t"
        "s_andn2_b32 s51, s51, s46\n\t"
        "s_ff1_i32_b32 s47, s51\n\t"
        "s_add_u32 s45, s47, s49\n\t"
        "s_and_b32 s45, s45, 3\n\t"
        "s_lshl_b32 s45, s45, 14\n\t"
        "ds_read_b64_tr_b16 v[156:157], v229 offset:14336\n\t"
        "ds_read_b64_tr_b16 v[158:159], v229 offset:15360\n\t"
        "buffer_load_dwordx4 v[220:223], v224, s[40:43], s44 offen offset:3072 sc1\n\t"
        "s_waitcnt vmcnt(11) lgkmcnt(14)\n\t"
        "v_mfma_f32_32x32x16_bf16 v[64:79], v[128:131], v[176:179], v[64:79]\n\t"
        "s_and_b32 s51, s47, 0x80000000\n\t"
        "v_or_b32 v225, s51, v224\n\t"
        "s_and_b32 s51, s47, 3\n\t"
        "s_lshl_b32 s51, s51, 15\n\t"
        "v_add_u32 v230, s51, v226\n\t"
        "v_add_u32 v231, s51, v227\n\t"
        "ds_read_b64_tr_b16 v[128:129], v228 offset:16384\n\t"
        "ds_read_b64_tr_b16 v[130:131], v228 offset:17408\n\t"
        "s_waitcnt lgkmcnt(14)\n\t"
        "v_mfma_f32_32x32x16_bf16 v[80:95], v[132:135], v[176:179], v[80:95]\n\t"
        "ds_read_b64_tr_b16 v[132:133], v229 offset:16384\n\t"
        "ds_read_b64_tr_b16 v[134:135], v229 offset:17408\n\t"
        "buffer_load_dwordx4 v[160:163], v225, s[40:43], s45 offen offset:0 sc1\n\t"
        "s_waitcnt vmcnt(11) lgkmcnt(14)\n\t"
        "v_mfma_f32_32x32x16_bf16 v[64:79], v[136:139], v[180:183], v[64:79]\n\t"
        "ds_read_b64_tr_b16 v[136:137], v228 offset:18432\n\t"
        "ds_read_b64_tr_b16 v[138:139], v228 offset:19456\n\t"
        "s_waitcnt lgkmcnt(14)\n\t"
        "v_mfma_f32_32x32x16_bf16 v[80:95], v[140:143], v[180:183], v[80:95]\n\t"
        "ds_read_b64_tr_b16 v[140:141], v229 offset:18432\n\t"
        "ds_read_b64_tr_b16 v[142:143], v229 offset:19456\n\t"
        "buffer_load_dwordx4 v[164:167], v225, s[40:43], s45 offen offset:1024 sc1\n\t"
        "s_waitcnt vmcnt(11) lgkmcnt(14)\n\t"
        "v_mfma_f32_32x32x16_bf16 v[64:79], v[144:147], v[184:187], v[64:79]\n\t"
        "ds_read_b64_tr_b16 v[144:145], v228 offset:20480\n\t"
        "ds_read_b64_tr_b16 v[146:147], v228 offset:21504\n\t"
        "s_waitcnt lgkmcnt(14)\n\t"
        "v_mfma_f32_32x32x16_bf16 v[80:95], v[148:151], v[184:187], v[80:95]\n\t"
        "ds_read_b64_tr_b16 v[148:149], v229 offset:20480\n\t"
        "ds_read_b64_tr_b16 v[150:151], v229 offset:21504\n\t"
        "buffer_load_dwordx4 v[168:171], v225, s[40:43], s45 offen offset:2048 sc1\n\t"
        "s_waitcnt vmcnt(11) lgkmcnt(14)\n\t"
        "v_mfma_f32_32x32x16_bf16 v[64:79], v[152:155], v[188:191], v[64:79]\n\t"
        "ds_read_b64_tr_b16 v[152:153], v228 offset:22528\n\t"
        "ds_read_b64_tr_b16 v[154:155], v228 offset:23552\n\t"
        "s_waitcnt lgkmcnt(14)\n\t"
        "v_mfma_f32_32x32x16_bf16 v[80:95], v[156:159], v[188:191], v[80:95]\n\t"
        "ds_read_b64_tr_b16 v[156:157], v229 offset:22528\n\t"
        "ds_read_b64_tr_b16 v[158:159], v229 offset:23552\n\t"
        "buffer_load_dwordx4 v[172:175], v225, s[40:43], s45 offen offset:3072 sc1\n\t"
        "s_waitcnt vmcnt(11) lgkmcnt(14)\n\t"
        "v_mfma_f32_32x32x16_bf16 v[64:79], v[128:131], v[192:195], v[64:79]\n\t"
        "ds_read_b64_tr_b16 v[128:129], v228 offset:24576\n\t"
        "ds_read_b64_tr_b16 v[130:131], v228 offset:25600\n\t"
        "s_waitcnt lgkmcnt(14)\n\t"
        "v_mfma_f32_32x32x16_bf16 v[80:95], v[132:135], v[192:195], v[80:95]\n\t"
        "ds_read_b64_tr_b16 v[132:133], v229 offset:24576\n\t"
        "ds_read_b64_tr_b16 v[134:135], v229 offset:25600\n\t"
        "s_add_u32 s45, s45, 0x1000\n\t"
        "buffer_load_dwordx4 v[176:179], v225, s[40:43], s45 offen offset:0 sc1\n\t"
        "s_waitcnt vmcnt(11) lgkmcnt(14)\n\t"
        "v_mfma_f32_32x32x16_bf16 v[64:79], v[136:139], v[196:199], v[64:79]\n\t"
        "ds_read_b64_tr_b16 v[136:137], v228 offset:26624\n\t"
        "ds_read_b64_tr_b16 v[138:139], v228 offset:27648\n\t"
        "s_waitcnt lgkmcnt(14)\n\t"
        "v_mfma_f32_32x32x16_bf16 v[80:95], v[140:143], v[196:199], v[80:95]\n\t"
        "ds_read_b64_tr_b16 v[140:141], v229 offset:26624\n\t"
        "ds_read_b64_tr_b16 v[142:143], v229 offset:27648\n\t"
        "buffer_load_dwordx4 v[180:183], v225, s[40:43], s45 offen offset:1024 sc1\n\t"
        "s_waitcnt vmcnt(11) lgkmcnt(14)\n\t"
        "v_mfma_f32_32x32x16_bf16 v[64:79], v[144:147], v[200:203], v[64:79]\n\t"
        "ds_read_b64_tr_b16 v[144:145], v228 offset:28672\n\t"
        "ds_read_b64_tr_b16 v[146:147], v228 offset:29696\n\t"
        "s_waitcnt lgkmcnt(14)\n\t"
        "v_mfma_f32_32x32x16_bf16 v[80:95], v[148:151], v[200:203], v[80:95]\n\t"
        "ds_read_b64_tr_b16 v[148:149], v229 offset:28672\n\t"
        "ds_read_b64_tr_b16 v[150:151], v229 offset:29696\n\t"
        "buffer_load_dwordx4 v[184:187], v225, s[40:43], s45 offen offset:2048 sc1\n\t"
        "s_waitcnt vmcnt(11) lgkmcnt(14)\n\t"
        "v_mfma_f32_32x32x16_bf16 v[64:79], v[152:155], v[204:207], v[64:79]\n\t"
        "ds_read_b64_tr_b16 v[152:153], v228 offset:30720\n\t"
        "ds_read_b64_tr_b16 v[154:155], v228 offset:31744\n\t"
        "s_waitcnt lgkmcnt(14)\n\t"
        "v_mfma_f32_32x32x16_bf16 v[80:95], v[156:159], v[204:207], v[80:95]\n\t"
        "ds_read_b64_tr_b16 v[156:157], v229 offset:30720\n\t"
        "ds_read_b64_tr_b16 v[158:159], v229 offset:31744\n\t"
        "buffer_load_dwordx4 v[188:191], v225, s[40:43], s45 offen offset:3072 sc1\n\t"
        "s_waitcnt vmcnt(11) lgkmcnt(14)\n\t"
        "v_mfma_f32_32x32x16_bf16 v[64:79], v[128:131], v[208:211], v[64:79]\n\t"
        "ds_read_b64_tr_b16 v[128:129], v230 offset:0\n\t"
        "ds_read_b64_tr_b16 v[130:131], v230 offset:1024\n\t"
        "s_waitcnt lgkmcnt(14)\n\t"
        "v_mfma_f32_32x32x16_bf16 v[80:95], v[132:135], v[208:211], v[80:95]\n\t"
        "ds_read_b64_tr_b16 v[132:133], v231 offset:0\n\t"
        "ds_read_b64_tr_b16 v[134:135], v231 offset:1024\n\t"
        "s_add_u32 s45, s45, 0x1000\n\t"
        "buffer_load_dwordx4 v[192:195], v225, s[40:43], s45 offen offset:0 sc1\n\t"
        "s_waitcnt vmcnt(11) lgkmcnt(14)\n\t"
        "v_mfma_f32_32x32x16_bf16 v[64:79], v[136:139], v[212:215], v[64:79]\n\t"
        "ds_read_b64_tr_b16 v[136:137], v230 offset:2048\n\t"
        "ds_read_b64_tr_b16 v[138:139], v230 offset:3072\n\t"
        "s_waitcnt lgkmcnt(14)\n\t"
        "v_mfma_f32_32x32x16_bf16 v[80:95], v[140:143], v[212:215], v[80:95]\n\t"
        "ds_read_b64_tr_b16 v[140:141], v231 offset:2048\n\t"
        "ds_read_b64_tr_b16 v[142:143], v231 offset:3072\n\t"
        "buffer_load_dwordx4 v[196:199], v225, s[40:43], s45 offen offset:1024 sc1\n\t"
        "s_waitcnt vmcnt(11) lgkmcnt(14)\n\t"
        "v_mfma_f32_32x32x16_bf16 v[64:79], v[144:147], v[216:219], v[64:79]\n\t"
        "ds_read_b64_tr_b16 v[144:145], v230 offset:4096\n\t"
        "ds_read_b64_tr_b16 v[146:147], v230 offset:5120\n\t"
        "s_waitcnt lgkmcnt(14)\n\t"
        "v_mfma_f32_32x32x16_bf16 v[80:95], v[148:151], v[216:219], v[80:95]\n\t"
        "ds_read_b64_tr_b16 v[148:149], v231 offset:4096\n\t"
        "ds_read_b64_tr_b16 v[150:151], v231 offset:5120\n\t"
        "buffer_load_dwordx4 v[200:203], v225, s[40:43], s45 offen offset:2048 sc1\n\t"
        "s_waitcnt vmcnt(11) lgkmcnt(14)\n\t"
        "v_mfma_f32_32x32x16_bf16 v[64:79], v[152:155], v[220:223], v[64:79]\n\t"
        "ds_read_b64_tr_b16 v[152:153], v230 offset:6144\n\t"
        "ds_read_b64_tr_b16 v[154:155], v230 offset:7168\n\t"
        "s_waitcnt lgkmcnt(14)\n\t"
        "v_mfma_f32_32x32x16_bf16 v[80:95], v[156:159], v[220:223], v[80:95]\n\t"
        "ds_read_b64_tr_b16 v[156:157], v231 offset:6144\n\t"
        "ds_read_b64_tr_b16 v[158:159], v231 offset:7168\n\t"
        "buffer_load_dwordx4 v[204:207], v225, s[40:43], s45 offen offset:3072 sc1\n\t"
        "s_mov_b32 s44, s45\n\t"
        "s_cmp_eq_u32 s47, 0\n\t"
        "s_cbranch_scc1 oseg0_%=\n\t"
        "s_cmp_eq_u32 s47, 1\n\t"
        "s_cbranch_scc1 oseg1_%=\n\t"
        "s_cmp_eq_u32 s47, 2\n\t"
        "s_cbranch_scc1 oseg2_%=\n\t"
        "s_cmp_eq_u32 s47, 3\n\t"
        "s_cbranch_scc1 oseg3_%=\n\t"
        "s_cmp_lg_u32 s46, 15\n\t"
        "s_cbranch_scc1 ostart_%=\n\t"
        "s_branch oend_%=\n\t"
        "oseg3_%=:\n\t"
        "s_or_b32 s46, s46, 8\n\t"
        "s_waitcnt vmcnt(11) lgkmcnt(14)\n\t"
        "v_mfma_f32_32x32x16_bf16 v[96:111], v[128:131], v[160:163], 0\n\t"
        "ds_read_b64_tr_b16 v[128:129], v228 offset:40960\n\t"
        "ds_read_b64_tr_b16 v[130:131], v228 offset:41984\n\t"
        "s_waitcnt lgkmcnt(14)\n\t"
        "v_mfma_f32_32x32x16_bf16 v[112:127], v[132:135], v[160:163], 0\n\t"
        "ds_read_b64_tr_b16 v[132:133], v229 offset:40960\n\t"
        "ds_read_b64_tr_b16 v[134:135], v229 offset:41984\n\t"
        "s_add_u32 s44, s44, 0x1000\n\t"
        "buffer_load_dwordx4 v[208:211], v224, s[40:43], s44 offen offset:0 sc1\n\t"
        "s_waitcnt vmcnt(11) lgkmcnt(14)\n\t"
        "v_mfma_f32_32x32x16_bf16 v[96:111], v[136:139], v[164:167], v[96:111]\n\t"
        "ds_read_b32 v233, v232\n\t"
        "ds_read_b64_tr_b16 v[136:137], v228 offset:43008\n\t"
        "ds_read_b64_tr_b16 v[138:139], v228 offset:44032\n\t"
        "s_waitcnt lgkmcnt(14)\n\t"
        "v_mfma_f32_32x32x16_bf16 v[112:127], v[140:143], v[164:167], v[112:127]\n\t"
        "ds_read_b64_tr_b16 v[140:141], v229 offset:43008\n\t"
        "ds_read_b64_tr_b16 v[142:143], v229 offset:44032\n\t"
        "buffer_load_dwordx4 v[212:215], v224, s[40:43], s44 offen offset:1024 sc1\n\t"
        "s_waitcnt vmcnt(11) lgkmcnt(14)\n\t"
        "v_mfma_f32_32x32x16_bf16 v[96:111], v[144:147], v[168:171], v[96:111]\n\t"
        "ds_read_b64_tr_b16 v[144:145], v228 offset:45056\n\t"
        "ds_read_b64_tr_b16 v[146:147], v228 offset:46080\n\t"
        "s_waitcnt lgkmcnt(14)\n\t"
        "v_mfma_f32_32x32x16_bf16 v[112:127], v[148:151], v[168:171], v[112:127]\n\t"
        "ds_read_b64_tr_b16 v[148:149], v229 offset:45056\n\t"
        "ds_read_b64_tr_b16 v[150:151], v229 offset:46080\n\t"
        "buffer_load_dwordx4 v[216:219], v224, s[40:43], s44 offen offset:2048 sc1\n\t"
        "s_waitcnt vmcnt(11) lgkmcnt(8)\n\t"
        "v_mfma_f32_32x32x16_bf16 v[96:111], v[152:155], v[172:175], v[96:111]\n\t"
        "v_readfirstlane_b32 s51, v233\n\t"
        "ds_read_b64_tr_b16 v[152:153], v228 offset:47104\n\t"
        "ds_read_b64_tr_b16 v[154:155], v228 offset:48128\n\t"
        "s_waitcnt lgkmcnt(14)\n\t"
        "v_mfma_f32_32x32x16_bf16 v[112:127], v[156:159], v[172:175], v[112:127]\n\t"
        "s_andn2_b32 s51, s51, s46\n\t"
        "s_ff1_i32_b32 s47, s51\n\t"
        "s_add_u32 s45, s47, s49\n\t"
        "s_and_b32 s45, s45, 3\n\t"
        "s_lshl_b32 s45, s45, 14\n\t"
        "ds_read_b64_tr_b16 v[156:157], v229 offset:47104\n\t"
        "ds_read_b64_tr_b16 v[158:159], v229 offset:48128\n\t"
        "buffer_load_dwordx4 v[220:223], v224, s[40:43], s44 offen offset:3072 sc1\n\t"
        "s_waitcnt vmcnt(11) lgkmcnt(14)\n\t"
        "v_mfma_f32_32x32x16_bf16 v[96:111], v[128:131], v[176:179], v[96:111]\n\t"
        "s_and_b32 s51, s47, 0x80000000\n\t"
        "v_or_b32 v225, s51, v224\n\t"
        "s_and_b32 s51, s47, 3\n\t"
        "s_lshl_b32 s51, s51, 15\n\t"
        "v_add_u32 v230, s51, v226\n\t"
        "v_add_u32 v231, s51, v227\n\t"
        "ds_read_b64_tr_b16 v[128:129], v228 offset:49152\n\t"
        "ds_read_b64_tr_b16 v[130:131], v228 offset:50176\n\t"
        "s_waitcnt lgkmcnt(14)\n\t"
        "v_mfma_f32_32x32x16_bf16 v[112:127], v[132:135], v[176:179], v[112:127]\n\t"
        "ds_read_b64_tr_b16 v[132:133], v229 offset:49152\n\t"
        "ds_read_b64_tr_b16 v[134:135], v229 offset:50176\n\t"
        "buffer_load_dwordx4 v[160:163], v225, s[40:43], s45 offen offset:0 sc1\n\t"
        "s_waitcnt vmcnt(11) lgkmcnt(14)\n\t"
        "v_mfma_f32_32x32x16_bf16 v[96:111], v[136:139], v[180:183], v[96:111]\n\t"
        "ds_read_b64_tr_b16 v[136:137], v228 offset:51200\n\t"
        "ds_read_b64_tr_b16 v[138:139], v228 offset:52224\n\t"
        "s_waitcnt lgkmcnt(14)\n\t"
        "v_mfma_f32_32x32x16_bf16 v[112:127], v[140:143], v[180:183], v[112:127]\n\t"
        "ds_read_b64_tr_b16 v[140:141], v229 offset:51200\n\t"
        "ds_read_b64_tr_b16 v[142:143], v229 offset:52224\n\t"
        "buffer_load_dwordx4 v[164:167], v225, s[40:43], s45 offen offset:1024 sc1\n\t"
        "s_waitcnt vmcnt(11) lgkmcnt(14)\n\t"
        "v_mfma_f32_32x32x16_bf16 v[96:111], v[144:147], v[184:187], v[96:111]\n\t"
        "ds_read_b64_tr_b16 v[144:145], v228 offset:53248\n\t"
        "ds_read_b64_tr_b16 v[146:147], v228 offset:54272\n\t"
        "s_waitcnt lgkmcnt(14)\n\t"
        "v_mfma_f32_32x32x16_bf16 v[112:127], v[148:151], v[184:187], v[112:127]\n\t"
        "ds_read_b64_tr_b16 v[148:149], v229 offset:53248\n\t"
        "ds_read_b64_tr_b16 v[150:151], v229 offset:54272\n\t"
        "buffer_load_dwordx4 v[168:171], v225, s[40:43], s45 offen offset:2048 sc1\n\t"
        "s_waitcnt vmcnt(11) lgkmcnt(14)\n\t"
        "v_mfma_f32_32x32x16_bf16 v[96:111], v[152:155], v[188:191], v[96:111]\n\t"
        "ds_read_b64_tr_b16 v[152:153], v228 offset:55296\n\t"
        "ds_read_b64_tr_b16 v[154:155], v228 offset:56320\n\t"
        "s_waitcnt lgkmcnt(14)\n\t"
        "v_mfma_f32_32x32x16_bf16 v[112:127], v[156:159], v[188:191], v[112:127]\n\t"
        "ds_read_b64_tr_b16 v[156:157], v229 offset:55296\n\t"
        "ds_read_b64_tr_b16 v[158:159], v229 offset:56320\n\t"
        "buffer_load_dwordx4 v[172:175], v225, s[40:43], s45 offen offset:3072 sc1\n\t"
        "s_waitcnt vmcnt(11) lgkmcnt(14)\n\t"
        "v_mfma_f32_32x32x16_bf16 v[96:111], v[128:131], v[192:195], v[96:111]\n\t"
        "ds_read_b64_tr_b16 v[128:129], v228 offset:57344\n\t"
        "ds_read_b64_tr_b16 v[130:131], v228 offset:58368\n\t"
        "s_waitcnt lgkmcnt(14)\n\t"
        "v_mfma_f32_32x32x16_bf16 v[112:127], v[132:135], v[192:195], v[112:127]\n\t"
        "ds_read_b64_tr_b16 v[132:133], v229 offset:57344\n\t"
        "ds_read_b64_tr_b16 v[134:135], v229 offset:58368\n\t"
        "s_add_u32 s45, s45, 0x1000\n\t"
        "buffer_load_dwordx4 v[176:179], v225, s[40:43], s45 offen offset:0 sc1\n\t"
        "s_waitcnt vmcnt(11) lgkmcnt(14)\n\t"
        "v_mfma_f32_32x32x16_bf16 v[96:111], v[136:139], v[196:199], v[96:111]\n\t"
        "ds_read_b64_tr_b16 v[136:137], v228 offset:59392\n\t"
        "ds_read_b64_tr_b16 v[138:139], v228 offset:60416\n\t"
        "s_waitcnt lgkmcnt(14)\n\t"
        "v_mfma_f32_32x32x16_bf16 v[112:127], v[140:143], v[196:199], v[112:127]\n\t"
        "ds_read_b64_tr_b16 v[140:141], v229 offset:59392\n\t"
        "ds_read_b64_tr_b16 v[142:143], v229 offset:60416\n\t"
        "buffer_load_dwordx4 v[180:183], v225, s[40:43], s45 offen offset:1024 sc1\n\t"
        "s_waitcnt vmcnt(11) lgkmcnt(14)\n\t"
        "v_mfma_f32_32x32x16_bf16 v[96:111], v[144:147], v[200:203], v[96:111]\n\t"
        "ds_read_b64_tr_b16 v[144:145], v228 offset:61440\n\t"
        "ds_read_b64_tr_b16 v[146:147], v228 offset:62464\n\t"
        "s_waitcnt lgkmcnt(14)\n\t"
        "v_mfma_f32_32x32x16_bf16 v[112:127], v[148:151], v[200:203], v[112:127]\n\t"
        "ds_read_b64_tr_b16 v[148:149], v229 offset:61440\n\t"
        "ds_read_b64_tr_b16 v[150:151], v229 offset:62464\n\t"
        "buffer_load_dwordx4 v[184:187], v225, s[40:43], s45 offen offset:2048 sc1\n\t"
        "s_waitcnt vmcnt(11) lgkmcnt(14)\n\t"
        "v_mfma_f32_32x32x16_bf16 v[96:111], v[152:155], v[204:207], v[96:111]\n\t"
        "ds_read_b64_tr_b16 v[152:153], v228 offset:63488\n\t"
        "ds_read_b64_tr_b16 v[154:155], v228 offset:64512\n\t"
        "s_waitcnt lgkmcnt(14)\n\t"
        "v_mfma_f32_32x32x16_bf16 v[112:127], v[156:159], v[204:207], v[112:127]\n\t"
        "ds_read_b64_tr_b16 v[156:157], v229 offset:63488\n\t"
        "ds_read_b64_tr_b16 v[158:159], v229 offset:64512\n\t"
        "buffer_load_dwordx4 v[188:191], v225, s[40:43], s45 offen offset:3072 sc1\n\t"
        "s_waitcnt vmcnt(11) lgkmcnt(14)\n\t"
        "v_mfma_f32_32x32x16_bf16 v[96:111], v[128:131], v[208:211], v[96:111]\n\t"
        "ds_read_b64_tr_b16 v[128:129], v230 offset:0\n\t"
        "ds_read_b64_tr_b16 v[130:131], v230 offset:1024\n\t"
        "s_waitcnt lgkmcnt(14)\n\t"
        "v_mfma_f32_32x32x16_bf16 v[112:127], v[132:135], v[208:211], v[112:127]\n\t"
        "ds_read_b64_tr_b16 v[132:133], v231 offset:0\n\t"
        "ds_read_b64_tr_b16 v[134:135], v231 offset:1024\n\t"
        "s_add_u32 s45, s45, 0x1000\n\t"
        "buffer_load_dwordx4 v[192:195], v225, s[40:43], s45 offen offset:0 sc1\n\t"
        "s_waitcnt vmcnt(11) lgkmcnt(14)\n\t"
        "v_mfma_f32_32x32x16_bf16 v[96:111], v[136:139], v[212:215], v[96:111]\n\t"
        "ds_read_b64_tr_b16 v[136:137], v230 offset:2048\n\t"
        "ds_read_b64_tr_b16 v[138:139], v230 offset:3072\n\t"
        "s_waitcnt lgkmcnt(14)\n\t"
        "v_mfma_f32_32x32x16_bf16 v[112:127], v[140:143], v[212:215], v[112:127]\n\t"
        "ds_read_b64_tr_b16 v[140:141], v231 offset:2048\n\t"
        "ds_read_b64_tr_b16 v[142:143], v231 offset:3072\n\t"
        "buffer_load_dwordx4 v[196:199], v225, s[40:43], s45 offen offset:1024 sc1\n\t"
        "s_waitcnt vmcnt(11) lgkmcnt(14)\n\t"
        "v_mfma_f32_32x32x16_bf16 v[96:111], v[144:147], v[216:219], v[96:111]\n\t"
        "ds_read_b64_tr_b16 v[144:145], v230 offset:4096\n\t"
        "ds_read_b64_tr_b16 v[146:147], v230 offset:5120\n\t"
        "s_waitcnt lgkmcnt(14)\n\t"
        "v_mfma_f32_32x32x16_bf16 v[112:127], v[148:151], v[216:219], v[112:127]\n\t"
        "ds_read_b64_tr_b16 v[148:149], v231 offset:4096\n\t"
        "ds_read_b64_tr_b16 v[150:151], v231 offset:5120\n\t"
        "buffer_load_dwordx4 v[200:203], v225, s[40:43], s45 offen offset:2048 sc1\n\t"
        "s_waitcnt vmcnt(11) lgkmcnt(14)\n\t"
        "v_mfma_f32_32x32x16_bf16 v[96:111], v[152:155], v[220:223], v[96:111]\n\t"
        "ds_read_b64_tr_b16 v[152:153], v230 offset:6144\n\t"
        "ds_read_b64_tr_b16 v[154:155], v230 offset:7168\n\t"
        "s_waitcnt lgkmcnt(14)\n\t"
        "v_mfma_f32_32x32x16_bf16 v[112:127], v[156:159], v[220:223], v[112:127]\n\t"
        "ds_read_b64_tr_b16 v[156:157], v231 offset:6144\n\t"
        "ds_read_b64_tr_b16 v[158:159], v231 offset:7168\n\t"
        "buffer_load_dwordx4 v[204:207], v225, s[40:43], s45 offen offset:3072 sc1\n\t"
        "s_mov_b32 s44, s45\n\t"
        "s_cmp_eq_u32 s47, 0\n\t"
        "s_cbranch_scc1 oseg0_%=\n\t"
        "s_cmp_eq_u32 s47, 1\n\t"
        "s_cbranch_scc1 oseg1_%=\n\t"
        "s_cmp_eq_u32 s47, 2\n\t"
        "s_cbranch_scc1 oseg2_%=\n\t"
        "s_cmp_eq_u32 s47, 3\n\t"
        "s_cbranch_scc1 oseg3_%=\n\t"
        "s_cmp_lg_u32 s46, 15\n\t"
        "s_cbranch_scc1 ostart_%=\n\t"
        "s_branch oend_%=\n\t"
        "oend_%=:\n\t"
        "ds_read_b64 v[128:129], v234 offset:0\n\t"
        "ds_read_b64 v[130:131], v234 offset:16\n\t"
        "ds_read_b64 v[132:133], v234 offset:32\n\t"
        "ds_read_b64 v[134:135], v234 offset:48\n\t"
        "ds_read_b64 v[136:137], v234 offset:64\n\t"
        "ds_read_b64 v[138:139], v234 offset:80\n\t"
        "ds_read_b64 v[140:141], v234 offset:96\n\t"
        "ds_read_b64 v[142:143], v234 offset:112\n\t"
        "s_waitcnt lgkmcnt(0)\n\t"
        "s_barrier\n\t"
        : "+{v[0:15]}"(acc[0]), "+{v[16:31]}"(acc[1]), "={v[32:47]}"(acc[2]), "={v[48:63]}"(acc[3]), "={v[64:79]}"(acc[4]), "={v[80:95]}"(acc[5]),
          "={v[96:111]}"(acc[6]), "={v[112:127]}"(acc[7]), "={v[128:143]}"(wrc),
          "={v[160:175]}"(fl0), "={v[176:191]}"(fl1), "={v[192:207]}"(fl2) STAMP_OUTS
        : "{v224}"(OFF_OF + L * (5 * MBY) + tile * TS + lane * 16), "{v226}"(a0), "{v227}"(a1), "{v228}"(a0 + 65536), "{v229}"(a1 + 65536),
          "{v232}"(lbase + L_W + 20 + 8 * L), "{v234}"(lbase + L_WR + hi * 8), "{s49}"((tn >> 2) + 1), "{s[40:43]}"(srd)
        : "v144", "v145", "v146", "v147", "v148", "v149", "v150", "v151", "v152", "v153", "v154", "v155", "v156", "v157", "v158", "v159", "v208", "v209", "v210", "v211", "v212", "v213", "v214", "v215", "v216", "v217", "v218", "v219", "v220", "v221", "v222", "v223", "v225", "v230", "v231", "v233", "s44", "s45", "s46", "s47", "s48", "s51", "vcc", "scc", "memory");
      }
      PHASE_VARS;
      STAMP_PUT(12 + 16 * L);
      STAMPV(15 + 16 * L);
      float s = 0.f, sq = 0.f, d = 0.f;
      const unsigned xdst = OFF_X1 + tile * TS + tn * 4096 + lane * 16;
#pragma unroll
      for (int i = 0; i < 4; ++i) {
#pragma unroll
        for (int e = 0; e < 4; ++e) {
          const int j = i >> 1, k = 8 * (i & 1) + 2 * e;
          x1p[i][e] = pack2((acc[j][k] + acc[2 + j][k]) + (acc[4 + j][k] + acc[6 + j][k]), (acc[j][k + 1] + acc[2 + j][k + 1]) + (acc[4 + j][k + 1] + acc[6 + j][k + 1]));
        }
        if (L == 0) stg(rs, xdst + i * 1024, x1p[i]);
        SBAR();
      }
#pragma unroll
      for (int g = 0; g < 8; ++g)
#pragma unroll
        for (int e = 0; e < 2; ++e) {
          const unsigned o = x1p[g >> 1][2 * (g & 1) + e];
          s = dot2(o, 0x3f803f80u, s); sq = dot2(o, o, sq); d = dot2(o, wrc[2 * g + e], d);
        }
      const u32x4 pst = {nzf(half_swap_sum(s)), nzf(half_swap_sum(sq)), nzf(half_swap_sum(d)), 0u};
      asm volatile("buffer_store_dwordx4 %0, %1, %2, 0 offen sc1\n\ts_waitcnt vmcnt(1)\n\tds_write_b32 %3, %4"
                   :: "v"(pst), "v"(stats_dst(sx, tile, tn, lane)), "s"(srd), "v"(lbase + L_W + 64 + 4 * wid), "v"(3),
                      "{v[160:175]}"(fl0), "{v[176:191]}"(fl1), "{v[192:207]}"(fl2) : "memory");
    }
    STAMPV(16 + 16 * L);
  }
  {
    PHASE_VARS;
    float S, Q, Dd, mean, var, mb1, rd1;
    __builtin_amdgcn_s_sleep(13);
    row_stats_sum(rs, OFF_SX + SX_LAYER, tile, lane, S, Q, Dd);
    STAMPV(33);
    row_stats_fin(rs, OFF_SX + SX_LAYER + SX_CAND + (tn * 16 + tm) * 128 + wid * 4, smem, 2, tile, lane, S, Q, Dd, U2F(words[0]), x1p, mean, var, mb1, rd1);
    STAMPV(34);
    u32x4 xo[4];
#pragma unroll
    for (int i = 0; i < 4; ++i) {
#pragma unroll
      for (int e = 0; e < 4; ++e) {
        const unsigned h2 = ln_pair(x1p[i][e], mb1, rd1);
        xo[i][e] = pack2(lo16(x1p[i][e]) + lo16(h2), hi16(x1p[i][e]) + hi16(h2));
      }
      *reinterpret_cast<u32x4*>(smem + wid * 4096 + r32 * 128 + ((2 * i + hi) ^ (r32 & 7)) * 16) = fo_swap(xo[i]);
    }
    asm volatile("s_waitcnt lgkmcnt(0)" ::: "memory");
#pragma unroll
    for (int i = 0; i < 4; ++i) { xo[i] = *reinterpret_cast<const u32x4*>(smem + wid * 4096 + i * 1024 + (lane >> 3) * 128 + ((lane ^ (lane >> 3)) & 7) * 16); PIN(xo[i]); }
    u16* dst = out + (size_t)(tile * 32 + (lane >> 3)) * D_ + tn * 64 + (lane & 7) * 8;
    lds_wait_ge(lbase + L_W + 4 + 4 * (lane & 1), 2);
    const int rel = (int)words[3] - tile * 32 - (lane >> 3);
#pragma unroll
    for (int i = 0; i < 4; ++i)
      if (rel != 8 * i) st16(dst + (size_t)i * 8 * D_, xo[i]);
    STAMPV(35);
    STAMP_END;
  }
}

extern "C" __global__ void end_marker() {}

extern "C" void kernel_launch(void* const* d_in, const int* in_sizes, int n_in, void* d_out, int out_size,
                              void* d_ws, size_t ws_size, hipStream_t stream) {
  char* ws = (char*)d_ws;
  mega_kernel<<<256, 512, 0, stream>>>((const u16*)d_in[0], (const u16*)d_in[1], (const u16*)d_in[2], (const u16*)d_in[4], (const u16*)d_in[5],
                                       (const u16*)d_in[6], (const u16*)d_in[7], ws, (u16*)d_out);
}
```

```cpp
#include <hip/hip_runtime.h>
#include <hip/hip_bf16.h>
#include <stdint.h>

typedef unsigned short u16;
typedef short s16x4 __attribute__((ext_vector_type(4)));
typedef short s16x8 __attribute__((ext_vector_type(8)));
typedef float f32x16 __attribute__((ext_vector_type(16)));
typedef float f32x8 __attribute__((ext_vector_type(8)));
typedef float f32x4 __attribute__((ext_vector_type(4)));
typedef float f32x2 __attribute__((ext_vector_type(2)));
typedef unsigned int u32x16 __attribute__((ext_vector_type(16)));
typedef unsigned int u32x8 __attribute__((ext_vector_type(8)));
typedef unsigned int u32x4 __attribute__((ext_vector_type(4)));
typedef unsigned int u32x2 __attribute__((ext_vector_type(2)));
typedef __bf16 bf16x2 __attribute__((ext_vector_type(2)));
typedef __bf16 bf16x8 __attribute__((ext_vector_type(8)));
typedef __bf16 bf16x16 __attribute__((ext_vector_type(16)));

#define DI __device__ __forceinline__
#define LDS_PTR(T, p) ((__attribute__((address_space(3))) T*)(p))
#define BARRIER() asm volatile("s_barrier" ::: "memory")
#define BAR_LGKM() asm volatile("s_waitcnt lgkmcnt(0)\n\ts_barrier" ::: "memory")
#define BAR_ALL() asm volatile("s_waitcnt vmcnt(0) lgkmcnt(0)\n\ts_barrier" ::: "memory")
#define DRAIN() asm volatile("s_waitcnt vmcnt(0)" ::: "memory")
#define MFMA32(a, b, c) __builtin_amdgcn_mfma_f32_32x32x16_bf16(a, b, c, 0, 0, 0)
#define MFMA16(a, b, c) __builtin_amdgcn_mfma_f32_16x16x32_bf16(a, b, c, 0, 0, 0)
#define PIN(x) asm volatile("" : "+v"(x))
#define EX2(x) __builtin_amdgcn_exp2f(x)
#define RCP(x) __builtin_amdgcn_rcpf(x)
#define SBAR() __builtin_amdgcn_sched_barrier(0)

#define STAMP_INIT
#define STAMPV(i)
#define STAMP_END
#define STAMP_DECL
#define STAMP_OUTS
#define STAMP_PUT(i)
#define JIT()
constexpr int D_ = 1024, F_ = 3584;
constexpr float QS = 0.17677669529663687f * 1.4426950408889634f;

constexpr unsigned MBY = 1u << 20;
constexpr unsigned TS = 65536 + 4096, HS = 262144 + 4096;
constexpr unsigned OFF_HF = 0, OFF_X1 = 5 * MBY;
constexpr unsigned OFF_OF = 10 * MBY;
constexpr unsigned OFF_KV = 20 * MBY;
constexpr unsigned OFF_FL = 40 * MBY;
constexpr unsigned FL_EPOCH = OFF_FL;
constexpr unsigned FL_KV = OFF_FL + 4096;
constexpr unsigned FL_O = FL_KV + 65536;
constexpr unsigned FL_X = FL_O + 65536;
constexpr unsigned OFF_CS = 41 * MBY;
constexpr unsigned OFF_SX = 42 * MBY;
constexpr unsigned SX_ST = 0;
constexpr unsigned SX_ROW = 512 * 1024;
constexpr unsigned SX_CAND = SX_ROW + 256 * 1024;
constexpr unsigned SX_GP = SX_CAND + 32 * 1024;
constexpr unsigned SX_YP = SX_GP + 224 * 1024;
constexpr unsigned SX_LAYER = SX_YP + 224 * 1024;
constexpr unsigned SX_QP = 2 * SX_LAYER;
constexpr unsigned SX_BYTES = SX_QP + 512 * 1024;
static_assert(SX_BYTES <= 6 * 256 * 128 * 16, "sentinel fill");
constexpr unsigned WS_BYTES = 48 * MBY, OOB = 0x80000000u;
constexpr unsigned SENT = 0xFFFFFFFFu;

constexpr int NS = 9, DD = 3, SLOTB = 12352, HALFB = 6176;
constexpr int L_XCH = 122880;
constexpr int L_SL = L_XCH + 8192;
constexpr int L_CAND = L_XCH + 32768;
constexpr int L_AB = L_XCH + 34816;
constexpr int L_FULL = 159744, L_FREE = L_FULL + 512;
constexpr int L_CS = L_FREE + 1024, L_PATCH = L_CS + 768;
constexpr int L_GU = L_PATCH + 768, L_TAG = L_GU + 256, L_WR = L_TAG + 256;
constexpr int L_X2F = L_WR + 128, L_X2P = L_X2F + 128;
constexpr int L_W = L_X2P + 128;
constexpr int LDS_BYTES = 163840;
static_assert(L_W + 128 <= LDS_BYTES && NS * SLOTB + 256 <= L_XCH && L_AB + 2048 <= L_FULL, "LDS map");

DI int fresh_lane() { int l; asm volatile("v_mbcnt_lo_u32_b32 %0, -1, 0\n\tv_mbcnt_hi_u32_b32 %0, -1, %0" : "=v"(l)); return l; }
#define LANE_VARS const int lane = fresh_lane(), r32 = lane & 31, hi = lane >> 5, tq = (lane & 15) >> 2, tp = lane & 3, tblk = (lane >> 4) & 1, \
  wrow = 4 * hi + tq, wsw = 4 * ((wrow >> 1) & 1), wch = 2 * tblk + (tp >> 1), c24 = r32 < 24 ? r32 : 23; \
  (void)r32; (void)hi; (void)wsw; (void)wch; (void)c24
DI int fresh_s(int v) { asm volatile("" : "+s"(v)); return v; }
#define PHASE_VARS const int tm = fresh_s(tm_), tn = fresh_s(tn_), wid = fresh_s(wid_), tile = tm * 4 + wid, hh = wid & 1, kh = wid >> 1; (void)tile; (void)hh; (void)kh; LANE_VARS
DI int kpos(int tm, int tn, int i) { return i == 0 ? tn : (tn + 1 + (1 * (3 - (tn & 3)) + i - 1) % 15) & 15; }
DI float bf2f(u16 v) { return __uint_as_float(((unsigned)v) << 16); }
DI unsigned pack2(float lo, float hi) { f32x2 v = {lo, hi}; return __builtin_bit_cast(unsigned, __builtin_convertvector(v, bf16x2)); }
DI float rbf(float f) { return __uint_as_float(pack2(f, 0.f) << 16); }
DI float lo16(unsigned u) { return __uint_as_float(u << 16); }
DI float hi16(unsigned u) { return __uint_as_float(u & 0xffff0000u); }
DI float opq(float r) { asm("" : "+v"(r)); return r; }
DI float fadd(float a, float b) { return opq(a + b); }
DI float fmul(float a, float b) { return opq(a * b); }
DI float ffma(float a, float b, float c) { return opq(__builtin_fmaf(a, b, c)); }
DI float U2F(unsigned u) { return __uint_as_float(u); }
DI float half_swap_sum(float v) {
  auto r = __builtin_amdgcn_permlane32_swap(__float_as_uint(v), __float_as_uint(v), false, false);
  return fadd(U2F(r[0]), U2F(r[1]));
}
DI float half_swap_max(float v) {
  auto r = __builtin_amdgcn_permlane32_swap(__float_as_uint(v), __float_as_uint(v), false, false);
  return fmaxf(__uint_as_float(r[0]), __uint_as_float(r[1]));
}
template <int CTRL> DI unsigned dpp(unsigned v) { return (unsigned)__builtin_amdgcn_update_dpp(0, (int)v, CTRL, 0xf, 0xf, false); }
DI float row_swap_sum(float v) {
  auto r = __builtin_amdgcn_permlane16_swap(__float_as_uint(v), __float_as_uint(v), false, false);
  return fadd(U2F(r[0]), U2F(r[1]));
}
DI float half_sum(float v) {
  v = fadd(v, U2F(dpp<0xB1>(__float_as_uint(v)))); v = fadd(v, U2F(dpp<0x4E>(__float_as_uint(v))));
  v = fadd(v, U2F(dpp<0x141>(__float_as_uint(v)))); v = fadd(v, U2F(dpp<0x140>(__float_as_uint(v))));
  return row_swap_sum(v);
}
DI float wave_sum(float v) { return half_swap_sum(half_sum(v)); }
DI unsigned half_max_u32(unsigned v) {
  v = max(v, dpp<0xB1>(v)); v = max(v, dpp<0x4E>(v)); v = max(v, dpp<0x141>(v)); v = max(v, dpp<0x140>(v));
  auto r = __builtin_amdgcn_permlane16_swap(v, v, false, false);
  return max(r[0], r[1]);
}
DI unsigned wave_max_u32(unsigned v) {
  v = half_max_u32(v);
  auto r = __builtin_amdgcn_permlane32_swap(v, v, false, false);
  return max(r[0], r[1]);
}
DI u32x4 fo_swap(u32x4 v) {
  auto a = __builtin_amdgcn_permlane32_swap(v[0], v[2], false, false);
  auto b = __builtin_amdgcn_permlane32_swap(v[1], v[3], false, false);
  u32x4 o; o[0] = a[0]; o[1] = b[0]; o[2] = a[1]; o[3] = b[1];
  return o;
}
DI void row_swap(u32x4& a, u32x4& b) {
#pragma unroll
  for (int e = 0; e < 4; ++e) { auto r = __builtin_amdgcn_permlane16_swap(a[e], b[e], false, false); a[e] = r[0]; b[e] = r[1]; }
}
DI void glds4(const void* sb, unsigned vo, unsigned lds_dst) {
  unsigned keep;
  asm volatile("s_mov_b32 %0, m0\n\ts_mov_b32 m0, %3\n\ts_nop 0\n\tglobal_load_lds_dword %1, %2\n\ts_mov_b32 m0, %0"
               : "=&s"(keep) : "v"(vo), "s"(sb), "s"(lds_dst) : "memory");
}
#define GL_ "global_load_lds_dwordx4 "
DI void glds_v4(const void* sb, const unsigned (&v)[4], unsigned m0v) {
  unsigned keep;
  asm volatile("s_mov_b32 %0, m0\n\ts_mov_b32 m0, %6\n\ts_nop 0\n\t" GL_ "%1, %5\n\t" GL_ "%2, %5 offset:1024\n\t" GL_ "%3, %5 offset:2048\n\t"
               GL_ "%4, %5 offset:3072\n\ts_mov_b32 m0, %0" : "=&s"(keep) : "v"(v[0]), "v"(v[1]), "v"(v[2]), "v"(v[3]), "s"(sb), "s"(m0v) : "memory");
}
#define GP_ "\n\ts_sleep %8\n\ts_waitcnt vmcnt(%7)\n\t"
template <int TH, int SL> DI void glds_v4t(const void* sb, const unsigned (&v)[4], unsigned m0v) {
  unsigned keep;
  asm volatile("s_mov_b32 %0, m0\n\ts_mov_b32 m0, %6\n\ts_nop 0\n\t" GL_ "%1, %5" GP_ GL_ "%2, %5 offset:1024" GP_ GL_ "%3, %5 offset:2048" GP_ GL_ "%4, %5 offset:3072" GP_ "s_mov_b32 m0, %0"
               : "=&s"(keep) : "v"(v[0]), "v"(v[1]), "v"(v[2]), "v"(v[3]), "s"(sb), "s"(m0v), "n"(TH), "n"(SL) : "memory");
}
DI void glds_v3(const void* sb, const unsigned (&v)[3], unsigned m0v) {
  unsigned keep;
  asm volatile("s_mov_b32 %0, m0\n\ts_mov_b32 m0, %5\n\ts_nop 0\n\t" GL_ "%1, %4\n\t" GL_ "%2, %4 offset:1024\n\t" GL_ "%3, %4 offset:2048\n\t"
               "s_mov_b32 m0, %0" : "=&s"(keep) : "v"(v[0]), "v"(v[1]), "v"(v[2]), "s"(sb), "s"(m0v) : "memory");
}
DI float dot2(unsigned a, unsigned b, float c) {
  return __builtin_amdgcn_fdot2_f32_bf16(__builtin_bit_cast(bf16x2, a), __builtin_bit_cast(bf16x2, b), c, false);
}
#define CODE_ADDR(sym, out) do { unsigned long long pc_; unsigned off_; \
  asm volatile("s_getpc_b64 %0\n1:\n\ts_mov_b32 %1, " #sym "-1b" : "=s"(pc_), "=s"(off_)); out = (const char*)(pc_ + (long long)(int)off_); } while (0)
DI void st16(void* p, u32x4 v) { asm volatile("global_store_dwordx4 %0, %1, off sc0 sc1\n\ts_nop 1" :: "v"(p), "v"(v) : "memory"); }
DI void ln_scalars(float mean, float var, float& mb, float& den) { mb = rbf(mean); den = rbf(__builtin_amdgcn_sqrtf(rbf(rbf(var) + 1e-5f))); }
DI unsigned ln_pair(unsigned v, float mb, float rd) {
  const unsigned c = pack2(lo16(v) - mb, hi16(v) - mb);
  return pack2(lo16(c) * rd, hi16(c) * rd);
}
DI unsigned ln_pair_s(unsigned v, float nmb, float rd) {
  const unsigned c = pack2(fadd(lo16(v), nmb), fadd(hi16(v), nmb));
  return pack2(fmul(lo16(c), rd), fmul(hi16(c), rd));
}
template <int N, int M> DI f32x4 sum4(const u32x4 (&v)[M]) {
  f32x4 t;
#pragma unroll
  for (int e = 0; e < 4; ++e) {
    t[e] = U2F(v[0][e]);
#pragma unroll
    for (int i = 1; i < N; ++i) t[e] = fadd(t[e], U2F(v[i][e]));
  }
  return t;
}
#define PERM_LO(x, y) __builtin_amdgcn_perm(y, x, 0x05040100u)
#define PERM_HI(x, y) __builtin_amdgcn_perm(y, x, 0x07060302u)
constexpr unsigned SPIN_CAP = 1u << 15;
DI unsigned nz(unsigned v) { return min(v, SENT - 1u); }
DI unsigned nzf(float v) { return nz(__float_as_uint(v)); }
DI unsigned mx4(unsigned m, const u32x4& v) { return max(max(max(m, v[0]), v[1]), max(v[2], v[3])); }
template <int N> DI bool none_sent(const u32x4 (&v)[N]) {
  unsigned m = 0u;
#pragma unroll
  for (int i = 0; i < N; ++i) m = mx4(m, v[i]);
  return m != SENT;
}
typedef __amdgpu_buffer_rsrc_t rsrc_t;
DI u32x4 ldg(rsrc_t rs, unsigned off) { return __builtin_amdgcn_raw_buffer_load_b128(rs, (int)off, 0, 16); }
DI void stg(rsrc_t rs, unsigned off, u32x4 v) { __builtin_amdgcn_raw_buffer_store_b128(v, rs, (int)off, 0, 16); }
DI void poll_pause(bool slow) { if (slow) __builtin_amdgcn_s_sleep(4); asm volatile("s_sleep 1" ::: "memory"); }
template <int N, class LD, class OKF> DI void poll_all(u32x4 (&out)[N], bool slow, LD ld, OKF okf) {
  for (unsigned spins = 0;; ++spins) {
    ld(out);
    SBAR();
    if (__all(okf(out)) || spins > SPIN_CAP) break;
    poll_pause(slow);
  }
}
DI void lds_set(unsigned addr, unsigned v) { asm volatile("s_waitcnt lgkmcnt(0)\n\tds_write_b32 %0, %1" :: "v"(addr), "v"(v) : "memory"); }
template <int SLEEP = 1> DI void lds_wait_ge(unsigned addr, unsigned need) {
  unsigned spin = 0x100000, v;
  asm volatile("1:\n\tds_read_b32 %0, %2\n\ts_sub_u32 %1, %1, 1\n\ts_waitcnt lgkmcnt(0)\n\ts_cbranch_scc1 2f\n\tv_cmp_gt_u32 vcc, %3, %0\n\ts_cbranch_vccz 2f\n\ts_sleep %4\n\ts_branch 1b\n2:"
               : "=&v"(v), "+&s"(spin) : "v"(addr), "s"(need), "n"(SLEEP) : "vcc", "scc", "memory");
}
DI s16x8 tr2(const char* p, int second) {
  s16x4 lo = __builtin_amdgcn_ds_read_tr16_b64_v4i16(LDS_PTR(s16x4, p)), hi = __builtin_amdgcn_ds_read_tr16_b64_v4i16(LDS_PTR(s16x4, p + second));
  return __builtin_shufflevector(lo, hi, 0, 1, 2, 3, 4, 5, 6, 7);
}
DI u32x16 cat4(const u32x4& a, const u32x4& b, const u32x4& c, const u32x4& d) {
  const u32x8 x = __builtin_shufflevector(a, b, 0, 1, 2, 3, 4, 5, 6, 7), y = __builtin_shufflevector(c, d, 0, 1, 2, 3, 4, 5, 6, 7);
  return __builtin_shufflevector(x, y, 0, 1, 2, 3, 4, 5, 6, 7, 8, 9, 10, 11, 12, 13, 14, 15);
}

DI unsigned stats_dst(unsigned sx, int tile, int tn, int lane) { return lane < 32 ? sx + SX_ST + tile * 8192 + tn * 512 + lane * 16 : OOB; }
DI void row_stats_sum(rsrc_t rs, unsigned sx, int tile, int lane, float& S, float& Q, float& Dd) {
  const unsigned exb = sx + SX_ST + tile * 8192 + (lane & 31) * 16;
  u32x4 pv[8];
  poll_all<8>(pv, false, [&](u32x4 (&v)[8]) {
#pragma unroll
    for (int i = 0; i < 8; ++i) v[i] = ldg(rs, exb + ((lane >> 5) * 8 + i) * 512);
  }, [&](const u32x4 (&v)[8]) { return none_sent(v); });
  S = 0.f; Q = 0.f; Dd = 0.f;
#pragma unroll
  for (int i = 0; i < 8; ++i) { S += __uint_as_float(pv[i][0]); Q += __uint_as_float(pv[i][1]); Dd += __uint_as_float(pv[i][2]); }
}
DI void row_stats_fin(rsrc_t rs, unsigned kdst, char* smem, int epoch, int tile, int lane, float S, float Q, float Dd, float wsum, const u32x4 (&x1p)[4],
                      float& mean, float& var, float& mb, float& rd) {
  S = half_swap_sum(S); Q = half_swap_sum(Q); Dd = half_swap_sum(Dd);
  mean = S * (1.0f / 1024.0f); var = fmaxf(Q * (1.0f / 1024.0f) - mean * mean, 0.f);
  float den;
  ln_scalars(mean, var, mb, den);
  rd = RCP(den);
  const unsigned rb = pack2((Dd - mb * wsum) * rd, 0.f) & 0xffffu;
  const unsigned mine = (((rb & 0x8000u) ? (~rb & 0xffffu) : (rb | 0x8000u)) << 16) | (unsigned)(2047 - (tile * 32 + (lane & 31)));
  const bool best = half_max_u32(mine) == mine;
  __builtin_amdgcn_raw_buffer_store_b32(mine, rs, (best && lane < 32) ? kdst : OOB, 0, 16);
  if (best) {
    char* c = smem + L_CAND + (tile & 3) * 144;
#pragma unroll
    for (int i = 0; i < 4; ++i) *reinterpret_cast<u32x4*>(c + (2 * i + (lane >> 5)) * 16) = x1p[i];
    *reinterpret_cast<u32x4*>(c + 128) = u32x4{mine, __float_as_uint(mb), __float_as_uint(rd), 0u};
  }
  lds_set((unsigned)(uintptr_t)smem + L_W + 32 + (tile & 3) * 4, epoch);
}

DI void attn_safe(rsrc_t rs, unsigned kvoff, const u32x8& q, const u32x4& ones, f32x16& o) {
  const s16x8 q0 = __builtin_bit_cast(s16x8, __builtin_shufflevector(q, q, 0, 1, 2, 3)), q1 = __builtin_bit_cast(s16x8, __builtin_shufflevector(q, q, 4, 5, 6, 7));
  f32x4 l = {0.f, 0.f, 0.f, 0.f};
#pragma unroll
  for (int e = 0; e < 16; ++e) o[e] = 0.f;
  float m = -INFINITY;
#pragma unroll 1
  for (int kt = 0; kt < 64; ++kt) {
    const unsigned p = kvoff + kt * 4096;
    const u32x4 k0 = ldg(rs, p), k1 = ldg(rs, p + 1024), v0 = ldg(rs, p + 2048), v1 = ldg(rs, p + 3072);
    f32x16 s = {};
    s = MFMA32(__builtin_bit_cast(s16x8, k0), q0, s);
    s = MFMA32(__builtin_bit_cast(s16x8, k1), q1, s);
    float mt = fmaxf(s[0], s[1]);
#pragma unroll
    for (int e = 2; e < 16; ++e) mt = fmaxf(mt, s[e]);
    const float mn = fmaxf(m, half_swap_max(mt));
    const float alpha = EX2(m - mn);
    m = mn;
#pragma unroll
    for (int e = 0; e < 16; ++e) { s[e] = EX2(s[e] - mn); o[e] *= alpha; }
    l[0] *= alpha;
    const bf16x16 pa = __builtin_bit_cast(bf16x16, __builtin_shufflevector(s, s, 0, 1, 2, 3, 4, 5, 6, 7));
    const bf16x16 pb = __builtin_bit_cast(bf16x16, __builtin_shufflevector(s, s, 8, 9, 10, 11, 12, 13, 14, 15));
    o = __builtin_amdgcn_smfmac_f32_32x32x32_bf16(__builtin_bit_cast(bf16x8, v0), pa, o, 0xdddd, 0, 0);
    l = __builtin_amdgcn_smfmac_f32_16x16x64_bf16(__builtin_bit_cast(bf16x8, ones), pa, l, 0xdddd, 0, 0);
    o = __builtin_amdgcn_smfmac_f32_32x32x32_bf16(__builtin_bit_cast(bf16x8, v1), pb, o, 0xdddd, 0, 0);
    l = __builtin_amdgcn_smfmac_f32_16x16x64_bf16(__builtin_bit_cast(bf16x8, ones), pb, l, 0xdddd, 0, 0);
  }
  o *= 1.0f / l[0];
}

extern "C" __global__ __launch_bounds__(512, 2) void mega_kernel(const u16* __restrict__ x0, const u16* __restrict__ wqkv, const u16* __restrict__ wo,
                                                              const u16* __restrict__ w_r, const u16* __restrict__ wg, const u16* __restrict__ wu,
                                                              const u16* __restrict__ wd, char* ws, u16* __restrict__ out) {
  __shared__ __attribute__((aligned(16))) char smem[LDS_BYTES];
  const int wid = __builtin_amdgcn_readfirstlane(threadIdx.x >> 6);
  STAMP_INIT;
  STAMPV(0);
  const int bid = blockIdx.x, xcd = bid & 7, loc = bid >> 3;
  const int tm = 8 * (xcd >> 2) + (loc >> 2), tn = 4 * (xcd & 3) + (loc & 3);
  const unsigned lbase = (unsigned)(uintptr_t)smem;
  const rsrc_t rs = __builtin_amdgcn_make_buffer_rsrc(ws, 0, (int)WS_BYTES, 0x00020000);
  volatile __attribute__((address_space(3))) unsigned* words = LDS_PTR(volatile unsigned, smem + L_W);
  u32x4 v[4];
  if (wid < 4)
    asm volatile("global_load_dwordx4 %0, %4, %5\n\tglobal_load_dwordx4 %1, %4, %5 offset:512\n\tglobal_load_dwordx4 %2, %4, %5 offset:1024\n\tglobal_load_dwordx4 %3, %4, %5 offset:1536"
                 : "=&v"(v[0]), "=&v"(v[1]), "=&v"(v[2]), "=&v"(v[3])
                 : "v"((threadIdx.x >> 5) * (D_ * 2) + (threadIdx.x & 31) * 16), "s"(x0 + (size_t)(tm * 128 + tn * 8) * D_) : "memory");
  else {
    asm volatile("" : "=v"(v[0]), "=v"(v[1]), "=v"(v[2]), "=v"(v[3]));
    if (wid == 6) {
      const char *c0, *c1;
      CODE_ADDR(mega_kernel, c0); CODE_ADDR(end_marker, c1);
      const unsigned o = (loc * 64 + (threadIdx.x & 63)) * 32;
      glds4(c0, (long long)o + 4 <= c1 - c0 ? o : 0u, lbase + NS * SLOTB);
    }
  }
  STAMPV(48);
  *reinterpret_cast<unsigned*>(smem + L_FULL + threadIdx.x * 4) = 0u;
  words[threadIdx.x & 31] = 0u;
  BAR_LGKM();
  if (wid < 4) {
    const int tid = threadIdx.x, lane = tid & 63, r32 = lane & 31, row = 2 * wid + (lane >> 5);
    char* sm = smem + L_XCH;
    unsigned dst = OFF_HF + (tm * 4 + (tn >> 2)) * TS + (tn & 3) * 128 + (tid >> 3) * 512 + (tid & 7) * 16;
    asm volatile("s_waitcnt vmcnt(0)" : "+v"(v[0]), "+v"(v[1]), "+v"(v[2]), "+v"(v[3]), "+v"(dst) :: "memory");
    STAMPV(49);
    float s = 0.f, s2 = 0.f;
#pragma unroll
    for (int i = 0; i < 4; ++i)
#pragma unroll
      for (int e = 0; e < 4; e += 2) { s = dot2(v[i][e], 0x3f803f80u, s); s2 = dot2(v[i][e + 1], 0x3f803f80u, s2); }
    const float mean = half_sum(s + s2) * (1.0f / 1024.0f);
    f32x2 m2 = {0.f, 0.f};
#pragma unroll
    for (int i = 0; i < 4; ++i)
#pragma unroll
      for (int e = 0; e < 4; ++e) { const f32x2 d = f32x2{lo16(v[i][e]), hi16(v[i][e])} - mean; m2 += d * d; }
    float mb, den;
    ln_scalars(mean, half_sum(m2[0] + m2[1]) * (1.0f / 1024.0f), mb, den);
    const float rd = 1.0f / den;
#pragma unroll
    for (int i = 0; i < 4; ++i) {
      char* p = sm + ((16 * i + (r32 >> 1)) * 2) * 144 + row * 16 + (r32 & 1) * 8;
      *reinterpret_cast<u32x2*>(p) = u32x2{ln_pair(v[i][0], mb, rd), ln_pair(v[i][1], mb, rd)};
      *reinterpret_cast<u32x2*>(p + 144) = u32x2{ln_pair(v[i][2], mb, rd), ln_pair(v[i][3], mb, rd)};
    }
    STAMPV(37);
    lds_set(lbase + L_W + 64 + 4 * wid, 1);
    lds_wait_ge<0>(lbase + L_W + 64 + 4 * (lane & 3), 1);
    STAMPV(50);
    u32x4 t[4];
#pragma unroll
    for (int i = 0; i < 4; ++i) t[i] = *reinterpret_cast<const u32x4*>(sm + (i * 32 + (tid >> 3)) * 144 + (tid & 7) * 16);
    SBAR();
#pragma unroll
    for (int i = 0; i < 4; ++i) stg(rs, dst + i * 16384, t[i]);
    STAMPV(51);
    DRAIN();
    lds_set(lbase + L_W + 64 + 4 * wid, 2);
    STAMPV(38);
  }
  const int lw = wid & 1;
  const char* wb = reinterpret_cast<const char*>(wqkv + (size_t)(16 * lw) * (3 * D_) + tn * 64);
  auto stage_lanes = [](unsigned (&wv)[3]) {
    const int lane = fresh_lane();
#pragma unroll
    for (int i = 0; i < 3; ++i) {
      const int li = lane + 64 * i, k = li / 24, cp = li % 24, c = cp ^ (4 * ((k >> 1) & 1));
      wv[i] = (unsigned)(k * (3 * D_ * 2) + (c >> 3) * (D_ * 2) + (c & 7) * 16 - i * 1024);
    }
  };
  auto stage = [&](const unsigned (&wv)[3], int s, int slot) {
    const char* p = wb + (size_t)(2 * kpos(tm, tn, s >> 1) + (s & 1)) * (32 * 3 * D_ * 2);
    glds_v3(p, wv, lbase + slot * SLOTB + lw * HALFB);
    glds_v3(p + 8 * (3 * D_ * 2), wv, lbase + slot * SLOTB + lw * HALFB + 3072);
  };
  auto exchange = [&](const u32x2& tag, unsigned line0, int me, bool bar = false) {
    const int lane = fresh_lane();
    __builtin_amdgcn_raw_buffer_store_b64(tag, rs, lane == 0 ? line0 + me * 128 : OOB, 0, 16);
    if (bar) BARRIER();
    JIT();
    unsigned po = lane < 16 ? line0 + lane * 128 : OOB;
    const u32x4 srd = {(unsigned)(uintptr_t)ws, (unsigned)((uintptr_t)ws >> 32) & 0xffffu, WS_BYTES, 0x00020000u};
    for (unsigned spins = 0;; ++spins) {
      u32x2 v = {0u, 0u};
      asm volatile("buffer_atomic_or_x2 %0, %1, %2, 0 offen sc0 sc1\n\ts_waitcnt vmcnt(0)" : "+v"(v) : "v"(po), "s"(srd) : "memory");
      po = ((v[0] == tag[0]) & (v[1] == tag[1])) ? OOB : po;
      if (__all(po == OOB) || spins > SPIN_CAP) break;
    }
  };
  if ((wid >> 1) == 2) {
    unsigned wv[3];
    stage_lanes(wv);
    const int r = tm & 7;
#pragma unroll 1
    for (int i = 0; i < NS; ++i) {
      const int s = i == 0 ? r : i <= r ? i - 1 : i;
      stage(wv, s, s);
      const int j = i - 1, full = j < 1 ? (j == 0 && r == 0) : j < r ? j : j + 1;
      asm volatile("s_waitcnt vmcnt(%2)\n\tds_write_b32 %0, %1" :: "v"(lbase + L_FULL + lw * 256), "v"(full), "n"(6 * 1) : "memory");
    }
    asm volatile("s_waitcnt vmcnt(0)\n\tds_write_b32 %0, %1" :: "v"(lbase + L_FULL + lw * 256), "v"(NS) : "memory");
    STAMPV(1);
  }
  if (wid == 7) {
    u32x2 tag = __builtin_amdgcn_raw_buffer_load_b64(rs, FL_EPOCH, 0, 16);
    tag[0] += 1u; tag[1] += 0x9E3779B9u;
    *reinterpret_cast<u32x2*>(smem + L_TAG) = tag;
    lds_wait_ge<0>(lbase + L_W + 64 + 4 * (fresh_lane() & 3), 2);
    exchange(tag, FL_X + tm * 2048, tn);
    lds_set(lbase + L_W + 24, 1);
  }
  if (wid >= 4) {
    auto wo_rows = [&](int g0, int n, int gs) {
      const int lane = fresh_lane();
      unsigned wov[4];
#pragma unroll
      for (int i = 0; i < 4; ++i) wov[i] = (unsigned)((8 * i + (lane >> 3)) * (D_ * 2) + ((lane & 7) ^ (4 * ((lane >> 4) & 1))) * 16 - i * 1024);
#pragma unroll 1
      for (int i = 0, k = (0 * (tm & 7)) % n; i < n; ++i, k = k + 1 == n ? 0 : k + 1) {
        const int g = g0 + gs * k;
        glds_v4t<3, 1>(reinterpret_cast<const char*>(wo) + (size_t)((((tn >> 2) + 1) * 256 + 32 * g) & 1023) * (D_ * 2) + tn * 128, wov, lbase + g * 4096);
      }
    };
    if (wid < 6) {
      const int lane = fresh_lane();
      unsigned wv[3];
      stage_lanes(wv);
      __builtin_amdgcn_s_setprio(3);
      const unsigned fra = lbase + L_FREE + (lane & 3) * 256, fla = lbase + L_FULL + lw * 256 + lane * 4;
      u32x4 pre[NS * 6];
#pragma unroll
      for (int i = 0; i < NS * 6; ++i) pre[i] = *reinterpret_cast<const u32x4*>(smem + lw * HALFB + lane * 16 + (i / 6) * SLOTB + (i % 6) * 1024);
#pragma unroll
      for (int i = 0; i < NS * 6; ++i) PIN(pre[i]);
#pragma unroll 1
      for (int L = 0; L < 2; ++L) {
        {
          constexpr unsigned STG = 32 * 3 * D_ * 2;
          unsigned off = (2 * kpos(tm, tn, NS >> 1) + (NS & 1)) * STG, dst = lbase + lw * HALFB, need = 32 * L + 3, cnt = 31 - NS, pub = 32 * L + NS - DD, keep, spin, t0, t1;
          asm volatile(
            "s_mov_b32 %[keep], m0\n\ts_branch 3f\n"
            "1:\n\ts_cbranch_vccnz 3f\n"
            "2:\n\ts_add_u32 s56, %[blo], %[off]\n\ts_addc_u32 s57, %[bhi], 0\n\ts_mov_b32 m0, %[dst]\n\ts_add_u32 s58, s56, %[half]\n\t"
            GL_ "%[w0], s[56:57]\n\ts_nop 7\n\t" GL_ "%[w1], s[56:57] offset:1024\n\ts_nop 7\n\t" GL_ "%[w2], s[56:57] offset:2048\n\ts_nop 7\n\t"
            "s_addc_u32 s59, s57, 0\n\ts_add_u32 m0, %[dst], 3072\n\ts_add_u32 %[off], %[off], %[stg]\n\t"
            GL_ "%[w0], s[58:59]\n\ts_nop 7\n\t" GL_ "%[w1], s[58:59] offset:1024\n\ts_nop 7\n\t" GL_ "%[w2], s[58:59] offset:2048\n\ts_nop 7\n\t"
            "ds_read_b32 %[t0], %[fra]\n\t"
            "s_cmp_eq_u32 %[off], %[wrap]\n\ts_cselect_b32 %[off], 0, %[off]\n\ts_cmp_eq_u32 %[off], %[own]\n\ts_cselect_b32 %[off], %[own2], %[off]\n\t"
            "s_add_u32 %[dst], %[dst], %[slot]\n\ts_cmp_eq_u32 %[dst], %[dend]\n\ts_cselect_b32 %[dst], %[dbeg], %[dst]\n\t"
            "v_add_u32 %[pub], 1, %[pub]\n\ts_add_u32 %[need], %[need], 1\n\tv_max_u32 %[t1], %[pmin], %[pub]\n\t"
            "s_waitcnt vmcnt(%[vm]) lgkmcnt(0)\n\tds_write_b32 %[fla], %[t1]\n\tv_cmp_gt_u32 vcc, %[need], %[t0]\n\t"
            "s_sub_u32 %[cnt], %[cnt], 1\n\ts_cbranch_scc0 1b\n\ts_branch 5f\n"
            "3:\n\ts_mov_b32 %[spin], 0x100000\n"
            "4:\n\tds_read_b32 %[t0], %[fra]\n\ts_sub_u32 %[spin], %[spin], 1\n\ts_waitcnt lgkmcnt(0)\n\ts_cbranch_scc1 2b\n\t"
            "v_cmp_gt_u32 vcc, %[need], %[t0]\n\ts_cbranch_vccz 2b\n\ts_sleep 1\n\ts_branch 4b\n"
            "5:\n\ts_mov_b32 m0, %[keep]"
            : [off] "+&s"(off), [dst] "+&s"(dst), [need] "+&s"(need), [cnt] "+&s"(cnt), [pub] "+&v"(pub), [keep] "=&s"(keep), [spin] "=&s"(spin), [t0] "=&v"(t0), [t1] "=&v"(t1)
            : [blo] "s"((unsigned)(uintptr_t)wb), [bhi] "s"((unsigned)((uintptr_t)wb >> 32)), [half] "s"(STG / 4), [stg] "s"(STG), [wrap] "s"(32 * STG), [own] "s"(2 * tn * STG),
              [own2] "s"(2 * ((tn + 1) & 15) * STG), [slot] "s"(SLOTB), [dbeg] "s"(lbase + lw * HALFB), [dend] "s"(lbase + lw * HALFB + NS * SLOTB), [pmin] "s"(32 * L + NS),
              [w0] "v"(wv[0]), [w1] "v"(wv[1]), [w2] "v"(wv[2]), [fra] "v"(fra), [fla] "v"(fla), [vm] "n"(DD * 6)
            : "s56", "s57", "s58", "s59", "vcc", "scc", "memory");
        }
#pragma unroll
        for (int d = DD - 1; d >= 0; --d) asm volatile("s_waitcnt vmcnt(%0)\n\tds_write_b32 %1, %2" :: "n"(d * 6), "v"(fla), "v"(32 * L + 32 - d) : "memory");
        u32x2 tag = *reinterpret_cast<const u32x2*>(smem + L_TAG);
        PIN(tag);
        if (lw == 0) {
          lds_wait_ge(lbase + L_W + 96 + 4 * (lane & 1), 1);
          lds_wait_ge<0>(lbase + L_W + 80 + 4 * (lane & 3), 2 * L + 1);
          exchange(tag, FL_KV + (L * 16 + tn) * 2048, tm, true);
          lds_wait_ge(fra, 32 * L + 32);
          lds_set(lbase + L_W + 16, L + 1);
        } else { BAR_LGKM(); lds_wait_ge<8>(lbase + L_W + 16, L + 1); }
        __builtin_amdgcn_s_sleep(24);
        wo_rows(lw, 15, 2);
        STAMPV(7 + 16 * L);
        DRAIN();
        const unsigned fo = FL_O + (L * 16 + tm) * 2048;
        if (lw) {
          lds_set(lbase + L_W + 112, 2 * L + 1);
          lds_wait_ge<4>(lbase + L_W + 48 + 4 * (lane & 3), L + 1);
          lds_wait_ge<0>(lbase + L_W + 80 + 4 * (lane & 3), 2 * L + 2);
          __builtin_amdgcn_raw_buffer_store_b64(tag, rs, lane == 0 ? fo + tn * 128 : OOB, 0, 16);
          JIT();
          wo_rows(30, 2, 1);
          DRAIN();
          lds_set(lbase + L_W + 112, 2 * L + 2);
        } else {
          lds_wait_ge<4>(lbase + L_W + 112, 2 * L + 1);
          lds_wait_ge<4>(lbase + L_W + 48 + 4 * (lane & 3), L + 1);
          unsigned po = lane < 16 ? fo + ((lane + 4 + (tn & 12)) & 15) * 128 : OOB;
          const u32x4 srd = {(unsigned)(uintptr_t)ws, (unsigned)((uintptr_t)ws >> 32) & 0xffffu, WS_BYTES, 0x00020000u};
          for (unsigned spins = 0;; ++spins) {
            u32x2 v = {0u, 0u};
            asm volatile("buffer_atomic_or_x2 %0, %1, %2, 0 offen sc0 sc1\n\ts_waitcnt vmcnt(0)" : "+v"(v) : "v"(po), "s"(srd) : "memory");
            po = ((v[0] == tag[0]) & (v[1] == tag[1])) ? OOB : po;
            unsigned m = (unsigned)__ballot(po == OOB);
            m &= m >> 1; m &= m >> 2;
            m = (m & 1u) | ((m >> 3) & 2u) | ((m >> 6) & 4u) | ((m >> 9) & 8u);
            if (m == 15u || spins > SPIN_CAP) break;
            words[5 + 2 * L] = words[28] == 2u * L + 2u ? m : m & 7u;
          }
          words[5 + 2 * L] = words[28] == 2u * L + 2u ? 15u : 7u;
          lds_wait_ge<0>(lbase + L_W + 112, 2 * L + 2);
          words[5 + 2 * L] = 15u;
          __builtin_amdgcn_raw_buffer_store_b64(tag, rs, (bid == 0 && lane == 0) ? FL_EPOCH : OOB, 0, 16);
        }
        BAR_ALL();
        if (L == 0) {
          char* d = smem + lw * HALFB + fresh_lane() * 16;
#pragma unroll
          for (int s = 0; s < NS; ++s) {
#pragma unroll
            for (int i = 0; i < 6; ++i) *reinterpret_cast<u32x4*>(d + s * SLOTB + i * 1024) = pre[s * 6 + i];
            lds_set(fla, 32 + s + 1);
          }
          STAMPV(2);
          lds_wait_ge<2>(lbase + L_W + 24, 2);
        }
      }
      STAMP_END;
      return;
    }
    const int a = wid - 6;
    const bool ffn = loc < 28;
    const int grp = xcd * 7 + (ffn ? loc >> 2 : 0), qa = (loc & 3) * 2 + a;
    const int kr = loc, cg = xcd;
    const unsigned wa = lbase + L_W;
    lds_wait_ge<2>(wa + 24, 1);
    __builtin_amdgcn_s_sleep(16);
    auto transpose8 = [](u32x4* v) {
      u32x4 t[8];
#pragma unroll
      for (int j = 0; j < 8; ++j)
#pragma unroll
        for (int e = 0; e < 4; ++e) t[j][e] = (j & 1) ? PERM_HI(v[2 * e][j >> 1], v[2 * e + 1][j >> 1]) : PERM_LO(v[2 * e][j >> 1], v[2 * e + 1][j >> 1]);
#pragma unroll
      for (int j = 0; j < 8; ++j) { v[j] = t[j]; PIN(v[j]); }
    };
    u32x4 gf[32];
    u32x4 wdf[16];
    auto wait_go = [&](int L) {
      __builtin_amdgcn_s_setprio(1);
      lds_wait_ge<8>(lbase + L_W + 16, L + 1);
      __builtin_amdgcn_s_sleep(24);
    };
    auto load_wd = [&](unsigned dv) {
      const rsrc_t rd = __builtin_amdgcn_make_buffer_rsrc(const_cast<u16*>(wd) + (size_t)grp * 64 * D_ + 128 * qa, 0, 64 * D_ * 2 - 256 * qa, 0x00020000);
#pragma unroll
      for (int i = 0; i < 16; ++i) {
        wdf[i] = __builtin_amdgcn_raw_buffer_load_b128(rd, dv, (32 * (i >> 3) + (i & 7)) * (D_ * 2), 0);
        asm volatile("s_sleep 2\n\ts_waitcnt vmcnt(2)" ::: "memory");
      }
    };
    {
      LANE_VARS;
      if (a == 0) {
        const u32x4 w0 = *reinterpret_cast<const u32x4*>(w_r + lane * 8), w1 = *reinterpret_cast<const u32x4*>(w_r + 512 + lane * 8);
        float t = 0.f;
#pragma unroll
        for (int e = 0; e < 4; ++e) t = fadd(fadd(t, fadd(lo16(w0[e]), hi16(w0[e]))), fadd(lo16(w1[e]), hi16(w1[e])));
        words[0] = __float_as_uint(wave_sum(t));
        if ((lane >> 3) == (tn & 7)) *reinterpret_cast<u32x4*>(smem + L_WR + (lane & 7) * 16) = tn < 8 ? w0 : w1;
      }
      const u16* p = wqkv + (size_t)(64 * tm + 32 * a + hi) * (3 * D_) + (c24 >> 3) * D_ + tn * 64 + (c24 & 7) * 8;
      float cs[8] = {0.f, 0.f, 0.f, 0.f, 0.f, 0.f, 0.f, 0.f};
      u32x4 v[16];
#pragma unroll
      for (int i = 0; i < 16; ++i) v[i] = *reinterpret_cast<const u32x4*>(p + (size_t)i * (2 * 3 * D_));
      SBAR();
      const u32x4 sv = {SENT, SENT, SENT, SENT};
#pragma unroll
      for (int i = 0; i < 6; ++i) {
        const unsigned o = ((i * 256 + bid) * 128 + a * 64 + lane) * 16;
        stg(rs, o < SX_BYTES ? OFF_SX + o : OOB, sv);
      }

#pragma unroll
      for (int i = 0; i < 16; ++i)
#pragma unroll
        for (int e = 0; e < 4; ++e) { cs[2 * e] = fadd(cs[2 * e], lo16(v[i][e])); cs[2 * e + 1] = fadd(cs[2 * e + 1], hi16(v[i][e])); }
      u32x4 o;
#pragma unroll
      for (int e = 0; e < 4; ++e) { const float x = half_swap_sum(cs[e]), y = half_swap_sum(cs[4 + e]); o[e] = __float_as_uint(hi ? y : x); }
      stg(rs, r32 < 24 ? OFF_CS + (tn * 32 + tm * 2 + a) * 768 + r32 * 32 + hi * 16 : OOB, o);
    }
    {
      DRAIN();
      lds_set(wa + 96 + 4 * a, 1);
      BAR_LGKM();
      LANE_VARS;
      const char* gp = reinterpret_cast<const char*>(((lane & 8) ? wu : wg) + (size_t)(128 * qa + 16 * hi + 4 * tblk) * F_ + 64 * grp + (lane & 7) * 8);
      unsigned dv = (lane >> 4) * (8 * D_ * 2) + (lane & 15) * 16;
      PIN(gp); PIN(dv);
      unsigned sv[3];
#pragma unroll
      for (int i = 0; i < 3; ++i) { const int li = lane + 64 * i, k = li / 24, c = li % 24; sv[i] = (unsigned)(k * (3 * D_ * 2) + (c >> 3) * (D_ * 2) + (c & 7) * 16 - i * 1024); PIN(sv[i]); }
      wait_go(0);
#pragma unroll
      for (int i = 0; i < 32; ++i) {
        gf[i] = *reinterpret_cast<const u32x4*>(gp + (size_t)(32 * (i >> 3) + 8 * ((i >> 2) & 1) + (i & 3)) * (F_ * 2));
        asm volatile("s_sleep 2\n\ts_waitcnt vmcnt(2)" ::: "memory");
      }
      load_wd(dv);
      STAMPV(7);
      if (1) __builtin_amdgcn_s_setprio(0);
      lds_wait_ge<4>(wa + 80 + 4 * (lane & 3), 2);
      __builtin_amdgcn_s_setprio(0);
#pragma unroll
      for (int g = 0; g < 4; ++g) glds_v3(wqkv + (size_t)(32 * kr + 8 * g) * (3 * D_) + (2 * cg + a) * 64, sv, lbase + L_SL + a * 12288 + g * 3072);
      transpose8(gf); transpose8(gf + 8); transpose8(gf + 16); transpose8(gf + 24);
      transpose8(wdf); transpose8(wdf + 8);
      {
        LANE_VARS;
        f32x4 t[2];
#pragma unroll
        for (int c = 0; c < 2; ++c) {
          u32x4 v[8];
#pragma unroll
          for (int i = 0; i < 8; ++i) v[i] = ldg(rs, OFF_CS + (tn * 32 + 16 * c + 2 * i + hi) * 768 + a * 384 + c24 * 16);
          SBAR();
          t[c] = sum4<8>(v);
          SBAR();
        }
#pragma unroll
        for (int e = 0; e < 4; ++e) t[0][e] = half_swap_sum(fadd(t[0][e], t[1][e]));
        if (lane < 24) *reinterpret_cast<f32x4*>(smem + L_CS + a * 384 + lane * 16) = t[0];
      }
      u32x2 tag = *reinterpret_cast<const u32x2*>(smem + L_TAG);
      PIN(tag);
      BAR_ALL();
      if (a) {
        lds_wait_ge<0>(wa + 64 + 4 * (lane & 3), 3);
        exchange(tag, FL_X + (16 + tm) * 2048, tn);
        lds_set(wa + 24, 2);
        STAMPV(12);
      }
    }
#pragma unroll 1
    for (int L = fresh_s(0);; ++L) {
      const unsigned sx = OFF_SX + L * SX_LAYER;
      STAMPV(1 + 16 * L);
      __builtin_amdgcn_s_setprio(1);
      u32x4 hf[4];
      unsigned key;
      { LANE_VARS;
      lds_wait_ge<0>(wa + 32 + 4 * tp, L + 1);
      if (a == 0) {
        const char* cp = smem + L_CAND + (r32 >> 3) * 144;
        const u32x4 c = *reinterpret_cast<const u32x4*>(cp + (lane & 7) * 16), rec = *reinterpret_cast<const u32x4*>(cp + 128);
        u32x4 o;
#pragma unroll
        for (int e = 0; e < 4; ++e) { unsigned h = ln_pair_s(c[e], -U2F(rec[1]), U2F(rec[2])); PIN(h); o[e] = nz(hi ? c[e] : h); }
        stg(rs, sx + SX_ROW + hi * 131072 + (tm * 4 + (r32 >> 3)) * 2048 + tn * 128 + (lane & 7) * 16, o);
        STAMPV(8 + 16 * L);
        lds_wait_ge<0>(wa + 104, L + 1);
        key = __builtin_amdgcn_readfirstlane(words[27]);
      } else {
        const u32x4 srd = {(unsigned)(uintptr_t)ws, (unsigned)((uintptr_t)ws >> 32) & 0xffffu, WS_BYTES, 0x00020000u};
        unsigned po = sx + SX_CAND + (tn * 16 + (lane >> 2)) * 128 + tp * 4;
        key = 0u;
        for (unsigned spins = 0;; ++spins) {
          unsigned v = 0u;
          asm volatile("buffer_atomic_or %0, %1, %2, 0 offen sc0 sc1\n\ts_waitcnt vmcnt(0)" : "+v"(v) : "v"(po), "s"(srd) : "memory");
          key = v == SENT ? key : max(key, v);
          po = v == SENT ? po : OOB;
          if (__all(po == OOB) || spins > SPIN_CAP) break;
        }
        key = __builtin_amdgcn_readfirstlane(wave_max_u32(key));
        words[27] = key;
        words[3] = 2047u - (key & 2047u);
        lds_set(wa + 104, L + 1);
        STAMPV(13 + 16 * L);
      }
      poll_all<4>(hf, false, [&](u32x4 (&t)[4]) {
#pragma unroll
        for (int i = 0; i < 4; ++i) t[i] = ldg(rs, sx + SX_ROW + ((2047 - (int)(key & 2047u)) >> 5) * 2048 + (16 * qa + 4 * i + (lane >> 4)) * 16);
      }, [&](const u32x4 (&t)[4]) { return none_sent(t); });
      if (L == 1) lds_set(wa + 4 + 4 * a, 2);
      }
      STAMPV(2 + 16 * L);
      f32x4 d[8];
      const f32x4 zero = {0.f, 0.f, 0.f, 0.f};
      SBAR();
      __builtin_amdgcn_s_setprio(3);
#pragma unroll
      for (int t = 0; t < 32; ++t) d[t & 7] = MFMA16(__builtin_bit_cast(s16x8, hf[t >> 3]), __builtin_bit_cast(s16x8, gf[t]), t < 8 ? zero : d[t & 7]);
      SBAR();
      __builtin_amdgcn_s_setprio(1);
      LANE_VARS;
      const int idx = 2047 - (int)(key & 2047u), win = idx >> 7;
      const bool owner = win == tm;
      auto pick2 = [&](const f32x4 (&d)[8]) {
        u32x2 o;
#pragma unroll
        for (int e = 0; e < 2; ++e) {
          const float x = tblk ? d[2 + e][0] : d[e][0], y = tblk ? d[6 + e][0] : d[4 + e][0];
          o[e] = nzf(hi ? y : x);
        }
        return o;
      };
      if (ffn) {
        const unsigned ppos = (lane & 15) * 32 + (lane >> 4) * 8;
        __builtin_amdgcn_raw_buffer_store_b64(pick2(d), rs, sx + SX_GP + (grp * 8 + qa) * 512 + ppos, 0, 16);
        STAMPV(9 + 16 * L);
        __builtin_amdgcn_s_sleep(16);
        u32x2 gu[8];
        for (unsigned spins = 0;; ++spins) {
#pragma unroll
          for (int i = 0; i < 8; ++i) gu[i] = __builtin_amdgcn_raw_buffer_load_b64(rs, sx + SX_GP + (grp * 8 + i) * 512 + lane * 8, 0, 16);
          SBAR();
          unsigned m = 0u;
#pragma unroll
          for (int i = 0; i < 8; ++i) m = max(max(m, gu[i][0]), gu[i][1]);
          if (__all(m != SENT) || spins > SPIN_CAP) break;
          poll_pause(L == 0);
        }
        STAMPV(10 + 16 * L);
        float t0 = opq(U2F(gu[0][0])), t1 = opq(U2F(gu[0][1]));
#pragma unroll
        for (int i = 1; i < 8; ++i) { t0 = fadd(t0, U2F(gu[i][0])); t1 = fadd(t1, U2F(gu[i][1])); }
        auto sw = __builtin_amdgcn_permlane32_swap(__float_as_uint(t0), __float_as_uint(t1), false, false);
        const float gv = rbf(U2F(sw[0])), uv = rbf(U2F(sw[1]));
        reinterpret_cast<u16*>(smem + L_GU + a * 128)[2 * r32 + hi] = (u16)pack2(fmul(rbf(fmul(gv, RCP(fadd(1.0f, __expf(-gv))))), uv), 0.f);
        asm volatile("s_waitcnt lgkmcnt(0)" ::: "memory");
        const s16x8 a0 = *reinterpret_cast<const s16x8*>(smem + L_GU + a * 128 + (lane >> 4) * 16), a1 = *reinterpret_cast<const s16x8*>(smem + L_GU + a * 128 + 64 + (lane >> 4) * 16);
        SBAR();
        __builtin_amdgcn_s_setprio(3);
#pragma unroll
        for (int t = 0; t < 16; ++t) d[t & 7] = MFMA16(t < 8 ? a0 : a1, __builtin_bit_cast(s16x8, wdf[t]), t < 8 ? zero : d[t & 7]);
        SBAR();
        __builtin_amdgcn_s_setprio(1);
        __builtin_amdgcn_raw_buffer_store_b64(pick2(d), rs, sx + SX_YP + grp * 4096 + qa * 512 + ppos, 0, 16);
      }
      STAMPV(3 + 16 * L);
      if (L == 1 && (cg != (kr & 7) || a)) { STAMP_END; __builtin_amdgcn_endpgm(); }
      s16x8 wpf[12];
#pragma unroll
      for (int t = 0; t < 12; ++t) wpf[t] = tr2(smem + L_SL + a * 12288 + (16 * hi + 4 * tblk + tq) * 384 + t * 32 + tp * 8, 8 * 384);
#pragma unroll
      for (int t = 0; t < 12; ++t) PIN(wpf[t]);
      if (L == 0 && !owner) lds_set(wa + 4 + 4 * a, 1);
      __builtin_amdgcn_s_sleep(9);
      if (L == 0 && !ffn) __builtin_amdgcn_s_sleep(64);
      const unsigned rk = key >> 16;
      float rsig = rbf(RCP(fadd(1.0f, __expf(-lo16((rk & 0x8000u) ? (rk & 0x7fffu) : (~rk & 0xffffu))))));
      PIN(rsig);
      const int n = 4 * (lane & 7) + 2 * tblk + hi;
      const int fo = ((n >> 4) * 2 + ((n >> 2) & 1)) * 8 + ((n >> 3) & 1) * 4 + (n & 3);
      unsigned odst = (L == 1 && !(lane & 8)) ? (idx * D_ + kr * 32 + n) * 2 : OOB;
      PIN(odst);
      u32x4 pv[8];
      poll_all<8>(pv, L == 0, [&](u32x4 (&t)[8]) {
#pragma unroll
        for (int i = 0; i < 7; ++i) t[i] = ldg(rs, sx + SX_YP + (8 * i + (lane >> 3)) * 4096 + kr * 128 + (lane & 7) * 16);
        t[7][0] = t[7][2] = __builtin_amdgcn_raw_buffer_load_b32(rs, sx + SX_ROW + (idx >> 5) * 2048 + kr * 64 + (fo >> 1) * 4, 0, 16);
        t[7][1] = t[7][3] = __builtin_amdgcn_raw_buffer_load_b32(rs, sx + SX_ROW + 131072 + (idx >> 5) * 2048 + kr * 64 + (fo >> 1) * 4, 0, 16);
      }, [&](const u32x4 (&t)[8]) { return none_sent(t); });
      const float hv = (fo & 1) ? hi16(pv[7][0]) : lo16(pv[7][0]), xv = (fo & 1) ? hi16(pv[7][1]) : lo16(pv[7][1]);
      STAMPV(4 + 16 * L);
      float ys;
      {
        const f32x4 t = sum4<7>(pv);
        auto p = __builtin_amdgcn_permlane32_swap(__float_as_uint(t[0]), __float_as_uint(t[1]), false, false);
        auto q = __builtin_amdgcn_permlane32_swap(__float_as_uint(t[2]), __float_as_uint(t[3]), false, false);
        auto r = __builtin_amdgcn_permlane16_swap(__float_as_uint(fadd(U2F(p[0]), U2F(p[1]))), __float_as_uint(fadd(U2F(q[0]), U2F(q[1]))), false, false);
        ys = fadd(U2F(r[0]), U2F(r[1]));
        ys = fadd(ys, U2F(dpp<0x128>(__float_as_uint(ys))));
      }
      const unsigned ob = pack2(fadd(xv, rbf(fadd(hv, rbf(fmul(rbf(ys), rsig))))), 0.f) & 0xffffu;
      __builtin_amdgcn_raw_buffer_store_b16((u16)ob, __builtin_amdgcn_make_buffer_rsrc(out, 0, 2048 * D_ * 2, 0x00020000), odst, 0, 0);
      if (L == 1) {
        STAMPV(5 + 16 * L);
        STAMP_END;
        __builtin_amdgcn_endpgm();
      }
      reinterpret_cast<u16*>(smem + L_X2F + a * 64)[fo] = (u16)ob;
      asm volatile("s_waitcnt lgkmcnt(0)" ::: "memory");
      {
        const s16x8 xa = *reinterpret_cast<const s16x8*>(smem + L_X2F + a * 64 + (lane >> 4) * 16);
        const u32x4 xf = *reinterpret_cast<const u32x4*>(smem + L_X2F + a * 64 + tp * 16);
        f32x4 z[12];
        SBAR();
        __builtin_amdgcn_s_setprio(3);
#pragma unroll
        for (int t = 0; t < 12; ++t) z[t] = MFMA16(xa, wpf[t], zero);
        SBAR();
        __builtin_amdgcn_s_setprio(1);
        const float x2 = lo16(ob), s1 = fmul(wave_sum(x2), 0.5f), s2 = fmul(wave_sum(fmul(x2, x2)), 0.5f);
        u32x4 o;
#pragma unroll
        for (int e = 0; e < 4; ++e) {
          const unsigned sp = lane < 52 ? (e == 0 ? __float_as_uint(s1) : __float_as_uint(s2)) : xf[e];
          const unsigned x = __float_as_uint(tblk ? z[4 + e][0] : z[e][0]), y = tblk ? sp : __float_as_uint(z[8 + e][0]);
          o[e] = nz(hi ? y : x);
        }
        const unsigned pos = lane < 24 ? lane * 16 : lane < 48 ? lane * 16 + 128 : lane < 52 ? (lane & 1) * 512 + 384 : 400 + tp * 16;
        stg(rs, lane < 56 ? OFF_SX + SX_QP + ((2 * cg + a) * 32 + kr) * 1024 + pos : OOB, o);
      }
      STAMPV(5 + 16 * L);
      if (owner) {
        __builtin_amdgcn_s_sleep(13);
        const int c25 = r32 < 25 ? r32 : 24, pl = 24 * a + c24, col = 64 * (pl >> 4) + (pl & 15);
        const int pfo = (col & ~15) + ((col >> 2) & 1) * 16 + ((col >> 3) & 1) * 8 + (col & 3);
        float cs[4];
#pragma unroll
        for (int e = 0; e < 4; ++e) { cs[e] = reinterpret_cast<const float*>(smem + L_CS)[col + 16 * e]; PIN(cs[e]); }
        u32x4 qv[17];
        poll_all<17>(qv, true, [&](u32x4 (&t)[17]) {
#pragma unroll
          for (int i = 0; i < 16; ++i) t[i] = ldg(rs, OFF_SX + SX_QP + (tn * 32 + 2 * i + hi) * 1024 + a * 512 + c25 * 16);
          t[16] = ldg(rs, OFF_SX + SX_QP + (tn * 34 + hi) * 1024 + 400 + tp * 16);
        }, [&](const u32x4 (&t)[17]) { return none_sent(t); });
        *reinterpret_cast<u32x4*>(smem + L_X2P + hi * 64 + tp * 16) = qv[16];
        f32x4 t = sum4<16>(qv);
#pragma unroll
        for (int e = 0; e < 4; ++e) t[e] = half_swap_sum(t[e]);
        const float mean = fmul(U2F(__builtin_amdgcn_readlane(__float_as_uint(t[0]), 24)), 1.0f / 1024.0f);
        const float var = fmaxf(ffma(-mean, mean, fmul(U2F(__builtin_amdgcn_readlane(__float_as_uint(t[1]), 24)), 1.0f / 1024.0f)), 0.f);
        float mb, den;
        ln_scalars(mean, var, mb, den);
        const float rd = RCP(den);
        if (lane < 24) {
#pragma unroll
          for (int e = 0; e < 4; ++e) reinterpret_cast<u16*>(smem + L_PATCH)[pfo + 32 * (e >> 1) + 4 * (e & 1)] = (u16)pack2(fmul(ffma(-mb, cs[e], t[e]), pl < 16 ? fmul(rd, QS) : rd), 0.f);
        }
        lds_set(wa + 4 + 4 * a, L + 1);
      }
      STAMPV(6 + 16 * L);
      BAR_ALL();
      unsigned dv = fresh_lane();
      dv = (dv >> 4) * (8 * D_ * 2) + (dv & 15) * 16;
      PIN(dv);
      wait_go(1);
      load_wd(dv);
      STAMPV(23);
      if (1) __builtin_amdgcn_s_setprio(0);
      lds_wait_ge<4>(wa + 80 + 4 * (fresh_lane() & 3), 4);
      __builtin_amdgcn_s_setprio(0);
      transpose8(wdf); transpose8(wdf + 8);
      BARRIER();
    }
  }
  const int tm_ = tm, tn_ = tn, wid_ = wid;
#define KSKIP "{s49}"(tn * 4096), "{s50}"(((tn + 1) & 15) * 4096)
  const u32x4 srd = {(unsigned)(uintptr_t)ws, (unsigned)((uintptr_t)ws >> 32) & 0xffffu, WS_BYTES, 0x00020000u};
  u32x4 x1p[4];
  asm volatile("" : "=v"(x1p[0]), "=v"(x1p[1]), "=v"(x1p[2]), "=v"(x1p[3]));
#pragma unroll 1
  for (int L = 0; L < 2; ++L) {
    const unsigned sx = OFF_SX + L * SX_LAYER;
    u32x8 q[4];
    u32x16 kva, kvb;
    f32x16 oz[5];
    {
      f32x16 acc[6];
      u32x16 stA, stB, xr1, xr2, xr3;
      u32x8 xra, xrb;
      u32x4 stC;
      asm volatile("" : "={v[112:127]}"(stB), "={v[128:131]}"(stC));
      { PHASE_VARS;
      stB[8] = (L ? OFF_X1 : OFF_HF) + tile * TS + tblk * 1024 + (hi * 32 + (lane & 15)) * 16;
      stB[9] = lbase + tblk * HALFB + wrow * 384 + (((4 * hh + (tp >> 1)) ^ wsw) << 4) + 8 * (tp & 1);
      stB[10] = lbase + tblk * HALFB + wrow * 384 + (((4 * (hh ^ 1) + (tp >> 1)) ^ wsw) << 4) + 8 * (tp & 1);
      stB[13] = lbase + L_FULL + (lane & 1) * 256;
      stB[15] = lbase + L_FREE + wid * 256 + lane * 4;
      stC[0] = 32 * L + 2; }
      unsigned soff, slot;
      if (L == 0) {
        lds_wait_ge<0>(lbase + L_W + 24, 1);
        STAMPV(39);
        soff = tn * 4096;
        asm volatile(
          "v_add_u32 v131, s44, v120\n\t"
          "buffer_load_dwordx4 v[132:135], v131, s[40:43], 0 offen offset:0 sc1\n\t"
          "buffer_load_dwordx4 v[136:139], v131, s[40:43], 0 offen offset:256 sc1\n\t"
          "buffer_load_dwordx4 v[140:143], v131, s[40:43], 0 offen offset:2048 sc1\n\t"
          "buffer_load_dwordx4 v[144:147], v131, s[40:43], 0 offen offset:2304 sc1\n\t"
          "s_mov_b32 s44, s51\n\t"
          "v_add_u32 v131, s44, v120\n\t"
          "buffer_load_dwordx4 v[148:151], v131, s[40:43], 0 offen offset:0 sc1\n\t"
          "buffer_load_dwordx4 v[152:155], v131, s[40:43], 0 offen offset:256 sc1\n\t"
          "buffer_load_dwordx4 v[156:159], v131, s[40:43], 0 offen offset:2048 sc1\n\t"
          "buffer_load_dwordx4 v[160:163], v131, s[40:43], 0 offen offset:2304 sc1\n\t"
          "s_addk_i32 s44, 0x1000\n\t"
          "s_bitset0_b32 s44, 16\n\t"
          "s_cmp_eq_u32 s44, s49\n\t"
          "s_cselect_b32 s44, s50, s44\n\t"
          "v_add_u32 v131, s44, v120\n\t"
          "buffer_load_dwordx4 v[164:167], v131, s[40:43], 0 offen offset:0 sc1\n\t"
          "buffer_load_dwordx4 v[168:171], v131, s[40:43], 0 offen offset:256 sc1\n\t"
          "buffer_load_dwordx4 v[172:175], v131, s[40:43], 0 offen offset:2048 sc1\n\t"
          "buffer_load_dwordx4 v[176:179], v131, s[40:43], 0 offen offset:2304 sc1\n\t"
          "s_addk_i32 s44, 0x1000\n\t"
          "s_bitset0_b32 s44, 16\n\t"
          "s_cmp_eq_u32 s44, s49\n\t"
          "s_cselect_b32 s44, s50, s44\n\t"
          "v_add_u32 v131, s44, v120\n\t"
          "buffer_load_dwordx4 v[180:183], v131, s[40:43], 0 offen offset:0 sc1\n\t"
          "buffer_load_dwordx4 v[184:187], v131, s[40:43], 0 offen offset:256 sc1\n\t"
          "buffer_load_dwordx4 v[188:191], v131, s[40:43], 0 offen offset:2048 sc1\n\t"
          "buffer_load_dwordx4 v[192:195], v131, s[40:43], 0 offen offset:2304 sc1\n\t"
          : "={v[132:139]}"(xra), "={v[140:147]}"(xrb), "={v[148:163]}"(xr1), "={v[164:179]}"(xr2), "={v[180:195]}"(xr3), "+{s44}"(soff), "+{v[128:131]}"(stC) : "{v[112:127]}"(stB), "{s[40:43]}"(srd), KSKIP, "{s51}"(kpos(tm, tn, 1) * 4096) : "scc", "memory");
      } else {
        u32x4 t[4] = {x1p[0], x1p[1], x1p[2], x1p[3]};
        row_swap(t[0], t[1]); row_swap(t[2], t[3]);
        xra = __builtin_shufflevector(t[0], t[1], 0, 1, 2, 3, 4, 5, 6, 7); xrb = __builtin_shufflevector(t[2], t[3], 0, 1, 2, 3, 4, 5, 6, 7);
        asm volatile("" : "={v[148:163]}"(xr1), "={v[164:179]}"(xr2), "={v[180:195]}"(xr3));
      }
      STAMPV(1 + 16 * L);
      asm volatile(
        "v_mov_b32 v123, v121\n\t"
        "v_mov_b32 v124, v122\n\t"
        "s_mov_b64 vcc, -1\n\t"
        "s_cbranch_vccnz 10f\n\t"
        "11:\n\t"
        "s_movk_i32 s46, 12352\n\t"
        "s_mov_b32 s47, 111168\n\t"
        "ds_read_b64_tr_b16 v[96:97], v123 offset:256\n\t"
        "ds_read_b64_tr_b16 v[98:99], v123 offset:3328\n\t"
        "ds_read_b64_tr_b16 v[100:101], v123 offset:288\n\t"
        "ds_read_b64_tr_b16 v[102:103], v123 offset:3360\n\t"
        "ds_read_b64_tr_b16 v[104:105], v124 offset:256\n\t"
        "ds_read_b64_tr_b16 v[106:107], v124 offset:3328\n\t"
        "ds_write_b32 v127, v128\n\t"
        "ds_read_b64_tr_b16 v[108:109], v124 offset:288\n\t"
        "ds_read_b64_tr_b16 v[110:111], v124 offset:3360\n\t"
        "ds_read_b64_tr_b16 v[112:113], v123 offset:128\n\t"
        "ds_read_b64_tr_b16 v[114:115], v123 offset:3200\n\t"
        "v_add_u32 v128, 2, v128\n\t"
        "s_waitcnt vmcnt(14) lgkmcnt(7)\n\t"
        "v_mfma_f32_16x16x32_bf16 v[64:67], v[132:135], v[96:99], 0\n\t"
        "ds_read_b64_tr_b16 v[116:117], v123 offset:160\n\t"
        "v_mfma_f32_16x16x32_bf16 v[72:75], v[136:139], v[96:99], 0\n\t"
        "ds_read_b64_tr_b16 v[118:119], v123 offset:3232\n\t"
        "v_mfma_f32_16x16x32_bf16 v[68:71], v[132:135], v[100:103], 0\n\t"
        "ds_read_b64_tr_b16 v[96:97], v124 offset:128\n\t"
        "v_mfma_f32_16x16x32_bf16 v[76:79], v[136:139], v[100:103], 0\n\t"
        "ds_read_b64_tr_b16 v[98:99], v124 offset:3200\n\t"
        "s_waitcnt lgkmcnt(6)\n\t"
        "v_mfma_f32_16x16x32_bf16 v[80:83], v[132:135], v[104:107], 0\n\t"
        "ds_read_b64_tr_b16 v[100:101], v124 offset:160\n\t"
        "v_mfma_f32_16x16x32_bf16 v[88:91], v[136:139], v[104:107], 0\n\t"
        "ds_read_b64_tr_b16 v[102:103], v124 offset:3232\n\t"
        "v_mfma_f32_16x16x32_bf16 v[84:87], v[132:135], v[108:111], 0\n\t"
        "ds_read_b64_tr_b16 v[104:105], v123 offset:0\n\t"
        "v_add_u32 v129, s46, v121\n\t"
        "v_mfma_f32_16x16x32_bf16 v[92:95], v[136:139], v[108:111], 0\n\t"
        "ds_read_b64_tr_b16 v[106:107], v123 offset:3072\n\t"
        "s_waitcnt lgkmcnt(6)\n\t"
        "v_mfma_f32_16x16x32_bf16 v[32:35], v[112:115], v[132:135], 0\n\t"
        "ds_read_b64_tr_b16 v[108:109], v123 offset:32\n\t"
        "v_mfma_f32_16x16x32_bf16 v[40:43], v[112:115], v[136:139], 0\n\t"
        "ds_read_b64_tr_b16 v[110:111], v123 offset:3104\n\t"
        "v_add_u32 v130, s46, v122\n\t"
        "v_mfma_f32_16x16x32_bf16 v[36:39], v[116:119], v[132:135], 0\n\t"
        "ds_read_b64_tr_b16 v[112:113], v124 offset:0\n\t"
        "v_mfma_f32_16x16x32_bf16 v[44:47], v[116:119], v[136:139], 0\n\t"
        "ds_read_b64_tr_b16 v[114:115], v124 offset:3072\n\t"
        "s_waitcnt lgkmcnt(6)\n\t"
        "v_mfma_f32_16x16x32_bf16 v[48:51], v[96:99], v[132:135], 0\n\t"
        "ds_read_b64_tr_b16 v[116:117], v124 offset:32\n\t"
        "v_mfma_f32_16x16x32_bf16 v[56:59], v[96:99], v[136:139], 0\n\t"
        "ds_read_b64_tr_b16 v[118:119], v124 offset:3104\n\t"
        "v_mfma_f32_16x16x32_bf16 v[52:55], v[100:103], v[132:135], 0\n\t"
        "ds_read_b64_tr_b16 v[96:97], v129 offset:256\n\t"
        "s_addk_i32 s46, 12352\n\t"
        "v_mfma_f32_16x16x32_bf16 v[60:63], v[100:103], v[136:139], 0\n\t"
        "ds_read_b64_tr_b16 v[98:99], v129 offset:3328\n\t"
        "s_waitcnt lgkmcnt(6)\n\t"
        "v_mfma_f32_16x16x32_bf16 v[0:3], v[104:107], v[132:135], 0\n\t"
        "ds_read_b64_tr_b16 v[100:101], v129 offset:288\n\t"
        "s_cmp_eq_u32 s46, s47\n\t"
        "v_mfma_f32_16x16x32_bf16 v[8:11], v[104:107], v[136:139], 0\n\t"
        "ds_read_b64_tr_b16 v[102:103], v129 offset:3360\n\t"
        "s_cselect_b32 s46, 0, s46\n\t"
        "v_mfma_f32_16x16x32_bf16 v[4:7], v[108:111], v[132:135], 0\n\t"
        "ds_read_b64_tr_b16 v[104:105], v130 offset:256\n\t"
        "v_mfma_f32_16x16x32_bf16 v[12:15], v[108:111], v[136:139], 0\n\t"
        "ds_read_b64_tr_b16 v[106:107], v130 offset:3328\n\t"
        "s_waitcnt lgkmcnt(6)\n\t"
        "v_mfma_f32_16x16x32_bf16 v[16:19], v[112:115], v[132:135], 0\n\t"
        "ds_read_b64_tr_b16 v[108:109], v130 offset:288\n\t"
        "v_mfma_f32_16x16x32_bf16 v[24:27], v[112:115], v[136:139], 0\n\t"
        "ds_read_b64_tr_b16 v[110:111], v130 offset:3360\n\t"
        "v_mfma_f32_16x16x32_bf16 v[20:23], v[116:119], v[132:135], 0\n\t"
        "ds_read_b64_tr_b16 v[112:113], v129 offset:128\n\t"
        "v_mfma_f32_16x16x32_bf16 v[28:31], v[116:119], v[136:139], 0\n\t"
        "ds_read_b64_tr_b16 v[114:115], v129 offset:3200\n\t"
        "s_waitcnt lgkmcnt(0)\n\t"
        "s_nop 7\n\t"
        "s_branch 9f\n\t"
        "10:\n\t"
        "s_mov_b32 s48, 0x10000\n\t"
        "8:\n\t"
        "ds_read_b32 v126, v125\n\t"
        "s_sub_u32 s48, s48, 1\n\t"
        "s_waitcnt lgkmcnt(0)\n\t"
        "s_cbranch_scc1 11b\n\t"
        "v_cmp_gt_u32 vcc, v128, v126\n\t"
        "s_cbranch_vccnz 8b\n\t"
        "s_branch 11b\n\t"
        "9:\n\t"
        : "={v[0:15]}"(acc[0]), "={v[16:31]}"(acc[1]), "={v[32:47]}"(acc[2]), "={v[48:63]}"(acc[3]), "={v[64:79]}"(acc[4]), "={v[80:95]}"(acc[5]),
          "={v[96:111]}"(stA), "+{v[112:127]}"(stB), "+{v[128:131]}"(stC), "={s46}"(slot), "+{v[140:147]}"(xrb)
        : "{v[132:139]}"(xra) : "s47", "s48", "vcc", "scc", "memory");
      STAMPV(2 + 16 * L);
      if (L == 1) {
        PHASE_VARS;
        float S, Q, Dd;
        __builtin_amdgcn_s_sleep(0);
        row_stats_sum(rs, OFF_SX, tile, lane, S, Q, Dd);
        STAMPV(36);
        auto request_x = [&]() {
          lds_wait_ge<0>(lbase + L_W + 24, 2);
          soff = kpos(tm, tn, 1) * 4096;
          asm volatile(
          "v_add_u32 v131, s44, v120\n\t"
          "buffer_load_dwordx4 v[148:151], v131, s[40:43], 0 offen offset:0 sc1\n\t"
          "buffer_load_dwordx4 v[152:155], v131, s[40:43], 0 offen offset:256 sc1\n\t"
          "buffer_load_dwordx4 v[156:159], v131, s[40:43], 0 offen offset:2048 sc1\n\t"
          "buffer_load_dwordx4 v[160:163], v131, s[40:43], 0 offen offset:2304 sc1\n\t"
          "s_addk_i32 s44, 0x1000\n\t"
          "s_bitset0_b32 s44, 16\n\t"
          "s_cmp_eq_u32 s44, s49\n\t"
          "s_cselect_b32 s44, s50, s44\n\t"
          "v_add_u32 v131, s44, v120\n\t"
          "buffer_load_dwordx4 v[164:167], v131, s[40:43], 0 offen offset:0 sc1\n\t"
          "buffer_load_dwordx4 v[168:171], v131, s[40:43], 0 offen offset:256 sc1\n\t"
          "buffer_load_dwordx4 v[172:175], v131, s[40:43], 0 offen offset:2048 sc1\n\t"
          "buffer_load_dwordx4 v[176:179], v131, s[40:43], 0 offen offset:2304 sc1\n\t"
          "s_addk_i32 s44, 0x1000\n\t"
          "s_bitset0_b32 s44, 16\n\t"
          "s_cmp_eq_u32 s44, s49\n\t"
          "s_cselect_b32 s44, s50, s44\n\t"
          "v_add_u32 v131, s44, v120\n\t"
          "buffer_load_dwordx4 v[180:183], v131, s[40:43], 0 offen offset:0 sc1\n\t"
          "buffer_load_dwordx4 v[184:187], v131, s[40:43], 0 offen offset:256 sc1\n\t"
          "buffer_load_dwordx4 v[188:191], v131, s[40:43], 0 offen offset:2048 sc1\n\t"
          "buffer_load_dwordx4 v[192:195], v131, s[40:43], 0 offen offset:2304 sc1\n\t"
            : "={v[148:163]}"(xr1), "={v[164:179]}"(xr2), "={v[180:195]}"(xr3), "+{s44}"(soff), "+{v[128:131]}"(stC) : "{v[112:127]}"(stB), "{s[40:43]}"(srd), KSKIP : "scc", "memory");
          SBAR();
        };
        float mean, var, mb1, rd1, mb2, den2;
        row_stats_fin(rs, OFF_SX + SX_CAND + (tn * 16 + tm) * 128 + wid * 4, smem, 1, tile, lane, S, Q, Dd, U2F(words[0]), x1p, mean, var, mb1, rd1);
        ln_scalars(mean + (mean - mb1) * rd1, var * (1.0f + rd1) * (1.0f + rd1), mb2, den2);
        const float rd2 = RCP(den2);
        const float al = (1.0f + rd1) * rd2;
        const float be = -(mb1 * rd1 + mb2) * RCP(1.0f + rd1);
        reinterpret_cast<float*>(smem + L_AB)[wid * 64 + lane] = hi ? be : al;
        reinterpret_cast<float*>(smem + L_AB)[256 + wid * 64 + lane] = hi ? 1.0f + rd1 : -mb1 * rd1;
        const float* ab = reinterpret_cast<const float*>(smem + L_AB) + wid * 64 + 32;
        const float bt[2] = {ab[lane & 15], ab[16 + (lane & 15)]};
#pragma unroll
        for (int jj = 0; jj < 2; ++jj) {
          if (jj == 0) request_x();
          f32x4 cs[2][2], bv[2];
          float cv[2];
#pragma unroll
          for (int i = 0; i < 2; ++i) {
            const float* p = reinterpret_cast<const float*>(smem + L_CS) + (jj ^ hh) * 32 + 16 * i;
            bv[i] = *reinterpret_cast<const f32x4*>(ab + 16 * i + 4 * (lane >> 4));
            cs[0][i] = *reinterpret_cast<const f32x4*>(p + 4 * (lane >> 4)); cs[1][i] = *reinterpret_cast<const f32x4*>(p + 64 + 4 * (lane >> 4));
            cv[i] = p[128 + (lane & 15)];
          }
          SBAR();
#pragma unroll
          for (int j = 0; j < 3; ++j)
#pragma unroll
            for (int e = 0; e < 16; ++e) acc[2 * j + jj][e] += j < 2 ? bt[e >> 3] * cs[j & 1][(e >> 2) & 1][e & 3] : bv[e >> 3][e & 3] * cv[(e >> 2) & 1];
          SBAR();
        }
        if (0 == 2) request_x();
      }
      unsigned ep[3];
      f32x2 al2;
      { PHASE_VARS;
        float* ab = reinterpret_cast<float*>(smem + L_AB) + wid * 64;
        if (L == 0) ab[lane] = 1.0f;
        al2 = f32x2{ab[lane & 15], ab[16 + (lane & 15)]};
        const unsigned dst = OFF_KV + L * (10 * MBY) + tn * 2 * HS + tile * 4096 + lane * 16;
        ep[0] = dst + hh * HS; ep[1] = dst + (hh ^ 1) * HS; ep[2] = lbase + L_AB + wid * 256 + (lane >> 4) * 16; }
      STAMPV(3 + 16 * L);
      soff = kpos(tm, tn, 1 + 2) * 4096;
      asm volatile(
        "s_mov_b32 s47, 111168\n\t"
        "v_add_u32 v131, s44, v120\n\t"
        "s_waitcnt vmcnt(12) lgkmcnt(6)\n\t"
        "v_mfma_f32_16x16x32_bf16 v[64:67], v[140:143], v[96:99], v[64:67]\n\t"
        "ds_read_b64_tr_b16 v[116:117], v129 offset:160\n\t"
        "v_mfma_f32_16x16x32_bf16 v[72:75], v[144:147], v[96:99], v[72:75]\n\t"
        "ds_read_b64_tr_b16 v[118:119], v129 offset:3232\n\t"
        "v_mfma_f32_16x16x32_bf16 v[68:71], v[140:143], v[100:103], v[68:71]\n\t"
        "ds_read_b64_tr_b16 v[96:97], v130 offset:128\n\t"
        "ds_read_b32 v126, v125\n\t"
        "v_mfma_f32_16x16x32_bf16 v[76:79], v[144:147], v[100:103], v[76:79]\n\t"
        "ds_read_b64_tr_b16 v[98:99], v130 offset:3200\n\t"
        "s_waitcnt lgkmcnt(7)\n\t"
        "v_mfma_f32_16x16x32_bf16 v[80:83], v[140:143], v[104:107], v[80:83]\n\t"
        "ds_read_b64_tr_b16 v[100:101], v130 offset:160\n\t"
        "v_mfma_f32_16x16x32_bf16 v[88:91], v[144:147], v[104:107], v[88:91]\n\t"
        "ds_read_b64_tr_b16 v[102:103], v130 offset:3232\n\t"
        "v_mfma_f32_16x16x32_bf16 v[84:87], v[140:143], v[108:111], v[84:87]\n\t"
        "ds_read_b64_tr_b16 v[104:105], v129 offset:0\n\t"
        "v_add_u32 v123, s46, v121\n\t"
        "v_mfma_f32_16x16x32_bf16 v[92:95], v[144:147], v[108:111], v[92:95]\n\t"
        "ds_read_b64_tr_b16 v[106:107], v129 offset:3072\n\t"
        "s_waitcnt lgkmcnt(5)\n\t"
        "v_mfma_f32_16x16x32_bf16 v[32:35], v[112:115], v[140:143], v[32:35]\n\t"
        "ds_read_b64_tr_b16 v[108:109], v129 offset:32\n\t"
        "v_cmp_gt_u32 vcc, v128, v126\n\t"
        "v_mfma_f32_16x16x32_bf16 v[40:43], v[112:115], v[144:147], v[40:43]\n\t"
        "ds_read_b64_tr_b16 v[110:111], v129 offset:3104\n\t"
        "v_add_u32 v124, s46, v122\n\t"
        "v_mfma_f32_16x16x32_bf16 v[36:39], v[116:119], v[140:143], v[36:39]\n\t"
        "ds_read_b64_tr_b16 v[112:113], v130 offset:0\n\t"
        "v_mfma_f32_16x16x32_bf16 v[44:47], v[116:119], v[144:147], v[44:47]\n\t"
        "ds_read_b64_tr_b16 v[114:115], v130 offset:3072\n\t"
        "s_waitcnt lgkmcnt(6)\n\t"
        "v_mfma_f32_16x16x32_bf16 v[48:51], v[96:99], v[140:143], v[48:51]\n\t"
        "ds_read_b64_tr_b16 v[116:117], v130 offset:32\n\t"
        "v_mfma_f32_16x16x32_bf16 v[56:59], v[96:99], v[144:147], v[56:59]\n\t"
        "ds_read_b64_tr_b16 v[118:119], v130 offset:3104\n\t"
        "s_cbranch_vccnz 12f\n\t"
        "13:\n\t"
        "v_mfma_f32_16x16x32_bf16 v[52:55], v[100:103], v[140:143], v[52:55]\n\t"
        "ds_read_b64_tr_b16 v[96:97], v123 offset:256\n\t"
        "s_addk_i32 s46, 12352\n\t"
        "v_mfma_f32_16x16x32_bf16 v[60:63], v[100:103], v[144:147], v[60:63]\n\t"
        "ds_read_b64_tr_b16 v[98:99], v123 offset:3328\n\t"
        "s_waitcnt lgkmcnt(6)\n\t"
        "v_mfma_f32_16x16x32_bf16 v[0:3], v[104:107], v[140:143], v[0:3]\n\t"
        "ds_read_b64_tr_b16 v[100:101], v123 offset:288\n\t"
        "s_cmp_eq_u32 s46, s47\n\t"
        "v_mfma_f32_16x16x32_bf16 v[8:11], v[104:107], v[144:147], v[8:11]\n\t"
        "ds_read_b64_tr_b16 v[102:103], v123 offset:3360\n\t"
        "s_cselect_b32 s46, 0, s46\n\t"
        "v_mfma_f32_16x16x32_bf16 v[4:7], v[108:111], v[140:143], v[4:7]\n\t"
        "ds_read_b64_tr_b16 v[104:105], v124 offset:256\n\t"
        "v_mfma_f32_16x16x32_bf16 v[12:15], v[108:111], v[144:147], v[12:15]\n\t"
        "ds_read_b64_tr_b16 v[106:107], v124 offset:3328\n\t"
        "s_waitcnt lgkmcnt(6)\n\t"
        "v_mfma_f32_16x16x32_bf16 v[16:19], v[112:115], v[140:143], v[16:19]\n\t"
        "ds_write_b32 v127, v128\n\t"
        "ds_read_b64_tr_b16 v[108:109], v124 offset:288\n\t"
        "v_mfma_f32_16x16x32_bf16 v[24:27], v[112:115], v[144:147], v[24:27]\n\t"
        "ds_read_b64_tr_b16 v[110:111], v124 offset:3360\n\t"
        "v_add_u32 v128, 2, v128\n\t"
        "v_mfma_f32_16x16x32_bf16 v[20:23], v[116:119], v[140:143], v[20:23]\n\t"
        "ds_read_b64_tr_b16 v[112:113], v123 offset:128\n\t"
        "v_mfma_f32_16x16x32_bf16 v[28:31], v[116:119], v[144:147], v[28:31]\n\t"
        "ds_read_b64_tr_b16 v[114:115], v123 offset:3200\n\t"
        "s_mov_b32 s45, 2\n\t"
        "1:\n\t"
        "s_waitcnt vmcnt(10) lgkmcnt(7)\n\t"
        "v_mfma_f32_16x16x32_bf16 v[64:67], v[148:151], v[96:99], v[64:67]\n\t"
        "ds_read_b64_tr_b16 v[116:117], v123 offset:160\n\t"
        "s_addk_i32 s44, 0x1000\n\t"
        "v_mfma_f32_16x16x32_bf16 v[72:75], v[152:155], v[96:99], v[72:75]\n\t"
        "ds_read_b64_tr_b16 v[118:119], v123 offset:3232\n\t"
        "s_bitset0_b32 s44, 16\n\t"
        "v_mfma_f32_16x16x32_bf16 v[68:71], v[148:151], v[100:103], v[68:71]\n\t"
        "ds_read_b64_tr_b16 v[96:97], v124 offset:128\n\t"
        "s_cmp_eq_u32 s44, s49\n\t"
        "v_mfma_f32_16x16x32_bf16 v[76:79], v[152:155], v[100:103], v[76:79]\n\t"
        "ds_read_b64_tr_b16 v[98:99], v124 offset:3200\n\t"
        "s_waitcnt lgkmcnt(6)\n\t"
        "v_mfma_f32_16x16x32_bf16 v[80:83], v[148:151], v[104:107], v[80:83]\n\t"
        "ds_read_b64_tr_b16 v[100:101], v124 offset:160\n\t"
        "s_cselect_b32 s44, s50, s44\n\t"
        "v_mfma_f32_16x16x32_bf16 v[88:91], v[152:155], v[104:107], v[88:91]\n\t"
        "ds_read_b64_tr_b16 v[102:103], v124 offset:3232\n\t"
        "v_add_u32 v131, s44, v120\n\t"
        "v_mfma_f32_16x16x32_bf16 v[84:87], v[148:151], v[108:111], v[84:87]\n\t"
        "ds_read_b64_tr_b16 v[104:105], v123 offset:0\n\t"
        "v_add_u32 v129, s46, v121\n\t"
        "v_mfma_f32_16x16x32_bf16 v[92:95], v[152:155], v[108:111], v[92:95]\n\t"
        "ds_read_b64_tr_b16 v[106:107], v123 offset:3072\n\t"
        "s_waitcnt lgkmcnt(6)\n\t"
        "v_mfma_f32_16x16x32_bf16 v[32:35], v[112:115], v[148:151], v[32:35]\n\t"
        "ds_read_b64_tr_b16 v[108:109], v123 offset:32\n\t"
        "v_mfma_f32_16x16x32_bf16 v[40:43], v[112:115], v[152:155], v[40:43]\n\t"
        "ds_read_b64_tr_b16 v[110:111], v123 offset:3104\n\t"
        "v_add_u32 v130, s46, v122\n\t"
        "v_mfma_f32_16x16x32_bf16 v[36:39], v[116:119], v[148:151], v[36:39]\n\t"
        "ds_read_b64_tr_b16 v[112:113], v124 offset:0\n\t"
        "buffer_load_dwordx4 v[132:135], v131, s[40:43], 0 offen offset:0 sc1\n\t"
        "v_mfma_f32_16x16x32_bf16 v[44:47], v[116:119], v[152:155], v[44:47]\n\t"
        "ds_read_b64_tr_b16 v[114:115], v124 offset:3072\n\t"
        "s_waitcnt lgkmcnt(6)\n\t"
        "v_mfma_f32_16x16x32_bf16 v[48:51], v[96:99], v[148:151], v[48:51]\n\t"
        "ds_read_b64_tr_b16 v[116:117], v124 offset:32\n\t"
        "v_mfma_f32_16x16x32_bf16 v[56:59], v[96:99], v[152:155], v[56:59]\n\t"
        "ds_read_b64_tr_b16 v[118:119], v124 offset:3104\n\t"
        "v_mfma_f32_16x16x32_bf16 v[52:55], v[100:103], v[148:151], v[52:55]\n\t"
        "ds_read_b64_tr_b16 v[96:97], v129 offset:256\n\t"
        "s_addk_i32 s46, 12352\n\t"
        "v_mfma_f32_16x16x32_bf16 v[60:63], v[100:103], v[152:155], v[60:63]\n\t"
        "ds_read_b64_tr_b16 v[98:99], v129 offset:3328\n\t"
        "s_waitcnt lgkmcnt(6)\n\t"
        "v_mfma_f32_16x16x32_bf16 v[0:3], v[104:107], v[148:151], v[0:3]\n\t"
        "ds_read_b64_tr_b16 v[100:101], v129 offset:288\n\t"
        "s_cmp_eq_u32 s46, s47\n\t"
        "v_mfma_f32_16x16x32_bf16 v[8:11], v[104:107], v[152:155], v[8:11]\n\t"
        "ds_read_b64_tr_b16 v[102:103], v129 offset:3360\n\t"
        "s_cselect_b32 s46, 0, s46\n\t"
        "v_mfma_f32_16x16x32_bf16 v[4:7], v[108:111], v[148:151], v[4:7]\n\t"
        "ds_read_b64_tr_b16 v[104:105], v130 offset:256\n\t"
        "buffer_load_dwordx4 v[136:139], v131, s[40:43], 0 offen offset:256 sc1\n\t"
        "v_mfma_f32_16x16x32_bf16 v[12:15], v[108:111], v[152:155], v[12:15]\n\t"
        "ds_read_b64_tr_b16 v[106:107], v130 offset:3328\n\t"
        "s_waitcnt lgkmcnt(6)\n\t"
        "v_mfma_f32_16x16x32_bf16 v[16:19], v[112:115], v[148:151], v[16:19]\n\t"
        "ds_read_b64_tr_b16 v[108:109], v130 offset:288\n\t"
        "v_mfma_f32_16x16x32_bf16 v[24:27], v[112:115], v[152:155], v[24:27]\n\t"
        "ds_read_b64_tr_b16 v[110:111], v130 offset:3360\n\t"
        "v_mfma_f32_16x16x32_bf16 v[20:23], v[116:119], v[148:151], v[20:23]\n\t"
        "ds_read_b64_tr_b16 v[112:113], v129 offset:128\n\t"
        "v_mfma_f32_16x16x32_bf16 v[28:31], v[116:119], v[152:155], v[28:31]\n\t"
        "ds_read_b64_tr_b16 v[114:115], v129 offset:3200\n\t"
        "s_waitcnt vmcnt(10) lgkmcnt(6)\n\t"
        "v_mfma_f32_16x16x32_bf16 v[64:67], v[156:159], v[96:99], v[64:67]\n\t"
        "ds_read_b64_tr_b16 v[116:117], v129 offset:160\n\t"
        "v_mfma_f32_16x16x32_bf16 v[72:75], v[160:163], v[96:99], v[72:75]\n\t"
        "ds_read_b64_tr_b16 v[118:119], v129 offset:3232\n\t"
        "v_mfma_f32_16x16x32_bf16 v[68:71], v[156:159], v[100:103], v[68:71]\n\t"
        "ds_read_b64_tr_b16 v[96:97], v130 offset:128\n\t"
        "ds_read_b32 v126, v125\n\t"
        "v_mfma_f32_16x16x32_bf16 v[76:79], v[160:163], v[100:103], v[76:79]\n\t"
        "ds_read_b64_tr_b16 v[98:99], v130 offset:3200\n\t"
        "s_waitcnt lgkmcnt(7)\n\t"
        "v_mfma_f32_16x16x32_bf16 v[80:83], v[156:159], v[104:107], v[80:83]\n\t"
        "ds_read_b64_tr_b16 v[100:101], v130 offset:160\n\t"
        "v_mfma_f32_16x16x32_bf16 v[88:91], v[160:163], v[104:107], v[88:91]\n\t"
        "ds_read_b64_tr_b16 v[102:103], v130 offset:3232\n\t"
        "v_mfma_f32_16x16x32_bf16 v[84:87], v[156:159], v[108:111], v[84:87]\n\t"
        "ds_read_b64_tr_b16 v[104:105], v129 offset:0\n\t"
        "v_add_u32 v123, s46, v121\n\t"
        "v_mfma_f32_16x16x32_bf16 v[92:95], v[160:163], v[108:111], v[92:95]\n\t"
        "ds_read_b64_tr_b16 v[106:107], v129 offset:3072\n\t"
        "s_waitcnt lgkmcnt(5)\n\t"
        "v_mfma_f32_16x16x32_bf16 v[32:35], v[112:115], v[156:159], v[32:35]\n\t"
        "ds_read_b64_tr_b16 v[108:109], v129 offset:32\n\t"
        "v_cmp_gt_u32 vcc, v128, v126\n\t"
        "v_mfma_f32_16x16x32_bf16 v[40:43], v[112:115], v[160:163], v[40:43]\n\t"
        "ds_read_b64_tr_b16 v[110:111], v129 offset:3104\n\t"
        "v_add_u32 v124, s46, v122\n\t"
        "v_mfma_f32_16x16x32_bf16 v[36:39], v[116:119], v[156:159], v[36:39]\n\t"
        "ds_read_b64_tr_b16 v[112:113], v130 offset:0\n\t"
        "buffer_load_dwordx4 v[140:143], v131, s[40:43], 0 offen offset:2048 sc1\n\t"
        "v_mfma_f32_16x16x32_bf16 v[44:47], v[116:119], v[160:163], v[44:47]\n\t"
        "ds_read_b64_tr_b16 v[114:115], v130 offset:3072\n\t"
        "s_waitcnt lgkmcnt(6)\n\t"
        "v_mfma_f32_16x16x32_bf16 v[48:51], v[96:99], v[156:159], v[48:51]\n\t"
        "ds_read_b64_tr_b16 v[116:117], v130 offset:32\n\t"
        "v_mfma_f32_16x16x32_bf16 v[56:59], v[96:99], v[160:163], v[56:59]\n\t"
        "ds_read_b64_tr_b16 v[118:119], v130 offset:3104\n\t"
        "s_cbranch_vccnz 14f\n\t"
        "15:\n\t"
        "v_mfma_f32_16x16x32_bf16 v[52:55], v[100:103], v[156:159], v[52:55]\n\t"
        "ds_read_b64_tr_b16 v[96:97], v123 offset:256\n\t"
        "s_addk_i32 s46, 12352\n\t"
        "v_mfma_f32_16x16x32_bf16 v[60:63], v[100:103], v[160:163], v[60:63]\n\t"
        "ds_read_b64_tr_b16 v[98:99], v123 offset:3328\n\t"
        "s_waitcnt lgkmcnt(6)\n\t"
        "v_mfma_f32_16x16x32_bf16 v[0:3], v[104:107], v[156:159], v[0:3]\n\t"
        "ds_read_b64_tr_b16 v[100:101], v123 offset:288\n\t"
        "s_cmp_eq_u32 s46, s47\n\t"
        "v_mfma_f32_16x16x32_bf16 v[8:11], v[104:107], v[160:163], v[8:11]\n\t"
        "ds_read_b64_tr_b16 v[102:103], v123 offset:3360\n\t"
        "s_cselect_b32 s46, 0, s46\n\t"
        "v_mfma_f32_16x16x32_bf16 v[4:7], v[108:111], v[156:159], v[4:7]\n\t"
        "ds_read_b64_tr_b16 v[104:105], v124 offset:256\n\t"
        "buffer_load_dwordx4 v[144:147], v131, s[40:43], 0 offen offset:2304 sc1\n\t"
        "v_mfma_f32_16x16x32_bf16 v[12:15], v[108:111], v[160:163], v[12:15]\n\t"
        "ds_read_b64_tr_b16 v[106:107], v124 offset:3328\n\t"
        "s_waitcnt lgkmcnt(6)\n\t"
        "v_mfma_f32_16x16x32_bf16 v[16:19], v[112:115], v[156:159], v[16:19]\n\t"
        "ds_write_b32 v127, v128\n\t"
        "ds_read_b64_tr_b16 v[108:109], v124 offset:288\n\t"
        "v_mfma_f32_16x16x32_bf16 v[24:27], v[112:115], v[160:163], v[24:27]\n\t"
        "ds_read_b64_tr_b16 v[110:111], v124 offset:3360\n\t"
        "v_add_u32 v128, 2, v128\n\t"
        "v_mfma_f32_16x16x32_bf16 v[20:23], v[116:119], v[156:159], v[20:23]\n\t"
        "ds_read_b64_tr_b16 v[112:113], v123 offset:128\n\t"
        "v_mfma_f32_16x16x32_bf16 v[28:31], v[116:119], v[160:163], v[28:31]\n\t"
        "ds_read_b64_tr_b16 v[114:115], v123 offset:3200\n\t"
        "s_waitcnt vmcnt(10) lgkmcnt(7)\n\t"
        "v_mfma_f32_16x16x32_bf16 v[64:67], v[164:167], v[96:99], v[64:67]\n\t"
        "ds_read_b64_tr_b16 v[116:117], v123 offset:160\n\t"
        "s_addk_i32 s44, 0x1000\n\t"
        "v_mfma_f32_16x16x32_bf16 v[72:75], v[168:171], v[96:99], v[72:75]\n\t"
        "ds_read_b64_tr_b16 v[118:119], v123 offset:3232\n\t"
        "s_bitset0_b32 s44, 16\n\t"
        "v_mfma_f32_16x16x32_bf16 v[68:71], v[164:167], v[100:103], v[68:71]\n\t"
        "ds_read_b64_tr_b16 v[96:97], v124 offset:128\n\t"
        "s_cmp_eq_u32 s44, s49\n\t"
        "v_mfma_f32_16x16x32_bf16 v[76:79], v[168:171], v[100:103], v[76:79]\n\t"
        "ds_read_b64_tr_b16 v[98:99], v124 offset:3200\n\t"
        "s_waitcnt lgkmcnt(6)\n\t"
        "v_mfma_f32_16x16x32_bf16 v[80:83], v[164:167], v[104:107], v[80:83]\n\t"
        "ds_read_b64_tr_b16 v[100:101], v124 offset:160\n\t"
        "s_cselect_b32 s44, s50, s44\n\t"
        "v_mfma_f32_16x16x32_bf16 v[88:91], v[168:171], v[104:107], v[88:91]\n\t"
        "ds_read_b64_tr_b16 v[102:103], v124 offset:3232\n\t"
        "v_add_u32 v131, s44, v120\n\t"
        "v_mfma_f32_16x16x32_bf16 v[84:87], v[164:167], v[108:111], v[84:87]\n\t"
        "ds_read_b64_tr_b16 v[104:105], v123 offset:0\n\t"
        "v_add_u32 v129, s46, v121\n\t"
        "v_mfma_f32_16x16x32_bf16 v[92:95], v[168:171], v[108:111], v[92:95]\n\t"
        "ds_read_b64_tr_b16 v[106:107], v123 offset:3072\n\t"
        "s_waitcnt lgkmcnt(6)\n\t"
        "v_mfma_f32_16x16x32_bf16 v[32:35], v[112:115], v[164:167], v[32:35]\n\t"
        "ds_read_b64_tr_b16 v[108:109], v123 offset:32\n\t"
        "v_mfma_f32_16x16x32_bf16 v[40:43], v[112:115], v[168:171], v[40:43]\n\t"
        "ds_read_b64_tr_b16 v[110:111], v123 offset:3104\n\t"
        "v_add_u32 v130, s46, v122\n\t"
        "v_mfma_f32_16x16x32_bf16 v[36:39], v[116:119], v[164:167], v[36:39]\n\t"
        "ds_read_b64_tr_b16 v[112:113], v124 offset:0\n\t"
        "buffer_load_dwordx4 v[148:151], v131, s[40:43], 0 offen offset:0 sc1\n\t"
        "v_mfma_f32_16x16x32_bf16 v[44:47], v[116:119], v[168:171], v[44:47]\n\t"
        "ds_read_b64_tr_b16 v[114:115], v124 offset:3072\n\t"
        "s_waitcnt lgkmcnt(6)\n\t"
        "v_mfma_f32_16x16x32_bf16 v[48:51], v[96:99], v[164:167], v[48:51]\n\t"
        "ds_read_b64_tr_b16 v[116:117], v124 offset:32\n\t"
        "v_mfma_f32_16x16x32_bf16 v[56:59], v[96:99], v[168:171], v[56:59]\n\t"
        "ds_read_b64_tr_b16 v[118:119], v124 offset:3104\n\t"
        "v_mfma_f32_16x16x32_bf16 v[52:55], v[100:103], v[164:167], v[52:55]\n\t"
        "ds_read_b64_tr_b16 v[96:97], v129 offset:256\n\t"
        "s_addk_i32 s46, 12352\n\t"
        "v_mfma_f32_16x16x32_bf16 v[60:63], v[100:103], v[168:171], v[60:63]\n\t"
        "ds_read_b64_tr_b16 v[98:99], v129 offset:3328\n\t"
        "s_waitcnt lgkmcnt(6)\n\t"
        "v_mfma_f32_16x16x32_bf16 v[0:3], v[104:107], v[164:167], v[0:3]\n\t"
        "ds_read_b64_tr_b16 v[100:101], v129 offset:288\n\t"
        "s_cmp_eq_u32 s46, s47\n\t"
        "v_mfma_f32_16x16x32_bf16 v[8:11], v[104:107], v[168:171], v[8:11]\n\t"
        "ds_read_b64_tr_b16 v[102:103], v129 offset:3360\n\t"
        "s_cselect_b32 s46, 0, s46\n\t"
        "v_mfma_f32_16x16x32_bf16 v[4:7], v[108:111], v[164:167], v[4:7]\n\t"
        "ds_read_b64_tr_b16 v[104:105], v130 offset:256\n\t"
        "buffer_load_dwordx4 v[152:155], v131, s[40:43], 0 offen offset:256 sc1\n\t"
        "v_mfma_f32_16x16x32_bf16 v[12:15], v[108:111], v[168:171], v[12:15]\n\t"
        "ds_read_b64_tr_b16 v[106:107], v130 offset:3328\n\t"
        "s_waitcnt lgkmcnt(6)\n\t"
        "v_mfma_f32_16x16x32_bf16 v[16:19], v[112:115], v[164:167], v[16:19]\n\t"
        "ds_read_b64_tr_b16 v[108:109], v130 offset:288\n\t"
        "v_mfma_f32_16x16x32_bf16 v[24:27], v[112:115], v[168:171], v[24:27]\n\t"
        "ds_read_b64_tr_b16 v[110:111], v130 offset:3360\n\t"
        "v_mfma_f32_16x16x32_bf16 v[20:23], v[116:119], v[164:167], v[20:23]\n\t"
        "ds_read_b64_tr_b16 v[112:113], v129 offset:128\n\t"
        "v_mfma_f32_16x16x32_bf16 v[28:31], v[116:119], v[168:171], v[28:31]\n\t"
        "ds_read_b64_tr_b16 v[114:115], v129 offset:3200\n\t"
        "s_waitcnt vmcnt(10) lgkmcnt(6)\n\t"
        "v_mfma_f32_16x16x32_bf16 v[64:67], v[172:175], v[96:99], v[64:67]\n\t"
        "ds_read_b64_tr_b16 v[116:117], v129 offset:160\n\t"
        "v_mfma_f32_16x16x32_bf16 v[72:75], v[176:179], v[96:99], v[72:75]\n\t"
        "ds_read_b64_tr_b16 v[118:119], v129 offset:3232\n\t"
        "v_mfma_f32_16x16x32_bf16 v[68:71], v[172:175], v[100:103], v[68:71]\n\t"
        "ds_read_b64_tr_b16 v[96:97], v130 offset:128\n\t"
        "ds_read_b32 v126, v125\n\t"
        "v_mfma_f32_16x16x32_bf16 v[76:79], v[176:179], v[100:103], v[76:79]\n\t"
        "ds_read_b64_tr_b16 v[98:99], v130 offset:3200\n\t"
        "s_waitcnt lgkmcnt(7)\n\t"
        "v_mfma_f32_16x16x32_bf16 v[80:83], v[172:175], v[104:107], v[80:83]\n\t"
        "ds_read_b64_tr_b16 v[100:101], v130 offset:160\n\t"
        "v_mfma_f32_16x16x32_bf16 v[88:91], v[176:179], v[104:107], v[88:91]\n\t"
        "ds_read_b64_tr_b16 v[102:103], v130 offset:3232\n\t"
        "v_mfma_f32_16x16x32_bf16 v[84:87], v[172:175], v[108:111], v[84:87]\n\t"
        "ds_read_b64_tr_b16 v[104:105], v129 offset:0\n\t"
        "v_add_u32 v123, s46, v121\n\t"
        "v_mfma_f32_16x16x32_bf16 v[92:95], v[176:179], v[108:111], v[92:95]\n\t"
        "ds_read_b64_tr_b16 v[106:107], v129 offset:3072\n\t"
        "s_waitcnt lgkmcnt(5)\n\t"
        "v_mfma_f32_16x16x32_bf16 v[32:35], v[112:115], v[172:175], v[32:35]\n\t"
        "ds_read_b64_tr_b16 v[108:109], v129 offset:32\n\t"
        "v_cmp_gt_u32 vcc, v128, v126\n\t"
        "v_mfma_f32_16x16x32_bf16 v[40:43], v[112:115], v[176:179], v[40:43]\n\t"
        "ds_read_b64_tr_b16 v[110:111], v129 offset:3104\n\t"
        "v_add_u32 v124, s46, v122\n\t"
        "v_mfma_f32_16x16x32_bf16 v[36:39], v[116:119], v[172:175], v[36:39]\n\t"
        "ds_read_b64_tr_b16 v[112:113], v130 offset:0\n\t"
        "buffer_load_dwordx4 v[156:159], v131, s[40:43], 0 offen offset:2048 sc1\n\t"
        "v_mfma_f32_16x16x32_bf16 v[44:47], v[116:119], v[176:179], v[44:47]\n\t"
        "ds_read_b64_tr_b16 v[114:115], v130 offset:3072\n\t"
        "s_waitcnt lgkmcnt(6)\n\t"
        "v_mfma_f32_16x16x32_bf16 v[48:51], v[96:99], v[172:175], v[48:51]\n\t"
        "ds_read_b64_tr_b16 v[116:117], v130 offset:32\n\t"
        "v_mfma_f32_16x16x32_bf16 v[56:59], v[96:99], v[176:179], v[56:59]\n\t"
        "ds_read_b64_tr_b16 v[118:119], v130 offset:3104\n\t"
        "s_cbranch_vccnz 16f\n\t"
        "17:\n\t"
        "v_mfma_f32_16x16x32_bf16 v[52:55], v[100:103], v[172:175], v[52:55]\n\t"
        "ds_read_b64_tr_b16 v[96:97], v123 offset:256\n\t"
        "s_addk_i32 s46, 12352\n\t"
        "v_mfma_f32_16x16x32_bf16 v[60:63], v[100:103], v[176:179], v[60:63]\n\t"
        "ds_read_b64_tr_b16 v[98:99], v123 offset:3328\n\t"
        "s_waitcnt lgkmcnt(6)\n\t"
        "v_mfma_f32_16x16x32_bf16 v[0:3], v[104:107], v[172:175], v[0:3]\n\t"
        "ds_read_b64_tr_b16 v[100:101], v123 offset:288\n\t"
        "s_cmp_eq_u32 s46, s47\n\t"
        "v_mfma_f32_16x16x32_bf16 v[8:11], v[104:107], v[176:179], v[8:11]\n\t"
        "ds_read_b64_tr_b16 v[102:103], v123 offset:3360\n\t"
        "s_cselect_b32 s46, 0, s46\n\t"
        "v_mfma_f32_16x16x32_bf16 v[4:7], v[108:111], v[172:175], v[4:7]\n\t"
        "ds_read_b64_tr_b16 v[104:105], v124 offset:256\n\t"
        "buffer_load_dwordx4 v[160:163], v131, s[40:43], 0 offen offset:2304 sc1\n\t"
        "v_mfma_f32_16x16x32_bf16 v[12:15], v[108:111], v[176:179], v[12:15]\n\t"
        "ds_read_b64_tr_b16 v[106:107], v124 offset:3328\n\t"
        "s_waitcnt lgkmcnt(6)\n\t"
        "v_mfma_f32_16x16x32_bf16 v[16:19], v[112:115], v[172:175], v[16:19]\n\t"
        "ds_write_b32 v127, v128\n\t"
        "ds_read_b64_tr_b16 v[108:109], v124 offset:288\n\t"
        "v_mfma_f32_16x16x32_bf16 v[24:27], v[112:115], v[176:179], v[24:27]\n\t"
        "ds_read_b64_tr_b16 v[110:111], v124 offset:3360\n\t"
        "v_add_u32 v128, 2, v128\n\t"
        "v_mfma_f32_16x16x32_bf16 v[20:23], v[116:119], v[172:175], v[20:23]\n\t"
        "ds_read_b64_tr_b16 v[112:113], v123 offset:128\n\t"
        "v_mfma_f32_16x16x32_bf16 v[28:31], v[116:119], v[176:179], v[28:31]\n\t"
        "ds_read_b64_tr_b16 v[114:115], v123 offset:3200\n\t"
        "s_waitcnt vmcnt(10) lgkmcnt(7)\n\t"
        "v_mfma_f32_16x16x32_bf16 v[64:67], v[180:183], v[96:99], v[64:67]\n\t"
        "ds_read_b64_tr_b16 v[116:117], v123 offset:160\n\t"
        "s_addk_i32 s44, 0x1000\n\t"
        "v_mfma_f32_16x16x32_bf16 v[72:75], v[184:187], v[96:99], v[72:75]\n\t"
        "ds_read_b64_tr_b16 v[118:119], v123 offset:3232\n\t"
        "s_bitset0_b32 s44, 16\n\t"
        "v_mfma_f32_16x16x32_bf16 v[68:71], v[180:183], v[100:103], v[68:71]\n\t"
        "ds_read_b64_tr_b16 v[96:97], v124 offset:128\n\t"
        "s_cmp_eq_u32 s44, s49\n\t"
        "v_mfma_f32_16x16x32_bf16 v[76:79], v[184:187], v[100:103], v[76:79]\n\t"
        "ds_read_b64_tr_b16 v[98:99], v124 offset:3200\n\t"
        "s_waitcnt lgkmcnt(6)\n\t"
        "v_mfma_f32_16x16x32_bf16 v[80:83], v[180:183], v[104:107], v[80:83]\n\t"
        "ds_read_b64_tr_b16 v[100:101], v124 offset:160\n\t"
        "s_cselect_b32 s44, s50, s44\n\t"
        "v_mfma_f32_16x16x32_bf16 v[88:91], v[184:187], v[104:107], v[88:91]\n\t"
        "ds_read_b64_tr_b16 v[102:103], v124 offset:3232\n\t"
        "v_add_u32 v131, s44, v120\n\t"
        "v_mfma_f32_16x16x32_bf16 v[84:87], v[180:183], v[108:111], v[84:87]\n\t"
        "ds_read_b64_tr_b16 v[104:105], v123 offset:0\n\t"
        "v_add_u32 v129, s46, v121\n\t"
        "v_mfma_f32_16x16x32_bf16 v[92:95], v[184:187], v[108:111], v[92:95]\n\t"
        "ds_read_b64_tr_b16 v[106:107], v123 offset:3072\n\t"
        "s_waitcnt lgkmcnt(6)\n\t"
        "v_mfma_f32_16x16x32_bf16 v[32:35], v[112:115], v[180:183], v[32:35]\n\t"
        "ds_read_b64_tr_b16 v[108:109], v123 offset:32\n\t"
        "v_mfma_f32_16x16x32_bf16 v[40:43], v[112:115], v[184:187], v[40:43]\n\t"
        "ds_read_b64_tr_b16 v[110:111], v123 offset:3104\n\t"
        "v_add_u32 v130, s46, v122\n\t"
        "v_mfma_f32_16x16x32_bf16 v[36:39], v[116:119], v[180:183], v[36:39]\n\t"
        "ds_read_b64_tr_b16 v[112:113], v124 offset:0\n\t"
        "buffer_load_dwordx4 v[164:167], v131, s[40:43], 0 offen offset:0 sc1\n\t"
        "v_mfma_f32_16x16x32_bf16 v[44:47], v[116:119], v[184:187], v[44:47]\n\t"
        "ds_read_b64_tr_b16 v[114:115], v124 offset:3072\n\t"
        "s_waitcnt lgkmcnt(6)\n\t"
        "v_mfma_f32_16x16x32_bf16 v[48:51], v[96:99], v[180:183], v[48:51]\n\t"
        "ds_read_b64_tr_b16 v[116:117], v124 offset:32\n\t"
        "v_mfma_f32_16x16x32_bf16 v[56:59], v[96:99], v[184:187], v[56:59]\n\t"
        "ds_read_b64_tr_b16 v[118:119], v124 offset:3104\n\t"
        "v_mfma_f32_16x16x32_bf16 v[52:55], v[100:103], v[180:183], v[52:55]\n\t"
        "ds_read_b64_tr_b16 v[96:97], v129 offset:256\n\t"
        "s_addk_i32 s46, 12352\n\t"
        "v_mfma_f32_16x16x32_bf16 v[60:63], v[100:103], v[184:187], v[60:63]\n\t"
        "ds_read_b64_tr_b16 v[98:99], v129 offset:3328\n\t"
        "s_waitcnt lgkmcnt(6)\n\t"
        "v_mfma_f32_16x16x32_bf16 v[0:3], v[104:107], v[180:183], v[0:3]\n\t"
        "ds_read_b64_tr_b16 v[100:101], v129 offset:288\n\t"
        "s_cmp_eq_u32 s46, s47\n\t"
        "v_mfma_f32_16x16x32_bf16 v[8:11], v[104:107], v[184:187], v[8:11]\n\t"
        "ds_read_b64_tr_b16 v[102:103], v129 offset:3360\n\t"
        "s_cselect_b32 s46, 0, s46\n\t"
        "v_mfma_f32_16x16x32_bf16 v[4:7], v[108:111], v[180:183], v[4:7]\n\t"
        "ds_read_b64_tr_b16 v[104:105], v130 offset:256\n\t"
        "buffer_load_dwordx4 v[168:171], v131, s[40:43], 0 offen offset:256 sc1\n\t"
        "v_mfma_f32_16x16x32_bf16 v[12:15], v[108:111], v[184:187], v[12:15]\n\t"
        "ds_read_b64_tr_b16 v[106:107], v130 offset:3328\n\t"
        "s_waitcnt lgkmcnt(6)\n\t"
        "v_mfma_f32_16x16x32_bf16 v[16:19], v[112:115], v[180:183], v[16:19]\n\t"
        "ds_read_b64_tr_b16 v[108:109], v130 offset:288\n\t"
        "v_mfma_f32_16x16x32_bf16 v[24:27], v[112:115], v[184:187], v[24:27]\n\t"
        "ds_read_b64_tr_b16 v[110:111], v130 offset:3360\n\t"
        "v_mfma_f32_16x16x32_bf16 v[20:23], v[116:119], v[180:183], v[20:23]\n\t"
        "ds_read_b64_tr_b16 v[112:113], v129 offset:128\n\t"
        "v_mfma_f32_16x16x32_bf16 v[28:31], v[116:119], v[184:187], v[28:31]\n\t"
        "ds_read_b64_tr_b16 v[114:115], v129 offset:3200\n\t"
        "s_waitcnt vmcnt(10) lgkmcnt(6)\n\t"
        "v_mfma_f32_16x16x32_bf16 v[64:67], v[188:191], v[96:99], v[64:67]\n\t"
        "ds_read_b64_tr_b16 v[116:117], v129 offset:160\n\t"
        "v_mfma_f32_16x16x32_bf16 v[72:75], v[192:195], v[96:99], v[72:75]\n\t"
        "ds_read_b64_tr_b16 v[118:119], v129 offset:3232\n\t"
        "v_mfma_f32_16x16x32_bf16 v[68:71], v[188:191], v[100:103], v[68:71]\n\t"
        "ds_read_b64_tr_b16 v[96:97], v130 offset:128\n\t"
        "ds_read_b32 v126, v125\n\t"
        "v_mfma_f32_16x16x32_bf16 v[76:79], v[192:195], v[100:103], v[76:79]\n\t"
        "ds_read_b64_tr_b16 v[98:99], v130 offset:3200\n\t"
        "s_waitcnt lgkmcnt(7)\n\t"
        "v_mfma_f32_16x16x32_bf16 v[80:83], v[188:191], v[104:107], v[80:83]\n\t"
        "ds_read_b64_tr_b16 v[100:101], v130 offset:160\n\t"
        "v_mfma_f32_16x16x32_bf16 v[88:91], v[192:195], v[104:107], v[88:91]\n\t"
        "ds_read_b64_tr_b16 v[102:103], v130 offset:3232\n\t"
        "v_mfma_f32_16x16x32_bf16 v[84:87], v[188:191], v[108:111], v[84:87]\n\t"
        "ds_read_b64_tr_b16 v[104:105], v129 offset:0\n\t"
        "v_add_u32 v123, s46, v121\n\t"
        "v_mfma_f32_16x16x32_bf16 v[92:95], v[192:195], v[108:111], v[92:95]\n\t"
        "ds_read_b64_tr_b16 v[106:107], v129 offset:3072\n\t"
        "s_waitcnt lgkmcnt(5)\n\t"
        "v_mfma_f32_16x16x32_bf16 v[32:35], v[112:115], v[188:191], v[32:35]\n\t"
        "ds_read_b64_tr_b16 v[108:109], v129 offset:32\n\t"
        "v_cmp_gt_u32 vcc, v128, v126\n\t"
        "v_mfma_f32_16x16x32_bf16 v[40:43], v[112:115], v[192:195], v[40:43]\n\t"
        "ds_read_b64_tr_b16 v[110:111], v129 offset:3104\n\t"
        "v_add_u32 v124, s46, v122\n\t"
        "v_mfma_f32_16x16x32_bf16 v[36:39], v[116:119], v[188:191], v[36:39]\n\t"
        "ds_read_b64_tr_b16 v[112:113], v130 offset:0\n\t"
        "buffer_load_dwordx4 v[172:175], v131, s[40:43], 0 offen offset:2048 sc1\n\t"
        "v_mfma_f32_16x16x32_bf16 v[44:47], v[116:119], v[192:195], v[44:47]\n\t"
        "ds_read_b64_tr_b16 v[114:115], v130 offset:3072\n\t"
        "s_waitcnt lgkmcnt(6)\n\t"
        "v_mfma_f32_16x16x32_bf16 v[48:51], v[96:99], v[188:191], v[48:51]\n\t"
        "ds_read_b64_tr_b16 v[116:117], v130 offset:32\n\t"
        "v_mfma_f32_16x16x32_bf16 v[56:59], v[96:99], v[192:195], v[56:59]\n\t"
        "ds_read_b64_tr_b16 v[118:119], v130 offset:3104\n\t"
        "s_cbranch_vccnz 18f\n\t"
        "19:\n\t"
        "v_mfma_f32_16x16x32_bf16 v[52:55], v[100:103], v[188:191], v[52:55]\n\t"
        "ds_read_b64_tr_b16 v[96:97], v123 offset:256\n\t"
        "s_addk_i32 s46, 12352\n\t"
        "v_mfma_f32_16x16x32_bf16 v[60:63], v[100:103], v[192:195], v[60:63]\n\t"
        "ds_read_b64_tr_b16 v[98:99], v123 offset:3328\n\t"
        "s_waitcnt lgkmcnt(6)\n\t"
        "v_mfma_f32_16x16x32_bf16 v[0:3], v[104:107], v[188:191], v[0:3]\n\t"
        "ds_read_b64_tr_b16 v[100:101], v123 offset:288\n\t"
        "s_cmp_eq_u32 s46, s47\n\t"
        "v_mfma_f32_16x16x32_bf16 v[8:11], v[104:107], v[192:195], v[8:11]\n\t"
        "ds_read_b64_tr_b16 v[102:103], v123 offset:3360\n\t"
        "s_cselect_b32 s46, 0, s46\n\t"
        "v_mfma_f32_16x16x32_bf16 v[4:7], v[108:111], v[188:191], v[4:7]\n\t"
        "ds_read_b64_tr_b16 v[104:105], v124 offset:256\n\t"
        "buffer_load_dwordx4 v[176:179], v131, s[40:43], 0 offen offset:2304 sc1\n\t"
        "v_mfma_f32_16x16x32_bf16 v[12:15], v[108:111], v[192:195], v[12:15]\n\t"
        "ds_read_b64_tr_b16 v[106:107], v124 offset:3328\n\t"
        "s_waitcnt lgkmcnt(6)\n\t"
        "v_mfma_f32_16x16x32_bf16 v[16:19], v[112:115], v[188:191], v[16:19]\n\t"
        "ds_write_b32 v127, v128\n\t"
        "ds_read_b64_tr_b16 v[108:109], v124 offset:288\n\t"
        "v_mfma_f32_16x16x32_bf16 v[24:27], v[112:115], v[192:195], v[24:27]\n\t"
        "ds_read_b64_tr_b16 v[110:111], v124 offset:3360\n\t"
        "v_add_u32 v128, 2, v128\n\t"
        "v_mfma_f32_16x16x32_bf16 v[20:23], v[116:119], v[188:191], v[20:23]\n\t"
        "ds_read_b64_tr_b16 v[112:113], v123 offset:128\n\t"
        "v_mfma_f32_16x16x32_bf16 v[28:31], v[116:119], v[192:195], v[28:31]\n\t"
        "ds_read_b64_tr_b16 v[114:115], v123 offset:3200\n\t"
        "s_waitcnt vmcnt(10) lgkmcnt(7)\n\t"
        "v_mfma_f32_16x16x32_bf16 v[64:67], v[132:135], v[96:99], v[64:67]\n\t"
        "ds_read_b64_tr_b16 v[116:117], v123 offset:160\n\t"
        "s_addk_i32 s44, 0x1000\n\t"
        "v_mfma_f32_16x16x32_bf16 v[72:75], v[136:139], v[96:99], v[72:75]\n\t"
        "ds_read_b64_tr_b16 v[118:119], v123 offset:3232\n\t"
        "s_bitset0_b32 s44, 16\n\t"
        "v_mfma_f32_16x16x32_bf16 v[68:71], v[132:135], v[100:103], v[68:71]\n\t"
        "ds_read_b64_tr_b16 v[96:97], v124 offset:128\n\t"
        "s_cmp_eq_u32 s44, s49\n\t"
        "v_mfma_f32_16x16x32_bf16 v[76:79], v[136:139], v[100:103], v[76:79]\n\t"
        "ds_read_b64_tr_b16 v[98:99], v124 offset:3200\n\t"
        "s_waitcnt lgkmcnt(6)\n\t"
        "v_mfma_f32_16x16x32_bf16 v[80:83], v[132:135], v[104:107], v[80:83]\n\t"
        "ds_read_b64_tr_b16 v[100:101], v124 offset:160\n\t"
        "s_cselect_b32 s44, s50, s44\n\t"
        "v_mfma_f32_16x16x32_bf16 v[88:91], v[136:139], v[104:107], v[88:91]\n\t"
        "ds_read_b64_tr_b16 v[102:103], v124 offset:3232\n\t"
        "v_add_u32 v131, s44, v120\n\t"
        "v_mfma_f32_16x16x32_bf16 v[84:87], v[132:135], v[108:111], v[84:87]\n\t"
        "ds_read_b64_tr_b16 v[104:105], v123 offset:0\n\t"
        "v_add_u32 v129, s46, v121\n\t"
        "v_mfma_f32_16x16x32_bf16 v[92:95], v[136:139], v[108:111], v[92:95]\n\t"
        "ds_read_b64_tr_b16 v[106:107], v123 offset:3072\n\t"
        "s_waitcnt lgkmcnt(6)\n\t"
        "v_mfma_f32_16x16x32_bf16 v[32:35], v[112:115], v[132:135], v[32:35]\n\t"
        "ds_read_b64_tr_b16 v[108:109], v123 offset:32\n\t"
        "v_mfma_f32_16x16x32_bf16 v[40:43], v[112:115], v[136:139], v[40:43]\n\t"
        "ds_read_b64_tr_b16 v[110:111], v123 offset:3104\n\t"
        "v_add_u32 v130, s46, v122\n\t"
        "v_mfma_f32_16x16x32_bf16 v[36:39], v[116:119], v[132:135], v[36:39]\n\t"
        "ds_read_b64_tr_b16 v[112:113], v124 offset:0\n\t"
        "buffer_load_dwordx4 v[180:183], v131, s[40:43], 0 offen offset:0 sc1\n\t"
        "v_mfma_f32_16x16x32_bf16 v[44:47], v[116:119], v[136:139], v[44:47]\n\t"
        "ds_read_b64_tr_b16 v[114:115], v124 offset:3072\n\t"
        "s_waitcnt lgkmcnt(6)\n\t"
        "v_mfma_f32_16x16x32_bf16 v[48:51], v[96:99], v[132:135], v[48:51]\n\t"
        "ds_read_b64_tr_b16 v[116:117], v124 offset:32\n\t"
        "v_mfma_f32_16x16x32_bf16 v[56:59], v[96:99], v[136:139], v[56:59]\n\t"
        "ds_read_b64_tr_b16 v[118:119], v124 offset:3104\n\t"
        "v_mfma_f32_16x16x32_bf16 v[52:55], v[100:103], v[132:135], v[52:55]\n\t"
        "ds_read_b64_tr_b16 v[96:97], v129 offset:256\n\t"
        "s_addk_i32 s46, 12352\n\t"
        "v_mfma_f32_16x16x32_bf16 v[60:63], v[100:103], v[136:139], v[60:63]\n\t"
        "ds_read_b64_tr_b16 v[98:99], v129 offset:3328\n\t"
        "s_waitcnt lgkmcnt(6)\n\t"
        "v_mfma_f32_16x16x32_bf16 v[0:3], v[104:107], v[132:135], v[0:3]\n\t"
        "ds_read_b64_tr_b16 v[100:101], v129 offset:288\n\t"
        "s_cmp_eq_u32 s46, s47\n\t"
        "v_mfma_f32_16x16x32_bf16 v[8:11], v[104:107], v[136:139], v[8:11]\n\t"
        "ds_read_b64_tr_b16 v[102:103], v129 offset:3360\n\t"
        "s_cselect_b32 s46, 0, s46\n\t"
        "v_mfma_f32_16x16x32_bf16 v[4:7], v[108:111], v[132:135], v[4:7]\n\t"
        "ds_read_b64_tr_b16 v[104:105], v130 offset:256\n\t"
        "buffer_load_dwordx4 v[184:187], v131, s[40:43], 0 offen offset:256 sc1\n\t"
        "v_mfma_f32_16x16x32_bf16 v[12:15], v[108:111], v[136:139], v[12:15]\n\t"
        "ds_read_b64_tr_b16 v[106:107], v130 offset:3328\n\t"
        "s_waitcnt lgkmcnt(6)\n\t"
        "v_mfma_f32_16x16x32_bf16 v[16:19], v[112:115], v[132:135], v[16:19]\n\t"
        "ds_read_b64_tr_b16 v[108:109], v130 offset:288\n\t"
        "v_mfma_f32_16x16x32_bf16 v[24:27], v[112:115], v[136:139], v[24:27]\n\t"
        "ds_read_b64_tr_b16 v[110:111], v130 offset:3360\n\t"
        "v_mfma_f32_16x16x32_bf16 v[20:23], v[116:119], v[132:135], v[20:23]\n\t"
        "ds_read_b64_tr_b16 v[112:113], v129 offset:128\n\t"
        "v_mfma_f32_16x16x32_bf16 v[28:31], v[116:119], v[136:139], v[28:31]\n\t"
        "ds_read_b64_tr_b16 v[114:115], v129 offset:3200\n\t"
        "s_waitcnt vmcnt(10) lgkmcnt(6)\n\t"
        "v_mfma_f32_16x16x32_bf16 v[64:67], v[140:143], v[96:99], v[64:67]\n\t"
        "ds_read_b64_tr_b16 v[116:117], v129 offset:160\n\t"
        "v_mfma_f32_16x16x32_bf16 v[72:75], v[144:147], v[96:99], v[72:75]\n\t"
        "ds_read_b64_tr_b16 v[118:119], v129 offset:3232\n\t"
        "v_mfma_f32_16x16x32_bf16 v[68:71], v[140:143], v[100:103], v[68:71]\n\t"
        "ds_read_b64_tr_b16 v[96:97], v130 offset:128\n\t"
        "ds_read_b32 v126, v125\n\t"
        "v_mfma_f32_16x16x32_bf16 v[76:79], v[144:147], v[100:103], v[76:79]\n\t"
        "ds_read_b64_tr_b16 v[98:99], v130 offset:3200\n\t"
        "s_waitcnt lgkmcnt(7)\n\t"
        "v_mfma_f32_16x16x32_bf16 v[80:83], v[140:143], v[104:107], v[80:83]\n\t"
        "ds_read_b64_tr_b16 v[100:101], v130 offset:160\n\t"
        "v_mfma_f32_16x16x32_bf16 v[88:91], v[144:147], v[104:107], v[88:91]\n\t"
        "ds_read_b64_tr_b16 v[102:103], v130 offset:3232\n\t"
        "v_mfma_f32_16x16x32_bf16 v[84:87], v[140:143], v[108:111], v[84:87]\n\t"
        "ds_read_b64_tr_b16 v[104:105], v129 offset:0\n\t"
        "v_add_u32 v123, s46, v121\n\t"
        "v_mfma_f32_16x16x32_bf16 v[92:95], v[144:147], v[108:111], v[92:95]\n\t"
        "ds_read_b64_tr_b16 v[106:107], v129 offset:3072\n\t"
        "s_waitcnt lgkmcnt(5)\n\t"
        "v_mfma_f32_16x16x32_bf16 v[32:35], v[112:115], v[140:143], v[32:35]\n\t"
        "ds_read_b64_tr_b16 v[108:109], v129 offset:32\n\t"
        "v_cmp_gt_u32 vcc, v128, v126\n\t"
        "v_mfma_f32_16x16x32_bf16 v[40:43], v[112:115], v[144:147], v[40:43]\n\t"
        "ds_read_b64_tr_b16 v[110:111], v129 offset:3104\n\t"
        "v_add_u32 v124, s46, v122\n\t"
        "v_mfma_f32_16x16x32_bf16 v[36:39], v[116:119], v[140:143], v[36:39]\n\t"
        "ds_read_b64_tr_b16 v[112:113], v130 offset:0\n\t"
        "buffer_load_dwordx4 v[188:191], v131, s[40:43], 0 offen offset:2048 sc1\n\t"
        "v_mfma_f32_16x16x32_bf16 v[44:47], v[116:119], v[144:147], v[44:47]\n\t"
        "ds_read_b64_tr_b16 v[114:115], v130 offset:3072\n\t"
        "s_waitcnt lgkmcnt(6)\n\t"
        "v_mfma_f32_16x16x32_bf16 v[48:51], v[96:99], v[140:143], v[48:51]\n\t"
        "ds_read_b64_tr_b16 v[116:117], v130 offset:32\n\t"
        "v_mfma_f32_16x16x32_bf16 v[56:59], v[96:99], v[144:147], v[56:59]\n\t"
        "ds_read_b64_tr_b16 v[118:119], v130 offset:3104\n\t"
        "s_cbranch_vccnz 20f\n\t"
        "21:\n\t"
        "v_mfma_f32_16x16x32_bf16 v[52:55], v[100:103], v[140:143], v[52:55]\n\t"
        "ds_read_b64_tr_b16 v[96:97], v123 offset:256\n\t"
        "s_addk_i32 s46, 12352\n\t"
        "v_mfma_f32_16x16x32_bf16 v[60:63], v[100:103], v[144:147], v[60:63]\n\t"
        "ds_read_b64_tr_b16 v[98:99], v123 offset:3328\n\t"
        "s_waitcnt lgkmcnt(6)\n\t"
        "v_mfma_f32_16x16x32_bf16 v[0:3], v[104:107], v[140:143], v[0:3]\n\t"
        "ds_read_b64_tr_b16 v[100:101], v123 offset:288\n\t"
        "s_cmp_eq_u32 s46, s47\n\t"
        "v_mfma_f32_16x16x32_bf16 v[8:11], v[104:107], v[144:147], v[8:11]\n\t"
        "ds_read_b64_tr_b16 v[102:103], v123 offset:3360\n\t"
        "s_cselect_b32 s46, 0, s46\n\t"
        "v_mfma_f32_16x16x32_bf16 v[4:7], v[108:111], v[140:143], v[4:7]\n\t"
        "ds_read_b64_tr_b16 v[104:105], v124 offset:256\n\t"
        "buffer_load_dwordx4 v[192:195], v131, s[40:43], 0 offen offset:2304 sc1\n\t"
        "v_mfma_f32_16x16x32_bf16 v[12:15], v[108:111], v[144:147], v[12:15]\n\t"
        "ds_read_b64_tr_b16 v[106:107], v124 offset:3328\n\t"
        "s_waitcnt lgkmcnt(6)\n\t"
        "v_mfma_f32_16x16x32_bf16 v[16:19], v[112:115], v[140:143], v[16:19]\n\t"
        "ds_write_b32 v127, v128\n\t"
        "ds_read_b64_tr_b16 v[108:109], v124 offset:288\n\t"
        "v_mfma_f32_16x16x32_bf16 v[24:27], v[112:115], v[144:147], v[24:27]\n\t"
        "ds_read_b64_tr_b16 v[110:111], v124 offset:3360\n\t"
        "v_add_u32 v128, 2, v128\n\t"
        "v_mfma_f32_16x16x32_bf16 v[20:23], v[116:119], v[140:143], v[20:23]\n\t"
        "ds_read_b64_tr_b16 v[112:113], v123 offset:128\n\t"
        "s_sub_u32 s45, s45, 1\n\t"
        "v_mfma_f32_16x16x32_bf16 v[28:31], v[116:119], v[144:147], v[28:31]\n\t"
        "ds_read_b64_tr_b16 v[114:115], v123 offset:3200\n\t"
        "s_cbranch_scc0 1b\n\t"
        "s_waitcnt vmcnt(10) lgkmcnt(7)\n\t"
        "v_mfma_f32_16x16x32_bf16 v[64:67], v[148:151], v[96:99], v[64:67]\n\t"
        "ds_read_b64_tr_b16 v[116:117], v121 offset:256\n\t"
        "ds_read_b32 v126, v125\n\t"
        "v_mfma_f32_16x16x32_bf16 v[72:75], v[152:155], v[96:99], v[72:75]\n\t"
        "ds_read_b64_tr_b16 v[118:119], v121 offset:3328\n\t"
        "v_add_u32 v128, 2, v128\n\t"
        "v_mfma_f32_16x16x32_bf16 v[68:71], v[148:151], v[100:103], v[68:71]\n\t"
        "ds_read_b64_tr_b16 v[96:97], v121 offset:288\n\t"
        "v_mfma_f32_16x16x32_bf16 v[76:79], v[152:155], v[100:103], v[76:79]\n\t"
        "ds_read_b64_tr_b16 v[98:99], v121 offset:3360\n\t"
        "s_waitcnt lgkmcnt(3)\n\t"
        "v_mfma_f32_16x16x32_bf16 v[80:83], v[148:151], v[104:107], v[80:83]\n\t"
        "ds_read_b64_tr_b16 v[100:101], v122 offset:256\n\t"
        "v_cmp_gt_u32 vcc, v128, v126\n\t"
        "v_mfma_f32_16x16x32_bf16 v[88:91], v[152:155], v[104:107], v[88:91]\n\t"
        "ds_read_b64_tr_b16 v[102:103], v122 offset:3328\n\t"
        "v_mfma_f32_16x16x32_bf16 v[84:87], v[148:151], v[108:111], v[84:87]\n\t"
        "ds_read_b64_tr_b16 v[104:105], v122 offset:288\n\t"
        "v_mfma_f32_16x16x32_bf16 v[92:95], v[152:155], v[108:111], v[92:95]\n\t"
        "ds_read_b64_tr_b16 v[106:107], v122 offset:3360\n\t"
        "s_cbranch_vccnz 38f\n\t"
        "39:\n\t"
        "s_waitcnt vmcnt(8) lgkmcnt(6)\n\t"
        "v_mfma_f32_16x16x32_bf16 v[32:35], v[112:115], v[148:151], v[32:35]\n\t"
        "ds_read_b64_tr_b16 v[108:109], v121 offset:12608\n\t"
        "v_mfma_f32_16x16x32_bf16 v[40:43], v[112:115], v[152:155], v[40:43]\n\t"
        "ds_read_b64_tr_b16 v[110:111], v121 offset:15680\n\t"
        "v_mfma_f32_16x16x32_bf16 v[64:67], v[156:159], v[116:119], v[64:67]\n\t"
        "ds_read_b64_tr_b16 v[112:113], v121 offset:12640\n\t"
        "v_mfma_f32_16x16x32_bf16 v[72:75], v[160:163], v[116:119], v[72:75]\n\t"
        "ds_read_b64_tr_b16 v[114:115], v121 offset:15712\n\t"
        "s_waitcnt lgkmcnt(6)\n\t"
        "v_mfma_f32_16x16x32_bf16 v[68:71], v[156:159], v[96:99], v[68:71]\n\t"
        "ds_read_b64_tr_b16 v[116:117], v122 offset:12608\n\t"
        "ds_read_b128 v[196:199], v215 offset:0\n\t"
        "v_mfma_f32_16x16x32_bf16 v[76:79], v[160:163], v[96:99], v[76:79]\n\t"
        "ds_read_b64_tr_b16 v[118:119], v122 offset:15680\n\t"
        "v_mfma_f32_16x16x32_bf16 v[80:83], v[156:159], v[100:103], v[80:83]\n\t"
        "ds_read_b64_tr_b16 v[96:97], v122 offset:12640\n\t"
        "v_mfma_f32_16x16x32_bf16 v[88:91], v[160:163], v[100:103], v[88:91]\n\t"
        "ds_read_b64_tr_b16 v[98:99], v122 offset:15712\n\t"
        "s_waitcnt vmcnt(6) lgkmcnt(7)\n\t"
        "v_mfma_f32_16x16x32_bf16 v[84:87], v[156:159], v[104:107], v[84:87]\n\t"
        "ds_read_b64_tr_b16 v[100:101], v121 offset:24960\n\t"
        "ds_read_b128 v[200:203], v215 offset:64\n\t"
        "v_mfma_f32_16x16x32_bf16 v[92:95], v[160:163], v[104:107], v[92:95]\n\t"
        "ds_read_b64_tr_b16 v[102:103], v121 offset:28032\n\t"
        "v_mfma_f32_16x16x32_bf16 v[64:67], v[164:167], v[108:111], v[64:67]\n\t"
        "ds_read_b64_tr_b16 v[104:105], v121 offset:24992\n\t"
        "v_mfma_f32_16x16x32_bf16 v[72:75], v[168:171], v[108:111], v[72:75]\n\t"
        "ds_read_b64_tr_b16 v[106:107], v121 offset:28064\n\t"
        "s_waitcnt lgkmcnt(7)\n\t"
        "v_mfma_f32_16x16x32_bf16 v[68:71], v[164:167], v[112:115], v[68:71]\n\t"
        "ds_read_b64_tr_b16 v[108:109], v122 offset:24960\n\t"
        "v_mfma_f32_16x16x32_bf16 v[76:79], v[168:171], v[112:115], v[76:79]\n\t"
        "ds_read_b64_tr_b16 v[110:111], v122 offset:28032\n\t"
        "v_mfma_f32_16x16x32_bf16 v[80:83], v[164:167], v[116:119], v[80:83]\n\t"
        "ds_read_b64_tr_b16 v[112:113], v122 offset:24992\n\t"
        "v_mfma_f32_16x16x32_bf16 v[88:91], v[168:171], v[116:119], v[88:91]\n\t"
        "ds_read_b64_tr_b16 v[114:115], v122 offset:28064\n\t"
        "s_waitcnt vmcnt(4) lgkmcnt(6)\n\t"
        "v_mfma_f32_16x16x32_bf16 v[84:87], v[164:167], v[96:99], v[84:87]\n\t"
        "ds_read_b64_tr_b16 v[116:117], v121 offset:37312\n\t"
        "v_mfma_f32_16x16x32_bf16 v[92:95], v[168:171], v[96:99], v[92:95]\n\t"
        "ds_read_b64_tr_b16 v[118:119], v121 offset:40384\n\t"
        "v_mfma_f32_16x16x32_bf16 v[64:67], v[172:175], v[100:103], v[64:67]\n\t"
        "ds_read_b64_tr_b16 v[96:97], v121 offset:37344\n\t"
        "v_mfma_f32_16x16x32_bf16 v[72:75], v[176:179], v[100:103], v[72:75]\n\t"
        "ds_read_b64_tr_b16 v[98:99], v121 offset:40416\n\t"
        "s_waitcnt lgkmcnt(6)\n\t"
        "v_mfma_f32_16x16x32_bf16 v[68:71], v[172:175], v[104:107], v[68:71]\n\t"
        "ds_read_b64_tr_b16 v[100:101], v122 offset:37312\n\t"
        "v_mfma_f32_16x16x32_bf16 v[76:79], v[176:179], v[104:107], v[76:79]\n\t"
        "ds_read_b64_tr_b16 v[102:103], v122 offset:40384\n\t"
        "v_mfma_f32_16x16x32_bf16 v[80:83], v[172:175], v[108:111], v[80:83]\n\t"
        "ds_read_b64_tr_b16 v[104:105], v122 offset:37344\n\t"
        "v_mfma_f32_16x16x32_bf16 v[88:91], v[176:179], v[108:111], v[88:91]\n\t"
        "ds_read_b64_tr_b16 v[106:107], v122 offset:40416\n\t"
        "s_waitcnt vmcnt(2) lgkmcnt(6)\n\t"
        "v_mfma_f32_16x16x32_bf16 v[84:87], v[172:175], v[112:115], v[84:87]\n\t"
        "ds_read_b64_tr_b16 v[108:109], v121 offset:49664\n\t"
        "v_mfma_f32_16x16x32_bf16 v[92:95], v[176:179], v[112:115], v[92:95]\n\t"
        "ds_read_b64_tr_b16 v[110:111], v121 offset:52736\n\t"
        "v_mfma_f32_16x16x32_bf16 v[64:67], v[180:183], v[116:119], v[64:67]\n\t"
        "ds_read_b64_tr_b16 v[112:113], v121 offset:49696\n\t"
        "v_mfma_f32_16x16x32_bf16 v[72:75], v[184:187], v[116:119], v[72:75]\n\t"
        "ds_read_b64_tr_b16 v[114:115], v121 offset:52768\n\t"
        "s_waitcnt lgkmcnt(6)\n\t"
        "v_mfma_f32_16x16x32_bf16 v[68:71], v[180:183], v[96:99], v[68:71]\n\t"
        "ds_read_b64_tr_b16 v[116:117], v122 offset:49664\n\t"
        "v_mfma_f32_16x16x32_bf16 v[76:79], v[184:187], v[96:99], v[76:79]\n\t"
        "ds_read_b64_tr_b16 v[118:119], v122 offset:52736\n\t"
        "v_mfma_f32_16x16x32_bf16 v[80:83], v[180:183], v[100:103], v[80:83]\n\t"
        "ds_read_b64_tr_b16 v[96:97], v122 offset:49696\n\t"
        "v_mfma_f32_16x16x32_bf16 v[88:91], v[184:187], v[100:103], v[88:91]\n\t"
        "ds_read_b64_tr_b16 v[98:99], v122 offset:52768\n\t"
        "s_waitcnt vmcnt(0) lgkmcnt(6)\n\t"
        "v_mfma_f32_16x16x32_bf16 v[84:87], v[180:183], v[104:107], v[84:87]\n\t"
        "ds_read_b64_tr_b16 v[100:101], v123 offset:160\n\t"
        "v_mfma_f32_16x16x32_bf16 v[92:95], v[184:187], v[104:107], v[92:95]\n\t"
        "ds_read_b64_tr_b16 v[102:103], v123 offset:3232\n\t"
        "v_mfma_f32_16x16x32_bf16 v[64:67], v[188:191], v[108:111], v[64:67]\n\t"
        "ds_read_b64_tr_b16 v[104:105], v124 offset:128\n\t"
        "v_mfma_f32_16x16x32_bf16 v[72:75], v[192:195], v[108:111], v[72:75]\n\t"
        "ds_read_b64_tr_b16 v[106:107], v124 offset:3200\n\t"
        "s_waitcnt lgkmcnt(6)\n\t"
        "v_mfma_f32_16x16x32_bf16 v[68:71], v[188:191], v[112:115], v[68:71]\n\t"
        "ds_read_b64_tr_b16 v[108:109], v124 offset:160\n\t"
        "v_mfma_f32_16x16x32_bf16 v[76:79], v[192:195], v[112:115], v[76:79]\n\t"
        "ds_read_b64_tr_b16 v[110:111], v124 offset:3232\n\t"
        "v_mfma_f32_16x16x32_bf16 v[80:83], v[188:191], v[116:119], v[80:83]\n\t"
        "ds_read_b64_tr_b16 v[112:113], v121 offset:128\n\t"
        "v_mfma_f32_16x16x32_bf16 v[88:91], v[192:195], v[116:119], v[88:91]\n\t"
        "ds_read_b64_tr_b16 v[114:115], v121 offset:3200\n\t"
        "s_waitcnt lgkmcnt(6)\n\t"
        "v_mfma_f32_16x16x32_bf16 v[84:87], v[188:191], v[96:99], v[84:87]\n\t"
        "ds_read_b64_tr_b16 v[116:117], v121 offset:160\n\t"
        "v_mfma_f32_16x16x32_bf16 v[92:95], v[192:195], v[96:99], v[92:95]\n\t"
        "ds_read_b64_tr_b16 v[118:119], v121 offset:3232\n\t"
        "v_mfma_f32_16x16x32_bf16 v[36:39], v[100:103], v[148:151], v[36:39]\n\t"
        "ds_read_b64_tr_b16 v[96:97], v122 offset:128\n\t"
        "v_mfma_f32_16x16x32_bf16 v[44:47], v[100:103], v[152:155], v[44:47]\n\t"
        "ds_read_b64_tr_b16 v[98:99], v122 offset:3200\n\t"
        "s_waitcnt lgkmcnt(6)\n\t"
        "v_mfma_f32_16x16x32_bf16 v[48:51], v[104:107], v[148:151], v[48:51]\n\t"
        "ds_read_b64_tr_b16 v[100:101], v122 offset:160\n\t"
        "v_mul_f32 v64, v196, v64\n\t"
        "v_mul_f32 v65, v197, v65\n\t"
        "v_mfma_f32_16x16x32_bf16 v[56:59], v[104:107], v[152:155], v[56:59]\n\t"
        "ds_read_b64_tr_b16 v[102:103], v122 offset:3232\n\t"
        "v_mul_f32 v66, v198, v66\n\t"
        "v_mul_f32 v67, v199, v67\n\t"
        "v_mfma_f32_16x16x32_bf16 v[52:55], v[108:111], v[148:151], v[52:55]\n\t"
        "ds_read_b64_tr_b16 v[104:105], v121 offset:12480\n\t"
        "v_mul_f32 v68, v196, v68\n\t"
        "v_mul_f32 v69, v197, v69\n\t"
        "v_mul_f32 v70, v198, v70\n\t"
        "v_mfma_f32_16x16x32_bf16 v[60:63], v[108:111], v[152:155], v[60:63]\n\t"
        "ds_read_b64_tr_b16 v[106:107], v121 offset:15552\n\t"
        "v_mul_f32 v71, v199, v71\n\t"
        "v_cvt_pk_bf16_f32 v64, v64, v65\n\t"
        "s_waitcnt lgkmcnt(6)\n\t"
        "v_mfma_f32_16x16x32_bf16 v[32:35], v[112:115], v[156:159], v[32:35]\n\t"
        "ds_read_b64_tr_b16 v[108:109], v121 offset:12512\n\t"
        "v_cvt_pk_bf16_f32 v65, v66, v67\n\t"
        "v_cvt_pk_bf16_f32 v66, v68, v69\n\t"
        "v_mfma_f32_16x16x32_bf16 v[40:43], v[112:115], v[160:163], v[40:43]\n\t"
        "ds_read_b64_tr_b16 v[110:111], v121 offset:15584\n\t"
        "v_cvt_pk_bf16_f32 v67, v70, v71\n\t"
        "v_mul_f32 v72, v200, v72\n\t"
        "v_mfma_f32_16x16x32_bf16 v[36:39], v[116:119], v[156:159], v[36:39]\n\t"
        "ds_read_b64_tr_b16 v[112:113], v122 offset:12480\n\t"
        "v_mul_f32 v73, v201, v73\n\t"
        "v_mul_f32 v74, v202, v74\n\t"
        "v_mul_f32 v75, v203, v75\n\t"
        "v_mfma_f32_16x16x32_bf16 v[44:47], v[116:119], v[160:163], v[44:47]\n\t"
        "ds_read_b64_tr_b16 v[114:115], v122 offset:15552\n\t"
        "v_mul_f32 v76, v200, v76\n\t"
        "v_mul_f32 v77, v201, v77\n\t"
        "s_waitcnt lgkmcnt(6)\n\t"
        "v_mfma_f32_16x16x32_bf16 v[48:51], v[96:99], v[156:159], v[48:51]\n\t"
        "ds_read_b64_tr_b16 v[116:117], v122 offset:12512\n\t"
        "v_mul_f32 v78, v202, v78\n\t"
        "v_mul_f32 v79, v203, v79\n\t"
        "v_mfma_f32_16x16x32_bf16 v[56:59], v[96:99], v[160:163], v[56:59]\n\t"
        "ds_read_b64_tr_b16 v[118:119], v122 offset:15584\n\t"
        "v_cvt_pk_bf16_f32 v68, v72, v73\n\t"
        "v_cvt_pk_bf16_f32 v69, v74, v75\n\t"
        "v_mfma_f32_16x16x32_bf16 v[52:55], v[100:103], v[156:159], v[52:55]\n\t"
        "ds_read_b64_tr_b16 v[96:97], v121 offset:24832\n\t"
        "v_cvt_pk_bf16_f32 v70, v76, v77\n\t"
        "v_cvt_pk_bf16_f32 v71, v78, v79\n\t"
        "v_permlane16_swap_b32 v64, v66\n\t"
        "v_mfma_f32_16x16x32_bf16 v[60:63], v[100:103], v[160:163], v[60:63]\n\t"
        "ds_read_b64_tr_b16 v[98:99], v121 offset:27904\n\t"
        "v_permlane16_swap_b32 v65, v67\n\t"
        "v_permlane16_swap_b32 v68, v70\n\t"
        "s_waitcnt lgkmcnt(6)\n\t"
        "v_mfma_f32_16x16x32_bf16 v[32:35], v[104:107], v[164:167], v[32:35]\n\t"
        "ds_read_b64_tr_b16 v[100:101], v121 offset:24864\n\t"
        "v_permlane16_swap_b32 v69, v71\n\t"
        "buffer_store_dwordx4 v[64:67], v213, s[40:43], 0 offen offset:2048 sc1\n\t"
        "v_mfma_f32_16x16x32_bf16 v[40:43], v[104:107], v[168:171], v[40:43]\n\t"
        "ds_read_b64_tr_b16 v[102:103], v121 offset:27936\n\t"
        "buffer_store_dwordx4 v[68:71], v213, s[40:43], 0 offen offset:3072 sc1\n\t"
        "v_mul_f32 v80, v196, v80\n\t"
        "v_mfma_f32_16x16x32_bf16 v[36:39], v[108:111], v[164:167], v[36:39]\n\t"
        "ds_read_b64_tr_b16 v[104:105], v122 offset:24832\n\t"
        "v_mul_f32 v81, v197, v81\n\t"
        "v_mul_f32 v82, v198, v82\n\t"
        "v_mul_f32 v83, v199, v83\n\t"
        "v_mfma_f32_16x16x32_bf16 v[44:47], v[108:111], v[168:171], v[44:47]\n\t"
        "ds_read_b64_tr_b16 v[106:107], v122 offset:27904\n\t"
        "v_mul_f32 v84, v196, v84\n\t"
        "v_mul_f32 v85, v197, v85\n\t"
        "s_waitcnt lgkmcnt(6)\n\t"
        "v_mfma_f32_16x16x32_bf16 v[48:51], v[112:115], v[164:167], v[48:51]\n\t"
        "ds_read_b64_tr_b16 v[108:109], v122 offset:24864\n\t"
        "v_mul_f32 v86, v198, v86\n\t"
        "v_mul_f32 v87, v199, v87\n\t"
        "v_mfma_f32_16x16x32_bf16 v[56:59], v[112:115], v[168:171], v[56:59]\n\t"
        "ds_read_b64_tr_b16 v[110:111], v122 offset:27936\n\t"
        "v_cvt_pk_bf16_f32 v80, v80, v81\n\t"
        "v_cvt_pk_bf16_f32 v81, v82, v83\n\t"
        "v_mfma_f32_16x16x32_bf16 v[52:55], v[116:119], v[164:167], v[52:55]\n\t"
        "ds_read_b64_tr_b16 v[112:113], v121 offset:37184\n\t"
        "v_cvt_pk_bf16_f32 v82, v84, v85\n\t"
        "v_cvt_pk_bf16_f32 v83, v86, v87\n\t"
        "v_mul_f32 v88, v200, v88\n\t"
        "v_mfma_f32_16x16x32_bf16 v[60:63], v[116:119], v[168:171], v[60:63]\n\t"
        "ds_read_b64_tr_b16 v[114:115], v121 offset:40256\n\t"
        "v_mul_f32 v89, v201, v89\n\t"
        "v_mul_f32 v90, v202, v90\n\t"
        "s_waitcnt lgkmcnt(6)\n\t"
        "v_mfma_f32_16x16x32_bf16 v[32:35], v[96:99], v[172:175], v[32:35]\n\t"
        "ds_read_b64_tr_b16 v[116:117], v121 offset:37216\n\t"
        "v_mul_f32 v91, v203, v91\n\t"
        "v_mul_f32 v92, v200, v92\n\t"
        "v_mfma_f32_16x16x32_bf16 v[40:43], v[96:99], v[176:179], v[40:43]\n\t"
        "ds_read_b64_tr_b16 v[118:119], v121 offset:40288\n\t"
        "v_mul_f32 v93, v201, v93\n\t"
        "v_mul_f32 v94, v202, v94\n\t"
        "v_mfma_f32_16x16x32_bf16 v[36:39], v[100:103], v[172:175], v[36:39]\n\t"
        "ds_read_b64_tr_b16 v[96:97], v122 offset:37184\n\t"
        "v_mul_f32 v95, v203, v95\n\t"
        "v_cvt_pk_bf16_f32 v84, v88, v89\n\t"
        "v_cvt_pk_bf16_f32 v85, v90, v91\n\t"
        "v_mfma_f32_16x16x32_bf16 v[44:47], v[100:103], v[176:179], v[44:47]\n\t"
        "ds_read_b64_tr_b16 v[98:99], v122 offset:40256\n\t"
        "v_cvt_pk_bf16_f32 v86, v92, v93\n\t"
        "v_cvt_pk_bf16_f32 v87, v94, v95\n\t"
        "s_waitcnt lgkmcnt(6)\n\t"
        "v_mfma_f32_16x16x32_bf16 v[48:51], v[104:107], v[172:175], v[48:51]\n\t"
        "ds_read_b64_tr_b16 v[100:101], v122 offset:37216\n\t"
        "v_permlane16_swap_b32 v80, v82\n\t"
        "v_permlane16_swap_b32 v81, v83\n\t"
        "v_mfma_f32_16x16x32_bf16 v[56:59], v[104:107], v[176:179], v[56:59]\n\t"
        "ds_read_b64_tr_b16 v[102:103], v122 offset:40288\n\t"
        "v_permlane16_swap_b32 v84, v86\n\t"
        "v_permlane16_swap_b32 v85, v87\n\t"
        "v_mfma_f32_16x16x32_bf16 v[52:55], v[108:111], v[172:175], v[52:55]\n\t"
        "ds_read_b64_tr_b16 v[104:105], v121 offset:49536\n\t"
        "buffer_store_dwordx4 v[80:83], v214, s[40:43], 0 offen offset:2048 sc1\n\t"
        "buffer_store_dwordx4 v[84:87], v214, s[40:43], 0 offen offset:3072 sc1\n\t"
        "v_mfma_f32_16x16x32_bf16 v[60:63], v[108:111], v[176:179], v[60:63]\n\t"
        "ds_read_b64_tr_b16 v[106:107], v121 offset:52608\n\t"
        "s_waitcnt lgkmcnt(6)\n\t"
        "v_mfma_f32_16x16x32_bf16 v[32:35], v[112:115], v[180:183], v[32:35]\n\t"
        "ds_read_b64_tr_b16 v[108:109], v121 offset:49568\n\t"
        "v_mfma_f32_16x16x32_bf16 v[40:43], v[112:115], v[184:187], v[40:43]\n\t"
        "ds_read_b64_tr_b16 v[110:111], v121 offset:52640\n\t"
        "v_mfma_f32_16x16x32_bf16 v[36:39], v[116:119], v[180:183], v[36:39]\n\t"
        "ds_read_b64_tr_b16 v[112:113], v122 offset:49536\n\t"
        "v_mfma_f32_16x16x32_bf16 v[44:47], v[116:119], v[184:187], v[44:47]\n\t"
        "ds_read_b64_tr_b16 v[114:115], v122 offset:52608\n\t"
        "s_waitcnt lgkmcnt(6)\n\t"
        "v_mfma_f32_16x16x32_bf16 v[48:51], v[96:99], v[180:183], v[48:51]\n\t"
        "ds_read_b64_tr_b16 v[116:117], v122 offset:49568\n\t"
        "v_mfma_f32_16x16x32_bf16 v[56:59], v[96:99], v[184:187], v[56:59]\n\t"
        "ds_read_b64_tr_b16 v[118:119], v122 offset:52640\n\t"
        "v_mfma_f32_16x16x32_bf16 v[52:55], v[100:103], v[180:183], v[52:55]\n\t"
        "ds_read_b64_tr_b16 v[96:97], v123 offset:0\n\t"
        "v_mfma_f32_16x16x32_bf16 v[60:63], v[100:103], v[184:187], v[60:63]\n\t"
        "ds_read_b64_tr_b16 v[98:99], v123 offset:3072\n\t"
        "s_waitcnt lgkmcnt(6)\n\t"
        "v_mfma_f32_16x16x32_bf16 v[32:35], v[104:107], v[188:191], v[32:35]\n\t"
        "ds_read_b64_tr_b16 v[100:101], v123 offset:32\n\t"
        "v_mfma_f32_16x16x32_bf16 v[40:43], v[104:107], v[192:195], v[40:43]\n\t"
        "ds_read_b64_tr_b16 v[102:103], v123 offset:3104\n\t"
        "v_mfma_f32_16x16x32_bf16 v[36:39], v[108:111], v[188:191], v[36:39]\n\t"
        "ds_read_b64_tr_b16 v[104:105], v124 offset:0\n\t"
        "v_mfma_f32_16x16x32_bf16 v[44:47], v[108:111], v[192:195], v[44:47]\n\t"
        "ds_read_b64_tr_b16 v[106:107], v124 offset:3072\n\t"
        "s_waitcnt lgkmcnt(6)\n\t"
        "v_mfma_f32_16x16x32_bf16 v[48:51], v[112:115], v[188:191], v[48:51]\n\t"
        "ds_read_b64_tr_b16 v[108:109], v124 offset:32\n\t"
        "v_mfma_f32_16x16x32_bf16 v[56:59], v[112:115], v[192:195], v[56:59]\n\t"
        "ds_read_b64_tr_b16 v[110:111], v124 offset:3104\n\t"
        "v_mfma_f32_16x16x32_bf16 v[52:55], v[116:119], v[188:191], v[52:55]\n\t"
        "ds_read_b64_tr_b16 v[112:113], v121 offset:0\n\t"
        "v_mfma_f32_16x16x32_bf16 v[60:63], v[116:119], v[192:195], v[60:63]\n\t"
        "ds_read_b64_tr_b16 v[114:115], v121 offset:3072\n\t"
        "s_waitcnt lgkmcnt(6)\n\t"
        "v_mfma_f32_16x16x32_bf16 v[0:3], v[96:99], v[148:151], v[0:3]\n\t"
        "ds_read_b64_tr_b16 v[116:117], v121 offset:32\n\t"
        "v_mfma_f32_16x16x32_bf16 v[8:11], v[96:99], v[152:155], v[8:11]\n\t"
        "ds_read_b64_tr_b16 v[118:119], v121 offset:3104\n\t"
        "v_mfma_f32_16x16x32_bf16 v[4:7], v[100:103], v[148:151], v[4:7]\n\t"
        "ds_read_b64_tr_b16 v[96:97], v122 offset:0\n\t"
        "v_mul_f32 v32, v210, v32\n\t"
        "v_mul_f32 v33, v210, v33\n\t"
        "v_mul_f32 v34, v210, v34\n\t"
        "v_mfma_f32_16x16x32_bf16 v[12:15], v[100:103], v[152:155], v[12:15]\n\t"
        "ds_read_b64_tr_b16 v[98:99], v122 offset:3072\n\t"
        "v_mul_f32 v35, v210, v35\n\t"
        "v_mul_f32 v36, v210, v36\n\t"
        "s_waitcnt lgkmcnt(6)\n\t"
        "v_mfma_f32_16x16x32_bf16 v[16:19], v[104:107], v[148:151], v[16:19]\n\t"
        "ds_read_b64_tr_b16 v[100:101], v122 offset:32\n\t"
        "v_mul_f32 v37, v210, v37\n\t"
        "v_mul_f32 v38, v210, v38\n\t"
        "v_mfma_f32_16x16x32_bf16 v[24:27], v[104:107], v[152:155], v[24:27]\n\t"
        "ds_read_b64_tr_b16 v[102:103], v122 offset:3104\n\t"
        "v_mul_f32 v39, v210, v39\n\t"
        "v_cvt_pk_bf16_f32 v32, v32, v33\n\t"
        "v_mfma_f32_16x16x32_bf16 v[20:23], v[108:111], v[148:151], v[20:23]\n\t"
        "ds_read_b64_tr_b16 v[104:105], v121 offset:12352\n\t"
        "v_cvt_pk_bf16_f32 v33, v34, v35\n\t"
        "v_cvt_pk_bf16_f32 v34, v36, v37\n\t"
        "v_cvt_pk_bf16_f32 v35, v38, v39\n\t"
        "v_mfma_f32_16x16x32_bf16 v[28:31], v[108:111], v[152:155], v[28:31]\n\t"
        "ds_read_b64_tr_b16 v[106:107], v121 offset:15424\n\t"
        "v_mul_f32 v40, v211, v40\n\t"
        "v_mul_f32 v41, v211, v41\n\t"
        "s_waitcnt lgkmcnt(6)\n\t"
        "v_mfma_f32_16x16x32_bf16 v[0:3], v[112:115], v[156:159], v[0:3]\n\t"
        "ds_read_b64_tr_b16 v[108:109], v121 offset:12384\n\t"
        "v_mul_f32 v42, v211, v42\n\t"
        "v_mul_f32 v43, v211, v43\n\t"
        "v_mfma_f32_16x16x32_bf16 v[8:11], v[112:115], v[160:163], v[8:11]\n\t"
        "ds_read_b64_tr_b16 v[110:111], v121 offset:15456\n\t"
        "v_mul_f32 v44, v211, v44\n\t"
        "v_mul_f32 v45, v211, v45\n\t"
        "v_mfma_f32_16x16x32_bf16 v[4:7], v[116:119], v[156:159], v[4:7]\n\t"
        "ds_read_b64_tr_b16 v[112:113], v122 offset:12352\n\t"
        "v_mul_f32 v46, v211, v46\n\t"
        "v_mul_f32 v47, v211, v47\n\t"
        "v_cvt_pk_bf16_f32 v36, v40, v41\n\t"
        "v_mfma_f32_16x16x32_bf16 v[12:15], v[116:119], v[160:163], v[12:15]\n\t"
        "ds_read_b64_tr_b16 v[114:115], v122 offset:15424\n\t"
        "v_cvt_pk_bf16_f32 v37, v42, v43\n\t"
        "v_cvt_pk_bf16_f32 v38, v44, v45\n\t"
        "s_waitcnt lgkmcnt(6)\n\t"
        "v_mfma_f32_16x16x32_bf16 v[16:19], v[96:99], v[156:159], v[16:19]\n\t"
        "ds_read_b64_tr_b16 v[116:117], v122 offset:12384\n\t"
        "v_cvt_pk_bf16_f32 v39, v46, v47\n\t"
        "v_permlane16_swap_b32 v32, v36\n\t"
        "v_mfma_f32_16x16x32_bf16 v[24:27], v[96:99], v[160:163], v[24:27]\n\t"
        "ds_read_b64_tr_b16 v[118:119], v122 offset:15456\n\t"
        "v_permlane16_swap_b32 v33, v37\n\t"
        "v_permlane16_swap_b32 v34, v38\n\t"
        "v_mfma_f32_16x16x32_bf16 v[20:23], v[100:103], v[156:159], v[20:23]\n\t"
        "ds_read_b64_tr_b16 v[96:97], v121 offset:24704\n\t"
        "v_permlane16_swap_b32 v35, v39\n\t"
        "buffer_store_dwordx4 v[32:35], v213, s[40:43], 0 offen offset:0 sc1\n\t"
        "buffer_store_dwordx4 v[36:39], v213, s[40:43], 0 offen offset:1024 sc1\n\t"
        "v_mfma_f32_16x16x32_bf16 v[28:31], v[100:103], v[160:163], v[28:31]\n\t"
        "ds_read_b64_tr_b16 v[98:99], v121 offset:27776\n\t"
        "v_mul_f32 v48, v210, v48\n\t"
        "v_mul_f32 v49, v210, v49\n\t"
        "s_waitcnt lgkmcnt(6)\n\t"
        "v_mfma_f32_16x16x32_bf16 v[0:3], v[104:107], v[164:167], v[0:3]\n\t"
        "ds_read_b64_tr_b16 v[100:101], v121 offset:24736\n\t"
        "v_mul_f32 v50, v210, v50\n\t"
        "v_mul_f32 v51, v210, v51\n\t"
        "v_mfma_f32_16x16x32_bf16 v[8:11], v[104:107], v[168:171], v[8:11]\n\t"
        "ds_read_b64_tr_b16 v[102:103], v121 offset:27808\n\t"
        "v_mul_f32 v52, v210, v52\n\t"
        "v_mul_f32 v53, v210, v53\n\t"
        "v_mfma_f32_16x16x32_bf16 v[4:7], v[108:111], v[164:167], v[4:7]\n\t"
        "ds_read_b64_tr_b16 v[104:105], v122 offset:24704\n\t"
        "v_mul_f32 v54, v210, v54\n\t"
        "v_mul_f32 v55, v210, v55\n\t"
        "v_cvt_pk_bf16_f32 v48, v48, v49\n\t"
        "v_mfma_f32_16x16x32_bf16 v[12:15], v[108:111], v[168:171], v[12:15]\n\t"
        "ds_read_b64_tr_b16 v[106:107], v122 offset:27776\n\t"
        "v_cvt_pk_bf16_f32 v49, v50, v51\n\t"
        "v_cvt_pk_bf16_f32 v50, v52, v53\n\t"
        "s_waitcnt lgkmcnt(6)\n\t"
        "v_mfma_f32_16x16x32_bf16 v[16:19], v[112:115], v[164:167], v[16:19]\n\t"
        "ds_read_b64_tr_b16 v[108:109], v122 offset:24736\n\t"
        "v_cvt_pk_bf16_f32 v51, v54, v55\n\t"
        "v_mul_f32 v56, v211, v56\n\t"
        "v_mfma_f32_16x16x32_bf16 v[24:27], v[112:115], v[168:171], v[24:27]\n\t"
        "ds_read_b64_tr_b16 v[110:111], v122 offset:27808\n\t"
        "v_mul_f32 v57, v211, v57\n\t"
        "v_mul_f32 v58, v211, v58\n\t"
        "v_mfma_f32_16x16x32_bf16 v[20:23], v[116:119], v[164:167], v[20:23]\n\t"
        "ds_read_b64_tr_b16 v[112:113], v121 offset:37056\n\t"
        "v_mul_f32 v59, v211, v59\n\t"
        "v_mul_f32 v60, v211, v60\n\t"
        "v_mul_f32 v61, v211, v61\n\t"
        "v_mfma_f32_16x16x32_bf16 v[28:31], v[116:119], v[168:171], v[28:31]\n\t"
        "ds_read_b64_tr_b16 v[114:115], v121 offset:40128\n\t"
        "v_mul_f32 v62, v211, v62\n\t"
        "v_mul_f32 v63, v211, v63\n\t"
        "s_waitcnt lgkmcnt(6)\n\t"
        "v_mfma_f32_16x16x32_bf16 v[0:3], v[96:99], v[172:175], v[0:3]\n\t"
        "ds_read_b64_tr_b16 v[116:117], v121 offset:37088\n\t"
        "v_cvt_pk_bf16_f32 v52, v56, v57\n\t"
        "v_cvt_pk_bf16_f32 v53, v58, v59\n\t"
        "v_mfma_f32_16x16x32_bf16 v[8:11], v[96:99], v[176:179], v[8:11]\n\t"
        "ds_read_b64_tr_b16 v[118:119], v121 offset:40160\n\t"
        "v_cvt_pk_bf16_f32 v54, v60, v61\n\t"
        "v_cvt_pk_bf16_f32 v55, v62, v63\n\t"
        "v_mfma_f32_16x16x32_bf16 v[4:7], v[100:103], v[172:175], v[4:7]\n\t"
        "ds_read_b64_tr_b16 v[96:97], v122 offset:37056\n\t"
        "v_permlane16_swap_b32 v48, v52\n\t"
        "v_permlane16_swap_b32 v49, v53\n\t"
        "v_permlane16_swap_b32 v50, v54\n\t"
        "v_mfma_f32_16x16x32_bf16 v[12:15], v[100:103], v[176:179], v[12:15]\n\t"
        "ds_read_b64_tr_b16 v[98:99], v122 offset:40128\n\t"
        "v_permlane16_swap_b32 v51, v55\n\t"
        "buffer_store_dwordx4 v[48:51], v214, s[40:43], 0 offen offset:0 sc1\n\t"
        "s_waitcnt lgkmcnt(6)\n\t"
        "v_mfma_f32_16x16x32_bf16 v[16:19], v[104:107], v[172:175], v[16:19]\n\t"
        "ds_read_b64_tr_b16 v[100:101], v122 offset:37088\n\t"
        "buffer_store_dwordx4 v[52:55], v214, s[40:43], 0 offen offset:1024 sc1\n\t"
        "v_mfma_f32_16x16x32_bf16 v[24:27], v[104:107], v[176:179], v[24:27]\n\t"
        "ds_read_b64_tr_b16 v[102:103], v122 offset:40160\n\t"
        "v_mfma_f32_16x16x32_bf16 v[20:23], v[108:111], v[172:175], v[20:23]\n\t"
        "ds_read_b64_tr_b16 v[104:105], v121 offset:49408\n\t"
        "v_mfma_f32_16x16x32_bf16 v[28:31], v[108:111], v[176:179], v[28:31]\n\t"
        "ds_read_b64_tr_b16 v[106:107], v121 offset:52480\n\t"
        "s_waitcnt lgkmcnt(6)\n\t"
        "v_mfma_f32_16x16x32_bf16 v[0:3], v[112:115], v[180:183], v[0:3]\n\t"
        "ds_read_b64_tr_b16 v[108:109], v121 offset:49440\n\t"
        "v_mfma_f32_16x16x32_bf16 v[8:11], v[112:115], v[184:187], v[8:11]\n\t"
        "ds_read_b64_tr_b16 v[110:111], v121 offset:52512\n\t"
        "v_mfma_f32_16x16x32_bf16 v[4:7], v[116:119], v[180:183], v[4:7]\n\t"
        "ds_read_b64_tr_b16 v[112:113], v122 offset:49408\n\t"
        "v_mfma_f32_16x16x32_bf16 v[12:15], v[116:119], v[184:187], v[12:15]\n\t"
        "ds_read_b64_tr_b16 v[114:115], v122 offset:52480\n\t"
        "s_waitcnt lgkmcnt(6)\n\t"
        "v_mfma_f32_16x16x32_bf16 v[16:19], v[96:99], v[180:183], v[16:19]\n\t"
        "ds_read_b64_tr_b16 v[116:117], v122 offset:49440\n\t"
        "v_mfma_f32_16x16x32_bf16 v[24:27], v[96:99], v[184:187], v[24:27]\n\t"
        "ds_read_b64_tr_b16 v[118:119], v122 offset:52512\n\t"
        "v_mfma_f32_16x16x32_bf16 v[20:23], v[100:103], v[180:183], v[20:23]\n\t"
        "v_mfma_f32_16x16x32_bf16 v[28:31], v[100:103], v[184:187], v[28:31]\n\t"
        "s_waitcnt lgkmcnt(4)\n\t"
        "v_mfma_f32_16x16x32_bf16 v[0:3], v[104:107], v[188:191], v[0:3]\n\t"
        "v_mfma_f32_16x16x32_bf16 v[8:11], v[104:107], v[192:195], v[8:11]\n\t"
        "v_mfma_f32_16x16x32_bf16 v[4:7], v[108:111], v[188:191], v[4:7]\n\t"
        "v_mfma_f32_16x16x32_bf16 v[12:15], v[108:111], v[192:195], v[12:15]\n\t"
        "s_waitcnt lgkmcnt(0)\n\t"
        "v_mfma_f32_16x16x32_bf16 v[16:19], v[112:115], v[188:191], v[16:19]\n\t"
        "v_mfma_f32_16x16x32_bf16 v[24:27], v[112:115], v[192:195], v[24:27]\n\t"
        "v_mfma_f32_16x16x32_bf16 v[20:23], v[116:119], v[188:191], v[20:23]\n\t"
        "v_mfma_f32_16x16x32_bf16 v[28:31], v[116:119], v[192:195], v[28:31]\n\t"
        "s_nop 7\n\t"
        "s_branch 9f\n\t"
        "12:\n\t"
        "s_mov_b32 s48, 0x10000\n\t"
        "8:\n\t"
        "ds_read_b32 v126, v125\n\t"
        "s_sub_u32 s48, s48, 1\n\t"
        "s_waitcnt lgkmcnt(0)\n\t"
        "s_cbranch_scc1 13b\n\t"
        "v_cmp_gt_u32 vcc, v128, v126\n\t"
        "s_cbranch_vccnz 8b\n\t"
        "s_branch 13b\n\t"
        "14:\n\t"
        "s_mov_b32 s48, 0x10000\n\t"
        "8:\n\t"
        "ds_read_b32 v126, v125\n\t"
        "s_sub_u32 s48, s48, 1\n\t"
        "s_waitcnt lgkmcnt(0)\n\t"
        "s_cbranch_scc1 15b\n\t"
        "v_cmp_gt_u32 vcc, v128, v126\n\t"
        "s_cbranch_vccnz 8b\n\t"
        "s_branch 15b\n\t"
        "16:\n\t"
        "s_mov_b32 s48, 0x10000\n\t"
        "8:\n\t"
        "ds_read_b32 v126, v125\n\t"
        "s_sub_u32 s48, s48, 1\n\t"
        "s_waitcnt lgkmcnt(0)\n\t"
        "s_cbranch_scc1 17b\n\t"
        "v_cmp_gt_u32 vcc, v128, v126\n\t"
        "s_cbranch_vccnz 8b\n\t"
        "s_branch 17b\n\t"
        "18:\n\t"
        "s_mov_b32 s48, 0x10000\n\t"
        "8:\n\t"
        "ds_read_b32 v126, v125\n\t"
        "s_sub_u32 s48, s48, 1\n\t"
        "s_waitcnt lgkmcnt(0)\n\t"
        "s_cbranch_scc1 19b\n\t"
        "v_cmp_gt_u32 vcc, v128, v126\n\t"
        "s_cbranch_vccnz 8b\n\t"
        "s_branch 19b\n\t"
        "20:\n\t"
        "s_mov_b32 s48, 0x10000\n\t"
        "8:\n\t"
        "ds_read_b32 v126, v125\n\t"
        "s_sub_u32 s48, s48, 1\n\t"
        "s_waitcnt lgkmcnt(0)\n\t"
        "s_cbranch_scc1 21b\n\t"
        "v_cmp_gt_u32 vcc, v128, v126\n\t"
        "s_cbranch_vccnz 8b\n\t"
        "s_branch 21b\n\t"
        "38:\n\t"
        "s_mov_b32 s48, 0x10000\n\t"
        "8:\n\t"
        "ds_read_b32 v126, v125\n\t"
        "s_sub_u32 s48, s48, 1\n\t"
        "s_waitcnt lgkmcnt(0)\n\t"
        "s_cbranch_scc1 39b\n\t"
        "v_cmp_gt_u32 vcc, v128, v126\n\t"
        "s_cbranch_vccnz 8b\n\t"
        "s_branch 39b\n\t"
        "9:\n\t"
        : "+{v[0:15]}"(acc[0]), "+{v[16:31]}"(acc[1]), "+{v[32:47]}"(acc[2]), "+{v[48:63]}"(acc[3]), "+{v[64:79]}"(acc[4]), "+{v[80:95]}"(acc[5]),
          "+{v[96:111]}"(stA), "+{v[112:127]}"(stB), "+{v[128:131]}"(stC), "+{v[148:163]}"(xr1), "+{v[164:179]}"(xr2), "+{v[180:195]}"(xr3),
          "+{s44}"(soff), "+{s46}"(slot), "+{v[140:147]}"(xrb)
        : "{s[40:43]}"(srd), KSKIP, "{v213}"(ep[0]), "{v214}"(ep[1]), "{v215}"(ep[2]), "{v[210:211]}"(al2) : "v196", "v197", "v198", "v199", "v200", "v201", "v202", "v203", "v132", "v133", "v134", "v135", "v136", "v137", "v138", "v139", "s45", "s47", "s48", "vcc", "scc", "memory");
      STAMPV(4 + 16 * L);
      PHASE_VARS;
      u32x4 kv[4][2];
#pragma unroll
      for (int j = 0; j < 4; ++j)
#pragma unroll
        for (int e = 0; e < 8; ++e) kv[j][e >> 2][e & 3] = __float_as_uint(acc[2 + j][e]);
      u32x4 qo[2][2];
      auto drained = [&]() { asm volatile("s_waitcnt vmcnt(0)\n\tds_write_b32 %0, %1" :: "v"(lbase + L_W + 80 + 4 * wid), "v"(2 * L + 1) : "memory"); };
      if (L == 0) {
#pragma unroll
        for (int e = 0; e < 16; ++e) {
          if (e == 8) { SBAR(); drained(); SBAR(); }
          const unsigned w = pack2(acc[e >> 3][2 * (e & 7)], acc[e >> 3][2 * (e & 7) + 1]);
          qo[e >> 3][(e >> 2) & 1][e & 3] = pack2(lo16(w) * QS, hi16(w) * QS);
        }
        row_swap(qo[0][0], qo[0][1]); row_swap(qo[1][0], qo[1][1]);
      } else {
        const f32x2 aq = al2 * QS;
#pragma unroll
        for (int e = 0; e < 16; ++e) qo[e >> 3][(e >> 2) & 1][e & 3] = pack2(acc[e >> 3][2 * (e & 7)] * aq[(e >> 2) & 1], acc[e >> 3][2 * (e & 7) + 1] * aq[(e >> 2) & 1]);
        row_swap(qo[0][0], qo[0][1]); row_swap(qo[1][0], qo[1][1]);
        SBAR();
        lds_wait_ge<0>(lbase + L_W + 4 + 4 * (lane & 1), 1);
        const int idx = (int)__builtin_amdgcn_readfirstlane(words[3]);
        if ((idx >> 5) == tile) {
          const int rel = (idx & 31) - 8 * hi, dfo = ((r32 >> 2) & 1) * 16 + ((r32 >> 3) & 1) * 8 + (r32 >> 4) * 4 + (r32 & 3);
          unsigned pv[2];
#pragma unroll
          for (int j = 0; j < 2; ++j) pv[j] = reinterpret_cast<const u16*>(smem + L_PATCH)[128 + (j ^ hh) * 32 + dfo] * 0x10001u;
          DRAIN();
          if (r32 == (idx & 31)) {
#pragma unroll
            for (int i = 0; i < 4; ++i) {
              const char* p = smem + L_PATCH + ((i >> 1) ^ hh) * 64 + (i & 1) * 32 + hi * 16;
              qo[i >> 1][i & 1] = *reinterpret_cast<const u32x4*>(p);
              kv[i >> 1][i & 1] = *reinterpret_cast<const u32x4*>(p + 128);
              x1p[i] = *reinterpret_cast<const u32x4*>(smem + L_X2P + (i * 2 + hi) * 16);
            }
            reinterpret_cast<float*>(smem + L_AB)[256 + wid * 64 + lane] = hi ? 1.0f : 0.0f;
#pragma unroll
            for (int i = 0; i < 4; ++i) stg(rs, ep[i >> 1] + (i & 1) * 1024, kv[i >> 1][i & 1]);
          }
#pragma unroll
          for (int i = 0; i < 4; ++i) {
#pragma unroll
            for (int e = 0; e < 4; ++e) {
              const int t0 = 16 * (i & 1) + 2 * e;
              const unsigned mask = rel == t0 ? 0xffffu : rel == t0 + 1 ? 0xffff0000u : 0u;
              kv[2 + (i >> 1)][i & 1][e] = (kv[2 + (i >> 1)][i & 1][e] & ~mask) | (pv[i >> 1] & mask);
            }
            stg(rs, (rel >> 3) == 2 * (i & 1) ? ep[i >> 1] + 2048 + (i & 1) * 1024 : OOB, kv[2 + (i >> 1)][i & 1]);
          }
        }
        if (8 < 16) drained();
      }
      SBAR();
#pragma unroll
      for (int j = 0; j < 2; ++j) {
        char* p = smem + L_XCH + ((j ^ hh) * 4 + wid) * 2048 + lane * 16;
        *reinterpret_cast<u32x4*>(p) = qo[j][0]; *reinterpret_cast<u32x4*>(p + 1024) = qo[j][1];
        p = smem + L_XCH + 16384 + wid * 4096 + j * 2048 + lane * 16;
        *reinterpret_cast<u32x4*>(p) = kv[2 * j + 1][0]; *reinterpret_cast<u32x4*>(p + 1024) = kv[2 * j + 1][1];
      }
      if (8 >= 16) drained();
      STAMPV(41 + 4 * L);
      BAR_LGKM();
      STAMPV(43 + 4 * L);
      kva = cat4(kv[0][0], kv[0][1], kv[2][0], kv[2][1]);
      const u32x4* pk = reinterpret_cast<const u32x4*>(smem + L_XCH + 16384 + (wid ^ 1) * 4096 + lane * 16);
#pragma unroll
      for (int u = 0; u < 4; ++u) {
        const u32x4* p = reinterpret_cast<const u32x4*>(smem + L_XCH + (hh * 4 + ((2 * kh + 2 + u) & 3)) * 2048 + lane * 16);
        q[u] = __builtin_shufflevector(p[0], p[64], 0, 1, 2, 3, 4, 5, 6, 7);
      }
      kvb = cat4(pk[0], pk[64], pk[128], pk[192]);
      SBAR();
      asm volatile(
        "v_mov_b64 v[0:1], 0\n\t"
        "v_mov_b64 v[2:3], 0\n\t"
        "v_mov_b64 v[4:5], 0\n\t"
        "v_mov_b64 v[6:7], 0\n\t"
        "v_mov_b64 v[8:9], 0\n\t"
        "v_mov_b64 v[10:11], 0\n\t"
        "v_mov_b64 v[12:13], 0\n\t"
        "v_mov_b64 v[14:15], 0\n\t"
        "v_mov_b64 v[16:17], 0\n\t"
        "v_mov_b64 v[18:19], 0\n\t"
        "v_mov_b64 v[20:21], 0\n\t"
        "v_mov_b64 v[22:23], 0\n\t"
        "v_mov_b64 v[24:25], 0\n\t"
        "v_mov_b64 v[26:27], 0\n\t"
        "v_mov_b64 v[28:29], 0\n\t"
        "v_mov_b64 v[30:31], 0\n\t"
        "v_mov_b64 v[32:33], 0\n\t"
        "v_mov_b64 v[34:35], 0\n\t"
        "v_mov_b64 v[36:37], 0\n\t"
        "v_mov_b64 v[38:39], 0\n\t"
        "v_mov_b64 v[40:41], 0\n\t"
        "v_mov_b64 v[42:43], 0\n\t"
        "v_mov_b64 v[44:45], 0\n\t"
        "v_mov_b64 v[46:47], 0\n\t"
        "v_mov_b64 v[48:49], 0\n\t"
        "v_mov_b64 v[50:51], 0\n\t"
        "v_mov_b64 v[52:53], 0\n\t"
        "v_mov_b64 v[54:55], 0\n\t"
        "v_mov_b64 v[56:57], 0\n\t"
        "v_mov_b64 v[58:59], 0\n\t"
        "v_mov_b64 v[60:61], 0\n\t"
        "v_mov_b64 v[62:63], 0\n\t"
        "v_mov_b64 v[128:129], 0\n\t"
        "v_mov_b64 v[130:131], 0\n\t"
        "v_mov_b64 v[132:133], 0\n\t"
        "v_mov_b64 v[134:135], 0\n\t"
        "v_mov_b64 v[136:137], 0\n\t"
        "v_mov_b64 v[138:139], 0\n\t"
        "v_mov_b64 v[140:141], 0\n\t"
        "v_mov_b64 v[142:143], 0\n\t"
        : "={v[0:15]}"(oz[0]), "={v[16:31]}"(oz[1]), "={v[32:47]}"(oz[2]), "={v[48:63]}"(oz[3]), "={v[128:143]}"(oz[4]));
    }
    STAMPV(5 + 16 * L);
    {
      u32x4 ones;
      unsigned kvoff;
      u32x8 fv, cv;
      asm volatile("" : "={v[184:191]}"(fv), "={v[216:223]}"(cv));
      { PHASE_VARS;
        const unsigned v = (lane & 3) == 0 ? 0x3F803F80u : 0u; const bool up = (lane & 4) != 0; ones[0] = ones[1] = up ? 0u : v; ones[2] = ones[3] = up ? v : 0u;
        kvoff = OFF_KV + L * (10 * MBY) + (tn * 2 + hh) * HS + kh * 8192 + lane * 16;
        fv[1] = lbase + L_FREE + wid * 256 + lane * 4; fv[2] = 32 * L + 32;
        cv[0] = lbase + L_XCH + wid * 8192 + lane * 16; cv[1] = lbase + L_XCH + (wid ^ 2) * 8192 + lane * 16;
        cv[2] = lbase + L_XCH + 32768 + wid * 512 + lane * 4; cv[3] = lbase + L_XCH + 32768 + (wid ^ 2) * 512 + lane * 4;
        cv[4] = lbase + L_W + 48 + wid * 4; cv[5] = lbase + L_W + 48 + (wid ^ 2) * 4; cv[6] = L + 1;
        cv[7] = ((tile * 32 + r32) * D_ + tn * 64 + hi * 8) * 2; }
      f32x16 o0 = oz[0], o1 = oz[1], o2 = oz[2], o3 = oz[3], lv = oz[4];
      unsigned soff = (tm | 7) * 16384;
      STAMP_DECL;
      asm volatile(
        "s_nop 1\n\t"
        "v_mfma_f32_32x32x16_bf16 v[96:111], v[152:155], v[64:67], 0\n\t"
        "v_mfma_f32_32x32x16_bf16 v[96:111], v[156:159], v[68:71], v[96:111]\n\t"
        "s_mov_b32 s46, 0\n\t"
        "s_mov_b64 s[54:55], s[42:43]\n\t"
        "ds_write_b32 v185, v186\n\t"
        "s_nop 8\n\t"
        "v_exp_f32 v96, v96\n\t"
        "v_exp_f32 v97, v97\n\t"
        "v_exp_f32 v98, v98\n\t"
        "v_exp_f32 v99, v99\n\t"
        "v_exp_f32 v100, v100\n\t"
        "v_exp_f32 v101, v101\n\t"
        "v_exp_f32 v102, v102\n\t"
        "v_mfma_f32_32x32x16_bf16 v[112:127], v[152:155], v[72:75], 0\n\t"
        "v_exp_f32 v103, v103\n\t"
        "v_exp_f32 v104, v104\n\t"
        "v_exp_f32 v105, v105\n\t"
        "v_mfma_f32_32x32x16_bf16 v[112:127], v[156:159], v[76:79], v[112:127]\n\t"
        "v_exp_f32 v106, v106\n\t"
        "v_exp_f32 v107, v107\n\t"
        "v_exp_f32 v108, v108\n\t"
        "v_smfmac_f32_32x32x32_bf16 v[0:15], v[160:163], v[96:103], v148\n\t"
        "v_exp_f32 v109, v109\n\t"
        "v_exp_f32 v110, v110\n\t"
        "v_exp_f32 v111, v111\n\t"
        "v_smfmac_f32_16x16x64_bf16 v[128:131], v[144:147], v[96:103], v148\n\t"
        "v_exp_f32 v112, v112\n\t"
        "v_exp_f32 v113, v113\n\t"
        "v_smfmac_f32_32x32x32_bf16 v[0:15], v[164:167], v[104:111], v148\n\t"
        "v_exp_f32 v114, v114\n\t"
        "v_exp_f32 v115, v115\n\t"
        "v_exp_f32 v116, v116\n\t"
        "v_smfmac_f32_16x16x64_bf16 v[128:131], v[144:147], v[104:111], v148\n\t"
        "v_exp_f32 v117, v117\n\t"
        "v_exp_f32 v118, v118\n\t"
        "v_mfma_f32_32x32x16_bf16 v[96:111], v[152:155], v[80:83], 0\n\t"
        "v_exp_f32 v119, v119\n\t"
        "v_exp_f32 v120, v120\n\t"
        "v_exp_f32 v121, v121\n\t"
        "v_mfma_f32_32x32x16_bf16 v[96:111], v[156:159], v[84:87], v[96:111]\n\t"
        "ds_read_b32 v151, v224\n\t"
        "v_exp_f32 v122, v122\n\t"
        "v_exp_f32 v123, v123\n\t"
        "v_exp_f32 v124, v124\n\t"
        "v_smfmac_f32_32x32x32_bf16 v[16:31], v[160:163], v[112:119], v148\n\t"
        "v_exp_f32 v125, v125\n\t"
        "v_exp_f32 v126, v126\n\t"
        "v_exp_f32 v127, v127\n\t"
        "v_smfmac_f32_16x16x64_bf16 v[132:135], v[144:147], v[112:119], v148\n\t"
        "s_cmp_lg_u32 s46, 0\n\t"
        "s_cbranch_scc1 25f\n\t"
        "s_waitcnt lgkmcnt(0)\n\t"
        "v_cmp_ne_u32 vcc, v222, v151\n\t"
        "s_cbranch_vccz 24f\n\t"
        "25:\n\t"
        "v_exp_f32 v96, v96\n\t"
        "v_exp_f32 v97, v97\n\t"
        "v_smfmac_f32_32x32x32_bf16 v[16:31], v[164:167], v[120:127], v148\n\t"
        "v_exp_f32 v98, v98\n\t"
        "v_exp_f32 v99, v99\n\t"
        "v_exp_f32 v100, v100\n\t"
        "v_smfmac_f32_16x16x64_bf16 v[132:135], v[144:147], v[120:127], v148\n\t"
        "v_exp_f32 v101, v101\n\t"
        "v_exp_f32 v102, v102\n\t"
        "v_mfma_f32_32x32x16_bf16 v[112:127], v[152:155], v[88:91], 0\n\t"
        "v_exp_f32 v103, v103\n\t"
        "v_exp_f32 v104, v104\n\t"
        "v_exp_f32 v105, v105\n\t"
        "v_mfma_f32_32x32x16_bf16 v[112:127], v[156:159], v[92:95], v[112:127]\n\t"
        "ds_read_b32 v151, v224\n\t"
        "v_exp_f32 v106, v106\n\t"
        "v_exp_f32 v107, v107\n\t"
        "v_exp_f32 v108, v108\n\t"
        "v_smfmac_f32_32x32x32_bf16 v[32:47], v[160:163], v[96:103], v148\n\t"
        "v_exp_f32 v109, v109\n\t"
        "v_exp_f32 v110, v110\n\t"
        "v_exp_f32 v111, v111\n\t"
        "v_smfmac_f32_16x16x64_bf16 v[136:139], v[144:147], v[96:103], v148\n\t"
        "s_cmp_lg_u32 s46, 0\n\t"
        "s_cbranch_scc1 27f\n\t"
        "s_waitcnt lgkmcnt(0)\n\t"
        "v_cmp_ne_u32 vcc, v222, v151\n\t"
        "s_cbranch_vccz 26f\n\t"
        "27:\n\t"
        "v_exp_f32 v112, v112\n\t"
        "v_exp_f32 v113, v113\n\t"
        "v_smfmac_f32_32x32x32_bf16 v[32:47], v[164:167], v[104:111], v148\n\t"
        "v_exp_f32 v114, v114\n\t"
        "v_exp_f32 v115, v115\n\t"
        "v_exp_f32 v116, v116\n\t"
        "v_smfmac_f32_16x16x64_bf16 v[136:139], v[144:147], v[104:111], v148\n\t"
        "v_exp_f32 v117, v117\n\t"
        "v_exp_f32 v118, v118\n\t"
        "v_mfma_f32_32x32x16_bf16 v[96:111], v[168:171], v[64:67], 0\n\t"
        "v_exp_f32 v119, v119\n\t"
        "v_exp_f32 v120, v120\n\t"
        "v_exp_f32 v121, v121\n\t"
        "v_mfma_f32_32x32x16_bf16 v[96:111], v[172:175], v[68:71], v[96:111]\n\t"
        "ds_read_b32 v151, v224\n\t"
        "v_exp_f32 v122, v122\n\t"
        "v_exp_f32 v123, v123\n\t"
        "v_exp_f32 v124, v124\n\t"
        "v_smfmac_f32_32x32x32_bf16 v[48:63], v[160:163], v[112:119], v148\n\t"
        "v_exp_f32 v125, v125\n\t"
        "v_exp_f32 v126, v126\n\t"
        "v_exp_f32 v127, v127\n\t"
        "v_smfmac_f32_16x16x64_bf16 v[140:143], v[144:147], v[112:119], v148\n\t"
        "s_cmp_lg_u32 s46, 0\n\t"
        "s_cbranch_scc1 29f\n\t"
        "s_waitcnt lgkmcnt(0)\n\t"
        "v_cmp_ne_u32 vcc, v222, v151\n\t"
        "s_cbranch_vccz 28f\n\t"
        "29:\n\t"
        "v_exp_f32 v96, v96\n\t"
        "v_exp_f32 v97, v97\n\t"
        "v_smfmac_f32_32x32x32_bf16 v[48:63], v[164:167], v[120:127], v148\n\t"
        "v_exp_f32 v98, v98\n\t"
        "v_exp_f32 v99, v99\n\t"
        "v_exp_f32 v100, v100\n\t"
        "v_smfmac_f32_16x16x64_bf16 v[140:143], v[144:147], v[120:127], v148\n\t"
        "v_exp_f32 v101, v101\n\t"
        "v_exp_f32 v102, v102\n\t"
        "v_mfma_f32_32x32x16_bf16 v[112:127], v[168:171], v[72:75], 0\n\t"
        "v_exp_f32 v103, v103\n\t"
        "v_exp_f32 v104, v104\n\t"
        "v_exp_f32 v105, v105\n\t"
        "v_mfma_f32_32x32x16_bf16 v[112:127], v[172:175], v[76:79], v[112:127]\n\t"
        "ds_read_b32 v151, v224\n\t"
        "v_exp_f32 v106, v106\n\t"
        "v_exp_f32 v107, v107\n\t"
        "v_exp_f32 v108, v108\n\t"
        "v_smfmac_f32_32x32x32_bf16 v[0:15], v[176:179], v[96:103], v148\n\t"
        "v_exp_f32 v109, v109\n\t"
        "v_exp_f32 v110, v110\n\t"
        "v_exp_f32 v111, v111\n\t"
        "v_smfmac_f32_16x16x64_bf16 v[128:131], v[144:147], v[96:103], v148\n\t"
        "s_cmp_lg_u32 s46, 0\n\t"
        "s_cbranch_scc1 31f\n\t"
        "s_waitcnt lgkmcnt(0)\n\t"
        "v_cmp_ne_u32 vcc, v222, v151\n\t"
        "s_cbranch_vccz 30f\n\t"
        "31:\n\t"
        "v_exp_f32 v112, v112\n\t"
        "v_exp_f32 v113, v113\n\t"
        "v_smfmac_f32_32x32x32_bf16 v[0:15], v[180:183], v[104:111], v148\n\t"
        "v_exp_f32 v114, v114\n\t"
        "v_exp_f32 v115, v115\n\t"
        "v_exp_f32 v116, v116\n\t"
        "v_smfmac_f32_16x16x64_bf16 v[128:131], v[144:147], v[104:111], v148\n\t"
        "v_exp_f32 v117, v117\n\t"
        "v_exp_f32 v118, v118\n\t"
        "v_mfma_f32_32x32x16_bf16 v[96:111], v[168:171], v[80:83], 0\n\t"
        "v_exp_f32 v119, v119\n\t"
        "v_exp_f32 v120, v120\n\t"
        "v_exp_f32 v121, v121\n\t"
        "v_mfma_f32_32x32x16_bf16 v[96:111], v[172:175], v[84:87], v[96:111]\n\t"
        "ds_read_b32 v151, v224\n\t"
        "v_exp_f32 v122, v122\n\t"
        "v_exp_f32 v123, v123\n\t"
        "v_exp_f32 v124, v124\n\t"
        "v_smfmac_f32_32x32x32_bf16 v[16:31], v[176:179], v[112:119], v148\n\t"
        "v_exp_f32 v125, v125\n\t"
        "v_exp_f32 v126, v126\n\t"
        "v_exp_f32 v127, v127\n\t"
        "v_smfmac_f32_16x16x64_bf16 v[132:135], v[144:147], v[112:119], v148\n\t"
        "s_cmp_lg_u32 s46, 0\n\t"
        "s_cbranch_scc1 33f\n\t"
        "s_waitcnt lgkmcnt(0)\n\t"
        "v_cmp_ne_u32 vcc, v222, v151\n\t"
        "s_cbranch_vccz 32f\n\t"
        "33:\n\t"
        "v_exp_f32 v96, v96\n\t"
        "v_exp_f32 v97, v97\n\t"
        "v_smfmac_f32_32x32x32_bf16 v[16:31], v[180:183], v[120:127], v148\n\t"
        "v_exp_f32 v98, v98\n\t"
        "v_exp_f32 v99, v99\n\t"
        "v_exp_f32 v100, v100\n\t"
        "v_smfmac_f32_16x16x64_bf16 v[132:135], v[144:147], v[120:127], v148\n\t"
        "v_exp_f32 v101, v101\n\t"
        "v_exp_f32 v102, v102\n\t"
        "v_mfma_f32_32x32x16_bf16 v[112:127], v[168:171], v[88:91], 0\n\t"
        "v_exp_f32 v103, v103\n\t"
        "v_exp_f32 v104, v104\n\t"
        "v_exp_f32 v105, v105\n\t"
        "v_mfma_f32_32x32x16_bf16 v[112:127], v[172:175], v[92:95], v[112:127]\n\t"
        "v_exp_f32 v106, v106\n\t"
        "v_exp_f32 v107, v107\n\t"
        "v_exp_f32 v108, v108\n\t"
        "v_smfmac_f32_32x32x32_bf16 v[32:47], v[176:179], v[96:103], v148\n\t"
        "v_exp_f32 v109, v109\n\t"
        "v_exp_f32 v110, v110\n\t"
        "v_exp_f32 v111, v111\n\t"
        "v_smfmac_f32_16x16x64_bf16 v[136:139], v[144:147], v[96:103], v148\n\t"
        "s_cmp_lg_u32 s46, 0\n\t"
        "s_cbranch_scc1 5f\n\t"
        "s_mov_b32 s45, 0x200000\n\t"
        "2:\n\t"
        "ds_read_b32 v151, v224\n\t"
        "s_sub_u32 s45, s45, 1\n\t"
        "s_waitcnt lgkmcnt(0)\n\t"
        "s_cbranch_scc1 3f\n\t"
        "v_cmp_ne_u32 vcc, v222, v151\n\t"
        "s_cbranch_vccnz 2b\n\t"
        "3:\n\t"
        "s_add_u32 s44, s44, 0x4000\n\t"
        "s_and_b32 s44, s44, 0x3ffff\n\t"
        "s_cmp_eq_u32 s44, s49\n\t"
        "s_cselect_b32 s44, s50, s44\n\t"
        "s_add_u32 s52, s40, s44\n\t"
        "s_addc_u32 s53, s41, 0\n\t"
        "buffer_load_dwordx4 v[184:187], v149, s[52:55], 0 offen offset:0 sc1\n\t"
        "buffer_load_dwordx4 v[188:191], v149, s[52:55], 0 offen offset:1024 sc1\n\t"
        "buffer_load_dwordx4 v[192:195], v149, s[52:55], 0 offen offset:2048 sc1\n\t"
        "buffer_load_dwordx4 v[196:199], v149, s[52:55], 0 offen offset:3072 sc1\n\t"
        "buffer_load_dwordx4 v[200:203], v150, s[52:55], 0 offen offset:0 sc1\n\t"
        "buffer_load_dwordx4 v[204:207], v150, s[52:55], 0 offen offset:1024 sc1\n\t"
        "buffer_load_dwordx4 v[208:211], v150, s[52:55], 0 offen offset:2048 sc1\n\t"
        "buffer_load_dwordx4 v[212:215], v150, s[52:55], 0 offen offset:3072 sc1\n\t"
        "5:\n\t"
        "s_add_u32 s44, s44, 0x4000\n\t"
        "s_and_b32 s44, s44, 0x3ffff\n\t"
        "s_cmp_eq_u32 s44, s49\n\t"
        "s_cselect_b32 s44, s50, s44\n\t"
        "s_add_u32 s52, s40, s44\n\t"
        "s_addc_u32 s53, s41, 0\n\t"
        "buffer_load_dwordx4 v[152:155], v149, s[52:55], 0 offen offset:0 sc1\n\t"
        "buffer_load_dwordx4 v[156:159], v149, s[52:55], 0 offen offset:1024 sc1\n\t"
        "buffer_load_dwordx4 v[160:163], v149, s[52:55], 0 offen offset:2048 sc1\n\t"
        "buffer_load_dwordx4 v[164:167], v149, s[52:55], 0 offen offset:3072 sc1\n\t"
        "s_mov_b32 s45, 6\n\t"
        "1:\n\t"
        "v_exp_f32 v112, v112\n\t"
        "v_exp_f32 v113, v113\n\t"
        "v_smfmac_f32_32x32x32_bf16 v[32:47], v[180:183], v[104:111], v148\n\t"
        "v_exp_f32 v114, v114\n\t"
        "v_exp_f32 v115, v115\n\t"
        "v_exp_f32 v116, v116\n\t"
        "v_smfmac_f32_16x16x64_bf16 v[136:139], v[144:147], v[104:111], v148\n\t"
        "s_waitcnt vmcnt(8)\n\t"
        "v_exp_f32 v117, v117\n\t"
        "v_exp_f32 v118, v118\n\t"
        "v_mfma_f32_32x32x16_bf16 v[96:111], v[184:187], v[64:67], 0\n\t"
        "v_exp_f32 v119, v119\n\t"
        "v_exp_f32 v120, v120\n\t"
        "v_exp_f32 v121, v121\n\t"
        "v_mfma_f32_32x32x16_bf16 v[96:111], v[188:191], v[68:71], v[96:111]\n\t"
        "v_exp_f32 v122, v122\n\t"
        "v_exp_f32 v123, v123\n\t"
        "v_exp_f32 v124, v124\n\t"
        "v_smfmac_f32_32x32x32_bf16 v[48:63], v[176:179], v[112:119], v148\n\t"
        "v_exp_f32 v125, v125\n\t"
        "v_exp_f32 v126, v126\n\t"
        "v_exp_f32 v127, v127\n\t"
        "v_smfmac_f32_16x16x64_bf16 v[140:143], v[144:147], v[112:119], v148\n\t"
        "buffer_load_dwordx4 v[168:171], v150, s[52:55], 0 offen offset:0 sc1\n\t"
        "v_exp_f32 v96, v96\n\t"
        "v_exp_f32 v97, v97\n\t"
        "v_smfmac_f32_32x32x32_bf16 v[48:63], v[180:183], v[120:127], v148\n\t"
        "v_exp_f32 v98, v98\n\t"
        "v_exp_f32 v99, v99\n\t"
        "v_exp_f32 v100, v100\n\t"
        "v_smfmac_f32_16x16x64_bf16 v[140:143], v[144:147], v[120:127], v148\n\t"
        "v_exp_f32 v101, v101\n\t"
        "v_exp_f32 v102, v102\n\t"
        "v_mfma_f32_32x32x16_bf16 v[112:127], v[184:187], v[72:75], 0\n\t"
        "v_exp_f32 v103, v103\n\t"
        "v_exp_f32 v104, v104\n\t"
        "v_exp_f32 v105, v105\n\t"
        "v_mfma_f32_32x32x16_bf16 v[112:127], v[188:191], v[76:79], v[112:127]\n\t"
        "v_exp_f32 v106, v106\n\t"
        "v_exp_f32 v107, v107\n\t"
        "v_exp_f32 v108, v108\n\t"
        "v_smfmac_f32_32x32x32_bf16 v[0:15], v[192:195], v[96:103], v148\n\t"
        "v_exp_f32 v109, v109\n\t"
        "v_exp_f32 v110, v110\n\t"
        "v_exp_f32 v111, v111\n\t"
        "v_smfmac_f32_16x16x64_bf16 v[128:131], v[144:147], v[96:103], v148\n\t"
        "buffer_load_dwordx4 v[172:175], v150, s[52:55], 0 offen offset:1024 sc1\n\t"
        "v_exp_f32 v112, v112\n\t"
        "v_exp_f32 v113, v113\n\t"
        "v_smfmac_f32_32x32x32_bf16 v[0:15], v[196:199], v[104:111], v148\n\t"
        "v_exp_f32 v114, v114\n\t"
        "v_exp_f32 v115, v115\n\t"
        "v_exp_f32 v116, v116\n\t"
        "v_smfmac_f32_16x16x64_bf16 v[128:131], v[144:147], v[104:111], v148\n\t"
        "v_exp_f32 v117, v117\n\t"
        "v_exp_f32 v118, v118\n\t"
        "v_mfma_f32_32x32x16_bf16 v[96:111], v[184:187], v[80:83], 0\n\t"
        "v_exp_f32 v119, v119\n\t"
        "v_exp_f32 v120, v120\n\t"
        "v_exp_f32 v121, v121\n\t"
        "v_mfma_f32_32x32x16_bf16 v[96:111], v[188:191], v[84:87], v[96:111]\n\t"
        "v_exp_f32 v122, v122\n\t"
        "v_exp_f32 v123, v123\n\t"
        "v_exp_f32 v124, v124\n\t"
        "v_smfmac_f32_32x32x32_bf16 v[16:31], v[192:195], v[112:119], v148\n\t"
        "v_exp_f32 v125, v125\n\t"
        "v_exp_f32 v126, v126\n\t"
        "v_exp_f32 v127, v127\n\t"
        "v_smfmac_f32_16x16x64_bf16 v[132:135], v[144:147], v[112:119], v148\n\t"
        "buffer_load_dwordx4 v[176:179], v150, s[52:55], 0 offen offset:2048 sc1\n\t"
        "v_exp_f32 v96, v96\n\t"
        "v_exp_f32 v97, v97\n\t"
        "v_smfmac_f32_32x32x32_bf16 v[16:31], v[196:199], v[120:127], v148\n\t"
        "v_exp_f32 v98, v98\n\t"
        "v_exp_f32 v99, v99\n\t"
        "v_exp_f32 v100, v100\n\t"
        "v_smfmac_f32_16x16x64_bf16 v[132:135], v[144:147], v[120:127], v148\n\t"
        "v_exp_f32 v101, v101\n\t"
        "v_exp_f32 v102, v102\n\t"
        "v_mfma_f32_32x32x16_bf16 v[112:127], v[184:187], v[88:91], 0\n\t"
        "v_exp_f32 v103, v103\n\t"
        "v_exp_f32 v104, v104\n\t"
        "v_exp_f32 v105, v105\n\t"
        "v_mfma_f32_32x32x16_bf16 v[112:127], v[188:191], v[92:95], v[112:127]\n\t"
        "v_exp_f32 v106, v106\n\t"
        "v_exp_f32 v107, v107\n\t"
        "v_exp_f32 v108, v108\n\t"
        "v_smfmac_f32_32x32x32_bf16 v[32:47], v[192:195], v[96:103], v148\n\t"
        "v_exp_f32 v109, v109\n\t"
        "v_exp_f32 v110, v110\n\t"
        "v_exp_f32 v111, v111\n\t"
        "v_smfmac_f32_16x16x64_bf16 v[136:139], v[144:147], v[96:103], v148\n\t"
        "buffer_load_dwordx4 v[180:183], v150, s[52:55], 0 offen offset:3072 sc1\n\t"
        "v_exp_f32 v112, v112\n\t"
        "v_exp_f32 v113, v113\n\t"
        "v_smfmac_f32_32x32x32_bf16 v[32:47], v[196:199], v[104:111], v148\n\t"
        "s_add_u32 s44, s44, 0x4000\n\t"
        "s_and_b32 s44, s44, 0x3ffff\n\t"
        "v_exp_f32 v114, v114\n\t"
        "v_exp_f32 v115, v115\n\t"
        "v_exp_f32 v116, v116\n\t"
        "v_smfmac_f32_16x16x64_bf16 v[136:139], v[144:147], v[104:111], v148\n\t"
        "s_waitcnt vmcnt(8)\n\t"
        "v_exp_f32 v117, v117\n\t"
        "v_exp_f32 v118, v118\n\t"
        "v_mfma_f32_32x32x16_bf16 v[96:111], v[200:203], v[64:67], 0\n\t"
        "s_cmp_eq_u32 s44, s49\n\t"
        "v_exp_f32 v119, v119\n\t"
        "v_exp_f32 v120, v120\n\t"
        "v_exp_f32 v121, v121\n\t"
        "v_mfma_f32_32x32x16_bf16 v[96:111], v[204:207], v[68:71], v[96:111]\n\t"
        "s_cselect_b32 s44, s50, s44\n\t"
        "v_exp_f32 v122, v122\n\t"
        "v_exp_f32 v123, v123\n\t"
        "v_exp_f32 v124, v124\n\t"
        "v_smfmac_f32_32x32x32_bf16 v[48:63], v[192:195], v[112:119], v148\n\t"
        "s_add_u32 s52, s40, s44\n\t"
        "s_addc_u32 s53, s41, 0\n\t"
        "v_exp_f32 v125, v125\n\t"
        "v_exp_f32 v126, v126\n\t"
        "v_exp_f32 v127, v127\n\t"
        "v_smfmac_f32_16x16x64_bf16 v[140:143], v[144:147], v[112:119], v148\n\t"
        "buffer_load_dwordx4 v[184:187], v149, s[52:55], 0 offen offset:0 sc1\n\t"
        "v_exp_f32 v96, v96\n\t"
        "v_exp_f32 v97, v97\n\t"
        "v_smfmac_f32_32x32x32_bf16 v[48:63], v[196:199], v[120:127], v148\n\t"
        "v_exp_f32 v98, v98\n\t"
        "v_exp_f32 v99, v99\n\t"
        "v_exp_f32 v100, v100\n\t"
        "v_smfmac_f32_16x16x64_bf16 v[140:143], v[144:147], v[120:127], v148\n\t"
        "v_exp_f32 v101, v101\n\t"
        "v_exp_f32 v102, v102\n\t"
        "v_mfma_f32_32x32x16_bf16 v[112:127], v[200:203], v[72:75], 0\n\t"
        "v_exp_f32 v103, v103\n\t"
        "v_exp_f32 v104, v104\n\t"
        "v_exp_f32 v105, v105\n\t"
        "v_mfma_f32_32x32x16_bf16 v[112:127], v[204:207], v[76:79], v[112:127]\n\t"
        "v_exp_f32 v106, v106\n\t"
        "v_exp_f32 v107, v107\n\t"
        "v_exp_f32 v108, v108\n\t"
        "v_smfmac_f32_32x32x32_bf16 v[0:15], v[208:211], v[96:103], v148\n\t"
        "v_exp_f32 v109, v109\n\t"
        "v_exp_f32 v110, v110\n\t"
        "v_exp_f32 v111, v111\n\t"
        "v_smfmac_f32_16x16x64_bf16 v[128:131], v[144:147], v[96:103], v148\n\t"
        "buffer_load_dwordx4 v[188:191], v149, s[52:55], 0 offen offset:1024 sc1\n\t"
        "v_exp_f32 v112, v112\n\t"
        "v_exp_f32 v113, v113\n\t"
        "v_smfmac_f32_32x32x32_bf16 v[0:15], v[212:215], v[104:111], v148\n\t"
        "v_exp_f32 v114, v114\n\t"
        "v_exp_f32 v115, v115\n\t"
        "v_exp_f32 v116, v116\n\t"
        "v_smfmac_f32_16x16x64_bf16 v[128:131], v[144:147], v[104:111], v148\n\t"
        "v_exp_f32 v117, v117\n\t"
        "v_exp_f32 v118, v118\n\t"
        "v_mfma_f32_32x32x16_bf16 v[96:111], v[200:203], v[80:83], 0\n\t"
        "v_exp_f32 v119, v119\n\t"
        "v_exp_f32 v120, v120\n\t"
        "v_exp_f32 v121, v121\n\t"
        "v_mfma_f32_32x32x16_bf16 v[96:111], v[204:207], v[84:87], v[96:111]\n\t"
        "v_exp_f32 v122, v122\n\t"
        "v_exp_f32 v123, v123\n\t"
        "v_exp_f32 v124, v124\n\t"
        "v_smfmac_f32_32x32x32_bf16 v[16:31], v[208:211], v[112:119], v148\n\t"
        "v_exp_f32 v125, v125\n\t"
        "v_exp_f32 v126, v126\n\t"
        "v_exp_f32 v127, v127\n\t"
        "v_smfmac_f32_16x16x64_bf16 v[132:135], v[144:147], v[112:119], v148\n\t"
        "buffer_load_dwordx4 v[192:195], v149, s[52:55], 0 offen offset:2048 sc1\n\t"
        "v_exp_f32 v96, v96\n\t"
        "v_exp_f32 v97, v97\n\t"
        "v_smfmac_f32_32x32x32_bf16 v[16:31], v[212:215], v[120:127], v148\n\t"
        "v_exp_f32 v98, v98\n\t"
        "v_exp_f32 v99, v99\n\t"
        "v_exp_f32 v100, v100\n\t"
        "v_smfmac_f32_16x16x64_bf16 v[132:135], v[144:147], v[120:127], v148\n\t"
        "v_exp_f32 v101, v101\n\t"
        "v_exp_f32 v102, v102\n\t"
        "v_mfma_f32_32x32x16_bf16 v[112:127], v[200:203], v[88:91], 0\n\t"
        "v_exp_f32 v103, v103\n\t"
        "v_exp_f32 v104, v104\n\t"
        "v_exp_f32 v105, v105\n\t"
        "v_mfma_f32_32x32x16_bf16 v[112:127], v[204:207], v[92:95], v[112:127]\n\t"
        "v_exp_f32 v106, v106\n\t"
        "v_exp_f32 v107, v107\n\t"
        "v_exp_f32 v108, v108\n\t"
        "v_smfmac_f32_32x32x32_bf16 v[32:47], v[208:211], v[96:103], v148\n\t"
        "v_exp_f32 v109, v109\n\t"
        "v_exp_f32 v110, v110\n\t"
        "v_exp_f32 v111, v111\n\t"
        "v_smfmac_f32_16x16x64_bf16 v[136:139], v[144:147], v[96:103], v148\n\t"
        "buffer_load_dwordx4 v[196:199], v149, s[52:55], 0 offen offset:3072 sc1\n\t"
        "v_exp_f32 v112, v112\n\t"
        "v_exp_f32 v113, v113\n\t"
        "v_smfmac_f32_32x32x32_bf16 v[32:47], v[212:215], v[104:111], v148\n\t"
        "v_exp_f32 v114, v114\n\t"
        "v_exp_f32 v115, v115\n\t"
        "v_exp_f32 v116, v116\n\t"
        "v_smfmac_f32_16x16x64_bf16 v[136:139], v[144:147], v[104:111], v148\n\t"
        "s_waitcnt vmcnt(8)\n\t"
        "v_exp_f32 v117, v117\n\t"
        "v_exp_f32 v118, v118\n\t"
        "v_mfma_f32_32x32x16_bf16 v[96:111], v[152:155], v[64:67], 0\n\t"
        "v_exp_f32 v119, v119\n\t"
        "v_exp_f32 v120, v120\n\t"
        "v_exp_f32 v121, v121\n\t"
        "v_mfma_f32_32x32x16_bf16 v[96:111], v[156:159], v[68:71], v[96:111]\n\t"
        "v_exp_f32 v122, v122\n\t"
        "v_exp_f32 v123, v123\n\t"
        "v_exp_f32 v124, v124\n\t"
        "v_smfmac_f32_32x32x32_bf16 v[48:63], v[208:211], v[112:119], v148\n\t"
        "v_exp_f32 v125, v125\n\t"
        "v_exp_f32 v126, v126\n\t"
        "v_exp_f32 v127, v127\n\t"
        "v_smfmac_f32_16x16x64_bf16 v[140:143], v[144:147], v[112:119], v148\n\t"
        "buffer_load_dwordx4 v[200:203], v150, s[52:55], 0 offen offset:0 sc1\n\t"
        "v_exp_f32 v96, v96\n\t"
        "v_exp_f32 v97, v97\n\t"
        "v_smfmac_f32_32x32x32_bf16 v[48:63], v[212:215], v[120:127], v148\n\t"
        "v_exp_f32 v98, v98\n\t"
        "v_exp_f32 v99, v99\n\t"
        "v_exp_f32 v100, v100\n\t"
        "v_smfmac_f32_16x16x64_bf16 v[140:143], v[144:147], v[120:127], v148\n\t"
        "v_exp_f32 v101, v101\n\t"
        "v_exp_f32 v102, v102\n\t"
        "v_mfma_f32_32x32x16_bf16 v[112:127], v[152:155], v[72:75], 0\n\t"
        "v_exp_f32 v103, v103\n\t"
        "v_exp_f32 v104, v104\n\t"
        "v_exp_f32 v105, v105\n\t"
        "v_mfma_f32_32x32x16_bf16 v[112:127], v[156:159], v[76:79], v[112:127]\n\t"
        "v_exp_f32 v106, v106\n\t"
        "v_exp_f32 v107, v107\n\t"
        "v_exp_f32 v108, v108\n\t"
        "v_smfmac_f32_32x32x32_bf16 v[0:15], v[160:163], v[96:103], v148\n\t"
        "v_exp_f32 v109, v109\n\t"
        "v_exp_f32 v110, v110\n\t"
        "v_exp_f32 v111, v111\n\t"
        "v_smfmac_f32_16x16x64_bf16 v[128:131], v[144:147], v[96:103], v148\n\t"
        "buffer_load_dwordx4 v[204:207], v150, s[52:55], 0 offen offset:1024 sc1\n\t"
        "v_exp_f32 v112, v112\n\t"
        "v_exp_f32 v113, v113\n\t"
        "v_smfmac_f32_32x32x32_bf16 v[0:15], v[164:167], v[104:111], v148\n\t"
        "v_exp_f32 v114, v114\n\t"
        "v_exp_f32 v115, v115\n\t"
        "v_exp_f32 v116, v116\n\t"
        "v_smfmac_f32_16x16x64_bf16 v[128:131], v[144:147], v[104:111], v148\n\t"
        "v_exp_f32 v117, v117\n\t"
        "v_exp_f32 v118, v118\n\t"
        "v_mfma_f32_32x32x16_bf16 v[96:111], v[152:155], v[80:83], 0\n\t"
        "v_exp_f32 v119, v119\n\t"
        "v_exp_f32 v120, v120\n\t"
        "v_exp_f32 v121, v121\n\t"
        "v_mfma_f32_32x32x16_bf16 v[96:111], v[156:159], v[84:87], v[96:111]\n\t"
        "v_exp_f32 v122, v122\n\t"
        "v_exp_f32 v123, v123\n\t"
        "v_exp_f32 v124, v124\n\t"
        "v_smfmac_f32_32x32x32_bf16 v[16:31], v[160:163], v[112:119], v148\n\t"
        "v_exp_f32 v125, v125\n\t"
        "v_exp_f32 v126, v126\n\t"
        "v_exp_f32 v127, v127\n\t"
        "v_smfmac_f32_16x16x64_bf16 v[132:135], v[144:147], v[112:119], v148\n\t"
        "buffer_load_dwordx4 v[208:211], v150, s[52:55], 0 offen offset:2048 sc1\n\t"
        "v_exp_f32 v96, v96\n\t"
        "v_exp_f32 v97, v97\n\t"
        "v_smfmac_f32_32x32x32_bf16 v[16:31], v[164:167], v[120:127], v148\n\t"
        "v_exp_f32 v98, v98\n\t"
        "v_exp_f32 v99, v99\n\t"
        "v_exp_f32 v100, v100\n\t"
        "v_smfmac_f32_16x16x64_bf16 v[132:135], v[144:147], v[120:127], v148\n\t"
        "v_exp_f32 v101, v101\n\t"
        "v_exp_f32 v102, v102\n\t"
        "v_mfma_f32_32x32x16_bf16 v[112:127], v[152:155], v[88:91], 0\n\t"
        "v_exp_f32 v103, v103\n\t"
        "v_exp_f32 v104, v104\n\t"
        "v_exp_f32 v105, v105\n\t"
        "v_mfma_f32_32x32x16_bf16 v[112:127], v[156:159], v[92:95], v[112:127]\n\t"
        "v_exp_f32 v106, v106\n\t"
        "v_exp_f32 v107, v107\n\t"
        "v_exp_f32 v108, v108\n\t"
        "v_smfmac_f32_32x32x32_bf16 v[32:47], v[160:163], v[96:103], v148\n\t"
        "v_exp_f32 v109, v109\n\t"
        "v_exp_f32 v110, v110\n\t"
        "v_exp_f32 v111, v111\n\t"
        "v_smfmac_f32_16x16x64_bf16 v[136:139], v[144:147], v[96:103], v148\n\t"
        "buffer_load_dwordx4 v[212:215], v150, s[52:55], 0 offen offset:3072 sc1\n\t"
        "v_exp_f32 v112, v112\n\t"
        "v_exp_f32 v113, v113\n\t"
        "v_smfmac_f32_32x32x32_bf16 v[32:47], v[164:167], v[104:111], v148\n\t"
        "s_add_u32 s44, s44, 0x4000\n\t"
        "s_and_b32 s44, s44, 0x3ffff\n\t"
        "v_exp_f32 v114, v114\n\t"
        "v_exp_f32 v115, v115\n\t"
        "v_exp_f32 v116, v116\n\t"
        "v_smfmac_f32_16x16x64_bf16 v[136:139], v[144:147], v[104:111], v148\n\t"
        "s_waitcnt vmcnt(8)\n\t"
        "v_exp_f32 v117, v117\n\t"
        "v_exp_f32 v118, v118\n\t"
        "v_mfma_f32_32x32x16_bf16 v[96:111], v[168:171], v[64:67], 0\n\t"
        "s_cmp_eq_u32 s44, s49\n\t"
        "v_exp_f32 v119, v119\n\t"
        "v_exp_f32 v120, v120\n\t"
        "v_exp_f32 v121, v121\n\t"
        "v_mfma_f32_32x32x16_bf16 v[96:111], v[172:175], v[68:71], v[96:111]\n\t"
        "s_cselect_b32 s44, s50, s44\n\t"
        "v_exp_f32 v122, v122\n\t"
        "v_exp_f32 v123, v123\n\t"
        "v_exp_f32 v124, v124\n\t"
        "v_smfmac_f32_32x32x32_bf16 v[48:63], v[160:163], v[112:119], v148\n\t"
        "s_add_u32 s52, s40, s44\n\t"
        "s_addc_u32 s53, s41, 0\n\t"
        "v_exp_f32 v125, v125\n\t"
        "v_exp_f32 v126, v126\n\t"
        "v_exp_f32 v127, v127\n\t"
        "v_smfmac_f32_16x16x64_bf16 v[140:143], v[144:147], v[112:119], v148\n\t"
        "buffer_load_dwordx4 v[152:155], v149, s[52:55], 0 offen offset:0 sc1\n\t"
        "v_exp_f32 v96, v96\n\t"
        "v_exp_f32 v97, v97\n\t"
        "v_smfmac_f32_32x32x32_bf16 v[48:63], v[164:167], v[120:127], v148\n\t"
        "v_exp_f32 v98, v98\n\t"
        "v_exp_f32 v99, v99\n\t"
        "v_exp_f32 v100, v100\n\t"
        "v_smfmac_f32_16x16x64_bf16 v[140:143], v[144:147], v[120:127], v148\n\t"
        "v_exp_f32 v101, v101\n\t"
        "v_exp_f32 v102, v102\n\t"
        "v_mfma_f32_32x32x16_bf16 v[112:127], v[168:171], v[72:75], 0\n\t"
        "v_exp_f32 v103, v103\n\t"
        "v_exp_f32 v104, v104\n\t"
        "v_exp_f32 v105, v105\n\t"
        "v_mfma_f32_32x32x16_bf16 v[112:127], v[172:175], v[76:79], v[112:127]\n\t"
        "v_exp_f32 v106, v106\n\t"
        "v_exp_f32 v107, v107\n\t"
        "v_exp_f32 v108, v108\n\t"
        "v_smfmac_f32_32x32x32_bf16 v[0:15], v[176:179], v[96:103], v148\n\t"
        "v_exp_f32 v109, v109\n\t"
        "v_exp_f32 v110, v110\n\t"
        "v_exp_f32 v111, v111\n\t"
        "v_smfmac_f32_16x16x64_bf16 v[128:131], v[144:147], v[96:103], v148\n\t"
        "buffer_load_dwordx4 v[156:159], v149, s[52:55], 0 offen offset:1024 sc1\n\t"
        "v_exp_f32 v112, v112\n\t"
        "v_exp_f32 v113, v113\n\t"
        "v_smfmac_f32_32x32x32_bf16 v[0:15], v[180:183], v[104:111], v148\n\t"
        "v_exp_f32 v114, v114\n\t"
        "v_exp_f32 v115, v115\n\t"
        "v_exp_f32 v116, v116\n\t"
        "v_smfmac_f32_16x16x64_bf16 v[128:131], v[144:147], v[104:111], v148\n\t"
        "v_exp_f32 v117, v117\n\t"
        "v_exp_f32 v118, v118\n\t"
        "v_mfma_f32_32x32x16_bf16 v[96:111], v[168:171], v[80:83], 0\n\t"
        "v_exp_f32 v119, v119\n\t"
        "v_exp_f32 v120, v120\n\t"
        "v_exp_f32 v121, v121\n\t"
        "v_mfma_f32_32x32x16_bf16 v[96:111], v[172:175], v[84:87], v[96:111]\n\t"
        "v_exp_f32 v122, v122\n\t"
        "v_exp_f32 v123, v123\n\t"
        "v_exp_f32 v124, v124\n\t"
        "v_smfmac_f32_32x32x32_bf16 v[16:31], v[176:179], v[112:119], v148\n\t"
        "v_exp_f32 v125, v125\n\t"
        "v_exp_f32 v126, v126\n\t"
        "v_exp_f32 v127, v127\n\t"
        "v_smfmac_f32_16x16x64_bf16 v[132:135], v[144:147], v[112:119], v148\n\t"
        "buffer_load_dwordx4 v[160:163], v149, s[52:55], 0 offen offset:2048 sc1\n\t"
        "v_exp_f32 v96, v96\n\t"
        "v_exp_f32 v97, v97\n\t"
        "v_smfmac_f32_32x32x32_bf16 v[16:31], v[180:183], v[120:127], v148\n\t"
        "v_exp_f32 v98, v98\n\t"
        "v_exp_f32 v99, v99\n\t"
        "v_exp_f32 v100, v100\n\t"
        "v_smfmac_f32_16x16x64_bf16 v[132:135], v[144:147], v[120:127], v148\n\t"
        "v_exp_f32 v101, v101\n\t"
        "v_exp_f32 v102, v102\n\t"
        "v_mfma_f32_32x32x16_bf16 v[112:127], v[168:171], v[88:91], 0\n\t"
        "v_exp_f32 v103, v103\n\t"
        "v_exp_f32 v104, v104\n\t"
        "v_exp_f32 v105, v105\n\t"
        "v_mfma_f32_32x32x16_bf16 v[112:127], v[172:175], v[92:95], v[112:127]\n\t"
        "v_exp_f32 v106, v106\n\t"
        "v_exp_f32 v107, v107\n\t"
        "v_exp_f32 v108, v108\n\t"
        "v_smfmac_f32_32x32x32_bf16 v[32:47], v[176:179], v[96:103], v148\n\t"
        "s_sub_u32 s45, s45, 1\n\t"
        "v_exp_f32 v109, v109\n\t"
        "v_exp_f32 v110, v110\n\t"
        "v_exp_f32 v111, v111\n\t"
        "v_smfmac_f32_16x16x64_bf16 v[136:139], v[144:147], v[96:103], v148\n\t"
        "buffer_load_dwordx4 v[164:167], v149, s[52:55], 0 offen offset:3072 sc1\n\t"
        "s_cbranch_scc0 1b\n\t"
        "v_exp_f32 v112, v112\n\t"
        "v_exp_f32 v113, v113\n\t"
        "v_smfmac_f32_32x32x32_bf16 v[32:47], v[180:183], v[104:111], v148\n\t"
        "v_exp_f32 v114, v114\n\t"
        "v_exp_f32 v115, v115\n\t"
        "v_exp_f32 v116, v116\n\t"
        "v_smfmac_f32_16x16x64_bf16 v[136:139], v[144:147], v[104:111], v148\n\t"
        "s_waitcnt vmcnt(8)\n\t"
        "v_exp_f32 v117, v117\n\t"
        "v_exp_f32 v118, v118\n\t"
        "v_mfma_f32_32x32x16_bf16 v[96:111], v[184:187], v[64:67], 0\n\t"
        "v_exp_f32 v119, v119\n\t"
        "v_exp_f32 v120, v120\n\t"
        "v_exp_f32 v121, v121\n\t"
        "v_mfma_f32_32x32x16_bf16 v[96:111], v[188:191], v[68:71], v[96:111]\n\t"
        "v_exp_f32 v122, v122\n\t"
        "v_exp_f32 v123, v123\n\t"
        "v_exp_f32 v124, v124\n\t"
        "v_smfmac_f32_32x32x32_bf16 v[48:63], v[176:179], v[112:119], v148\n\t"
        "v_exp_f32 v125, v125\n\t"
        "v_exp_f32 v126, v126\n\t"
        "v_exp_f32 v127, v127\n\t"
        "v_smfmac_f32_16x16x64_bf16 v[140:143], v[144:147], v[112:119], v148\n\t"
        "v_exp_f32 v96, v96\n\t"
        "v_exp_f32 v97, v97\n\t"
        "v_smfmac_f32_32x32x32_bf16 v[48:63], v[180:183], v[120:127], v148\n\t"
        "v_exp_f32 v98, v98\n\t"
        "v_exp_f32 v99, v99\n\t"
        "v_exp_f32 v100, v100\n\t"
        "v_smfmac_f32_16x16x64_bf16 v[140:143], v[144:147], v[120:127], v148\n\t"
        "v_exp_f32 v101, v101\n\t"
        "v_exp_f32 v102, v102\n\t"
        "v_mfma_f32_32x32x16_bf16 v[112:127], v[184:187], v[72:75], 0\n\t"
        "v_exp_f32 v103, v103\n\t"
        "v_exp_f32 v104, v104\n\t"
        "v_exp_f32 v105, v105\n\t"
        "v_mfma_f32_32x32x16_bf16 v[112:127], v[188:191], v[76:79], v[112:127]\n\t"
        "v_exp_f32 v106, v106\n\t"
        "v_exp_f32 v107, v107\n\t"
        "v_exp_f32 v108, v108\n\t"
        "v_smfmac_f32_32x32x32_bf16 v[0:15], v[192:195], v[96:103], v148\n\t"
        "v_exp_f32 v109, v109\n\t"
        "v_exp_f32 v110, v110\n\t"
        "v_exp_f32 v111, v111\n\t"
        "v_smfmac_f32_16x16x64_bf16 v[128:131], v[144:147], v[96:103], v148\n\t"
        "v_exp_f32 v112, v112\n\t"
        "v_exp_f32 v113, v113\n\t"
        "v_smfmac_f32_32x32x32_bf16 v[0:15], v[196:199], v[104:111], v148\n\t"
        "v_exp_f32 v114, v114\n\t"
        "v_exp_f32 v115, v115\n\t"
        "v_exp_f32 v116, v116\n\t"
        "v_smfmac_f32_16x16x64_bf16 v[128:131], v[144:147], v[104:111], v148\n\t"
        "s_waitcnt vmcnt(4)\n\t"
        "v_exp_f32 v117, v117\n\t"
        "v_exp_f32 v118, v118\n\t"
        "v_mfma_f32_32x32x16_bf16 v[96:111], v[200:203], v[64:67], 0\n\t"
        "v_exp_f32 v119, v119\n\t"
        "v_exp_f32 v120, v120\n\t"
        "v_exp_f32 v121, v121\n\t"
        "v_mfma_f32_32x32x16_bf16 v[96:111], v[204:207], v[68:71], v[96:111]\n\t"
        "v_exp_f32 v122, v122\n\t"
        "v_exp_f32 v123, v123\n\t"
        "v_exp_f32 v124, v124\n\t"
        "v_smfmac_f32_32x32x32_bf16 v[16:31], v[192:195], v[112:119], v148\n\t"
        "v_exp_f32 v125, v125\n\t"
        "v_exp_f32 v126, v126\n\t"
        "v_exp_f32 v127, v127\n\t"
        "v_smfmac_f32_16x16x64_bf16 v[132:135], v[144:147], v[112:119], v148\n\t"
        "s_cmp_lg_u32 s47, 0\n\t"
        "s_cbranch_scc1 4f\n\t"
        "global_load_dwordx4 %[r0], v223, %[xb] offset:0\n\t"
        "global_load_dwordx4 %[r1], v223, %[xb] offset:32\n\t"
        "global_load_dwordx4 %[r2], v223, %[xb] offset:64\n\t"
        "global_load_dwordx4 %[r3], v223, %[xb] offset:96\n\t"
        "4:\n\t"
        "v_exp_f32 v96, v96\n\t"
        "v_exp_f32 v97, v97\n\t"
        "v_smfmac_f32_32x32x32_bf16 v[16:31], v[196:199], v[120:127], v148\n\t"
        "v_exp_f32 v98, v98\n\t"
        "v_exp_f32 v99, v99\n\t"
        "v_exp_f32 v100, v100\n\t"
        "v_smfmac_f32_16x16x64_bf16 v[132:135], v[144:147], v[120:127], v148\n\t"
        "v_exp_f32 v101, v101\n\t"
        "v_exp_f32 v102, v102\n\t"
        "v_mfma_f32_32x32x16_bf16 v[112:127], v[200:203], v[72:75], 0\n\t"
        "v_exp_f32 v103, v103\n\t"
        "v_exp_f32 v104, v104\n\t"
        "v_exp_f32 v105, v105\n\t"
        "v_mfma_f32_32x32x16_bf16 v[112:127], v[204:207], v[76:79], v[112:127]\n\t"
        "v_exp_f32 v106, v106\n\t"
        "v_exp_f32 v107, v107\n\t"
        "v_exp_f32 v108, v108\n\t"
        "v_smfmac_f32_32x32x32_bf16 v[0:15], v[208:211], v[96:103], v148\n\t"
        "v_exp_f32 v109, v109\n\t"
        "v_exp_f32 v110, v110\n\t"
        "v_exp_f32 v111, v111\n\t"
        "v_smfmac_f32_16x16x64_bf16 v[128:131], v[144:147], v[96:103], v148\n\t"
        "v_exp_f32 v112, v112\n\t"
        "v_exp_f32 v113, v113\n\t"
        "v_smfmac_f32_32x32x32_bf16 v[0:15], v[212:215], v[104:111], v148\n\t"
        "v_exp_f32 v114, v114\n\t"
        "v_exp_f32 v115, v115\n\t"
        "v_exp_f32 v116, v116\n\t"
        "v_smfmac_f32_16x16x64_bf16 v[128:131], v[144:147], v[104:111], v148\n\t"
        "v_exp_f32 v117, v117\n\t"
        "v_exp_f32 v118, v118\n\t"
        "v_mfma_f32_32x32x16_bf16 v[96:111], v[184:187], v[80:83], 0\n\t"
        "v_exp_f32 v119, v119\n\t"
        "v_exp_f32 v120, v120\n\t"
        "v_exp_f32 v121, v121\n\t"
        "v_mfma_f32_32x32x16_bf16 v[96:111], v[188:191], v[84:87], v[96:111]\n\t"
        "ds_write_b128 v216, v[0:3] offset:0\n\t"
        "ds_write_b128 v216, v[4:7] offset:1024\n\t"
        "v_exp_f32 v122, v122\n\t"
        "v_exp_f32 v123, v123\n\t"
        "v_exp_f32 v124, v124\n\t"
        "v_smfmac_f32_32x32x32_bf16 v[16:31], v[208:211], v[112:119], v148\n\t"
        "ds_write_b128 v216, v[8:11] offset:2048\n\t"
        "ds_write_b128 v216, v[12:15] offset:3072\n\t"
        "v_exp_f32 v125, v125\n\t"
        "v_exp_f32 v126, v126\n\t"
        "v_exp_f32 v127, v127\n\t"
        "v_smfmac_f32_16x16x64_bf16 v[132:135], v[144:147], v[112:119], v148\n\t"
        "ds_write_b32 v218, v128 offset:0\n\t"
        "v_exp_f32 v96, v96\n\t"
        "v_exp_f32 v97, v97\n\t"
        "v_smfmac_f32_32x32x32_bf16 v[16:31], v[212:215], v[120:127], v148\n\t"
        "v_exp_f32 v98, v98\n\t"
        "v_exp_f32 v99, v99\n\t"
        "v_exp_f32 v100, v100\n\t"
        "v_smfmac_f32_16x16x64_bf16 v[132:135], v[144:147], v[120:127], v148\n\t"
        "v_exp_f32 v101, v101\n\t"
        "v_exp_f32 v102, v102\n\t"
        "v_mfma_f32_32x32x16_bf16 v[112:127], v[184:187], v[88:91], 0\n\t"
        "v_exp_f32 v103, v103\n\t"
        "v_exp_f32 v104, v104\n\t"
        "v_exp_f32 v105, v105\n\t"
        "v_mfma_f32_32x32x16_bf16 v[112:127], v[188:191], v[92:95], v[112:127]\n\t"
        "ds_write_b128 v216, v[16:19] offset:4096\n\t"
        "ds_write_b128 v216, v[20:23] offset:5120\n\t"
        "v_exp_f32 v106, v106\n\t"
        "v_exp_f32 v107, v107\n\t"
        "v_exp_f32 v108, v108\n\t"
        "v_smfmac_f32_32x32x32_bf16 v[32:47], v[192:195], v[96:103], v148\n\t"
        "ds_write_b128 v216, v[24:27] offset:6144\n\t"
        "ds_write_b128 v216, v[28:31] offset:7168\n\t"
        "v_exp_f32 v109, v109\n\t"
        "v_exp_f32 v110, v110\n\t"
        "v_exp_f32 v111, v111\n\t"
        "v_smfmac_f32_16x16x64_bf16 v[136:139], v[144:147], v[96:103], v148\n\t"
        "ds_write_b32 v218, v132 offset:256\n\t"
        "ds_write_b32 v220, v222\n\t"
        "v_exp_f32 v112, v112\n\t"
        "v_exp_f32 v113, v113\n\t"
        "v_smfmac_f32_32x32x32_bf16 v[32:47], v[196:199], v[104:111], v148\n\t"
        "v_exp_f32 v114, v114\n\t"
        "v_exp_f32 v115, v115\n\t"
        "v_exp_f32 v116, v116\n\t"
        "v_smfmac_f32_16x16x64_bf16 v[136:139], v[144:147], v[104:111], v148\n\t"
        "v_exp_f32 v117, v117\n\t"
        "v_exp_f32 v118, v118\n\t"
        "v_mfma_f32_32x32x16_bf16 v[96:111], v[200:203], v[80:83], 0\n\t"
        "v_exp_f32 v119, v119\n\t"
        "v_exp_f32 v120, v120\n\t"
        "v_exp_f32 v121, v121\n\t"
        "v_mfma_f32_32x32x16_bf16 v[96:111], v[204:207], v[84:87], v[96:111]\n\t"
        "v_exp_f32 v122, v122\n\t"
        "v_exp_f32 v123, v123\n\t"
        "v_exp_f32 v124, v124\n\t"
        "v_smfmac_f32_32x32x32_bf16 v[48:63], v[192:195], v[112:119], v148\n\t"
        "v_exp_f32 v125, v125\n\t"
        "v_exp_f32 v126, v126\n\t"
        "v_exp_f32 v127, v127\n\t"
        "v_smfmac_f32_16x16x64_bf16 v[140:143], v[144:147], v[112:119], v148\n\t"
        "v_exp_f32 v96, v96\n\t"
        "v_exp_f32 v97, v97\n\t"
        "v_smfmac_f32_32x32x32_bf16 v[48:63], v[196:199], v[120:127], v148\n\t"
        "v_exp_f32 v98, v98\n\t"
        "v_exp_f32 v99, v99\n\t"
        "v_exp_f32 v100, v100\n\t"
        "v_smfmac_f32_16x16x64_bf16 v[140:143], v[144:147], v[120:127], v148\n\t"
        "v_exp_f32 v101, v101\n\t"
        "v_exp_f32 v102, v102\n\t"
        "v_mfma_f32_32x32x16_bf16 v[112:127], v[200:203], v[88:91], 0\n\t"
        "v_exp_f32 v103, v103\n\t"
        "v_exp_f32 v104, v104\n\t"
        "v_exp_f32 v105, v105\n\t"
        "v_mfma_f32_32x32x16_bf16 v[112:127], v[204:207], v[92:95], v[112:127]\n\t"
        "ds_read_b32 v151, v221\n\t"
        "v_exp_f32 v106, v106\n\t"
        "v_exp_f32 v107, v107\n\t"
        "v_exp_f32 v108, v108\n\t"
        "v_smfmac_f32_32x32x32_bf16 v[32:47], v[208:211], v[96:103], v148\n\t"
        "v_exp_f32 v109, v109\n\t"
        "v_exp_f32 v110, v110\n\t"
        "v_exp_f32 v111, v111\n\t"
        "v_smfmac_f32_16x16x64_bf16 v[136:139], v[144:147], v[96:103], v148\n\t"
        "s_waitcnt lgkmcnt(0)\n\t"
        "v_cmp_ne_u32 vcc, v222, v151\n\t"
        "s_cbranch_vccnz 40f\n\t"
        "41:\n\t"
        "v_exp_f32 v112, v112\n\t"
        "v_exp_f32 v113, v113\n\t"
        "v_smfmac_f32_32x32x32_bf16 v[32:47], v[212:215], v[104:111], v148\n\t"
        "ds_read_b128 v[0:3], v217 offset:0\n\t"
        "ds_read_b128 v[4:7], v217 offset:1024\n\t"
        "ds_read_b128 v[8:11], v217 offset:2048\n\t"
        "ds_read_b128 v[12:15], v217 offset:3072\n\t"
        "v_exp_f32 v114, v114\n\t"
        "v_exp_f32 v115, v115\n\t"
        "v_exp_f32 v116, v116\n\t"
        "v_smfmac_f32_16x16x64_bf16 v[136:139], v[144:147], v[104:111], v148\n\t"
        "ds_read_b32 v128, v219 offset:0\n\t"
        "ds_read_b128 v[16:19], v217 offset:4096\n\t"
        "ds_read_b128 v[20:23], v217 offset:5120\n\t"
        "v_exp_f32 v117, v117\n\t"
        "v_exp_f32 v118, v118\n\t"
        "v_exp_f32 v119, v119\n\t"
        "v_exp_f32 v120, v120\n\t"
        "v_exp_f32 v121, v121\n\t"
        "v_exp_f32 v122, v122\n\t"
        "v_exp_f32 v123, v123\n\t"
        "v_exp_f32 v124, v124\n\t"
        "v_smfmac_f32_32x32x32_bf16 v[48:63], v[208:211], v[112:119], v148\n\t"
        "ds_read_b128 v[24:27], v217 offset:6144\n\t"
        "ds_read_b128 v[28:31], v217 offset:7168\n\t"
        "ds_read_b32 v132, v219 offset:256\n\t"
        "v_exp_f32 v125, v125\n\t"
        "v_exp_f32 v126, v126\n\t"
        "v_exp_f32 v127, v127\n\t"
        "v_smfmac_f32_16x16x64_bf16 v[140:143], v[144:147], v[112:119], v148\n\t"
        "s_nop 1\n\t"
        "v_smfmac_f32_32x32x32_bf16 v[48:63], v[212:215], v[120:127], v148\n\t"
        "v_smfmac_f32_16x16x64_bf16 v[140:143], v[144:147], v[120:127], v148\n\t"
        "s_waitcnt vmcnt(0) lgkmcnt(0)\n\t"
        "s_nop 6\n\t"
        "s_branch 9f\n\t"
        "24:\n\t"
        "s_add_u32 s44, s44, 0x4000\n\t"
        "s_and_b32 s44, s44, 0x3ffff\n\t"
        "s_cmp_eq_u32 s44, s49\n\t"
        "s_cselect_b32 s44, s50, s44\n\t"
        "s_add_u32 s52, s40, s44\n\t"
        "s_addc_u32 s53, s41, 0\n\t"
        "buffer_load_dwordx4 v[184:187], v149, s[52:55], 0 offen offset:0 sc1\n\t"
        "buffer_load_dwordx4 v[188:191], v149, s[52:55], 0 offen offset:1024 sc1\n\t"
        "buffer_load_dwordx4 v[192:195], v149, s[52:55], 0 offen offset:2048 sc1\n\t"
        "buffer_load_dwordx4 v[196:199], v149, s[52:55], 0 offen offset:3072 sc1\n\t"
        "buffer_load_dwordx4 v[200:203], v150, s[52:55], 0 offen offset:0 sc1\n\t"
        "buffer_load_dwordx4 v[204:207], v150, s[52:55], 0 offen offset:1024 sc1\n\t"
        "buffer_load_dwordx4 v[208:211], v150, s[52:55], 0 offen offset:2048 sc1\n\t"
        "buffer_load_dwordx4 v[212:215], v150, s[52:55], 0 offen offset:3072 sc1\n\t"
        "s_mov_b32 s46, 1\n\t"
        "s_branch 25b\n\t"
        "26:\n\t"
        "s_add_u32 s44, s44, 0x4000\n\t"
        "s_and_b32 s44, s44, 0x3ffff\n\t"
        "s_cmp_eq_u32 s44, s49\n\t"
        "s_cselect_b32 s44, s50, s44\n\t"
        "s_add_u32 s52, s40, s44\n\t"
        "s_addc_u32 s53, s41, 0\n\t"
        "buffer_load_dwordx4 v[184:187], v149, s[52:55], 0 offen offset:0 sc1\n\t"
        "buffer_load_dwordx4 v[188:191], v149, s[52:55], 0 offen offset:1024 sc1\n\t"
        "buffer_load_dwordx4 v[192:195], v149, s[52:55], 0 offen offset:2048 sc1\n\t"
        "buffer_load_dwordx4 v[196:199], v149, s[52:55], 0 offen offset:3072 sc1\n\t"
        "buffer_load_dwordx4 v[200:203], v150, s[52:55], 0 offen offset:0 sc1\n\t"
        "buffer_load_dwordx4 v[204:207], v150, s[52:55], 0 offen offset:1024 sc1\n\t"
        "buffer_load_dwordx4 v[208:211], v150, s[52:55], 0 offen offset:2048 sc1\n\t"
        "buffer_load_dwordx4 v[212:215], v150, s[52:55], 0 offen offset:3072 sc1\n\t"
        "s_mov_b32 s46, 1\n\t"
        "s_branch 27b\n\t"
        "28:\n\t"
        "s_add_u32 s44, s44, 0x4000\n\t"
        "s_and_b32 s44, s44, 0x3ffff\n\t"
        "s_cmp_eq_u32 s44, s49\n\t"
        "s_cselect_b32 s44, s50, s44\n\t"
        "s_add_u32 s52, s40, s44\n\t"
        "s_addc_u32 s53, s41, 0\n\t"
        "buffer_load_dwordx4 v[184:187], v149, s[52:55], 0 offen offset:0 sc1\n\t"
        "buffer_load_dwordx4 v[188:191], v149, s[52:55], 0 offen offset:1024 sc1\n\t"
        "buffer_load_dwordx4 v[192:195], v149, s[52:55], 0 offen offset:2048 sc1\n\t"
        "buffer_load_dwordx4 v[196:199], v149, s[52:55], 0 offen offset:3072 sc1\n\t"
        "buffer_load_dwordx4 v[200:203], v150, s[52:55], 0 offen offset:0 sc1\n\t"
        "buffer_load_dwordx4 v[204:207], v150, s[52:55], 0 offen offset:1024 sc1\n\t"
        "buffer_load_dwordx4 v[208:211], v150, s[52:55], 0 offen offset:2048 sc1\n\t"
        "buffer_load_dwordx4 v[212:215], v150, s[52:55], 0 offen offset:3072 sc1\n\t"
        "s_mov_b32 s46, 1\n\t"
        "s_branch 29b\n\t"
        "30:\n\t"
        "s_add_u32 s44, s44, 0x4000\n\t"
        "s_and_b32 s44, s44, 0x3ffff\n\t"
        "s_cmp_eq_u32 s44, s49\n\t"
        "s_cselect_b32 s44, s50, s44\n\t"
        "s_add_u32 s52, s40, s44\n\t"
        "s_addc_u32 s53, s41, 0\n\t"
        "buffer_load_dwordx4 v[184:187], v149, s[52:55], 0 offen offset:0 sc1\n\t"
        "buffer_load_dwordx4 v[188:191], v149, s[52:55], 0 offen offset:1024 sc1\n\t"
        "buffer_load_dwordx4 v[192:195], v149, s[52:55], 0 offen offset:2048 sc1\n\t"
        "buffer_load_dwordx4 v[196:199], v149, s[52:55], 0 offen offset:3072 sc1\n\t"
        "buffer_load_dwordx4 v[200:203], v150, s[52:55], 0 offen offset:0 sc1\n\t"
        "buffer_load_dwordx4 v[204:207], v150, s[52:55], 0 offen offset:1024 sc1\n\t"
        "buffer_load_dwordx4 v[208:211], v150, s[52:55], 0 offen offset:2048 sc1\n\t"
        "buffer_load_dwordx4 v[212:215], v150, s[52:55], 0 offen offset:3072 sc1\n\t"
        "s_mov_b32 s46, 1\n\t"
        "s_branch 31b\n\t"
        "32:\n\t"
        "s_add_u32 s44, s44, 0x4000\n\t"
        "s_and_b32 s44, s44, 0x3ffff\n\t"
        "s_cmp_eq_u32 s44, s49\n\t"
        "s_cselect_b32 s44, s50, s44\n\t"
        "s_add_u32 s52, s40, s44\n\t"
        "s_addc_u32 s53, s41, 0\n\t"
        "buffer_load_dwordx4 v[184:187], v149, s[52:55], 0 offen offset:0 sc1\n\t"
        "buffer_load_dwordx4 v[188:191], v149, s[52:55], 0 offen offset:1024 sc1\n\t"
        "buffer_load_dwordx4 v[192:195], v149, s[52:55], 0 offen offset:2048 sc1\n\t"
        "buffer_load_dwordx4 v[196:199], v149, s[52:55], 0 offen offset:3072 sc1\n\t"
        "buffer_load_dwordx4 v[200:203], v150, s[52:55], 0 offen offset:0 sc1\n\t"
        "buffer_load_dwordx4 v[204:207], v150, s[52:55], 0 offen offset:1024 sc1\n\t"
        "buffer_load_dwordx4 v[208:211], v150, s[52:55], 0 offen offset:2048 sc1\n\t"
        "buffer_load_dwordx4 v[212:215], v150, s[52:55], 0 offen offset:3072 sc1\n\t"
        "s_mov_b32 s46, 1\n\t"
        "s_branch 33b\n\t"
        "40:\n\t"
        "s_mov_b32 s45, 0x100000\n\t"
        "42:\n\t"
        "ds_read_b32 v151, v221\n\t"
        "s_sub_u32 s45, s45, 1\n\t"
        "s_waitcnt lgkmcnt(0)\n\t"
        "s_cbranch_scc1 41b\n\t"
        "v_cmp_ne_u32 vcc, v222, v151\n\t"
        "s_cbranch_vccnz 42b\n\t"
        "s_branch 41b\n\t"
        "9:\n\t"
        : "+{v[0:15]}"(o0), "+{v[16:31]}"(o1), "+{v[32:47]}"(o2), "+{v[48:63]}"(o3), "+{v[128:143]}"(lv), "+{s44}"(soff), "+{v[152:167]}"(kva), "+{v[168:183]}"(kvb), "+{v[184:191]}"(fv),
          [r0] "+v"(x1p[0]), [r1] "+v"(x1p[1]), [r2] "+v"(x1p[2]), [r3] "+v"(x1p[3]) STAMP_OUTS
        : "{v[64:71]}"(q[0]), "{v[72:79]}"(q[1]), "{v[80:87]}"(q[2]), "{v[88:95]}"(q[3]), "{v[144:147]}"(ones), "{v148}"(0xddddu), "{v149}"(kvoff), "{v150}"(kvoff + 4096),
          "{v[216:223]}"(cv), "{v224}"(lbase + L_W + 16), "{s[40:43]}"(srd), "{s47}"(L), "{s49}"(tm * 16384), "{s50}"(((tm + 1) & 15) * 16384), [xb] "s"(x0)
        : "v96", "v97", "v98", "v99", "v100", "v101", "v102", "v103", "v104", "v105", "v106", "v107", "v108", "v109", "v110", "v111", "v112", "v113", "v114", "v115", "v116", "v117", "v118", "v119", "v120", "v121", "v122", "v123", "v124", "v125", "v126", "v127", "v151", "v192", "v193", "v194", "v195", "v196", "v197", "v198", "v199", "v200", "v201", "v202", "v203", "v204", "v205", "v206", "v207", "v208", "v209", "v210", "v211", "v212", "v213", "v214", "v215", "s45", "s46", "s52", "s53", "s54", "s55", "vcc", "scc", "memory");
      STAMP_PUT(6 + 16 * L);
      STAMPV(9 + 16 * L);
      PHASE_VARS;
      const unsigned dst = OFF_OF + L * (5 * MBY) + (tm * 4 + 2 * kh) * TS + (tn * 2 + hh) * 2048 + lane * 16;
      auto put = [&](int u, const f32x16& o) {
#pragma unroll
        for (int c = 0; c < 2; ++c) {
          u32x4 w;
#pragma unroll
          for (int e = 0; e < 4; ++e) w[e] = pack2(o[8 * c + 2 * e], o[8 * c + 2 * e + 1]);
          stg(rs, dst + u * TS + c * 1024, w);
        }
      };
      const float la = lv[8] + lv[0], lb = lv[12] + lv[4];
      o2 = (o2 + o0) * RCP(la); put(0, o2);
      SBAR();
      o3 = (o3 + o1) * RCP(lb); put(1, o3);
      SBAR();
      {
        const f32x16 t = o2 + o3;
        float sum = 0.f;
#pragma unroll
        for (int e = 0; e < 16; e += 4) sum += (t[e] + t[e + 1]) + (t[e + 2] + t[e + 3]);
        if (__any(!(fabsf(sum) < 1e37f) | !(fmaxf(la, lb) < 1e27f) | !(fminf(la, lb) > 1e-9f) | !(la + lb == la + lb))) {
          DRAIN();
          attn_safe(rs, kvoff - kh * 8192, q[2], ones, o2); put(0, o2);
          attn_safe(rs, kvoff - kh * 8192, q[3], ones, o3); put(1, o3);
        }
      }
    }
    STAMPV(10 + 16 * L);
    {
      f32x16 acc[8];
      u32x16 wrc;
      u32x16 fl0, fl1, fl2;
      STAMP_DECL;
      { PHASE_VARS;
      float r0 = 0.f, r1 = 1.f;
      if (L == 0) {
#pragma unroll
        for (int i = 0; i < 4; ++i) x1p[i] = fo_swap(x1p[i]);
      } else {
        r0 = reinterpret_cast<const float*>(smem + L_AB)[256 + wid * 64 + r32]; r1 = reinterpret_cast<const float*>(smem + L_AB)[256 + wid * 64 + 32 + r32];
      }
#pragma unroll
      for (int e = 0; e < 16; ++e) { acc[e >> 3][2 * (e & 7)] = lo16(x1p[e >> 2][e & 3]) * r1 + r0; acc[e >> 3][2 * (e & 7) + 1] = hi16(x1p[e >> 2][e & 3]) * r1 + r0; }
      PIN(acc[0]); PIN(acc[1]);
      asm volatile("s_waitcnt vmcnt(0)\n\tds_write_b32 %0, %1" :: "v"(lbase + L_W + 80 + 4 * wid), "v"(2 * L + 2) : "memory");
      STAMPV(11 + 16 * L);
      const unsigned a0 = lbase + wrow * 128 + ((wch ^ wsw) << 4) + 8 * (tp & 1), a1 = lbase + wrow * 128 + (((4 + wch) ^ wsw) << 4) + 8 * (tp & 1);
      asm volatile(
        "s_mov_b32 s46, 0\n\t"
        "ostart_%=:\n\t"
        "s_mov_b32 s48, 0x200000\n\t"
        "2:\n\t"
        "ds_read_b32 v233, v232\n\t"
        "s_sub_u32 s48, s48, 1\n\t"
        "s_waitcnt lgkmcnt(0)\n\t"
        "s_cselect_b32 s47, 15, 0\n\t"
        "v_readfirstlane_b32 s51, v233\n\t"
        "s_nop 3\n\t"
        "s_or_b32 s51, s51, s47\n\t"
        "s_andn2_b32 s47, s51, s46\n\t"
        "s_cbranch_scc0 2b\n\t"
        "s_andn2_b32 s51, s51, s46\n\t"
        "s_ff1_i32_b32 s47, s51\n\t"
        "s_add_u32 s45, s47, s49\n\t"
        "s_and_b32 s45, s45, 3\n\t"
        "s_lshl_b32 s45, s45, 14\n\t"
        "s_and_b32 s51, s47, 0x80000000\n\t"
        "v_or_b32 v225, s51, v224\n\t"
        "s_and_b32 s51, s47, 3\n\t"
        "s_lshl_b32 s51, s51, 15\n\t"
        "v_add_u32 v230, s51, v226\n\t"
        "v_add_u32 v231, s51, v227\n\t"
        "buffer_load_dwordx4 v[160:163], v225, s[40:43], s45 offen offset:0 sc1\n\t"
        "buffer_load_dwordx4 v[164:167], v225, s[40:43], s45 offen offset:1024 sc1\n\t"
        "buffer_load_dwordx4 v[168:171], v225, s[40:43], s45 offen offset:2048 sc1\n\t"
        "buffer_load_dwordx4 v[172:175], v225, s[40:43], s45 offen offset:3072 sc1\n\t"
        "s_add_u32 s45, s45, 0x1000\n\t"
        "buffer_load_dwordx4 v[176:179], v225, s[40:43], s45 offen offset:0 sc1\n\t"
        "buffer_load_dwordx4 v[180:183], v225, s[40:43], s45 offen offset:1024 sc1\n\t"
        "buffer_load_dwordx4 v[184:187], v225, s[40:43], s45 offen offset:2048 sc1\n\t"
        "buffer_load_dwordx4 v[188:191], v225, s[40:43], s45 offen offset:3072 sc1\n\t"
        "s_add_u32 s45, s45, 0x1000\n\t"
        "buffer_load_dwordx4 v[192:195], v225, s[40:43], s45 offen offset:0 sc1\n\t"
        "buffer_load_dwordx4 v[196:199], v225, s[40:43], s45 offen offset:1024 sc1\n\t"
        "buffer_load_dwordx4 v[200:203], v225, s[40:43], s45 offen offset:2048 sc1\n\t"
        "buffer_load_dwordx4 v[204:207], v225, s[40:43], s45 offen offset:3072 sc1\n\t"
        "ds_read_b64_tr_b16 v[128:129], v230 offset:0\n\t"
        "ds_read_b64_tr_b16 v[130:131], v230 offset:1024\n\t"
        "ds_read_b64_tr_b16 v[132:133], v231 offset:0\n\t"
        "ds_read_b64_tr_b16 v[134:135], v231 offset:1024\n\t"
        "ds_read_b64_tr_b16 v[136:137], v230 offset:2048\n\t"
        "ds_read_b64_tr_b16 v[138:139], v230 offset:3072\n\t"
        "ds_read_b64_tr_b16 v[140:141], v231 offset:2048\n\t"
        "ds_read_b64_tr_b16 v[142:143], v231 offset:3072\n\t"
        "ds_read_b64_tr_b16 v[144:145], v230 offset:4096\n\t"
        "ds_read_b64_tr_b16 v[146:147], v230 offset:5120\n\t"
        "ds_read_b64_tr_b16 v[148:149], v231 offset:4096\n\t"
        "ds_read_b64_tr_b16 v[150:151], v231 offset:5120\n\t"
        "ds_read_b64_tr_b16 v[152:153], v230 offset:6144\n\t"
        "ds_read_b64_tr_b16 v[154:155], v230 offset:7168\n\t"
        "ds_read_b64_tr_b16 v[156:157], v231 offset:6144\n\t"
        "ds_read_b64_tr_b16 v[158:159], v231 offset:7168\n\t"
        "s_mov_b32 s44, s45\n\t"
        "s_cmp_eq_u32 s47, 0\n\t"
        "s_cbranch_scc1 oseg0_%=\n\t"
        "s_cmp_eq_u32 s47, 1\n\t"
        "s_cbranch_scc1 oseg1_%=\n\t"
        "s_cmp_eq_u32 s47, 2\n\t"
        "s_cbranch_scc1 oseg2_%=\n\t"
        "s_cmp_eq_u32 s47, 3\n\t"
        "s_cbranch_scc1 oseg3_%=\n\t"
        "oseg0_%=:\n\t"
        "s_or_b32 s46, s46, 1\n\t"
        "s_waitcnt vmcnt(11) lgkmcnt(14)\n\t"
        "v_mfma_f32_32x32x16_bf16 v[0:15], v[128:131], v[160:163], v[0:15]\n\t"
        "ds_read_b64_tr_b16 v[128:129], v226 offset:8192\n\t"
        "ds_read_b64_tr_b16 v[130:131], v226 offset:9216\n\t"
        "s_waitcnt lgkmcnt(14)\n\t"
        "v_mfma_f32_32x32x16_bf16 v[16:31], v[132:135], v[160:163], v[16:31]\n\t"
        "ds_read_b64_tr_b16 v[132:133], v227 offset:8192\n\t"
        "ds_read_b64_tr_b16 v[134:135], v227 offset:9216\n\t"
        "s_add_u32 s44, s44, 0x1000\n\t"
        "buffer_load_dwordx4 v[208:211], v224, s[40:43], s44 offen offset:0 sc1\n\t"
        "s_waitcnt vmcnt(11) lgkmcnt(14)\n\t"
        "v_mfma_f32_32x32x16_bf16 v[0:15], v[136:139], v[164:167], v[0:15]\n\t"
        "ds_read_b32 v233, v232\n\t"
        "ds_read_b64_tr_b16 v[136:137], v226 offset:10240\n\t"
        "ds_read_b64_tr_b16 v[138:139], v226 offset:11264\n\t"
        "s_waitcnt lgkmcnt(14)\n\t"
        "v_mfma_f32_32x32x16_bf16 v[16:31], v[140:143], v[164:167], v[16:31]\n\t"
        "ds_read_b64_tr_b16 v[140:141], v227 offset:10240\n\t"
        "ds_read_b64_tr_b16 v[142:143], v227 offset:11264\n\t"
        "buffer_load_dwordx4 v[212:215], v224, s[40:43], s44 offen offset:1024 sc1\n\t"
        "s_waitcnt vmcnt(11) lgkmcnt(14)\n\t"
        "v_mfma_f32_32x32x16_bf16 v[0:15], v[144:147], v[168:171], v[0:15]\n\t"
        "ds_read_b64_tr_b16 v[144:145], v226 offset:12288\n\t"
        "ds_read_b64_tr_b16 v[146:147], v226 offset:13312\n\t"
        "s_waitcnt lgkmcnt(14)\n\t"
        "v_mfma_f32_32x32x16_bf16 v[16:31], v[148:151], v[168:171], v[16:31]\n\t"
        "ds_read_b64_tr_b16 v[148:149], v227 offset:12288\n\t"
        "ds_read_b64_tr_b16 v[150:151], v227 offset:13312\n\t"
        "buffer_load_dwordx4 v[216:219], v224, s[40:43], s44 offen offset:2048 sc1\n\t"
        "s_waitcnt vmcnt(11) lgkmcnt(8)\n\t"
        "v_mfma_f32_32x32x16_bf16 v[0:15], v[152:155], v[172:175], v[0:15]\n\t"
        "v_readfirstlane_b32 s51, v233\n\t"
        "ds_read_b64_tr_b16 v[152:153], v226 offset:14336\n\t"
        "ds_read_b64_tr_b16 v[154:155], v226 offset:15360\n\t"
        "s_waitcnt lgkmcnt(14)\n\t"
        "v_mfma_f32_32x32x16_bf16 v[16:31], v[156:159], v[172:175], v[16:31]\n\t"
        "s_andn2_b32 s51, s51, s46\n\t"
        "s_ff1_i32_b32 s47, s51\n\t"
        "s_add_u32 s45, s47, s49\n\t"
        "s_and_b32 s45, s45, 3\n\t"
        "s_lshl_b32 s45, s45, 14\n\t"
        "ds_read_b64_tr_b16 v[156:157], v227 offset:14336\n\t"
        "ds_read_b64_tr_b16 v[158:159], v227 offset:15360\n\t"
        "buffer_load_dwordx4 v[220:223], v224, s[40:43], s44 offen offset:3072 sc1\n\t"
        "s_waitcnt vmcnt(11) lgkmcnt(14)\n\t"
        "v_mfma_f32_32x32x16_bf16 v[0:15], v[128:131], v[176:179], v[0:15]\n\t"
        "s_and_b32 s51, s47, 0x80000000\n\t"
        "v_or_b32 v225, s51, v224\n\t"
        "s_and_b32 s51, s47, 3\n\t"
        "s_lshl_b32 s51, s51, 15\n\t"
        "v_add_u32 v230, s51, v226\n\t"
        "v_add_u32 v231, s51, v227\n\t"
        "ds_read_b64_tr_b16 v[128:129], v226 offset:16384\n\t"
        "ds_read_b64_tr_b16 v[130:131], v226 offset:17408\n\t"
        "s_waitcnt lgkmcnt(14)\n\t"
        "v_mfma_f32_32x32x16_bf16 v[16:31], v[132:135], v[176:179], v[16:31]\n\t"
        "ds_read_b64_tr_b16 v[132:133], v227 offset:16384\n\t"
        "ds_read_b64_tr_b16 v[134:135], v227 offset:17408\n\t"
        "buffer_load_dwordx4 v[160:163], v225, s[40:43], s45 offen offset:0 sc1\n\t"
        "s_waitcnt vmcnt(11) lgkmcnt(14)\n\t"
        "v_mfma_f32_32x32x16_bf16 v[0:15], v[136:139], v[180:183], v[0:15]\n\t"
        "ds_read_b64_tr_b16 v[136:137], v226 offset:18432\n\t"
        "ds_read_b64_tr_b16 v[138:139], v226 offset:19456\n\t"
        "s_waitcnt lgkmcnt(14)\n\t"
        "v_mfma_f32_32x32x16_bf16 v[16:31], v[140:143], v[180:183], v[16:31]\n\t"
        "ds_read_b64_tr_b16 v[140:141], v227 offset:18432\n\t"
        "ds_read_b64_tr_b16 v[142:143], v227 offset:19456\n\t"
        "buffer_load_dwordx4 v[164:167], v225, s[40:43], s45 offen offset:1024 sc1\n\t"
        "s_waitcnt vmcnt(11) lgkmcnt(14)\n\t"
        "v_mfma_f32_32x32x16_bf16 v[0:15], v[144:147], v[184:187], v[0:15]\n\t"
        "ds_read_b64_tr_b16 v[144:145], v226 offset:20480\n\t"
        "ds_read_b64_tr_b16 v[146:147], v226 offset:21504\n\t"
        "s_waitcnt lgkmcnt(14)\n\t"
        "v_mfma_f32_32x32x16_bf16 v[16:31], v[148:151], v[184:187], v[16:31]\n\t"
        "ds_read_b64_tr_b16 v[148:149], v227 offset:20480\n\t"
        "ds_read_b64_tr_b16 v[150:151], v227 offset:21504\n\t"
        "buffer_load_dwordx4 v[168:171], v225, s[40:43], s45 offen offset:2048 sc1\n\t"
        "s_waitcnt vmcnt(11) lgkmcnt(14)\n\t"
        "v_mfma_f32_32x32x16_bf16 v[0:15], v[152:155], v[188:191], v[0:15]\n\t"
        "ds_read_b64_tr_b16 v[152:153], v226 offset:22528\n\t"
        "ds_read_b64_tr_b16 v[154:155], v226 offset:23552\n\t"
        "s_waitcnt lgkmcnt(14)\n\t"
        "v_mfma_f32_32x32x16_bf16 v[16:31], v[156:159], v[188:191], v[16:31]\n\t"
        "ds_read_b64_tr_b16 v[156:157], v227 offset:22528\n\t"
        "ds_read_b64_tr_b16 v[158:159], v227 offset:23552\n\t"
        "buffer_load_dwordx4 v[172:175], v225, s[40:43], s45 offen offset:3072 sc1\n\t"
        "s_waitcnt vmcnt(11) lgkmcnt(14)\n\t"
        "v_mfma_f32_32x32x16_bf16 v[0:15], v[128:131], v[192:195], v[0:15]\n\t"
        "ds_read_b64_tr_b16 v[128:129], v226 offset:24576\n\t"
        "ds_read_b64_tr_b16 v[130:131], v226 offset:25600\n\t"
        "s_waitcnt lgkmcnt(14)\n\t"
        "v_mfma_f32_32x32x16_bf16 v[16:31], v[132:135], v[192:195], v[16:31]\n\t"
        "ds_read_b64_tr_b16 v[132:133], v227 offset:24576\n\t"
        "ds_read_b64_tr_b16 v[134:135], v227 offset:25600\n\t"
        "s_add_u32 s45, s45, 0x1000\n\t"
        "buffer_load_dwordx4 v[176:179], v225, s[40:43], s45 offen offset:0 sc1\n\t"
        "s_waitcnt vmcnt(11) lgkmcnt(14)\n\t"
        "v_mfma_f32_32x32x16_bf16 v[0:15], v[136:139], v[196:199], v[0:15]\n\t"
        "ds_read_b64_tr_b16 v[136:137], v226 offset:26624\n\t"
        "ds_read_b64_tr_b16 v[138:139], v226 offset:27648\n\t"
        "s_waitcnt lgkmcnt(14)\n\t"
        "v_mfma_f32_32x32x16_bf16 v[16:31], v[140:143], v[196:199], v[16:31]\n\t"
        "ds_read_b64_tr_b16 v[140:141], v227 offset:26624\n\t"
        "ds_read_b64_tr_b16 v[142:143], v227 offset:27648\n\t"
        "buffer_load_dwordx4 v[180:183], v225, s[40:43], s45 offen offset:1024 sc1\n\t"
        "s_waitcnt vmcnt(11) lgkmcnt(14)\n\t"
        "v_mfma_f32_32x32x16_bf16 v[0:15], v[144:147], v[200:203], v[0:15]\n\t"
        "ds_read_b64_tr_b16 v[144:145], v226 offset:28672\n\t"
        "ds_read_b64_tr_b16 v[146:147], v226 offset:29696\n\t"
        "s_waitcnt lgkmcnt(14)\n\t"
        "v_mfma_f32_32x32x16_bf16 v[16:31], v[148:151], v[200:203], v[16:31]\n\t"
        "ds_read_b64_tr_b16 v[148:149], v227 offset:28672\n\t"
        "ds_read_b64_tr_b16 v[150:151], v227 offset:29696\n\t"
        "buffer_load_dwordx4 v[184:187], v225, s[40:43], s45 offen offset:2048 sc1\n\t"
        "s_waitcnt vmcnt(11) lgkmcnt(14)\n\t"
        "v_mfma_f32_32x32x16_bf16 v[0:15], v[152:155], v[204:207], v[0:15]\n\t"
        "ds_read_b64_tr_b16 v[152:153], v226 offset:30720\n\t"
        "ds_read_b64_tr_b16 v[154:155], v226 offset:31744\n\t"
        "s_waitcnt lgkmcnt(14)\n\t"
        "v_mfma_f32_32x32x16_bf16 v[16:31], v[156:159], v[204:207], v[16:31]\n\t"
        "ds_read_b64_tr_b16 v[156:157], v227 offset:30720\n\t"
        "ds_read_b64_tr_b16 v[158:159], v227 offset:31744\n\t"
        "buffer_load_dwordx4 v[188:191], v225, s[40:43], s45 offen offset:3072 sc1\n\t"
        "s_waitcnt vmcnt(11) lgkmcnt(14)\n\t"
        "v_mfma_f32_32x32x16_bf16 v[0:15], v[128:131], v[208:211], v[0:15]\n\t"
        "ds_read_b64_tr_b16 v[128:129], v230 offset:0\n\t"
        "ds_read_b64_tr_b16 v[130:131], v230 offset:1024\n\t"
        "s_waitcnt lgkmcnt(14)\n\t"
        "v_mfma_f32_32x32x16_bf16 v[16:31], v[132:135], v[208:211], v[16:31]\n\t"
        "ds_read_b64_tr_b16 v[132:133], v231 offset:0\n\t"
        "ds_read_b64_tr_b16 v[134:135], v231 offset:1024\n\t"
        "s_add_u32 s45, s45, 0x1000\n\t"
        "buffer_load_dwordx4 v[192:195], v225, s[40:43], s45 offen offset:0 sc1\n\t"
        "s_waitcnt vmcnt(11) lgkmcnt(14)\n\t"
        "v_mfma_f32_32x32x16_bf16 v[0:15], v[136:139], v[212:215], v[0:15]\n\t"
        "ds_read_b64_tr_b16 v[136:137], v230 offset:2048\n\t"
        "ds_read_b64_tr_b16 v[138:139], v230 offset:3072\n\t"
        "s_waitcnt lgkmcnt(14)\n\t"
        "v_mfma_f32_32x32x16_bf16 v[16:31], v[140:143], v[212:215], v[16:31]\n\t"
        "ds_read_b64_tr_b16 v[140:141], v231 offset:2048\n\t"
        "ds_read_b64_tr_b16 v[142:143], v231 offset:3072\n\t"
        "buffer_load_dwordx4 v[196:199], v225, s[40:43], s45 offen offset:1024 sc1\n\t"
        "s_waitcnt vmcnt(11) lgkmcnt(14)\n\t"
        "v_mfma_f32_32x32x16_bf16 v[0:15], v[144:147], v[216:219], v[0:15]\n\t"
        "ds_read_b64_tr_b16 v[144:145], v230 offset:4096\n\t"
        "ds_read_b64_tr_b16 v[146:147], v230 offset:5120\n\t"
        "s_waitcnt lgkmcnt(14)\n\t"
        "v_mfma_f32_32x32x16_bf16 v[16:31], v[148:151], v[216:219], v[16:31]\n\t"
        "ds_read_b64_tr_b16 v[148:149], v231 offset:4096\n\t"
        "ds_read_b64_tr_b16 v[150:151], v231 offset:5120\n\t"
        "buffer_load_dwordx4 v[200:203], v225, s[40:43], s45 offen offset:2048 sc1\n\t"
        "s_waitcnt vmcnt(11) lgkmcnt(14)\n\t"
        "v_mfma_f32_32x32x16_bf16 v[0:15], v[152:155], v[220:223], v[0:15]\n\t"
        "ds_read_b64_tr_b16 v[152:153], v230 offset:6144\n\t"
        "ds_read_b64_tr_b16 v[154:155], v230 offset:7168\n\t"
        "s_waitcnt lgkmcnt(14)\n\t"
        "v_mfma_f32_32x32x16_bf16 v[16:31], v[156:159], v[220:223], v[16:31]\n\t"
        "ds_read_b64_tr_b16 v[156:157], v231 offset:6144\n\t"
        "ds_read_b64_tr_b16 v[158:159], v231 offset:7168\n\t"
        "buffer_load_dwordx4 v[204:207], v225, s[40:43], s45 offen offset:3072 sc1\n\t"
        "s_mov_b32 s44, s45\n\t"
        "s_cmp_eq_u32 s47, 0\n\t"
        "s_cbranch_scc1 oseg0_%=\n\t"
        "s_cmp_eq_u32 s47, 1\n\t"
        "s_cbranch_scc1 oseg1_%=\n\t"
        "s_cmp_eq_u32 s47, 2\n\t"
        "s_cbranch_scc1 oseg2_%=\n\t"
        "s_cmp_eq_u32 s47, 3\n\t"
        "s_cbranch_scc1 oseg3_%=\n\t"
        "s_cmp_lg_u32 s46, 15\n\t"
        "s_cbranch_scc1 ostart_%=\n\t"
        "s_branch oend_%=\n\t"
        "oseg1_%=:\n\t"
        "s_or_b32 s46, s46, 2\n\t"
        "s_waitcnt vmcnt(11) lgkmcnt(14)\n\t"
        "v_mfma_f32_32x32x16_bf16 v[32:47], v[128:131], v[160:163], 0\n\t"
        "ds_read_b64_tr_b16 v[128:129], v226 offset:40960\n\t"
        "ds_read_b64_tr_b16 v[130:131], v226 offset:41984\n\t"
        "s_waitcnt lgkmcnt(14)\n\t"
        "v_mfma_f32_32x32x16_bf16 v[48:63], v[132:135], v[160:163], 0\n\t"
        "ds_read_b64_tr_b16 v[132:133], v227 offset:40960\n\t"
        "ds_read_b64_tr_b16 v[134:135], v227 offset:41984\n\t"
        "s_add_u32 s44, s44, 0x1000\n\t"
        "buffer_load_dwordx4 v[208:211], v224, s[40:43], s44 offen offset:0 sc1\n\t"
        "s_waitcnt vmcnt(11) lgkmcnt(14)\n\t"
        "v_mfma_f32_32x32x16_bf16 v[32:47], v[136:139], v[164:167], v[32:47]\n\t"
        "ds_read_b32 v233, v232\n\t"
        "ds_read_b64_tr_b16 v[136:137], v226 offset:43008\n\t"
        "ds_read_b64_tr_b16 v[138:139], v226 offset:44032\n\t"
        "s_waitcnt lgkmcnt(14)\n\t"
        "v_mfma_f32_32x32x16_bf16 v[48:63], v[140:143], v[164:167], v[48:63]\n\t"
        "ds_read_b64_tr_b16 v[140:141], v227 offset:43008\n\t"
        "ds_read_b64_tr_b16 v[142:143], v227 offset:44032\n\t"
        "buffer_load_dwordx4 v[212:215], v224, s[40:43], s44 offen offset:1024 sc1\n\t"
        "s_waitcnt vmcnt(11) lgkmcnt(14)\n\t"
        "v_mfma_f32_32x32x16_bf16 v[32:47], v[144:147], v[168:171], v[32:47]\n\t"
        "ds_read_b64_tr_b16 v[144:145], v226 offset:45056\n\t"
        "ds_read_b64_tr_b16 v[146:147], v226 offset:46080\n\t"
        "s_waitcnt lgkmcnt(14)\n\t"
        "v_mfma_f32_32x32x16_bf16 v[48:63], v[148:151], v[168:171], v[48:63]\n\t"
        "ds_read_b64_tr_b16 v[148:149], v227 offset:45056\n\t"
        "ds_read_b64_tr_b16 v[150:151], v227 offset:46080\n\t"
        "buffer_load_dwordx4 v[216:219], v224, s[40:43], s44 offen offset:2048 sc1\n\t"
        "s_waitcnt vmcnt(11) lgkmcnt(8)\n\t"
        "v_mfma_f32_32x32x16_bf16 v[32:47], v[152:155], v[172:175], v[32:47]\n\t"
        "v_readfirstlane_b32 s51, v233\n\t"
        "ds_read_b64_tr_b16 v[152:153], v226 offset:47104\n\t"
        "ds_read_b64_tr_b16 v[154:155], v226 offset:48128\n\t"
        "s_waitcnt lgkmcnt(14)\n\t"
        "v_mfma_f32_32x32x16_bf16 v[48:63], v[156:159], v[172:175], v[48:63]\n\t"
        "s_andn2_b32 s51, s51, s46\n\t"
        "s_ff1_i32_b32 s47, s51\n\t"
        "s_add_u32 s45, s47, s49\n\t"
        "s_and_b32 s45, s45, 3\n\t"
        "s_lshl_b32 s45, s45, 14\n\t"
        "ds_read_b64_tr_b16 v[156:157], v227 offset:47104\n\t"
        "ds_read_b64_tr_b16 v[158:159], v227 offset:48128\n\t"
        "buffer_load_dwordx4 v[220:223], v224, s[40:43], s44 offen offset:3072 sc1\n\t"
        "s_waitcnt vmcnt(11) lgkmcnt(14)\n\t"
        "v_mfma_f32_32x32x16_bf16 v[32:47], v[128:131], v[176:179], v[32:47]\n\t"
        "s_and_b32 s51, s47, 0x80000000\n\t"
        "v_or_b32 v225, s51, v224\n\t"
        "s_and_b32 s51, s47, 3\n\t"
        "s_lshl_b32 s51, s51, 15\n\t"
        "v_add_u32 v230, s51, v226\n\t"
        "v_add_u32 v231, s51, v227\n\t"
        "ds_read_b64_tr_b16 v[128:129], v226 offset:49152\n\t"
        "ds_read_b64_tr_b16 v[130:131], v226 offset:50176\n\t"
        "s_waitcnt lgkmcnt(14)\n\t"
        "v_mfma_f32_32x32x16_bf16 v[48:63], v[132:135], v[176:179], v[48:63]\n\t"
        "ds_read_b64_tr_b16 v[132:133], v227 offset:49152\n\t"
        "ds_read_b64_tr_b16 v[134:135], v227 offset:50176\n\t"
        "buffer_load_dwordx4 v[160:163], v225, s[40:43], s45 offen offset:0 sc1\n\t"
        "s_waitcnt vmcnt(11) lgkmcnt(14)\n\t"
        "v_mfma_f32_32x32x16_bf16 v[32:47], v[136:139], v[180:183], v[32:47]\n\t"
        "ds_read_b64_tr_b16 v[136:137], v226 offset:51200\n\t"
        "ds_read_b64_tr_b16 v[138:139], v226 offset:52224\n\t"
        "s_waitcnt lgkmcnt(14)\n\t"
        "v_mfma_f32_32x32x16_bf16 v[48:63], v[140:143], v[180:183], v[48:63]\n\t"
        "ds_read_b64_tr_b16 v[140:141], v227 offset:51200\n\t"
        "ds_read_b64_tr_b16 v[142:143], v227 offset:52224\n\t"
        "buffer_load_dwordx4 v[164:167], v225, s[40:43], s45 offen offset:1024 sc1\n\t"
        "s_waitcnt vmcnt(11) lgkmcnt(14)\n\t"
        "v_mfma_f32_32x32x16_bf16 v[32:47], v[144:147], v[184:187], v[32:47]\n\t"
        "ds_read_b64_tr_b16 v[144:145], v226 offset:53248\n\t"
        "ds_read_b64_tr_b16 v[146:147], v226 offset:54272\n\t"
        "s_waitcnt lgkmcnt(14)\n\t"
        "v_mfma_f32_32x32x16_bf16 v[48:63], v[148:151], v[184:187], v[48:63]\n\t"
        "ds_read_b64_tr_b16 v[148:149], v227 offset:53248\n\t"
        "ds_read_b64_tr_b16 v[150:151], v227 offset:54272\n\t"
        "buffer_load_dwordx4 v[168:171], v225, s[40:43], s45 offen offset:2048 sc1\n\t"
        "s_waitcnt vmcnt(11) lgkmcnt(14)\n\t"
        "v_mfma_f32_32x32x16_bf16 v[32:47], v[152:155], v[188:191], v[32:47]\n\t"
        "ds_read_b64_tr_b16 v[152:153], v226 offset:55296\n\t"
        "ds_read_b64_tr_b16 v[154:155], v226 offset:56320\n\t"
        "s_waitcnt lgkmcnt(14)\n\t"
        "v_mfma_f32_32x32x16_bf16 v[48:63], v[156:159], v[188:191], v[48:63]\n\t"
        "ds_read_b64_tr_b16 v[156:157], v227 offset:55296\n\t"
        "ds_read_b64_tr_b16 v[158:159], v227 offset:56320\n\t"
        "buffer_load_dwordx4 v[172:175], v225, s[40:43], s45 offen offset:3072 sc1\n\t"
        "s_waitcnt vmcnt(11) lgkmcnt(14)\n\t"
        "v_mfma_f32_32x32x16_bf16 v[32:47], v[128:131], v[192:195], v[32:47]\n\t"
        "ds_read_b64_tr_b16 v[128:129], v226 offset:57344\n\t"
        "ds_read_b64_tr_b16 v[130:131], v226 offset:58368\n\t"
        "s_waitcnt lgkmcnt(14)\n\t"
        "v_mfma_f32_32x32x16_bf16 v[48:63], v[132:135], v[192:195], v[48:63]\n\t"
        "ds_read_b64_tr_b16 v[132:133], v227 offset:57344\n\t"
        "ds_read_b64_tr_b16 v[134:135], v227 offset:58368\n\t"
        "s_add_u32 s45, s45, 0x1000\n\t"
        "buffer_load_dwordx4 v[176:179], v225, s[40:43], s45 offen offset:0 sc1\n\t"
        "s_waitcnt vmcnt(11) lgkmcnt(14)\n\t"
        "v_mfma_f32_32x32x16_bf16 v[32:47], v[136:139], v[196:199], v[32:47]\n\t"
        "ds_read_b64_tr_b16 v[136:137], v226 offset:59392\n\t"
        "ds_read_b64_tr_b16 v[138:139], v226 offset:60416\n\t"
        "s_waitcnt lgkmcnt(14)\n\t"
        "v_mfma_f32_32x32x16_bf16 v[48:63], v[140:143], v[196:199], v[48:63]\n\t"
        "ds_read_b64_tr_b16 v[140:141], v227 offset:59392\n\t"
        "ds_read_b64_tr_b16 v[142:143], v227 offset:60416\n\t"
        "buffer_load_dwordx4 v[180:183], v225, s[40:43], s45 offen offset:1024 sc1\n\t"
        "s_waitcnt vmcnt(11) lgkmcnt(14)\n\t"
        "v_mfma_f32_32x32x16_bf16 v[32:47], v[144:147], v[200:203], v[32:47]\n\t"
        "ds_read_b64_tr_b16 v[144:145], v226 offset:61440\n\t"
        "ds_read_b64_tr_b16 v[146:147], v226 offset:62464\n\t"
        "s_waitcnt lgkmcnt(14)\n\t"
        "v_mfma_f32_32x32x16_bf16 v[48:63], v[148:151], v[200:203], v[48:63]\n\t"
        "ds_read_b64_tr_b16 v[148:149], v227 offset:61440\n\t"
        "ds_read_b64_tr_b16 v[150:151], v227 offset:62464\n\t"
        "buffer_load_dwordx4 v[184:187], v225, s[40:43], s45 offen offset:2048 sc1\n\t"
        "s_waitcnt vmcnt(11) lgkmcnt(14)\n\t"
        "v_mfma_f32_32x32x16_bf16 v[32:47], v[152:155], v[204:207], v[32:47]\n\t"
        "ds_read_b64_tr_b16 v[152:153], v226 offset:63488\n\t"
        "ds_read_b64_tr_b16 v[154:155], v226 offset:64512\n\t"
        "s_waitcnt lgkmcnt(14)\n\t"
        "v_mfma_f32_32x32x16_bf16 v[48:63], v[156:159], v[204:207], v[48:63]\n\t"
        "ds_read_b64_tr_b16 v[156:157], v227 offset:63488\n\t"
        "ds_read_b64_tr_b16 v[158:159], v227 offset:64512\n\t"
        "buffer_load_dwordx4 v[188:191], v225, s[40:43], s45 offen offset:3072 sc1\n\t"
        "s_waitcnt vmcnt(11) lgkmcnt(14)\n\t"
        "v_mfma_f32_32x32x16_bf16 v[32:47], v[128:131], v[208:211], v[32:47]\n\t"
        "ds_read_b64_tr_b16 v[128:129], v230 offset:0\n\t"
        "ds_read_b64_tr_b16 v[130:131], v230 offset:1024\n\t"
        "s_waitcnt lgkmcnt(14)\n\t"
        "v_mfma_f32_32x32x16_bf16 v[48:63], v[132:135], v[208:211], v[48:63]\n\t"
        "ds_read_b64_tr_b16 v[132:133], v231 offset:0\n\t"
        "ds_read_b64_tr_b16 v[134:135], v231 offset:1024\n\t"
        "s_add_u32 s45, s45, 0x1000\n\t"
        "buffer_load_dwordx4 v[192:195], v225, s[40:43], s45 offen offset:0 sc1\n\t"
        "s_waitcnt vmcnt(11) lgkmcnt(14)\n\t"
        "v_mfma_f32_32x32x16_bf16 v[32:47], v[136:139], v[212:215], v[32:47]\n\t"
        "ds_read_b64_tr_b16 v[136:137], v230 offset:2048\n\t"
        "ds_read_b64_tr_b16 v[138:139], v230 offset:3072\n\t"
        "s_waitcnt lgkmcnt(14)\n\t"
        "v_mfma_f32_32x32x16_bf16 v[48:63], v[140:143], v[212:215], v[48:63]\n\t"
        "ds_read_b64_tr_b16 v[140:141], v231 offset:2048\n\t"
        "ds_read_b64_tr_b16 v[142:143], v231 offset:3072\n\t"
        "buffer_load_dwordx4 v[196:199], v225, s[40:43], s45 offen offset:1024 sc1\n\t"
        "s_waitcnt vmcnt(11) lgkmcnt(14)\n\t"
        "v_mfma_f32_32x32x16_bf16 v[32:47], v[144:147], v[216:219], v[32:47]\n\t"
        "ds_read_b64_tr_b16 v[144:145], v230 offset:4096\n\t"
        "ds_read_b64_tr_b16 v[146:147], v230 offset:5120\n\t"
        "s_waitcnt lgkmcnt(14)\n\t"
        "v_mfma_f32_32x32x16_bf16 v[48:63], v[148:151], v[216:219], v[48:63]\n\t"
        "ds_read_b64_tr_b16 v[148:149], v231 offset:4096\n\t"
        "ds_read_b64_tr_b16 v[150:151], v231 offset:5120\n\t"
        "buffer_load_dwordx4 v[200:203], v225, s[40:43], s45 offen offset:2048 sc1\n\t"
        "s_waitcnt vmcnt(11) lgkmcnt(14)\n\t"
        "v_mfma_f32_32x32x16_bf16 v[32:47], v[152:155], v[220:223], v[32:47]\n\t"
        "ds_read_b64_tr_b16 v[152:153], v230 offset:6144\n\t"
        "ds_read_b64_tr_b16 v[154:155], v230 offset:7168\n\t"
        "s_waitcnt lgkmcnt(14)\n\t"
        "v_mfma_f32_32x32x16_bf16 v[48:63], v[156:159], v[220:223], v[48:63]\n\t"
        "ds_read_b64_tr_b16 v[156:157], v231 offset:6144\n\t"
        "ds_read_b64_tr_b16 v[158:159], v231 offset:7168\n\t"
        "buffer_load_dwordx4 v[204:207], v225, s[40:43], s45 offen offset:3072 sc1\n\t"
        "s_mov_b32 s44, s45\n\t"
        "s_cmp_eq_u32 s47, 0\n\t"
        "s_cbranch_scc1 oseg0_%=\n\t"
        "s_cmp_eq_u32 s47, 1\n\t"
        "s_cbranch_scc1 oseg1_%=\n\t"
        "s_cmp_eq_u32 s47, 2\n\t"
        "s_cbranch_scc1 oseg2_%=\n\t"
        "s_cmp_eq_u32 s47, 3\n\t"
        "s_cbranch_scc1 oseg3_%=\n\t"
        "s_cmp_lg_u32 s46, 15\n\t"
        "s_cbranch_scc1 ostart_%=\n\t"
        "s_branch oend_%=\n\t"
        "oseg2_%=:\n\t"
        "s_or_b32 s46, s46, 4\n\t"
        "s_waitcnt vmcnt(11) lgkmcnt(14)\n\t"
        "v_mfma_f32_32x32x16_bf16 v[64:79], v[128:131], v[160:163], 0\n\t"
        "ds_read_b64_tr_b16 v[128:129], v228 offset:8192\n\t"
        "ds_read_b64_tr_b16 v[130:131], v228 offset:9216\n\t"
        "s_waitcnt lgkmcnt(14)\n\t"
        "v_mfma_f32_32x32x16_bf16 v[80:95], v[132:135], v[160:163], 0\n\t"
        "ds_read_b64_tr_b16 v[132:133], v229 offset:8192\n\t"
        "ds_read_b64_tr_b16 v[134:135], v229 offset:9216\n\t"
        "s_add_u32 s44, s44, 0x1000\n\t"
        "buffer_load_dwordx4 v[208:211], v224, s[40:43], s44 offen offset:0 sc1\n\t"
        "s_waitcnt vmcnt(11) lgkmcnt(14)\n\t"
        "v_mfma_f32_32x32x16_bf16 v[64:79], v[136:139], v[164:167], v[64:79]\n\t"
        "ds_read_b32 v233, v232\n\t"
        "ds_read_b64_tr_b16 v[136:137], v228 offset:10240\n\t"
        "ds_read_b64_tr_b16 v[138:139], v228 offset:11264\n\t"
        "s_waitcnt lgkmcnt(14)\n\t"
        "v_mfma_f32_32x32x16_bf16 v[80:95], v[140:143], v[164:167], v[80:95]\n\t"
        "ds_read_b64_tr_b16 v[140:141], v229 offset:10240\n\t"
        "ds_read_b64_tr_b16 v[142:143], v229 offset:11264\n\t"
        "buffer_load_dwordx4 v[212:215], v224, s[40:43], s44 offen offset:1024 sc1\n\t"
        "s_waitcnt vmcnt(11) lgkmcnt(14)\n\t"
        "v_mfma_f32_32x32x16_bf16 v[64:79], v[144:147], v[168:171], v[64:79]\n\t"
        "ds_read_b64_tr_b16 v[144:145], v228 offset:12288\n\t"
        "ds_read_b64_tr_b16 v[146:147], v228 offset:13312\n\t"
        "s_waitcnt lgkmcnt(14)\n\t"
        "v_mfma_f32_32x32x16_bf16 v[80:95], v[148:151], v[168:171], v[80:95]\n\t"
        "ds_read_b64_tr_b16 v[148:149], v229 offset:12288\n\t"
        "ds_read_b64_tr_b16 v[150:151], v229 offset:13312\n\t"
        "buffer_load_dwordx4 v[216:219], v224, s[40:43], s44 offen offset:2048 sc1\n\t"
        "s_waitcnt vmcnt(11) lgkmcnt(8)\n\t"
        "v_mfma_f32_32x32x16_bf16 v[64:79], v[152:155], v[172:175], v[64:79]\n\t"
        "v_readfirstlane_b32 s51, v233\n\t"
        "ds_read_b64_tr_b16 v[152:153], v228 offset:14336\n\t"
        "ds_read_b64_tr_b16 v[154:155], v228 offset:15360\n\t"
        "s_waitcnt lgkmcnt(14)\n\t"
        "v_mfma_f32_32x32x16_bf16 v[80:95], v[156:159], v[172:175], v[80:95]\n\t"
        "s_andn2_b32 s51, s51, s46\n\t"
        "s_ff1_i32_b32 s47, s51\n\t"
        "s_add_u32 s45, s47, s49\n\t"
        "s_and_b32 s45, s45, 3\n\t"
        "s_lshl_b32 s45, s45, 14\n\t"
        "ds_read_b64_tr_b16 v[156:157], v229 offset:14336\n\t"
        "ds_read_b64_tr_b16 v[158:159], v229 offset:15360\n\t"
        "buffer_load_dwordx4 v[220:223], v224, s[40:43], s44 offen offset:3072 sc1\n\t"
        "s_waitcnt vmcnt(11) lgkmcnt(14)\n\t"
        "v_mfma_f32_32x32x16_bf16 v[64:79], v[128:131], v[176:179], v[64:79]\n\t"
        "s_and_b32 s51, s47, 0x80000000\n\t"
        "v_or_b32 v225, s51, v224\n\t"
        "s_and_b32 s51, s47, 3\n\t"
        "s_lshl_b32 s51, s51, 15\n\t"
        "v_add_u32 v230, s51, v226\n\t"
        "v_add_u32 v231, s51, v227\n\t"
        "ds_read_b64_tr_b16 v[128:129], v228 offset:16384\n\t"
        "ds_read_b64_tr_b16 v[130:131], v228 offset:17408\n\t"
        "s_waitcnt lgkmcnt(14)\n\t"
        "v_mfma_f32_32x32x16_bf16 v[80:95], v[132:135], v[176:179], v[80:95]\n\t"
        "ds_read_b64_tr_b16 v[132:133], v229 offset:16384\n\t"
        "ds_read_b64_tr_b16 v[134:135], v229 offset:17408\n\t"
        "buffer_load_dwordx4 v[160:163], v225, s[40:43], s45 offen offset:0 sc1\n\t"
        "s_waitcnt vmcnt(11) lgkmcnt(14)\n\t"
        "v_mfma_f32_32x32x16_bf16 v[64:79], v[136:139], v[180:183], v[64:79]\n\t"
        "ds_read_b64_tr_b16 v[136:137], v228 offset:18432\n\t"
        "ds_read_b64_tr_b16 v[138:139], v228 offset:19456\n\t"
        "s_waitcnt lgkmcnt(14)\n\t"
        "v_mfma_f32_32x32x16_bf16 v[80:95], v[140:143], v[180:183], v[80:95]\n\t"
        "ds_read_b64_tr_b16 v[140:141], v229 offset:18432\n\t"
        "ds_read_b64_tr_b16 v[142:143], v229 offset:19456\n\t"
        "buffer_load_dwordx4 v[164:167], v225, s[40:43], s45 offen offset:1024 sc1\n\t"
        "s_waitcnt vmcnt(11) lgkmcnt(14)\n\t"
        "v_mfma_f32_32x32x16_bf16 v[64:79], v[144:147], v[184:187], v[64:79]\n\t"
        "ds_read_b64_tr_b16 v[144:145], v228 offset:20480\n\t"
        "ds_read_b64_tr_b16 v[146:147], v228 offset:21504\n\t"
        "s_waitcnt lgkmcnt(14)\n\t"
        "v_mfma_f32_32x32x16_bf16 v[80:95], v[148:151], v[184:187], v[80:95]\n\t"
        "ds_read_b64_tr_b16 v[148:149], v229 offset:20480\n\t"
        "ds_read_b64_tr_b16 v[150:151], v229 offset:21504\n\t"
        "buffer_load_dwordx4 v[168:171], v225, s[40:43], s45 offen offset:2048 sc1\n\t"
        "s_waitcnt vmcnt(11) lgkmcnt(14)\n\t"
        "v_mfma_f32_32x32x16_bf16 v[64:79], v[152:155], v[188:191], v[64:79]\n\t"
        "ds_read_b64_tr_b16 v[152:153], v228 offset:22528\n\t"
        "ds_read_b64_tr_b16 v[154:155], v228 offset:23552\n\t"
        "s_waitcnt lgkmcnt(14)\n\t"
        "v_mfma_f32_32x32x16_bf16 v[80:95], v[156:159], v[188:191], v[80:95]\n\t"
        "ds_read_b64_tr_b16 v[156:157], v229 offset:22528\n\t"
        "ds_read_b64_tr_b16 v[158:159], v229 offset:23552\n\t"
        "buffer_load_dwordx4 v[172:175], v225, s[40:43], s45 offen offset:3072 sc1\n\t"
        "s_waitcnt vmcnt(11) lgkmcnt(14)\n\t"
        "v_mfma_f32_32x32x16_bf16 v[64:79], v[128:131], v[192:195], v[64:79]\n\t"
        "ds_read_b64_tr_b16 v[128:129], v228 offset:24576\n\t"
        "ds_read_b64_tr_b16 v[130:131], v228 offset:25600\n\t"
        "s_waitcnt lgkmcnt(14)\n\t"
        "v_mfma_f32_32x32x16_bf16 v[80:95], v[132:135], v[192:195], v[80:95]\n\t"
        "ds_read_b64_tr_b16 v[132:133], v229 offset:24576\n\t"
        "ds_read_b64_tr_b16 v[134:135], v229 offset:25600\n\t"
        "s_add_u32 s45, s45, 0x1000\n\t"
        "buffer_load_dwordx4 v[176:179], v225, s[40:43], s45 offen offset:0 sc1\n\t"
        "s_waitcnt vmcnt(11) lgkmcnt(14)\n\t"
        "v_mfma_f32_32x32x16_bf16 v[64:79], v[136:139], v[196:199], v[64:79]\n\t"
        "ds_read_b64_tr_b16 v[136:137], v228 offset:26624\n\t"
        "ds_read_b64_tr_b16 v[138:139], v228 offset:27648\n\t"
        "s_waitcnt lgkmcnt(14)\n\t"
        "v_mfma_f32_32x32x16_bf16 v[80:95], v[140:143], v[196:199], v[80:95]\n\t"
        "ds_read_b64_tr_b16 v[140:141], v229 offset:26624\n\t"
        "ds_read_b64_tr_b16 v[142:143], v229 offset:27648\n\t"
        "buffer_load_dwordx4 v[180:183], v225, s[40:43], s45 offen offset:1024 sc1\n\t"
        "s_waitcnt vmcnt(11) lgkmcnt(14)\n\t"
        "v_mfma_f32_32x32x16_bf16 v[64:79], v[144:147], v[200:203], v[64:79]\n\t"
        "ds_read_b64_tr_b16 v[144:145], v228 offset:28672\n\t"
        "ds_read_b64_tr_b16 v[146:147], v228 offset:29696\n\t"
        "s_waitcnt lgkmcnt(14)\n\t"
        "v_mfma_f32_32x32x16_bf16 v[80:95], v[148:151], v[200:203], v[80:95]\n\t"
        "ds_read_b64_tr_b16 v[148:149], v229 offset:28672\n\t"
        "ds_read_b64_tr_b16 v[150:151], v229 offset:29696\n\t"
        "buffer_load_dwordx4 v[184:187], v225, s[40:43], s45 offen offset:2048 sc1\n\t"
        "s_waitcnt vmcnt(11) lgkmcnt(14)\n\t"
        "v_mfma_f32_32x32x16_bf16 v[64:79], v[152:155], v[204:207], v[64:79]\n\t"
        "ds_read_b64_tr_b16 v[152:153], v228 offset:30720\n\t"
        "ds_read_b64_tr_b16 v[154:155], v228 offset:31744\n\t"
        "s_waitcnt lgkmcnt(14)\n\t"
        "v_mfma_f32_32x32x16_bf16 v[80:95], v[156:159], v[204:207], v[80:95]\n\t"
        "ds_read_b64_tr_b16 v[156:157], v229 offset:30720\n\t"
        "ds_read_b64_tr_b16 v[158:159], v229 offset:31744\n\t"
        "buffer_load_dwordx4 v[188:191], v225, s[40:43], s45 offen offset:3072 sc1\n\t"
        "s_waitcnt vmcnt(11) lgkmcnt(14)\n\t"
        "v_mfma_f32_32x32x16_bf16 v[64:79], v[128:131], v[208:211], v[64:79]\n\t"
        "ds_read_b64_tr_b16 v[128:129], v230 offset:0\n\t"
        "ds_read_b64_tr_b16 v[130:131], v230 offset:1024\n\t"
        "s_waitcnt lgkmcnt(14)\n\t"
        "v_mfma_f32_32x32x16_bf16 v[80:95], v[132:135], v[208:211], v[80:95]\n\t"
        "ds_read_b64_tr_b16 v[132:133], v231 offset:0\n\t"
        "ds_read_b64_tr_b16 v[134:135], v231 offset:1024\n\t"
        "s_add_u32 s45, s45, 0x1000\n\t"
        "buffer_load_dwordx4 v[192:195], v225, s[40:43], s45 offen offset:0 sc1\n\t"
        "s_waitcnt vmcnt(11) lgkmcnt(14)\n\t"
        "v_mfma_f32_32x32x16_bf16 v[64:79], v[136:139], v[212:215], v[64:79]\n\t"
        "ds_read_b64_tr_b16 v[136:137], v230 offset:2048\n\t"
        "ds_read_b64_tr_b16 v[138:139], v230 offset:3072\n\t"
        "s_waitcnt lgkmcnt(14)\n\t"
        "v_mfma_f32_32x32x16_bf16 v[80:95], v[140:143], v[212:215], v[80:95]\n\t"
        "ds_read_b64_tr_b16 v[140:141], v231 offset:2048\n\t"
        "ds_read_b64_tr_b16 v[142:143], v231 offset:3072\n\t"
        "buffer_load_dwordx4 v[196:199], v225, s[40:43], s45 offen offset:1024 sc1\n\t"
        "s_waitcnt vmcnt(11) lgkmcnt(14)\n\t"
        "v_mfma_f32_32x32x16_bf16 v[64:79], v[144:147], v[216:219], v[64:79]\n\t"
        "ds_read_b64_tr_b16 v[144:145], v230 offset:4096\n\t"
        "ds_read_b64_tr_b16 v[146:147], v230 offset:5120\n\t"
        "s_waitcnt lgkmcnt(14)\n\t"
        "v_mfma_f32_32x32x16_bf16 v[80:95], v[148:151], v[216:219], v[80:95]\n\t"
        "ds_read_b64_tr_b16 v[148:149], v231 offset:4096\n\t"
        "ds_read_b64_tr_b16 v[150:151], v231 offset:5120\n\t"
        "buffer_load_dwordx4 v[200:203], v225, s[40:43], s45 offen offset:2048 sc1\n\t"
        "s_waitcnt vmcnt(11) lgkmcnt(14)\n\t"
        "v_mfma_f32_32x32x16_bf16 v[64:79], v[152:155], v[220:223], v[64:79]\n\t"
        "ds_read_b64_tr_b16 v[152:153], v230 offset:6144\n\t"
        "ds_read_b64_tr_b16 v[154:155], v230 offset:7168\n\t"
        "s_waitcnt lgkmcnt(14)\n\t"
        "v_mfma_f32_32x32x16_bf16 v[80:95], v[156:159], v[220:223], v[80:95]\n\t"
        "ds_read_b64_tr_b16 v[156:157], v231 offset:6144\n\t"
        "ds_read_b64_tr_b16 v[158:159], v231 offset:7168\n\t"
        "buffer_load_dwordx4 v[204:207], v225, s[40:43], s45 offen offset:3072 sc1\n\t"
        "s_mov_b32 s44, s45\n\t"
        "s_cmp_eq_u32 s47, 0\n\t"
        "s_cbranch_scc1 oseg0_%=\n\t"
        "s_cmp_eq_u32 s47, 1\n\t"
        "s_cbranch_scc1 oseg1_%=\n\t"
        "s_cmp_eq_u32 s47, 2\n\t"
        "s_cbranch_scc1 oseg2_%=\n\t"
        "s_cmp_eq_u32 s47, 3\n\t"
        "s_cbranch_scc1 oseg3_%=\n\t"
        "s_cmp_lg_u32 s46, 15\n\t"
        "s_cbranch_scc1 ostart_%=\n\t"
        "s_branch oend_%=\n\t"
        "oseg3_%=:\n\t"
        "s_or_b32 s46, s46, 8\n\t"
        "s_waitcnt vmcnt(11) lgkmcnt(14)\n\t"
        "v_mfma_f32_32x32x16_bf16 v[96:111], v[128:131], v[160:163], 0\n\t"
        "ds_read_b64_tr_b16 v[128:129], v228 offset:40960\n\t"
        "ds_read_b64_tr_b16 v[130:131], v228 offset:41984\n\t"
        "s_waitcnt lgkmcnt(14)\n\t"
        "v_mfma_f32_32x32x16_bf16 v[112:127], v[132:135], v[160:163], 0\n\t"
        "ds_read_b64_tr_b16 v[132:133], v229 offset:40960\n\t"
        "ds_read_b64_tr_b16 v[134:135], v229 offset:41984\n\t"
        "s_add_u32 s44, s44, 0x1000\n\t"
        "buffer_load_dwordx4 v[208:211], v224, s[40:43], s44 offen offset:0 sc1\n\t"
        "s_waitcnt vmcnt(11) lgkmcnt(14)\n\t"
        "v_mfma_f32_32x32x16_bf16 v[96:111], v[136:139], v[164:167], v[96:111]\n\t"
        "ds_read_b32 v233, v232\n\t"
        "ds_read_b64_tr_b16 v[136:137], v228 offset:43008\n\t"
        "ds_read_b64_tr_b16 v[138:139], v228 offset:44032\n\t"
        "s_waitcnt lgkmcnt(14)\n\t"
        "v_mfma_f32_32x32x16_bf16 v[112:127], v[140:143], v[164:167], v[112:127]\n\t"
        "ds_read_b64_tr_b16 v[140:141], v229 offset:43008\n\t"
        "ds_read_b64_tr_b16 v[142:143], v229 offset:44032\n\t"
        "buffer_load_dwordx4 v[212:215], v224, s[40:43], s44 offen offset:1024 sc1\n\t"
        "s_waitcnt vmcnt(11) lgkmcnt(14)\n\t"
        "v_mfma_f32_32x32x16_bf16 v[96:111], v[144:147], v[168:171], v[96:111]\n\t"
        "ds_read_b64_tr_b16 v[144:145], v228 offset:45056\n\t"
        "ds_read_b64_tr_b16 v[146:147], v228 offset:46080\n\t"
        "s_waitcnt lgkmcnt(14)\n\t"
        "v_mfma_f32_32x32x16_bf16 v[112:127], v[148:151], v[168:171], v[112:127]\n\t"
        "ds_read_b64_tr_b16 v[148:149], v229 offset:45056\n\t"
        "ds_read_b64_tr_b16 v[150:151], v229 offset:46080\n\t"
        "buffer_load_dwordx4 v[216:219], v224, s[40:43], s44 offen offset:2048 sc1\n\t"
        "s_waitcnt vmcnt(11) lgkmcnt(8)\n\t"
        "v_mfma_f32_32x32x16_bf16 v[96:111], v[152:155], v[172:175], v[96:111]\n\t"
        "v_readfirstlane_b32 s51, v233\n\t"
        "ds_read_b64_tr_b16 v[152:153], v228 offset:47104\n\t"
        "ds_read_b64_tr_b16 v[154:155], v228 offset:48128\n\t"
        "s_waitcnt lgkmcnt(14)\n\t"
        "v_mfma_f32_32x32x16_bf16 v[112:127], v[156:159], v[172:175], v[112:127]\n\t"
        "s_andn2_b32 s51, s51, s46\n\t"
        "s_ff1_i32_b32 s47, s51\n\t"
        "s_add_u32 s45, s47, s49\n\t"
        "s_and_b32 s45, s45, 3\n\t"
        "s_lshl_b32 s45, s45, 14\n\t"
        "ds_read_b64_tr_b16 v[156:157], v229 offset:47104\n\t"
        "ds_read_b64_tr_b16 v[158:159], v229 offset:48128\n\t"
        "buffer_load_dwordx4 v[220:223], v224, s[40:43], s44 offen offset:3072 sc1\n\t"
        "s_waitcnt vmcnt(11) lgkmcnt(14)\n\t"
        "v_mfma_f32_32x32x16_bf16 v[96:111], v[128:131], v[176:179], v[96:111]\n\t"
        "s_and_b32 s51, s47, 0x80000000\n\t"
        "v_or_b32 v225, s51, v224\n\t"
        "s_and_b32 s51, s47, 3\n\t"
        "s_lshl_b32 s51, s51, 15\n\t"
        "v_add_u32 v230, s51, v226\n\t"
        "v_add_u32 v231, s51, v227\n\t"
        "ds_read_b64_tr_b16 v[128:129], v228 offset:49152\n\t"
        "ds_read_b64_tr_b16 v[130:131], v228 offset:50176\n\t"
        "s_waitcnt lgkmcnt(14)\n\t"
        "v_mfma_f32_32x32x16_bf16 v[112:127], v[132:135], v[176:179], v[112:127]\n\t"
        "ds_read_b64_tr_b16 v[132:133], v229 offset:49152\n\t"
        "ds_read_b64_tr_b16 v[134:135], v229 offset:50176\n\t"
        "buffer_load_dwordx4 v[160:163], v225, s[40:43], s45 offen offset:0 sc1\n\t"
        "s_waitcnt vmcnt(11) lgkmcnt(14)\n\t"
        "v_mfma_f32_32x32x16_bf16 v[96:111], v[136:139], v[180:183], v[96:111]\n\t"
        "ds_read_b64_tr_b16 v[136:137], v228 offset:51200\n\t"
        "ds_read_b64_tr_b16 v[138:139], v228 offset:52224\n\t"
        "s_waitcnt lgkmcnt(14)\n\t"
        "v_mfma_f32_32x32x16_bf16 v[112:127], v[140:143], v[180:183], v[112:127]\n\t"
        "ds_read_b64_tr_b16 v[140:141], v229 offset:51200\n\t"
        "ds_read_b64_tr_b16 v[142:143], v229 offset:52224\n\t"
        "buffer_load_dwordx4 v[164:167], v225, s[40:43], s45 offen offset:1024 sc1\n\t"
        "s_waitcnt vmcnt(11) lgkmcnt(14)\n\t"
        "v_mfma_f32_32x32x16_bf16 v[96:111], v[144:147], v[184:187], v[96:111]\n\t"
        "ds_read_b64_tr_b16 v[144:145], v228 offset:53248\n\t"
        "ds_read_b64_tr_b16 v[146:147], v228 offset:54272\n\t"
        "s_waitcnt lgkmcnt(14)\n\t"
        "v_mfma_f32_32x32x16_bf16 v[112:127], v[148:151], v[184:187], v[112:127]\n\t"
        "ds_read_b64_tr_b16 v[148:149], v229 offset:53248\n\t"
        "ds_read_b64_tr_b16 v[150:151], v229 offset:54272\n\t"
        "buffer_load_dwordx4 v[168:171], v225, s[40:43], s45 offen offset:2048 sc1\n\t"
        "s_waitcnt vmcnt(11) lgkmcnt(14)\n\t"
        "v_mfma_f32_32x32x16_bf16 v[96:111], v[152:155], v[188:191], v[96:111]\n\t"
        "ds_read_b64_tr_b16 v[152:153], v228 offset:55296\n\t"
        "ds_read_b64_tr_b16 v[154:155], v228 offset:56320\n\t"
        "s_waitcnt lgkmcnt(14)\n\t"
        "v_mfma_f32_32x32x16_bf16 v[112:127], v[156:159], v[188:191], v[112:127]\n\t"
        "ds_read_b64_tr_b16 v[156:157], v229 offset:55296\n\t"
        "ds_read_b64_tr_b16 v[158:159], v229 offset:56320\n\t"
        "buffer_load_dwordx4 v[172:175], v225, s[40:43], s45 offen offset:3072 sc1\n\t"
        "s_waitcnt vmcnt(11) lgkmcnt(14)\n\t"
        "v_mfma_f32_32x32x16_bf16 v[96:111], v[128:131], v[192:195], v[96:111]\n\t"
        "ds_read_b64_tr_b16 v[128:129], v228 offset:57344\n\t"
        "ds_read_b64_tr_b16 v[130:131], v228 offset:58368\n\t"
        "s_waitcnt lgkmcnt(14)\n\t"
        "v_mfma_f32_32x32x16_bf16 v[112:127], v[132:135], v[192:195], v[112:127]\n\t"
        "ds_read_b64_tr_b16 v[132:133], v229 offset:57344\n\t"
        "ds_read_b64_tr_b16 v[134:135], v229 offset:58368\n\t"
        "s_add_u32 s45, s45, 0x1000\n\t"
        "buffer_load_dwordx4 v[176:179], v225, s[40:43], s45 offen offset:0 sc1\n\t"
        "s_waitcnt vmcnt(11) lgkmcnt(14)\n\t"
        "v_mfma_f32_32x32x16_bf16 v[96:111], v[136:139], v[196:199], v[96:111]\n\t"
        "ds_read_b64_tr_b16 v[136:137], v228 offset:59392\n\t"
        "ds_read_b64_tr_b16 v[138:139], v228 offset:60416\n\t"
        "s_waitcnt lgkmcnt(14)\n\t"
        "v_mfma_f32_32x32x16_bf16 v[112:127], v[140:143], v[196:199], v[112:127]\n\t"
        "ds_read_b64_tr_b16 v[140:141], v229 offset:59392\n\t"
        "ds_read_b64_tr_b16 v[142:143], v229 offset:60416\n\t"
        "buffer_load_dwordx4 v[180:183], v225, s[40:43], s45 offen offset:1024 sc1\n\t"
        "s_waitcnt vmcnt(11) lgkmcnt(14)\n\t"
        "v_mfma_f32_32x32x16_bf16 v[96:111], v[144:147], v[200:203], v[96:111]\n\t"
        "ds_read_b64_tr_b16 v[144:145], v228 offset:61440\n\t"
        "ds_read_b64_tr_b16 v[146:147], v228 offset:62464\n\t"
        "s_waitcnt lgkmcnt(14)\n\t"
        "v_mfma_f32_32x32x16_bf16 v[112:127], v[148:151], v[200:203], v[112:127]\n\t"
        "ds_read_b64_tr_b16 v[148:149], v229 offset:61440\n\t"
        "ds_read_b64_tr_b16 v[150:151], v229 offset:62464\n\t"
        "buffer_load_dwordx4 v[184:187], v225, s[40:43], s45 offen offset:2048 sc1\n\t"
        "s_waitcnt vmcnt(11) lgkmcnt(14)\n\t"
        "v_mfma_f32_32x32x16_bf16 v[96:111], v[152:155], v[204:207], v[96:111]\n\t"
        "ds_read_b64_tr_b16 v[152:153], v228 offset:63488\n\t"
        "ds_read_b64_tr_b16 v[154:155], v228 offset:64512\n\t"
        "s_waitcnt lgkmcnt(14)\n\t"
        "v_mfma_f32_32x32x16_bf16 v[112:127], v[156:159], v[204:207], v[112:127]\n\t"
        "ds_read_b64_tr_b16 v[156:157], v229 offset:63488\n\t"
        "ds_read_b64_tr_b16 v[158:159], v229 offset:64512\n\t"
        "buffer_load_dwordx4 v[188:191], v225, s[40:43], s45 offen offset:3072 sc1\n\t"
        "s_waitcnt vmcnt(11) lgkmcnt(14)\n\t"
        "v_mfma_f32_32x32x16_bf16 v[96:111], v[128:131], v[208:211], v[96:111]\n\t"
        "ds_read_b64_tr_b16 v[128:129], v230 offset:0\n\t"
        "ds_read_b64_tr_b16 v[130:131], v230 offset:1024\n\t"
        "s_waitcnt lgkmcnt(14)\n\t"
        "v_mfma_f32_32x32x16_bf16 v[112:127], v[132:135], v[208:211], v[112:127]\n\t"
        "ds_read_b64_tr_b16 v[132:133], v231 offset:0\n\t"
        "ds_read_b64_tr_b16 v[134:135], v231 offset:1024\n\t"
        "s_add_u32 s45, s45, 0x1000\n\t"
        "buffer_load_dwordx4 v[192:195], v225, s[40:43], s45 offen offset:0 sc1\n\t"
        "s_waitcnt vmcnt(11) lgkmcnt(14)\n\t"
        "v_mfma_f32_32x32x16_bf16 v[96:111], v[136:139], v[212:215], v[96:111]\n\t"
        "ds_read_b64_tr_b16 v[136:137], v230 offset:2048\n\t"
        "ds_read_b64_tr_b16 v[138:139], v230 offset:3072\n\t"
        "s_waitcnt lgkmcnt(14)\n\t"
        "v_mfma_f32_32x32x16_bf16 v[112:127], v[140:143], v[212:215], v[112:127]\n\t"
        "ds_read_b64_tr_b16 v[140:141], v231 offset:2048\n\t"
        "ds_read_b64_tr_b16 v[142:143], v231 offset:3072\n\t"
        "buffer_load_dwordx4 v[196:199], v225, s[40:43], s45 offen offset:1024 sc1\n\t"
        "s_waitcnt vmcnt(11) lgkmcnt(14)\n\t"
        "v_mfma_f32_32x32x16_bf16 v[96:111], v[144:147], v[216:219], v[96:111]\n\t"
        "ds_read_b64_tr_b16 v[144:145], v230 offset:4096\n\t"
        "ds_read_b64_tr_b16 v[146:147], v230 offset:5120\n\t"
        "s_waitcnt lgkmcnt(14)\n\t"
        "v_mfma_f32_32x32x16_bf16 v[112:127], v[148:151], v[216:219], v[112:127]\n\t"
        "ds_read_b64_tr_b16 v[148:149], v231 offset:4096\n\t"
        "ds_read_b64_tr_b16 v[150:151], v231 offset:5120\n\t"
        "buffer_load_dwordx4 v[200:203], v225, s[40:43], s45 offen offset:2048 sc1\n\t"
        "s_waitcnt vmcnt(11) lgkmcnt(14)\n\t"
        "v_mfma_f32_32x32x16_bf16 v[96:111], v[152:155], v[220:223], v[96:111]\n\t"
        "ds_read_b64_tr_b16 v[152:153], v230 offset:6144\n\t"
        "ds_read_b64_tr_b16 v[154:155], v230 offset:7168\n\t"
        "s_waitcnt lgkmcnt(14)\n\t"
        "v_mfma_f32_32x32x16_bf16 v[112:127], v[156:159], v[220:223], v[112:127]\n\t"
        "ds_read_b64_tr_b16 v[156:157], v231 offset:6144\n\t"
        "ds_read_b64_tr_b16 v[158:159], v231 offset:7168\n\t"
        "buffer_load_dwordx4 v[204:207], v225, s[40:43], s45 offen offset:3072 sc1\n\t"
        "s_mov_b32 s44, s45\n\t"
        "s_cmp_eq_u32 s47, 0\n\t"
        "s_cbranch_scc1 oseg0_%=\n\t"
        "s_cmp_eq_u32 s47, 1\n\t"
        "s_cbranch_scc1 oseg1_%=\n\t"
        "s_cmp_eq_u32 s47, 2\n\t"
        "s_cbranch_scc1 oseg2_%=\n\t"
        "s_cmp_eq_u32 s47, 3\n\t"
        "s_cbranch_scc1 oseg3_%=\n\t"
        "s_cmp_lg_u32 s46, 15\n\t"
        "s_cbranch_scc1 ostart_%=\n\t"
        "s_branch oend_%=\n\t"
        "oend_%=:\n\t"
        "ds_read_b64 v[128:129], v234 offset:0\n\t"
        "ds_read_b64 v[130:131], v234 offset:16\n\t"
        "ds_read_b64 v[132:133], v234 offset:32\n\t"
        "ds_read_b64 v[134:135], v234 offset:48\n\t"
        "ds_read_b64 v[136:137], v234 offset:64\n\t"
        "ds_read_b64 v[138:139], v234 offset:80\n\t"
        "ds_read_b64 v[140:141], v234 offset:96\n\t"
        "ds_read_b64 v[142:143], v234 offset:112\n\t"
        "s_waitcnt lgkmcnt(0)\n\t"
        "s_barrier\n\t"
        : "+{v[0:15]}"(acc[0]), "+{v[16:31]}"(acc[1]), "={v[32:47]}"(acc[2]), "={v[48:63]}"(acc[3]), "={v[64:79]}"(acc[4]), "={v[80:95]}"(acc[5]),
          "={v[96:111]}"(acc[6]), "={v[112:127]}"(acc[7]), "={v[128:143]}"(wrc),
          "={v[160:175]}"(fl0), "={v[176:191]}"(fl1), "={v[192:207]}"(fl2) STAMP_OUTS
        : "{v224}"(OFF_OF + L * (5 * MBY) + tile * TS + lane * 16), "{v226}"(a0), "{v227}"(a1), "{v228}"(a0 + 65536), "{v229}"(a1 + 65536),
          "{v232}"(lbase + L_W + 20 + 8 * L), "{v234}"(lbase + L_WR + hi * 8), "{s49}"((tn >> 2) + 1), "{s[40:43]}"(srd)
        : "v144", "v145", "v146", "v147", "v148", "v149", "v150", "v151", "v152", "v153", "v154", "v155", "v156", "v157", "v158", "v159", "v208", "v209", "v210", "v211", "v212", "v213", "v214", "v215", "v216", "v217", "v218", "v219", "v220", "v221", "v222", "v223", "v225", "v230", "v231", "v233", "s44", "s45", "s46", "s47", "s48", "s51", "vcc", "scc", "memory");
      }
      PHASE_VARS;
      STAMP_PUT(12 + 16 * L);
      STAMPV(15 + 16 * L);
      float s = 0.f, sq = 0.f, d = 0.f;
      const unsigned xdst = OFF_X1 + tile * TS + tn * 4096 + lane * 16;
#pragma unroll
      for (int i = 0; i < 4; ++i) {
#pragma unroll
        for (int e = 0; e < 4; ++e) {
          const int j = i >> 1, k = 8 * (i & 1) + 2 * e;
          x1p[i][e] = pack2((acc[j][k] + acc[2 + j][k]) + (acc[4 + j][k] + acc[6 + j][k]), (acc[j][k + 1] + acc[2 + j][k + 1]) + (acc[4 + j][k + 1] + acc[6 + j][k + 1]));
        }
        if (L == 0) stg(rs, xdst + i * 1024, x1p[i]);
        SBAR();
      }
#pragma unroll
      for (int g = 0; g < 8; ++g)
#pragma unroll
        for (int e = 0; e < 2; ++e) {
          const unsigned o = x1p[g >> 1][2 * (g & 1) + e];
          s = dot2(o, 0x3f803f80u, s); sq = dot2(o, o, sq); d = dot2(o, wrc[2 * g + e], d);
        }
      const u32x4 pst = {nzf(half_swap_sum(s)), nzf(half_swap_sum(sq)), nzf(half_swap_sum(d)), 0u};
      asm volatile("buffer_store_dwordx4 %0, %1, %2, 0 offen sc1\n\ts_waitcnt vmcnt(1)\n\tds_write_b32 %3, %4"
                   :: "v"(pst), "v"(stats_dst(sx, tile, tn, lane)), "s"(srd), "v"(lbase + L_W + 64 + 4 * wid), "v"(3),
                      "{v[160:175]}"(fl0), "{v[176:191]}"(fl1), "{v[192:207]}"(fl2) : "memory");
    }
    STAMPV(16 + 16 * L);
  }
  {
    PHASE_VARS;
    float S, Q, Dd, mean, var, mb1, rd1;
    __builtin_amdgcn_s_sleep(13);
    row_stats_sum(rs, OFF_SX + SX_LAYER, tile, lane, S, Q, Dd);
    STAMPV(33);
    row_stats_fin(rs, OFF_SX + SX_LAYER + SX_CAND + (tn * 16 + tm) * 128 + wid * 4, smem, 2, tile, lane, S, Q, Dd, U2F(words[0]), x1p, mean, var, mb1, rd1);
    STAMPV(34);
    u32x4 xo[4];
#pragma unroll
    for (int i = 0; i < 4; ++i) {
#pragma unroll
      for (int e = 0; e < 4; ++e) {
        const unsigned h2 = ln_pair(x1p[i][e], mb1, rd1);
        xo[i][e] = pack2(lo16(x1p[i][e]) + lo16(h2), hi16(x1p[i][e]) + hi16(h2));
      }
      *reinterpret_cast<u32x4*>(smem + wid * 4096 + r32 * 128 + ((2 * i + hi) ^ (r32 & 7)) * 16) = fo_swap(xo[i]);
    }
    asm volatile("s_waitcnt lgkmcnt(0)" ::: "memory");
#pragma unroll
    for (int i = 0; i < 4; ++i) { xo[i] = *reinterpret_cast<const u32x4*>(smem + wid * 4096 + i * 1024 + (lane >> 3) * 128 + ((lane ^ (lane >> 3)) & 7) * 16); PIN(xo[i]); }
    u16* dst = out + (size_t)(tile * 32 + (lane >> 3)) * D_ + tn * 64 + (lane & 7) * 8;
    lds_wait_ge(lbase + L_W + 4 + 4 * (lane & 1), 2);
    const int rel = (int)words[3] - tile * 32 - (lane >> 3);
#pragma unroll
    for (int i = 0; i < 4; ++i)
      if (rel != 8 * i) st16(dst + (size_t)i * 8 * D_, xo[i]);
    STAMPV(35);
    STAMP_END;
  }
}

extern "C" __global__ void end_marker() {}

extern "C" void kernel_launch(void* const* d_in, const int* in_sizes, int n_in, void* d_out, int out_size,
                              void* d_ws, size_t ws_size, hipStream_t stream) {
  char* ws = (char*)d_ws;
  mega_kernel<<<256, 512, 0, stream>>>((const u16*)d_in[0], (const u16*)d_in[1], (const u16*)d_in[2], (const u16*)d_in[4], (const u16*)d_in[5],
                                       (const u16*)d_in[6], (const u16*)d_in[7], ws, (u16*)d_out);
}
```

```cpp
#include <hip/hip_runtime.h>
#include <hip/hip_bf16.h>
#include <stdint.h>

typedef unsigned short u16;
typedef short s16x4 __attribute__((ext_vector_type(4)));
typedef short s16x8 __attribute__((ext_vector_type(8)));
typedef float f32x16 __attribute__((ext_vector_type(16)));
typedef float f32x4 __attribute__((ext_vector_type(4)));
typedef float f32x2 __attribute__((ext_vector_type(2)));
typedef unsigned int u32x16 __attribute__((ext_vector_type(16)));
typedef unsigned int u32x8 __attribute__((ext_vector_type(8)));
typedef unsigned int u32x4 __attribute__((ext_vector_type(4)));
typedef unsigned int u32x2 __attribute__((ext_vector_type(2)));
typedef __bf16 bf16x2 __attribute__((ext_vector_type(2)));
typedef __bf16 bf16x8 __attribute__((ext_vector_type(8)));
typedef __bf16 bf16x16 __attribute__((ext_vector_type(16)));

#define DI __device__ __forceinline__
#define LDS_PTR(T, p) ((__attribute__((address_space(3))) T*)(p))
#define BARRIER() asm volatile("s_barrier" ::: "memory")
#define BAR_LGKM() asm volatile("s_waitcnt lgkmcnt(0)\n\ts_barrier" ::: "memory")
#define BAR_ALL() asm volatile("s_waitcnt vmcnt(0) lgkmcnt(0)\n\ts_barrier" ::: "memory")
#define DRAIN() asm volatile("s_waitcnt vmcnt(0)" ::: "memory")
#define MFMA32(a, b, c) __builtin_amdgcn_mfma_f32_32x32x16_bf16(a, b, c, 0, 0, 0)
#define MFMA16(a, b, c) __builtin_amdgcn_mfma_f32_16x16x32_bf16(a, b, c, 0, 0, 0)
#define PIN(x) asm volatile("" : "+v"(x))
#define EX2(x) __builtin_amdgcn_exp2f(x)
#define RCP(x) __builtin_amdgcn_rcpf(x)
#define SBAR() __builtin_amdgcn_sched_barrier(0)

#define STAMP_INIT
#define STAMPV(i)
#define STAMP_END
#define STAMP_DECL
#define STAMP_OUTS
#define STAMP_PUT(i)
#define JIT()
constexpr int D_ = 1024, F_ = 3584;
constexpr float QS = 0.17677669529663687f * 1.4426950408889634f;

constexpr unsigned MBY = 1u << 20;
constexpr unsigned TS = 65536 + 4096, HS = 262144 + 4096;
constexpr unsigned OFF_HF = 0, OFF_X1 = 5 * MBY;
constexpr unsigned OFF_OF = 10 * MBY;
constexpr unsigned OFF_KV = 20 * MBY;
constexpr unsigned OFF_FL = 40 * MBY;
constexpr unsigned FL_EPOCH = OFF_FL;
constexpr unsigned FL_KV = OFF_FL + 4096;
constexpr unsigned FL_O = FL_KV + 65536;
constexpr unsigned FL_X = FL_O + 65536;
constexpr unsigned OFF_CS = 41 * MBY;
constexpr unsigned OFF_SX = 42 * MBY;
constexpr unsigned SX_ST = 0;
constexpr unsigned SX_ROW = 512 * 1024;
constexpr unsigned SX_CAND = SX_ROW + 256 * 1024;
constexpr unsigned SX_GP = SX_CAND + 32 * 1024;
constexpr unsigned SX_YP = SX_GP + 224 * 1024;
constexpr unsigned SX_LAYER = SX_YP + 224 * 1024;
constexpr unsigned SX_QP = 2 * SX_LAYER;
constexpr unsigned SX_BYTES = SX_QP + 512 * 1024;
static_assert(SX_BYTES <= 6 * 256 * 128 * 16, "sentinel fill");
constexpr unsigned WS_BYTES = 48 * MBY, OOB = 0x80000000u;
constexpr unsigned SENT = 0xFFFFFFFFu;

constexpr int NS = 9, DD = 3, SLOTB = 12352, HALFB = 6176;
constexpr int L_XCH = 122880;
constexpr int L_KX = L_XCH - 8192, L_QX = L_XCH + 8192;
constexpr int L_SL = L_XCH + 8192;
constexpr int L_CAND = L_XCH + 32768;
constexpr int L_AB = L_XCH + 34816;
constexpr int L_FULL = 159744, L_FREE = L_FULL + 512;
constexpr int L_CS = L_FREE + 1024, L_PATCH = L_CS + 768;
constexpr int L_GU = L_PATCH + 768, L_TAG = L_GU + 256, L_WR = L_TAG + 256;
constexpr int L_X2F = L_WR + 128, L_X2P = L_X2F + 128;
constexpr int L_W = L_X2P + 128;
constexpr int LDS_BYTES = 163840;
static_assert(L_W + 128 <= LDS_BYTES && NS * SLOTB + 256 <= L_KX && L_AB + 2048 <= L_FULL, "LDS map");

DI int fresh_lane() { int l; asm volatile("v_mbcnt_lo_u32_b32 %0, -1, 0\n\tv_mbcnt_hi_u32_b32 %0, -1, %0" : "=v"(l)); return l; }
#define LANE_VARS const int lane = fresh_lane(), r32 = lane & 31, hi = lane >> 5, tq = (lane & 15) >> 2, tp = lane & 3, tblk = (lane >> 4) & 1, \
  wrow = 4 * hi + tq, wsw = 4 * ((wrow >> 1) & 1), wch = 2 * tblk + (tp >> 1), c24 = r32 < 24 ? r32 : 23; \
  (void)r32; (void)hi; (void)wsw; (void)wch; (void)c24
DI int fresh_s(int v) { asm volatile("" : "+s"(v)); return v; }
#define PHASE_VARS const int tm = fresh_s(tm_), tn = fresh_s(tn_), wid = fresh_s(wid_), tile = tm * 4 + wid, hh = wid & 1, kh = wid >> 1; (void)tile; (void)hh; (void)kh; LANE_VARS
DI int kpos(int tm, int tn, int i) { return i == 0 ? tn : (tn + 1 + (1 * (3 - (tn & 3)) + i - 1) % 15) & 15; }
DI unsigned pack2(float lo, float hi) { f32x2 v = {lo, hi}; return __builtin_bit_cast(unsigned, __builtin_convertvector(v, bf16x2)); }
DI float rbf(float f) { return __uint_as_float(pack2(f, 0.f) << 16); }
DI float lo16(unsigned u) { return __uint_as_float(u << 16); }
DI float hi16(unsigned u) { return __uint_as_float(u & 0xffff0000u); }
DI float opq(float r) { asm("" : "+v"(r)); return r; }
DI float fadd(float a, float b) { return opq(a + b); }
DI float fmul(float a, float b) { return opq(a * b); }
DI float ffma(float a, float b, float c) { return opq(__builtin_fmaf(a, b, c)); }
DI float U2F(unsigned u) { return __uint_as_float(u); }
DI float half_swap_sum(float v) {
  auto r = __builtin_amdgcn_permlane32_swap(__float_as_uint(v), __float_as_uint(v), false, false);
  return fadd(U2F(r[0]), U2F(r[1]));
}
DI float half_swap_max(float v) {
  auto r = __builtin_amdgcn_permlane32_swap(__float_as_uint(v), __float_as_uint(v), false, false);
  return fmaxf(__uint_as_float(r[0]), __uint_as_float(r[1]));
}
template <int CTRL> DI unsigned dpp(unsigned v) { return (unsigned)__builtin_amdgcn_update_dpp(0, (int)v, CTRL, 0xf, 0xf, false); }
DI float row_swap_sum(float v) {
  auto r = __builtin_amdgcn_permlane16_swap(__float_as_uint(v), __float_as_uint(v), false, false);
  return fadd(U2F(r[0]), U2F(r[1]));
}
DI float half_sum(float v) {
  v = fadd(v, U2F(dpp<0xB1>(__float_as_uint(v)))); v = fadd(v, U2F(dpp<0x4E>(__float_as_uint(v))));
  v = fadd(v, U2F(dpp<0x141>(__float_as_uint(v)))); v = fadd(v, U2F(dpp<0x140>(__float_as_uint(v))));
  return row_swap_sum(v);
}
DI float wave_sum(float v) { return half_swap_sum(half_sum(v)); }
DI unsigned half_max_u32(unsigned v) {
  v = max(v, dpp<0xB1>(v)); v = max(v, dpp<0x4E>(v)); v = max(v, dpp<0x141>(v)); v = max(v, dpp<0x140>(v));
  auto r = __builtin_amdgcn_permlane16_swap(v, v, false, false);
  return max(r[0], r[1]);
}
DI unsigned wave_max_u32(unsigned v) {
  v = half_max_u32(v);
  auto r = __builtin_amdgcn_permlane32_swap(v, v, false, false);
  return max(r[0], r[1]);
}
DI u32x4 fo_swap(u32x4 v) {
  auto a = __builtin_amdgcn_permlane32_swap(v[0], v[2], false, false);
  auto b = __builtin_amdgcn_permlane32_swap(v[1], v[3], false, false);
  u32x4 o; o[0] = a[0]; o[1] = b[0]; o[2] = a[1]; o[3] = b[1];
  return o;
}
DI void row_swap(u32x4& a, u32x4& b) {
#pragma unroll
  for (int e = 0; e < 4; ++e) { auto r = __builtin_amdgcn_permlane16_swap(a[e], b[e], false, false); a[e] = r[0]; b[e] = r[1]; }
}
DI void glds4(const void* sb, unsigned vo, unsigned lds_dst) {
  unsigned keep;
  asm volatile("s_mov_b32 %0, m0\n\ts_mov_b32 m0, %3\n\ts_nop 0\n\tglobal_load_lds_dword %1, %2\n\ts_mov_b32 m0, %0"
               : "=&s"(keep) : "v"(vo), "s"(sb), "s"(lds_dst) : "memory");
}
#define GL_ "global_load_lds_dwordx4 "
#define GP_ "\n\ts_sleep %8\n\ts_waitcnt vmcnt(%7)\n\t"
template <int TH, int SL> DI void glds_v4t(const void* sb, const unsigned (&v)[4], unsigned m0v) {
  unsigned keep;
  asm volatile("s_mov_b32 %0, m0\n\ts_mov_b32 m0, %6\n\ts_nop 0\n\t" GL_ "%1, %5" GP_ GL_ "%2, %5 offset:1024" GP_ GL_ "%3, %5 offset:2048" GP_ GL_ "%4, %5 offset:3072" GP_ "s_mov_b32 m0, %0"
               : "=&s"(keep) : "v"(v[0]), "v"(v[1]), "v"(v[2]), "v"(v[3]), "s"(sb), "s"(m0v), "n"(TH), "n"(SL) : "memory");
}
DI void glds_v3(const void* sb, const unsigned (&v)[3], unsigned m0v) {
  unsigned keep;
  asm volatile("s_mov_b32 %0, m0\n\ts_mov_b32 m0, %5\n\ts_nop 0\n\t" GL_ "%1, %4\n\t" GL_ "%2, %4 offset:1024\n\t" GL_ "%3, %4 offset:2048\n\t"
               "s_mov_b32 m0, %0" : "=&s"(keep) : "v"(v[0]), "v"(v[1]), "v"(v[2]), "s"(sb), "s"(m0v) : "memory");
}
DI float dot2(unsigned a, unsigned b, float c) {
  return __builtin_amdgcn_fdot2_f32_bf16(__builtin_bit_cast(bf16x2, a), __builtin_bit_cast(bf16x2, b), c, false);
}
#define CODE_ADDR(sym, out) do { unsigned long long pc_; unsigned off_; \
  asm volatile("s_getpc_b64 %0\n1:\n\ts_mov_b32 %1, " #sym "-1b" : "=s"(pc_), "=s"(off_)); out = (const char*)(pc_ + (long long)(int)off_); } while (0)
DI void st16(void* p, u32x4 v) { asm volatile("global_store_dwordx4 %0, %1, off sc0 sc1\n\ts_nop 1" :: "v"(p), "v"(v) : "memory"); }
DI void ln_scalars(float mean, float var, float& mb, float& den) { mb = rbf(mean); den = rbf(__builtin_amdgcn_sqrtf(rbf(rbf(var) + 1e-5f))); }
DI unsigned ln_pair(unsigned v, float mb, float rd) {
  const unsigned c = pack2(lo16(v) - mb, hi16(v) - mb);
  return pack2(lo16(c) * rd, hi16(c) * rd);
}
DI unsigned ln_pair_s(unsigned v, float nmb, float rd) {
  const unsigned c = pack2(fadd(lo16(v), nmb), fadd(hi16(v), nmb));
  return pack2(fmul(lo16(c), rd), fmul(hi16(c), rd));
}
template <int N, int M> DI f32x4 sum4(const u32x4 (&v)[M]) {
  f32x4 t;
#pragma unroll
  for (int e = 0; e < 4; ++e) {
    t[e] = U2F(v[0][e]);
#pragma unroll
    for (int i = 1; i < N; ++i) t[e] = fadd(t[e], U2F(v[i][e]));
  }
  return t;
}
#define PERM_LO(x, y) __builtin_amdgcn_perm(y, x, 0x05040100u)
#define PERM_HI(x, y) __builtin_amdgcn_perm(y, x, 0x07060302u)
constexpr unsigned SPIN_CAP = 1u << 15;
DI unsigned nz(unsigned v) { return min(v, SENT - 1u); }
DI unsigned nzf(float v) { return nz(__float_as_uint(v)); }
DI unsigned mx4(unsigned m, const u32x4& v) { return max(max(max(m, v[0]), v[1]), max(v[2], v[3])); }
template <int N> DI bool none_sent(const u32x4 (&v)[N]) {
  unsigned m = 0u;
#pragma unroll
  for (int i = 0; i < N; ++i) m = mx4(m, v[i]);
  return m != SENT;
}
typedef __amdgpu_buffer_rsrc_t rsrc_t;
DI u32x4 ldg(rsrc_t rs, unsigned off) { return __builtin_amdgcn_raw_buffer_load_b128(rs, (int)off, 0, 16); }
DI void stg(rsrc_t rs, unsigned off, u32x4 v) { __builtin_amdgcn_raw_buffer_store_b128(v, rs, (int)off, 0, 16); }
DI void poll_pause(bool slow) { if (slow) __builtin_amdgcn_s_sleep(4); asm volatile("s_sleep 1" ::: "memory"); }
template <int N, class LD, class OKF> DI void poll_all(u32x4 (&out)[N], bool slow, LD ld, OKF okf) {
  for (unsigned spins = 0;; ++spins) {
    ld(out);
    SBAR();
    if (__all(okf(out)) || spins > SPIN_CAP) break;
    poll_pause(slow);
  }
}
DI void lds_set(unsigned addr, unsigned v) { asm volatile("s_waitcnt lgkmcnt(0)\n\tds_write_b32 %0, %1" :: "v"(addr), "v"(v) : "memory"); }
template <int SLEEP = 1> DI void lds_wait_ge(unsigned addr, unsigned need) {
  unsigned spin = 0x100000, v;
  asm volatile("1:\n\tds_read_b32 %0, %2\n\ts_sub_u32 %1, %1, 1\n\ts_waitcnt lgkmcnt(0)\n\ts_cbranch_scc1 2f\n\tv_cmp_gt_u32 vcc, %3, %0\n\ts_cbranch_vccz 2f\n\ts_sleep %4\n\ts_branch 1b\n2:"
               : "=&v"(v), "+&s"(spin) : "v"(addr), "s"(need), "n"(SLEEP) : "vcc", "scc", "memory");
}
DI s16x8 tr2(const char* p, int second) {
  s16x4 lo = __builtin_amdgcn_ds_read_tr16_b64_v4i16(LDS_PTR(s16x4, p)), hi = __builtin_amdgcn_ds_read_tr16_b64_v4i16(LDS_PTR(s16x4, p + second));
  return __builtin_shufflevector(lo, hi, 0, 1, 2, 3, 4, 5, 6, 7);
}
DI u32x16 cat4(const u32x4& a, const u32x4& b, const u32x4& c, const u32x4& d) {
  const u32x8 x = __builtin_shufflevector(a, b, 0, 1, 2, 3, 4, 5, 6, 7), y = __builtin_shufflevector(c, d, 0, 1, 2, 3, 4, 5, 6, 7);
  return __builtin_shufflevector(x, y, 0, 1, 2, 3, 4, 5, 6, 7, 8, 9, 10, 11, 12, 13, 14, 15);
}

DI unsigned stats_dst(unsigned sx, int tile, int tn, int lane) { return lane < 32 ? sx + SX_ST + tile * 8192 + tn * 512 + lane * 16 : OOB; }
DI void row_stats_sum(rsrc_t rs, unsigned sx, int tile, int lane, float& S, float& Q, float& Dd) {
  const unsigned exb = sx + SX_ST + tile * 8192 + (lane & 31) * 16;
  u32x4 pv[8];
  poll_all<8>(pv, false, [&](u32x4 (&v)[8]) {
#pragma unroll
    for (int i = 0; i < 8; ++i) v[i] = ldg(rs, exb + ((lane >> 5) * 8 + i) * 512);
  }, [&](const u32x4 (&v)[8]) { return none_sent(v); });
  S = 0.f; Q = 0.f; Dd = 0.f;
#pragma unroll
  for (int i = 0; i < 8; ++i) { S += __uint_as_float(pv[i][0]); Q += __uint_as_float(pv[i][1]); Dd += __uint_as_float(pv[i][2]); }
}
DI void row_stats_fin(rsrc_t rs, unsigned kdst, char* smem, int epoch, int tile, int lane, float S, float Q, float Dd, float wsum, const u32x4 (&x1p)[4],
                      float& mean, float& var, float& mb, float& rd) {
  S = half_swap_sum(S); Q = half_swap_sum(Q); Dd = half_swap_sum(Dd);
  mean = S * (1.0f / 1024.0f); var = fmaxf(Q * (1.0f / 1024.0f) - mean * mean, 0.f);
  float den;
  ln_scalars(mean, var, mb, den);
  rd = RCP(den);
  const unsigned rb = pack2((Dd - mb * wsum) * rd, 0.f) & 0xffffu;
  const unsigned mine = (((rb & 0x8000u) ? (~rb & 0xffffu) : (rb | 0x8000u)) << 16) | (unsigned)(2047 - (tile * 32 + (lane & 31)));
  const bool best = half_max_u32(mine) == mine;
  __builtin_amdgcn_raw_buffer_store_b32(mine, rs, (best && lane < 32) ? kdst : OOB, 0, 16);
  if (best) {
    char* c = smem + L_CAND + (tile & 3) * 144;
#pragma unroll
    for (int i = 0; i < 4; ++i) *reinterpret_cast<u32x4*>(c + (2 * i + (lane >> 5)) * 16) = x1p[i];
    *reinterpret_cast<u32x4*>(c + 128) = u32x4{mine, __float_as_uint(mb), __float_as_uint(rd), 0u};
  }
  lds_set((unsigned)(uintptr_t)smem + L_W + 32 + (tile & 3) * 4, epoch);
}

DI void attn_safe(rsrc_t rs, unsigned kvoff, const u32x8& q, const u32x4& ones, f32x16& o) {
  const s16x8 q0 = __builtin_bit_cast(s16x8, __builtin_shufflevector(q, q, 0, 1, 2, 3)), q1 = __builtin_bit_cast(s16x8, __builtin_shufflevector(q, q, 4, 5, 6, 7));
  f32x4 l = {0.f, 0.f, 0.f, 0.f};
#pragma unroll
  for (int e = 0; e < 16; ++e) o[e] = 0.f;
  float m = -INFINITY;
#pragma unroll 1
  for (int kt = 0; kt < 64; ++kt) {
    const unsigned p = kvoff + kt * 4096;
    const u32x4 k0 = ldg(rs, p), k1 = ldg(rs, p + 1024), v0 = ldg(rs, p + 2048), v1 = ldg(rs, p + 3072);
    f32x16 s = {};
    s = MFMA32(__builtin_bit_cast(s16x8, k0), q0, s);
    s = MFMA32(__builtin_bit_cast(s16x8, k1), q1, s);
    float mt = fmaxf(s[0], s[1]);
#pragma unroll
    for (int e = 2; e < 16; ++e) mt = fmaxf(mt, s[e]);
    const float mn = fmaxf(m, half_swap_max(mt));
    const float alpha = EX2(m - mn);
    m = mn;
#pragma unroll
    for (int e = 0; e < 16; ++e) { s[e] = EX2(s[e] - mn); o[e] *= alpha; }
    l[0] *= alpha;
    const bf16x16 pa = __builtin_bit_cast(bf16x16, __builtin_shufflevector(s, s, 0, 1, 2, 3, 4, 5, 6, 7));
    const bf16x16 pb = __builtin_bit_cast(bf16x16, __builtin_shufflevector(s, s, 8, 9, 10, 11, 12, 13, 14, 15));
    o = __builtin_amdgcn_smfmac_f32_32x32x32_bf16(__builtin_bit_cast(bf16x8, v0), pa, o, 0xdddd, 0, 0);
    l = __builtin_amdgcn_smfmac_f32_16x16x64_bf16(__builtin_bit_cast(bf16x8, ones), pa, l, 0xdddd, 0, 0);
    o = __builtin_amdgcn_smfmac_f32_32x32x32_bf16(__builtin_bit_cast(bf16x8, v1), pb, o, 0xdddd, 0, 0);
    l = __builtin_amdgcn_smfmac_f32_16x16x64_bf16(__builtin_bit_cast(bf16x8, ones), pb, l, 0xdddd, 0, 0);
  }
  o *= 1.0f / l[0];
}

extern "C" __global__ __launch_bounds__(512, 2) void mega_kernel(const u16* __restrict__ x0, const u16* __restrict__ wqkv, const u16* __restrict__ wo,
                                                              const u16* __restrict__ w_r, const u16* __restrict__ wg, const u16* __restrict__ wu,
                                                              const u16* __restrict__ wd, char* ws, u16* __restrict__ out) {
  __shared__ __attribute__((aligned(16))) char smem[LDS_BYTES];
  const int wid = __builtin_amdgcn_readfirstlane(threadIdx.x >> 6);
  STAMP_INIT;
  STAMPV(0);
  const int bid = blockIdx.x, xcd = bid & 7, loc = bid >> 3;
  const int tm = 8 * (xcd >> 2) + (loc >> 2), tn = 4 * (xcd & 3) + (loc & 3);
  const unsigned lbase = (unsigned)(uintptr_t)smem;
  const rsrc_t rs = __builtin_amdgcn_make_buffer_rsrc(ws, 0, (int)WS_BYTES, 0x00020000);
  volatile __attribute__((address_space(3))) unsigned* words = LDS_PTR(volatile unsigned, smem + L_W);
  u32x4 v[4];
  if (wid < 4)
    asm volatile("global_load_dwordx4 %0, %4, %5\n\tglobal_load_dwordx4 %1, %4, %5 offset:512\n\tglobal_load_dwordx4 %2, %4, %5 offset:1024\n\tglobal_load_dwordx4 %3, %4, %5 offset:1536"
                 : "=&v"(v[0]), "=&v"(v[1]), "=&v"(v[2]), "=&v"(v[3])
                 : "v"((threadIdx.x >> 5) * (D_ * 2) + (threadIdx.x & 31) * 16), "s"(x0 + (size_t)(tm * 128 + tn * 8) * D_) : "memory");
  else {
    asm volatile("" : "=v"(v[0]), "=v"(v[1]), "=v"(v[2]), "=v"(v[3]));
    if (wid == 6) {
      const char *c0, *c1;
      CODE_ADDR(mega_kernel, c0); CODE_ADDR(end_marker, c1);
      const unsigned o = (loc * 64 + (threadIdx.x & 63)) * 32;
      glds4(c0, (long long)o + 4 <= c1 - c0 ? o : 0u, lbase + NS * SLOTB);
    }
  }
  STAMPV(48);
  *reinterpret_cast<unsigned*>(smem + L_FULL + threadIdx.x * 4) = 0u;
  words[threadIdx.x & 31] = 0u;
  BAR_LGKM();
  if (wid < 4) {
    const int tid = threadIdx.x, lane = tid & 63, r32 = lane & 31, row = 2 * wid + (lane >> 5);
    char* sm = smem + L_XCH;
    unsigned dst = OFF_HF + (tm * 4 + (tn >> 2)) * TS + (tn & 3) * 128 + (tid >> 3) * 512 + (tid & 7) * 16;
    asm volatile("s_waitcnt vmcnt(0)" : "+v"(v[0]), "+v"(v[1]), "+v"(v[2]), "+v"(v[3]), "+v"(dst) :: "memory");
    STAMPV(49);
    float s = 0.f, s2 = 0.f;
#pragma unroll
    for (int i = 0; i < 4; ++i)
#pragma unroll
      for (int e = 0; e < 4; e += 2) { s = dot2(v[i][e], 0x3f803f80u, s); s2 = dot2(v[i][e + 1], 0x3f803f80u, s2); }
    const float mean = half_sum(s + s2) * (1.0f / 1024.0f);
    f32x2 m2 = {0.f, 0.f};
#pragma unroll
    for (int i = 0; i < 4; ++i)
#pragma unroll
      for (int e = 0; e < 4; ++e) { const f32x2 d = f32x2{lo16(v[i][e]), hi16(v[i][e])} - mean; m2 += d * d; }
    float mb, den;
    ln_scalars(mean, half_sum(m2[0] + m2[1]) * (1.0f / 1024.0f), mb, den);
    const float rd = 1.0f / den;
#pragma unroll
    for (int i = 0; i < 4; ++i) {
      char* p = sm + ((16 * i + (r32 >> 1)) * 2) * 144 + row * 16 + (r32 & 1) * 8;
      *reinterpret_cast<u32x2*>(p) = u32x2{ln_pair(v[i][0], mb, rd), ln_pair(v[i][1], mb, rd)};
      *reinterpret_cast<u32x2*>(p + 144) = u32x2{ln_pair(v[i][2], mb, rd), ln_pair(v[i][3], mb, rd)};
    }
    STAMPV(37);
    lds_set(lbase + L_W + 64 + 4 * wid, 1);
    lds_wait_ge<0>(lbase + L_W + 64 + 4 * (lane & 3), 1);
    STAMPV(50);
    u32x4 t[4];
#pragma unroll
    for (int i = 0; i < 4; ++i) t[i] = *reinterpret_cast<const u32x4*>(sm + (i * 32 + (tid >> 3)) * 144 + (tid & 7) * 16);
    SBAR();
#pragma unroll
    for (int i = 0; i < 4; ++i) stg(rs, dst + i * 16384, t[i]);
    STAMPV(51);
    DRAIN();
    lds_set(lbase + L_W + 64 + 4 * wid, 2);
    STAMPV(38);
  }
  const int lw = wid & 1;
  const char* wb = reinterpret_cast<const char*>(wqkv + (size_t)(16 * lw) * (3 * D_) + tn * 64);
  auto stage_lanes = [](unsigned (&wv)[3]) {
    const int lane = fresh_lane();
#pragma unroll
    for (int i = 0; i < 3; ++i) {
      const int li = lane + 64 * i, k = li / 24, cp = li % 24, c = cp ^ (4 * ((k >> 1) & 1));
      wv[i] = (unsigned)(k * (3 * D_ * 2) + (c >> 3) * (D_ * 2) + (c & 7) * 16 - i * 1024);
    }
  };
  auto stage = [&](const unsigned (&wv)[3], int s, int slot) {
    const char* p = wb + (size_t)(2 * kpos(tm, tn, s >> 1) + (s & 1)) * (32 * 3 * D_ * 2);
    glds_v3(p, wv, lbase + slot * SLOTB + lw * HALFB);
    glds_v3(p + 8 * (3 * D_ * 2), wv, lbase + slot * SLOTB + lw * HALFB + 3072);
  };
  auto exchange = [&](const u32x2& tag, unsigned line0, int me, bool bar = false) {
    const int lane = fresh_lane();
    __builtin_amdgcn_raw_buffer_store_b64(tag, rs, lane == 0 ? line0 + me * 128 : OOB, 0, 16);
    if (bar) BARRIER();
    JIT();
    unsigned po = lane < 16 ? line0 + lane * 128 : OOB;
    const u32x4 srd = {(unsigned)(uintptr_t)ws, (unsigned)((uintptr_t)ws >> 32) & 0xffffu, WS_BYTES, 0x00020000u};
    for (unsigned spins = 0;; ++spins) {
      u32x2 v = {0u, 0u};
      asm volatile("buffer_atomic_or_x2 %0, %1, %2, 0 offen sc0 sc1\n\ts_waitcnt vmcnt(0)" : "+v"(v) : "v"(po), "s"(srd) : "memory");
      po = ((v[0] == tag[0]) & (v[1] == tag[1])) ? OOB : po;
      if (__all(po == OOB) || spins > SPIN_CAP) break;
    }
  };
  if ((wid >> 1) == 2) {
    unsigned wv[3];
    stage_lanes(wv);
    const int r = tm & 7;
#pragma unroll 1
    for (int i = 0; i < NS; ++i) {
      const int s = i == 0 ? r : i <= r ? i - 1 : i;
      stage(wv, s, s);
      const int j = i - 1, full = j < 1 ? (j == 0 && r == 0) : j < r ? j : j + 1;
      asm volatile("s_waitcnt vmcnt(%2)\n\tds_write_b32 %0, %1" :: "v"(lbase + L_FULL + lw * 256), "v"(full), "n"(6 * 1) : "memory");
    }
    asm volatile("s_waitcnt vmcnt(0)\n\tds_write_b32 %0, %1" :: "v"(lbase + L_FULL + lw * 256), "v"(NS) : "memory");
    STAMPV(1);
  }
  if (wid == 7) {
    u32x2 tag = __builtin_amdgcn_raw_buffer_load_b64(rs, FL_EPOCH, 0, 16);
    tag[0] += 1u; tag[1] += 0x9E3779B9u;
    *reinterpret_cast<u32x2*>(smem + L_TAG) = tag;
    lds_wait_ge<0>(lbase + L_W + 64 + 4 * (fresh_lane() & 3), 2);
    exchange(tag, FL_X + tm * 2048, tn);
    lds_set(lbase + L_W + 24, 1);
  }
  if (wid >= 4) {
    auto wo_rows = [&](int g0, int n, int gs) {
      const int lane = fresh_lane();
      unsigned wov[4];
#pragma unroll
      for (int i = 0; i < 4; ++i) wov[i] = (unsigned)((8 * i + (lane >> 3)) * (D_ * 2) + ((lane & 7) ^ (4 * ((lane >> 4) & 1))) * 16 - i * 1024);
#pragma unroll 1
      for (int i = 0, k = (0 * (tm & 7)) % n; i < n; ++i, k = k + 1 == n ? 0 : k + 1) {
        const int g = g0 + gs * k;
        glds_v4t<3, 1>(reinterpret_cast<const char*>(wo) + (size_t)((((tn >> 2) + 1) * 256 + 32 * g) & 1023) * (D_ * 2) + tn * 128, wov, lbase + g * 4096);
      }
    };
    if (wid < 6) {
      const int lane = fresh_lane();
      unsigned wv[3];
      stage_lanes(wv);
      __builtin_amdgcn_s_setprio(3);
      const unsigned fra = lbase + L_FREE + (lane & 3) * 256, fla = lbase + L_FULL + lw * 256 + lane * 4;
      u32x4 pre[NS * 6];
#pragma unroll
      for (int i = 0; i < NS * 6; ++i) pre[i] = *reinterpret_cast<const u32x4*>(smem + lw * HALFB + lane * 16 + (i / 6) * SLOTB + (i % 6) * 1024);
#pragma unroll
      for (int i = 0; i < NS * 6; ++i) PIN(pre[i]);
#pragma unroll 1
      for (int L = 0; L < 2; ++L) {
        {
          constexpr unsigned STG = 32 * 3 * D_ * 2;
          unsigned off = (2 * kpos(tm, tn, NS >> 1) + (NS & 1)) * STG, dst = lbase + lw * HALFB, need = 32 * L + 3, cnt = 31 - NS, pub = 32 * L + NS - DD, keep, spin, t0, t1;
          asm volatile(
            "s_mov_b32 %[keep], m0\n\ts_branch 3f\n"
            "1:\n\ts_cbranch_vccnz 3f\n"
            "2:\n\ts_add_u32 s56, %[blo], %[off]\n\ts_addc_u32 s57, %[bhi], 0\n\ts_mov_b32 m0, %[dst]\n\ts_add_u32 s58, s56, %[half]\n\t"
            GL_ "%[w0], s[56:57]\n\ts_nop 7\n\t" GL_ "%[w1], s[56:57] offset:1024\n\ts_nop 7\n\t" GL_ "%[w2], s[56:57] offset:2048\n\ts_nop 7\n\t"
            "s_addc_u32 s59, s57, 0\n\ts_add_u32 m0, %[dst], 3072\n\ts_add_u32 %[off], %[off], %[stg]\n\t"
            GL_ "%[w0], s[58:59]\n\ts_nop 7\n\t" GL_ "%[w1], s[58:59] offset:1024\n\ts_nop 7\n\t" GL_ "%[w2], s[58:59] offset:2048\n\ts_nop 7\n\t"
            "ds_read_b32 %[t0], %[fra]\n\t"
            "s_cmp_eq_u32 %[off], %[wrap]\n\ts_cselect_b32 %[off], 0, %[off]\n\ts_cmp_eq_u32 %[off], %[own]\n\ts_cselect_b32 %[off], %[own2], %[off]\n\t"
            "s_add_u32 %[dst], %[dst], %[slot]\n\ts_cmp_eq_u32 %[dst], %[dend]\n\ts_cselect_b32 %[dst], %[dbeg], %[dst]\n\t"
            "v_add_u32 %[pub], 1, %[pub]\n\ts_add_u32 %[need], %[need], 1\n\tv_max_u32 %[t1], %[pmin], %[pub]\n\t"
            "s_waitcnt vmcnt(%[vm]) lgkmcnt(0)\n\tds_write_b32 %[fla], %[t1]\n\tv_cmp_gt_u32 vcc, %[need], %[t0]\n\t"
            "s_sub_u32 %[cnt], %[cnt], 1\n\ts_cbranch_scc0 1b\n\ts_branch 5f\n"
            "3:\n\ts_mov_b32 %[spin], 0x100000\n"
            "4:\n\tds_read_b32 %[t0], %[fra]\n\ts_sub_u32 %[spin], %[spin], 1\n\ts_waitcnt lgkmcnt(0)\n\ts_cbranch_scc1 2b\n\t"
            "v_cmp_gt_u32 vcc, %[need], %[t0]\n\ts_cbranch_vccz 2b\n\ts_sleep 1\n\ts_branch 4b\n"
            "5:\n\ts_mov_b32 m0, %[keep]"
            : [off] "+&s"(off), [dst] "+&s"(dst), [need] "+&s"(need), [cnt] "+&s"(cnt), [pub] "+&v"(pub), [keep] "=&s"(keep), [spin] "=&s"(spin), [t0] "=&v"(t0), [t1] "=&v"(t1)
            : [blo] "s"((unsigned)(uintptr_t)wb), [bhi] "s"((unsigned)((uintptr_t)wb >> 32)), [half] "s"(STG / 4), [stg] "s"(STG), [wrap] "s"(32 * STG), [own] "s"(2 * tn * STG),
              [own2] "s"(2 * ((tn + 1) & 15) * STG), [slot] "s"(SLOTB), [dbeg] "s"(lbase + lw * HALFB), [dend] "s"(lbase + lw * HALFB + NS * SLOTB), [pmin] "s"(32 * L + NS),
              [w0] "v"(wv[0]), [w1] "v"(wv[1]), [w2] "v"(wv[2]), [fra] "v"(fra), [fla] "v"(fla), [vm] "n"(DD * 6)
            : "s56", "s57", "s58", "s59", "vcc", "scc", "memory");
        }
#pragma unroll
        for (int d = DD - 1; d >= 0; --d) asm volatile("s_waitcnt vmcnt(%0)\n\tds_write_b32 %1, %2" :: "n"(d * 6), "v"(fla), "v"(32 * L + 32 - d) : "memory");
        u32x2 tag = *reinterpret_cast<const u32x2*>(smem + L_TAG);
        PIN(tag);
        if (lw == 0) {
          lds_wait_ge(lbase + L_W + 96 + 4 * (lane & 1), 1);
          lds_wait_ge<0>(lbase + L_W + 80 + 4 * (lane & 3), 2 * L + 1);
          exchange(tag, FL_KV + (L * 16 + tn) * 2048, tm, true);
          lds_wait_ge(fra, 32 * L + 32);
          lds_set(lbase + L_W + 16, L + 1);
        } else { BAR_LGKM(); lds_wait_ge<8>(lbase + L_W + 16, L + 1); }
        __builtin_amdgcn_s_sleep(24);
        wo_rows(lw, 15, 2);
        STAMPV(7 + 16 * L);
        DRAIN();
        const unsigned fo = FL_O + (L * 16 + tm) * 2048;
        if (lw) {
          lds_set(lbase + L_W + 112, 2 * L + 1);
          lds_wait_ge<4>(lbase + L_W + 48 + 4 * (lane & 3), L + 1);
          lds_wait_ge<0>(lbase + L_W + 80 + 4 * (lane & 3), 2 * L + 2);
          __builtin_amdgcn_raw_buffer_store_b64(tag, rs, lane == 0 ? fo + tn * 128 : OOB, 0, 16);
          JIT();
          wo_rows(30, 2, 1);
          DRAIN();
          lds_set(lbase + L_W + 112, 2 * L + 2);
        } else {
          lds_wait_ge<4>(lbase + L_W + 112, 2 * L + 1);
          lds_wait_ge<4>(lbase + L_W + 48 + 4 * (lane & 3), L + 1);
          unsigned po = lane < 16 ? fo + ((lane + 4 + (tn & 12)) & 15) * 128 : OOB;
          const u32x4 srd = {(unsigned)(uintptr_t)ws, (unsigned)((uintptr_t)ws >> 32) & 0xffffu, WS_BYTES, 0x00020000u};
          for (unsigned spins = 0;; ++spins) {
            u32x2 v = {0u, 0u};
            asm volatile("buffer_atomic_or_x2 %0, %1, %2, 0 offen sc0 sc1\n\ts_waitcnt vmcnt(0)" : "+v"(v) : "v"(po), "s"(srd) : "memory");
            po = ((v[0] == tag[0]) & (v[1] == tag[1])) ? OOB : po;
            unsigned m = (unsigned)__ballot(po == OOB);
            m &= m >> 1; m &= m >> 2;
            m = (m & 1u) | ((m >> 3) & 2u) | ((m >> 6) & 4u) | ((m >> 9) & 8u);
            if (m == 15u || spins > SPIN_CAP) break;
            words[5 + 2 * L] = words[28] == 2u * L + 2u ? m : m & 7u;
          }
          words[5 + 2 * L] = words[28] == 2u * L + 2u ? 15u : 7u;
          lds_wait_ge<0>(lbase + L_W + 112, 2 * L + 2);
          words[5 + 2 * L] = 15u;
          __builtin_amdgcn_raw_buffer_store_b64(tag, rs, (bid == 0 && lane == 0) ? FL_EPOCH : OOB, 0, 16);
        }
        BAR_ALL();
        if (L == 0) {
          char* d = smem + lw * HALFB + fresh_lane() * 16;
#pragma unroll
          for (int s = 0; s < NS; ++s) {
#pragma unroll
            for (int i = 0; i < 6; ++i) *reinterpret_cast<u32x4*>(d + s * SLOTB + i * 1024) = pre[s * 6 + i];
            lds_set(fla, 32 + s + 1);
            if (s == 1) lds_wait_ge(lbase + L_W + 64 + 4 * (lane & 3), 3);
          }
          STAMPV(2);
          lds_wait_ge<2>(lbase + L_W + 24, 2);
        }
      }
      STAMP_END;
      return;
    }
    const int a = wid - 6;
    const bool ffn = loc < 28;
    const int grp = xcd * 7 + (ffn ? loc >> 2 : 0), qa = (loc & 3) * 2 + a;
    const int kr = loc, cg = xcd;
    const unsigned wa = lbase + L_W;
    lds_wait_ge<2>(wa + 24, 1);
    __builtin_amdgcn_s_sleep(16);
    auto transpose8 = [](u32x4* v) {
      u32x4 t[8];
#pragma unroll
      for (int j = 0; j < 8; ++j)
#pragma unroll
        for (int e = 0; e < 4; ++e) t[j][e] = (j & 1) ? PERM_HI(v[2 * e][j >> 1], v[2 * e + 1][j >> 1]) : PERM_LO(v[2 * e][j >> 1], v[2 * e + 1][j >> 1]);
#pragma unroll
      for (int j = 0; j < 8; ++j) { v[j] = t[j]; PIN(v[j]); }
    };
    u32x4 gf[32];
    u32x4 wdf[16];
    auto wait_go = [&](int L) {
      __builtin_amdgcn_s_setprio(1);
      lds_wait_ge<8>(lbase + L_W + 16, L + 1);
      __builtin_amdgcn_s_sleep(24);
    };
    auto load_wd = [&](unsigned dv) {
      const rsrc_t rd = __builtin_amdgcn_make_buffer_rsrc(const_cast<u16*>(wd) + (size_t)grp * 64 * D_ + 128 * qa, 0, 64 * D_ * 2 - 256 * qa, 0x00020000);
#pragma unroll
      for (int i = 0; i < 16; ++i) {
        wdf[i] = __builtin_amdgcn_raw_buffer_load_b128(rd, dv, (32 * (i >> 3) + (i & 7)) * (D_ * 2), 0);
        asm volatile("s_sleep 2\n\ts_waitcnt vmcnt(2)" ::: "memory");
      }
    };
    {
      LANE_VARS;
      if (a == 0) {
        const u32x4 w0 = *reinterpret_cast<const u32x4*>(w_r + lane * 8), w1 = *reinterpret_cast<const u32x4*>(w_r + 512 + lane * 8);
        float t = 0.f;
#pragma unroll
        for (int e = 0; e < 4; ++e) t = fadd(fadd(t, fadd(lo16(w0[e]), hi16(w0[e]))), fadd(lo16(w1[e]), hi16(w1[e])));
        words[0] = __float_as_uint(wave_sum(t));
        if ((lane >> 3) == (tn & 7)) *reinterpret_cast<u32x4*>(smem + L_WR + (lane & 7) * 16) = tn < 8 ? w0 : w1;
      }
      const u16* p = wqkv + (size_t)(64 * tm + 32 * a + hi) * (3 * D_) + (c24 >> 3) * D_ + tn * 64 + (c24 & 7) * 8;
      float cs[8] = {0.f, 0.f, 0.f, 0.f, 0.f, 0.f, 0.f, 0.f};
      u32x4 v[16];
#pragma unroll
      for (int i = 0; i < 16; ++i) v[i] = *reinterpret_cast<const u32x4*>(p + (size_t)i * (2 * 3 * D_));
      SBAR();
      const u32x4 sv = {SENT, SENT, SENT, SENT};
#pragma unroll
      for (int i = 0; i < 6; ++i) {
        const unsigned o = ((i * 256 + bid) * 128 + a * 64 + lane) * 16;
        stg(rs, o < SX_BYTES ? OFF_SX + o : OOB, sv);
      }

#pragma unroll
      for (int i = 0; i < 16; ++i)
#pragma unroll
        for (int e = 0; e < 4; ++e) { cs[2 * e] = fadd(cs[2 * e], lo16(v[i][e])); cs[2 * e + 1] = fadd(cs[2 * e + 1], hi16(v[i][e])); }
      u32x4 o;
#pragma unroll
      for (int e = 0; e < 4; ++e) { const float x = half_swap_sum(cs[e]), y = half_swap_sum(cs[4 + e]); o[e] = __float_as_uint(hi ? y : x); }
      stg(rs, r32 < 24 ? OFF_CS + (tn * 32 + tm * 2 + a) * 768 + r32 * 32 + hi * 16 : OOB, o);
    }
    {
      DRAIN();
      lds_set(wa + 96 + 4 * a, 1);
      BAR_LGKM();
      LANE_VARS;
      uintptr_t gp = reinterpret_cast<uintptr_t>(((lane & 8) ? wu : wg) + (size_t)(128 * qa + 16 * hi + 4 * tblk) * F_ + 64 * grp + (lane & 7) * 8);
      unsigned dv = (lane >> 4) * (8 * D_ * 2) + (lane & 15) * 16;
      PIN(gp); PIN(dv);
      unsigned sv[3];
#pragma unroll
      for (int i = 0; i < 3; ++i) { const int li = lane + 64 * i, k = li / 24, c = li % 24; sv[i] = (unsigned)(k * (3 * D_ * 2) + (c >> 3) * (D_ * 2) + (c & 7) * 16 - i * 1024); PIN(sv[i]); }
      wait_go(0);
#pragma unroll
      for (int i = 0; i < 32; ++i) {
        gf[i] = *reinterpret_cast<const __attribute__((address_space(1))) u32x4*>(gp + (size_t)(32 * (i >> 3) + 8 * ((i >> 2) & 1) + (i & 3)) * (F_ * 2));
        asm volatile("s_sleep 2\n\ts_waitcnt vmcnt(2)" ::: "memory");
      }
      load_wd(dv);
      STAMPV(7);
      __builtin_amdgcn_s_setprio(0);
      lds_wait_ge<4>(wa + 80 + 4 * (lane & 3), 2);
#pragma unroll
      for (int g = 0; g < 4; ++g) glds_v3(wqkv + (size_t)(32 * kr + 8 * g) * (3 * D_) + (2 * cg + a) * 64, sv, lbase + L_SL + a * 12288 + g * 3072);
      transpose8(gf); transpose8(gf + 8); transpose8(gf + 16); transpose8(gf + 24);
      transpose8(wdf); transpose8(wdf + 8);
      {
        LANE_VARS;
        f32x4 t[2];
#pragma unroll
        for (int c = 0; c < 2; ++c) {
          u32x4 v[8];
#pragma unroll
          for (int i = 0; i < 8; ++i) v[i] = ldg(rs, OFF_CS + (tn * 32 + 16 * c + 2 * i + hi) * 768 + a * 384 + c24 * 16);
          SBAR();
          t[c] = sum4<8>(v);
          SBAR();
        }
#pragma unroll
        for (int e = 0; e < 4; ++e) t[0][e] = half_swap_sum(fadd(t[0][e], t[1][e]));
        if (lane < 24) *reinterpret_cast<f32x4*>(smem + L_CS + a * 384 + lane * 16) = t[0];
      }
      u32x2 tag = *reinterpret_cast<const u32x2*>(smem + L_TAG);
      PIN(tag);
      BAR_ALL();
      if (a) {
        lds_wait_ge<0>(wa + 64 + 4 * (lane & 3), 3);
        exchange(tag, FL_X + (16 + tm) * 2048, tn);
        lds_set(wa + 24, 2);
        STAMPV(12);
      }
    }
#pragma unroll 1
    for (int L = fresh_s(0);; ++L) {
      const unsigned sx = OFF_SX + L * SX_LAYER;
      STAMPV(1 + 16 * L);
      __builtin_amdgcn_s_setprio(1);
      u32x4 hf[4];
      unsigned key;
      { LANE_VARS;
      lds_wait_ge<0>(wa + 32 + 4 * tp, L + 1);
      if (a == 0) {
        const char* cp = smem + L_CAND + (r32 >> 3) * 144;
        const u32x4 c = *reinterpret_cast<const u32x4*>(cp + (lane & 7) * 16), rec = *reinterpret_cast<const u32x4*>(cp + 128);
        u32x4 o;
#pragma unroll
        for (int e = 0; e < 4; ++e) { unsigned h = ln_pair_s(c[e], -U2F(rec[1]), U2F(rec[2])); PIN(h); o[e] = nz(hi ? c[e] : h); }
        stg(rs, sx + SX_ROW + hi * 131072 + (tm * 4 + (r32 >> 3)) * 2048 + tn * 128 + (lane & 7) * 16, o);
        STAMPV(8 + 16 * L);
        lds_wait_ge<0>(wa + 104, L + 1);
        key = __builtin_amdgcn_readfirstlane(words[27]);
      } else {
        const u32x4 srd = {(unsigned)(uintptr_t)ws, (unsigned)((uintptr_t)ws >> 32) & 0xffffu, WS_BYTES, 0x00020000u};
        unsigned po = sx + SX_CAND + (tn * 16 + (lane >> 2)) * 128 + tp * 4;
        key = 0u;
        for (unsigned spins = 0;; ++spins) {
          unsigned v = 0u;
          asm volatile("buffer_atomic_or %0, %1, %2, 0 offen sc0 sc1\n\ts_waitcnt vmcnt(0)" : "+v"(v) : "v"(po), "s"(srd) : "memory");
          key = v == SENT ? key : max(key, v);
          po = v == SENT ? po : OOB;
          if (__all(po == OOB) || spins > SPIN_CAP) break;
        }
        key = __builtin_amdgcn_readfirstlane(wave_max_u32(key));
        words[27] = key;
        words[3] = 2047u - (key & 2047u);
        lds_set(wa + 104, L + 1);
        STAMPV(13 + 16 * L);
      }
      poll_all<4>(hf, false, [&](u32x4 (&t)[4]) {
#pragma unroll
        for (int i = 0; i < 4; ++i) t[i] = ldg(rs, sx + SX_ROW + ((2047 - (int)(key & 2047u)) >> 5) * 2048 + (16 * qa + 4 * i + (lane >> 4)) * 16);
      }, [&](const u32x4 (&t)[4]) { return none_sent(t); });
      if (L == 1) lds_set(wa + 4 + 4 * a, 2);
      }
      STAMPV(2 + 16 * L);
      f32x4 d[8];
      const f32x4 zero = {0.f, 0.f, 0.f, 0.f};
      SBAR();
      __builtin_amdgcn_s_setprio(3);
#pragma unroll
      for (int t = 0; t < 32; ++t) d[t & 7] = MFMA16(__builtin_bit_cast(s16x8, hf[t >> 3]), __builtin_bit_cast(s16x8, gf[t]), t < 8 ? zero : d[t & 7]);
      SBAR();
      __builtin_amdgcn_s_setprio(1);
      LANE_VARS;
      const int idx = 2047 - (int)(key & 2047u), win = idx >> 7;
      const bool owner = win == tm;
      auto pick2 = [&](const f32x4 (&d)[8]) {
        u32x2 o;
#pragma unroll
        for (int e = 0; e < 2; ++e) {
          const float x = tblk ? d[2 + e][0] : d[e][0], y = tblk ? d[6 + e][0] : d[4 + e][0];
          o[e] = nzf(hi ? y : x);
        }
        return o;
      };
      if (ffn) {
        const unsigned ppos = (lane & 15) * 32 + (lane >> 4) * 8;
        __builtin_amdgcn_raw_buffer_store_b64(pick2(d), rs, sx + SX_GP + (grp * 8 + qa) * 512 + ppos, 0, 16);
        STAMPV(9 + 16 * L);
        __builtin_amdgcn_s_sleep(16);
        u32x2 gu[8];
        for (unsigned spins = 0;; ++spins) {
#pragma unroll
          for (int i = 0; i < 8; ++i) gu[i] = __builtin_amdgcn_raw_buffer_load_b64(rs, sx + SX_GP + (grp * 8 + i) * 512 + lane * 8, 0, 16);
          SBAR();
          unsigned m = 0u;
#pragma unroll
          for (int i = 0; i < 8; ++i) m = max(max(m, gu[i][0]), gu[i][1]);
          if (__all(m != SENT) || spins > SPIN_CAP) break;
          poll_pause(L == 0);
        }
        STAMPV(10 + 16 * L);
        float t0 = opq(U2F(gu[0][0])), t1 = opq(U2F(gu[0][1]));
#pragma unroll
        for (int i = 1; i < 8; ++i) { t0 = fadd(t0, U2F(gu[i][0])); t1 = fadd(t1, U2F(gu[i][1])); }
        auto sw = __builtin_amdgcn_permlane32_swap(__float_as_uint(t0), __float_as_uint(t1), false, false);
        const float gv = rbf(U2F(sw[0])), uv = rbf(U2F(sw[1]));
        reinterpret_cast<u16*>(smem + L_GU + a * 128)[2 * r32 + hi] = (u16)pack2(fmul(rbf(fmul(gv, RCP(fadd(1.0f, __expf(-gv))))), uv), 0.f);
        asm volatile("s_waitcnt lgkmcnt(0)" ::: "memory");
        const s16x8 a0 = *reinterpret_cast<const s16x8*>(smem + L_GU + a * 128 + (lane >> 4) * 16), a1 = *reinterpret_cast<const s16x8*>(smem + L_GU + a * 128 + 64 + (lane >> 4) * 16);
        SBAR();
        __builtin_amdgcn_s_setprio(3);
#pragma unroll
        for (int t = 0; t < 16; ++t) d[t & 7] = MFMA16(t < 8 ? a0 : a1, __builtin_bit_cast(s16x8, wdf[t]), t < 8 ? zero : d[t & 7]);
        SBAR();
        __builtin_amdgcn_s_setprio(1);
        __builtin_amdgcn_raw_buffer_store_b64(pick2(d), rs, sx + SX_YP + grp * 4096 + qa * 512 + ppos, 0, 16);
      }
      STAMPV(3 + 16 * L);
      if (L == 1 && (cg != (kr & 7) || a)) { STAMP_END; __builtin_amdgcn_endpgm(); }
      s16x8 wpf[12];
#pragma unroll
      for (int t = 0; t < 12; ++t) wpf[t] = tr2(smem + L_SL + a * 12288 + (16 * hi + 4 * tblk + tq) * 384 + t * 32 + tp * 8, 8 * 384);
#pragma unroll
      for (int t = 0; t < 12; ++t) PIN(wpf[t]);
      if (L == 0 && !owner) lds_set(wa + 4 + 4 * a, 1);
      __builtin_amdgcn_s_sleep(9);
      if (L == 0 && !ffn) __builtin_amdgcn_s_sleep(64);
      const unsigned rk = key >> 16;
      float rsig = rbf(RCP(fadd(1.0f, __expf(-lo16((rk & 0x8000u) ? (rk & 0x7fffu) : (~rk & 0xffffu))))));
      PIN(rsig);
      const int n = 4 * (lane & 7) + 2 * tblk + hi;
      const int fo = ((n >> 4) * 2 + ((n >> 2) & 1)) * 8 + ((n >> 3) & 1) * 4 + (n & 3);
      unsigned odst = (L == 1 && !(lane & 8)) ? (idx * D_ + kr * 32 + n) * 2 : OOB;
      PIN(odst);
      u32x4 pv[8];
      poll_all<8>(pv, L == 0, [&](u32x4 (&t)[8]) {
#pragma unroll
        for (int i = 0; i < 7; ++i) t[i] = ldg(rs, sx + SX_YP + (8 * i + (lane >> 3)) * 4096 + kr * 128 + (lane & 7) * 16);
        t[7][0] = t[7][2] = __builtin_amdgcn_raw_buffer_load_b32(rs, sx + SX_ROW + (idx >> 5) * 2048 + kr * 64 + (fo >> 1) * 4, 0, 16);
        t[7][1] = t[7][3] = __builtin_amdgcn_raw_buffer_load_b32(rs, sx + SX_ROW + 131072 + (idx >> 5) * 2048 + kr * 64 + (fo >> 1) * 4, 0, 16);
      }, [&](const u32x4 (&t)[8]) { return none_sent(t); });
      const float hv = (fo & 1) ? hi16(pv[7][0]) : lo16(pv[7][0]), xv = (fo & 1) ? hi16(pv[7][1]) : lo16(pv[7][1]);
      STAMPV(4 + 16 * L);
      float ys;
      {
        const f32x4 t = sum4<7>(pv);
        auto p = __builtin_amdgcn_permlane32_swap(__float_as_uint(t[0]), __float_as_uint(t[1]), false, false);
        auto q = __builtin_amdgcn_permlane32_swap(__float_as_uint(t[2]), __float_as_uint(t[3]), false, false);
        auto r = __builtin_amdgcn_permlane16_swap(__float_as_uint(fadd(U2F(p[0]), U2F(p[1]))), __float_as_uint(fadd(U2F(q[0]), U2F(q[1]))), false, false);
        ys = fadd(U2F(r[0]), U2F(r[1]));
        ys = fadd(ys, U2F(dpp<0x128>(__float_as_uint(ys))));
      }
      const unsigned ob = pack2(fadd(xv, rbf(fadd(hv, rbf(fmul(rbf(ys), rsig))))), 0.f) & 0xffffu;
      __builtin_amdgcn_raw_buffer_store_b16((u16)ob, __builtin_amdgcn_make_buffer_rsrc(out, 0, 2048 * D_ * 2, 0x00020000), odst, 0, 0);
      if (L == 1) {
        STAMPV(5 + 16 * L);
        STAMP_END;
        __builtin_amdgcn_endpgm();
      }
      reinterpret_cast<u16*>(smem + L_X2F + a * 64)[fo] = (u16)ob;
      asm volatile("s_waitcnt lgkmcnt(0)" ::: "memory");
      {
        const s16x8 xa = *reinterpret_cast<const s16x8*>(smem + L_X2F + a * 64 + (lane >> 4) * 16);
        const u32x4 xf = *reinterpret_cast<const u32x4*>(smem + L_X2F + a * 64 + tp * 16);
        f32x4 z[12];
        SBAR();
        __builtin_amdgcn_s_setprio(3);
#pragma unroll
        for (int t = 0; t < 12; ++t) z[t] = MFMA16(xa, wpf[t], zero);
        SBAR();
        __builtin_amdgcn_s_setprio(1);
        const float x2 = lo16(ob), s1 = fmul(wave_sum(x2), 0.5f), s2 = fmul(wave_sum(fmul(x2, x2)), 0.5f);
        u32x4 o;
#pragma unroll
        for (int e = 0; e < 4; ++e) {
          const unsigned sp = lane < 52 ? (e == 0 ? __float_as_uint(s1) : __float_as_uint(s2)) : xf[e];
          const unsigned x = __float_as_uint(tblk ? z[4 + e][0] : z[e][0]), y = tblk ? sp : __float_as_uint(z[8 + e][0]);
          o[e] = nz(hi ? y : x);
        }
        const unsigned pos = lane < 24 ? lane * 16 : lane < 48 ? lane * 16 + 128 : lane < 52 ? (lane & 1) * 512 + 384 : 400 + tp * 16;
        stg(rs, lane < 56 ? OFF_SX + SX_QP + ((2 * cg + a) * 32 + kr) * 1024 + pos : OOB, o);
      }
      STAMPV(5 + 16 * L);
      if (owner) {
        __builtin_amdgcn_s_sleep(13);
        const int c25 = r32 < 25 ? r32 : 24, pl = 24 * a + c24, col = 64 * (pl >> 4) + (pl & 15);
        const int pfo = (col & ~15) + ((col >> 2) & 1) * 16 + ((col >> 3) & 1) * 8 + (col & 3);
        float cs[4];
#pragma unroll
        for (int e = 0; e < 4; ++e) { cs[e] = reinterpret_cast<const float*>(smem + L_CS)[col + 16 * e]; PIN(cs[e]); }
        u32x4 qv[17];
        poll_all<17>(qv, true, [&](u32x4 (&t)[17]) {
#pragma unroll
          for (int i = 0; i < 16; ++i) t[i] = ldg(rs, OFF_SX + SX_QP + (tn * 32 + 2 * i + hi) * 1024 + a * 512 + c25 * 16);
          t[16] = ldg(rs, OFF_SX + SX_QP + (tn * 34 + hi) * 1024 + 400 + tp * 16);
        }, [&](const u32x4 (&t)[17]) { return none_sent(t); });
        *reinterpret_cast<u32x4*>(smem + L_X2P + hi * 64 + tp * 16) = qv[16];
        f32x4 t = sum4<16>(qv);
#pragma unroll
        for (int e = 0; e < 4; ++e) t[e] = half_swap_sum(t[e]);
        const float mean = fmul(U2F(__builtin_amdgcn_readlane(__float_as_uint(t[0]), 24)), 1.0f / 1024.0f);
        const float var = fmaxf(ffma(-mean, mean, fmul(U2F(__builtin_amdgcn_readlane(__float_as_uint(t[1]), 24)), 1.0f / 1024.0f)), 0.f);
        float mb, den;
        ln_scalars(mean, var, mb, den);
        const float rd = RCP(den);
        if (lane < 24) {
#pragma unroll
          for (int e = 0; e < 4; ++e) reinterpret_cast<u16*>(smem + L_PATCH)[pfo + 32 * (e >> 1) + 4 * (e & 1)] = (u16)pack2(fmul(ffma(-mb, cs[e], t[e]), pl < 16 ? fmul(rd, QS) : rd), 0.f);
        }
        lds_set(wa + 4 + 4 * a, L + 1);
      }
      STAMPV(6 + 16 * L);
      BAR_ALL();
      unsigned dv = fresh_lane();
      dv = (dv >> 4) * (8 * D_ * 2) + (dv & 15) * 16;
      PIN(dv);
      wait_go(1);
      load_wd(dv);
      STAMPV(23);
      __builtin_amdgcn_s_setprio(0);
      lds_wait_ge<4>(wa + 80 + 4 * (fresh_lane() & 3), 4);
      transpose8(wdf); transpose8(wdf + 8);
      BARRIER();
    }
  }
  const int tm_ = tm, tn_ = tn, wid_ = wid;
#define KSKIP "{s49}"(tn * 4096), "{s50}"(((tn + 1) & 15) * 4096)
  const u32x4 srd = {(unsigned)(uintptr_t)ws, (unsigned)((uintptr_t)ws >> 32) & 0xffffu, WS_BYTES, 0x00020000u};
  u32x4 x1p[4];
  asm volatile("" : "=v"(x1p[0]), "=v"(x1p[1]), "=v"(x1p[2]), "=v"(x1p[3]));
#pragma unroll 1
  for (int L = 0; L < 2; ++L) {
    const unsigned sx = OFF_SX + L * SX_LAYER;
    u32x8 q[4];
    u32x16 kva, kvb;
    f32x16 oz[5];
    {
      f32x16 acc[6];
      u32x16 stA, stB, xr1, xr2, xr3;
      u32x8 xra, xrb;
      u32x4 stC;
      asm volatile("" : "={v[112:127]}"(stB), "={v[128:131]}"(stC));
      { PHASE_VARS;
      stB[8] = (L ? OFF_X1 : OFF_HF) + tile * TS + tblk * 1024 + (hi * 32 + (lane & 15)) * 16;
      stB[9] = lbase + tblk * HALFB + wrow * 384 + (((4 * hh + (tp >> 1)) ^ wsw) << 4) + 8 * (tp & 1);
      stB[10] = lbase + tblk * HALFB + wrow * 384 + (((4 * (hh ^ 1) + (tp >> 1)) ^ wsw) << 4) + 8 * (tp & 1);
      stB[13] = lbase + L_FULL + (lane & 1) * 256;
      stB[15] = lbase + L_FREE + wid * 256 + lane * 4;
      stC[0] = 32 * L + 2; }
      unsigned soff, slot;
      if (L == 0) {
        lds_wait_ge<0>(lbase + L_W + 24, 1);
        STAMPV(39);
        soff = tn * 4096;
        asm volatile(
          "v_add_u32 v131, s44, v120\n\t"
          "buffer_load_dwordx4 v[132:135], v131, s[40:43], 0 offen offset:0 sc1\n\t"
          "buffer_load_dwordx4 v[136:139], v131, s[40:43], 0 offen offset:256 sc1\n\t"
          "buffer_load_dwordx4 v[140:143], v131, s[40:43], 0 offen offset:2048 sc1\n\t"
          "buffer_load_dwordx4 v[144:147], v131, s[40:43], 0 offen offset:2304 sc1\n\t"
          "s_mov_b32 s44, s51\n\t"
          "v_add_u32 v131, s44, v120\n\t"
          "buffer_load_dwordx4 v[148:151], v131, s[40:43], 0 offen offset:0 sc1\n\t"
          "buffer_load_dwordx4 v[152:155], v131, s[40:43], 0 offen offset:256 sc1\n\t"
          "buffer_load_dwordx4 v[156:159], v131, s[40:43], 0 offen offset:2048 sc1\n\t"
          "buffer_load_dwordx4 v[160:163], v131, s[40:43], 0 offen offset:2304 sc1\n\t"
          "s_addk_i32 s44, 0x1000\n\t"
          "s_bitset0_b32 s44, 16\n\t"
          "s_cmp_eq_u32 s44, s49\n\t"
          "s_cselect_b32 s44, s50, s44\n\t"
          "v_add_u32 v131, s44, v120\n\t"
          "buffer_load_dwordx4 v[164:167], v131, s[40:43], 0 offen offset:0 sc1\n\t"
          "buffer_load_dwordx4 v[168:171], v131, s[40:43], 0 offen offset:256 sc1\n\t"
          "buffer_load_dwordx4 v[172:175], v131, s[40:43], 0 offen offset:2048 sc1\n\t"
          "buffer_load_dwordx4 v[176:179], v131, s[40:43], 0 offen offset:2304 sc1\n\t"
          "s_addk_i32 s44, 0x1000\n\t"
          "s_bitset0_b32 s44, 16\n\t"
          "s_cmp_eq_u32 s44, s49\n\t"
          "s_cselect_b32 s44, s50, s44\n\t"
          "v_add_u32 v131, s44, v120\n\t"
          "buffer_load_dwordx4 v[180:183], v131, s[40:43], 0 offen offset:0 sc1\n\t"
          "buffer_load_dwordx4 v[184:187], v131, s[40:43], 0 offen offset:256 sc1\n\t"
          "buffer_load_dwordx4 v[188:191], v131, s[40:43], 0 offen offset:2048 sc1\n\t"
          "buffer_load_dwordx4 v[192:195], v131, s[40:43], 0 offen offset:2304 sc1\n\t"
          : "={v[132:139]}"(xra), "={v[140:147]}"(xrb), "={v[148:163]}"(xr1), "={v[164:179]}"(xr2), "={v[180:195]}"(xr3), "+{s44}"(soff), "+{v[128:131]}"(stC) : "{v[112:127]}"(stB), "{s[40:43]}"(srd), KSKIP, "{s51}"(kpos(tm, tn, 1) * 4096) : "scc", "memory");
      } else {
        u32x4 t[4] = {x1p[0], x1p[1], x1p[2], x1p[3]};
        row_swap(t[0], t[1]); row_swap(t[2], t[3]);
        xra = __builtin_shufflevector(t[0], t[1], 0, 1, 2, 3, 4, 5, 6, 7); xrb = __builtin_shufflevector(t[2], t[3], 0, 1, 2, 3, 4, 5, 6, 7);
        asm volatile("" : "={v[148:163]}"(xr1), "={v[164:179]}"(xr2), "={v[180:195]}"(xr3));
      }
      STAMPV(1 + 16 * L);
      asm volatile(
        "v_mov_b32 v123, v121\n\t"
        "v_mov_b32 v124, v122\n\t"
        "s_mov_b64 vcc, -1\n\t"
        "s_cbranch_vccnz 10f\n\t"
        "11:\n\t"
        "s_movk_i32 s46, 12352\n\t"
        "s_mov_b32 s47, 111168\n\t"
        "ds_read_b64_tr_b16 v[96:97], v123 offset:256\n\t"
        "ds_read_b64_tr_b16 v[98:99], v123 offset:3328\n\t"
        "ds_read_b64_tr_b16 v[100:101], v123 offset:288\n\t"
        "ds_read_b64_tr_b16 v[102:103], v123 offset:3360\n\t"
        "ds_read_b64_tr_b16 v[104:105], v124 offset:256\n\t"
        "ds_read_b64_tr_b16 v[106:107], v124 offset:3328\n\t"
        "ds_write_b32 v127, v128\n\t"
        "ds_read_b64_tr_b16 v[108:109], v124 offset:288\n\t"
        "ds_read_b64_tr_b16 v[110:111], v124 offset:3360\n\t"
        "ds_read_b64_tr_b16 v[112:113], v123 offset:128\n\t"
        "ds_read_b64_tr_b16 v[114:115], v123 offset:3200\n\t"
        "v_add_u32 v128, 2, v128\n\t"
        "s_waitcnt vmcnt(14) lgkmcnt(7)\n\t"
        "v_mfma_f32_16x16x32_bf16 v[64:67], v[132:135], v[96:99], 0\n\t"
        "ds_read_b64_tr_b16 v[116:117], v123 offset:160\n\t"
        "v_mfma_f32_16x16x32_bf16 v[72:75], v[136:139], v[96:99], 0\n\t"
        "ds_read_b64_tr_b16 v[118:119], v123 offset:3232\n\t"
        "v_mfma_f32_16x16x32_bf16 v[68:71], v[132:135], v[100:103], 0\n\t"
        "ds_read_b64_tr_b16 v[96:97], v124 offset:128\n\t"
        "v_mfma_f32_16x16x32_bf16 v[76:79], v[136:139], v[100:103], 0\n\t"
        "ds_read_b64_tr_b16 v[98:99], v124 offset:3200\n\t"
        "s_waitcnt lgkmcnt(6)\n\t"
        "v_mfma_f32_16x16x32_bf16 v[80:83], v[132:135], v[104:107], 0\n\t"
        "ds_read_b64_tr_b16 v[100:101], v124 offset:160\n\t"
        "v_mfma_f32_16x16x32_bf16 v[88:91], v[136:139], v[104:107], 0\n\t"
        "ds_read_b64_tr_b16 v[102:103], v124 offset:3232\n\t"
        "v_mfma_f32_16x16x32_bf16 v[84:87], v[132:135], v[108:111], 0\n\t"
        "ds_read_b64_tr_b16 v[104:105], v123 offset:0\n\t"
        "v_add_u32 v129, s46, v121\n\t"
        "v_mfma_f32_16x16x32_bf16 v[92:95], v[136:139], v[108:111], 0\n\t"
        "ds_read_b64_tr_b16 v[106:107], v123 offset:3072\n\t"
        "s_waitcnt lgkmcnt(6)\n\t"
        "v_mfma_f32_16x16x32_bf16 v[32:35], v[112:115], v[132:135], 0\n\t"
        "ds_read_b64_tr_b16 v[108:109], v123 offset:32\n\t"
        "v_mfma_f32_16x16x32_bf16 v[40:43], v[112:115], v[136:139], 0\n\t"
        "ds_read_b64_tr_b16 v[110:111], v123 offset:3104\n\t"
        "v_add_u32 v130, s46, v122\n\t"
        "v_mfma_f32_16x16x32_bf16 v[36:39], v[116:119], v[132:135], 0\n\t"
        "ds_read_b64_tr_b16 v[112:113], v124 offset:0\n\t"
        "v_mfma_f32_16x16x32_bf16 v[44:47], v[116:119], v[136:139], 0\n\t"
        "ds_read_b64_tr_b16 v[114:115], v124 offset:3072\n\t"
        "s_waitcnt lgkmcnt(6)\n\t"
        "v_mfma_f32_16x16x32_bf16 v[48:51], v[96:99], v[132:135], 0\n\t"
        "ds_read_b64_tr_b16 v[116:117], v124 offset:32\n\t"
        "v_mfma_f32_16x16x32_bf16 v[56:59], v[96:99], v[136:139], 0\n\t"
        "ds_read_b64_tr_b16 v[118:119], v124 offset:3104\n\t"
        "v_mfma_f32_16x16x32_bf16 v[52:55], v[100:103], v[132:135], 0\n\t"
        "ds_read_b64_tr_b16 v[96:97], v129 offset:256\n\t"
        "s_addk_i32 s46, 12352\n\t"
        "v_mfma_f32_16x16x32_bf16 v[60:63], v[100:103], v[136:139], 0\n\t"
        "ds_read_b64_tr_b16 v[98:99], v129 offset:3328\n\t"
        "s_waitcnt lgkmcnt(6)\n\t"
        "v_mfma_f32_16x16x32_bf16 v[0:3], v[104:107], v[132:135], 0\n\t"
        "ds_read_b64_tr_b16 v[100:101], v129 offset:288\n\t"
        "s_cmp_eq_u32 s46, s47\n\t"
        "v_mfma_f32_16x16x32_bf16 v[8:11], v[104:107], v[136:139], 0\n\t"
        "ds_read_b64_tr_b16 v[102:103], v129 offset:3360\n\t"
        "s_cselect_b32 s46, 0, s46\n\t"
        "v_mfma_f32_16x16x32_bf16 v[4:7], v[108:111], v[132:135], 0\n\t"
        "ds_read_b64_tr_b16 v[104:105], v130 offset:256\n\t"
        "v_mfma_f32_16x16x32_bf16 v[12:15], v[108:111], v[136:139], 0\n\t"
        "ds_read_b64_tr_b16 v[106:107], v130 offset:3328\n\t"
        "s_waitcnt lgkmcnt(6)\n\t"
        "v_mfma_f32_16x16x32_bf16 v[16:19], v[112:115], v[132:135], 0\n\t"
        "ds_read_b64_tr_b16 v[108:109], v130 offset:288\n\t"
        "v_mfma_f32_16x16x32_bf16 v[24:27], v[112:115], v[136:139], 0\n\t"
        "ds_read_b64_tr_b16 v[110:111], v130 offset:3360\n\t"
        "v_mfma_f32_16x16x32_bf16 v[20:23], v[116:119], v[132:135], 0\n\t"
        "ds_read_b64_tr_b16 v[112:113], v129 offset:128\n\t"
        "v_mfma_f32_16x16x32_bf16 v[28:31], v[116:119], v[136:139], 0\n\t"
        "ds_read_b64_tr_b16 v[114:115], v129 offset:3200\n\t"
        "s_waitcnt lgkmcnt(0)\n\t"
        "s_nop 7\n\t"
        "s_branch 9f\n\t"
        "10:\n\t"
        "s_mov_b32 s48, 0x10000\n\t"
        "8:\n\t"
        "ds_read_b32 v126, v125\n\t"
        "s_sub_u32 s48, s48, 1\n\t"
        "s_waitcnt lgkmcnt(0)\n\t"
        "s_cbranch_scc1 11b\n\t"
        "v_cmp_gt_u32 vcc, v128, v126\n\t"
        "s_cbranch_vccnz 8b\n\t"
        "s_branch 11b\n\t"
        "9:\n\t"
        : "={v[0:15]}"(acc[0]), "={v[16:31]}"(acc[1]), "={v[32:47]}"(acc[2]), "={v[48:63]}"(acc[3]), "={v[64:79]}"(acc[4]), "={v[80:95]}"(acc[5]),
          "={v[96:111]}"(stA), "+{v[112:127]}"(stB), "+{v[128:131]}"(stC), "={s46}"(slot), "+{v[140:147]}"(xrb)
        : "{v[132:139]}"(xra) : "s47", "s48", "vcc", "scc", "memory");
      STAMPV(2 + 16 * L);
      if (L == 1) {
        PHASE_VARS;
        float S, Q, Dd;
        __builtin_amdgcn_s_sleep(0);
        row_stats_sum(rs, OFF_SX, tile, lane, S, Q, Dd);
        STAMPV(36);
        auto request_x = [&]() {
          lds_wait_ge<0>(lbase + L_W + 24, 2);
          soff = kpos(tm, tn, 1) * 4096;
          asm volatile(
          "v_add_u32 v131, s44, v120\n\t"
          "buffer_load_dwordx4 v[148:151], v131, s[40:43], 0 offen offset:0 sc1\n\t"
          "buffer_load_dwordx4 v[152:155], v131, s[40:43], 0 offen offset:256 sc1\n\t"
          "buffer_load_dwordx4 v[156:159], v131, s[40:43], 0 offen offset:2048 sc1\n\t"
          "buffer_load_dwordx4 v[160:163], v131, s[40:43], 0 offen offset:2304 sc1\n\t"
          "s_addk_i32 s44, 0x1000\n\t"
          "s_bitset0_b32 s44, 16\n\t"
          "s_cmp_eq_u32 s44, s49\n\t"
          "s_cselect_b32 s44, s50, s44\n\t"
          "v_add_u32 v131, s44, v120\n\t"
          "buffer_load_dwordx4 v[164:167], v131, s[40:43], 0 offen offset:0 sc1\n\t"
          "buffer_load_dwordx4 v[168:171], v131, s[40:43], 0 offen offset:256 sc1\n\t"
          "buffer_load_dwordx4 v[172:175], v131, s[40:43], 0 offen offset:2048 sc1\n\t"
          "buffer_load_dwordx4 v[176:179], v131, s[40:43], 0 offen offset:2304 sc1\n\t"
          "s_addk_i32 s44, 0x1000\n\t"
          "s_bitset0_b32 s44, 16\n\t"
          "s_cmp_eq_u32 s44, s49\n\t"
          "s_cselect_b32 s44, s50, s44\n\t"
          "v_add_u32 v131, s44, v120\n\t"
          "buffer_load_dwordx4 v[180:183], v131, s[40:43], 0 offen offset:0 sc1\n\t"
          "buffer_load_dwordx4 v[184:187], v131, s[40:43], 0 offen offset:256 sc1\n\t"
          "buffer_load_dwordx4 v[188:191], v131, s[40:43], 0 offen offset:2048 sc1\n\t"
          "buffer_load_dwordx4 v[192:195], v131, s[40:43], 0 offen offset:2304 sc1\n\t"
            : "={v[148:163]}"(xr1), "={v[164:179]}"(xr2), "={v[180:195]}"(xr3), "+{s44}"(soff), "+{v[128:131]}"(stC) : "{v[112:127]}"(stB), "{s[40:43]}"(srd), KSKIP : "scc", "memory");
          SBAR();
        };
        float mean, var, mb1, rd1, mb2, den2;
        row_stats_fin(rs, OFF_SX + SX_CAND + (tn * 16 + tm) * 128 + wid * 4, smem, 1, tile, lane, S, Q, Dd, U2F(words[0]), x1p, mean, var, mb1, rd1);
        ln_scalars(mean + (mean - mb1) * rd1, var * (1.0f + rd1) * (1.0f + rd1), mb2, den2);
        const float rd2 = RCP(den2);
        const float al = (1.0f + rd1) * rd2;
        const float be = -(mb1 * rd1 + mb2) * RCP(1.0f + rd1);
        reinterpret_cast<float*>(smem + L_AB)[wid * 64 + lane] = hi ? be : al;
        reinterpret_cast<float*>(smem + L_AB)[256 + wid * 64 + lane] = hi ? 1.0f + rd1 : -mb1 * rd1;
        const float* ab = reinterpret_cast<const float*>(smem + L_AB) + wid * 64 + 32;
        const float bt[2] = {ab[lane & 15], ab[16 + (lane & 15)]};
#pragma unroll
        for (int jj = 0; jj < 2; ++jj) {
          if (jj == 0) request_x();
          f32x4 cs[2][2], bv[2];
          float cv[2];
#pragma unroll
          for (int i = 0; i < 2; ++i) {
            const float* p = reinterpret_cast<const float*>(smem + L_CS) + (jj ^ hh) * 32 + 16 * i;
            bv[i] = *reinterpret_cast<const f32x4*>(ab + 16 * i + 4 * (lane >> 4));
            cs[0][i] = *reinterpret_cast<const f32x4*>(p + 4 * (lane >> 4)); cs[1][i] = *reinterpret_cast<const f32x4*>(p + 64 + 4 * (lane >> 4));
            cv[i] = p[128 + (lane & 15)];
          }
          SBAR();
#pragma unroll
          for (int j = 0; j < 3; ++j)
#pragma unroll
            for (int e = 0; e < 16; ++e) acc[2 * j + jj][e] += j < 2 ? bt[e >> 3] * cs[j & 1][(e >> 2) & 1][e & 3] : bv[e >> 3][e & 3] * cv[(e >> 2) & 1];
          SBAR();
        }
        if (0 == 2) request_x();
      }
      unsigned ep[4];
      f32x2 al2;
      { PHASE_VARS;
        float* ab = reinterpret_cast<float*>(smem + L_AB) + wid * 64;
        if (L == 0) ab[lane] = 1.0f;
        al2 = f32x2{ab[lane & 15], ab[16 + (lane & 15)]};
        const unsigned dst = OFF_KV + L * (10 * MBY) + tn * 2 * HS + tile * 4096 + lane * 16;
        ep[0] = dst + hh * HS; ep[1] = dst + (hh ^ 1) * HS; ep[2] = lbase + L_AB + wid * 256 + (lane >> 4) * 16; ep[3] = lbase + L_KX + wid * 4096 + lane * 16; }
      STAMPV(3 + 16 * L);
      soff = kpos(tm, tn, 1 + 2) * 4096;
      asm volatile(
        "s_mov_b32 s47, 111168\n\t"
        "v_add_u32 v131, s44, v120\n\t"
        "s_waitcnt vmcnt(12) lgkmcnt(6)\n\t"
        "v_mfma_f32_16x16x32_bf16 v[64:67], v[140:143], v[96:99], v[64:67]\n\t"
        "ds_read_b64_tr_b16 v[116:117], v129 offset:160\n\t"
        "v_mfma_f32_16x16x32_bf16 v[72:75], v[144:147], v[96:99], v[72:75]\n\t"
        "ds_read_b64_tr_b16 v[118:119], v129 offset:3232\n\t"
        "v_mfma_f32_16x16x32_bf16 v[68:71], v[140:143], v[100:103], v[68:71]\n\t"
        "ds_read_b64_tr_b16 v[96:97], v130 offset:128\n\t"
        "ds_read_b32 v126, v125\n\t"
        "v_mfma_f32_16x16x32_bf16 v[76:79], v[144:147], v[100:103], v[76:79]\n\t"
        "ds_read_b64_tr_b16 v[98:99], v130 offset:3200\n\t"
        "s_waitcnt lgkmcnt(7)\n\t"
        "v_mfma_f32_16x16x32_bf16 v[80:83], v[140:143], v[104:107], v[80:83]\n\t"
        "ds_read_b64_tr_b16 v[100:101], v130 offset:160\n\t"
        "v_mfma_f32_16x16x32_bf16 v[88:91], v[144:147], v[104:107], v[88:91]\n\t"
        "ds_read_b64_tr_b16 v[102:103], v130 offset:3232\n\t"
        "v_mfma_f32_16x16x32_bf16 v[84:87], v[140:143], v[108:111], v[84:87]\n\t"
        "ds_read_b64_tr_b16 v[104:105], v129 offset:0\n\t"
        "v_add_u32 v123, s46, v121\n\t"
        "v_mfma_f32_16x16x32_bf16 v[92:95], v[144:147], v[108:111], v[92:95]\n\t"
        "ds_read_b64_tr_b16 v[106:107], v129 offset:3072\n\t"
        "s_waitcnt lgkmcnt(5)\n\t"
        "v_mfma_f32_16x16x32_bf16 v[32:35], v[112:115], v[140:143], v[32:35]\n\t"
        "ds_read_b64_tr_b16 v[108:109], v129 offset:32\n\t"
        "v_cmp_gt_u32 vcc, v128, v126\n\t"
        "v_mfma_f32_16x16x32_bf16 v[40:43], v[112:115], v[144:147], v[40:43]\n\t"
        "ds_read_b64_tr_b16 v[110:111], v129 offset:3104\n\t"
        "v_add_u32 v124, s46, v122\n\t"
        "v_mfma_f32_16x16x32_bf16 v[36:39], v[116:119], v[140:143], v[36:39]\n\t"
        "ds_read_b64_tr_b16 v[112:113], v130 offset:0\n\t"
        "v_mfma_f32_16x16x32_bf16 v[44:47], v[116:119], v[144:147], v[44:47]\n\t"
        "ds_read_b64_tr_b16 v[114:115], v130 offset:3072\n\t"
        "s_waitcnt lgkmcnt(6)\n\t"
        "v_mfma_f32_16x16x32_bf16 v[48:51], v[96:99], v[140:143], v[48:51]\n\t"
        "ds_read_b64_tr_b16 v[116:117], v130 offset:32\n\t"
        "v_mfma_f32_16x16x32_bf16 v[56:59], v[96:99], v[144:147], v[56:59]\n\t"
        "ds_read_b64_tr_b16 v[118:119], v130 offset:3104\n\t"
        "s_cbranch_vccnz 12f\n\t"
        "13:\n\t"
        "v_mfma_f32_16x16x32_bf16 v[52:55], v[100:103], v[140:143], v[52:55]\n\t"
        "ds_read_b64_tr_b16 v[96:97], v123 offset:256\n\t"
        "s_addk_i32 s46, 12352\n\t"
        "v_mfma_f32_16x16x32_bf16 v[60:63], v[100:103], v[144:147], v[60:63]\n\t"
        "ds_read_b64_tr_b16 v[98:99], v123 offset:3328\n\t"
        "s_waitcnt lgkmcnt(6)\n\t"
        "v_mfma_f32_16x16x32_bf16 v[0:3], v[104:107], v[140:143], v[0:3]\n\t"
        "ds_read_b64_tr_b16 v[100:101], v123 offset:288\n\t"
        "s_cmp_eq_u32 s46, s47\n\t"
        "v_mfma_f32_16x16x32_bf16 v[8:11], v[104:107], v[144:147], v[8:11]\n\t"
        "ds_read_b64_tr_b16 v[102:103], v123 offset:3360\n\t"
        "s_cselect_b32 s46, 0, s46\n\t"
        "v_mfma_f32_16x16x32_bf16 v[4:7], v[108:111], v[140:143], v[4:7]\n\t"
        "ds_read_b64_tr_b16 v[104:105], v124 offset:256\n\t"
        "v_mfma_f32_16x16x32_bf16 v[12:15], v[108:111], v[144:147], v[12:15]\n\t"
        "ds_read_b64_tr_b16 v[106:107], v124 offset:3328\n\t"
        "s_waitcnt lgkmcnt(6)\n\t"
        "v_mfma_f32_16x16x32_bf16 v[16:19], v[112:115], v[140:143], v[16:19]\n\t"
        "ds_write_b32 v127, v128\n\t"
        "ds_read_b64_tr_b16 v[108:109], v124 offset:288\n\t"
        "v_mfma_f32_16x16x32_bf16 v[24:27], v[112:115], v[144:147], v[24:27]\n\t"
        "ds_read_b64_tr_b16 v[110:111], v124 offset:3360\n\t"
        "v_add_u32 v128, 2, v128\n\t"
        "v_mfma_f32_16x16x32_bf16 v[20:23], v[116:119], v[140:143], v[20:23]\n\t"
        "ds_read_b64_tr_b16 v[112:113], v123 offset:128\n\t"
        "v_mfma_f32_16x16x32_bf16 v[28:31], v[116:119], v[144:147], v[28:31]\n\t"
        "ds_read_b64_tr_b16 v[114:115], v123 offset:3200\n\t"
        "s_mov_b32 s45, 2\n\t"
        "1:\n\t"
        "s_waitcnt vmcnt(10) lgkmcnt(7)\n\t"
        "v_mfma_f32_16x16x32_bf16 v[64:67], v[148:151], v[96:99], v[64:67]\n\t"
        "ds_read_b64_tr_b16 v[116:117], v123 offset:160\n\t"
        "s_addk_i32 s44, 0x1000\n\t"
        "v_mfma_f32_16x16x32_bf16 v[72:75], v[152:155], v[96:99], v[72:75]\n\t"
        "ds_read_b64_tr_b16 v[118:119], v123 offset:3232\n\t"
        "s_bitset0_b32 s44, 16\n\t"
        "v_mfma_f32_16x16x32_bf16 v[68:71], v[148:151], v[100:103], v[68:71]\n\t"
        "ds_read_b64_tr_b16 v[96:97], v124 offset:128\n\t"
        "s_cmp_eq_u32 s44, s49\n\t"
        "v_mfma_f32_16x16x32_bf16 v[76:79], v[152:155], v[100:103], v[76:79]\n\t"
        "ds_read_b64_tr_b16 v[98:99], v124 offset:3200\n\t"
        "s_waitcnt lgkmcnt(6)\n\t"
        "v_mfma_f32_16x16x32_bf16 v[80:83], v[148:151], v[104:107], v[80:83]\n\t"
        "ds_read_b64_tr_b16 v[100:101], v124 offset:160\n\t"
        "s_cselect_b32 s44, s50, s44\n\t"
        "v_mfma_f32_16x16x32_bf16 v[88:91], v[152:155], v[104:107], v[88:91]\n\t"
        "ds_read_b64_tr_b16 v[102:103], v124 offset:3232\n\t"
        "v_add_u32 v131, s44, v120\n\t"
        "v_mfma_f32_16x16x32_bf16 v[84:87], v[148:151], v[108:111], v[84:87]\n\t"
        "ds_read_b64_tr_b16 v[104:105], v123 offset:0\n\t"
        "v_add_u32 v129, s46, v121\n\t"
        "v_mfma_f32_16x16x32_bf16 v[92:95], v[152:155], v[108:111], v[92:95]\n\t"
        "ds_read_b64_tr_b16 v[106:107], v123 offset:3072\n\t"
        "s_waitcnt lgkmcnt(6)\n\t"
        "v_mfma_f32_16x16x32_bf16 v[32:35], v[112:115], v[148:151], v[32:35]\n\t"
        "ds_read_b64_tr_b16 v[108:109], v123 offset:32\n\t"
        "v_mfma_f32_16x16x32_bf16 v[40:43], v[112:115], v[152:155], v[40:43]\n\t"
        "ds_read_b64_tr_b16 v[110:111], v123 offset:3104\n\t"
        "v_add_u32 v130, s46, v122\n\t"
        "v_mfma_f32_16x16x32_bf16 v[36:39], v[116:119], v[148:151], v[36:39]\n\t"
        "ds_read_b64_tr_b16 v[112:113], v124 offset:0\n\t"
        "buffer_load_dwordx4 v[132:135], v131, s[40:43], 0 offen offset:0 sc1\n\t"
        "v_mfma_f32_16x16x32_bf16 v[44:47], v[116:119], v[152:155], v[44:47]\n\t"
        "ds_read_b64_tr_b16 v[114:115], v124 offset:3072\n\t"
        "s_waitcnt lgkmcnt(6)\n\t"
        "v_mfma_f32_16x16x32_bf16 v[48:51], v[96:99], v[148:151], v[48:51]\n\t"
        "ds_read_b64_tr_b16 v[116:117], v124 offset:32\n\t"
        "v_mfma_f32_16x16x32_bf16 v[56:59], v[96:99], v[152:155], v[56:59]\n\t"
        "ds_read_b64_tr_b16 v[118:119], v124 offset:3104\n\t"
        "v_mfma_f32_16x16x32_bf16 v[52:55], v[100:103], v[148:151], v[52:55]\n\t"
        "ds_read_b64_tr_b16 v[96:97], v129 offset:256\n\t"
        "s_addk_i32 s46, 12352\n\t"
        "v_mfma_f32_16x16x32_bf16 v[60:63], v[100:103], v[152:155], v[60:63]\n\t"
        "ds_read_b64_tr_b16 v[98:99], v129 offset:3328\n\t"
        "s_waitcnt lgkmcnt(6)\n\t"
        "v_mfma_f32_16x16x32_bf16 v[0:3], v[104:107], v[148:151], v[0:3]\n\t"
        "ds_read_b64_tr_b16 v[100:101], v129 offset:288\n\t"
        "s_cmp_eq_u32 s46, s47\n\t"
        "v_mfma_f32_16x16x32_bf16 v[8:11], v[104:107], v[152:155], v[8:11]\n\t"
        "ds_read_b64_tr_b16 v[102:103], v129 offset:3360\n\t"
        "s_cselect_b32 s46, 0, s46\n\t"
        "v_mfma_f32_16x16x32_bf16 v[4:7], v[108:111], v[148:151], v[4:7]\n\t"
        "ds_read_b64_tr_b16 v[104:105], v130 offset:256\n\t"
        "buffer_load_dwordx4 v[136:139], v131, s[40:43], 0 offen offset:256 sc1\n\t"
        "v_mfma_f32_16x16x32_bf16 v[12:15], v[108:111], v[152:155], v[12:15]\n\t"
        "ds_read_b64_tr_b16 v[106:107], v130 offset:3328\n\t"
        "s_waitcnt lgkmcnt(6)\n\t"
        "v_mfma_f32_16x16x32_bf16 v[16:19], v[112:115], v[148:151], v[16:19]\n\t"
        "ds_read_b64_tr_b16 v[108:109], v130 offset:288\n\t"
        "v_mfma_f32_16x16x32_bf16 v[24:27], v[112:115], v[152:155], v[24:27]\n\t"
        "ds_read_b64_tr_b16 v[110:111], v130 offset:3360\n\t"
        "v_mfma_f32_16x16x32_bf16 v[20:23], v[116:119], v[148:151], v[20:23]\n\t"
        "ds_read_b64_tr_b16 v[112:113], v129 offset:128\n\t"
        "v_mfma_f32_16x16x32_bf16 v[28:31], v[116:119], v[152:155], v[28:31]\n\t"
        "ds_read_b64_tr_b16 v[114:115], v129 offset:3200\n\t"
        "s_waitcnt vmcnt(10) lgkmcnt(6)\n\t"
        "v_mfma_f32_16x16x32_bf16 v[64:67], v[156:159], v[96:99], v[64:67]\n\t"
        "ds_read_b64_tr_b16 v[116:117], v129 offset:160\n\t"
        "v_mfma_f32_16x16x32_bf16 v[72:75], v[160:163], v[96:99], v[72:75]\n\t"
        "ds_read_b64_tr_b16 v[118:119], v129 offset:3232\n\t"
        "v_mfma_f32_16x16x32_bf16 v[68:71], v[156:159], v[100:103], v[68:71]\n\t"
        "ds_read_b64_tr_b16 v[96:97], v130 offset:128\n\t"
        "ds_read_b32 v126, v125\n\t"
        "v_mfma_f32_16x16x32_bf16 v[76:79], v[160:163], v[100:103], v[76:79]\n\t"
        "ds_read_b64_tr_b16 v[98:99], v130 offset:3200\n\t"
        "s_waitcnt lgkmcnt(7)\n\t"
        "v_mfma_f32_16x16x32_bf16 v[80:83], v[156:159], v[104:107], v[80:83]\n\t"
        "ds_read_b64_tr_b16 v[100:101], v130 offset:160\n\t"
        "v_mfma_f32_16x16x32_bf16 v[88:91], v[160:163], v[104:107], v[88:91]\n\t"
        "ds_read_b64_tr_b16 v[102:103], v130 offset:3232\n\t"
        "v_mfma_f32_16x16x32_bf16 v[84:87], v[156:159], v[108:111], v[84:87]\n\t"
        "ds_read_b64_tr_b16 v[104:105], v129 offset:0\n\t"
        "v_add_u32 v123, s46, v121\n\t"
        "v_mfma_f32_16x16x32_bf16 v[92:95], v[160:163], v[108:111], v[92:95]\n\t"
        "ds_read_b64_tr_b16 v[106:107], v129 offset:3072\n\t"
        "s_waitcnt lgkmcnt(5)\n\t"
        "v_mfma_f32_16x16x32_bf16 v[32:35], v[112:115], v[156:159], v[32:35]\n\t"
        "ds_read_b64_tr_b16 v[108:109], v129 offset:32\n\t"
        "v_cmp_gt_u32 vcc, v128, v126\n\t"
        "v_mfma_f32_16x16x32_bf16 v[40:43], v[112:115], v[160:163], v[40:43]\n\t"
        "ds_read_b64_tr_b16 v[110:111], v129 offset:3104\n\t"
        "v_add_u32 v124, s46, v122\n\t"
        "v_mfma_f32_16x16x32_bf16 v[36:39], v[116:119], v[156:159], v[36:39]\n\t"
        "ds_read_b64_tr_b16 v[112:113], v130 offset:0\n\t"
        "buffer_load_dwordx4 v[140:143], v131, s[40:43], 0 offen offset:2048 sc1\n\t"
        "v_mfma_f32_16x16x32_bf16 v[44:47], v[116:119], v[160:163], v[44:47]\n\t"
        "ds_read_b64_tr_b16 v[114:115], v130 offset:3072\n\t"
        "s_waitcnt lgkmcnt(6)\n\t"
        "v_mfma_f32_16x16x32_bf16 v[48:51], v[96:99], v[156:159], v[48:51]\n\t"
        "ds_read_b64_tr_b16 v[116:117], v130 offset:32\n\t"
        "v_mfma_f32_16x16x32_bf16 v[56:59], v[96:99], v[160:163], v[56:59]\n\t"
        "ds_read_b64_tr_b16 v[118:119], v130 offset:3104\n\t"
        "s_cbranch_vccnz 14f\n\t"
        "15:\n\t"
        "v_mfma_f32_16x16x32_bf16 v[52:55], v[100:103], v[156:159], v[52:55]\n\t"
        "ds_read_b64_tr_b16 v[96:97], v123 offset:256\n\t"
        "s_addk_i32 s46, 12352\n\t"
        "v_mfma_f32_16x16x32_bf16 v[60:63], v[100:103], v[160:163], v[60:63]\n\t"
        "ds_read_b64_tr_b16 v[98:99], v123 offset:3328\n\t"
        "s_waitcnt lgkmcnt(6)\n\t"
        "v_mfma_f32_16x16x32_bf16 v[0:3], v[104:107], v[156:159], v[0:3]\n\t"
        "ds_read_b64_tr_b16 v[100:101], v123 offset:288\n\t"
        "s_cmp_eq_u32 s46, s47\n\t"
        "v_mfma_f32_16x16x32_bf16 v[8:11], v[104:107], v[160:163], v[8:11]\n\t"
        "ds_read_b64_tr_b16 v[102:103], v123 offset:3360\n\t"
        "s_cselect_b32 s46, 0, s46\n\t"
        "v_mfma_f32_16x16x32_bf16 v[4:7], v[108:111], v[156:159], v[4:7]\n\t"
        "ds_read_b64_tr_b16 v[104:105], v124 offset:256\n\t"
        "buffer_load_dwordx4 v[144:147], v131, s[40:43], 0 offen offset:2304 sc1\n\t"
        "v_mfma_f32_16x16x32_bf16 v[12:15], v[108:111], v[160:163], v[12:15]\n\t"
        "ds_read_b64_tr_b16 v[106:107], v124 offset:3328\n\t"
        "s_waitcnt lgkmcnt(6)\n\t"
        "v_mfma_f32_16x16x32_bf16 v[16:19], v[112:115], v[156:159], v[16:19]\n\t"
        "ds_write_b32 v127, v128\n\t"
        "ds_read_b64_tr_b16 v[108:109], v124 offset:288\n\t"
        "v_mfma_f32_16x16x32_bf16 v[24:27], v[112:115], v[160:163], v[24:27]\n\t"
        "ds_read_b64_tr_b16 v[110:111], v124 offset:3360\n\t"
        "v_add_u32 v128, 2, v128\n\t"
        "v_mfma_f32_16x16x32_bf16 v[20:23], v[116:119], v[156:159], v[20:23]\n\t"
        "ds_read_b64_tr_b16 v[112:113], v123 offset:128\n\t"
        "v_mfma_f32_16x16x32_bf16 v[28:31], v[116:119], v[160:163], v[28:31]\n\t"
        "ds_read_b64_tr_b16 v[114:115], v123 offset:3200\n\t"
        "s_waitcnt vmcnt(10) lgkmcnt(7)\n\t"
        "v_mfma_f32_16x16x32_bf16 v[64:67], v[164:167], v[96:99], v[64:67]\n\t"
        "ds_read_b64_tr_b16 v[116:117], v123 offset:160\n\t"
        "s_addk_i32 s44, 0x1000\n\t"
        "v_mfma_f32_16x16x32_bf16 v[72:75], v[168:171], v[96:99], v[72:75]\n\t"
        "ds_read_b64_tr_b16 v[118:119], v123 offset:3232\n\t"
        "s_bitset0_b32 s44, 16\n\t"
        "v_mfma_f32_16x16x32_bf16 v[68:71], v[164:167], v[100:103], v[68:71]\n\t"
        "ds_read_b64_tr_b16 v[96:97], v124 offset:128\n\t"
        "s_cmp_eq_u32 s44, s49\n\t"
        "v_mfma_f32_16x16x32_bf16 v[76:79], v[168:171], v[100:103], v[76:79]\n\t"
        "ds_read_b64_tr_b16 v[98:99], v124 offset:3200\n\t"
        "s_waitcnt lgkmcnt(6)\n\t"
        "v_mfma_f32_16x16x32_bf16 v[80:83], v[164:167], v[104:107], v[80:83]\n\t"
        "ds_read_b64_tr_b16 v[100:101], v124 offset:160\n\t"
        "s_cselect_b32 s44, s50, s44\n\t"
        "v_mfma_f32_16x16x32_bf16 v[88:91], v[168:171], v[104:107], v[88:91]\n\t"
        "ds_read_b64_tr_b16 v[102:103], v124 offset:3232\n\t"
        "v_add_u32 v131, s44, v120\n\t"
        "v_mfma_f32_16x16x32_bf16 v[84:87], v[164:167], v[108:111], v[84:87]\n\t"
        "ds_read_b64_tr_b16 v[104:105], v123 offset:0\n\t"
        "v_add_u32 v129, s46, v121\n\t"
        "v_mfma_f32_16x16x32_bf16 v[92:95], v[168:171], v[108:111], v[92:95]\n\t"
        "ds_read_b64_tr_b16 v[106:107], v123 offset:3072\n\t"
        "s_waitcnt lgkmcnt(6)\n\t"
        "v_mfma_f32_16x16x32_bf16 v[32:35], v[112:115], v[164:167], v[32:35]\n\t"
        "ds_read_b64_tr_b16 v[108:109], v123 offset:32\n\t"
        "v_mfma_f32_16x16x32_bf16 v[40:43], v[112:115], v[168:171], v[40:43]\n\t"
        "ds_read_b64_tr_b16 v[110:111], v123 offset:3104\n\t"
        "v_add_u32 v130, s46, v122\n\t"
        "v_mfma_f32_16x16x32_bf16 v[36:39], v[116:119], v[164:167], v[36:39]\n\t"
        "ds_read_b64_tr_b16 v[112:113], v124 offset:0\n\t"
        "buffer_load_dwordx4 v[148:151], v131, s[40:43], 0 offen offset:0 sc1\n\t"
        "v_mfma_f32_16x16x32_bf16 v[44:47], v[116:119], v[168:171], v[44:47]\n\t"
        "ds_read_b64_tr_b16 v[114:115], v124 offset:3072\n\t"
        "s_waitcnt lgkmcnt(6)\n\t"
        "v_mfma_f32_16x16x32_bf16 v[48:51], v[96:99], v[164:167], v[48:51]\n\t"
        "ds_read_b64_tr_b16 v[116:117], v124 offset:32\n\t"
        "v_mfma_f32_16x16x32_bf16 v[56:59], v[96:99], v[168:171], v[56:59]\n\t"
        "ds_read_b64_tr_b16 v[118:119], v124 offset:3104\n\t"
        "v_mfma_f32_16x16x32_bf16 v[52:55], v[100:103], v[164:167], v[52:55]\n\t"
        "ds_read_b64_tr_b16 v[96:97], v129 offset:256\n\t"
        "s_addk_i32 s46, 12352\n\t"
        "v_mfma_f32_16x16x32_bf16 v[60:63], v[100:103], v[168:171], v[60:63]\n\t"
        "ds_read_b64_tr_b16 v[98:99], v129 offset:3328\n\t"
        "s_waitcnt lgkmcnt(6)\n\t"
        "v_mfma_f32_16x16x32_bf16 v[0:3], v[104:107], v[164:167], v[0:3]\n\t"
        "ds_read_b64_tr_b16 v[100:101], v129 offset:288\n\t"
        "s_cmp_eq_u32 s46, s47\n\t"
        "v_mfma_f32_16x16x32_bf16 v[8:11], v[104:107], v[168:171], v[8:11]\n\t"
        "ds_read_b64_tr_b16 v[102:103], v129 offset:3360\n\t"
        "s_cselect_b32 s46, 0, s46\n\t"
        "v_mfma_f32_16x16x32_bf16 v[4:7], v[108:111], v[164:167], v[4:7]\n\t"
        "ds_read_b64_tr_b16 v[104:105], v130 offset:256\n\t"
        "buffer_load_dwordx4 v[152:155], v131, s[40:43], 0 offen offset:256 sc1\n\t"
        "v_mfma_f32_16x16x32_bf16 v[12:15], v[108:111], v[168:171], v[12:15]\n\t"
        "ds_read_b64_tr_b16 v[106:107], v130 offset:3328\n\t"
        "s_waitcnt lgkmcnt(6)\n\t"
        "v_mfma_f32_16x16x32_bf16 v[16:19], v[112:115], v[164:167], v[16:19]\n\t"
        "ds_read_b64_tr_b16 v[108:109], v130 offset:288\n\t"
        "v_mfma_f32_16x16x32_bf16 v[24:27], v[112:115], v[168:171], v[24:27]\n\t"
        "ds_read_b64_tr_b16 v[110:111], v130 offset:3360\n\t"
        "v_mfma_f32_16x16x32_bf16 v[20:23], v[116:119], v[164:167], v[20:23]\n\t"
        "ds_read_b64_tr_b16 v[112:113], v129 offset:128\n\t"
        "v_mfma_f32_16x16x32_bf16 v[28:31], v[116:119], v[168:171], v[28:31]\n\t"
        "ds_read_b64_tr_b16 v[114:115], v129 offset:3200\n\t"
        "s_waitcnt vmcnt(10) lgkmcnt(6)\n\t"
        "v_mfma_f32_16x16x32_bf16 v[64:67], v[172:175], v[96:99], v[64:67]\n\t"
        "ds_read_b64_tr_b16 v[116:117], v129 offset:160\n\t"
        "v_mfma_f32_16x16x32_bf16 v[72:75], v[176:179], v[96:99], v[72:75]\n\t"
        "ds_read_b64_tr_b16 v[118:119], v129 offset:3232\n\t"
        "v_mfma_f32_16x16x32_bf16 v[68:71], v[172:175], v[100:103], v[68:71]\n\t"
        "ds_read_b64_tr_b16 v[96:97], v130 offset:128\n\t"
        "ds_read_b32 v126, v125\n\t"
        "v_mfma_f32_16x16x32_bf16 v[76:79], v[176:179], v[100:103], v[76:79]\n\t"
        "ds_read_b64_tr_b16 v[98:99], v130 offset:3200\n\t"
        "s_waitcnt lgkmcnt(7)\n\t"
        "v_mfma_f32_16x16x32_bf16 v[80:83], v[172:175], v[104:107], v[80:83]\n\t"
        "ds_read_b64_tr_b16 v[100:101], v130 offset:160\n\t"
        "v_mfma_f32_16x16x32_bf16 v[88:91], v[176:179], v[104:107], v[88:91]\n\t"
        "ds_read_b64_tr_b16 v[102:103], v130 offset:3232\n\t"
        "v_mfma_f32_16x16x32_bf16 v[84:87], v[172:175], v[108:111], v[84:87]\n\t"
        "ds_read_b64_tr_b16 v[104:105], v129 offset:0\n\t"
        "v_add_u32 v123, s46, v121\n\t"
        "v_mfma_f32_16x16x32_bf16 v[92:95], v[176:179], v[108:111], v[92:95]\n\t"
        "ds_read_b64_tr_b16 v[106:107], v129 offset:3072\n\t"
        "s_waitcnt lgkmcnt(5)\n\t"
        "v_mfma_f32_16x16x32_bf16 v[32:35], v[112:115], v[172:175], v[32:35]\n\t"
        "ds_read_b64_tr_b16 v[108:109], v129 offset:32\n\t"
        "v_cmp_gt_u32 vcc, v128, v126\n\t"
        "v_mfma_f32_16x16x32_bf16 v[40:43], v[112:115], v[176:179], v[40:43]\n\t"
        "ds_read_b64_tr_b16 v[110:111], v129 offset:3104\n\t"
        "v_add_u32 v124, s46, v122\n\t"
        "v_mfma_f32_16x16x32_bf16 v[36:39], v[116:119], v[172:175], v[36:39]\n\t"
        "ds_read_b64_tr_b16 v[112:113], v130 offset:0\n\t"
        "buffer_load_dwordx4 v[156:159], v131, s[40:43], 0 offen offset:2048 sc1\n\t"
        "v_mfma_f32_16x16x32_bf16 v[44:47], v[116:119], v[176:179], v[44:47]\n\t"
        "ds_read_b64_tr_b16 v[114:115], v130 offset:3072\n\t"
        "s_waitcnt lgkmcnt(6)\n\t"
        "v_mfma_f32_16x16x32_bf16 v[48:51], v[96:99], v[172:175], v[48:51]\n\t"
        "ds_read_b64_tr_b16 v[116:117], v130 offset:32\n\t"
        "v_mfma_f32_16x16x32_bf16 v[56:59], v[96:99], v[176:179], v[56:59]\n\t"
        "ds_read_b64_tr_b16 v[118:119], v130 offset:3104\n\t"
        "s_cbranch_vccnz 16f\n\t"
        "17:\n\t"
        "v_mfma_f32_16x16x32_bf16 v[52:55], v[100:103], v[172:175], v[52:55]\n\t"
        "ds_read_b64_tr_b16 v[96:97], v123 offset:256\n\t"
        "s_addk_i32 s46, 12352\n\t"
        "v_mfma_f32_16x16x32_bf16 v[60:63], v[100:103], v[176:179], v[60:63]\n\t"
        "ds_read_b64_tr_b16 v[98:99], v123 offset:3328\n\t"
        "s_waitcnt lgkmcnt(6)\n\t"
        "v_mfma_f32_16x16x32_bf16 v[0:3], v[104:107], v[172:175], v[0:3]\n\t"
        "ds_read_b64_tr_b16 v[100:101], v123 offset:288\n\t"
        "s_cmp_eq_u32 s46, s47\n\t"
        "v_mfma_f32_16x16x32_bf16 v[8:11], v[104:107], v[176:179], v[8:11]\n\t"
        "ds_read_b64_tr_b16 v[102:103], v123 offset:3360\n\t"
        "s_cselect_b32 s46, 0, s46\n\t"
        "v_mfma_f32_16x16x32_bf16 v[4:7], v[108:111], v[172:175], v[4:7]\n\t"
        "ds_read_b64_tr_b16 v[104:105], v124 offset:256\n\t"
        "buffer_load_dwordx4 v[160:163], v131, s[40:43], 0 offen offset:2304 sc1\n\t"
        "v_mfma_f32_16x16x32_bf16 v[12:15], v[108:111], v[176:179], v[12:15]\n\t"
        "ds_read_b64_tr_b16 v[106:107], v124 offset:3328\n\t"
        "s_waitcnt lgkmcnt(6)\n\t"
        "v_mfma_f32_16x16x32_bf16 v[16:19], v[112:115], v[172:175], v[16:19]\n\t"
        "ds_write_b32 v127, v128\n\t"
        "ds_read_b64_tr_b16 v[108:109], v124 offset:288\n\t"
        "v_mfma_f32_16x16x32_bf16 v[24:27], v[112:115], v[176:179], v[24:27]\n\t"
        "ds_read_b64_tr_b16 v[110:111], v124 offset:3360\n\t"
        "v_add_u32 v128, 2, v128\n\t"
        "v_mfma_f32_16x16x32_bf16 v[20:23], v[116:119], v[172:175], v[20:23]\n\t"
        "ds_read_b64_tr_b16 v[112:113], v123 offset:128\n\t"
        "v_mfma_f32_16x16x32_bf16 v[28:31], v[116:119], v[176:179], v[28:31]\n\t"
        "ds_read_b64_tr_b16 v[114:115], v123 offset:3200\n\t"
        "s_waitcnt vmcnt(10) lgkmcnt(7)\n\t"
        "v_mfma_f32_16x16x32_bf16 v[64:67], v[180:183], v[96:99], v[64:67]\n\t"
        "ds_read_b64_tr_b16 v[116:117], v123 offset:160\n\t"
        "s_addk_i32 s44, 0x1000\n\t"
        "v_mfma_f32_16x16x32_bf16 v[72:75], v[184:187], v[96:99], v[72:75]\n\t"
        "ds_read_b64_tr_b16 v[118:119], v123 offset:3232\n\t"
        "s_bitset0_b32 s44, 16\n\t"
        "v_mfma_f32_16x16x32_bf16 v[68:71], v[180:183], v[100:103], v[68:71]\n\t"
        "ds_read_b64_tr_b16 v[96:97], v124 offset:128\n\t"
        "s_cmp_eq_u32 s44, s49\n\t"
        "v_mfma_f32_16x16x32_bf16 v[76:79], v[184:187], v[100:103], v[76:79]\n\t"
        "ds_read_b64_tr_b16 v[98:99], v124 offset:3200\n\t"
        "s_waitcnt lgkmcnt(6)\n\t"
        "v_mfma_f32_16x16x32_bf16 v[80:83], v[180:183], v[104:107], v[80:83]\n\t"
        "ds_read_b64_tr_b16 v[100:101], v124 offset:160\n\t"
        "s_cselect_b32 s44, s50, s44\n\t"
        "v_mfma_f32_16x16x32_bf16 v[88:91], v[184:187], v[104:107], v[88:91]\n\t"
        "ds_read_b64_tr_b16 v[102:103], v124 offset:3232\n\t"
        "v_add_u32 v131, s44, v120\n\t"
        "v_mfma_f32_16x16x32_bf16 v[84:87], v[180:183], v[108:111], v[84:87]\n\t"
        "ds_read_b64_tr_b16 v[104:105], v123 offset:0\n\t"
        "v_add_u32 v129, s46, v121\n\t"
        "v_mfma_f32_16x16x32_bf16 v[92:95], v[184:187], v[108:111], v[92:95]\n\t"
        "ds_read_b64_tr_b16 v[106:107], v123 offset:3072\n\t"
        "s_waitcnt lgkmcnt(6)\n\t"
        "v_mfma_f32_16x16x32_bf16 v[32:35], v[112:115], v[180:183], v[32:35]\n\t"
        "ds_read_b64_tr_b16 v[108:109], v123 offset:32\n\t"
        "v_mfma_f32_16x16x32_bf16 v[40:43], v[112:115], v[184:187], v[40:43]\n\t"
        "ds_read_b64_tr_b16 v[110:111], v123 offset:3104\n\t"
        "v_add_u32 v130, s46, v122\n\t"
        "v_mfma_f32_16x16x32_bf16 v[36:39], v[116:119], v[180:183], v[36:39]\n\t"
        "ds_read_b64_tr_b16 v[112:113], v124 offset:0\n\t"
        "buffer_load_dwordx4 v[164:167], v131, s[40:43], 0 offen offset:0 sc1\n\t"
        "v_mfma_f32_16x16x32_bf16 v[44:47], v[116:119], v[184:187], v[44:47]\n\t"
        "ds_read_b64_tr_b16 v[114:115], v124 offset:3072\n\t"
        "s_waitcnt lgkmcnt(6)\n\t"
        "v_mfma_f32_16x16x32_bf16 v[48:51], v[96:99], v[180:183], v[48:51]\n\t"
        "ds_read_b64_tr_b16 v[116:117], v124 offset:32\n\t"
        "v_mfma_f32_16x16x32_bf16 v[56:59], v[96:99], v[184:187], v[56:59]\n\t"
        "ds_read_b64_tr_b16 v[118:119], v124 offset:3104\n\t"
        "v_mfma_f32_16x16x32_bf16 v[52:55], v[100:103], v[180:183], v[52:55]\n\t"
        "ds_read_b64_tr_b16 v[96:97], v129 offset:256\n\t"
        "s_addk_i32 s46, 12352\n\t"
        "v_mfma_f32_16x16x32_bf16 v[60:63], v[100:103], v[184:187], v[60:63]\n\t"
        "ds_read_b64_tr_b16 v[98:99], v129 offset:3328\n\t"
        "s_waitcnt lgkmcnt(6)\n\t"
        "v_mfma_f32_16x16x32_bf16 v[0:3], v[104:107], v[180:183], v[0:3]\n\t"
        "ds_read_b64_tr_b16 v[100:101], v129 offset:288\n\t"
        "s_cmp_eq_u32 s46, s47\n\t"
        "v_mfma_f32_16x16x32_bf16 v[8:11], v[104:107], v[184:187], v[8:11]\n\t"
        "ds_read_b64_tr_b16 v[102:103], v129 offset:3360\n\t"
        "s_cselect_b32 s46, 0, s46\n\t"
        "v_mfma_f32_16x16x32_bf16 v[4:7], v[108:111], v[180:183], v[4:7]\n\t"
        "ds_read_b64_tr_b16 v[104:105], v130 offset:256\n\t"
        "buffer_load_dwordx4 v[168:171], v131, s[40:43], 0 offen offset:256 sc1\n\t"
        "v_mfma_f32_16x16x32_bf16 v[12:15], v[108:111], v[184:187], v[12:15]\n\t"
        "ds_read_b64_tr_b16 v[106:107], v130 offset:3328\n\t"
        "s_waitcnt lgkmcnt(6)\n\t"
        "v_mfma_f32_16x16x32_bf16 v[16:19], v[112:115], v[180:183], v[16:19]\n\t"
        "ds_read_b64_tr_b16 v[108:109], v130 offset:288\n\t"
        "v_mfma_f32_16x16x32_bf16 v[24:27], v[112:115], v[184:187], v[24:27]\n\t"
        "ds_read_b64_tr_b16 v[110:111], v130 offset:3360\n\t"
        "v_mfma_f32_16x16x32_bf16 v[20:23], v[116:119], v[180:183], v[20:23]\n\t"
        "ds_read_b64_tr_b16 v[112:113], v129 offset:128\n\t"
        "v_mfma_f32_16x16x32_bf16 v[28:31], v[116:119], v[184:187], v[28:31]\n\t"
        "ds_read_b64_tr_b16 v[114:115], v129 offset:3200\n\t"
        "s_waitcnt vmcnt(10) lgkmcnt(6)\n\t"
        "v_mfma_f32_16x16x32_bf16 v[64:67], v[188:191], v[96:99], v[64:67]\n\t"
        "ds_read_b64_tr_b16 v[116:117], v129 offset:160\n\t"
        "v_mfma_f32_16x16x32_bf16 v[72:75], v[192:195], v[96:99], v[72:75]\n\t"
        "ds_read_b64_tr_b16 v[118:119], v129 offset:3232\n\t"
        "v_mfma_f32_16x16x32_bf16 v[68:71], v[188:191], v[100:103], v[68:71]\n\t"
        "ds_read_b64_tr_b16 v[96:97], v130 offset:128\n\t"
        "ds_read_b32 v126, v125\n\t"
        "v_mfma_f32_16x16x32_bf16 v[76:79], v[192:195], v[100:103], v[76:79]\n\t"
        "ds_read_b64_tr_b16 v[98:99], v130 offset:3200\n\t"
        "s_waitcnt lgkmcnt(7)\n\t"
        "v_mfma_f32_16x16x32_bf16 v[80:83], v[188:191], v[104:107], v[80:83]\n\t"
        "ds_read_b64_tr_b16 v[100:101], v130 offset:160\n\t"
        "v_mfma_f32_16x16x32_bf16 v[88:91], v[192:195], v[104:107], v[88:91]\n\t"
        "ds_read_b64_tr_b16 v[102:103], v130 offset:3232\n\t"
        "v_mfma_f32_16x16x32_bf16 v[84:87], v[188:191], v[108:111], v[84:87]\n\t"
        "ds_read_b64_tr_b16 v[104:105], v129 offset:0\n\t"
        "v_add_u32 v123, s46, v121\n\t"
        "v_mfma_f32_16x16x32_bf16 v[92:95], v[192:195], v[108:111], v[92:95]\n\t"
        "ds_read_b64_tr_b16 v[106:107], v129 offset:3072\n\t"
        "s_waitcnt lgkmcnt(5)\n\t"
        "v_mfma_f32_16x16x32_bf16 v[32:35], v[112:115], v[188:191], v[32:35]\n\t"
        "ds_read_b64_tr_b16 v[108:109], v129 offset:32\n\t"
        "v_cmp_gt_u32 vcc, v128, v126\n\t"
        "v_mfma_f32_16x16x32_bf16 v[40:43], v[112:115], v[192:195], v[40:43]\n\t"
        "ds_read_b64_tr_b16 v[110:111], v129 offset:3104\n\t"
        "v_add_u32 v124, s46, v122\n\t"
        "v_mfma_f32_16x16x32_bf16 v[36:39], v[116:119], v[188:191], v[36:39]\n\t"
        "ds_read_b64_tr_b16 v[112:113], v130 offset:0\n\t"
        "buffer_load_dwordx4 v[172:175], v131, s[40:43], 0 offen offset:2048 sc1\n\t"
        "v_mfma_f32_16x16x32_bf16 v[44:47], v[116:119], v[192:195], v[44:47]\n\t"
        "ds_read_b64_tr_b16 v[114:115], v130 offset:3072\n\t"
        "s_waitcnt lgkmcnt(6)\n\t"
        "v_mfma_f32_16x16x32_bf16 v[48:51], v[96:99], v[188:191], v[48:51]\n\t"
        "ds_read_b64_tr_b16 v[116:117], v130 offset:32\n\t"
        "v_mfma_f32_16x16x32_bf16 v[56:59], v[96:99], v[192:195], v[56:59]\n\t"
        "ds_read_b64_tr_b16 v[118:119], v130 offset:3104\n\t"
        "s_cbranch_vccnz 18f\n\t"
        "19:\n\t"
        "v_mfma_f32_16x16x32_bf16 v[52:55], v[100:103], v[188:191], v[52:55]\n\t"
        "ds_read_b64_tr_b16 v[96:97], v123 offset:256\n\t"
        "s_addk_i32 s46, 12352\n\t"
        "v_mfma_f32_16x16x32_bf16 v[60:63], v[100:103], v[192:195], v[60:63]\n\t"
        "ds_read_b64_tr_b16 v[98:99], v123 offset:3328\n\t"
        "s_waitcnt lgkmcnt(6)\n\t"
        "v_mfma_f32_16x16x32_bf16 v[0:3], v[104:107], v[188:191], v[0:3]\n\t"
        "ds_read_b64_tr_b16 v[100:101], v123 offset:288\n\t"
        "s_cmp_eq_u32 s46, s47\n\t"
        "v_mfma_f32_16x16x32_bf16 v[8:11], v[104:107], v[192:195], v[8:11]\n\t"
        "ds_read_b64_tr_b16 v[102:103], v123 offset:3360\n\t"
        "s_cselect_b32 s46, 0, s46\n\t"
        "v_mfma_f32_16x16x32_bf16 v[4:7], v[108:111], v[188:191], v[4:7]\n\t"
        "ds_read_b64_tr_b16 v[104:105], v124 offset:256\n\t"
        "buffer_load_dwordx4 v[176:179], v131, s[40:43], 0 offen offset:2304 sc1\n\t"
        "v_mfma_f32_16x16x32_bf16 v[12:15], v[108:111], v[192:195], v[12:15]\n\t"
        "ds_read_b64_tr_b16 v[106:107], v124 offset:3328\n\t"
        "s_waitcnt lgkmcnt(6)\n\t"
        "v_mfma_f32_16x16x32_bf16 v[16:19], v[112:115], v[188:191], v[16:19]\n\t"
        "ds_write_b32 v127, v128\n\t"
        "ds_read_b64_tr_b16 v[108:109], v124 offset:288\n\t"
        "v_mfma_f32_16x16x32_bf16 v[24:27], v[112:115], v[192:195], v[24:27]\n\t"
        "ds_read_b64_tr_b16 v[110:111], v124 offset:3360\n\t"
        "v_add_u32 v128, 2, v128\n\t"
        "v_mfma_f32_16x16x32_bf16 v[20:23], v[116:119], v[188:191], v[20:23]\n\t"
        "ds_read_b64_tr_b16 v[112:113], v123 offset:128\n\t"
        "v_mfma_f32_16x16x32_bf16 v[28:31], v[116:119], v[192:195], v[28:31]\n\t"
        "ds_read_b64_tr_b16 v[114:115], v123 offset:3200\n\t"
        "s_waitcnt vmcnt(10) lgkmcnt(7)\n\t"
        "v_mfma_f32_16x16x32_bf16 v[64:67], v[132:135], v[96:99], v[64:67]\n\t"
        "ds_read_b64_tr_b16 v[116:117], v123 offset:160\n\t"
        "s_addk_i32 s44, 0x1000\n\t"
        "v_mfma_f32_16x16x32_bf16 v[72:75], v[136:139], v[96:99], v[72:75]\n\t"
        "ds_read_b64_tr_b16 v[118:119], v123 offset:3232\n\t"
        "s_bitset0_b32 s44, 16\n\t"
        "v_mfma_f32_16x16x32_bf16 v[68:71], v[132:135], v[100:103], v[68:71]\n\t"
        "ds_read_b64_tr_b16 v[96:97], v124 offset:128\n\t"
        "s_cmp_eq_u32 s44, s49\n\t"
        "v_mfma_f32_16x16x32_bf16 v[76:79], v[136:139], v[100:103], v[76:79]\n\t"
        "ds_read_b64_tr_b16 v[98:99], v124 offset:3200\n\t"
        "s_waitcnt lgkmcnt(6)\n\t"
        "v_mfma_f32_16x16x32_bf16 v[80:83], v[132:135], v[104:107], v[80:83]\n\t"
        "ds_read_b64_tr_b16 v[100:101], v124 offset:160\n\t"
        "s_cselect_b32 s44, s50, s44\n\t"
        "v_mfma_f32_16x16x32_bf16 v[88:91], v[136:139], v[104:107], v[88:91]\n\t"
        "ds_read_b64_tr_b16 v[102:103], v124 offset:3232\n\t"
        "v_add_u32 v131, s44, v120\n\t"
        "v_mfma_f32_16x16x32_bf16 v[84:87], v[132:135], v[108:111], v[84:87]\n\t"
        "ds_read_b64_tr_b16 v[104:105], v123 offset:0\n\t"
        "v_add_u32 v129, s46, v121\n\t"
        "v_mfma_f32_16x16x32_bf16 v[92:95], v[136:139], v[108:111], v[92:95]\n\t"
        "ds_read_b64_tr_b16 v[106:107], v123 offset:3072\n\t"
        "s_waitcnt lgkmcnt(6)\n\t"
        "v_mfma_f32_16x16x32_bf16 v[32:35], v[112:115], v[132:135], v[32:35]\n\t"
        "ds_read_b64_tr_b16 v[108:109], v123 offset:32\n\t"
        "v_mfma_f32_16x16x32_bf16 v[40:43], v[112:115], v[136:139], v[40:43]\n\t"
        "ds_read_b64_tr_b16 v[110:111], v123 offset:3104\n\t"
        "v_add_u32 v130, s46, v122\n\t"
        "v_mfma_f32_16x16x32_bf16 v[36:39], v[116:119], v[132:135], v[36:39]\n\t"
        "ds_read_b64_tr_b16 v[112:113], v124 offset:0\n\t"
        "buffer_load_dwordx4 v[180:183], v131, s[40:43], 0 offen offset:0 sc1\n\t"
        "v_mfma_f32_16x16x32_bf16 v[44:47], v[116:119], v[136:139], v[44:47]\n\t"
        "ds_read_b64_tr_b16 v[114:115], v124 offset:3072\n\t"
        "s_waitcnt lgkmcnt(6)\n\t"
        "v_mfma_f32_16x16x32_bf16 v[48:51], v[96:99], v[132:135], v[48:51]\n\t"
        "ds_read_b64_tr_b16 v[116:117], v124 offset:32\n\t"
        "v_mfma_f32_16x16x32_bf16 v[56:59], v[96:99], v[136:139], v[56:59]\n\t"
        "ds_read_b64_tr_b16 v[118:119], v124 offset:3104\n\t"
        "v_mfma_f32_16x16x32_bf16 v[52:55], v[100:103], v[132:135], v[52:55]\n\t"
        "ds_read_b64_tr_b16 v[96:97], v129 offset:256\n\t"
        "s_addk_i32 s46, 12352\n\t"
        "v_mfma_f32_16x16x32_bf16 v[60:63], v[100:103], v[136:139], v[60:63]\n\t"
        "ds_read_b64_tr_b16 v[98:99], v129 offset:3328\n\t"
        "s_waitcnt lgkmcnt(6)\n\t"
        "v_mfma_f32_16x16x32_bf16 v[0:3], v[104:107], v[132:135], v[0:3]\n\t"
        "ds_read_b64_tr_b16 v[100:101], v129 offset:288\n\t"
        "s_cmp_eq_u32 s46, s47\n\t"
        "v_mfma_f32_16x16x32_bf16 v[8:11], v[104:107], v[136:139], v[8:11]\n\t"
        "ds_read_b64_tr_b16 v[102:103], v129 offset:3360\n\t"
        "s_cselect_b32 s46, 0, s46\n\t"
        "v_mfma_f32_16x16x32_bf16 v[4:7], v[108:111], v[132:135], v[4:7]\n\t"
        "ds_read_b64_tr_b16 v[104:105], v130 offset:256\n\t"
        "buffer_load_dwordx4 v[184:187], v131, s[40:43], 0 offen offset:256 sc1\n\t"
        "v_mfma_f32_16x16x32_bf16 v[12:15], v[108:111], v[136:139], v[12:15]\n\t"
        "ds_read_b64_tr_b16 v[106:107], v130 offset:3328\n\t"
        "s_waitcnt lgkmcnt(6)\n\t"
        "v_mfma_f32_16x16x32_bf16 v[16:19], v[112:115], v[132:135], v[16:19]\n\t"
        "ds_read_b64_tr_b16 v[108:109], v130 offset:288\n\t"
        "v_mfma_f32_16x16x32_bf16 v[24:27], v[112:115], v[136:139], v[24:27]\n\t"
        "ds_read_b64_tr_b16 v[110:111], v130 offset:3360\n\t"
        "v_mfma_f32_16x16x32_bf16 v[20:23], v[116:119], v[132:135], v[20:23]\n\t"
        "ds_read_b64_tr_b16 v[112:113], v129 offset:128\n\t"
        "v_mfma_f32_16x16x32_bf16 v[28:31], v[116:119], v[136:139], v[28:31]\n\t"
        "ds_read_b64_tr_b16 v[114:115], v129 offset:3200\n\t"
        "s_waitcnt vmcnt(10) lgkmcnt(6)\n\t"
        "v_mfma_f32_16x16x32_bf16 v[64:67], v[140:143], v[96:99], v[64:67]\n\t"
        "ds_read_b64_tr_b16 v[116:117], v129 offset:160\n\t"
        "v_mfma_f32_16x16x32_bf16 v[72:75], v[144:147], v[96:99], v[72:75]\n\t"
        "ds_read_b64_tr_b16 v[118:119], v129 offset:3232\n\t"
        "v_mfma_f32_16x16x32_bf16 v[68:71], v[140:143], v[100:103], v[68:71]\n\t"
        "ds_read_b64_tr_b16 v[96:97], v130 offset:128\n\t"
        "ds_read_b32 v126, v125\n\t"
        "v_mfma_f32_16x16x32_bf16 v[76:79], v[144:147], v[100:103], v[76:79]\n\t"
        "ds_read_b64_tr_b16 v[98:99], v130 offset:3200\n\t"
        "s_waitcnt lgkmcnt(7)\n\t"
        "v_mfma_f32_16x16x32_bf16 v[80:83], v[140:143], v[104:107], v[80:83]\n\t"
        "ds_read_b64_tr_b16 v[100:101], v130 offset:160\n\t"
        "v_mfma_f32_16x16x32_bf16 v[88:91], v[144:147], v[104:107], v[88:91]\n\t"
        "ds_read_b64_tr_b16 v[102:103], v130 offset:3232\n\t"
        "v_mfma_f32_16x16x32_bf16 v[84:87], v[140:143], v[108:111], v[84:87]\n\t"
        "ds_read_b64_tr_b16 v[104:105], v129 offset:0\n\t"
        "v_add_u32 v123, s46, v121\n\t"
        "v_mfma_f32_16x16x32_bf16 v[92:95], v[144:147], v[108:111], v[92:95]\n\t"
        "ds_read_b64_tr_b16 v[106:107], v129 offset:3072\n\t"
        "s_waitcnt lgkmcnt(5)\n\t"
        "v_mfma_f32_16x16x32_bf16 v[32:35], v[112:115], v[140:143], v[32:35]\n\t"
        "ds_read_b64_tr_b16 v[108:109], v129 offset:32\n\t"
        "v_cmp_gt_u32 vcc, v128, v126\n\t"
        "v_mfma_f32_16x16x32_bf16 v[40:43], v[112:115], v[144:147], v[40:43]\n\t"
        "ds_read_b64_tr_b16 v[110:111], v129 offset:3104\n\t"
        "v_add_u32 v124, s46, v122\n\t"
        "v_mfma_f32_16x16x32_bf16 v[36:39], v[116:119], v[140:143], v[36:39]\n\t"
        "ds_read_b64_tr_b16 v[112:113], v130 offset:0\n\t"
        "buffer_load_dwordx4 v[188:191], v131, s[40:43], 0 offen offset:2048 sc1\n\t"
        "v_mfma_f32_16x16x32_bf16 v[44:47], v[116:119], v[144:147], v[44:47]\n\t"
        "ds_read_b64_tr_b16 v[114:115], v130 offset:3072\n\t"
        "s_waitcnt lgkmcnt(6)\n\t"
        "v_mfma_f32_16x16x32_bf16 v[48:51], v[96:99], v[140:143], v[48:51]\n\t"
        "ds_read_b64_tr_b16 v[116:117], v130 offset:32\n\t"
        "v_mfma_f32_16x16x32_bf16 v[56:59], v[96:99], v[144:147], v[56:59]\n\t"
        "ds_read_b64_tr_b16 v[118:119], v130 offset:3104\n\t"
        "s_cbranch_vccnz 20f\n\t"
        "21:\n\t"
        "v_mfma_f32_16x16x32_bf16 v[52:55], v[100:103], v[140:143], v[52:55]\n\t"
        "ds_read_b64_tr_b16 v[96:97], v123 offset:256\n\t"
        "s_addk_i32 s46, 12352\n\t"
        "v_mfma_f32_16x16x32_bf16 v[60:63], v[100:103], v[144:147], v[60:63]\n\t"
        "ds_read_b64_tr_b16 v[98:99], v123 offset:3328\n\t"
        "s_waitcnt lgkmcnt(6)\n\t"
        "v_mfma_f32_16x16x32_bf16 v[0:3], v[104:107], v[140:143], v[0:3]\n\t"
        "ds_read_b64_tr_b16 v[100:101], v123 offset:288\n\t"
        "s_cmp_eq_u32 s46, s47\n\t"
        "v_mfma_f32_16x16x32_bf16 v[8:11], v[104:107], v[144:147], v[8:11]\n\t"
        "ds_read_b64_tr_b16 v[102:103], v123 offset:3360\n\t"
        "s_cselect_b32 s46, 0, s46\n\t"
        "v_mfma_f32_16x16x32_bf16 v[4:7], v[108:111], v[140:143], v[4:7]\n\t"
        "ds_read_b64_tr_b16 v[104:105], v124 offset:256\n\t"
        "buffer_load_dwordx4 v[192:195], v131, s[40:43], 0 offen offset:2304 sc1\n\t"
        "v_mfma_f32_16x16x32_bf16 v[12:15], v[108:111], v[144:147], v[12:15]\n\t"
        "ds_read_b64_tr_b16 v[106:107], v124 offset:3328\n\t"
        "s_waitcnt lgkmcnt(6)\n\t"
        "v_mfma_f32_16x16x32_bf16 v[16:19], v[112:115], v[140:143], v[16:19]\n\t"
        "ds_write_b32 v127, v128\n\t"
        "ds_read_b64_tr_b16 v[108:109], v124 offset:288\n\t"
        "v_mfma_f32_16x16x32_bf16 v[24:27], v[112:115], v[144:147], v[24:27]\n\t"
        "ds_read_b64_tr_b16 v[110:111], v124 offset:3360\n\t"
        "v_add_u32 v128, 2, v128\n\t"
        "v_mfma_f32_16x16x32_bf16 v[20:23], v[116:119], v[140:143], v[20:23]\n\t"
        "ds_read_b64_tr_b16 v[112:113], v123 offset:128\n\t"
        "s_sub_u32 s45, s45, 1\n\t"
        "v_mfma_f32_16x16x32_bf16 v[28:31], v[116:119], v[144:147], v[28:31]\n\t"
        "ds_read_b64_tr_b16 v[114:115], v123 offset:3200\n\t"
        "s_cbranch_scc0 1b\n\t"
        "s_waitcnt vmcnt(10) lgkmcnt(7)\n\t"
        "v_mfma_f32_16x16x32_bf16 v[64:67], v[148:151], v[96:99], v[64:67]\n\t"
        "ds_read_b64_tr_b16 v[116:117], v121 offset:256\n\t"
        "ds_read_b32 v126, v125\n\t"
        "v_mfma_f32_16x16x32_bf16 v[72:75], v[152:155], v[96:99], v[72:75]\n\t"
        "ds_read_b64_tr_b16 v[118:119], v121 offset:3328\n\t"
        "v_add_u32 v128, 2, v128\n\t"
        "v_mfma_f32_16x16x32_bf16 v[68:71], v[148:151], v[100:103], v[68:71]\n\t"
        "ds_read_b64_tr_b16 v[96:97], v121 offset:288\n\t"
        "v_mfma_f32_16x16x32_bf16 v[76:79], v[152:155], v[100:103], v[76:79]\n\t"
        "ds_read_b64_tr_b16 v[98:99], v121 offset:3360\n\t"
        "s_waitcnt lgkmcnt(3)\n\t"
        "v_mfma_f32_16x16x32_bf16 v[80:83], v[148:151], v[104:107], v[80:83]\n\t"
        "ds_read_b64_tr_b16 v[100:101], v122 offset:256\n\t"
        "v_cmp_gt_u32 vcc, v128, v126\n\t"
        "v_mfma_f32_16x16x32_bf16 v[88:91], v[152:155], v[104:107], v[88:91]\n\t"
        "ds_read_b64_tr_b16 v[102:103], v122 offset:3328\n\t"
        "v_mfma_f32_16x16x32_bf16 v[84:87], v[148:151], v[108:111], v[84:87]\n\t"
        "ds_read_b64_tr_b16 v[104:105], v122 offset:288\n\t"
        "v_mfma_f32_16x16x32_bf16 v[92:95], v[152:155], v[108:111], v[92:95]\n\t"
        "ds_read_b64_tr_b16 v[106:107], v122 offset:3360\n\t"
        "s_cbranch_vccnz 38f\n\t"
        "39:\n\t"
        "s_waitcnt vmcnt(8) lgkmcnt(6)\n\t"
        "v_mfma_f32_16x16x32_bf16 v[32:35], v[112:115], v[148:151], v[32:35]\n\t"
        "ds_read_b64_tr_b16 v[108:109], v121 offset:12608\n\t"
        "v_mfma_f32_16x16x32_bf16 v[40:43], v[112:115], v[152:155], v[40:43]\n\t"
        "ds_read_b64_tr_b16 v[110:111], v121 offset:15680\n\t"
        "v_mfma_f32_16x16x32_bf16 v[64:67], v[156:159], v[116:119], v[64:67]\n\t"
        "ds_read_b64_tr_b16 v[112:113], v121 offset:12640\n\t"
        "v_mfma_f32_16x16x32_bf16 v[72:75], v[160:163], v[116:119], v[72:75]\n\t"
        "ds_read_b64_tr_b16 v[114:115], v121 offset:15712\n\t"
        "s_waitcnt lgkmcnt(6)\n\t"
        "v_mfma_f32_16x16x32_bf16 v[68:71], v[156:159], v[96:99], v[68:71]\n\t"
        "ds_read_b64_tr_b16 v[116:117], v122 offset:12608\n\t"
        "ds_read_b128 v[196:199], v215 offset:0\n\t"
        "v_mfma_f32_16x16x32_bf16 v[76:79], v[160:163], v[96:99], v[76:79]\n\t"
        "ds_read_b64_tr_b16 v[118:119], v122 offset:15680\n\t"
        "v_mfma_f32_16x16x32_bf16 v[80:83], v[156:159], v[100:103], v[80:83]\n\t"
        "ds_read_b64_tr_b16 v[96:97], v122 offset:12640\n\t"
        "v_mfma_f32_16x16x32_bf16 v[88:91], v[160:163], v[100:103], v[88:91]\n\t"
        "ds_read_b64_tr_b16 v[98:99], v122 offset:15712\n\t"
        "s_waitcnt vmcnt(6) lgkmcnt(7)\n\t"
        "v_mfma_f32_16x16x32_bf16 v[84:87], v[156:159], v[104:107], v[84:87]\n\t"
        "ds_read_b64_tr_b16 v[100:101], v121 offset:24960\n\t"
        "ds_read_b128 v[200:203], v215 offset:64\n\t"
        "v_mfma_f32_16x16x32_bf16 v[92:95], v[160:163], v[104:107], v[92:95]\n\t"
        "ds_read_b64_tr_b16 v[102:103], v121 offset:28032\n\t"
        "v_mfma_f32_16x16x32_bf16 v[64:67], v[164:167], v[108:111], v[64:67]\n\t"
        "ds_read_b64_tr_b16 v[104:105], v121 offset:24992\n\t"
        "v_mfma_f32_16x16x32_bf16 v[72:75], v[168:171], v[108:111], v[72:75]\n\t"
        "ds_read_b64_tr_b16 v[106:107], v121 offset:28064\n\t"
        "s_waitcnt lgkmcnt(7)\n\t"
        "v_mfma_f32_16x16x32_bf16 v[68:71], v[164:167], v[112:115], v[68:71]\n\t"
        "ds_read_b64_tr_b16 v[108:109], v122 offset:24960\n\t"
        "v_mfma_f32_16x16x32_bf16 v[76:79], v[168:171], v[112:115], v[76:79]\n\t"
        "ds_read_b64_tr_b16 v[110:111], v122 offset:28032\n\t"
        "v_mfma_f32_16x16x32_bf16 v[80:83], v[164:167], v[116:119], v[80:83]\n\t"
        "ds_read_b64_tr_b16 v[112:113], v122 offset:24992\n\t"
        "v_mfma_f32_16x16x32_bf16 v[88:91], v[168:171], v[116:119], v[88:91]\n\t"
        "ds_read_b64_tr_b16 v[114:115], v122 offset:28064\n\t"
        "s_waitcnt vmcnt(4) lgkmcnt(6)\n\t"
        "v_mfma_f32_16x16x32_bf16 v[84:87], v[164:167], v[96:99], v[84:87]\n\t"
        "ds_read_b64_tr_b16 v[116:117], v121 offset:37312\n\t"
        "v_mfma_f32_16x16x32_bf16 v[92:95], v[168:171], v[96:99], v[92:95]\n\t"
        "ds_read_b64_tr_b16 v[118:119], v121 offset:40384\n\t"
        "v_mfma_f32_16x16x32_bf16 v[64:67], v[172:175], v[100:103], v[64:67]\n\t"
        "ds_read_b64_tr_b16 v[96:97], v121 offset:37344\n\t"
        "v_mfma_f32_16x16x32_bf16 v[72:75], v[176:179], v[100:103], v[72:75]\n\t"
        "ds_read_b64_tr_b16 v[98:99], v121 offset:40416\n\t"
        "s_waitcnt lgkmcnt(6)\n\t"
        "v_mfma_f32_16x16x32_bf16 v[68:71], v[172:175], v[104:107], v[68:71]\n\t"
        "ds_read_b64_tr_b16 v[100:101], v122 offset:37312\n\t"
        "v_mfma_f32_16x16x32_bf16 v[76:79], v[176:179], v[104:107], v[76:79]\n\t"
        "ds_read_b64_tr_b16 v[102:103], v122 offset:40384\n\t"
        "v_mfma_f32_16x16x32_bf16 v[80:83], v[172:175], v[108:111], v[80:83]\n\t"
        "ds_read_b64_tr_b16 v[104:105], v122 offset:37344\n\t"
        "v_mfma_f32_16x16x32_bf16 v[88:91], v[176:179], v[108:111], v[88:91]\n\t"
        "ds_read_b64_tr_b16 v[106:107], v122 offset:40416\n\t"
        "s_waitcnt vmcnt(2) lgkmcnt(6)\n\t"
        "v_mfma_f32_16x16x32_bf16 v[84:87], v[172:175], v[112:115], v[84:87]\n\t"
        "ds_read_b64_tr_b16 v[108:109], v121 offset:49664\n\t"
        "v_mfma_f32_16x16x32_bf16 v[92:95], v[176:179], v[112:115], v[92:95]\n\t"
        "ds_read_b64_tr_b16 v[110:111], v121 offset:52736\n\t"
        "v_mfma_f32_16x16x32_bf16 v[64:67], v[180:183], v[116:119], v[64:67]\n\t"
        "ds_read_b64_tr_b16 v[112:113], v121 offset:49696\n\t"
        "v_mfma_f32_16x16x32_bf16 v[72:75], v[184:187], v[116:119], v[72:75]\n\t"
        "ds_read_b64_tr_b16 v[114:115], v121 offset:52768\n\t"
        "s_waitcnt lgkmcnt(6)\n\t"
        "v_mfma_f32_16x16x32_bf16 v[68:71], v[180:183], v[96:99], v[68:71]\n\t"
        "ds_read_b64_tr_b16 v[116:117], v122 offset:49664\n\t"
        "v_mfma_f32_16x16x32_bf16 v[76:79], v[184:187], v[96:99], v[76:79]\n\t"
        "ds_read_b64_tr_b16 v[118:119], v122 offset:52736\n\t"
        "v_mfma_f32_16x16x32_bf16 v[80:83], v[180:183], v[100:103], v[80:83]\n\t"
        "ds_read_b64_tr_b16 v[96:97], v122 offset:49696\n\t"
        "v_mfma_f32_16x16x32_bf16 v[88:91], v[184:187], v[100:103], v[88:91]\n\t"
        "ds_read_b64_tr_b16 v[98:99], v122 offset:52768\n\t"
        "s_waitcnt vmcnt(0) lgkmcnt(6)\n\t"
        "v_mfma_f32_16x16x32_bf16 v[84:87], v[180:183], v[104:107], v[84:87]\n\t"
        "ds_read_b64_tr_b16 v[100:101], v123 offset:160\n\t"
        "v_mfma_f32_16x16x32_bf16 v[92:95], v[184:187], v[104:107], v[92:95]\n\t"
        "ds_read_b64_tr_b16 v[102:103], v123 offset:3232\n\t"
        "v_mfma_f32_16x16x32_bf16 v[64:67], v[188:191], v[108:111], v[64:67]\n\t"
        "ds_read_b64_tr_b16 v[104:105], v124 offset:128\n\t"
        "v_mfma_f32_16x16x32_bf16 v[72:75], v[192:195], v[108:111], v[72:75]\n\t"
        "ds_read_b64_tr_b16 v[106:107], v124 offset:3200\n\t"
        "s_waitcnt lgkmcnt(6)\n\t"
        "v_mfma_f32_16x16x32_bf16 v[68:71], v[188:191], v[112:115], v[68:71]\n\t"
        "ds_read_b64_tr_b16 v[108:109], v124 offset:160\n\t"
        "v_mfma_f32_16x16x32_bf16 v[76:79], v[192:195], v[112:115], v[76:79]\n\t"
        "ds_read_b64_tr_b16 v[110:111], v124 offset:3232\n\t"
        "v_mfma_f32_16x16x32_bf16 v[80:83], v[188:191], v[116:119], v[80:83]\n\t"
        "ds_read_b64_tr_b16 v[112:113], v121 offset:128\n\t"
        "v_mfma_f32_16x16x32_bf16 v[88:91], v[192:195], v[116:119], v[88:91]\n\t"
        "ds_read_b64_tr_b16 v[114:115], v121 offset:3200\n\t"
        "s_waitcnt lgkmcnt(6)\n\t"
        "v_mfma_f32_16x16x32_bf16 v[84:87], v[188:191], v[96:99], v[84:87]\n\t"
        "ds_read_b64_tr_b16 v[116:117], v121 offset:160\n\t"
        "v_mfma_f32_16x16x32_bf16 v[92:95], v[192:195], v[96:99], v[92:95]\n\t"
        "ds_read_b64_tr_b16 v[118:119], v121 offset:3232\n\t"
        "v_mfma_f32_16x16x32_bf16 v[36:39], v[100:103], v[148:151], v[36:39]\n\t"
        "ds_read_b64_tr_b16 v[96:97], v122 offset:128\n\t"
        "v_mfma_f32_16x16x32_bf16 v[44:47], v[100:103], v[152:155], v[44:47]\n\t"
        "ds_read_b64_tr_b16 v[98:99], v122 offset:3200\n\t"
        "s_waitcnt lgkmcnt(6)\n\t"
        "v_mfma_f32_16x16x32_bf16 v[48:51], v[104:107], v[148:151], v[48:51]\n\t"
        "ds_read_b64_tr_b16 v[100:101], v122 offset:160\n\t"
        "v_mul_f32 v64, v196, v64\n\t"
        "v_mul_f32 v65, v197, v65\n\t"
        "v_mfma_f32_16x16x32_bf16 v[56:59], v[104:107], v[152:155], v[56:59]\n\t"
        "ds_read_b64_tr_b16 v[102:103], v122 offset:3232\n\t"
        "v_mul_f32 v66, v198, v66\n\t"
        "v_mul_f32 v67, v199, v67\n\t"
        "v_mfma_f32_16x16x32_bf16 v[52:55], v[108:111], v[148:151], v[52:55]\n\t"
        "ds_read_b64_tr_b16 v[104:105], v121 offset:12480\n\t"
        "v_mul_f32 v68, v196, v68\n\t"
        "v_mul_f32 v69, v197, v69\n\t"
        "v_mul_f32 v70, v198, v70\n\t"
        "v_mfma_f32_16x16x32_bf16 v[60:63], v[108:111], v[152:155], v[60:63]\n\t"
        "ds_read_b64_tr_b16 v[106:107], v121 offset:15552\n\t"
        "v_mul_f32 v71, v199, v71\n\t"
        "v_cvt_pk_bf16_f32 v64, v64, v65\n\t"
        "s_waitcnt lgkmcnt(6)\n\t"
        "v_mfma_f32_16x16x32_bf16 v[32:35], v[112:115], v[156:159], v[32:35]\n\t"
        "ds_read_b64_tr_b16 v[108:109], v121 offset:12512\n\t"
        "v_cvt_pk_bf16_f32 v65, v66, v67\n\t"
        "v_cvt_pk_bf16_f32 v66, v68, v69\n\t"
        "v_mfma_f32_16x16x32_bf16 v[40:43], v[112:115], v[160:163], v[40:43]\n\t"
        "ds_read_b64_tr_b16 v[110:111], v121 offset:15584\n\t"
        "v_cvt_pk_bf16_f32 v67, v70, v71\n\t"
        "v_mul_f32 v72, v200, v72\n\t"
        "v_mfma_f32_16x16x32_bf16 v[36:39], v[116:119], v[156:159], v[36:39]\n\t"
        "ds_read_b64_tr_b16 v[112:113], v122 offset:12480\n\t"
        "v_mul_f32 v73, v201, v73\n\t"
        "v_mul_f32 v74, v202, v74\n\t"
        "v_mul_f32 v75, v203, v75\n\t"
        "v_mfma_f32_16x16x32_bf16 v[44:47], v[116:119], v[160:163], v[44:47]\n\t"
        "ds_read_b64_tr_b16 v[114:115], v122 offset:15552\n\t"
        "v_mul_f32 v76, v200, v76\n\t"
        "v_mul_f32 v77, v201, v77\n\t"
        "s_waitcnt lgkmcnt(6)\n\t"
        "v_mfma_f32_16x16x32_bf16 v[48:51], v[96:99], v[156:159], v[48:51]\n\t"
        "ds_read_b64_tr_b16 v[116:117], v122 offset:12512\n\t"
        "v_mul_f32 v78, v202, v78\n\t"
        "v_mul_f32 v79, v203, v79\n\t"
        "v_mfma_f32_16x16x32_bf16 v[56:59], v[96:99], v[160:163], v[56:59]\n\t"
        "ds_read_b64_tr_b16 v[118:119], v122 offset:15584\n\t"
        "v_cvt_pk_bf16_f32 v68, v72, v73\n\t"
        "v_cvt_pk_bf16_f32 v69, v74, v75\n\t"
        "v_mfma_f32_16x16x32_bf16 v[52:55], v[100:103], v[156:159], v[52:55]\n\t"
        "ds_read_b64_tr_b16 v[96:97], v121 offset:24832\n\t"
        "v_cvt_pk_bf16_f32 v70, v76, v77\n\t"
        "v_cvt_pk_bf16_f32 v71, v78, v79\n\t"
        "v_permlane16_swap_b32 v64, v66\n\t"
        "v_mfma_f32_16x16x32_bf16 v[60:63], v[100:103], v[160:163], v[60:63]\n\t"
        "ds_read_b64_tr_b16 v[98:99], v121 offset:27904\n\t"
        "v_permlane16_swap_b32 v65, v67\n\t"
        "v_permlane16_swap_b32 v68, v70\n\t"
        "s_waitcnt lgkmcnt(6)\n\t"
        "v_mfma_f32_16x16x32_bf16 v[32:35], v[104:107], v[164:167], v[32:35]\n\t"
        "ds_read_b64_tr_b16 v[100:101], v121 offset:24864\n\t"
        "v_permlane16_swap_b32 v69, v71\n\t"
        "buffer_store_dwordx4 v[64:67], v213, s[40:43], 0 offen offset:2048 sc1\n\t"
        "v_mfma_f32_16x16x32_bf16 v[40:43], v[104:107], v[168:171], v[40:43]\n\t"
        "ds_read_b64_tr_b16 v[102:103], v121 offset:27936\n\t"
        "buffer_store_dwordx4 v[68:71], v213, s[40:43], 0 offen offset:3072 sc1\n\t"
        "v_mul_f32 v80, v196, v80\n\t"
        "v_mfma_f32_16x16x32_bf16 v[36:39], v[108:111], v[164:167], v[36:39]\n\t"
        "ds_read_b64_tr_b16 v[104:105], v122 offset:24832\n\t"
        "v_mul_f32 v81, v197, v81\n\t"
        "v_mul_f32 v82, v198, v82\n\t"
        "v_mul_f32 v83, v199, v83\n\t"
        "v_mfma_f32_16x16x32_bf16 v[44:47], v[108:111], v[168:171], v[44:47]\n\t"
        "ds_read_b64_tr_b16 v[106:107], v122 offset:27904\n\t"
        "v_mul_f32 v84, v196, v84\n\t"
        "v_mul_f32 v85, v197, v85\n\t"
        "s_waitcnt lgkmcnt(6)\n\t"
        "v_mfma_f32_16x16x32_bf16 v[48:51], v[112:115], v[164:167], v[48:51]\n\t"
        "ds_read_b64_tr_b16 v[108:109], v122 offset:24864\n\t"
        "v_mul_f32 v86, v198, v86\n\t"
        "v_mul_f32 v87, v199, v87\n\t"
        "v_mfma_f32_16x16x32_bf16 v[56:59], v[112:115], v[168:171], v[56:59]\n\t"
        "ds_read_b64_tr_b16 v[110:111], v122 offset:27936\n\t"
        "v_cvt_pk_bf16_f32 v80, v80, v81\n\t"
        "v_cvt_pk_bf16_f32 v81, v82, v83\n\t"
        "v_mfma_f32_16x16x32_bf16 v[52:55], v[116:119], v[164:167], v[52:55]\n\t"
        "ds_read_b64_tr_b16 v[112:113], v121 offset:37184\n\t"
        "v_cvt_pk_bf16_f32 v82, v84, v85\n\t"
        "v_cvt_pk_bf16_f32 v83, v86, v87\n\t"
        "v_mul_f32 v88, v200, v88\n\t"
        "v_mfma_f32_16x16x32_bf16 v[60:63], v[116:119], v[168:171], v[60:63]\n\t"
        "ds_read_b64_tr_b16 v[114:115], v121 offset:40256\n\t"
        "v_mul_f32 v89, v201, v89\n\t"
        "v_mul_f32 v90, v202, v90\n\t"
        "s_waitcnt lgkmcnt(6)\n\t"
        "v_mfma_f32_16x16x32_bf16 v[32:35], v[96:99], v[172:175], v[32:35]\n\t"
        "ds_read_b64_tr_b16 v[116:117], v121 offset:37216\n\t"
        "v_mul_f32 v91, v203, v91\n\t"
        "v_mul_f32 v92, v200, v92\n\t"
        "v_mfma_f32_16x16x32_bf16 v[40:43], v[96:99], v[176:179], v[40:43]\n\t"
        "ds_read_b64_tr_b16 v[118:119], v121 offset:40288\n\t"
        "v_mul_f32 v93, v201, v93\n\t"
        "v_mul_f32 v94, v202, v94\n\t"
        "v_mfma_f32_16x16x32_bf16 v[36:39], v[100:103], v[172:175], v[36:39]\n\t"
        "ds_read_b64_tr_b16 v[96:97], v122 offset:37184\n\t"
        "v_mul_f32 v95, v203, v95\n\t"
        "v_cvt_pk_bf16_f32 v84, v88, v89\n\t"
        "v_cvt_pk_bf16_f32 v85, v90, v91\n\t"
        "v_mfma_f32_16x16x32_bf16 v[44:47], v[100:103], v[176:179], v[44:47]\n\t"
        "ds_read_b64_tr_b16 v[98:99], v122 offset:40256\n\t"
        "v_cvt_pk_bf16_f32 v86, v92, v93\n\t"
        "v_cvt_pk_bf16_f32 v87, v94, v95\n\t"
        "s_waitcnt lgkmcnt(6)\n\t"
        "v_mfma_f32_16x16x32_bf16 v[48:51], v[104:107], v[172:175], v[48:51]\n\t"
        "ds_read_b64_tr_b16 v[100:101], v122 offset:37216\n\t"
        "v_permlane16_swap_b32 v80, v82\n\t"
        "v_permlane16_swap_b32 v81, v83\n\t"
        "v_mfma_f32_16x16x32_bf16 v[56:59], v[104:107], v[176:179], v[56:59]\n\t"
        "ds_read_b64_tr_b16 v[102:103], v122 offset:40288\n\t"
        "v_permlane16_swap_b32 v84, v86\n\t"
        "v_permlane16_swap_b32 v85, v87\n\t"
        "v_mfma_f32_16x16x32_bf16 v[52:55], v[108:111], v[172:175], v[52:55]\n\t"
        "ds_read_b64_tr_b16 v[104:105], v121 offset:49536\n\t"
        "buffer_store_dwordx4 v[80:83], v214, s[40:43], 0 offen offset:2048 sc1\n\t"
        "buffer_store_dwordx4 v[84:87], v214, s[40:43], 0 offen offset:3072 sc1\n\t"
        "ds_write_b128 v212, v[80:83] offset:2048\n\t"
        "v_mfma_f32_16x16x32_bf16 v[60:63], v[108:111], v[176:179], v[60:63]\n\t"
        "ds_read_b64_tr_b16 v[106:107], v121 offset:52608\n\t"
        "ds_write_b128 v212, v[84:87] offset:3072\n\t"
        "s_waitcnt lgkmcnt(8)\n\t"
        "v_mfma_f32_16x16x32_bf16 v[32:35], v[112:115], v[180:183], v[32:35]\n\t"
        "ds_read_b64_tr_b16 v[108:109], v121 offset:49568\n\t"
        "v_mfma_f32_16x16x32_bf16 v[40:43], v[112:115], v[184:187], v[40:43]\n\t"
        "ds_read_b64_tr_b16 v[110:111], v121 offset:52640\n\t"
        "v_mfma_f32_16x16x32_bf16 v[36:39], v[116:119], v[180:183], v[36:39]\n\t"
        "ds_read_b64_tr_b16 v[112:113], v122 offset:49536\n\t"
        "v_mfma_f32_16x16x32_bf16 v[44:47], v[116:119], v[184:187], v[44:47]\n\t"
        "ds_read_b64_tr_b16 v[114:115], v122 offset:52608\n\t"
        "s_waitcnt lgkmcnt(8)\n\t"
        "v_mfma_f32_16x16x32_bf16 v[48:51], v[96:99], v[180:183], v[48:51]\n\t"
        "ds_read_b64_tr_b16 v[116:117], v122 offset:49568\n\t"
        "v_mfma_f32_16x16x32_bf16 v[56:59], v[96:99], v[184:187], v[56:59]\n\t"
        "ds_read_b64_tr_b16 v[118:119], v122 offset:52640\n\t"
        "v_mfma_f32_16x16x32_bf16 v[52:55], v[100:103], v[180:183], v[52:55]\n\t"
        "ds_read_b64_tr_b16 v[96:97], v123 offset:0\n\t"
        "v_mfma_f32_16x16x32_bf16 v[60:63], v[100:103], v[184:187], v[60:63]\n\t"
        "ds_read_b64_tr_b16 v[98:99], v123 offset:3072\n\t"
        "s_waitcnt lgkmcnt(6)\n\t"
        "v_mfma_f32_16x16x32_bf16 v[32:35], v[104:107], v[188:191], v[32:35]\n\t"
        "ds_read_b64_tr_b16 v[100:101], v123 offset:32\n\t"
        "v_mfma_f32_16x16x32_bf16 v[40:43], v[104:107], v[192:195], v[40:43]\n\t"
        "ds_read_b64_tr_b16 v[102:103], v123 offset:3104\n\t"
        "v_mfma_f32_16x16x32_bf16 v[36:39], v[108:111], v[188:191], v[36:39]\n\t"
        "ds_read_b64_tr_b16 v[104:105], v124 offset:0\n\t"
        "v_mfma_f32_16x16x32_bf16 v[44:47], v[108:111], v[192:195], v[44:47]\n\t"
        "ds_read_b64_tr_b16 v[106:107], v124 offset:3072\n\t"
        "s_waitcnt lgkmcnt(6)\n\t"
        "v_mfma_f32_16x16x32_bf16 v[48:51], v[112:115], v[188:191], v[48:51]\n\t"
        "ds_read_b64_tr_b16 v[108:109], v124 offset:32\n\t"
        "v_mfma_f32_16x16x32_bf16 v[56:59], v[112:115], v[192:195], v[56:59]\n\t"
        "ds_read_b64_tr_b16 v[110:111], v124 offset:3104\n\t"
        "v_mfma_f32_16x16x32_bf16 v[52:55], v[116:119], v[188:191], v[52:55]\n\t"
        "ds_read_b64_tr_b16 v[112:113], v121 offset:0\n\t"
        "v_mfma_f32_16x16x32_bf16 v[60:63], v[116:119], v[192:195], v[60:63]\n\t"
        "ds_read_b64_tr_b16 v[114:115], v121 offset:3072\n\t"
        "s_waitcnt lgkmcnt(6)\n\t"
        "v_mfma_f32_16x16x32_bf16 v[0:3], v[96:99], v[148:151], v[0:3]\n\t"
        "ds_read_b64_tr_b16 v[116:117], v121 offset:32\n\t"
        "v_mfma_f32_16x16x32_bf16 v[8:11], v[96:99], v[152:155], v[8:11]\n\t"
        "ds_read_b64_tr_b16 v[118:119], v121 offset:3104\n\t"
        "v_mfma_f32_16x16x32_bf16 v[4:7], v[100:103], v[148:151], v[4:7]\n\t"
        "ds_read_b64_tr_b16 v[96:97], v122 offset:0\n\t"
        "v_mul_f32 v32, v210, v32\n\t"
        "v_mul_f32 v33, v210, v33\n\t"
        "v_mul_f32 v34, v210, v34\n\t"
        "v_mfma_f32_16x16x32_bf16 v[12:15], v[100:103], v[152:155], v[12:15]\n\t"
        "ds_read_b64_tr_b16 v[98:99], v122 offset:3072\n\t"
        "v_mul_f32 v35, v210, v35\n\t"
        "v_mul_f32 v36, v210, v36\n\t"
        "s_waitcnt lgkmcnt(6)\n\t"
        "v_mfma_f32_16x16x32_bf16 v[16:19], v[104:107], v[148:151], v[16:19]\n\t"
        "ds_read_b64_tr_b16 v[100:101], v122 offset:32\n\t"
        "v_mul_f32 v37, v210, v37\n\t"
        "v_mul_f32 v38, v210, v38\n\t"
        "v_mfma_f32_16x16x32_bf16 v[24:27], v[104:107], v[152:155], v[24:27]\n\t"
        "ds_read_b64_tr_b16 v[102:103], v122 offset:3104\n\t"
        "v_mul_f32 v39, v210, v39\n\t"
        "v_cvt_pk_bf16_f32 v32, v32, v33\n\t"
        "v_mfma_f32_16x16x32_bf16 v[20:23], v[108:111], v[148:151], v[20:23]\n\t"
        "ds_read_b64_tr_b16 v[104:105], v121 offset:12352\n\t"
        "v_cvt_pk_bf16_f32 v33, v34, v35\n\t"
        "v_cvt_pk_bf16_f32 v34, v36, v37\n\t"
        "v_cvt_pk_bf16_f32 v35, v38, v39\n\t"
        "v_mfma_f32_16x16x32_bf16 v[28:31], v[108:111], v[152:155], v[28:31]\n\t"
        "ds_read_b64_tr_b16 v[106:107], v121 offset:15424\n\t"
        "v_mul_f32 v40, v211, v40\n\t"
        "v_mul_f32 v41, v211, v41\n\t"
        "s_waitcnt lgkmcnt(6)\n\t"
        "v_mfma_f32_16x16x32_bf16 v[0:3], v[112:115], v[156:159], v[0:3]\n\t"
        "ds_read_b64_tr_b16 v[108:109], v121 offset:12384\n\t"
        "v_mul_f32 v42, v211, v42\n\t"
        "v_mul_f32 v43, v211, v43\n\t"
        "v_mfma_f32_16x16x32_bf16 v[8:11], v[112:115], v[160:163], v[8:11]\n\t"
        "ds_read_b64_tr_b16 v[110:111], v121 offset:15456\n\t"
        "v_mul_f32 v44, v211, v44\n\t"
        "v_mul_f32 v45, v211, v45\n\t"
        "v_mfma_f32_16x16x32_bf16 v[4:7], v[116:119], v[156:159], v[4:7]\n\t"
        "ds_read_b64_tr_b16 v[112:113], v122 offset:12352\n\t"
        "v_mul_f32 v46, v211, v46\n\t"
        "v_mul_f32 v47, v211, v47\n\t"
        "v_cvt_pk_bf16_f32 v36, v40, v41\n\t"
        "v_mfma_f32_16x16x32_bf16 v[12:15], v[116:119], v[160:163], v[12:15]\n\t"
        "ds_read_b64_tr_b16 v[114:115], v122 offset:15424\n\t"
        "v_cvt_pk_bf16_f32 v37, v42, v43\n\t"
        "v_cvt_pk_bf16_f32 v38, v44, v45\n\t"
        "s_waitcnt lgkmcnt(6)\n\t"
        "v_mfma_f32_16x16x32_bf16 v[16:19], v[96:99], v[156:159], v[16:19]\n\t"
        "ds_read_b64_tr_b16 v[116:117], v122 offset:12384\n\t"
        "v_cvt_pk_bf16_f32 v39, v46, v47\n\t"
        "v_permlane16_swap_b32 v32, v36\n\t"
        "v_mfma_f32_16x16x32_bf16 v[24:27], v[96:99], v[160:163], v[24:27]\n\t"
        "ds_read_b64_tr_b16 v[118:119], v122 offset:15456\n\t"
        "v_permlane16_swap_b32 v33, v37\n\t"
        "v_permlane16_swap_b32 v34, v38\n\t"
        "v_mfma_f32_16x16x32_bf16 v[20:23], v[100:103], v[156:159], v[20:23]\n\t"
        "ds_read_b64_tr_b16 v[96:97], v121 offset:24704\n\t"
        "v_permlane16_swap_b32 v35, v39\n\t"
        "buffer_store_dwordx4 v[32:35], v213, s[40:43], 0 offen offset:0 sc1\n\t"
        "buffer_store_dwordx4 v[36:39], v213, s[40:43], 0 offen offset:1024 sc1\n\t"
        "v_mfma_f32_16x16x32_bf16 v[28:31], v[100:103], v[160:163], v[28:31]\n\t"
        "ds_read_b64_tr_b16 v[98:99], v121 offset:27776\n\t"
        "v_mul_f32 v48, v210, v48\n\t"
        "v_mul_f32 v49, v210, v49\n\t"
        "s_waitcnt lgkmcnt(6)\n\t"
        "v_mfma_f32_16x16x32_bf16 v[0:3], v[104:107], v[164:167], v[0:3]\n\t"
        "ds_read_b64_tr_b16 v[100:101], v121 offset:24736\n\t"
        "v_mul_f32 v50, v210, v50\n\t"
        "v_mul_f32 v51, v210, v51\n\t"
        "v_mfma_f32_16x16x32_bf16 v[8:11], v[104:107], v[168:171], v[8:11]\n\t"
        "ds_read_b64_tr_b16 v[102:103], v121 offset:27808\n\t"
        "v_mul_f32 v52, v210, v52\n\t"
        "v_mul_f32 v53, v210, v53\n\t"
        "v_mfma_f32_16x16x32_bf16 v[4:7], v[108:111], v[164:167], v[4:7]\n\t"
        "ds_read_b64_tr_b16 v[104:105], v122 offset:24704\n\t"
        "v_mul_f32 v54, v210, v54\n\t"
        "v_mul_f32 v55, v210, v55\n\t"
        "v_cvt_pk_bf16_f32 v48, v48, v49\n\t"
        "v_mfma_f32_16x16x32_bf16 v[12:15], v[108:111], v[168:171], v[12:15]\n\t"
        "ds_read_b64_tr_b16 v[106:107], v122 offset:27776\n\t"
        "v_cvt_pk_bf16_f32 v49, v50, v51\n\t"
        "v_cvt_pk_bf16_f32 v50, v52, v53\n\t"
        "s_waitcnt lgkmcnt(6)\n\t"
        "v_mfma_f32_16x16x32_bf16 v[16:19], v[112:115], v[164:167], v[16:19]\n\t"
        "ds_read_b64_tr_b16 v[108:109], v122 offset:24736\n\t"
        "v_cvt_pk_bf16_f32 v51, v54, v55\n\t"
        "v_mul_f32 v56, v211, v56\n\t"
        "v_mfma_f32_16x16x32_bf16 v[24:27], v[112:115], v[168:171], v[24:27]\n\t"
        "ds_read_b64_tr_b16 v[110:111], v122 offset:27808\n\t"
        "v_mul_f32 v57, v211, v57\n\t"
        "v_mul_f32 v58, v211, v58\n\t"
        "v_mfma_f32_16x16x32_bf16 v[20:23], v[116:119], v[164:167], v[20:23]\n\t"
        "ds_read_b64_tr_b16 v[112:113], v121 offset:37056\n\t"
        "v_mul_f32 v59, v211, v59\n\t"
        "v_mul_f32 v60, v211, v60\n\t"
        "v_mul_f32 v61, v211, v61\n\t"
        "v_mfma_f32_16x16x32_bf16 v[28:31], v[116:119], v[168:171], v[28:31]\n\t"
        "ds_read_b64_tr_b16 v[114:115], v121 offset:40128\n\t"
        "v_mul_f32 v62, v211, v62\n\t"
        "v_mul_f32 v63, v211, v63\n\t"
        "s_waitcnt lgkmcnt(6)\n\t"
        "v_mfma_f32_16x16x32_bf16 v[0:3], v[96:99], v[172:175], v[0:3]\n\t"
        "ds_read_b64_tr_b16 v[116:117], v121 offset:37088\n\t"
        "v_cvt_pk_bf16_f32 v52, v56, v57\n\t"
        "v_cvt_pk_bf16_f32 v53, v58, v59\n\t"
        "v_mfma_f32_16x16x32_bf16 v[8:11], v[96:99], v[176:179], v[8:11]\n\t"
        "ds_read_b64_tr_b16 v[118:119], v121 offset:40160\n\t"
        "v_cvt_pk_bf16_f32 v54, v60, v61\n\t"
        "v_cvt_pk_bf16_f32 v55, v62, v63\n\t"
        "v_mfma_f32_16x16x32_bf16 v[4:7], v[100:103], v[172:175], v[4:7]\n\t"
        "ds_read_b64_tr_b16 v[96:97], v122 offset:37056\n\t"
        "v_permlane16_swap_b32 v48, v52\n\t"
        "v_permlane16_swap_b32 v49, v53\n\t"
        "v_permlane16_swap_b32 v50, v54\n\t"
        "v_mfma_f32_16x16x32_bf16 v[12:15], v[100:103], v[176:179], v[12:15]\n\t"
        "ds_read_b64_tr_b16 v[98:99], v122 offset:40128\n\t"
        "v_permlane16_swap_b32 v51, v55\n\t"
        "buffer_store_dwordx4 v[48:51], v214, s[40:43], 0 offen offset:0 sc1\n\t"
        "s_waitcnt lgkmcnt(6)\n\t"
        "v_mfma_f32_16x16x32_bf16 v[16:19], v[104:107], v[172:175], v[16:19]\n\t"
        "ds_read_b64_tr_b16 v[100:101], v122 offset:37088\n\t"
        "buffer_store_dwordx4 v[52:55], v214, s[40:43], 0 offen offset:1024 sc1\n\t"
        "ds_write_b128 v212, v[48:51] offset:0\n\t"
        "v_mfma_f32_16x16x32_bf16 v[24:27], v[104:107], v[176:179], v[24:27]\n\t"
        "ds_read_b64_tr_b16 v[102:103], v122 offset:40160\n\t"
        "ds_write_b128 v212, v[52:55] offset:1024\n\t"
        "v_mfma_f32_16x16x32_bf16 v[20:23], v[108:111], v[172:175], v[20:23]\n\t"
        "ds_read_b64_tr_b16 v[104:105], v121 offset:49408\n\t"
        "v_mfma_f32_16x16x32_bf16 v[28:31], v[108:111], v[176:179], v[28:31]\n\t"
        "ds_read_b64_tr_b16 v[106:107], v121 offset:52480\n\t"
        "s_waitcnt lgkmcnt(8)\n\t"
        "v_mfma_f32_16x16x32_bf16 v[0:3], v[112:115], v[180:183], v[0:3]\n\t"
        "ds_read_b64_tr_b16 v[108:109], v121 offset:49440\n\t"
        "v_mfma_f32_16x16x32_bf16 v[8:11], v[112:115], v[184:187], v[8:11]\n\t"
        "ds_read_b64_tr_b16 v[110:111], v121 offset:52512\n\t"
        "v_mfma_f32_16x16x32_bf16 v[4:7], v[116:119], v[180:183], v[4:7]\n\t"
        "ds_read_b64_tr_b16 v[112:113], v122 offset:49408\n\t"
        "v_mfma_f32_16x16x32_bf16 v[12:15], v[116:119], v[184:187], v[12:15]\n\t"
        "ds_read_b64_tr_b16 v[114:115], v122 offset:52480\n\t"
        "s_waitcnt lgkmcnt(7)\n\t"
        "v_mfma_f32_16x16x32_bf16 v[16:19], v[96:99], v[180:183], v[16:19]\n\t"
        "ds_read_b64_tr_b16 v[116:117], v122 offset:49440\n\t"
        "v_mfma_f32_16x16x32_bf16 v[24:27], v[96:99], v[184:187], v[24:27]\n\t"
        "ds_read_b64_tr_b16 v[118:119], v122 offset:52512\n\t"
        "v_mfma_f32_16x16x32_bf16 v[20:23], v[100:103], v[180:183], v[20:23]\n\t"
        "v_mfma_f32_16x16x32_bf16 v[28:31], v[100:103], v[184:187], v[28:31]\n\t"
        "s_waitcnt lgkmcnt(4)\n\t"
        "v_mfma_f32_16x16x32_bf16 v[0:3], v[104:107], v[188:191], v[0:3]\n\t"
        "v_mfma_f32_16x16x32_bf16 v[8:11], v[104:107], v[192:195], v[8:11]\n\t"
        "v_mfma_f32_16x16x32_bf16 v[4:7], v[108:111], v[188:191], v[4:7]\n\t"
        "v_mfma_f32_16x16x32_bf16 v[12:15], v[108:111], v[192:195], v[12:15]\n\t"
        "s_waitcnt lgkmcnt(0)\n\t"
        "v_mfma_f32_16x16x32_bf16 v[16:19], v[112:115], v[188:191], v[16:19]\n\t"
        "v_mfma_f32_16x16x32_bf16 v[24:27], v[112:115], v[192:195], v[24:27]\n\t"
        "v_mfma_f32_16x16x32_bf16 v[20:23], v[116:119], v[188:191], v[20:23]\n\t"
        "v_mfma_f32_16x16x32_bf16 v[28:31], v[116:119], v[192:195], v[28:31]\n\t"
        "s_nop 7\n\t"
        "s_branch 9f\n\t"
        "12:\n\t"
        "s_mov_b32 s48, 0x10000\n\t"
        "8:\n\t"
        "ds_read_b32 v126, v125\n\t"
        "s_sub_u32 s48, s48, 1\n\t"
        "s_waitcnt lgkmcnt(0)\n\t"
        "s_cbranch_scc1 13b\n\t"
        "v_cmp_gt_u32 vcc, v128, v126\n\t"
        "s_cbranch_vccnz 8b\n\t"
        "s_branch 13b\n\t"
        "14:\n\t"
        "s_mov_b32 s48, 0x10000\n\t"
        "8:\n\t"
        "ds_read_b32 v126, v125\n\t"
        "s_sub_u32 s48, s48, 1\n\t"
        "s_waitcnt lgkmcnt(0)\n\t"
        "s_cbranch_scc1 15b\n\t"
        "v_cmp_gt_u32 vcc, v128, v126\n\t"
        "s_cbranch_vccnz 8b\n\t"
        "s_branch 15b\n\t"
        "16:\n\t"
        "s_mov_b32 s48, 0x10000\n\t"
        "8:\n\t"
        "ds_read_b32 v126, v125\n\t"
        "s_sub_u32 s48, s48, 1\n\t"
        "s_waitcnt lgkmcnt(0)\n\t"
        "s_cbranch_scc1 17b\n\t"
        "v_cmp_gt_u32 vcc, v128, v126\n\t"
        "s_cbranch_vccnz 8b\n\t"
        "s_branch 17b\n\t"
        "18:\n\t"
        "s_mov_b32 s48, 0x10000\n\t"
        "8:\n\t"
        "ds_read_b32 v126, v125\n\t"
        "s_sub_u32 s48, s48, 1\n\t"
        "s_waitcnt lgkmcnt(0)\n\t"
        "s_cbranch_scc1 19b\n\t"
        "v_cmp_gt_u32 vcc, v128, v126\n\t"
        "s_cbranch_vccnz 8b\n\t"
        "s_branch 19b\n\t"
        "20:\n\t"
        "s_mov_b32 s48, 0x10000\n\t"
        "8:\n\t"
        "ds_read_b32 v126, v125\n\t"
        "s_sub_u32 s48, s48, 1\n\t"
        "s_waitcnt lgkmcnt(0)\n\t"
        "s_cbranch_scc1 21b\n\t"
        "v_cmp_gt_u32 vcc, v128, v126\n\t"
        "s_cbranch_vccnz 8b\n\t"
        "s_branch 21b\n\t"
        "38:\n\t"
        "s_mov_b32 s48, 0x10000\n\t"
        "8:\n\t"
        "ds_read_b32 v126, v125\n\t"
        "s_sub_u32 s48, s48, 1\n\t"
        "s_waitcnt lgkmcnt(0)\n\t"
        "s_cbranch_scc1 39b\n\t"
        "v_cmp_gt_u32 vcc, v128, v126\n\t"
        "s_cbranch_vccnz 8b\n\t"
        "s_branch 39b\n\t"
        "9:\n\t"
        : "+{v[0:15]}"(acc[0]), "+{v[16:31]}"(acc[1]), "+{v[32:47]}"(acc[2]), "+{v[48:63]}"(acc[3]), "+{v[64:79]}"(acc[4]), "+{v[80:95]}"(acc[5]),
          "+{v[96:111]}"(stA), "+{v[112:127]}"(stB), "+{v[128:131]}"(stC), "+{v[148:163]}"(xr1), "+{v[164:179]}"(xr2), "+{v[180:195]}"(xr3),
          "+{s44}"(soff), "+{s46}"(slot), "+{v[140:147]}"(xrb)
        : "{s[40:43]}"(srd), KSKIP, "{v213}"(ep[0]), "{v214}"(ep[1]), "{v215}"(ep[2]), "{v212}"(ep[3]), "{v[210:211]}"(al2) : "v196", "v197", "v198", "v199", "v200", "v201", "v202", "v203", "v132", "v133", "v134", "v135", "v136", "v137", "v138", "v139", "s45", "s47", "s48", "vcc", "scc", "memory");
      STAMPV(4 + 16 * L);
      PHASE_VARS;
      u32x4 kv[4][2];
#pragma unroll
      for (int j = 0; j < 4; ++j)
#pragma unroll
        for (int e = 0; e < 8; ++e) kv[j][e >> 2][e & 3] = __float_as_uint(acc[2 + j][e]);
      u32x4 qo[2][2];
      auto drained = [&]() { asm volatile("s_waitcnt vmcnt(0)\n\tds_write_b32 %0, %1" :: "v"(lbase + L_W + 80 + 4 * wid), "v"(2 * L + 1) : "memory"); };
      if (L == 0) {
#pragma unroll
        for (int e = 0; e < 16; ++e) {
          if (e == 8) { SBAR(); drained(); SBAR(); }
          const unsigned w = pack2(acc[e >> 3][2 * (e & 7)], acc[e >> 3][2 * (e & 7) + 1]);
          qo[e >> 3][(e >> 2) & 1][e & 3] = pack2(lo16(w) * QS, hi16(w) * QS);
        }
        row_swap(qo[0][0], qo[0][1]); row_swap(qo[1][0], qo[1][1]);
      } else {
        lds_wait_ge<0>(lbase + L_W + 4 + 4 * (lane & 1), 1);
        const unsigned idxv = words[3];
        const f32x2 aq = al2 * QS;
#pragma unroll
        for (int e = 0; e < 16; ++e) qo[e >> 3][(e >> 2) & 1][e & 3] = pack2(acc[e >> 3][2 * (e & 7)] * aq[(e >> 2) & 1], acc[e >> 3][2 * (e & 7) + 1] * aq[(e >> 2) & 1]);
        row_swap(qo[0][0], qo[0][1]); row_swap(qo[1][0], qo[1][1]);
        SBAR();
        const int idx = (int)__builtin_amdgcn_readfirstlane(idxv);
        if ((idx >> 5) == tile) {
          const int rel = (idx & 31) - 8 * hi, dfo = ((r32 >> 2) & 1) * 16 + ((r32 >> 3) & 1) * 8 + (r32 >> 4) * 4 + (r32 & 3);
          unsigned pv[2];
#pragma unroll
          for (int j = 0; j < 2; ++j) pv[j] = reinterpret_cast<const u16*>(smem + L_PATCH)[128 + (j ^ hh) * 32 + dfo] * 0x10001u;
          DRAIN();
          if (r32 == (idx & 31)) {
#pragma unroll
            for (int i = 0; i < 4; ++i) {
              const char* p = smem + L_PATCH + ((i >> 1) ^ hh) * 64 + (i & 1) * 32 + hi * 16;
              qo[i >> 1][i & 1] = *reinterpret_cast<const u32x4*>(p);
              kv[i >> 1][i & 1] = *reinterpret_cast<const u32x4*>(p + 128);
              x1p[i] = *reinterpret_cast<const u32x4*>(smem + L_X2P + (i * 2 + hi) * 16);
            }
            reinterpret_cast<float*>(smem + L_AB)[256 + wid * 64 + lane] = hi ? 1.0f : 0.0f;
#pragma unroll
            for (int i = 0; i < 4; ++i) stg(rs, ep[i >> 1] + (i & 1) * 1024, kv[i >> 1][i & 1]);
          }
#pragma unroll
          for (int i = 0; i < 4; ++i) {
#pragma unroll
            for (int e = 0; e < 4; ++e) {
              const int t0 = 16 * (i & 1) + 2 * e;
              const unsigned mask = rel == t0 ? 0xffffu : rel == t0 + 1 ? 0xffff0000u : 0u;
              kv[2 + (i >> 1)][i & 1][e] = (kv[2 + (i >> 1)][i & 1][e] & ~mask) | (pv[i >> 1] & mask);
            }
            stg(rs, (rel >> 3) == 2 * (i & 1) ? ep[i >> 1] + 2048 + (i & 1) * 1024 : OOB, kv[2 + (i >> 1)][i & 1]);
          }
#pragma unroll
          for (int i = 0; i < 4; ++i) *reinterpret_cast<u32x4*>(smem + L_KX + wid * 4096 + i * 1024 + lane * 16) = kv[(i & 2) + 1][i & 1];
        }
        if (8 < 16) drained();
      }
      SBAR();
#pragma unroll
      for (int j = 0; j < 2; ++j) {
        char* p = smem + L_QX + ((j ^ hh) * 4 + wid) * 2048 + lane * 16;
        *reinterpret_cast<u32x4*>(p) = qo[j][0]; *reinterpret_cast<u32x4*>(p + 1024) = qo[j][1];
      }
      if (8 >= 16) drained();
      STAMPV(41 + 4 * L);
      BAR_LGKM();
      STAMPV(43 + 4 * L);
      kva = cat4(kv[0][0], kv[0][1], kv[2][0], kv[2][1]);
      const u32x4* pk = reinterpret_cast<const u32x4*>(smem + L_KX + (wid ^ 1) * 4096 + lane * 16);
#pragma unroll
      for (int u = 0; u < 4; ++u) {
        const u32x4* p = reinterpret_cast<const u32x4*>(smem + L_QX + (hh * 4 + ((2 * kh + 2 + u) & 3)) * 2048 + lane * 16);
        q[u] = __builtin_shufflevector(p[0], p[64], 0, 1, 2, 3, 4, 5, 6, 7);
      }
      kvb = cat4(pk[0], pk[64], pk[128], pk[192]);
      SBAR();
      asm volatile(
        "v_mov_b64 v[0:1], 0\n\t"
        "v_mov_b64 v[2:3], 0\n\t"
        "v_mov_b64 v[4:5], 0\n\t"
        "v_mov_b64 v[6:7], 0\n\t"
        "v_mov_b64 v[8:9], 0\n\t"
        "v_mov_b64 v[10:11], 0\n\t"
        "v_mov_b64 v[12:13], 0\n\t"
        "v_mov_b64 v[14:15], 0\n\t"
        "v_mov_b64 v[16:17], 0\n\t"
        "v_mov_b64 v[18:19], 0\n\t"
        "v_mov_b64 v[20:21], 0\n\t"
        "v_mov_b64 v[22:23], 0\n\t"
        "v_mov_b64 v[24:25], 0\n\t"
        "v_mov_b64 v[26:27], 0\n\t"
        "v_mov_b64 v[28:29], 0\n\t"
        "v_mov_b64 v[30:31], 0\n\t"
        "v_mov_b64 v[32:33], 0\n\t"
        "v_mov_b64 v[34:35], 0\n\t"
        "v_mov_b64 v[36:37], 0\n\t"
        "v_mov_b64 v[38:39], 0\n\t"
        "v_mov_b64 v[40:41], 0\n\t"
        "v_mov_b64 v[42:43], 0\n\t"
        "v_mov_b64 v[44:45], 0\n\t"
        "v_mov_b64 v[46:47], 0\n\t"
        "v_mov_b64 v[48:49], 0\n\t"
        "v_mov_b64 v[50:51], 0\n\t"
        "v_mov_b64 v[52:53], 0\n\t"
        "v_mov_b64 v[54:55], 0\n\t"
        "v_mov_b64 v[56:57], 0\n\t"
        "v_mov_b64 v[58:59], 0\n\t"
        "v_mov_b64 v[60:61], 0\n\t"
        "v_mov_b64 v[62:63], 0\n\t"
        "v_mov_b64 v[128:129], 0\n\t"
        "v_mov_b64 v[130:131], 0\n\t"
        "v_mov_b64 v[132:133], 0\n\t"
        "v_mov_b64 v[134:135], 0\n\t"
        "v_mov_b64 v[136:137], 0\n\t"
        "v_mov_b64 v[138:139], 0\n\t"
        "v_mov_b64 v[140:141], 0\n\t"
        "v_mov_b64 v[142:143], 0\n\t"
        : "={v[0:15]}"(oz[0]), "={v[16:31]}"(oz[1]), "={v[32:47]}"(oz[2]), "={v[48:63]}"(oz[3]), "={v[128:143]}"(oz[4]));
    }
    STAMPV(5 + 16 * L);
    {
      u32x4 ones;
      unsigned kvoff;
      u32x8 fv, cv;
      asm volatile("" : "={v[184:191]}"(fv), "={v[216:223]}"(cv));
      { PHASE_VARS;
        const unsigned v = (lane & 3) == 0 ? 0x3F803F80u : 0u; const bool up = (lane & 4) != 0; ones[0] = ones[1] = up ? 0u : v; ones[2] = ones[3] = up ? v : 0u;
        kvoff = OFF_KV + L * (10 * MBY) + (tn * 2 + hh) * HS + kh * 8192 + lane * 16;
        fv[1] = lbase + L_FREE + wid * 256 + lane * 4; fv[2] = 32 * L + 32;
        cv[0] = lbase + L_XCH + wid * 8192 + lane * 16; cv[1] = lbase + L_XCH + (wid ^ 2) * 8192 + lane * 16;
        cv[2] = lbase + L_XCH + 32768 + wid * 512 + lane * 4; cv[3] = lbase + L_XCH + 32768 + (wid ^ 2) * 512 + lane * 4;
        cv[4] = lbase + L_W + 48 + wid * 4; cv[5] = lbase + L_W + 48 + (wid ^ 2) * 4; cv[6] = L + 1;
        cv[7] = ((tile * 32 + r32) * D_ + tn * 64 + hi * 8) * 2; }
      f32x16 o0 = oz[0], o1 = oz[1], o2 = oz[2], o3 = oz[3], lv = oz[4];
      unsigned soff = (tm | 7) * 16384;
      STAMP_DECL;
      asm volatile(
        "s_nop 1\n\t"
        "v_mfma_f32_32x32x16_bf16 v[96:111], v[152:155], v[64:67], 0\n\t"
        "v_mfma_f32_32x32x16_bf16 v[96:111], v[156:159], v[68:71], v[96:111]\n\t"
        "s_mov_b32 s46, 0\n\t"
        "s_mov_b64 s[54:55], s[42:43]\n\t"
        "ds_write_b32 v185, v186\n\t"
        "s_nop 8\n\t"
        "v_exp_f32 v96, v96\n\t"
        "v_exp_f32 v97, v97\n\t"
        "v_exp_f32 v98, v98\n\t"
        "v_exp_f32 v99, v99\n\t"
        "v_exp_f32 v100, v100\n\t"
        "v_exp_f32 v101, v101\n\t"
        "v_exp_f32 v102, v102\n\t"
        "v_mfma_f32_32x32x16_bf16 v[112:127], v[152:155], v[72:75], 0\n\t"
        "v_exp_f32 v103, v103\n\t"
        "v_exp_f32 v104, v104\n\t"
        "v_exp_f32 v105, v105\n\t"
        "v_mfma_f32_32x32x16_bf16 v[112:127], v[156:159], v[76:79], v[112:127]\n\t"
        "v_exp_f32 v106, v106\n\t"
        "v_exp_f32 v107, v107\n\t"
        "v_exp_f32 v108, v108\n\t"
        "v_smfmac_f32_32x32x32_bf16 v[0:15], v[160:163], v[96:103], v148\n\t"
        "v_exp_f32 v109, v109\n\t"
        "v_exp_f32 v110, v110\n\t"
        "v_exp_f32 v111, v111\n\t"
        "v_smfmac_f32_16x16x64_bf16 v[128:131], v[144:147], v[96:103], v148\n\t"
        "v_exp_f32 v112, v112\n\t"
        "v_exp_f32 v113, v113\n\t"
        "v_smfmac_f32_32x32x32_bf16 v[0:15], v[164:167], v[104:111], v148\n\t"
        "v_exp_f32 v114, v114\n\t"
        "v_exp_f32 v115, v115\n\t"
        "v_exp_f32 v116, v116\n\t"
        "v_smfmac_f32_16x16x64_bf16 v[128:131], v[144:147], v[104:111], v148\n\t"
        "v_exp_f32 v117, v117\n\t"
        "v_exp_f32 v118, v118\n\t"
        "v_mfma_f32_32x32x16_bf16 v[96:111], v[152:155], v[80:83], 0\n\t"
        "v_exp_f32 v119, v119\n\t"
        "v_exp_f32 v120, v120\n\t"
        "v_exp_f32 v121, v121\n\t"
        "v_mfma_f32_32x32x16_bf16 v[96:111], v[156:159], v[84:87], v[96:111]\n\t"
        "ds_read_b32 v151, v224\n\t"
        "v_exp_f32 v122, v122\n\t"
        "v_exp_f32 v123, v123\n\t"
        "v_exp_f32 v124, v124\n\t"
        "v_smfmac_f32_32x32x32_bf16 v[16:31], v[160:163], v[112:119], v148\n\t"
        "v_exp_f32 v125, v125\n\t"
        "v_exp_f32 v126, v126\n\t"
        "v_exp_f32 v127, v127\n\t"
        "v_smfmac_f32_16x16x64_bf16 v[132:135], v[144:147], v[112:119], v148\n\t"
        "s_cmp_lg_u32 s46, 0\n\t"
        "s_cbranch_scc1 25f\n\t"
        "s_waitcnt lgkmcnt(0)\n\t"
        "v_cmp_ne_u32 vcc, v222, v151\n\t"
        "s_cbranch_vccz 24f\n\t"
        "25:\n\t"
        "v_exp_f32 v96, v96\n\t"
        "v_exp_f32 v97, v97\n\t"
        "v_smfmac_f32_32x32x32_bf16 v[16:31], v[164:167], v[120:127], v148\n\t"
        "v_exp_f32 v98, v98\n\t"
        "v_exp_f32 v99, v99\n\t"
        "v_exp_f32 v100, v100\n\t"
        "v_smfmac_f32_16x16x64_bf16 v[132:135], v[144:147], v[120:127], v148\n\t"
        "v_exp_f32 v101, v101\n\t"
        "v_exp_f32 v102, v102\n\t"
        "v_mfma_f32_32x32x16_bf16 v[112:127], v[152:155], v[88:91], 0\n\t"
        "v_exp_f32 v103, v103\n\t"
        "v_exp_f32 v104, v104\n\t"
        "v_exp_f32 v105, v105\n\t"
        "v_mfma_f32_32x32x16_bf16 v[112:127], v[156:159], v[92:95], v[112:127]\n\t"
        "ds_read_b32 v151, v224\n\t"
        "v_exp_f32 v106, v106\n\t"
        "v_exp_f32 v107, v107\n\t"
        "v_exp_f32 v108, v108\n\t"
        "v_smfmac_f32_32x32x32_bf16 v[32:47], v[160:163], v[96:103], v148\n\t"
        "v_exp_f32 v109, v109\n\t"
        "v_exp_f32 v110, v110\n\t"
        "v_exp_f32 v111, v111\n\t"
        "v_smfmac_f32_16x16x64_bf16 v[136:139], v[144:147], v[96:103], v148\n\t"
        "s_cmp_lg_u32 s46, 0\n\t"
        "s_cbranch_scc1 27f\n\t"
        "s_waitcnt lgkmcnt(0)\n\t"
        "v_cmp_ne_u32 vcc, v222, v151\n\t"
        "s_cbranch_vccz 26f\n\t"
        "27:\n\t"
        "v_exp_f32 v112, v112\n\t"
        "v_exp_f32 v113, v113\n\t"
        "v_smfmac_f32_32x32x32_bf16 v[32:47], v[164:167], v[104:111], v148\n\t"
        "v_exp_f32 v114, v114\n\t"
        "v_exp_f32 v115, v115\n\t"
        "v_exp_f32 v116, v116\n\t"
        "v_smfmac_f32_16x16x64_bf16 v[136:139], v[144:147], v[104:111], v148\n\t"
        "v_exp_f32 v117, v117\n\t"
        "v_exp_f32 v118, v118\n\t"
        "v_mfma_f32_32x32x16_bf16 v[96:111], v[168:171], v[64:67], 0\n\t"
        "v_exp_f32 v119, v119\n\t"
        "v_exp_f32 v120, v120\n\t"
        "v_exp_f32 v121, v121\n\t"
        "v_mfma_f32_32x32x16_bf16 v[96:111], v[172:175], v[68:71], v[96:111]\n\t"
        "ds_read_b32 v151, v224\n\t"
        "v_exp_f32 v122, v122\n\t"
        "v_exp_f32 v123, v123\n\t"
        "v_exp_f32 v124, v124\n\t"
        "v_smfmac_f32_32x32x32_bf16 v[48:63], v[160:163], v[112:119], v148\n\t"
        "v_exp_f32 v125, v125\n\t"
        "v_exp_f32 v126, v126\n\t"
        "v_exp_f32 v127, v127\n\t"
        "v_smfmac_f32_16x16x64_bf16 v[140:143], v[144:147], v[112:119], v148\n\t"
        "s_cmp_lg_u32 s46, 0\n\t"
        "s_cbranch_scc1 29f\n\t"
        "s_waitcnt lgkmcnt(0)\n\t"
        "v_cmp_ne_u32 vcc, v222, v151\n\t"
        "s_cbranch_vccz 28f\n\t"
        "29:\n\t"
        "v_exp_f32 v96, v96\n\t"
        "v_exp_f32 v97, v97\n\t"
        "v_smfmac_f32_32x32x32_bf16 v[48:63], v[164:167], v[120:127], v148\n\t"
        "v_exp_f32 v98, v98\n\t"
        "v_exp_f32 v99, v99\n\t"
        "v_exp_f32 v100, v100\n\t"
        "v_smfmac_f32_16x16x64_bf16 v[140:143], v[144:147], v[120:127], v148\n\t"
        "v_exp_f32 v101, v101\n\t"
        "v_exp_f32 v102, v102\n\t"
        "v_mfma_f32_32x32x16_bf16 v[112:127], v[168:171], v[72:75], 0\n\t"
        "v_exp_f32 v103, v103\n\t"
        "v_exp_f32 v104, v104\n\t"
        "v_exp_f32 v105, v105\n\t"
        "v_mfma_f32_32x32x16_bf16 v[112:127], v[172:175], v[76:79], v[112:127]\n\t"
        "ds_read_b32 v151, v224\n\t"
        "v_exp_f32 v106, v106\n\t"
        "v_exp_f32 v107, v107\n\t"
        "v_exp_f32 v108, v108\n\t"
        "v_smfmac_f32_32x32x32_bf16 v[0:15], v[176:179], v[96:103], v148\n\t"
        "v_exp_f32 v109, v109\n\t"
        "v_exp_f32 v110, v110\n\t"
        "v_exp_f32 v111, v111\n\t"
        "v_smfmac_f32_16x16x64_bf16 v[128:131], v[144:147], v[96:103], v148\n\t"
        "s_cmp_lg_u32 s46, 0\n\t"
        "s_cbranch_scc1 31f\n\t"
        "s_waitcnt lgkmcnt(0)\n\t"
        "v_cmp_ne_u32 vcc, v222, v151\n\t"
        "s_cbranch_vccz 30f\n\t"
        "31:\n\t"
        "v_exp_f32 v112, v112\n\t"
        "v_exp_f32 v113, v113\n\t"
        "v_smfmac_f32_32x32x32_bf16 v[0:15], v[180:183], v[104:111], v148\n\t"
        "v_exp_f32 v114, v114\n\t"
        "v_exp_f32 v115, v115\n\t"
        "v_exp_f32 v116, v116\n\t"
        "v_smfmac_f32_16x16x64_bf16 v[128:131], v[144:147], v[104:111], v148\n\t"
        "v_exp_f32 v117, v117\n\t"
        "v_exp_f32 v118, v118\n\t"
        "v_mfma_f32_32x32x16_bf16 v[96:111], v[168:171], v[80:83], 0\n\t"
        "v_exp_f32 v119, v119\n\t"
        "v_exp_f32 v120, v120\n\t"
        "v_exp_f32 v121, v121\n\t"
        "v_mfma_f32_32x32x16_bf16 v[96:111], v[172:175], v[84:87], v[96:111]\n\t"
        "ds_read_b32 v151, v224\n\t"
        "v_exp_f32 v122, v122\n\t"
        "v_exp_f32 v123, v123\n\t"
        "v_exp_f32 v124, v124\n\t"
        "v_smfmac_f32_32x32x32_bf16 v[16:31], v[176:179], v[112:119], v148\n\t"
        "v_exp_f32 v125, v125\n\t"
        "v_exp_f32 v126, v126\n\t"
        "v_exp_f32 v127, v127\n\t"
        "v_smfmac_f32_16x16x64_bf16 v[132:135], v[144:147], v[112:119], v148\n\t"
        "s_cmp_lg_u32 s46, 0\n\t"
        "s_cbranch_scc1 33f\n\t"
        "s_waitcnt lgkmcnt(0)\n\t"
        "v_cmp_ne_u32 vcc, v222, v151\n\t"
        "s_cbranch_vccz 32f\n\t"
        "33:\n\t"
        "v_exp_f32 v96, v96\n\t"
        "v_exp_f32 v97, v97\n\t"
        "v_smfmac_f32_32x32x32_bf16 v[16:31], v[180:183], v[120:127], v148\n\t"
        "v_exp_f32 v98, v98\n\t"
        "v_exp_f32 v99, v99\n\t"
        "v_exp_f32 v100, v100\n\t"
        "v_smfmac_f32_16x16x64_bf16 v[132:135], v[144:147], v[120:127], v148\n\t"
        "v_exp_f32 v101, v101\n\t"
        "v_exp_f32 v102, v102\n\t"
        "v_mfma_f32_32x32x16_bf16 v[112:127], v[168:171], v[88:91], 0\n\t"
        "v_exp_f32 v103, v103\n\t"
        "v_exp_f32 v104, v104\n\t"
        "v_exp_f32 v105, v105\n\t"
        "v_mfma_f32_32x32x16_bf16 v[112:127], v[172:175], v[92:95], v[112:127]\n\t"
        "v_exp_f32 v106, v106\n\t"
        "v_exp_f32 v107, v107\n\t"
        "v_exp_f32 v108, v108\n\t"
        "v_smfmac_f32_32x32x32_bf16 v[32:47], v[176:179], v[96:103], v148\n\t"
        "v_exp_f32 v109, v109\n\t"
        "v_exp_f32 v110, v110\n\t"
        "v_exp_f32 v111, v111\n\t"
        "v_smfmac_f32_16x16x64_bf16 v[136:139], v[144:147], v[96:103], v148\n\t"
        "s_cmp_lg_u32 s46, 0\n\t"
        "s_cbranch_scc1 5f\n\t"
        "s_mov_b32 s45, 0x200000\n\t"
        "2:\n\t"
        "ds_read_b32 v151, v224\n\t"
        "s_sub_u32 s45, s45, 1\n\t"
        "s_waitcnt lgkmcnt(0)\n\t"
        "s_cbranch_scc1 3f\n\t"
        "v_cmp_ne_u32 vcc, v222, v151\n\t"
        "s_cbranch_vccnz 2b\n\t"
        "3:\n\t"
        "s_add_u32 s44, s44, 0x4000\n\t"
        "s_and_b32 s44, s44, 0x3ffff\n\t"
        "s_cmp_eq_u32 s44, s49\n\t"
        "s_cselect_b32 s44, s50, s44\n\t"
        "s_add_u32 s52, s40, s44\n\t"
        "s_addc_u32 s53, s41, 0\n\t"
        "buffer_load_dwordx4 v[184:187], v149, s[52:55], 0 offen offset:0 sc1\n\t"
        "buffer_load_dwordx4 v[188:191], v149, s[52:55], 0 offen offset:1024 sc1\n\t"
        "buffer_load_dwordx4 v[192:195], v149, s[52:55], 0 offen offset:2048 sc1\n\t"
        "buffer_load_dwordx4 v[196:199], v149, s[52:55], 0 offen offset:3072 sc1\n\t"
        "buffer_load_dwordx4 v[200:203], v150, s[52:55], 0 offen offset:0 sc1\n\t"
        "buffer_load_dwordx4 v[204:207], v150, s[52:55], 0 offen offset:1024 sc1\n\t"
        "buffer_load_dwordx4 v[208:211], v150, s[52:55], 0 offen offset:2048 sc1\n\t"
        "buffer_load_dwordx4 v[212:215], v150, s[52:55], 0 offen offset:3072 sc1\n\t"
        "5:\n\t"
        "s_add_u32 s44, s44, 0x4000\n\t"
        "s_and_b32 s44, s44, 0x3ffff\n\t"
        "s_cmp_eq_u32 s44, s49\n\t"
        "s_cselect_b32 s44, s50, s44\n\t"
        "s_add_u32 s52, s40, s44\n\t"
        "s_addc_u32 s53, s41, 0\n\t"
        "buffer_load_dwordx4 v[152:155], v149, s[52:55], 0 offen offset:0 sc1\n\t"
        "buffer_load_dwordx4 v[156:159], v149, s[52:55], 0 offen offset:1024 sc1\n\t"
        "buffer_load_dwordx4 v[160:163], v149, s[52:55], 0 offen offset:2048 sc1\n\t"
        "buffer_load_dwordx4 v[164:167], v149, s[52:55], 0 offen offset:3072 sc1\n\t"
        "s_mov_b32 s45, 6\n\t"
        "1:\n\t"
        "v_exp_f32 v112, v112\n\t"
        "v_exp_f32 v113, v113\n\t"
        "v_smfmac_f32_32x32x32_bf16 v[32:47], v[180:183], v[104:111], v148\n\t"
        "v_exp_f32 v114, v114\n\t"
        "v_exp_f32 v115, v115\n\t"
        "v_exp_f32 v116, v116\n\t"
        "v_smfmac_f32_16x16x64_bf16 v[136:139], v[144:147], v[104:111], v148\n\t"
        "s_waitcnt vmcnt(8)\n\t"
        "v_exp_f32 v117, v117\n\t"
        "v_exp_f32 v118, v118\n\t"
        "v_mfma_f32_32x32x16_bf16 v[96:111], v[184:187], v[64:67], 0\n\t"
        "v_exp_f32 v119, v119\n\t"
        "v_exp_f32 v120, v120\n\t"
        "v_exp_f32 v121, v121\n\t"
        "v_mfma_f32_32x32x16_bf16 v[96:111], v[188:191], v[68:71], v[96:111]\n\t"
        "v_exp_f32 v122, v122\n\t"
        "v_exp_f32 v123, v123\n\t"
        "v_exp_f32 v124, v124\n\t"
        "v_smfmac_f32_32x32x32_bf16 v[48:63], v[176:179], v[112:119], v148\n\t"
        "v_exp_f32 v125, v125\n\t"
        "v_exp_f32 v126, v126\n\t"
        "v_exp_f32 v127, v127\n\t"
        "v_smfmac_f32_16x16x64_bf16 v[140:143], v[144:147], v[112:119], v148\n\t"
        "buffer_load_dwordx4 v[168:171], v150, s[52:55], 0 offen offset:0 sc1\n\t"
        "v_exp_f32 v96, v96\n\t"
        "v_exp_f32 v97, v97\n\t"
        "v_smfmac_f32_32x32x32_bf16 v[48:63], v[180:183], v[120:127], v148\n\t"
        "v_exp_f32 v98, v98\n\t"
        "v_exp_f32 v99, v99\n\t"
        "v_exp_f32 v100, v100\n\t"
        "v_smfmac_f32_16x16x64_bf16 v[140:143], v[144:147], v[120:127], v148\n\t"
        "v_exp_f32 v101, v101\n\t"
        "v_exp_f32 v102, v102\n\t"
        "v_mfma_f32_32x32x16_bf16 v[112:127], v[184:187], v[72:75], 0\n\t"
        "v_exp_f32 v103, v103\n\t"
        "v_exp_f32 v104, v104\n\t"
        "v_exp_f32 v105, v105\n\t"
        "v_mfma_f32_32x32x16_bf16 v[112:127], v[188:191], v[76:79], v[112:127]\n\t"
        "v_exp_f32 v106, v106\n\t"
        "v_exp_f32 v107, v107\n\t"
        "v_exp_f32 v108, v108\n\t"
        "v_smfmac_f32_32x32x32_bf16 v[0:15], v[192:195], v[96:103], v148\n\t"
        "v_exp_f32 v109, v109\n\t"
        "v_exp_f32 v110, v110\n\t"
        "v_exp_f32 v111, v111\n\t"
        "v_smfmac_f32_16x16x64_bf16 v[128:131], v[144:147], v[96:103], v148\n\t"
        "buffer_load_dwordx4 v[172:175], v150, s[52:55], 0 offen offset:1024 sc1\n\t"
        "v_exp_f32 v112, v112\n\t"
        "v_exp_f32 v113, v113\n\t"
        "v_smfmac_f32_32x32x32_bf16 v[0:15], v[196:199], v[104:111], v148\n\t"
        "v_exp_f32 v114, v114\n\t"
        "v_exp_f32 v115, v115\n\t"
        "v_exp_f32 v116, v116\n\t"
        "v_smfmac_f32_16x16x64_bf16 v[128:131], v[144:147], v[104:111], v148\n\t"
        "v_exp_f32 v117, v117\n\t"
        "v_exp_f32 v118, v118\n\t"
        "v_mfma_f32_32x32x16_bf16 v[96:111], v[184:187], v[80:83], 0\n\t"
        "v_exp_f32 v119, v119\n\t"
        "v_exp_f32 v120, v120\n\t"
        "v_exp_f32 v121, v121\n\t"
        "v_mfma_f32_32x32x16_bf16 v[96:111], v[188:191], v[84:87], v[96:111]\n\t"
        "v_exp_f32 v122, v122\n\t"
        "v_exp_f32 v123, v123\n\t"
        "v_exp_f32 v124, v124\n\t"
        "v_smfmac_f32_32x32x32_bf16 v[16:31], v[192:195], v[112:119], v148\n\t"
        "v_exp_f32 v125, v125\n\t"
        "v_exp_f32 v126, v126\n\t"
        "v_exp_f32 v127, v127\n\t"
        "v_smfmac_f32_16x16x64_bf16 v[132:135], v[144:147], v[112:119], v148\n\t"
        "buffer_load_dwordx4 v[176:179], v150, s[52:55], 0 offen offset:2048 sc1\n\t"
        "v_exp_f32 v96, v96\n\t"
        "v_exp_f32 v97, v97\n\t"
        "v_smfmac_f32_32x32x32_bf16 v[16:31], v[196:199], v[120:127], v148\n\t"
        "v_exp_f32 v98, v98\n\t"
        "v_exp_f32 v99, v99\n\t"
        "v_exp_f32 v100, v100\n\t"
        "v_smfmac_f32_16x16x64_bf16 v[132:135], v[144:147], v[120:127], v148\n\t"
        "v_exp_f32 v101, v101\n\t"
        "v_exp_f32 v102, v102\n\t"
        "v_mfma_f32_32x32x16_bf16 v[112:127], v[184:187], v[88:91], 0\n\t"
        "v_exp_f32 v103, v103\n\t"
        "v_exp_f32 v104, v104\n\t"
        "v_exp_f32 v105, v105\n\t"
        "v_mfma_f32_32x32x16_bf16 v[112:127], v[188:191], v[92:95], v[112:127]\n\t"
        "v_exp_f32 v106, v106\n\t"
        "v_exp_f32 v107, v107\n\t"
        "v_exp_f32 v108, v108\n\t"
        "v_smfmac_f32_32x32x32_bf16 v[32:47], v[192:195], v[96:103], v148\n\t"
        "v_exp_f32 v109, v109\n\t"
        "v_exp_f32 v110, v110\n\t"
        "v_exp_f32 v111, v111\n\t"
        "v_smfmac_f32_16x16x64_bf16 v[136:139], v[144:147], v[96:103], v148\n\t"
        "buffer_load_dwordx4 v[180:183], v150, s[52:55], 0 offen offset:3072 sc1\n\t"
        "v_exp_f32 v112, v112\n\t"
        "v_exp_f32 v113, v113\n\t"
        "v_smfmac_f32_32x32x32_bf16 v[32:47], v[196:199], v[104:111], v148\n\t"
        "s_add_u32 s44, s44, 0x4000\n\t"
        "s_and_b32 s44, s44, 0x3ffff\n\t"
        "v_exp_f32 v114, v114\n\t"
        "v_exp_f32 v115, v115\n\t"
        "v_exp_f32 v116, v116\n\t"
        "v_smfmac_f32_16x16x64_bf16 v[136:139], v[144:147], v[104:111], v148\n\t"
        "s_waitcnt vmcnt(8)\n\t"
        "v_exp_f32 v117, v117\n\t"
        "v_exp_f32 v118, v118\n\t"
        "v_mfma_f32_32x32x16_bf16 v[96:111], v[200:203], v[64:67], 0\n\t"
        "s_cmp_eq_u32 s44, s49\n\t"
        "v_exp_f32 v119, v119\n\t"
        "v_exp_f32 v120, v120\n\t"
        "v_exp_f32 v121, v121\n\t"
        "v_mfma_f32_32x32x16_bf16 v[96:111], v[204:207], v[68:71], v[96:111]\n\t"
        "s_cselect_b32 s44, s50, s44\n\t"
        "v_exp_f32 v122, v122\n\t"
        "v_exp_f32 v123, v123\n\t"
        "v_exp_f32 v124, v124\n\t"
        "v_smfmac_f32_32x32x32_bf16 v[48:63], v[192:195], v[112:119], v148\n\t"
        "s_add_u32 s52, s40, s44\n\t"
        "s_addc_u32 s53, s41, 0\n\t"
        "v_exp_f32 v125, v125\n\t"
        "v_exp_f32 v126, v126\n\t"
        "v_exp_f32 v127, v127\n\t"
        "v_smfmac_f32_16x16x64_bf16 v[140:143], v[144:147], v[112:119], v148\n\t"
        "buffer_load_dwordx4 v[184:187], v149, s[52:55], 0 offen offset:0 sc1\n\t"
        "v_exp_f32 v96, v96\n\t"
        "v_exp_f32 v97, v97\n\t"
        "v_smfmac_f32_32x32x32_bf16 v[48:63], v[196:199], v[120:127], v148\n\t"
        "v_exp_f32 v98, v98\n\t"
        "v_exp_f32 v99, v99\n\t"
        "v_exp_f32 v100, v100\n\t"
        "v_smfmac_f32_16x16x64_bf16 v[140:143], v[144:147], v[120:127], v148\n\t"
        "v_exp_f32 v101, v101\n\t"
        "v_exp_f32 v102, v102\n\t"
        "v_mfma_f32_32x32x16_bf16 v[112:127], v[200:203], v[72:75], 0\n\t"
        "v_exp_f32 v103, v103\n\t"
        "v_exp_f32 v104, v104\n\t"
        "v_exp_f32 v105, v105\n\t"
        "v_mfma_f32_32x32x16_bf16 v[112:127], v[204:207], v[76:79], v[112:127]\n\t"
        "v_exp_f32 v106, v106\n\t"
        "v_exp_f32 v107, v107\n\t"
        "v_exp_f32 v108, v108\n\t"
        "v_smfmac_f32_32x32x32_bf16 v[0:15], v[208:211], v[96:103], v148\n\t"
        "v_exp_f32 v109, v109\n\t"
        "v_exp_f32 v110, v110\n\t"
        "v_exp_f32 v111, v111\n\t"
        "v_smfmac_f32_16x16x64_bf16 v[128:131], v[144:147], v[96:103], v148\n\t"
        "buffer_load_dwordx4 v[188:191], v149, s[52:55], 0 offen offset:1024 sc1\n\t"
        "v_exp_f32 v112, v112\n\t"
        "v_exp_f32 v113, v113\n\t"
        "v_smfmac_f32_32x32x32_bf16 v[0:15], v[212:215], v[104:111], v148\n\t"
        "v_exp_f32 v114, v114\n\t"
        "v_exp_f32 v115, v115\n\t"
        "v_exp_f32 v116, v116\n\t"
        "v_smfmac_f32_16x16x64_bf16 v[128:131], v[144:147], v[104:111], v148\n\t"
        "v_exp_f32 v117, v117\n\t"
        "v_exp_f32 v118, v118\n\t"
        "v_mfma_f32_32x32x16_bf16 v[96:111], v[200:203], v[80:83], 0\n\t"
        "v_exp_f32 v119, v119\n\t"
        "v_exp_f32 v120, v120\n\t"
        "v_exp_f32 v121, v121\n\t"
        "v_mfma_f32_32x32x16_bf16 v[96:111], v[204:207], v[84:87], v[96:111]\n\t"
        "v_exp_f32 v122, v122\n\t"
        "v_exp_f32 v123, v123\n\t"
        "v_exp_f32 v124, v124\n\t"
        "v_smfmac_f32_32x32x32_bf16 v[16:31], v[208:211], v[112:119], v148\n\t"
        "v_exp_f32 v125, v125\n\t"
        "v_exp_f32 v126, v126\n\t"
        "v_exp_f32 v127, v127\n\t"
        "v_smfmac_f32_16x16x64_bf16 v[132:135], v[144:147], v[112:119], v148\n\t"
        "buffer_load_dwordx4 v[192:195], v149, s[52:55], 0 offen offset:2048 sc1\n\t"
        "v_exp_f32 v96, v96\n\t"
        "v_exp_f32 v97, v97\n\t"
        "v_smfmac_f32_32x32x32_bf16 v[16:31], v[212:215], v[120:127], v148\n\t"
        "v_exp_f32 v98, v98\n\t"
        "v_exp_f32 v99, v99\n\t"
        "v_exp_f32 v100, v100\n\t"
        "v_smfmac_f32_16x16x64_bf16 v[132:135], v[144:147], v[120:127], v148\n\t"
        "v_exp_f32 v101, v101\n\t"
        "v_exp_f32 v102, v102\n\t"
        "v_mfma_f32_32x32x16_bf16 v[112:127], v[200:203], v[88:91], 0\n\t"
        "v_exp_f32 v103, v103\n\t"
        "v_exp_f32 v104, v104\n\t"
        "v_exp_f32 v105, v105\n\t"
        "v_mfma_f32_32x32x16_bf16 v[112:127], v[204:207], v[92:95], v[112:127]\n\t"
        "v_exp_f32 v106, v106\n\t"
        "v_exp_f32 v107, v107\n\t"
        "v_exp_f32 v108, v108\n\t"
        "v_smfmac_f32_32x32x32_bf16 v[32:47], v[208:211], v[96:103], v148\n\t"
        "v_exp_f32 v109, v109\n\t"
        "v_exp_f32 v110, v110\n\t"
        "v_exp_f32 v111, v111\n\t"
        "v_smfmac_f32_16x16x64_bf16 v[136:139], v[144:147], v[96:103], v148\n\t"
        "buffer_load_dwordx4 v[196:199], v149, s[52:55], 0 offen offset:3072 sc1\n\t"
        "v_exp_f32 v112, v112\n\t"
        "v_exp_f32 v113, v113\n\t"
        "v_smfmac_f32_32x32x32_bf16 v[32:47], v[212:215], v[104:111], v148\n\t"
        "v_exp_f32 v114, v114\n\t"
        "v_exp_f32 v115, v115\n\t"
        "v_exp_f32 v116, v116\n\t"
        "v_smfmac_f32_16x16x64_bf16 v[136:139], v[144:147], v[104:111], v148\n\t"
        "s_waitcnt vmcnt(8)\n\t"
        "v_exp_f32 v117, v117\n\t"
        "v_exp_f32 v118, v118\n\t"
        "v_mfma_f32_32x32x16_bf16 v[96:111], v[152:155], v[64:67], 0\n\t"
        "v_exp_f32 v119, v119\n\t"
        "v_exp_f32 v120, v120\n\t"
        "v_exp_f32 v121, v121\n\t"
        "v_mfma_f32_32x32x16_bf16 v[96:111], v[156:159], v[68:71], v[96:111]\n\t"
        "v_exp_f32 v122, v122\n\t"
        "v_exp_f32 v123, v123\n\t"
        "v_exp_f32 v124, v124\n\t"
        "v_smfmac_f32_32x32x32_bf16 v[48:63], v[208:211], v[112:119], v148\n\t"
        "v_exp_f32 v125, v125\n\t"
        "v_exp_f32 v126, v126\n\t"
        "v_exp_f32 v127, v127\n\t"
        "v_smfmac_f32_16x16x64_bf16 v[140:143], v[144:147], v[112:119], v148\n\t"
        "buffer_load_dwordx4 v[200:203], v150, s[52:55], 0 offen offset:0 sc1\n\t"
        "v_exp_f32 v96, v96\n\t"
        "v_exp_f32 v97, v97\n\t"
        "v_smfmac_f32_32x32x32_bf16 v[48:63], v[212:215], v[120:127], v148\n\t"
        "v_exp_f32 v98, v98\n\t"
        "v_exp_f32 v99, v99\n\t"
        "v_exp_f32 v100, v100\n\t"
        "v_smfmac_f32_16x16x64_bf16 v[140:143], v[144:147], v[120:127], v148\n\t"
        "v_exp_f32 v101, v101\n\t"
        "v_exp_f32 v102, v102\n\t"
        "v_mfma_f32_32x32x16_bf16 v[112:127], v[152:155], v[72:75], 0\n\t"
        "v_exp_f32 v103, v103\n\t"
        "v_exp_f32 v104, v104\n\t"
        "v_exp_f32 v105, v105\n\t"
        "v_mfma_f32_32x32x16_bf16 v[112:127], v[156:159], v[76:79], v[112:127]\n\t"
        "v_exp_f32 v106, v106\n\t"
        "v_exp_f32 v107, v107\n\t"
        "v_exp_f32 v108, v108\n\t"
        "v_smfmac_f32_32x32x32_bf16 v[0:15], v[160:163], v[96:103], v148\n\t"
        "v_exp_f32 v109, v109\n\t"
        "v_exp_f32 v110, v110\n\t"
        "v_exp_f32 v111, v111\n\t"
        "v_smfmac_f32_16x16x64_bf16 v[128:131], v[144:147], v[96:103], v148\n\t"
        "buffer_load_dwordx4 v[204:207], v150, s[52:55], 0 offen offset:1024 sc1\n\t"
        "v_exp_f32 v112, v112\n\t"
        "v_exp_f32 v113, v113\n\t"
        "v_smfmac_f32_32x32x32_bf16 v[0:15], v[164:167], v[104:111], v148\n\t"
        "v_exp_f32 v114, v114\n\t"
        "v_exp_f32 v115, v115\n\t"
        "v_exp_f32 v116, v116\n\t"
        "v_smfmac_f32_16x16x64_bf16 v[128:131], v[144:147], v[104:111], v148\n\t"
        "v_exp_f32 v117, v117\n\t"
        "v_exp_f32 v118, v118\n\t"
        "v_mfma_f32_32x32x16_bf16 v[96:111], v[152:155], v[80:83], 0\n\t"
        "v_exp_f32 v119, v119\n\t"
        "v_exp_f32 v120, v120\n\t"
        "v_exp_f32 v121, v121\n\t"
        "v_mfma_f32_32x32x16_bf16 v[96:111], v[156:159], v[84:87], v[96:111]\n\t"
        "v_exp_f32 v122, v122\n\t"
        "v_exp_f32 v123, v123\n\t"
        "v_exp_f32 v124, v124\n\t"
        "v_smfmac_f32_32x32x32_bf16 v[16:31], v[160:163], v[112:119], v148\n\t"
        "v_exp_f32 v125, v125\n\t"
        "v_exp_f32 v126, v126\n\t"
        "v_exp_f32 v127, v127\n\t"
        "v_smfmac_f32_16x16x64_bf16 v[132:135], v[144:147], v[112:119], v148\n\t"
        "buffer_load_dwordx4 v[208:211], v150, s[52:55], 0 offen offset:2048 sc1\n\t"
        "v_exp_f32 v96, v96\n\t"
        "v_exp_f32 v97, v97\n\t"
        "v_smfmac_f32_32x32x32_bf16 v[16:31], v[164:167], v[120:127], v148\n\t"
        "v_exp_f32 v98, v98\n\t"
        "v_exp_f32 v99, v99\n\t"
        "v_exp_f32 v100, v100\n\t"
        "v_smfmac_f32_16x16x64_bf16 v[132:135], v[144:147], v[120:127], v148\n\t"
        "v_exp_f32 v101, v101\n\t"
        "v_exp_f32 v102, v102\n\t"
        "v_mfma_f32_32x32x16_bf16 v[112:127], v[152:155], v[88:91], 0\n\t"
        "v_exp_f32 v103, v103\n\t"
        "v_exp_f32 v104, v104\n\t"
        "v_exp_f32 v105, v105\n\t"
        "v_mfma_f32_32x32x16_bf16 v[112:127], v[156:159], v[92:95], v[112:127]\n\t"
        "v_exp_f32 v106, v106\n\t"
        "v_exp_f32 v107, v107\n\t"
        "v_exp_f32 v108, v108\n\t"
        "v_smfmac_f32_32x32x32_bf16 v[32:47], v[160:163], v[96:103], v148\n\t"
        "v_exp_f32 v109, v109\n\t"
        "v_exp_f32 v110, v110\n\t"
        "v_exp_f32 v111, v111\n\t"
        "v_smfmac_f32_16x16x64_bf16 v[136:139], v[144:147], v[96:103], v148\n\t"
        "buffer_load_dwordx4 v[212:215], v150, s[52:55], 0 offen offset:3072 sc1\n\t"
        "v_exp_f32 v112, v112\n\t"
        "v_exp_f32 v113, v113\n\t"
        "v_smfmac_f32_32x32x32_bf16 v[32:47], v[164:167], v[104:111], v148\n\t"
        "s_add_u32 s44, s44, 0x4000\n\t"
        "s_and_b32 s44, s44, 0x3ffff\n\t"
        "v_exp_f32 v114, v114\n\t"
        "v_exp_f32 v115, v115\n\t"
        "v_exp_f32 v116, v116\n\t"
        "v_smfmac_f32_16x16x64_bf16 v[136:139], v[144:147], v[104:111], v148\n\t"
        "s_waitcnt vmcnt(8)\n\t"
        "v_exp_f32 v117, v117\n\t"
        "v_exp_f32 v118, v118\n\t"
        "v_mfma_f32_32x32x16_bf16 v[96:111], v[168:171], v[64:67], 0\n\t"
        "s_cmp_eq_u32 s44, s49\n\t"
        "v_exp_f32 v119, v119\n\t"
        "v_exp_f32 v120, v120\n\t"
        "v_exp_f32 v121, v121\n\t"
        "v_mfma_f32_32x32x16_bf16 v[96:111], v[172:175], v[68:71], v[96:111]\n\t"
        "s_cselect_b32 s44, s50, s44\n\t"
        "v_exp_f32 v122, v122\n\t"
        "v_exp_f32 v123, v123\n\t"
        "v_exp_f32 v124, v124\n\t"
        "v_smfmac_f32_32x32x32_bf16 v[48:63], v[160:163], v[112:119], v148\n\t"
        "s_add_u32 s52, s40, s44\n\t"
        "s_addc_u32 s53, s41, 0\n\t"
        "v_exp_f32 v125, v125\n\t"
        "v_exp_f32 v126, v126\n\t"
        "v_exp_f32 v127, v127\n\t"
        "v_smfmac_f32_16x16x64_bf16 v[140:143], v[144:147], v[112:119], v148\n\t"
        "buffer_load_dwordx4 v[152:155], v149, s[52:55], 0 offen offset:0 sc1\n\t"
        "v_exp_f32 v96, v96\n\t"
        "v_exp_f32 v97, v97\n\t"
        "v_smfmac_f32_32x32x32_bf16 v[48:63], v[164:167], v[120:127], v148\n\t"
        "v_exp_f32 v98, v98\n\t"
        "v_exp_f32 v99, v99\n\t"
        "v_exp_f32 v100, v100\n\t"
        "v_smfmac_f32_16x16x64_bf16 v[140:143], v[144:147], v[120:127], v148\n\t"
        "v_exp_f32 v101, v101\n\t"
        "v_exp_f32 v102, v102\n\t"
        "v_mfma_f32_32x32x16_bf16 v[112:127], v[168:171], v[72:75], 0\n\t"
        "v_exp_f32 v103, v103\n\t"
        "v_exp_f32 v104, v104\n\t"
        "v_exp_f32 v105, v105\n\t"
        "v_mfma_f32_32x32x16_bf16 v[112:127], v[172:175], v[76:79], v[112:127]\n\t"
        "v_exp_f32 v106, v106\n\t"
        "v_exp_f32 v107, v107\n\t"
        "v_exp_f32 v108, v108\n\t"
        "v_smfmac_f32_32x32x32_bf16 v[0:15], v[176:179], v[96:103], v148\n\t"
        "v_exp_f32 v109, v109\n\t"
        "v_exp_f32 v110, v110\n\t"
        "v_exp_f32 v111, v111\n\t"
        "v_smfmac_f32_16x16x64_bf16 v[128:131], v[144:147], v[96:103], v148\n\t"
        "buffer_load_dwordx4 v[156:159], v149, s[52:55], 0 offen offset:1024 sc1\n\t"
        "v_exp_f32 v112, v112\n\t"
        "v_exp_f32 v113, v113\n\t"
        "v_smfmac_f32_32x32x32_bf16 v[0:15], v[180:183], v[104:111], v148\n\t"
        "v_exp_f32 v114, v114\n\t"
        "v_exp_f32 v115, v115\n\t"
        "v_exp_f32 v116, v116\n\t"
        "v_smfmac_f32_16x16x64_bf16 v[128:131], v[144:147], v[104:111], v148\n\t"
        "v_exp_f32 v117, v117\n\t"
        "v_exp_f32 v118, v118\n\t"
        "v_mfma_f32_32x32x16_bf16 v[96:111], v[168:171], v[80:83], 0\n\t"
        "v_exp_f32 v119, v119\n\t"
        "v_exp_f32 v120, v120\n\t"
        "v_exp_f32 v121, v121\n\t"
        "v_mfma_f32_32x32x16_bf16 v[96:111], v[172:175], v[84:87], v[96:111]\n\t"
        "v_exp_f32 v122, v122\n\t"
        "v_exp_f32 v123, v123\n\t"
        "v_exp_f32 v124, v124\n\t"
        "v_smfmac_f32_32x32x32_bf16 v[16:31], v[176:179], v[112:119], v148\n\t"
        "v_exp_f32 v125, v125\n\t"
        "v_exp_f32 v126, v126\n\t"
        "v_exp_f32 v127, v127\n\t"
        "v_smfmac_f32_16x16x64_bf16 v[132:135], v[144:147], v[112:119], v148\n\t"
        "buffer_load_dwordx4 v[160:163], v149, s[52:55], 0 offen offset:2048 sc1\n\t"
        "v_exp_f32 v96, v96\n\t"
        "v_exp_f32 v97, v97\n\t"
        "v_smfmac_f32_32x32x32_bf16 v[16:31], v[180:183], v[120:127], v148\n\t"
        "v_exp_f32 v98, v98\n\t"
        "v_exp_f32 v99, v99\n\t"
        "v_exp_f32 v100, v100\n\t"
        "v_smfmac_f32_16x16x64_bf16 v[132:135], v[144:147], v[120:127], v148\n\t"
        "v_exp_f32 v101, v101\n\t"
        "v_exp_f32 v102, v102\n\t"
        "v_mfma_f32_32x32x16_bf16 v[112:127], v[168:171], v[88:91], 0\n\t"
        "v_exp_f32 v103, v103\n\t"
        "v_exp_f32 v104, v104\n\t"
        "v_exp_f32 v105, v105\n\t"
        "v_mfma_f32_32x32x16_bf16 v[112:127], v[172:175], v[92:95], v[112:127]\n\t"
        "v_exp_f32 v106, v106\n\t"
        "v_exp_f32 v107, v107\n\t"
        "v_exp_f32 v108, v108\n\t"
        "v_smfmac_f32_32x32x32_bf16 v[32:47], v[176:179], v[96:103], v148\n\t"
        "s_sub_u32 s45, s45, 1\n\t"
        "v_exp_f32 v109, v109\n\t"
        "v_exp_f32 v110, v110\n\t"
        "v_exp_f32 v111, v111\n\t"
        "v_smfmac_f32_16x16x64_bf16 v[136:139], v[144:147], v[96:103], v148\n\t"
        "buffer_load_dwordx4 v[164:167], v149, s[52:55], 0 offen offset:3072 sc1\n\t"
        "s_cbranch_scc0 1b\n\t"
        "v_exp_f32 v112, v112\n\t"
        "v_exp_f32 v113, v113\n\t"
        "v_smfmac_f32_32x32x32_bf16 v[32:47], v[180:183], v[104:111], v148\n\t"
        "v_exp_f32 v114, v114\n\t"
        "v_exp_f32 v115, v115\n\t"
        "v_exp_f32 v116, v116\n\t"
        "v_smfmac_f32_16x16x64_bf16 v[136:139], v[144:147], v[104:111], v148\n\t"
        "s_waitcnt vmcnt(8)\n\t"
        "v_exp_f32 v117, v117\n\t"
        "v_exp_f32 v118, v118\n\t"
        "v_mfma_f32_32x32x16_bf16 v[96:111], v[184:187], v[64:67], 0\n\t"
        "v_exp_f32 v119, v119\n\t"
        "v_exp_f32 v120, v120\n\t"
        "v_exp_f32 v121, v121\n\t"
        "v_mfma_f32_32x32x16_bf16 v[96:111], v[188:191], v[68:71], v[96:111]\n\t"
        "v_exp_f32 v122, v122\n\t"
        "v_exp_f32 v123, v123\n\t"
        "v_exp_f32 v124, v124\n\t"
        "v_smfmac_f32_32x32x32_bf16 v[48:63], v[176:179], v[112:119], v148\n\t"
        "v_exp_f32 v125, v125\n\t"
        "v_exp_f32 v126, v126\n\t"
        "v_exp_f32 v127, v127\n\t"
        "v_smfmac_f32_16x16x64_bf16 v[140:143], v[144:147], v[112:119], v148\n\t"
        "v_exp_f32 v96, v96\n\t"
        "v_exp_f32 v97, v97\n\t"
        "v_smfmac_f32_32x32x32_bf16 v[48:63], v[180:183], v[120:127], v148\n\t"
        "v_exp_f32 v98, v98\n\t"
        "v_exp_f32 v99, v99\n\t"
        "v_exp_f32 v100, v100\n\t"
        "v_smfmac_f32_16x16x64_bf16 v[140:143], v[144:147], v[120:127], v148\n\t"
        "v_exp_f32 v101, v101\n\t"
        "v_exp_f32 v102, v102\n\t"
        "v_mfma_f32_32x32x16_bf16 v[112:127], v[184:187], v[72:75], 0\n\t"
        "v_exp_f32 v103, v103\n\t"
        "v_exp_f32 v104, v104\n\t"
        "v_exp_f32 v105, v105\n\t"
        "v_mfma_f32_32x32x16_bf16 v[112:127], v[188:191], v[76:79], v[112:127]\n\t"
        "v_exp_f32 v106, v106\n\t"
        "v_exp_f32 v107, v107\n\t"
        "v_exp_f32 v108, v108\n\t"
        "v_smfmac_f32_32x32x32_bf16 v[0:15], v[192:195], v[96:103], v148\n\t"
        "v_exp_f32 v109, v109\n\t"
        "v_exp_f32 v110, v110\n\t"
        "v_exp_f32 v111, v111\n\t"
        "v_smfmac_f32_16x16x64_bf16 v[128:131], v[144:147], v[96:103], v148\n\t"
        "v_exp_f32 v112, v112\n\t"
        "v_exp_f32 v113, v113\n\t"
        "v_smfmac_f32_32x32x32_bf16 v[0:15], v[196:199], v[104:111], v148\n\t"
        "v_exp_f32 v114, v114\n\t"
        "v_exp_f32 v115, v115\n\t"
        "v_exp_f32 v116, v116\n\t"
        "v_smfmac_f32_16x16x64_bf16 v[128:131], v[144:147], v[104:111], v148\n\t"
        "s_waitcnt vmcnt(4)\n\t"
        "v_exp_f32 v117, v117\n\t"
        "v_exp_f32 v118, v118\n\t"
        "v_mfma_f32_32x32x16_bf16 v[96:111], v[200:203], v[64:67], 0\n\t"
        "v_exp_f32 v119, v119\n\t"
        "v_exp_f32 v120, v120\n\t"
        "v_exp_f32 v121, v121\n\t"
        "v_mfma_f32_32x32x16_bf16 v[96:111], v[204:207], v[68:71], v[96:111]\n\t"
        "v_exp_f32 v122, v122\n\t"
        "v_exp_f32 v123, v123\n\t"
        "v_exp_f32 v124, v124\n\t"
        "v_smfmac_f32_32x32x32_bf16 v[16:31], v[192:195], v[112:119], v148\n\t"
        "v_exp_f32 v125, v125\n\t"
        "v_exp_f32 v126, v126\n\t"
        "v_exp_f32 v127, v127\n\t"
        "v_smfmac_f32_16x16x64_bf16 v[132:135], v[144:147], v[112:119], v148\n\t"
        "s_cmp_lg_u32 s47, 0\n\t"
        "s_cbranch_scc1 4f\n\t"
        "global_load_dwordx4 %[r0], v223, %[xb] offset:0\n\t"
        "global_load_dwordx4 %[r1], v223, %[xb] offset:32\n\t"
        "global_load_dwordx4 %[r2], v223, %[xb] offset:64\n\t"
        "global_load_dwordx4 %[r3], v223, %[xb] offset:96\n\t"
        "4:\n\t"
        "v_exp_f32 v96, v96\n\t"
        "v_exp_f32 v97, v97\n\t"
        "v_smfmac_f32_32x32x32_bf16 v[16:31], v[196:199], v[120:127], v148\n\t"
        "v_exp_f32 v98, v98\n\t"
        "v_exp_f32 v99, v99\n\t"
        "v_exp_f32 v100, v100\n\t"
        "v_smfmac_f32_16x16x64_bf16 v[132:135], v[144:147], v[120:127], v148\n\t"
        "v_exp_f32 v101, v101\n\t"
        "v_exp_f32 v102, v102\n\t"
        "v_mfma_f32_32x32x16_bf16 v[112:127], v[200:203], v[72:75], 0\n\t"
        "v_exp_f32 v103, v103\n\t"
        "v_exp_f32 v104, v104\n\t"
        "v_exp_f32 v105, v105\n\t"
        "v_mfma_f32_32x32x16_bf16 v[112:127], v[204:207], v[76:79], v[112:127]\n\t"
        "v_exp_f32 v106, v106\n\t"
        "v_exp_f32 v107, v107\n\t"
        "v_exp_f32 v108, v108\n\t"
        "v_smfmac_f32_32x32x32_bf16 v[0:15], v[208:211], v[96:103], v148\n\t"
        "v_exp_f32 v109, v109\n\t"
        "v_exp_f32 v110, v110\n\t"
        "v_exp_f32 v111, v111\n\t"
        "v_smfmac_f32_16x16x64_bf16 v[128:131], v[144:147], v[96:103], v148\n\t"
        "v_exp_f32 v112, v112\n\t"
        "v_exp_f32 v113, v113\n\t"
        "v_smfmac_f32_32x32x32_bf16 v[0:15], v[212:215], v[104:111], v148\n\t"
        "v_exp_f32 v114, v114\n\t"
        "v_exp_f32 v115, v115\n\t"
        "v_exp_f32 v116, v116\n\t"
        "v_smfmac_f32_16x16x64_bf16 v[128:131], v[144:147], v[104:111], v148\n\t"
        "v_exp_f32 v117, v117\n\t"
        "v_exp_f32 v118, v118\n\t"
        "v_mfma_f32_32x32x16_bf16 v[96:111], v[184:187], v[80:83], 0\n\t"
        "v_exp_f32 v119, v119\n\t"
        "v_exp_f32 v120, v120\n\t"
        "v_exp_f32 v121, v121\n\t"
        "v_mfma_f32_32x32x16_bf16 v[96:111], v[188:191], v[84:87], v[96:111]\n\t"
        "ds_write_b128 v216, v[0:3] offset:0\n\t"
        "ds_write_b128 v216, v[4:7] offset:1024\n\t"
        "v_exp_f32 v122, v122\n\t"
        "v_exp_f32 v123, v123\n\t"
        "v_exp_f32 v124, v124\n\t"
        "v_smfmac_f32_32x32x32_bf16 v[16:31], v[208:211], v[112:119], v148\n\t"
        "ds_write_b128 v216, v[8:11] offset:2048\n\t"
        "ds_write_b128 v216, v[12:15] offset:3072\n\t"
        "v_exp_f32 v125, v125\n\t"
        "v_exp_f32 v126, v126\n\t"
        "v_exp_f32 v127, v127\n\t"
        "v_smfmac_f32_16x16x64_bf16 v[132:135], v[144:147], v[112:119], v148\n\t"
        "ds_write_b32 v218, v128 offset:0\n\t"
        "v_exp_f32 v96, v96\n\t"
        "v_exp_f32 v97, v97\n\t"
        "v_smfmac_f32_32x32x32_bf16 v[16:31], v[212:215], v[120:127], v148\n\t"
        "v_exp_f32 v98, v98\n\t"
        "v_exp_f32 v99, v99\n\t"
        "v_exp_f32 v100, v100\n\t"
        "v_smfmac_f32_16x16x64_bf16 v[132:135], v[144:147], v[120:127], v148\n\t"
        "v_exp_f32 v101, v101\n\t"
        "v_exp_f32 v102, v102\n\t"
        "v_mfma_f32_32x32x16_bf16 v[112:127], v[184:187], v[88:91], 0\n\t"
        "v_exp_f32 v103, v103\n\t"
        "v_exp_f32 v104, v104\n\t"
        "v_exp_f32 v105, v105\n\t"
        "v_mfma_f32_32x32x16_bf16 v[112:127], v[188:191], v[92:95], v[112:127]\n\t"
        "ds_write_b128 v216, v[16:19] offset:4096\n\t"
        "ds_write_b128 v216, v[20:23] offset:5120\n\t"
        "v_exp_f32 v106, v106\n\t"
        "v_exp_f32 v107, v107\n\t"
        "v_exp_f32 v108, v108\n\t"
        "v_smfmac_f32_32x32x32_bf16 v[32:47], v[192:195], v[96:103], v148\n\t"
        "ds_write_b128 v216, v[24:27] offset:6144\n\t"
        "ds_write_b128 v216, v[28:31] offset:7168\n\t"
        "v_exp_f32 v109, v109\n\t"
        "v_exp_f32 v110, v110\n\t"
        "v_exp_f32 v111, v111\n\t"
        "v_smfmac_f32_16x16x64_bf16 v[136:139], v[144:147], v[96:103], v148\n\t"
        "ds_write_b32 v218, v132 offset:256\n\t"
        "ds_write_b32 v220, v222\n\t"
        "v_exp_f32 v112, v112\n\t"
        "v_exp_f32 v113, v113\n\t"
        "v_smfmac_f32_32x32x32_bf16 v[32:47], v[196:199], v[104:111], v148\n\t"
        "v_exp_f32 v114, v114\n\t"
        "v_exp_f32 v115, v115\n\t"
        "v_exp_f32 v116, v116\n\t"
        "v_smfmac_f32_16x16x64_bf16 v[136:139], v[144:147], v[104:111], v148\n\t"
        "v_exp_f32 v117, v117\n\t"
        "v_exp_f32 v118, v118\n\t"
        "v_mfma_f32_32x32x16_bf16 v[96:111], v[200:203], v[80:83], 0\n\t"
        "v_exp_f32 v119, v119\n\t"
        "v_exp_f32 v120, v120\n\t"
        "v_exp_f32 v121, v121\n\t"
        "v_mfma_f32_32x32x16_bf16 v[96:111], v[204:207], v[84:87], v[96:111]\n\t"
        "v_exp_f32 v122, v122\n\t"
        "v_exp_f32 v123, v123\n\t"
        "v_exp_f32 v124, v124\n\t"
        "v_smfmac_f32_32x32x32_bf16 v[48:63], v[192:195], v[112:119], v148\n\t"
        "v_exp_f32 v125, v125\n\t"
        "v_exp_f32 v126, v126\n\t"
        "v_exp_f32 v127, v127\n\t"
        "v_smfmac_f32_16x16x64_bf16 v[140:143], v[144:147], v[112:119], v148\n\t"
        "v_exp_f32 v96, v96\n\t"
        "v_exp_f32 v97, v97\n\t"
        "v_smfmac_f32_32x32x32_bf16 v[48:63], v[196:199], v[120:127], v148\n\t"
        "v_exp_f32 v98, v98\n\t"
        "v_exp_f32 v99, v99\n\t"
        "v_exp_f32 v100, v100\n\t"
        "v_smfmac_f32_16x16x64_bf16 v[140:143], v[144:147], v[120:127], v148\n\t"
        "v_exp_f32 v101, v101\n\t"
        "v_exp_f32 v102, v102\n\t"
        "v_mfma_f32_32x32x16_bf16 v[112:127], v[200:203], v[88:91], 0\n\t"
        "v_exp_f32 v103, v103\n\t"
        "v_exp_f32 v104, v104\n\t"
        "v_exp_f32 v105, v105\n\t"
        "v_mfma_f32_32x32x16_bf16 v[112:127], v[204:207], v[92:95], v[112:127]\n\t"
        "ds_read_b32 v151, v221\n\t"
        "v_exp_f32 v106, v106\n\t"
        "v_exp_f32 v107, v107\n\t"
        "v_exp_f32 v108, v108\n\t"
        "v_smfmac_f32_32x32x32_bf16 v[32:47], v[208:211], v[96:103], v148\n\t"
        "v_exp_f32 v109, v109\n\t"
        "v_exp_f32 v110, v110\n\t"
        "v_exp_f32 v111, v111\n\t"
        "v_smfmac_f32_16x16x64_bf16 v[136:139], v[144:147], v[96:103], v148\n\t"
        "s_waitcnt lgkmcnt(0)\n\t"
        "v_cmp_ne_u32 vcc, v222, v151\n\t"
        "s_cbranch_vccnz 40f\n\t"
        "41:\n\t"
        "v_exp_f32 v112, v112\n\t"
        "v_exp_f32 v113, v113\n\t"
        "v_smfmac_f32_32x32x32_bf16 v[32:47], v[212:215], v[104:111], v148\n\t"
        "ds_read_b128 v[0:3], v217 offset:0\n\t"
        "ds_read_b128 v[4:7], v217 offset:1024\n\t"
        "ds_read_b128 v[8:11], v217 offset:2048\n\t"
        "ds_read_b128 v[12:15], v217 offset:3072\n\t"
        "v_exp_f32 v114, v114\n\t"
        "v_exp_f32 v115, v115\n\t"
        "v_exp_f32 v116, v116\n\t"
        "v_smfmac_f32_16x16x64_bf16 v[136:139], v[144:147], v[104:111], v148\n\t"
        "ds_read_b32 v128, v219 offset:0\n\t"
        "ds_read_b128 v[16:19], v217 offset:4096\n\t"
        "ds_read_b128 v[20:23], v217 offset:5120\n\t"
        "v_exp_f32 v117, v117\n\t"
        "v_exp_f32 v118, v118\n\t"
        "v_exp_f32 v119, v119\n\t"
        "v_exp_f32 v120, v120\n\t"
        "v_exp_f32 v121, v121\n\t"
        "v_exp_f32 v122, v122\n\t"
        "v_exp_f32 v123, v123\n\t"
        "v_exp_f32 v124, v124\n\t"
        "v_smfmac_f32_32x32x32_bf16 v[48:63], v[208:211], v[112:119], v148\n\t"
        "ds_read_b128 v[24:27], v217 offset:6144\n\t"
        "ds_read_b128 v[28:31], v217 offset:7168\n\t"
        "ds_read_b32 v132, v219 offset:256\n\t"
        "v_exp_f32 v125, v125\n\t"
        "v_exp_f32 v126, v126\n\t"
        "v_exp_f32 v127, v127\n\t"
        "v_smfmac_f32_16x16x64_bf16 v[140:143], v[144:147], v[112:119], v148\n\t"
        "s_nop 1\n\t"
        "v_smfmac_f32_32x32x32_bf16 v[48:63], v[212:215], v[120:127], v148\n\t"
        "v_smfmac_f32_16x16x64_bf16 v[140:143], v[144:147], v[120:127], v148\n\t"
        "s_waitcnt vmcnt(0) lgkmcnt(0)\n\t"
        "s_nop 6\n\t"
        "s_branch 9f\n\t"
        "24:\n\t"
        "s_add_u32 s44, s44, 0x4000\n\t"
        "s_and_b32 s44, s44, 0x3ffff\n\t"
        "s_cmp_eq_u32 s44, s49\n\t"
        "s_cselect_b32 s44, s50, s44\n\t"
        "s_add_u32 s52, s40, s44\n\t"
        "s_addc_u32 s53, s41, 0\n\t"
        "buffer_load_dwordx4 v[184:187], v149, s[52:55], 0 offen offset:0 sc1\n\t"
        "buffer_load_dwordx4 v[188:191], v149, s[52:55], 0 offen offset:1024 sc1\n\t"
        "buffer_load_dwordx4 v[192:195], v149, s[52:55], 0 offen offset:2048 sc1\n\t"
        "buffer_load_dwordx4 v[196:199], v149, s[52:55], 0 offen offset:3072 sc1\n\t"
        "buffer_load_dwordx4 v[200:203], v150, s[52:55], 0 offen offset:0 sc1\n\t"
        "buffer_load_dwordx4 v[204:207], v150, s[52:55], 0 offen offset:1024 sc1\n\t"
        "buffer_load_dwordx4 v[208:211], v150, s[52:55], 0 offen offset:2048 sc1\n\t"
        "buffer_load_dwordx4 v[212:215], v150, s[52:55], 0 offen offset:3072 sc1\n\t"
        "s_mov_b32 s46, 1\n\t"
        "s_branch 25b\n\t"
        "26:\n\t"
        "s_add_u32 s44, s44, 0x4000\n\t"
        "s_and_b32 s44, s44, 0x3ffff\n\t"
        "s_cmp_eq_u32 s44, s49\n\t"
        "s_cselect_b32 s44, s50, s44\n\t"
        "s_add_u32 s52, s40, s44\n\t"
        "s_addc_u32 s53, s41, 0\n\t"
        "buffer_load_dwordx4 v[184:187], v149, s[52:55], 0 offen offset:0 sc1\n\t"
        "buffer_load_dwordx4 v[188:191], v149, s[52:55], 0 offen offset:1024 sc1\n\t"
        "buffer_load_dwordx4 v[192:195], v149, s[52:55], 0 offen offset:2048 sc1\n\t"
        "buffer_load_dwordx4 v[196:199], v149, s[52:55], 0 offen offset:3072 sc1\n\t"
        "buffer_load_dwordx4 v[200:203], v150, s[52:55], 0 offen offset:0 sc1\n\t"
        "buffer_load_dwordx4 v[204:207], v150, s[52:55], 0 offen offset:1024 sc1\n\t"
        "buffer_load_dwordx4 v[208:211], v150, s[52:55], 0 offen offset:2048 sc1\n\t"
        "buffer_load_dwordx4 v[212:215], v150, s[52:55], 0 offen offset:3072 sc1\n\t"
        "s_mov_b32 s46, 1\n\t"
        "s_branch 27b\n\t"
        "28:\n\t"
        "s_add_u32 s44, s44, 0x4000\n\t"
        "s_and_b32 s44, s44, 0x3ffff\n\t"
        "s_cmp_eq_u32 s44, s49\n\t"
        "s_cselect_b32 s44, s50, s44\n\t"
        "s_add_u32 s52, s40, s44\n\t"
        "s_addc_u32 s53, s41, 0\n\t"
        "buffer_load_dwordx4 v[184:187], v149, s[52:55], 0 offen offset:0 sc1\n\t"
        "buffer_load_dwordx4 v[188:191], v149, s[52:55], 0 offen offset:1024 sc1\n\t"
        "buffer_load_dwordx4 v[192:195], v149, s[52:55], 0 offen offset:2048 sc1\n\t"
        "buffer_load_dwordx4 v[196:199], v149, s[52:55], 0 offen offset:3072 sc1\n\t"
        "buffer_load_dwordx4 v[200:203], v150, s[52:55], 0 offen offset:0 sc1\n\t"
        "buffer_load_dwordx4 v[204:207], v150, s[52:55], 0 offen offset:1024 sc1\n\t"
        "buffer_load_dwordx4 v[208:211], v150, s[52:55], 0 offen offset:2048 sc1\n\t"
        "buffer_load_dwordx4 v[212:215], v150, s[52:55], 0 offen offset:3072 sc1\n\t"
        "s_mov_b32 s46, 1\n\t"
        "s_branch 29b\n\t"
        "30:\n\t"
        "s_add_u32 s44, s44, 0x4000\n\t"
        "s_and_b32 s44, s44, 0x3ffff\n\t"
        "s_cmp_eq_u32 s44, s49\n\t"
        "s_cselect_b32 s44, s50, s44\n\t"
        "s_add_u32 s52, s40, s44\n\t"
        "s_addc_u32 s53, s41, 0\n\t"
        "buffer_load_dwordx4 v[184:187], v149, s[52:55], 0 offen offset:0 sc1\n\t"
        "buffer_load_dwordx4 v[188:191], v149, s[52:55], 0 offen offset:1024 sc1\n\t"
        "buffer_load_dwordx4 v[192:195], v149, s[52:55], 0 offen offset:2048 sc1\n\t"
        "buffer_load_dwordx4 v[196:199], v149, s[52:55], 0 offen offset:3072 sc1\n\t"
        "buffer_load_dwordx4 v[200:203], v150, s[52:55], 0 offen offset:0 sc1\n\t"
        "buffer_load_dwordx4 v[204:207], v150, s[52:55], 0 offen offset:1024 sc1\n\t"
        "buffer_load_dwordx4 v[208:211], v150, s[52:55], 0 offen offset:2048 sc1\n\t"
        "buffer_load_dwordx4 v[212:215], v150, s[52:55], 0 offen offset:3072 sc1\n\t"
        "s_mov_b32 s46, 1\n\t"
        "s_branch 31b\n\t"
        "32:\n\t"
        "s_add_u32 s44, s44, 0x4000\n\t"
        "s_and_b32 s44, s44, 0x3ffff\n\t"
        "s_cmp_eq_u32 s44, s49\n\t"
        "s_cselect_b32 s44, s50, s44\n\t"
        "s_add_u32 s52, s40, s44\n\t"
        "s_addc_u32 s53, s41, 0\n\t"
        "buffer_load_dwordx4 v[184:187], v149, s[52:55], 0 offen offset:0 sc1\n\t"
        "buffer_load_dwordx4 v[188:191], v149, s[52:55], 0 offen offset:1024 sc1\n\t"
        "buffer_load_dwordx4 v[192:195], v149, s[52:55], 0 offen offset:2048 sc1\n\t"
        "buffer_load_dwordx4 v[196:199], v149, s[52:55], 0 offen offset:3072 sc1\n\t"
        "buffer_load_dwordx4 v[200:203], v150, s[52:55], 0 offen offset:0 sc1\n\t"
        "buffer_load_dwordx4 v[204:207], v150, s[52:55], 0 offen offset:1024 sc1\n\t"
        "buffer_load_dwordx4 v[208:211], v150, s[52:55], 0 offen offset:2048 sc1\n\t"
        "buffer_load_dwordx4 v[212:215], v150, s[52:55], 0 offen offset:3072 sc1\n\t"
        "s_mov_b32 s46, 1\n\t"
        "s_branch 33b\n\t"
        "40:\n\t"
        "s_mov_b32 s45, 0x100000\n\t"
        "42:\n\t"
        "ds_read_b32 v151, v221\n\t"
        "s_sub_u32 s45, s45, 1\n\t"
        "s_waitcnt lgkmcnt(0)\n\t"
        "s_cbranch_scc1 41b\n\t"
        "v_cmp_ne_u32 vcc, v222, v151\n\t"
        "s_cbranch_vccnz 42b\n\t"
        "s_branch 41b\n\t"
        "9:\n\t"
        : "+{v[0:15]}"(o0), "+{v[16:31]}"(o1), "+{v[32:47]}"(o2), "+{v[48:63]}"(o3), "+{v[128:143]}"(lv), "+{s44}"(soff), "+{v[152:167]}"(kva), "+{v[168:183]}"(kvb), "+{v[184:191]}"(fv),
          [r0] "+v"(x1p[0]), [r1] "+v"(x1p[1]), [r2] "+v"(x1p[2]), [r3] "+v"(x1p[3]) STAMP_OUTS
        : "{v[64:71]}"(q[0]), "{v[72:79]}"(q[1]), "{v[80:87]}"(q[2]), "{v[88:95]}"(q[3]), "{v[144:147]}"(ones), "{v148}"(0xddddu), "{v149}"(kvoff), "{v150}"(kvoff + 4096),
          "{v[216:223]}"(cv), "{v224}"(lbase + L_W + 16), "{s[40:43]}"(srd), "{s47}"(L), "{s49}"(tm * 16384), "{s50}"(((tm + 1) & 15) * 16384), [xb] "s"(x0)
        : "v96", "v97", "v98", "v99", "v100", "v101", "v102", "v103", "v104", "v105", "v106", "v107", "v108", "v109", "v110", "v111", "v112", "v113", "v114", "v115", "v116", "v117", "v118", "v119", "v120", "v121", "v122", "v123", "v124", "v125", "v126", "v127", "v151", "v192", "v193", "v194", "v195", "v196", "v197", "v198", "v199", "v200", "v201", "v202", "v203", "v204", "v205", "v206", "v207", "v208", "v209", "v210", "v211", "v212", "v213", "v214", "v215", "s45", "s46", "s52", "s53", "s54", "s55", "vcc", "scc", "memory");
      STAMP_PUT(6 + 16 * L);
      STAMPV(9 + 16 * L);
      PHASE_VARS;
      const unsigned dst = OFF_OF + L * (5 * MBY) + (tm * 4 + 2 * kh) * TS + (tn * 2 + hh) * 2048 + lane * 16;
      auto put = [&](int u, const f32x16& o) {
#pragma unroll
        for (int c = 0; c < 2; ++c) {
          u32x4 w;
#pragma unroll
          for (int e = 0; e < 4; ++e) w[e] = pack2(o[8 * c + 2 * e], o[8 * c + 2 * e + 1]);
          stg(rs, dst + u * TS + c * 1024, w);
        }
      };
      const float la = lv[8] + lv[0], lb = lv[12] + lv[4];
      o2 = (o2 + o0) * RCP(la); put(0, o2);
      SBAR();
      o3 = (o3 + o1) * RCP(lb); put(1, o3);
      SBAR();
      {
        const f32x16 t = o2 + o3;
        float sum = 0.f;
#pragma unroll
        for (int e = 0; e < 16; e += 4) sum += (t[e] + t[e + 1]) + (t[e + 2] + t[e + 3]);
        if (__any(!(fabsf(sum) < 1e37f) | !(fmaxf(la, lb) < 1e27f) | !(fminf(la, lb) > 1e-9f) | !(la + lb == la + lb))) {
          DRAIN();
          attn_safe(rs, kvoff - kh * 8192, q[2], ones, o2); put(0, o2);
          attn_safe(rs, kvoff - kh * 8192, q[3], ones, o3); put(1, o3);
        }
      }
    }
    STAMPV(10 + 16 * L);
    {
      f32x16 acc[8];
      u32x16 wrc;
      u32x16 fl0, fl1, fl2;
      STAMP_DECL;
      { PHASE_VARS;
      float r0 = 0.f, r1 = 1.f;
      if (L == 0) {
#pragma unroll
        for (int i = 0; i < 4; ++i) x1p[i] = fo_swap(x1p[i]);
      } else {
        r0 = reinterpret_cast<const float*>(smem + L_AB)[256 + wid * 64 + r32]; r1 = reinterpret_cast<const float*>(smem + L_AB)[256 + wid * 64 + 32 + r32];
      }
#pragma unroll
      for (int e = 0; e < 16; ++e) { acc[e >> 3][2 * (e & 7)] = lo16(x1p[e >> 2][e & 3]) * r1 + r0; acc[e >> 3][2 * (e & 7) + 1] = hi16(x1p[e >> 2][e & 3]) * r1 + r0; }
      PIN(acc[0]); PIN(acc[1]);
      asm volatile("s_waitcnt vmcnt(0)\n\tds_write_b32 %0, %1" :: "v"(lbase + L_W + 80 + 4 * wid), "v"(2 * L + 2) : "memory");
      STAMPV(11 + 16 * L);
      const unsigned a0 = lbase + wrow * 128 + ((wch ^ wsw) << 4) + 8 * (tp & 1), a1 = lbase + wrow * 128 + (((4 + wch) ^ wsw) << 4) + 8 * (tp & 1);
      asm volatile(
        "s_mov_b32 s46, 0\n\t"
        "ostart_%=:\n\t"
        "s_mov_b32 s48, 0x200000\n\t"
        "2:\n\t"
        "ds_read_b32 v233, v232\n\t"
        "s_sub_u32 s48, s48, 1\n\t"
        "s_waitcnt lgkmcnt(0)\n\t"
        "s_cselect_b32 s47, 15, 0\n\t"
        "v_readfirstlane_b32 s51, v233\n\t"
        "s_nop 3\n\t"
        "s_or_b32 s51, s51, s47\n\t"
        "s_andn2_b32 s47, s51, s46\n\t"
        "s_cbranch_scc0 2b\n\t"
        "s_andn2_b32 s51, s51, s46\n\t"
        "s_ff1_i32_b32 s47, s51\n\t"
        "s_add_u32 s45, s47, s49\n\t"
        "s_and_b32 s45, s45, 3\n\t"
        "s_lshl_b32 s45, s45, 14\n\t"
        "s_and_b32 s51, s47, 0x80000000\n\t"
        "v_or_b32 v225, s51, v224\n\t"
        "s_and_b32 s51, s47, 3\n\t"
        "s_lshl_b32 s51, s51, 15\n\t"
        "v_add_u32 v230, s51, v226\n\t"
        "v_add_u32 v231, s51, v227\n\t"
        "buffer_load_dwordx4 v[160:163], v225, s[40:43], s45 offen offset:0 sc1\n\t"
        "buffer_load_dwordx4 v[164:167], v225, s[40:43], s45 offen offset:1024 sc1\n\t"
        "buffer_load_dwordx4 v[168:171], v225, s[40:43], s45 offen offset:2048 sc1\n\t"
        "buffer_load_dwordx4 v[172:175], v225, s[40:43], s45 offen offset:3072 sc1\n\t"
        "s_add_u32 s45, s45, 0x1000\n\t"
        "buffer_load_dwordx4 v[176:179], v225, s[40:43], s45 offen offset:0 sc1\n\t"
        "buffer_load_dwordx4 v[180:183], v225, s[40:43], s45 offen offset:1024 sc1\n\t"
        "buffer_load_dwordx4 v[184:187], v225, s[40:43], s45 offen offset:2048 sc1\n\t"
        "buffer_load_dwordx4 v[188:191], v225, s[40:43], s45 offen offset:3072 sc1\n\t"
        "s_add_u32 s45, s45, 0x1000\n\t"
        "buffer_load_dwordx4 v[192:195], v225, s[40:43], s45 offen offset:0 sc1\n\t"
        "buffer_load_dwordx4 v[196:199], v225, s[40:43], s45 offen offset:1024 sc1\n\t"
        "buffer_load_dwordx4 v[200:203], v225, s[40:43], s45 offen offset:2048 sc1\n\t"
        "buffer_load_dwordx4 v[204:207], v225, s[40:43], s45 offen offset:3072 sc1\n\t"
        "ds_read_b64_tr_b16 v[128:129], v230 offset:0\n\t"
        "ds_read_b64_tr_b16 v[130:131], v230 offset:1024\n\t"
        "ds_read_b64_tr_b16 v[132:133], v231 offset:0\n\t"
        "ds_read_b64_tr_b16 v[134:135], v231 offset:1024\n\t"
        "ds_read_b64_tr_b16 v[136:137], v230 offset:2048\n\t"
        "ds_read_b64_tr_b16 v[138:139], v230 offset:3072\n\t"
        "ds_read_b64_tr_b16 v[140:141], v231 offset:2048\n\t"
        "ds_read_b64_tr_b16 v[142:143], v231 offset:3072\n\t"
        "ds_read_b64_tr_b16 v[144:145], v230 offset:4096\n\t"
        "ds_read_b64_tr_b16 v[146:147], v230 offset:5120\n\t"
        "ds_read_b64_tr_b16 v[148:149], v231 offset:4096\n\t"
        "ds_read_b64_tr_b16 v[150:151], v231 offset:5120\n\t"
        "ds_read_b64_tr_b16 v[152:153], v230 offset:6144\n\t"
        "ds_read_b64_tr_b16 v[154:155], v230 offset:7168\n\t"
        "ds_read_b64_tr_b16 v[156:157], v231 offset:6144\n\t"
        "ds_read_b64_tr_b16 v[158:159], v231 offset:7168\n\t"
        "s_mov_b32 s44, s45\n\t"
        "s_cmp_eq_u32 s47, 0\n\t"
        "s_cbranch_scc1 oseg0_%=\n\t"
        "s_cmp_eq_u32 s47, 1\n\t"
        "s_cbranch_scc1 oseg1_%=\n\t"
        "s_cmp_eq_u32 s47, 2\n\t"
        "s_cbranch_scc1 oseg2_%=\n\t"
        "s_cmp_eq_u32 s47, 3\n\t"
        "s_cbranch_scc1 oseg3_%=\n\t"
        "oseg0_%=:\n\t"
        "s_or_b32 s46, s46, 1\n\t"
        "s_waitcnt vmcnt(11) lgkmcnt(14)\n\t"
        "v_mfma_f32_32x32x16_bf16 v[0:15], v[128:131], v[160:163], v[0:15]\n\t"
        "ds_read_b64_tr_b16 v[128:129], v226 offset:8192\n\t"
        "ds_read_b64_tr_b16 v[130:131], v226 offset:9216\n\t"
        "s_waitcnt lgkmcnt(14)\n\t"
        "v_mfma_f32_32x32x16_bf16 v[16:31], v[132:135], v[160:163], v[16:31]\n\t"
        "ds_read_b64_tr_b16 v[132:133], v227 offset:8192\n\t"
        "ds_read_b64_tr_b16 v[134:135], v227 offset:9216\n\t"
        "s_add_u32 s44, s44, 0x1000\n\t"
        "buffer_load_dwordx4 v[208:211], v224, s[40:43], s44 offen offset:0 sc1\n\t"
        "s_waitcnt vmcnt(11) lgkmcnt(14)\n\t"
        "v_mfma_f32_32x32x16_bf16 v[0:15], v[136:139], v[164:167], v[0:15]\n\t"
        "ds_read_b32 v233, v232\n\t"
        "ds_read_b64_tr_b16 v[136:137], v226 offset:10240\n\t"
        "ds_read_b64_tr_b16 v[138:139], v226 offset:11264\n\t"
        "s_waitcnt lgkmcnt(14)\n\t"
        "v_mfma_f32_32x32x16_bf16 v[16:31], v[140:143], v[164:167], v[16:31]\n\t"
        "ds_read_b64_tr_b16 v[140:141], v227 offset:10240\n\t"
        "ds_read_b64_tr_b16 v[142:143], v227 offset:11264\n\t"
        "buffer_load_dwordx4 v[212:215], v224, s[40:43], s44 offen offset:1024 sc1\n\t"
        "s_waitcnt vmcnt(11) lgkmcnt(14)\n\t"
        "v_mfma_f32_32x32x16_bf16 v[0:15], v[144:147], v[168:171], v[0:15]\n\t"
        "ds_read_b64_tr_b16 v[144:145], v226 offset:12288\n\t"
        "ds_read_b64_tr_b16 v[146:147], v226 offset:13312\n\t"
        "s_waitcnt lgkmcnt(14)\n\t"
        "v_mfma_f32_32x32x16_bf16 v[16:31], v[148:151], v[168:171], v[16:31]\n\t"
        "ds_read_b64_tr_b16 v[148:149], v227 offset:12288\n\t"
        "ds_read_b64_tr_b16 v[150:151], v227 offset:13312\n\t"
        "buffer_load_dwordx4 v[216:219], v224, s[40:43], s44 offen offset:2048 sc1\n\t"
        "s_waitcnt vmcnt(11) lgkmcnt(8)\n\t"
        "v_mfma_f32_32x32x16_bf16 v[0:15], v[152:155], v[172:175], v[0:15]\n\t"
        "v_readfirstlane_b32 s51, v233\n\t"
        "ds_read_b64_tr_b16 v[152:153], v226 offset:14336\n\t"
        "ds_read_b64_tr_b16 v[154:155], v226 offset:15360\n\t"
        "s_waitcnt lgkmcnt(14)\n\t"
        "v_mfma_f32_32x32x16_bf16 v[16:31], v[156:159], v[172:175], v[16:31]\n\t"
        "s_andn2_b32 s51, s51, s46\n\t"
        "s_ff1_i32_b32 s47, s51\n\t"
        "s_add_u32 s45, s47, s49\n\t"
        "s_and_b32 s45, s45, 3\n\t"
        "s_lshl_b32 s45, s45, 14\n\t"
        "ds_read_b64_tr_b16 v[156:157], v227 offset:14336\n\t"
        "ds_read_b64_tr_b16 v[158:159], v227 offset:15360\n\t"
        "buffer_load_dwordx4 v[220:223], v224, s[40:43], s44 offen offset:3072 sc1\n\t"
        "s_waitcnt vmcnt(11) lgkmcnt(14)\n\t"
        "v_mfma_f32_32x32x16_bf16 v[0:15], v[128:131], v[176:179], v[0:15]\n\t"
        "s_and_b32 s51, s47, 0x80000000\n\t"
        "v_or_b32 v225, s51, v224\n\t"
        "s_and_b32 s51, s47, 3\n\t"
        "s_lshl_b32 s51, s51, 15\n\t"
        "v_add_u32 v230, s51, v226\n\t"
        "v_add_u32 v231, s51, v227\n\t"
        "ds_read_b64_tr_b16 v[128:129], v226 offset:16384\n\t"
        "ds_read_b64_tr_b16 v[130:131], v226 offset:17408\n\t"
        "s_waitcnt lgkmcnt(14)\n\t"
        "v_mfma_f32_32x32x16_bf16 v[16:31], v[132:135], v[176:179], v[16:31]\n\t"
        "ds_read_b64_tr_b16 v[132:133], v227 offset:16384\n\t"
        "ds_read_b64_tr_b16 v[134:135], v227 offset:17408\n\t"
        "buffer_load_dwordx4 v[160:163], v225, s[40:43], s45 offen offset:0 sc1\n\t"
        "s_waitcnt vmcnt(11) lgkmcnt(14)\n\t"
        "v_mfma_f32_32x32x16_bf16 v[0:15], v[136:139], v[180:183], v[0:15]\n\t"
        "ds_read_b64_tr_b16 v[136:137], v226 offset:18432\n\t"
        "ds_read_b64_tr_b16 v[138:139], v226 offset:19456\n\t"
        "s_waitcnt lgkmcnt(14)\n\t"
        "v_mfma_f32_32x32x16_bf16 v[16:31], v[140:143], v[180:183], v[16:31]\n\t"
        "ds_read_b64_tr_b16 v[140:141], v227 offset:18432\n\t"
        "ds_read_b64_tr_b16 v[142:143], v227 offset:19456\n\t"
        "buffer_load_dwordx4 v[164:167], v225, s[40:43], s45 offen offset:1024 sc1\n\t"
        "s_waitcnt vmcnt(11) lgkmcnt(14)\n\t"
        "v_mfma_f32_32x32x16_bf16 v[0:15], v[144:147], v[184:187], v[0:15]\n\t"
        "ds_read_b64_tr_b16 v[144:145], v226 offset:20480\n\t"
        "ds_read_b64_tr_b16 v[146:147], v226 offset:21504\n\t"
        "s_waitcnt lgkmcnt(14)\n\t"
        "v_mfma_f32_32x32x16_bf16 v[16:31], v[148:151], v[184:187], v[16:31]\n\t"
        "ds_read_b64_tr_b16 v[148:149], v227 offset:20480\n\t"
        "ds_read_b64_tr_b16 v[150:151], v227 offset:21504\n\t"
        "buffer_load_dwordx4 v[168:171], v225, s[40:43], s45 offen offset:2048 sc1\n\t"
        "s_waitcnt vmcnt(11) lgkmcnt(14)\n\t"
        "v_mfma_f32_32x32x16_bf16 v[0:15], v[152:155], v[188:191], v[0:15]\n\t"
        "ds_read_b64_tr_b16 v[152:153], v226 offset:22528\n\t"
        "ds_read_b64_tr_b16 v[154:155], v226 offset:23552\n\t"
        "s_waitcnt lgkmcnt(14)\n\t"
        "v_mfma_f32_32x32x16_bf16 v[16:31], v[156:159], v[188:191], v[16:31]\n\t"
        "ds_read_b64_tr_b16 v[156:157], v227 offset:22528\n\t"
        "ds_read_b64_tr_b16 v[158:159], v227 offset:23552\n\t"
        "buffer_load_dwordx4 v[172:175], v225, s[40:43], s45 offen offset:3072 sc1\n\t"
        "s_waitcnt vmcnt(11) lgkmcnt(14)\n\t"
        "v_mfma_f32_32x32x16_bf16 v[0:15], v[128:131], v[192:195], v[0:15]\n\t"
        "ds_read_b64_tr_b16 v[128:129], v226 offset:24576\n\t"
        "ds_read_b64_tr_b16 v[130:131], v226 offset:25600\n\t"
        "s_waitcnt lgkmcnt(14)\n\t"
        "v_mfma_f32_32x32x16_bf16 v[16:31], v[132:135], v[192:195], v[16:31]\n\t"
        "ds_read_b64_tr_b16 v[132:133], v227 offset:24576\n\t"
        "ds_read_b64_tr_b16 v[134:135], v227 offset:25600\n\t"
        "s_add_u32 s45, s45, 0x1000\n\t"
        "buffer_load_dwordx4 v[176:179], v225, s[40:43], s45 offen offset:0 sc1\n\t"
        "s_waitcnt vmcnt(11) lgkmcnt(14)\n\t"
        "v_mfma_f32_32x32x16_bf16 v[0:15], v[136:139], v[196:199], v[0:15]\n\t"
        "ds_read_b64_tr_b16 v[136:137], v226 offset:26624\n\t"
        "ds_read_b64_tr_b16 v[138:139], v226 offset:27648\n\t"
        "s_waitcnt lgkmcnt(14)\n\t"
        "v_mfma_f32_32x32x16_bf16 v[16:31], v[140:143], v[196:199], v[16:31]\n\t"
        "ds_read_b64_tr_b16 v[140:141], v227 offset:26624\n\t"
        "ds_read_b64_tr_b16 v[142:143], v227 offset:27648\n\t"
        "buffer_load_dwordx4 v[180:183], v225, s[40:43], s45 offen offset:1024 sc1\n\t"
        "s_waitcnt vmcnt(11) lgkmcnt(14)\n\t"
        "v_mfma_f32_32x32x16_bf16 v[0:15], v[144:147], v[200:203], v[0:15]\n\t"
        "ds_read_b64_tr_b16 v[144:145], v226 offset:28672\n\t"
        "ds_read_b64_tr_b16 v[146:147], v226 offset:29696\n\t"
        "s_waitcnt lgkmcnt(14)\n\t"
        "v_mfma_f32_32x32x16_bf16 v[16:31], v[148:151], v[200:203], v[16:31]\n\t"
        "ds_read_b64_tr_b16 v[148:149], v227 offset:28672\n\t"
        "ds_read_b64_tr_b16 v[150:151], v227 offset:29696\n\t"
        "buffer_load_dwordx4 v[184:187], v225, s[40:43], s45 offen offset:2048 sc1\n\t"
        "s_waitcnt vmcnt(11) lgkmcnt(14)\n\t"
        "v_mfma_f32_32x32x16_bf16 v[0:15], v[152:155], v[204:207], v[0:15]\n\t"
        "ds_read_b64_tr_b16 v[152:153], v226 offset:30720\n\t"
        "ds_read_b64_tr_b16 v[154:155], v226 offset:31744\n\t"
        "s_waitcnt lgkmcnt(14)\n\t"
        "v_mfma_f32_32x32x16_bf16 v[16:31], v[156:159], v[204:207], v[16:31]\n\t"
        "ds_read_b64_tr_b16 v[156:157], v227 offset:30720\n\t"
        "ds_read_b64_tr_b16 v[158:159], v227 offset:31744\n\t"
        "buffer_load_dwordx4 v[188:191], v225, s[40:43], s45 offen offset:3072 sc1\n\t"
        "s_waitcnt vmcnt(11) lgkmcnt(14)\n\t"
        "v_mfma_f32_32x32x16_bf16 v[0:15], v[128:131], v[208:211], v[0:15]\n\t"
        "ds_read_b64_tr_b16 v[128:129], v230 offset:0\n\t"
        "ds_read_b64_tr_b16 v[130:131], v230 offset:1024\n\t"
        "s_waitcnt lgkmcnt(14)\n\t"
        "v_mfma_f32_32x32x16_bf16 v[16:31], v[132:135], v[208:211], v[16:31]\n\t"
        "ds_read_b64_tr_b16 v[132:133], v231 offset:0\n\t"
        "ds_read_b64_tr_b16 v[134:135], v231 offset:1024\n\t"
        "s_add_u32 s45, s45, 0x1000\n\t"
        "buffer_load_dwordx4 v[192:195], v225, s[40:43], s45 offen offset:0 sc1\n\t"
        "s_waitcnt vmcnt(11) lgkmcnt(14)\n\t"
        "v_mfma_f32_32x32x16_bf16 v[0:15], v[136:139], v[212:215], v[0:15]\n\t"
        "ds_read_b64_tr_b16 v[136:137], v230 offset:2048\n\t"
        "ds_read_b64_tr_b16 v[138:139], v230 offset:3072\n\t"
        "s_waitcnt lgkmcnt(14)\n\t"
        "v_mfma_f32_32x32x16_bf16 v[16:31], v[140:143], v[212:215], v[16:31]\n\t"
        "ds_read_b64_tr_b16 v[140:141], v231 offset:2048\n\t"
        "ds_read_b64_tr_b16 v[142:143], v231 offset:3072\n\t"
        "buffer_load_dwordx4 v[196:199], v225, s[40:43], s45 offen offset:1024 sc1\n\t"
        "s_waitcnt vmcnt(11) lgkmcnt(14)\n\t"
        "v_mfma_f32_32x32x16_bf16 v[0:15], v[144:147], v[216:219], v[0:15]\n\t"
        "ds_read_b64_tr_b16 v[144:145], v230 offset:4096\n\t"
        "ds_read_b64_tr_b16 v[146:147], v230 offset:5120\n\t"
        "s_waitcnt lgkmcnt(14)\n\t"
        "v_mfma_f32_32x32x16_bf16 v[16:31], v[148:151], v[216:219], v[16:31]\n\t"
        "ds_read_b64_tr_b16 v[148:149], v231 offset:4096\n\t"
        "ds_read_b64_tr_b16 v[150:151], v231 offset:5120\n\t"
        "buffer_load_dwordx4 v[200:203], v225, s[40:43], s45 offen offset:2048 sc1\n\t"
        "s_waitcnt vmcnt(11) lgkmcnt(14)\n\t"
        "v_mfma_f32_32x32x16_bf16 v[0:15], v[152:155], v[220:223], v[0:15]\n\t"
        "ds_read_b64_tr_b16 v[152:153], v230 offset:6144\n\t"
        "ds_read_b64_tr_b16 v[154:155], v230 offset:7168\n\t"
        "s_waitcnt lgkmcnt(14)\n\t"
        "v_mfma_f32_32x32x16_bf16 v[16:31], v[156:159], v[220:223], v[16:31]\n\t"
        "ds_read_b64_tr_b16 v[156:157], v231 offset:6144\n\t"
        "ds_read_b64_tr_b16 v[158:159], v231 offset:7168\n\t"
        "buffer_load_dwordx4 v[204:207], v225, s[40:43], s45 offen offset:3072 sc1\n\t"
        "s_mov_b32 s44, s45\n\t"
        "s_cmp_eq_u32 s47, 0\n\t"
        "s_cbranch_scc1 oseg0_%=\n\t"
        "s_cmp_eq_u32 s47, 1\n\t"
        "s_cbranch_scc1 oseg1_%=\n\t"
        "s_cmp_eq_u32 s47, 2\n\t"
        "s_cbranch_scc1 oseg2_%=\n\t"
        "s_cmp_eq_u32 s47, 3\n\t"
        "s_cbranch_scc1 oseg3_%=\n\t"
        "s_cmp_lg_u32 s46, 15\n\t"
        "s_cbranch_scc1 ostart_%=\n\t"
        "s_branch oend_%=\n\t"
        "oseg1_%=:\n\t"
        "s_or_b32 s46, s46, 2\n\t"
        "s_waitcnt vmcnt(11) lgkmcnt(14)\n\t"
        "v_mfma_f32_32x32x16_bf16 v[32:47], v[128:131], v[160:163], 0\n\t"
        "ds_read_b64_tr_b16 v[128:129], v226 offset:40960\n\t"
        "ds_read_b64_tr_b16 v[130:131], v226 offset:41984\n\t"
        "s_waitcnt lgkmcnt(14)\n\t"
        "v_mfma_f32_32x32x16_bf16 v[48:63], v[132:135], v[160:163], 0\n\t"
        "ds_read_b64_tr_b16 v[132:133], v227 offset:40960\n\t"
        "ds_read_b64_tr_b16 v[134:135], v227 offset:41984\n\t"
        "s_add_u32 s44, s44, 0x1000\n\t"
        "buffer_load_dwordx4 v[208:211], v224, s[40:43], s44 offen offset:0 sc1\n\t"
        "s_waitcnt vmcnt(11) lgkmcnt(14)\n\t"
        "v_mfma_f32_32x32x16_bf16 v[32:47], v[136:139], v[164:167], v[32:47]\n\t"
        "ds_read_b32 v233, v232\n\t"
        "ds_read_b64_tr_b16 v[136:137], v226 offset:43008\n\t"
        "ds_read_b64_tr_b16 v[138:139], v226 offset:44032\n\t"
        "s_waitcnt lgkmcnt(14)\n\t"
        "v_mfma_f32_32x32x16_bf16 v[48:63], v[140:143], v[164:167], v[48:63]\n\t"
        "ds_read_b64_tr_b16 v[140:141], v227 offset:43008\n\t"
        "ds_read_b64_tr_b16 v[142:143], v227 offset:44032\n\t"
        "buffer_load_dwordx4 v[212:215], v224, s[40:43], s44 offen offset:1024 sc1\n\t"
        "s_waitcnt vmcnt(11) lgkmcnt(14)\n\t"
        "v_mfma_f32_32x32x16_bf16 v[32:47], v[144:147], v[168:171], v[32:47]\n\t"
        "ds_read_b64_tr_b16 v[144:145], v226 offset:45056\n\t"
        "ds_read_b64_tr_b16 v[146:147], v226 offset:46080\n\t"
        "s_waitcnt lgkmcnt(14)\n\t"
        "v_mfma_f32_32x32x16_bf16 v[48:63], v[148:151], v[168:171], v[48:63]\n\t"
        "ds_read_b64_tr_b16 v[148:149], v227 offset:45056\n\t"
        "ds_read_b64_tr_b16 v[150:151], v227 offset:46080\n\t"
        "buffer_load_dwordx4 v[216:219], v224, s[40:43], s44 offen offset:2048 sc1\n\t"
        "s_waitcnt vmcnt(11) lgkmcnt(8)\n\t"
        "v_mfma_f32_32x32x16_bf16 v[32:47], v[152:155], v[172:175], v[32:47]\n\t"
        "v_readfirstlane_b32 s51, v233\n\t"
        "ds_read_b64_tr_b16 v[152:153], v226 offset:47104\n\t"
        "ds_read_b64_tr_b16 v[154:155], v226 offset:48128\n\t"
        "s_waitcnt lgkmcnt(14)\n\t"
        "v_mfma_f32_32x32x16_bf16 v[48:63], v[156:159], v[172:175], v[48:63]\n\t"
        "s_andn2_b32 s51, s51, s46\n\t"
        "s_ff1_i32_b32 s47, s51\n\t"
        "s_add_u32 s45, s47, s49\n\t"
        "s_and_b32 s45, s45, 3\n\t"
        "s_lshl_b32 s45, s45, 14\n\t"
        "ds_read_b64_tr_b16 v[156:157], v227 offset:47104\n\t"
        "ds_read_b64_tr_b16 v[158:159], v227 offset:48128\n\t"
        "buffer_load_dwordx4 v[220:223], v224, s[40:43], s44 offen offset:3072 sc1\n\t"
        "s_waitcnt vmcnt(11) lgkmcnt(14)\n\t"
        "v_mfma_f32_32x32x16_bf16 v[32:47], v[128:131], v[176:179], v[32:47]\n\t"
        "s_and_b32 s51, s47, 0x80000000\n\t"
        "v_or_b32 v225, s51, v224\n\t"
        "s_and_b32 s51, s47, 3\n\t"
        "s_lshl_b32 s51, s51, 15\n\t"
        "v_add_u32 v230, s51, v226\n\t"
        "v_add_u32 v231, s51, v227\n\t"
        "ds_read_b64_tr_b16 v[128:129], v226 offset:49152\n\t"
        "ds_read_b64_tr_b16 v[130:131], v226 offset:50176\n\t"
        "s_waitcnt lgkmcnt(14)\n\t"
        "v_mfma_f32_32x32x16_bf16 v[48:63], v[132:135], v[176:179], v[48:63]\n\t"
        "ds_read_b64_tr_b16 v[132:133], v227 offset:49152\n\t"
        "ds_read_b64_tr_b16 v[134:135], v227 offset:50176\n\t"
        "buffer_load_dwordx4 v[160:163], v225, s[40:43], s45 offen offset:0 sc1\n\t"
        "s_waitcnt vmcnt(11) lgkmcnt(14)\n\t"
        "v_mfma_f32_32x32x16_bf16 v[32:47], v[136:139], v[180:183], v[32:47]\n\t"
        "ds_read_b64_tr_b16 v[136:137], v226 offset:51200\n\t"
        "ds_read_b64_tr_b16 v[138:139], v226 offset:52224\n\t"
        "s_waitcnt lgkmcnt(14)\n\t"
        "v_mfma_f32_32x32x16_bf16 v[48:63], v[140:143], v[180:183], v[48:63]\n\t"
        "ds_read_b64_tr_b16 v[140:141], v227 offset:51200\n\t"
        "ds_read_b64_tr_b16 v[142:143], v227 offset:52224\n\t"
        "buffer_load_dwordx4 v[164:167], v225, s[40:43], s45 offen offset:1024 sc1\n\t"
        "s_waitcnt vmcnt(11) lgkmcnt(14)\n\t"
        "v_mfma_f32_32x32x16_bf16 v[32:47], v[144:147], v[184:187], v[32:47]\n\t"
        "ds_read_b64_tr_b16 v[144:145], v226 offset:53248\n\t"
        "ds_read_b64_tr_b16 v[146:147], v226 offset:54272\n\t"
        "s_waitcnt lgkmcnt(14)\n\t"
        "v_mfma_f32_32x32x16_bf16 v[48:63], v[148:151], v[184:187], v[48:63]\n\t"
        "ds_read_b64_tr_b16 v[148:149], v227 offset:53248\n\t"
        "ds_read_b64_tr_b16 v[150:151], v227 offset:54272\n\t"
        "buffer_load_dwordx4 v[168:171], v225, s[40:43], s45 offen offset:2048 sc1\n\t"
        "s_waitcnt vmcnt(11) lgkmcnt(14)\n\t"
        "v_mfma_f32_32x32x16_bf16 v[32:47], v[152:155], v[188:191], v[32:47]\n\t"
        "ds_read_b64_tr_b16 v[152:153], v226 offset:55296\n\t"
        "ds_read_b64_tr_b16 v[154:155], v226 offset:56320\n\t"
        "s_waitcnt lgkmcnt(14)\n\t"
        "v_mfma_f32_32x32x16_bf16 v[48:63], v[156:159], v[188:191], v[48:63]\n\t"
        "ds_read_b64_tr_b16 v[156:157], v227 offset:55296\n\t"
        "ds_read_b64_tr_b16 v[158:159], v227 offset:56320\n\t"
        "buffer_load_dwordx4 v[172:175], v225, s[40:43], s45 offen offset:3072 sc1\n\t"
        "s_waitcnt vmcnt(11) lgkmcnt(14)\n\t"
        "v_mfma_f32_32x32x16_bf16 v[32:47], v[128:131], v[192:195], v[32:47]\n\t"
        "ds_read_b64_tr_b16 v[128:129], v226 offset:57344\n\t"
        "ds_read_b64_tr_b16 v[130:131], v226 offset:58368\n\t"
        "s_waitcnt lgkmcnt(14)\n\t"
        "v_mfma_f32_32x32x16_bf16 v[48:63], v[132:135], v[192:195], v[48:63]\n\t"
        "ds_read_b64_tr_b16 v[132:133], v227 offset:57344\n\t"
        "ds_read_b64_tr_b16 v[134:135], v227 offset:58368\n\t"
        "s_add_u32 s45, s45, 0x1000\n\t"
        "buffer_load_dwordx4 v[176:179], v225, s[40:43], s45 offen offset:0 sc1\n\t"
        "s_waitcnt vmcnt(11) lgkmcnt(14)\n\t"
        "v_mfma_f32_32x32x16_bf16 v[32:47], v[136:139], v[196:199], v[32:47]\n\t"
        "ds_read_b64_tr_b16 v[136:137], v226 offset:59392\n\t"
        "ds_read_b64_tr_b16 v[138:139], v226 offset:60416\n\t"
        "s_waitcnt lgkmcnt(14)\n\t"
        "v_mfma_f32_32x32x16_bf16 v[48:63], v[140:143], v[196:199], v[48:63]\n\t"
        "ds_read_b64_tr_b16 v[140:141], v227 offset:59392\n\t"
        "ds_read_b64_tr_b16 v[142:143], v227 offset:60416\n\t"
        "buffer_load_dwordx4 v[180:183], v225, s[40:43], s45 offen offset:1024 sc1\n\t"
        "s_waitcnt vmcnt(11) lgkmcnt(14)\n\t"
        "v_mfma_f32_32x32x16_bf16 v[32:47], v[144:147], v[200:203], v[32:47]\n\t"
        "ds_read_b64_tr_b16 v[144:145], v226 offset:61440\n\t"
        "ds_read_b64_tr_b16 v[146:147], v226 offset:62464\n\t"
        "s_waitcnt lgkmcnt(14)\n\t"
        "v_mfma_f32_32x32x16_bf16 v[48:63], v[148:151], v[200:203], v[48:63]\n\t"
        "ds_read_b64_tr_b16 v[148:149], v227 offset:61440\n\t"
        "ds_read_b64_tr_b16 v[150:151], v227 offset:62464\n\t"
        "buffer_load_dwordx4 v[184:187], v225, s[40:43], s45 offen offset:2048 sc1\n\t"
        "s_waitcnt vmcnt(11) lgkmcnt(14)\n\t"
        "v_mfma_f32_32x32x16_bf16 v[32:47], v[152:155], v[204:207], v[32:47]\n\t"
        "ds_read_b64_tr_b16 v[152:153], v226 offset:63488\n\t"
        "ds_read_b64_tr_b16 v[154:155], v226 offset:64512\n\t"
        "s_waitcnt lgkmcnt(14)\n\t"
        "v_mfma_f32_32x32x16_bf16 v[48:63], v[156:159], v[204:207], v[48:63]\n\t"
        "ds_read_b64_tr_b16 v[156:157], v227 offset:63488\n\t"
        "ds_read_b64_tr_b16 v[158:159], v227 offset:64512\n\t"
        "buffer_load_dwordx4 v[188:191], v225, s[40:43], s45 offen offset:3072 sc1\n\t"
        "s_waitcnt vmcnt(11) lgkmcnt(14)\n\t"
        "v_mfma_f32_32x32x16_bf16 v[32:47], v[128:131], v[208:211], v[32:47]\n\t"
        "ds_read_b64_tr_b16 v[128:129], v230 offset:0\n\t"
        "ds_read_b64_tr_b16 v[130:131], v230 offset:1024\n\t"
        "s_waitcnt lgkmcnt(14)\n\t"
        "v_mfma_f32_32x32x16_bf16 v[48:63], v[132:135], v[208:211], v[48:63]\n\t"
        "ds_read_b64_tr_b16 v[132:133], v231 offset:0\n\t"
        "ds_read_b64_tr_b16 v[134:135], v231 offset:1024\n\t"
        "s_add_u32 s45, s45, 0x1000\n\t"
        "buffer_load_dwordx4 v[192:195], v225, s[40:43], s45 offen offset:0 sc1\n\t"
        "s_waitcnt vmcnt(11) lgkmcnt(14)\n\t"
        "v_mfma_f32_32x32x16_bf16 v[32:47], v[136:139], v[212:215], v[32:47]\n\t"
        "ds_read_b64_tr_b16 v[136:137], v230 offset:2048\n\t"
        "ds_read_b64_tr_b16 v[138:139], v230 offset:3072\n\t"
        "s_waitcnt lgkmcnt(14)\n\t"
        "v_mfma_f32_32x32x16_bf16 v[48:63], v[140:143], v[212:215], v[48:63]\n\t"
        "ds_read_b64_tr_b16 v[140:141], v231 offset:2048\n\t"
        "ds_read_b64_tr_b16 v[142:143], v231 offset:3072\n\t"
        "buffer_load_dwordx4 v[196:199], v225, s[40:43], s45 offen offset:1024 sc1\n\t"
        "s_waitcnt vmcnt(11) lgkmcnt(14)\n\t"
        "v_mfma_f32_32x32x16_bf16 v[32:47], v[144:147], v[216:219], v[32:47]\n\t"
        "ds_read_b64_tr_b16 v[144:145], v230 offset:4096\n\t"
        "ds_read_b64_tr_b16 v[146:147], v230 offset:5120\n\t"
        "s_waitcnt lgkmcnt(14)\n\t"
        "v_mfma_f32_32x32x16_bf16 v[48:63], v[148:151], v[216:219], v[48:63]\n\t"
        "ds_read_b64_tr_b16 v[148:149], v231 offset:4096\n\t"
        "ds_read_b64_tr_b16 v[150:151], v231 offset:5120\n\t"
        "buffer_load_dwordx4 v[200:203], v225, s[40:43], s45 offen offset:2048 sc1\n\t"
        "s_waitcnt vmcnt(11) lgkmcnt(14)\n\t"
        "v_mfma_f32_32x32x16_bf16 v[32:47], v[152:155], v[220:223], v[32:47]\n\t"
        "ds_read_b64_tr_b16 v[152:153], v230 offset:6144\n\t"
        "ds_read_b64_tr_b16 v[154:155], v230 offset:7168\n\t"
        "s_waitcnt lgkmcnt(14)\n\t"
        "v_mfma_f32_32x32x16_bf16 v[48:63], v[156:159], v[220:223], v[48:63]\n\t"
        "ds_read_b64_tr_b16 v[156:157], v231 offset:6144\n\t"
        "ds_read_b64_tr_b16 v[158:159], v231 offset:7168\n\t"
        "buffer_load_dwordx4 v[204:207], v225, s[40:43], s45 offen offset:3072 sc1\n\t"
        "s_mov_b32 s44, s45\n\t"
        "s_cmp_eq_u32 s47, 0\n\t"
        "s_cbranch_scc1 oseg0_%=\n\t"
        "s_cmp_eq_u32 s47, 1\n\t"
        "s_cbranch_scc1 oseg1_%=\n\t"
        "s_cmp_eq_u32 s47, 2\n\t"
        "s_cbranch_scc1 oseg2_%=\n\t"
        "s_cmp_eq_u32 s47, 3\n\t"
        "s_cbranch_scc1 oseg3_%=\n\t"
        "s_cmp_lg_u32 s46, 15\n\t"
        "s_cbranch_scc1 ostart_%=\n\t"
        "s_branch oend_%=\n\t"
        "oseg2_%=:\n\t"
        "s_or_b32 s46, s46, 4\n\t"
        "s_waitcnt vmcnt(11) lgkmcnt(14)\n\t"
        "v_mfma_f32_32x32x16_bf16 v[64:79], v[128:131], v[160:163], 0\n\t"
        "ds_read_b64_tr_b16 v[128:129], v228 offset:8192\n\t"
        "ds_read_b64_tr_b16 v[130:131], v228 offset:9216\n\t"
        "s_waitcnt lgkmcnt(14)\n\t"
        "v_mfma_f32_32x32x16_bf16 v[80:95], v[132:135], v[160:163], 0\n\t"
        "ds_read_b64_tr_b16 v[132:133], v229 offset:8192\n\t"
        "ds_read_b64_tr_b16 v[134:135], v229 offset:9216\n\t"
        "s_add_u32 s44, s44, 0x1000\n\t"
        "buffer_load_dwordx4 v[208:211], v224, s[40:43], s44 offen offset:0 sc1\n\t"
        "s_waitcnt vmcnt(11) lgkmcnt(14)\n\t"
        "v_mfma_f32_32x32x16_bf16 v[64:79], v[136:139], v[164:167], v[64:79]\n\t"
        "ds_read_b32 v233, v232\n\t"
        "ds_read_b64_tr_b16 v[136:137], v228 offset:10240\n\t"
        "ds_read_b64_tr_b16 v[138:139], v228 offset:11264\n\t"
        "s_waitcnt lgkmcnt(14)\n\t"
        "v_mfma_f32_32x32x16_bf16 v[80:95], v[140:143], v[164:167], v[80:95]\n\t"
        "ds_read_b64_tr_b16 v[140:141], v229 offset:10240\n\t"
        "ds_read_b64_tr_b16 v[142:143], v229 offset:11264\n\t"
        "buffer_load_dwordx4 v[212:215], v224, s[40:43], s44 offen offset:1024 sc1\n\t"
        "s_waitcnt vmcnt(11) lgkmcnt(14)\n\t"
        "v_mfma_f32_32x32x16_bf16 v[64:79], v[144:147], v[168:171], v[64:79]\n\t"
        "ds_read_b64_tr_b16 v[144:145], v228 offset:12288\n\t"
        "ds_read_b64_tr_b16 v[146:147], v228 offset:13312\n\t"
        "s_waitcnt lgkmcnt(14)\n\t"
        "v_mfma_f32_32x32x16_bf16 v[80:95], v[148:151], v[168:171], v[80:95]\n\t"
        "ds_read_b64_tr_b16 v[148:149], v229 offset:12288\n\t"
        "ds_read_b64_tr_b16 v[150:151], v229 offset:13312\n\t"
        "buffer_load_dwordx4 v[216:219], v224, s[40:43], s44 offen offset:2048 sc1\n\t"
        "s_waitcnt vmcnt(11) lgkmcnt(8)\n\t"
        "v_mfma_f32_32x32x16_bf16 v[64:79], v[152:155], v[172:175], v[64:79]\n\t"
        "v_readfirstlane_b32 s51, v233\n\t"
        "ds_read_b64_tr_b16 v[152:153], v228 offset:14336\n\t"
        "ds_read_b64_tr_b16 v[154:155], v228 offset:15360\n\t"
        "s_waitcnt lgkmcnt(14)\n\t"
        "v_mfma_f32_32x32x16_bf16 v[80:95], v[156:159], v[172:175], v[80:95]\n\t"
        "s_andn2_b32 s51, s51, s46\n\t"
        "s_ff1_i32_b32 s47, s51\n\t"
        "s_add_u32 s45, s47, s49\n\t"
        "s_and_b32 s45, s45, 3\n\t"
        "s_lshl_b32 s45, s45, 14\n\t"
        "ds_read_b64_tr_b16 v[156:157], v229 offset:14336\n\t"
        "ds_read_b64_tr_b16 v[158:159], v229 offset:15360\n\t"
        "buffer_load_dwordx4 v[220:223], v224, s[40:43], s44 offen offset:3072 sc1\n\t"
        "s_waitcnt vmcnt(11) lgkmcnt(14)\n\t"
        "v_mfma_f32_32x32x16_bf16 v[64:79], v[128:131], v[176:179], v[64:79]\n\t"
        "s_and_b32 s51, s47, 0x80000000\n\t"
        "v_or_b32 v225, s51, v224\n\t"
        "s_and_b32 s51, s47, 3\n\t"
        "s_lshl_b32 s51, s51, 15\n\t"
        "v_add_u32 v230, s51, v226\n\t"
        "v_add_u32 v231, s51, v227\n\t"
        "ds_read_b64_tr_b16 v[128:129], v228 offset:16384\n\t"
        "ds_read_b64_tr_b16 v[130:131], v228 offset:17408\n\t"
        "s_waitcnt lgkmcnt(14)\n\t"
        "v_mfma_f32_32x32x16_bf16 v[80:95], v[132:135], v[176:179], v[80:95]\n\t"
        "ds_read_b64_tr_b16 v[132:133], v229 offset:16384\n\t"
        "ds_read_b64_tr_b16 v[134:135], v229 offset:17408\n\t"
        "buffer_load_dwordx4 v[160:163], v225, s[40:43], s45 offen offset:0 sc1\n\t"
        "s_waitcnt vmcnt(11) lgkmcnt(14)\n\t"
        "v_mfma_f32_32x32x16_bf16 v[64:79], v[136:139], v[180:183], v[64:79]\n\t"
        "ds_read_b64_tr_b16 v[136:137], v228 offset:18432\n\t"
        "ds_read_b64_tr_b16 v[138:139], v228 offset:19456\n\t"
        "s_waitcnt lgkmcnt(14)\n\t"
        "v_mfma_f32_32x32x16_bf16 v[80:95], v[140:143], v[180:183], v[80:95]\n\t"
        "ds_read_b64_tr_b16 v[140:141], v229 offset:18432\n\t"
        "ds_read_b64_tr_b16 v[142:143], v229 offset:19456\n\t"
        "buffer_load_dwordx4 v[164:167], v225, s[40:43], s45 offen offset:1024 sc1\n\t"
        "s_waitcnt vmcnt(11) lgkmcnt(14)\n\t"
        "v_mfma_f32_32x32x16_bf16 v[64:79], v[144:147], v[184:187], v[64:79]\n\t"
        "ds_read_b64_tr_b16 v[144:145], v228 offset:20480\n\t"
        "ds_read_b64_tr_b16 v[146:147], v228 offset:21504\n\t"
        "s_waitcnt lgkmcnt(14)\n\t"
        "v_mfma_f32_32x32x16_bf16 v[80:95], v[148:151], v[184:187], v[80:95]\n\t"
        "ds_read_b64_tr_b16 v[148:149], v229 offset:20480\n\t"
        "ds_read_b64_tr_b16 v[150:151], v229 offset:21504\n\t"
        "buffer_load_dwordx4 v[168:171], v225, s[40:43], s45 offen offset:2048 sc1\n\t"
        "s_waitcnt vmcnt(11) lgkmcnt(14)\n\t"
        "v_mfma_f32_32x32x16_bf16 v[64:79], v[152:155], v[188:191], v[64:79]\n\t"
        "ds_read_b64_tr_b16 v[152:153], v228 offset:22528\n\t"
        "ds_read_b64_tr_b16 v[154:155], v228 offset:23552\n\t"
        "s_waitcnt lgkmcnt(14)\n\t"
        "v_mfma_f32_32x32x16_bf16 v[80:95], v[156:159], v[188:191], v[80:95]\n\t"
        "ds_read_b64_tr_b16 v[156:157], v229 offset:22528\n\t"
        "ds_read_b64_tr_b16 v[158:159], v229 offset:23552\n\t"
        "buffer_load_dwordx4 v[172:175], v225, s[40:43], s45 offen offset:3072 sc1\n\t"
        "s_waitcnt vmcnt(11) lgkmcnt(14)\n\t"
        "v_mfma_f32_32x32x16_bf16 v[64:79], v[128:131], v[192:195], v[64:79]\n\t"
        "ds_read_b64_tr_b16 v[128:129], v228 offset:24576\n\t"
        "ds_read_b64_tr_b16 v[130:131], v228 offset:25600\n\t"
        "s_waitcnt lgkmcnt(14)\n\t"
        "v_mfma_f32_32x32x16_bf16 v[80:95], v[132:135], v[192:195], v[80:95]\n\t"
        "ds_read_b64_tr_b16 v[132:133], v229 offset:24576\n\t"
        "ds_read_b64_tr_b16 v[134:135], v229 offset:25600\n\t"
        "s_add_u32 s45, s45, 0x1000\n\t"
        "buffer_load_dwordx4 v[176:179], v225, s[40:43], s45 offen offset:0 sc1\n\t"
        "s_waitcnt vmcnt(11) lgkmcnt(14)\n\t"
        "v_mfma_f32_32x32x16_bf16 v[64:79], v[136:139], v[196:199], v[64:79]\n\t"
        "ds_read_b64_tr_b16 v[136:137], v228 offset:26624\n\t"
        "ds_read_b64_tr_b16 v[138:139], v228 offset:27648\n\t"
        "s_waitcnt lgkmcnt(14)\n\t"
        "v_mfma_f32_32x32x16_bf16 v[80:95], v[140:143], v[196:199], v[80:95]\n\t"
        "ds_read_b64_tr_b16 v[140:141], v229 offset:26624\n\t"
        "ds_read_b64_tr_b16 v[142:143], v229 offset:27648\n\t"
        "buffer_load_dwordx4 v[180:183], v225, s[40:43], s45 offen offset:1024 sc1\n\t"
        "s_waitcnt vmcnt(11) lgkmcnt(14)\n\t"
        "v_mfma_f32_32x32x16_bf16 v[64:79], v[144:147], v[200:203], v[64:79]\n\t"
        "ds_read_b64_tr_b16 v[144:145], v228 offset:28672\n\t"
        "ds_read_b64_tr_b16 v[146:147], v228 offset:29696\n\t"
        "s_waitcnt lgkmcnt(14)\n\t"
        "v_mfma_f32_32x32x16_bf16 v[80:95], v[148:151], v[200:203], v[80:95]\n\t"
        "ds_read_b64_tr_b16 v[148:149], v229 offset:28672\n\t"
        "ds_read_b64_tr_b16 v[150:151], v229 offset:29696\n\t"
        "buffer_load_dwordx4 v[184:187], v225, s[40:43], s45 offen offset:2048 sc1\n\t"
        "s_waitcnt vmcnt(11) lgkmcnt(14)\n\t"
        "v_mfma_f32_32x32x16_bf16 v[64:79], v[152:155], v[204:207], v[64:79]\n\t"
        "ds_read_b64_tr_b16 v[152:153], v228 offset:30720\n\t"
        "ds_read_b64_tr_b16 v[154:155], v228 offset:31744\n\t"
        "s_waitcnt lgkmcnt(14)\n\t"
        "v_mfma_f32_32x32x16_bf16 v[80:95], v[156:159], v[204:207], v[80:95]\n\t"
        "ds_read_b64_tr_b16 v[156:157], v229 offset:30720\n\t"
        "ds_read_b64_tr_b16 v[158:159], v229 offset:31744\n\t"
        "buffer_load_dwordx4 v[188:191], v225, s[40:43], s45 offen offset:3072 sc1\n\t"
        "s_waitcnt vmcnt(11) lgkmcnt(14)\n\t"
        "v_mfma_f32_32x32x16_bf16 v[64:79], v[128:131], v[208:211], v[64:79]\n\t"
        "ds_read_b64_tr_b16 v[128:129], v230 offset:0\n\t"
        "ds_read_b64_tr_b16 v[130:131], v230 offset:1024\n\t"
        "s_waitcnt lgkmcnt(14)\n\t"
        "v_mfma_f32_32x32x16_bf16 v[80:95], v[132:135], v[208:211], v[80:95]\n\t"
        "ds_read_b64_tr_b16 v[132:133], v231 offset:0\n\t"
        "ds_read_b64_tr_b16 v[134:135], v231 offset:1024\n\t"
        "s_add_u32 s45, s45, 0x1000\n\t"
        "buffer_load_dwordx4 v[192:195], v225, s[40:43], s45 offen offset:0 sc1\n\t"
        "s_waitcnt vmcnt(11) lgkmcnt(14)\n\t"
        "v_mfma_f32_32x32x16_bf16 v[64:79], v[136:139], v[212:215], v[64:79]\n\t"
        "ds_read_b64_tr_b16 v[136:137], v230 offset:2048\n\t"
        "ds_read_b64_tr_b16 v[138:139], v230 offset:3072\n\t"
        "s_waitcnt lgkmcnt(14)\n\t"
        "v_mfma_f32_32x32x16_bf16 v[80:95], v[140:143], v[212:215], v[80:95]\n\t"
        "ds_read_b64_tr_b16 v[140:141], v231 offset:2048\n\t"
        "ds_read_b64_tr_b16 v[142:143], v231 offset:3072\n\t"
        "buffer_load_dwordx4 v[196:199], v225, s[40:43], s45 offen offset:1024 sc1\n\t"
        "s_waitcnt vmcnt(11) lgkmcnt(14)\n\t"
        "v_mfma_f32_32x32x16_bf16 v[64:79], v[144:147], v[216:219], v[64:79]\n\t"
        "ds_read_b64_tr_b16 v[144:145], v230 offset:4096\n\t"
        "ds_read_b64_tr_b16 v[146:147], v230 offset:5120\n\t"
        "s_waitcnt lgkmcnt(14)\n\t"
        "v_mfma_f32_32x32x16_bf16 v[80:95], v[148:151], v[216:219], v[80:95]\n\t"
        "ds_read_b64_tr_b16 v[148:149], v231 offset:4096\n\t"
        "ds_read_b64_tr_b16 v[150:151], v231 offset:5120\n\t"
        "buffer_load_dwordx4 v[200:203], v225, s[40:43], s45 offen offset:2048 sc1\n\t"
        "s_waitcnt vmcnt(11) lgkmcnt(14)\n\t"
        "v_mfma_f32_32x32x16_bf16 v[64:79], v[152:155], v[220:223], v[64:79]\n\t"
        "ds_read_b64_tr_b16 v[152:153], v230 offset:6144\n\t"
        "ds_read_b64_tr_b16 v[154:155], v230 offset:7168\n\t"
        "s_waitcnt lgkmcnt(14)\n\t"
        "v_mfma_f32_32x32x16_bf16 v[80:95], v[156:159], v[220:223], v[80:95]\n\t"
        "ds_read_b64_tr_b16 v[156:157], v231 offset:6144\n\t"
        "ds_read_b64_tr_b16 v[158:159], v231 offset:7168\n\t"
        "buffer_load_dwordx4 v[204:207], v225, s[40:43], s45 offen offset:3072 sc1\n\t"
        "s_mov_b32 s44, s45\n\t"
        "s_cmp_eq_u32 s47, 0\n\t"
        "s_cbranch_scc1 oseg0_%=\n\t"
        "s_cmp_eq_u32 s47, 1\n\t"
        "s_cbranch_scc1 oseg1_%=\n\t"
        "s_cmp_eq_u32 s47, 2\n\t"
        "s_cbranch_scc1 oseg2_%=\n\t"
        "s_cmp_eq_u32 s47, 3\n\t"
        "s_cbranch_scc1 oseg3_%=\n\t"
        "s_cmp_lg_u32 s46, 15\n\t"
        "s_cbranch_scc1 ostart_%=\n\t"
        "s_branch oend_%=\n\t"
        "oseg3_%=:\n\t"
        "s_or_b32 s46, s46, 8\n\t"
        "s_waitcnt vmcnt(11) lgkmcnt(14)\n\t"
        "v_mfma_f32_32x32x16_bf16 v[96:111], v[128:131], v[160:163], 0\n\t"
        "ds_read_b64_tr_b16 v[128:129], v228 offset:40960\n\t"
        "ds_read_b64_tr_b16 v[130:131], v228 offset:41984\n\t"
        "s_waitcnt lgkmcnt(14)\n\t"
        "v_mfma_f32_32x32x16_bf16 v[112:127], v[132:135], v[160:163], 0\n\t"
        "ds_read_b64_tr_b16 v[132:133], v229 offset:40960\n\t"
        "ds_read_b64_tr_b16 v[134:135], v229 offset:41984\n\t"
        "s_add_u32 s44, s44, 0x1000\n\t"
        "buffer_load_dwordx4 v[208:211], v224, s[40:43], s44 offen offset:0 sc1\n\t"
        "s_waitcnt vmcnt(11) lgkmcnt(14)\n\t"
        "v_mfma_f32_32x32x16_bf16 v[96:111], v[136:139], v[164:167], v[96:111]\n\t"
        "ds_read_b32 v233, v232\n\t"
        "ds_read_b64_tr_b16 v[136:137], v228 offset:43008\n\t"
        "ds_read_b64_tr_b16 v[138:139], v228 offset:44032\n\t"
        "s_waitcnt lgkmcnt(14)\n\t"
        "v_mfma_f32_32x32x16_bf16 v[112:127], v[140:143], v[164:167], v[112:127]\n\t"
        "ds_read_b64_tr_b16 v[140:141], v229 offset:43008\n\t"
        "ds_read_b64_tr_b16 v[142:143], v229 offset:44032\n\t"
        "buffer_load_dwordx4 v[212:215], v224, s[40:43], s44 offen offset:1024 sc1\n\t"
        "s_waitcnt vmcnt(11) lgkmcnt(14)\n\t"
        "v_mfma_f32_32x32x16_bf16 v[96:111], v[144:147], v[168:171], v[96:111]\n\t"
        "ds_read_b64_tr_b16 v[144:145], v228 offset:45056\n\t"
        "ds_read_b64_tr_b16 v[146:147], v228 offset:46080\n\t"
        "s_waitcnt lgkmcnt(14)\n\t"
        "v_mfma_f32_32x32x16_bf16 v[112:127], v[148:151], v[168:171], v[112:127]\n\t"
        "ds_read_b64_tr_b16 v[148:149], v229 offset:45056\n\t"
        "ds_read_b64_tr_b16 v[150:151], v229 offset:46080\n\t"
        "buffer_load_dwordx4 v[216:219], v224, s[40:43], s44 offen offset:2048 sc1\n\t"
        "s_waitcnt vmcnt(11) lgkmcnt(8)\n\t"
        "v_mfma_f32_32x32x16_bf16 v[96:111], v[152:155], v[172:175], v[96:111]\n\t"
        "v_readfirstlane_b32 s51, v233\n\t"
        "ds_read_b64_tr_b16 v[152:153], v228 offset:47104\n\t"
        "ds_read_b64_tr_b16 v[154:155], v228 offset:48128\n\t"
        "s_waitcnt lgkmcnt(14)\n\t"
        "v_mfma_f32_32x32x16_bf16 v[112:127], v[156:159], v[172:175], v[112:127]\n\t"
        "s_andn2_b32 s51, s51, s46\n\t"
        "s_ff1_i32_b32 s47, s51\n\t"
        "s_add_u32 s45, s47, s49\n\t"
        "s_and_b32 s45, s45, 3\n\t"
        "s_lshl_b32 s45, s45, 14\n\t"
        "ds_read_b64_tr_b16 v[156:157], v229 offset:47104\n\t"
        "ds_read_b64_tr_b16 v[158:159], v229 offset:48128\n\t"
        "buffer_load_dwordx4 v[220:223], v224, s[40:43], s44 offen offset:3072 sc1\n\t"
        "s_waitcnt vmcnt(11) lgkmcnt(14)\n\t"
        "v_mfma_f32_32x32x16_bf16 v[96:111], v[128:131], v[176:179], v[96:111]\n\t"
        "s_and_b32 s51, s47, 0x80000000\n\t"
        "v_or_b32 v225, s51, v224\n\t"
        "s_and_b32 s51, s47, 3\n\t"
        "s_lshl_b32 s51, s51, 15\n\t"
        "v_add_u32 v230, s51, v226\n\t"
        "v_add_u32 v231, s51, v227\n\t"
        "ds_read_b64_tr_b16 v[128:129], v228 offset:49152\n\t"
        "ds_read_b64_tr_b16 v[130:131], v228 offset:50176\n\t"
        "s_waitcnt lgkmcnt(14)\n\t"
        "v_mfma_f32_32x32x16_bf16 v[112:127], v[132:135], v[176:179], v[112:127]\n\t"
        "ds_read_b64_tr_b16 v[132:133], v229 offset:49152\n\t"
        "ds_read_b64_tr_b16 v[134:135], v229 offset:50176\n\t"
        "buffer_load_dwordx4 v[160:163], v225, s[40:43], s45 offen offset:0 sc1\n\t"
        "s_waitcnt vmcnt(11) lgkmcnt(14)\n\t"
        "v_mfma_f32_32x32x16_bf16 v[96:111], v[136:139], v[180:183], v[96:111]\n\t"
        "ds_read_b64_tr_b16 v[136:137], v228 offset:51200\n\t"
        "ds_read_b64_tr_b16 v[138:139], v228 offset:52224\n\t"
        "s_waitcnt lgkmcnt(14)\n\t"
        "v_mfma_f32_32x32x16_bf16 v[112:127], v[140:143], v[180:183], v[112:127]\n\t"
        "ds_read_b64_tr_b16 v[140:141], v229 offset:51200\n\t"
        "ds_read_b64_tr_b16 v[142:143], v229 offset:52224\n\t"
        "buffer_load_dwordx4 v[164:167], v225, s[40:43], s45 offen offset:1024 sc1\n\t"
        "s_waitcnt vmcnt(11) lgkmcnt(14)\n\t"
        "v_mfma_f32_32x32x16_bf16 v[96:111], v[144:147], v[184:187], v[96:111]\n\t"
        "ds_read_b64_tr_b16 v[144:145], v228 offset:53248\n\t"
        "ds_read_b64_tr_b16 v[146:147], v228 offset:54272\n\t"
        "s_waitcnt lgkmcnt(14)\n\t"
        "v_mfma_f32_32x32x16_bf16 v[112:127], v[148:151], v[184:187], v[112:127]\n\t"
        "ds_read_b64_tr_b16 v[148:149], v229 offset:53248\n\t"
        "ds_read_b64_tr_b16 v[150:151], v229 offset:54272\n\t"
        "buffer_load_dwordx4 v[168:171], v225, s[40:43], s45 offen offset:2048 sc1\n\t"
        "s_waitcnt vmcnt(11) lgkmcnt(14)\n\t"
        "v_mfma_f32_32x32x16_bf16 v[96:111], v[152:155], v[188:191], v[96:111]\n\t"
        "ds_read_b64_tr_b16 v[152:153], v228 offset:55296\n\t"
        "ds_read_b64_tr_b16 v[154:155], v228 offset:56320\n\t"
        "s_waitcnt lgkmcnt(14)\n\t"
        "v_mfma_f32_32x32x16_bf16 v[112:127], v[156:159], v[188:191], v[112:127]\n\t"
        "ds_read_b64_tr_b16 v[156:157], v229 offset:55296\n\t"
        "ds_read_b64_tr_b16 v[158:159], v229 offset:56320\n\t"
        "buffer_load_dwordx4 v[172:175], v225, s[40:43], s45 offen offset:3072 sc1\n\t"
        "s_waitcnt vmcnt(11) lgkmcnt(14)\n\t"
        "v_mfma_f32_32x32x16_bf16 v[96:111], v[128:131], v[192:195], v[96:111]\n\t"
        "ds_read_b64_tr_b16 v[128:129], v228 offset:57344\n\t"
        "ds_read_b64_tr_b16 v[130:131], v228 offset:58368\n\t"
        "s_waitcnt lgkmcnt(14)\n\t"
        "v_mfma_f32_32x32x16_bf16 v[112:127], v[132:135], v[192:195], v[112:127]\n\t"
        "ds_read_b64_tr_b16 v[132:133], v229 offset:57344\n\t"
        "ds_read_b64_tr_b16 v[134:135], v229 offset:58368\n\t"
        "s_add_u32 s45, s45, 0x1000\n\t"
        "buffer_load_dwordx4 v[176:179], v225, s[40:43], s45 offen offset:0 sc1\n\t"
        "s_waitcnt vmcnt(11) lgkmcnt(14)\n\t"
        "v_mfma_f32_32x32x16_bf16 v[96:111], v[136:139], v[196:199], v[96:111]\n\t"
        "ds_read_b64_tr_b16 v[136:137], v228 offset:59392\n\t"
        "ds_read_b64_tr_b16 v[138:139], v228 offset:60416\n\t"
        "s_waitcnt lgkmcnt(14)\n\t"
        "v_mfma_f32_32x32x16_bf16 v[112:127], v[140:143], v[196:199], v[112:127]\n\t"
        "ds_read_b64_tr_b16 v[140:141], v229 offset:59392\n\t"
        "ds_read_b64_tr_b16 v[142:143], v229 offset:60416\n\t"
        "buffer_load_dwordx4 v[180:183], v225, s[40:43], s45 offen offset:1024 sc1\n\t"
        "s_waitcnt vmcnt(11) lgkmcnt(14)\n\t"
        "v_mfma_f32_32x32x16_bf16 v[96:111], v[144:147], v[200:203], v[96:111]\n\t"
        "ds_read_b64_tr_b16 v[144:145], v228 offset:61440\n\t"
        "ds_read_b64_tr_b16 v[146:147], v228 offset:62464\n\t"
        "s_waitcnt lgkmcnt(14)\n\t"
        "v_mfma_f32_32x32x16_bf16 v[112:127], v[148:151], v[200:203], v[112:127]\n\t"
        "ds_read_b64_tr_b16 v[148:149], v229 offset:61440\n\t"
        "ds_read_b64_tr_b16 v[150:151], v229 offset:62464\n\t"
        "buffer_load_dwordx4 v[184:187], v225, s[40:43], s45 offen offset:2048 sc1\n\t"
        "s_waitcnt vmcnt(11) lgkmcnt(14)\n\t"
        "v_mfma_f32_32x32x16_bf16 v[96:111], v[152:155], v[204:207], v[96:111]\n\t"
        "ds_read_b64_tr_b16 v[152:153], v228 offset:63488\n\t"
        "ds_read_b64_tr_b16 v[154:155], v228 offset:64512\n\t"
        "s_waitcnt lgkmcnt(14)\n\t"
        "v_mfma_f32_32x32x16_bf16 v[112:127], v[156:159], v[204:207], v[112:127]\n\t"
        "ds_read_b64_tr_b16 v[156:157], v229 offset:63488\n\t"
        "ds_read_b64_tr_b16 v[158:159], v229 offset:64512\n\t"
        "buffer_load_dwordx4 v[188:191], v225, s[40:43], s45 offen offset:3072 sc1\n\t"
        "s_waitcnt vmcnt(11) lgkmcnt(14)\n\t"
        "v_mfma_f32_32x32x16_bf16 v[96:111], v[128:131], v[208:211], v[96:111]\n\t"
        "ds_read_b64_tr_b16 v[128:129], v230 offset:0\n\t"
        "ds_read_b64_tr_b16 v[130:131], v230 offset:1024\n\t"
        "s_waitcnt lgkmcnt(14)\n\t"
        "v_mfma_f32_32x32x16_bf16 v[112:127], v[132:135], v[208:211], v[112:127]\n\t"
        "ds_read_b64_tr_b16 v[132:133], v231 offset:0\n\t"
        "ds_read_b64_tr_b16 v[134:135], v231 offset:1024\n\t"
        "s_add_u32 s45, s45, 0x1000\n\t"
        "buffer_load_dwordx4 v[192:195], v225, s[40:43], s45 offen offset:0 sc1\n\t"
        "s_waitcnt vmcnt(11) lgkmcnt(14)\n\t"
        "v_mfma_f32_32x32x16_bf16 v[96:111], v[136:139], v[212:215], v[96:111]\n\t"
        "ds_read_b64_tr_b16 v[136:137], v230 offset:2048\n\t"
        "ds_read_b64_tr_b16 v[138:139], v230 offset:3072\n\t"
        "s_waitcnt lgkmcnt(14)\n\t"
        "v_mfma_f32_32x32x16_bf16 v[112:127], v[140:143], v[212:215], v[112:127]\n\t"
        "ds_read_b64_tr_b16 v[140:141], v231 offset:2048\n\t"
        "ds_read_b64_tr_b16 v[142:143], v231 offset:3072\n\t"
        "buffer_load_dwordx4 v[196:199], v225, s[40:43], s45 offen offset:1024 sc1\n\t"
        "s_waitcnt vmcnt(11) lgkmcnt(14)\n\t"
        "v_mfma_f32_32x32x16_bf16 v[96:111], v[144:147], v[216:219], v[96:111]\n\t"
        "ds_read_b64_tr_b16 v[144:145], v230 offset:4096\n\t"
        "ds_read_b64_tr_b16 v[146:147], v230 offset:5120\n\t"
        "s_waitcnt lgkmcnt(14)\n\t"
        "v_mfma_f32_32x32x16_bf16 v[112:127], v[148:151], v[216:219], v[112:127]\n\t"
        "ds_read_b64_tr_b16 v[148:149], v231 offset:4096\n\t"
        "ds_read_b64_tr_b16 v[150:151], v231 offset:5120\n\t"
        "buffer_load_dwordx4 v[200:203], v225, s[40:43], s45 offen offset:2048 sc1\n\t"
        "s_waitcnt vmcnt(11) lgkmcnt(14)\n\t"
        "v_mfma_f32_32x32x16_bf16 v[96:111], v[152:155], v[220:223], v[96:111]\n\t"
        "ds_read_b64_tr_b16 v[152:153], v230 offset:6144\n\t"
        "ds_read_b64_tr_b16 v[154:155], v230 offset:7168\n\t"
        "s_waitcnt lgkmcnt(14)\n\t"
        "v_mfma_f32_32x32x16_bf16 v[112:127], v[156:159], v[220:223], v[112:127]\n\t"
        "ds_read_b64_tr_b16 v[156:157], v231 offset:6144\n\t"
        "ds_read_b64_tr_b16 v[158:159], v231 offset:7168\n\t"
        "buffer_load_dwordx4 v[204:207], v225, s[40:43], s45 offen offset:3072 sc1\n\t"
        "s_mov_b32 s44, s45\n\t"
        "s_cmp_eq_u32 s47, 0\n\t"
        "s_cbranch_scc1 oseg0_%=\n\t"
        "s_cmp_eq_u32 s47, 1\n\t"
        "s_cbranch_scc1 oseg1_%=\n\t"
        "s_cmp_eq_u32 s47, 2\n\t"
        "s_cbranch_scc1 oseg2_%=\n\t"
        "s_cmp_eq_u32 s47, 3\n\t"
        "s_cbranch_scc1 oseg3_%=\n\t"
        "s_cmp_lg_u32 s46, 15\n\t"
        "s_cbranch_scc1 ostart_%=\n\t"
        "s_branch oend_%=\n\t"
        "oend_%=:\n\t"
        "ds_read_b64 v[128:129], v234 offset:0\n\t"
        "ds_read_b64 v[130:131], v234 offset:16\n\t"
        "ds_read_b64 v[132:133], v234 offset:32\n\t"
        "ds_read_b64 v[134:135], v234 offset:48\n\t"
        "ds_read_b64 v[136:137], v234 offset:64\n\t"
        "ds_read_b64 v[138:139], v234 offset:80\n\t"
        "ds_read_b64 v[140:141], v234 offset:96\n\t"
        "ds_read_b64 v[142:143], v234 offset:112\n\t"
        "s_waitcnt lgkmcnt(0)\n\t"
        "s_barrier\n\t"
        : "+{v[0:15]}"(acc[0]), "+{v[16:31]}"(acc[1]), "={v[32:47]}"(acc[2]), "={v[48:63]}"(acc[3]), "={v[64:79]}"(acc[4]), "={v[80:95]}"(acc[5]),
          "={v[96:111]}"(acc[6]), "={v[112:127]}"(acc[7]), "={v[128:143]}"(wrc),
          "={v[160:175]}"(fl0), "={v[176:191]}"(fl1), "={v[192:207]}"(fl2) STAMP_OUTS
        : "{v224}"(OFF_OF + L * (5 * MBY) + tile * TS + lane * 16), "{v226}"(a0), "{v227}"(a1), "{v228}"(a0 + 65536), "{v229}"(a1 + 65536),
          "{v232}"(lbase + L_W + 20 + 8 * L), "{v234}"(lbase + L_WR + hi * 8), "{s49}"((tn >> 2) + 1), "{s[40:43]}"(srd)
        : "v144", "v145", "v146", "v147", "v148", "v149", "v150", "v151", "v152", "v153", "v154", "v155", "v156", "v157", "v158", "v159", "v208", "v209", "v210", "v211", "v212", "v213", "v214", "v215", "v216", "v217", "v218", "v219", "v220", "v221", "v222", "v223", "v225", "v230", "v231", "v233", "s44", "s45", "s46", "s47", "s48", "s51", "vcc", "scc", "memory");
      }
      PHASE_VARS;
      STAMP_PUT(12 + 16 * L);
      STAMPV(15 + 16 * L);
      float s = 0.f, sq = 0.f, d = 0.f;
      const unsigned xdst = OFF_X1 + tile * TS + tn * 4096 + lane * 16;
#pragma unroll
      for (int i = 0; i < 4; ++i) {
#pragma unroll
        for (int e = 0; e < 4; ++e) {
          const int j = i >> 1, k = 8 * (i & 1) + 2 * e;
          x1p[i][e] = pack2((acc[j][k] + acc[2 + j][k]) + (acc[4 + j][k] + acc[6 + j][k]), (acc[j][k + 1] + acc[2 + j][k + 1]) + (acc[4 + j][k + 1] + acc[6 + j][k + 1]));
        }
        if (L == 0) stg(rs, xdst + i * 1024, x1p[i]);
        SBAR();
      }
#pragma unroll
      for (int g = 0; g < 8; ++g)
#pragma unroll
        for (int e = 0; e < 2; ++e) {
          const unsigned o = x1p[g >> 1][2 * (g & 1) + e];
          s = dot2(o, 0x3f803f80u, s); sq = dot2(o, o, sq); d = dot2(o, wrc[2 * g + e], d);
        }
      const u32x4 pst = {nzf(half_swap_sum(s)), nzf(half_swap_sum(sq)), nzf(half_swap_sum(d)), 0u};
      asm volatile("buffer_store_dwordx4 %0, %1, %2, 0 offen sc1\n\ts_waitcnt vmcnt(1)\n\tds_write_b32 %3, %4"
                   :: "v"(pst), "v"(stats_dst(sx, tile, tn, lane)), "s"(srd), "v"(lbase + L_W + 64 + 4 * wid), "v"(3),
                      "{v[160:175]}"(fl0), "{v[176:191]}"(fl1), "{v[192:207]}"(fl2) : "memory");
    }
    STAMPV(16 + 16 * L);
  }
  {
    PHASE_VARS;
    float S, Q, Dd, mean, var, mb1, rd1;
    __builtin_amdgcn_s_sleep(13);
    row_stats_sum(rs, OFF_SX + SX_LAYER, tile, lane, S, Q, Dd);
    STAMPV(33);
    row_stats_fin(rs, OFF_SX + SX_LAYER + SX_CAND + (tn * 16 + tm) * 128 + wid * 4, smem, 2, tile, lane, S, Q, Dd, U2F(words[0]), x1p, mean, var, mb1, rd1);
    STAMPV(34);
    u32x4 xo[4];
#pragma unroll
    for (int i = 0; i < 4; ++i) {
#pragma unroll
      for (int e = 0; e < 4; ++e) {
        const unsigned h2 = ln_pair(x1p[i][e], mb1, rd1);
        xo[i][e] = pack2(lo16(x1p[i][e]) + lo16(h2), hi16(x1p[i][e]) + hi16(h2));
      }
      *reinterpret_cast<u32x4*>(smem + wid * 4096 + r32 * 128 + ((2 * i + hi) ^ (r32 & 7)) * 16) = fo_swap(xo[i]);
    }
    asm volatile("s_waitcnt lgkmcnt(0)" ::: "memory");
#pragma unroll
    for (int i = 0; i < 4; ++i) { xo[i] = *reinterpret_cast<const u32x4*>(smem + wid * 4096 + i * 1024 + (lane >> 3) * 128 + ((lane ^ (lane >> 3)) & 7) * 16); PIN(xo[i]); }
    u16* dst = out + (size_t)(tile * 32 + (lane >> 3)) * D_ + tn * 64 + (lane & 7) * 8;
    lds_wait_ge(lbase + L_W + 4 + 4 * (lane & 1), 2);
    const int rel = (int)words[3] - tile * 32 - (lane >> 3);
#pragma unroll
    for (int i = 0; i < 4; ++i)
      if (rel != 8 * i) st16(dst + (size_t)i * 8 * D_, xo[i]);
    STAMPV(35);
    STAMP_END;
  }
}

extern "C" __global__ void end_marker() {}

extern "C" void kernel_launch(void* const* d_in, const int* in_sizes, int n_in, void* d_out, int out_size,
                              void* d_ws, size_t ws_size, hipStream_t stream) {
  char* ws = (char*)d_ws;
  mega_kernel<<<256, 512, 0, stream>>>((const u16*)d_in[0], (const u16*)d_in[1], (const u16*)d_in[2], (const u16*)d_in[4], (const u16*)d_in[5],
                                       (const u16*)d_in[6], (const u16*)d_in[7], ws, (u16*)d_out);
}
```

```cpp
#include <hip/hip_runtime.h>
#include <hip/hip_bf16.h>
#include <stdint.h>

typedef unsigned short u16;
typedef short s16x4 __attribute__((ext_vector_type(4)));
typedef short s16x8 __attribute__((ext_vector_type(8)));
typedef float f32x16 __attribute__((ext_vector_type(16)));
typedef float f32x4 __attribute__((ext_vector_type(4)));
typedef float f32x2 __attribute__((ext_vector_type(2)));
typedef unsigned int u32x16 __attribute__((ext_vector_type(16)));
typedef unsigned int u32x8 __attribute__((ext_vector_type(8)));
typedef unsigned int u32x4 __attribute__((ext_vector_type(4)));
typedef unsigned int u32x2 __attribute__((ext_vector_type(2)));
typedef __bf16 bf16x2 __attribute__((ext_vector_type(2)));
typedef __bf16 bf16x8 __attribute__((ext_vector_type(8)));
typedef __bf16 bf16x16 __attribute__((ext_vector_type(16)));

#define DI __device__ __forceinline__
#define LDS_PTR(T, p) ((__attribute__((address_space(3))) T*)(p))
#define BARRIER() asm volatile("s_barrier" ::: "memory")
#define BAR_LGKM() asm volatile("s_waitcnt lgkmcnt(0)\n\ts_barrier" ::: "memory")
#define BAR_ALL() asm volatile("s_waitcnt vmcnt(0) lgkmcnt(0)\n\ts_barrier" ::: "memory")
#define DRAIN() asm volatile("s_waitcnt vmcnt(0)" ::: "memory")
#define MFMA32(a, b, c) __builtin_amdgcn_mfma_f32_32x32x16_bf16(a, b, c, 0, 0, 0)
#define MFMA16(a, b, c) __builtin_amdgcn_mfma_f32_16x16x32_bf16(a, b, c, 0, 0, 0)
#define PIN(x) asm volatile("" : "+v"(x))
#define EX2(x) __builtin_amdgcn_exp2f(x)
#define RCP(x) __builtin_amdgcn_rcpf(x)
#define SBAR() __builtin_amdgcn_sched_barrier(0)

#define STAMP_INIT
#define STAMPV(i)
#define STAMP_END
#define STAMP_DECL
#define STAMP_OUTS
#define STAMP_PUT(i)
#define JIT()
constexpr int D_ = 1024, F_ = 3584;
constexpr float QS = 0.17677669529663687f * 1.4426950408889634f;

constexpr unsigned MBY = 1u << 20;
constexpr unsigned TS = 65536 + 4096, HS = 262144 + 4096;
constexpr unsigned OFF_HF = 0, OFF_X1 = 5 * MBY;
constexpr unsigned OFF_OF = 10 * MBY;
constexpr unsigned OFF_KV = 20 * MBY;
constexpr unsigned OFF_FL = 40 * MBY;
constexpr unsigned FL_EPOCH = OFF_FL;
constexpr unsigned FL_KV = OFF_FL + 4096;
constexpr unsigned FL_O = FL_KV + 65536;
constexpr unsigned FL_X = FL_O + 65536;
constexpr unsigned OFF_CS = 41 * MBY;
constexpr unsigned OFF_SX = 42 * MBY;
constexpr unsigned SX_ST = 0;
constexpr unsigned SX_ROW = 512 * 1024;
constexpr unsigned SX_CAND = SX_ROW + 256 * 1024;
constexpr unsigned SX_GP = SX_CAND + 32 * 1024;
constexpr unsigned SX_YP = SX_GP + 224 * 1024;
constexpr unsigned SX_LAYER = SX_YP + 224 * 1024;
constexpr unsigned SX_QP = 2 * SX_LAYER;
constexpr unsigned SX_BYTES = SX_QP + 512 * 1024;
static_assert(SX_BYTES <= 6 * 256 * 128 * 16, "sentinel fill");
constexpr unsigned WS_BYTES = 48 * MBY, OOB = 0x80000000u;
constexpr unsigned SENT = 0xFFFFFFFFu;

constexpr int NS = 9, DD = 3, SLOTB = 12352, HALFB = 6176;
constexpr int L_XCH = 122880;
constexpr int L_KX = L_XCH - 8192, L_QX = L_XCH + 8192;
constexpr int L_SL = L_XCH + 8192;
constexpr int L_CAND = L_XCH + 32768;
constexpr int L_AB = L_XCH + 34816;
constexpr int L_FULL = 159744, L_FREE = L_FULL + 512;
constexpr int L_CS = L_FREE + 1024, L_PATCH = L_CS + 768;
constexpr int L_GU = L_PATCH + 768, L_TAG = L_GU + 256, L_WR = L_TAG + 256;
constexpr int L_X2F = L_WR + 128, L_X2P = L_X2F + 128;
constexpr int L_W = L_X2P + 128;
constexpr int LDS_BYTES = 163840;
static_assert(L_W + 128 <= LDS_BYTES && NS * SLOTB + 256 <= L_KX && L_AB + 2048 <= L_FULL, "LDS map");

DI int fresh_lane() { int l; asm volatile("v_mbcnt_lo_u32_b32 %0, -1, 0\n\tv_mbcnt_hi_u32_b32 %0, -1, %0" : "=v"(l)); return l; }
#define LANE_VARS const int lane = fresh_lane(), r32 = lane & 31, hi = lane >> 5, tq = (lane & 15) >> 2, tp = lane & 3, tblk = (lane >> 4) & 1, \
  wrow = 4 * hi + tq, wsw = 4 * ((wrow >> 1) & 1), wch = 2 * tblk + (tp >> 1), c24 = r32 < 24 ? r32 : 23; \
  (void)r32; (void)hi; (void)wsw; (void)wch; (void)c24
DI int fresh_s(int v) { asm volatile("" : "+s"(v)); return v; }
#define PHASE_VARS const int tm = fresh_s(tm_), tn = fresh_s(tn_), wid = fresh_s(wid_), tile = tm * 4 + wid, hh = wid & 1, kh = wid >> 1; (void)tile; (void)hh; (void)kh; LANE_VARS
DI int kpos(int tm, int tn, int i) { return i == 0 ? tn : (tn + 1 + (1 * (3 - (tn & 3)) + i - 1) % 15) & 15; }
DI unsigned pack2(float lo, float hi) { f32x2 v = {lo, hi}; return __builtin_bit_cast(unsigned, __builtin_convertvector(v, bf16x2)); }
DI float rbf(float f) { return __uint_as_float(pack2(f, 0.f) << 16); }
DI float lo16(unsigned u) { return __uint_as_float(u << 16); }
DI float hi16(unsigned u) { return __uint_as_float(u & 0xffff0000u); }
DI float opq(float r) { asm("" : "+v"(r)); return r; }
DI float fadd(float a, float b) { return opq(a + b); }
DI float fmul(float a, float b) { return opq(a * b); }
DI float ffma(float a, float b, float c) { return opq(__builtin_fmaf(a, b, c)); }
DI float U2F(unsigned u) { return __uint_as_float(u); }
DI float half_swap_sum(float v) {
  auto r = __builtin_amdgcn_permlane32_swap(__float_as_uint(v), __float_as_uint(v), false, false);
  return fadd(U2F(r[0]), U2F(r[1]));
}
DI float half_swap_max(float v) {
  auto r = __builtin_amdgcn_permlane32_swap(__float_as_uint(v), __float_as_uint(v), false, false);
  return fmaxf(__uint_as_float(r[0]), __uint_as_float(r[1]));
}
template <int CTRL> DI unsigned dpp(unsigned v) { return (unsigned)__builtin_amdgcn_update_dpp(0, (int)v, CTRL, 0xf, 0xf, false); }
DI float row_swap_sum(float v) {
  auto r = __builtin_amdgcn_permlane16_swap(__float_as_uint(v), __float_as_uint(v), false, false);
  return fadd(U2F(r[0]), U2F(r[1]));
}
DI float half_sum(float v) {
  v = fadd(v, U2F(dpp<0xB1>(__float_as_uint(v)))); v = fadd(v, U2F(dpp<0x4E>(__float_as_uint(v))));
  v = fadd(v, U2F(dpp<0x141>(__float_as_uint(v)))); v = fadd(v, U2F(dpp<0x140>(__float_as_uint(v))));
  return row_swap_sum(v);
}
DI float wave_sum(float v) { return half_swap_sum(half_sum(v)); }
DI unsigned half_max_u32(unsigned v) {
  v = max(v, dpp<0xB1>(v)); v = max(v, dpp<0x4E>(v)); v = max(v, dpp<0x141>(v)); v = max(v, dpp<0x140>(v));
  auto r = __builtin_amdgcn_permlane16_swap(v, v, false, false);
  return max(r[0], r[1]);
}
DI unsigned wave_max_u32(unsigned v) {
  v = half_max_u32(v);
  auto r = __builtin_amdgcn_permlane32_swap(v, v, false, false);
  return max(r[0], r[1]);
}
DI u32x4 fo_swap(u32x4 v) {
  auto a = __builtin_amdgcn_permlane32_swap(v[0], v[2], false, false);
  auto b = __builtin_amdgcn_permlane32_swap(v[1], v[3], false, false);
  u32x4 o; o[0] = a[0]; o[1] = b[0]; o[2] = a[1]; o[3] = b[1];
  return o;
}
DI void row_swap(u32x4& a, u32x4& b) {
#pragma unroll
  for (int e = 0; e < 4; ++e) { auto r = __builtin_amdgcn_permlane16_swap(a[e], b[e], false, false); a[e] = r[0]; b[e] = r[1]; }
}
DI void glds4(const void* sb, unsigned vo, unsigned lds_dst) {
  unsigned keep;
  asm volatile("s_mov_b32 %0, m0\n\ts_mov_b32 m0, %3\n\ts_nop 0\n\tglobal_load_lds_dword %1, %2\n\ts_mov_b32 m0, %0"
               : "=&s"(keep) : "v"(vo), "s"(sb), "s"(lds_dst) : "memory");
}
#define GL_ "global_load_lds_dwordx4 "
#define GP_ "\n\ts_sleep %8\n\ts_waitcnt vmcnt(%7)\n\t"
template <int TH, int SL> DI void glds_v4t(const void* sb, const unsigned (&v)[4], unsigned m0v) {
  unsigned keep;
  asm volatile("s_mov_b32 %0, m0\n\ts_mov_b32 m0, %6\n\ts_nop 0\n\t" GL_ "%1, %5" GP_ GL_ "%2, %5 offset:1024" GP_ GL_ "%3, %5 offset:2048" GP_ GL_ "%4, %5 offset:3072" GP_ "s_mov_b32 m0, %0"
               : "=&s"(keep) : "v"(v[0]), "v"(v[1]), "v"(v[2]), "v"(v[3]), "s"(sb), "s"(m0v), "n"(TH), "n"(SL) : "memory");
}
DI void glds_v3(const void* sb, const unsigned (&v)[3], unsigned m0v) {
  unsigned keep;
  asm volatile("s_mov_b32 %0, m0\n\ts_mov_b32 m0, %5\n\ts_nop 0\n\t" GL_ "%1, %4\n\t" GL_ "%2, %4 offset:1024\n\t" GL_ "%3, %4 offset:2048\n\t"
               "s_mov_b32 m0, %0" : "=&s"(keep) : "v"(v[0]), "v"(v[1]), "v"(v[2]), "s"(sb), "s"(m0v) : "memory");
}
DI float dot2(unsigned a, unsigned b, float c) {
  return __builtin_amdgcn_fdot2_f32_bf16(__builtin_bit_cast(bf16x2, a), __builtin_bit_cast(bf16x2, b), c, false);
}
#define CODE_ADDR(sym, out) do { unsigned long long pc_; unsigned off_; \
  asm volatile("s_getpc_b64 %0\n1:\n\ts_mov_b32 %1, " #sym "-1b" : "=s"(pc_), "=s"(off_)); out = (const char*)(pc_ + (long long)(int)off_); } while (0)
DI void st16(void* p, u32x4 v) { asm volatile("global_store_dwordx4 %0, %1, off sc0 sc1\n\ts_nop 1" :: "v"(p), "v"(v) : "memory"); }
DI void ln_scalars(float mean, float var, float& mb, float& den) { mb = rbf(mean); den = rbf(__builtin_amdgcn_sqrtf(rbf(rbf(var) + 1e-5f))); }
DI unsigned ln_pair(unsigned v, float mb, float rd) {
  const unsigned c = pack2(lo16(v) - mb, hi16(v) - mb);
  return pack2(lo16(c) * rd, hi16(c) * rd);
}
DI unsigned ln_pair_s(unsigned v, float nmb, float rd) {
  const unsigned c = pack2(fadd(lo16(v), nmb), fadd(hi16(v), nmb));
  return pack2(fmul(lo16(c), rd), fmul(hi16(c), rd));
}
template <int N, int M> DI f32x4 sum4(const u32x4 (&v)[M]) {
  f32x4 t;
#pragma unroll
  for (int e = 0; e < 4; ++e) {
    t[e] = U2F(v[0][e]);
#pragma unroll
    for (int i = 1; i < N; ++i) t[e] = fadd(t[e], U2F(v[i][e]));
  }
  return t;
}
#define PERM_LO(x, y) __builtin_amdgcn_perm(y, x, 0x05040100u)
#define PERM_HI(x, y) __builtin_amdgcn_perm(y, x, 0x07060302u)
constexpr unsigned SPIN_CAP = 1u << 15;
DI unsigned nz(unsigned v) { return min(v, SENT - 1u); }
DI unsigned nzf(float v) { return nz(__float_as_uint(v)); }
DI unsigned mx4(unsigned m, const u32x4& v) { return max(max(max(m, v[0]), v[1]), max(v[2], v[3])); }
template <int N> DI bool none_sent(const u32x4 (&v)[N]) {
  unsigned m = 0u;
#pragma unroll
  for (int i = 0; i < N; ++i) m = mx4(m, v[i]);
  return m != SENT;
}
typedef __amdgpu_buffer_rsrc_t rsrc_t;
DI u32x4 ldg(rsrc_t rs, unsigned off) { return __builtin_amdgcn_raw_buffer_load_b128(rs, (int)off, 0, 16); }
DI void stg(rsrc_t rs, unsigned off, u32x4 v) { __builtin_amdgcn_raw_buffer_store_b128(v, rs, (int)off, 0, 16); }
DI void poll_pause(bool slow) { if (slow) __builtin_amdgcn_s_sleep(4); asm volatile("s_sleep 1" ::: "memory"); }
template <int N, class LD, class OKF> DI void poll_all(u32x4 (&out)[N], bool slow, LD ld, OKF okf) {
  for (unsigned spins = 0;; ++spins) {
    ld(out);
    SBAR();
    if (__all(okf(out)) || spins > SPIN_CAP) break;
    poll_pause(slow);
  }
}
DI void lds_set(unsigned addr, unsigned v) { asm volatile("s_waitcnt lgkmcnt(0)\n\tds_write_b32 %0, %1" :: "v"(addr), "v"(v) : "memory"); }
template <int SLEEP = 1> DI void lds_wait_ge(unsigned addr, unsigned need) {
  unsigned spin = 0x100000, v;
  asm volatile("1:\n\tds_read_b32 %0, %2\n\ts_sub_u32 %1, %1, 1\n\ts_waitcnt lgkmcnt(0)\n\ts_cbranch_scc1 2f\n\tv_cmp_gt_u32 vcc, %3, %0\n\ts_cbranch_vccz 2f\n\ts_sleep %4\n\ts_branch 1b\n2:"
               : "=&v"(v), "+&s"(spin) : "v"(addr), "s"(need), "n"(SLEEP) : "vcc", "scc", "memory");
}
DI s16x8 tr2(const char* p, int second) {
  s16x4 lo = __builtin_amdgcn_ds_read_tr16_b64_v4i16(LDS_PTR(s16x4, p)), hi = __builtin_amdgcn_ds_read_tr16_b64_v4i16(LDS_PTR(s16x4, p + second));
  return __builtin_shufflevector(lo, hi, 0, 1, 2, 3, 4, 5, 6, 7);
}
DI u32x16 cat4(const u32x4& a, const u32x4& b, const u32x4& c, const u32x4& d) {
  const u32x8 x = __builtin_shufflevector(a, b, 0, 1, 2, 3, 4, 5, 6, 7), y = __builtin_shufflevector(c, d, 0, 1, 2, 3, 4, 5, 6, 7);
  return __builtin_shufflevector(x, y, 0, 1, 2, 3, 4, 5, 6, 7, 8, 9, 10, 11, 12, 13, 14, 15);
}

DI unsigned stats_dst(unsigned sx, int tile, int tn, int lane) { return lane < 32 ? sx + SX_ST + tile * 8192 + tn * 512 + lane * 16 : OOB; }
DI void row_stats_sum(rsrc_t rs, unsigned sx, int tile, int lane, float& S, float& Q, float& Dd) {
  const unsigned exb = sx + SX_ST + tile * 8192 + (lane & 31) * 16;
  u32x4 pv[8];
  poll_all<8>(pv, false, [&](u32x4 (&v)[8]) {
#pragma unroll
    for (int i = 0; i < 8; ++i) v[i] = ldg(rs, exb + ((lane >> 5) * 8 + i) * 512);
  }, [&](const u32x4 (&v)[8]) { return none_sent(v); });
  S = 0.f; Q = 0.f; Dd = 0.f;
#pragma unroll
  for (int i = 0; i < 8; ++i) { S += __uint_as_float(pv[i][0]); Q += __uint_as_float(pv[i][1]); Dd += __uint_as_float(pv[i][2]); }
}
DI void row_stats_fin(rsrc_t rs, unsigned kdst, char* smem, int epoch, int tile, int lane, float S, float Q, float Dd, float wsum, const u32x4 (&x1p)[4],
                      float& mean, float& var, float& mb, float& rd) {
  S = half_swap_sum(S); Q = half_swap_sum(Q); Dd = half_swap_sum(Dd);
  mean = S * (1.0f / 1024.0f); var = fmaxf(Q * (1.0f / 1024.0f) - mean * mean, 0.f);
  float den;
  ln_scalars(mean, var, mb, den);
  rd = RCP(den);
  const unsigned rb = pack2((Dd - mb * wsum) * rd, 0.f) & 0xffffu;
  const unsigned mine = (((rb & 0x8000u) ? (~rb & 0xffffu) : (rb | 0x8000u)) << 16) | (unsigned)(2047 - (tile * 32 + (lane & 31)));
  const bool best = half_max_u32(mine) == mine;
  __builtin_amdgcn_raw_buffer_store_b32(mine, rs, (best && lane < 32) ? kdst : OOB, 0, 16);
  if (best) {
    char* c = smem + L_CAND + (tile & 3) * 144;
#pragma unroll
    for (int i = 0; i < 4; ++i) *reinterpret_cast<u32x4*>(c + (2 * i + (lane >> 5)) * 16) = x1p[i];
    *reinterpret_cast<u32x4*>(c + 128) = u32x4{mine, __float_as_uint(mb), __float_as_uint(rd), 0u};
  }
  lds_set((unsigned)(uintptr_t)smem + L_W + 32 + (tile & 3) * 4, epoch);
}

DI void attn_safe(rsrc_t rs, unsigned kvoff, const u32x8& q, const u32x4& ones, f32x16& o) {
  const s16x8 q0 = __builtin_bit_cast(s16x8, __builtin_shufflevector(q, q, 0, 1, 2, 3)), q1 = __builtin_bit_cast(s16x8, __builtin_shufflevector(q, q, 4, 5, 6, 7));
  f32x4 l = {0.f, 0.f, 0.f, 0.f};
#pragma unroll
  for (int e = 0; e < 16; ++e) o[e] = 0.f;
  float m = -INFINITY;
#pragma unroll 1
  for (int kt = 0; kt < 64; ++kt) {
    const unsigned p = kvoff + kt * 4096;
    const u32x4 k0 = ldg(rs, p), k1 = ldg(rs, p + 1024), v0 = ldg(rs, p + 2048), v1 = ldg(rs, p + 3072);
    f32x16 s = {};
    s = MFMA32(__builtin_bit_cast(s16x8, k0), q0, s);
    s = MFMA32(__builtin_bit_cast(s16x8, k1), q1, s);
    float mt = fmaxf(s[0], s[1]);
#pragma unroll
    for (int e = 2; e < 16; ++e) mt = fmaxf(mt, s[e]);
    const float mn = fmaxf(m, half_swap_max(mt));
    const float alpha = EX2(m - mn);
    m = mn;
#pragma unroll
    for (int e = 0; e < 16; ++e) { s[e] = EX2(s[e] - mn); o[e] *= alpha; }
    l[0] *= alpha;
    const bf16x16 pa = __builtin_bit_cast(bf16x16, __builtin_shufflevector(s, s, 0, 1, 2, 3, 4, 5, 6, 7));
    const bf16x16 pb = __builtin_bit_cast(bf16x16, __builtin_shufflevector(s, s, 8, 9, 10, 11, 12, 13, 14, 15));
    o = __builtin_amdgcn_smfmac_f32_32x32x32_bf16(__builtin_bit_cast(bf16x8, v0), pa, o, 0xdddd, 0, 0);
    l = __builtin_amdgcn_smfmac_f32_16x16x64_bf16(__builtin_bit_cast(bf16x8, ones), pa, l, 0xdddd, 0, 0);
    o = __builtin_amdgcn_smfmac_f32_32x32x32_bf16(__builtin_bit_cast(bf16x8, v1), pb, o, 0xdddd, 0, 0);
    l = __builtin_amdgcn_smfmac_f32_16x16x64_bf16(__builtin_bit_cast(bf16x8, ones), pb, l, 0xdddd, 0, 0);
  }
  o *= 1.0f / l[0];
}

extern "C" __global__ __launch_bounds__(512, 2) void mega_kernel(const u16* __restrict__ x0, const u16* __restrict__ wqkv, const u16* __restrict__ wo,
                                                              const u16* __restrict__ w_r, const u16* __restrict__ wg, const u16* __restrict__ wu,
                                                              const u16* __restrict__ wd, char* ws, u16* __restrict__ out) {
  __shared__ __attribute__((aligned(16))) char smem[LDS_BYTES];
  const int wid = __builtin_amdgcn_readfirstlane(threadIdx.x >> 6);
  STAMP_INIT;
  STAMPV(0);
  const int bid = blockIdx.x, xcd = bid & 7, loc = bid >> 3;
  const int tm = 8 * (xcd >> 2) + (loc >> 2), tn = 4 * (xcd & 3) + (loc & 3);
  const unsigned lbase = (unsigned)(uintptr_t)smem;
  const rsrc_t rs = __builtin_amdgcn_make_buffer_rsrc(ws, 0, (int)WS_BYTES, 0x00020000);
  volatile __attribute__((address_space(3))) unsigned* words = LDS_PTR(volatile unsigned, smem + L_W);
  u32x4 v[4];
  if (wid < 4)
    asm volatile("global_load_dwordx4 %0, %4, %5\n\tglobal_load_dwordx4 %1, %4, %5 offset:512\n\tglobal_load_dwordx4 %2, %4, %5 offset:1024\n\tglobal_load_dwordx4 %3, %4, %5 offset:1536"
                 : "=&v"(v[0]), "=&v"(v[1]), "=&v"(v[2]), "=&v"(v[3])
                 : "v"((threadIdx.x >> 5) * (D_ * 2) + (threadIdx.x & 31) * 16), "s"(x0 + (size_t)(tm * 128 + tn * 8) * D_) : "memory");
  else {
    asm volatile("" : "=v"(v[0]), "=v"(v[1]), "=v"(v[2]), "=v"(v[3]));
    if (wid == 6) {
      const char *c0, *c1;
      CODE_ADDR(mega_kernel, c0); CODE_ADDR(end_marker, c1);
      const unsigned o = (loc * 64 + (threadIdx.x & 63)) * 32;
      glds4(c0, (long long)o + 4 <= c1 - c0 ? o : 0u, lbase + NS * SLOTB);
    }
  }
  STAMPV(48);
  *reinterpret_cast<unsigned*>(smem + L_FULL + threadIdx.x * 4) = 0u;
  words[threadIdx.x & 31] = 0u;
  BAR_LGKM();
  if (wid < 4) {
    const int tid = threadIdx.x, lane = tid & 63, r32 = lane & 31, row = 2 * wid + (lane >> 5);
    char* sm = smem + L_XCH;
    unsigned dst = OFF_HF + (tm * 4 + (tn >> 2)) * TS + (tn & 3) * 128 + (tid >> 3) * 512 + (tid & 7) * 16;
    asm volatile("s_waitcnt vmcnt(0)" : "+v"(v[0]), "+v"(v[1]), "+v"(v[2]), "+v"(v[3]), "+v"(dst) :: "memory");
    STAMPV(49);
    float s = 0.f, s2 = 0.f;
#pragma unroll
    for (int i = 0; i < 4; ++i)
#pragma unroll
      for (int e = 0; e < 4; e += 2) { s = dot2(v[i][e], 0x3f803f80u, s); s2 = dot2(v[i][e + 1], 0x3f803f80u, s2); }
    const float mean = half_sum(s + s2) * (1.0f / 1024.0f);
    f32x2 m2 = {0.f, 0.f};
#pragma unroll
    for (int i = 0; i < 4; ++i)
#pragma unroll
      for (int e = 0; e < 4; ++e) { const f32x2 d = f32x2{lo16(v[i][e]), hi16(v[i][e])} - mean; m2 += d * d; }
    float mb, den;
    ln_scalars(mean, half_sum(m2[0] + m2[1]) * (1.0f / 1024.0f), mb, den);
    const float rd = 1.0f / den;
#pragma unroll
    for (int i = 0; i < 4; ++i) {
      char* p = sm + ((16 * i + (r32 >> 1)) * 2) * 144 + row * 16 + (r32 & 1) * 8;
      *reinterpret_cast<u32x2*>(p) = u32x2{ln_pair(v[i][0], mb, rd), ln_pair(v[i][1], mb, rd)};
      *reinterpret_cast<u32x2*>(p + 144) = u32x2{ln_pair(v[i][2], mb, rd), ln_pair(v[i][3], mb, rd)};
    }
    STAMPV(37);
    lds_set(lbase + L_W + 64 + 4 * wid, 1);
    lds_wait_ge<0>(lbase + L_W + 64 + 4 * (lane & 3), 1);
    STAMPV(50);
    u32x4 t[4];
#pragma unroll
    for (int i = 0; i < 4; ++i) t[i] = *reinterpret_cast<const u32x4*>(sm + (i * 32 + (tid >> 3)) * 144 + (tid & 7) * 16);
    SBAR();
#pragma unroll
    for (int i = 0; i < 4; ++i) stg(rs, dst + i * 16384, t[i]);
    STAMPV(51);
    DRAIN();
    lds_set(lbase + L_W + 64 + 4 * wid, 2);
    STAMPV(38);
  }
  const int lw = wid & 1;
  const char* wb = reinterpret_cast<const char*>(wqkv + (size_t)(16 * lw) * (3 * D_) + tn * 64);
  auto stage_lanes = [](unsigned (&wv)[3]) {
    const int lane = fresh_lane();
#pragma unroll
    for (int i = 0; i < 3; ++i) {
      const int li = lane + 64 * i, k = li / 24, cp = li % 24, c = cp ^ (4 * ((k >> 1) & 1));
      wv[i] = (unsigned)(k * (3 * D_ * 2) + (c >> 3) * (D_ * 2) + (c & 7) * 16 - i * 1024);
    }
  };
  auto stage = [&](const unsigned (&wv)[3], int s, int slot) {
    const char* p = wb + (size_t)(2 * kpos(tm, tn, s >> 1) + (s & 1)) * (32 * 3 * D_ * 2);
    glds_v3(p, wv, lbase + slot * SLOTB + lw * HALFB);
    glds_v3(p + 8 * (3 * D_ * 2), wv, lbase + slot * SLOTB + lw * HALFB + 3072);
  };
  auto exchange = [&](const u32x2& tag, unsigned line0, int me, bool bar = false) {
    const int lane = fresh_lane();
    __builtin_amdgcn_raw_buffer_store_b64(tag, rs, lane == 0 ? line0 + me * 128 : OOB, 0, 16);
    if (bar) BARRIER();
    JIT();
    unsigned po = lane < 16 ? line0 + lane * 128 : OOB;
    const u32x4 srd = {(unsigned)(uintptr_t)ws, (unsigned)((uintptr_t)ws >> 32) & 0xffffu, WS_BYTES, 0x00020000u};
    for (unsigned spins = 0;; ++spins) {
      u32x2 v = {0u, 0u};
      asm volatile("buffer_atomic_or_x2 %0, %1, %2, 0 offen sc0 sc1\n\ts_waitcnt vmcnt(0)" : "+v"(v) : "v"(po), "s"(srd) : "memory");
      po = ((v[0] == tag[0]) & (v[1] == tag[1])) ? OOB : po;
      if (__all(po == OOB) || spins > SPIN_CAP) break;
    }
  };
  if ((wid >> 1) == 2) {
    unsigned wv[3];
    stage_lanes(wv);
    const int r = tm & 7;
#pragma unroll 1
    for (int i = 0; i < NS; ++i) {
      const int s = i == 0 ? r : i <= r ? i - 1 : i;
      stage(wv, s, s);
      const int j = i - 1, full = j < 1 ? (j == 0 && r == 0) : j < r ? j : j + 1;
      asm volatile("s_waitcnt vmcnt(%2)\n\tds_write_b32 %0, %1" :: "v"(lbase + L_FULL + lw * 256), "v"(full), "n"(6 * 1) : "memory");
    }
    asm volatile("s_waitcnt vmcnt(0)\n\tds_write_b32 %0, %1" :: "v"(lbase + L_FULL + lw * 256), "v"(NS) : "memory");
    STAMPV(1);
  }
  if (wid == 7) {
    u32x2 tag = __builtin_amdgcn_raw_buffer_load_b64(rs, FL_EPOCH, 0, 16);
    tag[0] += 1u; tag[1] += 0x9E3779B9u;
    *reinterpret_cast<u32x2*>(smem + L_TAG) = tag;
    lds_wait_ge<0>(lbase + L_W + 64 + 4 * (fresh_lane() & 3), 2);
    exchange(tag, FL_X + tm * 2048, tn);
    lds_set(lbase + L_W + 24, 1);
  }
  if (wid >= 4) {
    auto wo_rows = [&](int g0, int n, int gs) {
      const int lane = fresh_lane();
      unsigned wov[4];
#pragma unroll
      for (int i = 0; i < 4; ++i) wov[i] = (unsigned)((8 * i + (lane >> 3)) * (D_ * 2) + ((lane & 7) ^ (4 * ((lane >> 4) & 1)) ^ (i >> 1)) * 16 - i * 1024);
#pragma unroll 1
      for (int i = 0, k = (0 * (tm & 7)) % n; i < n; ++i, k = k + 1 == n ? 0 : k + 1) {
        const int g = g0 + gs * k;
        glds_v4t<3, 1>(reinterpret_cast<const char*>(wo) + (size_t)((((tn >> 2) + 1) * 256 + 32 * g) & 1023) * (D_ * 2) + tn * 128, wov, lbase + g * 4096);
      }
    };
    if (wid < 6) {
      const int lane = fresh_lane();
      unsigned wv[3];
      stage_lanes(wv);
      __builtin_amdgcn_s_setprio(3);
      const unsigned fra = lbase + L_FREE + (lane & 3) * 256, fla = lbase + L_FULL + lw * 256 + lane * 4;
      u32x4 pre[NS * 6];
#pragma unroll
      for (int i = 0; i < NS * 6; ++i) pre[i] = *reinterpret_cast<const u32x4*>(smem + lw * HALFB + lane * 16 + (i / 6) * SLOTB + (i % 6) * 1024);
#pragma unroll
      for (int i = 0; i < NS * 6; ++i) PIN(pre[i]);
#pragma unroll 1
      for (int L = 0; L < 2; ++L) {
        {
          constexpr unsigned STG = 32 * 3 * D_ * 2;
          unsigned off = (2 * kpos(tm, tn, NS >> 1) + (NS & 1)) * STG, dst = lbase + lw * HALFB, need = 32 * L + 3, cnt = 31 - NS, pub = 32 * L + NS - DD, keep, spin, t0, t1;
          asm volatile(
            "s_mov_b32 %[keep], m0\n\ts_branch 3f\n"
            "1:\n\ts_cbranch_vccnz 3f\n"
            "2:\n\ts_add_u32 s56, %[blo], %[off]\n\ts_addc_u32 s57, %[bhi], 0\n\ts_mov_b32 m0, %[dst]\n\ts_add_u32 s58, s56, %[half]\n\t"
            GL_ "%[w0], s[56:57]\n\ts_nop 7\n\t" GL_ "%[w1], s[56:57] offset:1024\n\ts_nop 7\n\t" GL_ "%[w2], s[56:57] offset:2048\n\ts_nop 7\n\t"
            "s_addc_u32 s59, s57, 0\n\ts_add_u32 m0, %[dst], 3072\n\ts_add_u32 %[off], %[off], %[stg]\n\t"
            GL_ "%[w0], s[58:59]\n\ts_nop 7\n\t" GL_ "%[w1], s[58:59] offset:1024\n\ts_nop 7\n\t" GL_ "%[w2], s[58:59] offset:2048\n\ts_nop 7\n\t"
            "ds_read_b32 %[t0], %[fra]\n\t"
            "s_cmp_eq_u32 %[off], %[wrap]\n\ts_cselect_b32 %[off], 0, %[off]\n\ts_cmp_eq_u32 %[off], %[own]\n\ts_cselect_b32 %[off], %[own2], %[off]\n\t"
            "s_add_u32 %[dst], %[dst], %[slot]\n\ts_cmp_eq_u32 %[dst], %[dend]\n\ts_cselect_b32 %[dst], %[dbeg], %[dst]\n\t"
            "v_add_u32 %[pub], 1, %[pub]\n\ts_add_u32 %[need], %[need], 1\n\tv_max_u32 %[t1], %[pmin], %[pub]\n\t"
            "s_waitcnt vmcnt(%[vm]) lgkmcnt(0)\n\tds_write_b32 %[fla], %[t1]\n\tv_cmp_gt_u32 vcc, %[need], %[t0]\n\t"
            "s_sub_u32 %[cnt], %[cnt], 1\n\ts_cbranch_scc0 1b\n\ts_branch 5f\n"
            "3:\n\ts_mov_b32 %[spin], 0x100000\n"
            "4:\n\tds_read_b32 %[t0], %[fra]\n\ts_sub_u32 %[spin], %[spin], 1\n\ts_waitcnt lgkmcnt(0)\n\ts_cbranch_scc1 2b\n\t"
            "v_cmp_gt_u32 vcc, %[need], %[t0]\n\ts_cbranch_vccz 2b\n\ts_sleep 1\n\ts_branch 4b\n"
            "5:\n\ts_mov_b32 m0, %[keep]"
            : [off] "+&s"(off), [dst] "+&s"(dst), [need] "+&s"(need), [cnt] "+&s"(cnt), [pub] "+&v"(pub), [keep] "=&s"(keep), [spin] "=&s"(spin), [t0] "=&v"(t0), [t1] "=&v"(t1)
            : [blo] "s"((unsigned)(uintptr_t)wb), [bhi] "s"((unsigned)((uintptr_t)wb >> 32)), [half] "s"(STG / 4), [stg] "s"(STG), [wrap] "s"(32 * STG), [own] "s"(2 * tn * STG),
              [own2] "s"(2 * ((tn + 1) & 15) * STG), [slot] "s"(SLOTB), [dbeg] "s"(lbase + lw * HALFB), [dend] "s"(lbase + lw * HALFB + NS * SLOTB), [pmin] "s"(32 * L + NS),
              [w0] "v"(wv[0]), [w1] "v"(wv[1]), [w2] "v"(wv[2]), [fra] "v"(fra), [fla] "v"(fla), [vm] "n"(DD * 6)
            : "s56", "s57", "s58", "s59", "vcc", "scc", "memory");
        }
#pragma unroll
        for (int d = DD - 1; d >= 0; --d) asm volatile("s_waitcnt vmcnt(%0)\n\tds_write_b32 %1, %2" :: "n"(d * 6), "v"(fla), "v"(32 * L + 32 - d) : "memory");
        u32x2 tag = *reinterpret_cast<const u32x2*>(smem + L_TAG);
        PIN(tag);
        if (lw == 0) {
          lds_wait_ge(lbase + L_W + 96 + 4 * (lane & 1), 1);
          lds_wait_ge<0>(lbase + L_W + 80 + 4 * (lane & 3), 2 * L + 1);
          exchange(tag, FL_KV + (L * 16 + tn) * 2048, tm, true);
          lds_wait_ge(fra, 32 * L + 32);
          lds_set(lbase + L_W + 16, L + 1);
        } else { BAR_LGKM(); lds_wait_ge<8>(lbase + L_W + 16, L + 1); }
        __builtin_amdgcn_s_sleep(24);
        wo_rows(lw, 15, 2);
        STAMPV(7 + 16 * L);
        DRAIN();
        const unsigned fo = FL_O + (L * 16 + tm) * 2048;
        if (lw) {
          lds_set(lbase + L_W + 112, 2 * L + 1);
          lds_wait_ge<4>(lbase + L_W + 48 + 4 * (lane & 3), L + 1);
          lds_wait_ge<0>(lbase + L_W + 80 + 4 * (lane & 3), 2 * L + 2);
          __builtin_amdgcn_raw_buffer_store_b64(tag, rs, lane == 0 ? fo + tn * 128 : OOB, 0, 16);
          JIT();
          wo_rows(30, 2, 1);
          DRAIN();
          lds_set(lbase + L_W + 112, 2 * L + 2);
        } else {
          lds_wait_ge<4>(lbase + L_W + 112, 2 * L + 1);
          lds_wait_ge<4>(lbase + L_W + 48 + 4 * (lane & 3), L + 1);
          unsigned po = lane < 16 ? fo + ((lane + 4 + (tn & 12)) & 15) * 128 : OOB;
          const u32x4 srd = {(unsigned)(uintptr_t)ws, (unsigned)((uintptr_t)ws >> 32) & 0xffffu, WS_BYTES, 0x00020000u};
          for (unsigned spins = 0;; ++spins) {
            u32x2 v = {0u, 0u};
            asm volatile("buffer_atomic_or_x2 %0, %1, %2, 0 offen sc0 sc1\n\ts_waitcnt vmcnt(0)" : "+v"(v) : "v"(po), "s"(srd) : "memory");
            po = ((v[0] == tag[0]) & (v[1] == tag[1])) ? OOB : po;
            unsigned m = (unsigned)__ballot(po == OOB);
            m &= m >> 1; m &= m >> 2;
            m = (m & 1u) | ((m >> 3) & 2u) | ((m >> 6) & 4u) | ((m >> 9) & 8u);
            if (m == 15u || spins > SPIN_CAP) break;
            words[5 + 2 * L] = words[28] == 2u * L + 2u ? m : m & 7u;
          }
          words[5 + 2 * L] = words[28] == 2u * L + 2u ? 15u : 7u;
          lds_wait_ge<0>(lbase + L_W + 112, 2 * L + 2);
          words[5 + 2 * L] = 15u;
          __builtin_amdgcn_raw_buffer_store_b64(tag, rs, (bid == 0 && lane == 0) ? FL_EPOCH : OOB, 0, 16);
        }
        BAR_ALL();
        if (L == 0) {
          char* d = smem + lw * HALFB + fresh_lane() * 16;
#pragma unroll
          for (int s = 0; s < NS; ++s) {
#pragma unroll
            for (int i = 0; i < 6; ++i) *reinterpret_cast<u32x4*>(d + s * SLOTB + i * 1024) = pre[s * 6 + i];
            lds_set(fla, 32 + s + 1);
            if (s == 1) lds_wait_ge(lbase + L_W + 64 + 4 * (lane & 3), 3);
          }
          STAMPV(2);
          lds_wait_ge<2>(lbase + L_W + 24, 2);
        }
      }
      STAMP_END;
      return;
    }
    const int a = wid - 6;
    const bool ffn = loc < 28;
    const int grp = xcd * 7 + (ffn ? loc >> 2 : 0), qa = (loc & 3) * 2 + a;
    const int kr = loc, cg = xcd;
    const unsigned wa = lbase + L_W;
    lds_wait_ge<2>(wa + 24, 1);
    __builtin_amdgcn_s_sleep(16);
    auto transpose8 = [](u32x4* v) {
      u32x4 t[8];
#pragma unroll
      for (int j = 0; j < 8; ++j)
#pragma unroll
        for (int e = 0; e < 4; ++e) t[j][e] = (j & 1) ? PERM_HI(v[2 * e][j >> 1], v[2 * e + 1][j >> 1]) : PERM_LO(v[2 * e][j >> 1], v[2 * e + 1][j >> 1]);
#pragma unroll
      for (int j = 0; j < 8; ++j) { v[j] = t[j]; PIN(v[j]); }
    };
    u32x4 gf[32];
    u32x4 wdf[16];
    auto wait_go = [&](int L) {
      __builtin_amdgcn_s_setprio(1);
      lds_wait_ge<8>(lbase + L_W + 16, L + 1);
      __builtin_amdgcn_s_sleep(24);
    };
    auto load_wd = [&](unsigned dv) {
      const rsrc_t rd = __builtin_amdgcn_make_buffer_rsrc(const_cast<u16*>(wd) + (size_t)grp * 64 * D_ + 128 * qa, 0, 64 * D_ * 2 - 256 * qa, 0x00020000);
#pragma unroll
      for (int i = 0; i < 16; ++i) {
        wdf[i] = __builtin_amdgcn_raw_buffer_load_b128(rd, dv, (32 * (i >> 3) + (i & 7)) * (D_ * 2), 0);
        asm volatile("s_sleep 2\n\ts_waitcnt vmcnt(2)" ::: "memory");
      }
    };
    {
      LANE_VARS;
      if (a == 0) {
        const u32x4 w0 = *reinterpret_cast<const u32x4*>(w_r + lane * 8), w1 = *reinterpret_cast<const u32x4*>(w_r + 512 + lane * 8);
        float t = 0.f;
#pragma unroll
        for (int e = 0; e < 4; ++e) t = fadd(fadd(t, fadd(lo16(w0[e]), hi16(w0[e]))), fadd(lo16(w1[e]), hi16(w1[e])));
        words[0] = __float_as_uint(wave_sum(t));
        if ((lane >> 3) == (tn & 7)) *reinterpret_cast<u32x4*>(smem + L_WR + (lane & 7) * 16) = tn < 8 ? w0 : w1;
      }
      const u16* p = wqkv + (size_t)(64 * tm + 32 * a + hi) * (3 * D_) + (c24 >> 3) * D_ + tn * 64 + (c24 & 7) * 8;
      float cs[8] = {0.f, 0.f, 0.f, 0.f, 0.f, 0.f, 0.f, 0.f};
      u32x4 v[16];
#pragma unroll
      for (int i = 0; i < 16; ++i) v[i] = *reinterpret_cast<const u32x4*>(p + (size_t)i * (2 * 3 * D_));
      SBAR();
      const u32x4 sv = {SENT, SENT, SENT, SENT};
#pragma unroll
      for (int i = 0; i < 6; ++i) {
        const unsigned o = ((i * 256 + bid) * 128 + a * 64 + lane) * 16;
        stg(rs, o < SX_BYTES ? OFF_SX + o : OOB, sv);
      }

#pragma unroll
      for (int i = 0; i < 16; ++i)
#pragma unroll
        for (int e = 0; e < 4; ++e) { cs[2 * e] = fadd(cs[2 * e], lo16(v[i][e])); cs[2 * e + 1] = fadd(cs[2 * e + 1], hi16(v[i][e])); }
      u32x4 o;
#pragma unroll
      for (int e = 0; e < 4; ++e) { const float x = half_swap_sum(cs[e]), y = half_swap_sum(cs[4 + e]); o[e] = __float_as_uint(hi ? y : x); }
      stg(rs, r32 < 24 ? OFF_CS + (tn * 32 + tm * 2 + a) * 768 + r32 * 32 + hi * 16 : OOB, o);
    }
    {
      DRAIN();
      lds_set(wa + 96 + 4 * a, 1);
      BAR_LGKM();
      LANE_VARS;
      uintptr_t gp = reinterpret_cast<uintptr_t>(((lane & 8) ? wu : wg) + (size_t)(128 * qa + 16 * hi + 4 * tblk) * F_ + 64 * grp + (lane & 7) * 8);
      unsigned dv = (lane >> 4) * (8 * D_ * 2) + (lane & 15) * 16;
      PIN(gp); PIN(dv);
      unsigned sv[3];
#pragma unroll
      for (int i = 0; i < 3; ++i) { const int li = lane + 64 * i, k = li / 24, c = li % 24; sv[i] = (unsigned)(k * (3 * D_ * 2) + (c >> 3) * (D_ * 2) + (c & 7) * 16 - i * 1024); PIN(sv[i]); }
      wait_go(0);
#pragma unroll
      for (int i = 0; i < 32; ++i) {
        gf[i] = *reinterpret_cast<const __attribute__((address_space(1))) u32x4*>(gp + (size_t)(32 * (i >> 3) + 8 * ((i >> 2) & 1) + (i & 3)) * (F_ * 2));
        asm volatile("s_sleep 2\n\ts_waitcnt vmcnt(2)" ::: "memory");
      }
      load_wd(dv);
      STAMPV(7);
      __builtin_amdgcn_s_setprio(0);
      lds_wait_ge<4>(wa + 80 + 4 * (lane & 3), 2);
#pragma unroll
      for (int g = 0; g < 4; ++g) glds_v3(wqkv + (size_t)(32 * kr + 8 * g) * (3 * D_) + (2 * cg + a) * 64, sv, lbase + L_SL + a * 12288 + g * 3072);
      transpose8(gf); transpose8(gf + 8); transpose8(gf + 16); transpose8(gf + 24);
      transpose8(wdf); transpose8(wdf + 8);
      {
        LANE_VARS;
        f32x4 t[2];
#pragma unroll
        for (int c = 0; c < 2; ++c) {
          u32x4 v[8];
#pragma unroll
          for (int i = 0; i < 8; ++i) v[i] = ldg(rs, OFF_CS + (tn * 32 + 16 * c + 2 * i + hi) * 768 + a * 384 + c24 * 16);
          SBAR();
          t[c] = sum4<8>(v);
          SBAR();
        }
#pragma unroll
        for (int e = 0; e < 4; ++e) t[0][e] = half_swap_sum(fadd(t[0][e], t[1][e]));
        if (lane < 24) *reinterpret_cast<f32x4*>(smem + L_CS + a * 384 + lane * 16) = t[0];
      }
      u32x2 tag = *reinterpret_cast<const u32x2*>(smem + L_TAG);
      PIN(tag);
      BAR_ALL();
      if (a) {
        lds_wait_ge<0>(wa + 64 + 4 * (lane & 3), 3);
        exchange(tag, FL_X + (16 + tm) * 2048, tn);
        lds_set(wa + 24, 2);
        STAMPV(12);
      }
    }
#pragma unroll 1
    for (int L = fresh_s(0);; ++L) {
      const unsigned sx = OFF_SX + L * SX_LAYER;
      STAMPV(1 + 16 * L);
      __builtin_amdgcn_s_setprio(1);
      u32x4 hf[4];
      unsigned key;
      { LANE_VARS;
      lds_wait_ge<0>(wa + 32 + 4 * tp, L + 1);
      if (a == 0) {
        const char* cp = smem + L_CAND + (r32 >> 3) * 144;
        const u32x4 c = *reinterpret_cast<const u32x4*>(cp + (lane & 7) * 16), rec = *reinterpret_cast<const u32x4*>(cp + 128);
        u32x4 o;
#pragma unroll
        for (int e = 0; e < 4; ++e) { unsigned h = ln_pair_s(c[e], -U2F(rec[1]), U2F(rec[2])); PIN(h); o[e] = nz(hi ? c[e] : h); }
        stg(rs, sx + SX_ROW + hi * 131072 + (tm * 4 + (r32 >> 3)) * 2048 + tn * 128 + (lane & 7) * 16, o);
        STAMPV(8 + 16 * L);
        lds_wait_ge<0>(wa + 104, L + 1);
        key = __builtin_amdgcn_readfirstlane(words[27]);
      } else {
        const u32x4 srd = {(unsigned)(uintptr_t)ws, (unsigned)((uintptr_t)ws >> 32) & 0xffffu, WS_BYTES, 0x00020000u};
        unsigned po = sx + SX_CAND + (tn * 16 + (lane >> 2)) * 128 + tp * 4;
        key = 0u;
        for (unsigned spins = 0;; ++spins) {
          unsigned v = 0u;
          asm volatile("buffer_atomic_or %0, %1, %2, 0 offen sc0 sc1\n\ts_waitcnt vmcnt(0)" : "+v"(v) : "v"(po), "s"(srd) : "memory");
          key = v == SENT ? key : max(key, v);
          po = v == SENT ? po : OOB;
          if (__all(po == OOB) || spins > SPIN_CAP) break;
        }
        key = __builtin_amdgcn_readfirstlane(wave_max_u32(key));
        words[27] = key;
        words[3] = 2047u - (key & 2047u);
        lds_set(wa + 104, L + 1);
        STAMPV(13 + 16 * L);
      }
      poll_all<4>(hf, false, [&](u32x4 (&t)[4]) {
#pragma unroll
        for (int i = 0; i < 4; ++i) t[i] = ldg(rs, sx + SX_ROW + ((2047 - (int)(key & 2047u)) >> 5) * 2048 + (16 * qa + 4 * i + (lane >> 4)) * 16);
      }, [&](const u32x4 (&t)[4]) { return none_sent(t); });
      if (L == 1) lds_set(wa + 4 + 4 * a, 2);
      }
      STAMPV(2 + 16 * L);
      f32x4 d[8];
      const f32x4 zero = {0.f, 0.f, 0.f, 0.f};
      SBAR();
      __builtin_amdgcn_s_setprio(3);
#pragma unroll
      for (int t = 0; t < 32; ++t) d[t & 7] = MFMA16(__builtin_bit_cast(s16x8, hf[t >> 3]), __builtin_bit_cast(s16x8, gf[t]), t < 8 ? zero : d[t & 7]);
      SBAR();
      __builtin_amdgcn_s_setprio(1);
      LANE_VARS;
      const int idx = 2047 - (int)(key & 2047u), win = idx >> 7;
      const bool owner = win == tm;
      auto pick2 = [&](const f32x4 (&d)[8]) {
        u32x2 o;
#pragma unroll
        for (int e = 0; e < 2; ++e) {
          const float x = tblk ? d[2 + e][0] : d[e][0], y = tblk ? d[6 + e][0] : d[4 + e][0];
          o[e] = nzf(hi ? y : x);
        }
        return o;
      };
      if (ffn) {
        const unsigned ppos = (lane & 15) * 32 + (lane >> 4) * 8;
        __builtin_amdgcn_raw_buffer_store_b64(pick2(d), rs, sx + SX_GP + (grp * 8 + qa) * 512 + ppos, 0, 16);
        STAMPV(9 + 16 * L);
        __builtin_amdgcn_s_sleep(16);
        u32x2 gu[8];
        for (unsigned spins = 0;; ++spins) {
#pragma unroll
          for (int i = 0; i < 8; ++i) gu[i] = __builtin_amdgcn_raw_buffer_load_b64(rs, sx + SX_GP + (grp * 8 + i) * 512 + lane * 8, 0, 16);
          SBAR();
          unsigned m = 0u;
#pragma unroll
          for (int i = 0; i < 8; ++i) m = max(max(m, gu[i][0]), gu[i][1]);
          if (__all(m != SENT) || spins > SPIN_CAP) break;
          poll_pause(L == 0);
        }
        STAMPV(10 + 16 * L);
        float t0 = opq(U2F(gu[0][0])), t1 = opq(U2F(gu[0][1]));
#pragma unroll
        for (int i = 1; i < 8; ++i) { t0 = fadd(t0, U2F(gu[i][0])); t1 = fadd(t1, U2F(gu[i][1])); }
        auto sw = __builtin_amdgcn_permlane32_swap(__float_as_uint(t0), __float_as_uint(t1), false, false);
        const float gv = rbf(U2F(sw[0])), uv = rbf(U2F(sw[1]));
        reinterpret_cast<u16*>(smem + L_GU + a * 128)[2 * r32 + hi] = (u16)pack2(fmul(rbf(fmul(gv, RCP(fadd(1.0f, __expf(-gv))))), uv), 0.f);
        asm volatile("s_waitcnt lgkmcnt(0)" ::: "memory");
        const s16x8 a0 = *reinterpret_cast<const s16x8*>(smem + L_GU + a * 128 + (lane >> 4) * 16), a1 = *reinterpret_cast<const s16x8*>(smem + L_GU + a * 128 + 64 + (lane >> 4) * 16);
        SBAR();
        __builtin_amdgcn_s_setprio(3);
#pragma unroll
        for (int t = 0; t < 16; ++t) d[t & 7] = MFMA16(t < 8 ? a0 : a1, __builtin_bit_cast(s16x8, wdf[t]), t < 8 ? zero : d[t & 7]);
        SBAR();
        __builtin_amdgcn_s_setprio(1);
        __builtin_amdgcn_raw_buffer_store_b64(pick2(d), rs, sx + SX_YP + grp * 4096 + qa * 512 + ppos, 0, 16);
      }
      STAMPV(3 + 16 * L);
      if (L == 1 && (cg != (kr & 7) || a)) { STAMP_END; __builtin_amdgcn_endpgm(); }
      s16x8 wpf[12];
#pragma unroll
      for (int t = 0; t < 12; ++t) wpf[t] = tr2(smem + L_SL + a * 12288 + (16 * hi + 4 * tblk + tq) * 384 + t * 32 + tp * 8, 8 * 384);
#pragma unroll
      for (int t = 0; t < 12; ++t) PIN(wpf[t]);
      if (L == 0 && !owner) lds_set(wa + 4 + 4 * a, 1);
      __builtin_amdgcn_s_sleep(9);
      if (L == 0 && !ffn) __builtin_amdgcn_s_sleep(64);
      const unsigned rk = key >> 16;
      float rsig = rbf(RCP(fadd(1.0f, __expf(-lo16((rk & 0x8000u) ? (rk & 0x7fffu) : (~rk & 0xffffu))))));
      PIN(rsig);
      const int n = 4 * (lane & 7) + 2 * tblk + hi;
      const int fo = ((n >> 4) * 2 + ((n >> 2) & 1)) * 8 + ((n >> 3) & 1) * 4 + (n & 3);
      unsigned odst = (L == 1 && !(lane & 8)) ? (idx * D_ + kr * 32 + n) * 2 : OOB;
      PIN(odst);
      u32x4 pv[8];
      poll_all<8>(pv, L == 0, [&](u32x4 (&t)[8]) {
#pragma unroll
        for (int i = 0; i < 7; ++i) t[i] = ldg(rs, sx + SX_YP + (8 * i + (lane >> 3)) * 4096 + kr * 128 + (lane & 7) * 16);
        t[7][0] = t[7][2] = __builtin_amdgcn_raw_buffer_load_b32(rs, sx + SX_ROW + (idx >> 5) * 2048 + kr * 64 + (fo >> 1) * 4, 0, 16);
        t[7][1] = t[7][3] = __builtin_amdgcn_raw_buffer_load_b32(rs, sx + SX_ROW + 131072 + (idx >> 5) * 2048 + kr * 64 + (fo >> 1) * 4, 0, 16);
      }, [&](const u32x4 (&t)[8]) { return none_sent(t); });
      const float hv = (fo & 1) ? hi16(pv[7][0]) : lo16(pv[7][0]), xv = (fo & 1) ? hi16(pv[7][1]) : lo16(pv[7][1]);
      STAMPV(4 + 16 * L);
      float ys;
      {
        const f32x4 t = sum4<7>(pv);
        auto p = __builtin_amdgcn_permlane32_swap(__float_as_uint(t[0]), __float_as_uint(t[1]), false, false);
        auto q = __builtin_amdgcn_permlane32_swap(__float_as_uint(t[2]), __float_as_uint(t[3]), false, false);
        auto r = __builtin_amdgcn_permlane16_swap(__float_as_uint(fadd(U2F(p[0]), U2F(p[1]))), __float_as_uint(fadd(U2F(q[0]), U2F(q[1]))), false, false);
        ys = fadd(U2F(r[0]), U2F(r[1]));
        ys = fadd(ys, U2F(dpp<0x128>(__float_as_uint(ys))));
      }
      const unsigned ob = pack2(fadd(xv, rbf(fadd(hv, rbf(fmul(rbf(ys), rsig))))), 0.f) & 0xffffu;
      __builtin_amdgcn_raw_buffer_store_b16((u16)ob, __builtin_amdgcn_make_buffer_rsrc(out, 0, 2048 * D_ * 2, 0x00020000), odst, 0, 0);
      if (L == 1) {
        STAMPV(5 + 16 * L);
        STAMP_END;
        __builtin_amdgcn_endpgm();
      }
      reinterpret_cast<u16*>(smem + L_X2F + a * 64)[fo] = (u16)ob;
      asm volatile("s_waitcnt lgkmcnt(0)" ::: "memory");
      {
        const s16x8 xa = *reinterpret_cast<const s16x8*>(smem + L_X2F + a * 64 + (lane >> 4) * 16);
        const u32x4 xf = *reinterpret_cast<const u32x4*>(smem + L_X2F + a * 64 + tp * 16);
        f32x4 z[12];
        SBAR();
        __builtin_amdgcn_s_setprio(3);
#pragma unroll
        for (int t = 0; t < 12; ++t) z[t] = MFMA16(xa, wpf[t], zero);
        SBAR();
        __builtin_amdgcn_s_setprio(1);
        const float x2 = lo16(ob), s1 = fmul(wave_sum(x2), 0.5f), s2 = fmul(wave_sum(fmul(x2, x2)), 0.5f);
        u32x4 o;
#pragma unroll
        for (int e = 0; e < 4; ++e) {
          const unsigned sp = lane < 52 ? (e == 0 ? __float_as_uint(s1) : __float_as_uint(s2)) : xf[e];
          const unsigned x = __float_as_uint(tblk ? z[4 + e][0] : z[e][0]), y = tblk ? sp : __float_as_uint(z[8 + e][0]);
          o[e] = nz(hi ? y : x);
        }
        const unsigned pos = lane < 24 ? lane * 16 : lane < 48 ? lane * 16 + 128 : lane < 52 ? (lane & 1) * 512 + 384 : 400 + tp * 16;
        stg(rs, lane < 56 ? OFF_SX + SX_QP + ((2 * cg + a) * 32 + kr) * 1024 + pos : OOB, o);
      }
      STAMPV(5 + 16 * L);
      if (owner) {
        __builtin_amdgcn_s_sleep(13);
        const int c25 = r32 < 25 ? r32 : 24, pl = 24 * a + c24, col = 64 * (pl >> 4) + (pl & 15);
        const int pfo = (col & ~15) + ((col >> 2) & 1) * 16 + ((col >> 3) & 1) * 8 + (col & 3);
        float cs[4];
#pragma unroll
        for (int e = 0; e < 4; ++e) { cs[e] = reinterpret_cast<const float*>(smem + L_CS)[col + 16 * e]; PIN(cs[e]); }
        u32x4 qv[17];
        poll_all<17>(qv, true, [&](u32x4 (&t)[17]) {
#pragma unroll
          for (int i = 0; i < 16; ++i) t[i] = ldg(rs, OFF_SX + SX_QP + (tn * 32 + 2 * i + hi) * 1024 + a * 512 + c25 * 16);
          t[16] = ldg(rs, OFF_SX + SX_QP + (tn * 34 + hi) * 1024 + 400 + tp * 16);
        }, [&](const u32x4 (&t)[17]) { return none_sent(t); });
        *reinterpret_cast<u32x4*>(smem + L_X2P + hi * 64 + tp * 16) = qv[16];
        f32x4 t = sum4<16>(qv);
#pragma unroll
        for (int e = 0; e < 4; ++e) t[e] = half_swap_sum(t[e]);
        const float mean = fmul(U2F(__builtin_amdgcn_readlane(__float_as_uint(t[0]), 24)), 1.0f / 1024.0f);
        const float var = fmaxf(ffma(-mean, mean, fmul(U2F(__builtin_amdgcn_readlane(__float_as_uint(t[1]), 24)), 1.0f / 1024.0f)), 0.f);
        float mb, den;
        ln_scalars(mean, var, mb, den);
        const float rd = RCP(den);
        if (lane < 24) {
#pragma unroll
          for (int e = 0; e < 4; ++e) reinterpret_cast<u16*>(smem + L_PATCH)[pfo + 32 * (e >> 1) + 4 * (e & 1)] = (u16)pack2(fmul(ffma(-mb, cs[e], t[e]), pl < 16 ? fmul(rd, QS) : rd), 0.f);
        }
        lds_set(wa + 4 + 4 * a, L + 1);
      }
      STAMPV(6 + 16 * L);
      BAR_ALL();
      unsigned dv = fresh_lane();
      dv = (dv >> 4) * (8 * D_ * 2) + (dv & 15) * 16;
      PIN(dv);
      wait_go(1);
      load_wd(dv);
      STAMPV(23);
      __builtin_amdgcn_s_setprio(0);
      lds_wait_ge<4>(wa + 80 + 4 * (fresh_lane() & 3), 4);
      transpose8(wdf); transpose8(wdf + 8);
      BARRIER();
    }
  }
  const int tm_ = tm, tn_ = tn, wid_ = wid;
#define KSKIP "{s49}"(tn * 4096), "{s50}"(((tn + 1) & 15) * 4096)
  const u32x4 srd = {(unsigned)(uintptr_t)ws, (unsigned)((uintptr_t)ws >> 32) & 0xffffu, WS_BYTES, 0x00020000u};
  u32x4 x1p[4];
  asm volatile("" : "=v"(x1p[0]), "=v"(x1p[1]), "=v"(x1p[2]), "=v"(x1p[3]));
#pragma unroll 1
  for (int L = 0; L < 2; ++L) {
    const unsigned sx = OFF_SX + L * SX_LAYER;
    u32x8 q[4];
    u32x16 kva, kvb;
    f32x16 oz[5];
    {
      f32x16 acc[6];
      u32x16 stA, stB, xr1, xr2, xr3;
      u32x8 xra, xrb;
      u32x4 stC;
      asm volatile("" : "={v[112:127]}"(stB), "={v[128:131]}"(stC));
      { PHASE_VARS;
      stB[8] = (L ? OFF_X1 : OFF_HF) + tile * TS + tblk * 1024 + (hi * 32 + (lane & 15)) * 16;
      stB[9] = lbase + tblk * HALFB + wrow * 384 + (((4 * hh + (tp >> 1)) ^ wsw) << 4) + 8 * (tp & 1);
      stB[10] = lbase + tblk * HALFB + wrow * 384 + (((4 * (hh ^ 1) + (tp >> 1)) ^ wsw) << 4) + 8 * (tp & 1);
      stB[13] = lbase + L_FULL + (lane & 1) * 256;
      stB[15] = lbase + L_FREE + wid * 256 + lane * 4;
      stC[0] = 32 * L + 2; }
      unsigned soff, slot;
      if (L == 0) {
        lds_wait_ge<0>(lbase + L_W + 24, 1);
        STAMPV(39);
        soff = tn * 4096;
        asm volatile(
          "v_add_u32 v131, s44, v120\n\t"
          "buffer_load_dwordx4 v[132:135], v131, s[40:43], 0 offen offset:0 sc1\n\t"
          "buffer_load_dwordx4 v[136:139], v131, s[40:43], 0 offen offset:256 sc1\n\t"
          "buffer_load_dwordx4 v[140:143], v131, s[40:43], 0 offen offset:2048 sc1\n\t"
          "buffer_load_dwordx4 v[144:147], v131, s[40:43], 0 offen offset:2304 sc1\n\t"
          "s_mov_b32 s44, s51\n\t"
          "v_add_u32 v131, s44, v120\n\t"
          "buffer_load_dwordx4 v[148:151], v131, s[40:43], 0 offen offset:0 sc1\n\t"
          "buffer_load_dwordx4 v[152:155], v131, s[40:43], 0 offen offset:256 sc1\n\t"
          "buffer_load_dwordx4 v[156:159], v131, s[40:43], 0 offen offset:2048 sc1\n\t"
          "buffer_load_dwordx4 v[160:163], v131, s[40:43], 0 offen offset:2304 sc1\n\t"
          "s_addk_i32 s44, 0x1000\n\t"
          "s_bitset0_b32 s44, 16\n\t"
          "s_cmp_eq_u32 s44, s49\n\t"
          "s_cselect_b32 s44, s50, s44\n\t"
          "v_add_u32 v131, s44, v120\n\t"
          "buffer_load_dwordx4 v[164:167], v131, s[40:43], 0 offen offset:0 sc1\n\t"
          "buffer_load_dwordx4 v[168:171], v131, s[40:43], 0 offen offset:256 sc1\n\t"
          "buffer_load_dwordx4 v[172:175], v131, s[40:43], 0 offen offset:2048 sc1\n\t"
          "buffer_load_dwordx4 v[176:179], v131, s[40:43], 0 offen offset:2304 sc1\n\t"
          "s_addk_i32 s44, 0x1000\n\t"
          "s_bitset0_b32 s44, 16\n\t"
          "s_cmp_eq_u32 s44, s49\n\t"
          "s_cselect_b32 s44, s50, s44\n\t"
          "v_add_u32 v131, s44, v120\n\t"
          "buffer_load_dwordx4 v[180:183], v131, s[40:43], 0 offen offset:0 sc1\n\t"
          "buffer_load_dwordx4 v[184:187], v131, s[40:43], 0 offen offset:256 sc1\n\t"
          "buffer_load_dwordx4 v[188:191], v131, s[40:43], 0 offen offset:2048 sc1\n\t"
          "buffer_load_dwordx4 v[192:195], v131, s[40:43], 0 offen offset:2304 sc1\n\t"
          : "={v[132:139]}"(xra), "={v[140:147]}"(xrb), "={v[148:163]}"(xr1), "={v[164:179]}"(xr2), "={v[180:195]}"(xr3), "+{s44}"(soff), "+{v[128:131]}"(stC) : "{v[112:127]}"(stB), "{s[40:43]}"(srd), KSKIP, "{s51}"(kpos(tm, tn, 1) * 4096) : "scc", "memory");
      } else {
        u32x4 t[4] = {x1p[0], x1p[1], x1p[2], x1p[3]};
        row_swap(t[0], t[1]); row_swap(t[2], t[3]);
        xra = __builtin_shufflevector(t[0], t[1], 0, 1, 2, 3, 4, 5, 6, 7); xrb = __builtin_shufflevector(t[2], t[3], 0, 1, 2, 3, 4, 5, 6, 7);
        asm volatile("" : "={v[148:163]}"(xr1), "={v[164:179]}"(xr2), "={v[180:195]}"(xr3));
      }
      STAMPV(1 + 16 * L);
      asm volatile(
        "v_mov_b32 v123, v121\n\t"
        "v_mov_b32 v124, v122\n\t"
        "s_mov_b64 vcc, -1\n\t"
        "s_cbranch_vccnz 10f\n\t"
        "11:\n\t"
        "s_movk_i32 s46, 12352\n\t"
        "s_mov_b32 s47, 111168\n\t"
        "ds_read_b64_tr_b16 v[96:97], v123 offset:256\n\t"
        "ds_read_b64_tr_b16 v[98:99], v123 offset:3328\n\t"
        "ds_read_b64_tr_b16 v[100:101], v123 offset:288\n\t"
        "ds_read_b64_tr_b16 v[102:103], v123 offset:3360\n\t"
        "ds_read_b64_tr_b16 v[104:105], v124 offset:256\n\t"
        "ds_read_b64_tr_b16 v[106:107], v124 offset:3328\n\t"
        "ds_write_b32 v127, v128\n\t"
        "ds_read_b64_tr_b16 v[108:109], v124 offset:288\n\t"
        "ds_read_b64_tr_b16 v[110:111], v124 offset:3360\n\t"
        "ds_read_b64_tr_b16 v[112:113], v123 offset:128\n\t"
        "ds_read_b64_tr_b16 v[114:115], v123 offset:3200\n\t"
        "v_add_u32 v128, 2, v128\n\t"
        "s_waitcnt vmcnt(14) lgkmcnt(7)\n\t"
        "v_mfma_f32_16x16x32_bf16 v[64:67], v[132:135], v[96:99], 0\n\t"
        "ds_read_b64_tr_b16 v[116:117], v123 offset:160\n\t"
        "v_mfma_f32_16x16x32_bf16 v[72:75], v[136:139], v[96:99], 0\n\t"
        "ds_read_b64_tr_b16 v[118:119], v123 offset:3232\n\t"
        "v_mfma_f32_16x16x32_bf16 v[68:71], v[132:135], v[100:103], 0\n\t"
        "ds_read_b64_tr_b16 v[96:97], v124 offset:128\n\t"
        "v_mfma_f32_16x16x32_bf16 v[76:79], v[136:139], v[100:103], 0\n\t"
        "ds_read_b64_tr_b16 v[98:99], v124 offset:3200\n\t"
        "s_waitcnt lgkmcnt(6)\n\t"
        "v_mfma_f32_16x16x32_bf16 v[80:83], v[132:135], v[104:107], 0\n\t"
        "ds_read_b64_tr_b16 v[100:101], v124 offset:160\n\t"
        "v_mfma_f32_16x16x32_bf16 v[88:91], v[136:139], v[104:107], 0\n\t"
        "ds_read_b64_tr_b16 v[102:103], v124 offset:3232\n\t"
        "v_mfma_f32_16x16x32_bf16 v[84:87], v[132:135], v[108:111], 0\n\t"
        "ds_read_b64_tr_b16 v[104:105], v123 offset:0\n\t"
        "v_add_u32 v129, s46, v121\n\t"
        "v_mfma_f32_16x16x32_bf16 v[92:95], v[136:139], v[108:111], 0\n\t"
        "ds_read_b64_tr_b16 v[106:107], v123 offset:3072\n\t"
        "s_waitcnt lgkmcnt(6)\n\t"
        "v_mfma_f32_16x16x32_bf16 v[32:35], v[112:115], v[132:135], 0\n\t"
        "ds_read_b64_tr_b16 v[108:109], v123 offset:32\n\t"
        "v_mfma_f32_16x16x32_bf16 v[40:43], v[112:115], v[136:139], 0\n\t"
        "ds_read_b64_tr_b16 v[110:111], v123 offset:3104\n\t"
        "v_add_u32 v130, s46, v122\n\t"
        "v_mfma_f32_16x16x32_bf16 v[36:39], v[116:119], v[132:135], 0\n\t"
        "ds_read_b64_tr_b16 v[112:113], v124 offset:0\n\t"
        "v_mfma_f32_16x16x32_bf16 v[44:47], v[116:119], v[136:139], 0\n\t"
        "ds_read_b64_tr_b16 v[114:115], v124 offset:3072\n\t"
        "s_waitcnt lgkmcnt(6)\n\t"
        "v_mfma_f32_16x16x32_bf16 v[48:51], v[96:99], v[132:135], 0\n\t"
        "ds_read_b64_tr_b16 v[116:117], v124 offset:32\n\t"
        "v_mfma_f32_16x16x32_bf16 v[56:59], v[96:99], v[136:139], 0\n\t"
        "ds_read_b64_tr_b16 v[118:119], v124 offset:3104\n\t"
        "v_mfma_f32_16x16x32_bf16 v[52:55], v[100:103], v[132:135], 0\n\t"
        "ds_read_b64_tr_b16 v[96:97], v129 offset:256\n\t"
        "s_addk_i32 s46, 12352\n\t"
        "v_mfma_f32_16x16x32_bf16 v[60:63], v[100:103], v[136:139], 0\n\t"
        "ds_read_b64_tr_b16 v[98:99], v129 offset:3328\n\t"
        "s_waitcnt lgkmcnt(6)\n\t"
        "v_mfma_f32_16x16x32_bf16 v[0:3], v[104:107], v[132:135], 0\n\t"
        "ds_read_b64_tr_b16 v[100:101], v129 offset:288\n\t"
        "s_cmp_eq_u32 s46, s47\n\t"
        "v_mfma_f32_16x16x32_bf16 v[8:11], v[104:107], v[136:139], 0\n\t"
        "ds_read_b64_tr_b16 v[102:103], v129 offset:3360\n\t"
        "s_cselect_b32 s46, 0, s46\n\t"
        "v_mfma_f32_16x16x32_bf16 v[4:7], v[108:111], v[132:135], 0\n\t"
        "ds_read_b64_tr_b16 v[104:105], v130 offset:256\n\t"
        "v_mfma_f32_16x16x32_bf16 v[12:15], v[108:111], v[136:139], 0\n\t"
        "ds_read_b64_tr_b16 v[106:107], v130 offset:3328\n\t"
        "s_waitcnt lgkmcnt(6)\n\t"
        "v_mfma_f32_16x16x32_bf16 v[16:19], v[112:115], v[132:135], 0\n\t"
        "ds_read_b64_tr_b16 v[108:109], v130 offset:288\n\t"
        "v_mfma_f32_16x16x32_bf16 v[24:27], v[112:115], v[136:139], 0\n\t"
        "ds_read_b64_tr_b16 v[110:111], v130 offset:3360\n\t"
        "v_mfma_f32_16x16x32_bf16 v[20:23], v[116:119], v[132:135], 0\n\t"
        "ds_read_b64_tr_b16 v[112:113], v129 offset:128\n\t"
        "v_mfma_f32_16x16x32_bf16 v[28:31], v[116:119], v[136:139], 0\n\t"
        "ds_read_b64_tr_b16 v[114:115], v129 offset:3200\n\t"
        "s_waitcnt lgkmcnt(0)\n\t"
        "s_nop 7\n\t"
        "s_branch 9f\n\t"
        "10:\n\t"
        "s_mov_b32 s48, 0x10000\n\t"
        "8:\n\t"
        "ds_read_b32 v126, v125\n\t"
        "s_sub_u32 s48, s48, 1\n\t"
        "s_waitcnt lgkmcnt(0)\n\t"
        "s_cbranch_scc1 11b\n\t"
        "v_cmp_gt_u32 vcc, v128, v126\n\t"
        "s_cbranch_vccnz 8b\n\t"
        "s_branch 11b\n\t"
        "9:\n\t"
        : "={v[0:15]}"(acc[0]), "={v[16:31]}"(acc[1]), "={v[32:47]}"(acc[2]), "={v[48:63]}"(acc[3]), "={v[64:79]}"(acc[4]), "={v[80:95]}"(acc[5]),
          "={v[96:111]}"(stA), "+{v[112:127]}"(stB), "+{v[128:131]}"(stC), "={s46}"(slot), "+{v[140:147]}"(xrb)
        : "{v[132:139]}"(xra) : "s47", "s48", "vcc", "scc", "memory");
      STAMPV(2 + 16 * L);
      if (L == 1) {
        PHASE_VARS;
        float S, Q, Dd;
        __builtin_amdgcn_s_sleep(0);
        row_stats_sum(rs, OFF_SX, tile, lane, S, Q, Dd);
        STAMPV(36);
        auto request_x = [&]() {
          lds_wait_ge<0>(lbase + L_W + 24, 2);
          soff = kpos(tm, tn, 1) * 4096;
          asm volatile(
          "v_add_u32 v131, s44, v120\n\t"
          "buffer_load_dwordx4 v[148:151], v131, s[40:43], 0 offen offset:0 sc1\n\t"
          "buffer_load_dwordx4 v[152:155], v131, s[40:43], 0 offen offset:256 sc1\n\t"
          "buffer_load_dwordx4 v[156:159], v131, s[40:43], 0 offen offset:2048 sc1\n\t"
          "buffer_load_dwordx4 v[160:163], v131, s[40:43], 0 offen offset:2304 sc1\n\t"
          "s_addk_i32 s44, 0x1000\n\t"
          "s_bitset0_b32 s44, 16\n\t"
          "s_cmp_eq_u32 s44, s49\n\t"
          "s_cselect_b32 s44, s50, s44\n\t"
          "v_add_u32 v131, s44, v120\n\t"
          "buffer_load_dwordx4 v[164:167], v131, s[40:43], 0 offen offset:0 sc1\n\t"
          "buffer_load_dwordx4 v[168:171], v131, s[40:43], 0 offen offset:256 sc1\n\t"
          "buffer_load_dwordx4 v[172:175], v131, s[40:43], 0 offen offset:2048 sc1\n\t"
          "buffer_load_dwordx4 v[176:179], v131, s[40:43], 0 offen offset:2304 sc1\n\t"
          "s_addk_i32 s44, 0x1000\n\t"
          "s_bitset0_b32 s44, 16\n\t"
          "s_cmp_eq_u32 s44, s49\n\t"
          "s_cselect_b32 s44, s50, s44\n\t"
          "v_add_u32 v131, s44, v120\n\t"
          "buffer_load_dwordx4 v[180:183], v131, s[40:43], 0 offen offset:0 sc1\n\t"
          "buffer_load_dwordx4 v[184:187], v131, s[40:43], 0 offen offset:256 sc1\n\t"
          "buffer_load_dwordx4 v[188:191], v131, s[40:43], 0 offen offset:2048 sc1\n\t"
          "buffer_load_dwordx4 v[192:195], v131, s[40:43], 0 offen offset:2304 sc1\n\t"
            : "={v[148:163]}"(xr1), "={v[164:179]}"(xr2), "={v[180:195]}"(xr3), "+{s44}"(soff), "+{v[128:131]}"(stC) : "{v[112:127]}"(stB), "{s[40:43]}"(srd), KSKIP : "scc", "memory");
          SBAR();
        };
        float mean, var, mb1, rd1, mb2, den2;
        row_stats_fin(rs, OFF_SX + SX_CAND + (tn * 16 + tm) * 128 + wid * 4, smem, 1, tile, lane, S, Q, Dd, U2F(words[0]), x1p, mean, var, mb1, rd1);
        ln_scalars(mean + (mean - mb1) * rd1, var * (1.0f + rd1) * (1.0f + rd1), mb2, den2);
        const float rd2 = RCP(den2);
        const float al = (1.0f + rd1) * rd2;
        const float be = -(mb1 * rd1 + mb2) * RCP(1.0f + rd1);
        reinterpret_cast<float*>(smem + L_AB)[wid * 64 + lane] = hi ? be : al;
        reinterpret_cast<float*>(smem + L_AB)[256 + wid * 64 + lane] = hi ? 1.0f + rd1 : -mb1 * rd1;
        const float* ab = reinterpret_cast<const float*>(smem + L_AB) + wid * 64 + 32;
        const float bt[2] = {ab[lane & 15], ab[16 + (lane & 15)]};
#pragma unroll
        for (int jj = 0; jj < 2; ++jj) {
          if (jj == 0) request_x();
          f32x4 cs[2][2], bv[2];
          float cv[2];
#pragma unroll
          for (int i = 0; i < 2; ++i) {
            const float* p = reinterpret_cast<const float*>(smem + L_CS) + (jj ^ hh) * 32 + 16 * i;
            bv[i] = *reinterpret_cast<const f32x4*>(ab + 16 * i + 4 * (lane >> 4));
            cs[0][i] = *reinterpret_cast<const f32x4*>(p + 4 * (lane >> 4)); cs[1][i] = *reinterpret_cast<const f32x4*>(p + 64 + 4 * (lane >> 4));
            cv[i] = p[128 + (lane & 15)];
          }
          SBAR();
#pragma unroll
          for (int j = 0; j < 3; ++j)
#pragma unroll
            for (int e = 0; e < 16; ++e) acc[2 * j + jj][e] += j < 2 ? bt[e >> 3] * cs[j & 1][(e >> 2) & 1][e & 3] : bv[e >> 3][e & 3] * cv[(e >> 2) & 1];
          SBAR();
        }
        if (0 == 2) request_x();
      }
      unsigned ep[4];
      f32x2 al2;
      { PHASE_VARS;
        float* ab = reinterpret_cast<float*>(smem + L_AB) + wid * 64;
        if (L == 0) ab[lane] = 1.0f;
        al2 = f32x2{ab[lane & 15], ab[16 + (lane & 15)]};
        const unsigned dst = OFF_KV + L * (10 * MBY) + tn * 2 * HS + tile * 4096 + lane * 16;
        ep[0] = dst + hh * HS; ep[1] = dst + (hh ^ 1) * HS; ep[2] = lbase + L_AB + wid * 256 + (lane >> 4) * 16; ep[3] = lbase + L_KX + wid * 4096 + lane * 16; }
      STAMPV(3 + 16 * L);
      soff = kpos(tm, tn, 1 + 2) * 4096;
      asm volatile(
        "s_mov_b32 s47, 111168\n\t"
        "v_add_u32 v131, s44, v120\n\t"
        "s_waitcnt vmcnt(12) lgkmcnt(6)\n\t"
        "v_mfma_f32_16x16x32_bf16 v[64:67], v[140:143], v[96:99], v[64:67]\n\t"
        "ds_read_b64_tr_b16 v[116:117], v129 offset:160\n\t"
        "v_mfma_f32_16x16x32_bf16 v[72:75], v[144:147], v[96:99], v[72:75]\n\t"
        "ds_read_b64_tr_b16 v[118:119], v129 offset:3232\n\t"
        "v_mfma_f32_16x16x32_bf16 v[68:71], v[140:143], v[100:103], v[68:71]\n\t"
        "ds_read_b64_tr_b16 v[96:97], v130 offset:128\n\t"
        "ds_read_b32 v126, v125\n\t"
        "v_mfma_f32_16x16x32_bf16 v[76:79], v[144:147], v[100:103], v[76:79]\n\t"
        "ds_read_b64_tr_b16 v[98:99], v130 offset:3200\n\t"
        "s_waitcnt lgkmcnt(7)\n\t"
        "v_mfma_f32_16x16x32_bf16 v[80:83], v[140:143], v[104:107], v[80:83]\n\t"
        "ds_read_b64_tr_b16 v[100:101], v130 offset:160\n\t"
        "v_mfma_f32_16x16x32_bf16 v[88:91], v[144:147], v[104:107], v[88:91]\n\t"
        "ds_read_b64_tr_b16 v[102:103], v130 offset:3232\n\t"
        "v_mfma_f32_16x16x32_bf16 v[84:87], v[140:143], v[108:111], v[84:87]\n\t"
        "ds_read_b64_tr_b16 v[104:105], v129 offset:0\n\t"
        "v_add_u32 v123, s46, v121\n\t"
        "v_mfma_f32_16x16x32_bf16 v[92:95], v[144:147], v[108:111], v[92:95]\n\t"
        "ds_read_b64_tr_b16 v[106:107], v129 offset:3072\n\t"
        "s_waitcnt lgkmcnt(5)\n\t"
        "v_mfma_f32_16x16x32_bf16 v[32:35], v[112:115], v[140:143], v[32:35]\n\t"
        "ds_read_b64_tr_b16 v[108:109], v129 offset:32\n\t"
        "v_cmp_gt_u32 vcc, v128, v126\n\t"
        "v_mfma_f32_16x16x32_bf16 v[40:43], v[112:115], v[144:147], v[40:43]\n\t"
        "ds_read_b64_tr_b16 v[110:111], v129 offset:3104\n\t"
        "v_add_u32 v124, s46, v122\n\t"
        "v_mfma_f32_16x16x32_bf16 v[36:39], v[116:119], v[140:143], v[36:39]\n\t"
        "ds_read_b64_tr_b16 v[112:113], v130 offset:0\n\t"
        "v_mfma_f32_16x16x32_bf16 v[44:47], v[116:119], v[144:147], v[44:47]\n\t"
        "ds_read_b64_tr_b16 v[114:115], v130 offset:3072\n\t"
        "s_waitcnt lgkmcnt(6)\n\t"
        "v_mfma_f32_16x16x32_bf16 v[48:51], v[96:99], v[140:143], v[48:51]\n\t"
        "ds_read_b64_tr_b16 v[116:117], v130 offset:32\n\t"
        "v_mfma_f32_16x16x32_bf16 v[56:59], v[96:99], v[144:147], v[56:59]\n\t"
        "ds_read_b64_tr_b16 v[118:119], v130 offset:3104\n\t"
        "s_cbranch_vccnz 12f\n\t"
        "13:\n\t"
        "v_mfma_f32_16x16x32_bf16 v[52:55], v[100:103], v[140:143], v[52:55]\n\t"
        "ds_read_b64_tr_b16 v[96:97], v123 offset:256\n\t"
        "s_addk_i32 s46, 12352\n\t"
        "v_mfma_f32_16x16x32_bf16 v[60:63], v[100:103], v[144:147], v[60:63]\n\t"
        "ds_read_b64_tr_b16 v[98:99], v123 offset:3328\n\t"
        "s_waitcnt lgkmcnt(6)\n\t"
        "v_mfma_f32_16x16x32_bf16 v[0:3], v[104:107], v[140:143], v[0:3]\n\t"
        "ds_read_b64_tr_b16 v[100:101], v123 offset:288\n\t"
        "s_cmp_eq_u32 s46, s47\n\t"
        "v_mfma_f32_16x16x32_bf16 v[8:11], v[104:107], v[144:147], v[8:11]\n\t"
        "ds_read_b64_tr_b16 v[102:103], v123 offset:3360\n\t"
        "s_cselect_b32 s46, 0, s46\n\t"
        "v_mfma_f32_16x16x32_bf16 v[4:7], v[108:111], v[140:143], v[4:7]\n\t"
        "ds_read_b64_tr_b16 v[104:105], v124 offset:256\n\t"
        "v_mfma_f32_16x16x32_bf16 v[12:15], v[108:111], v[144:147], v[12:15]\n\t"
        "ds_read_b64_tr_b16 v[106:107], v124 offset:3328\n\t"
        "s_waitcnt lgkmcnt(6)\n\t"
        "v_mfma_f32_16x16x32_bf16 v[16:19], v[112:115], v[140:143], v[16:19]\n\t"
        "ds_write_b32 v127, v128\n\t"
        "ds_read_b64_tr_b16 v[108:109], v124 offset:288\n\t"
        "v_mfma_f32_16x16x32_bf16 v[24:27], v[112:115], v[144:147], v[24:27]\n\t"
        "ds_read_b64_tr_b16 v[110:111], v124 offset:3360\n\t"
        "v_add_u32 v128, 2, v128\n\t"
        "v_mfma_f32_16x16x32_bf16 v[20:23], v[116:119], v[140:143], v[20:23]\n\t"
        "ds_read_b64_tr_b16 v[112:113], v123 offset:128\n\t"
        "v_mfma_f32_16x16x32_bf16 v[28:31], v[116:119], v[144:147], v[28:31]\n\t"
        "ds_read_b64_tr_b16 v[114:115], v123 offset:3200\n\t"
        "s_mov_b32 s45, 2\n\t"
        "1:\n\t"
        "s_waitcnt vmcnt(10) lgkmcnt(7)\n\t"
        "v_mfma_f32_16x16x32_bf16 v[64:67], v[148:151], v[96:99], v[64:67]\n\t"
        "ds_read_b64_tr_b16 v[116:117], v123 offset:160\n\t"
        "s_addk_i32 s44, 0x1000\n\t"
        "v_mfma_f32_16x16x32_bf16 v[72:75], v[152:155], v[96:99], v[72:75]\n\t"
        "ds_read_b64_tr_b16 v[118:119], v123 offset:3232\n\t"
        "s_bitset0_b32 s44, 16\n\t"
        "v_mfma_f32_16x16x32_bf16 v[68:71], v[148:151], v[100:103], v[68:71]\n\t"
        "ds_read_b64_tr_b16 v[96:97], v124 offset:128\n\t"
        "s_cmp_eq_u32 s44, s49\n\t"
        "v_mfma_f32_16x16x32_bf16 v[76:79], v[152:155], v[100:103], v[76:79]\n\t"
        "ds_read_b64_tr_b16 v[98:99], v124 offset:3200\n\t"
        "s_waitcnt lgkmcnt(6)\n\t"
        "v_mfma_f32_16x16x32_bf16 v[80:83], v[148:151], v[104:107], v[80:83]\n\t"
        "ds_read_b64_tr_b16 v[100:101], v124 offset:160\n\t"
        "s_cselect_b32 s44, s50, s44\n\t"
        "v_mfma_f32_16x16x32_bf16 v[88:91], v[152:155], v[104:107], v[88:91]\n\t"
        "ds_read_b64_tr_b16 v[102:103], v124 offset:3232\n\t"
        "v_add_u32 v131, s44, v120\n\t"
        "v_mfma_f32_16x16x32_bf16 v[84:87], v[148:151], v[108:111], v[84:87]\n\t"
        "ds_read_b64_tr_b16 v[104:105], v123 offset:0\n\t"
        "v_add_u32 v129, s46, v121\n\t"
        "v_mfma_f32_16x16x32_bf16 v[92:95], v[152:155], v[108:111], v[92:95]\n\t"
        "ds_read_b64_tr_b16 v[106:107], v123 offset:3072\n\t"
        "s_waitcnt lgkmcnt(6)\n\t"
        "v_mfma_f32_16x16x32_bf16 v[32:35], v[112:115], v[148:151], v[32:35]\n\t"
        "ds_read_b64_tr_b16 v[108:109], v123 offset:32\n\t"
        "v_mfma_f32_16x16x32_bf16 v[40:43], v[112:115], v[152:155], v[40:43]\n\t"
        "ds_read_b64_tr_b16 v[110:111], v123 offset:3104\n\t"
        "v_add_u32 v130, s46, v122\n\t"
        "v_mfma_f32_16x16x32_bf16 v[36:39], v[116:119], v[148:151], v[36:39]\n\t"
        "ds_read_b64_tr_b16 v[112:113], v124 offset:0\n\t"
        "buffer_load_dwordx4 v[132:135], v131, s[40:43], 0 offen offset:0 sc1\n\t"
        "v_mfma_f32_16x16x32_bf16 v[44:47], v[116:119], v[152:155], v[44:47]\n\t"
        "ds_read_b64_tr_b16 v[114:115], v124 offset:3072\n\t"
        "s_waitcnt lgkmcnt(6)\n\t"
        "v_mfma_f32_16x16x32_bf16 v[48:51], v[96:99], v[148:151], v[48:51]\n\t"
        "ds_read_b64_tr_b16 v[116:117], v124 offset:32\n\t"
        "v_mfma_f32_16x16x32_bf16 v[56:59], v[96:99], v[152:155], v[56:59]\n\t"
        "ds_read_b64_tr_b16 v[118:119], v124 offset:3104\n\t"
        "v_mfma_f32_16x16x32_bf16 v[52:55], v[100:103], v[148:151], v[52:55]\n\t"
        "ds_read_b64_tr_b16 v[96:97], v129 offset:256\n\t"
        "s_addk_i32 s46, 12352\n\t"
        "v_mfma_f32_16x16x32_bf16 v[60:63], v[100:103], v[152:155], v[60:63]\n\t"
        "ds_read_b64_tr_b16 v[98:99], v129 offset:3328\n\t"
        "s_waitcnt lgkmcnt(6)\n\t"
        "v_mfma_f32_16x16x32_bf16 v[0:3], v[104:107], v[148:151], v[0:3]\n\t"
        "ds_read_b64_tr_b16 v[100:101], v129 offset:288\n\t"
        "s_cmp_eq_u32 s46, s47\n\t"
        "v_mfma_f32_16x16x32_bf16 v[8:11], v[104:107], v[152:155], v[8:11]\n\t"
        "ds_read_b64_tr_b16 v[102:103], v129 offset:3360\n\t"
        "s_cselect_b32 s46, 0, s46\n\t"
        "v_mfma_f32_16x16x32_bf16 v[4:7], v[108:111], v[148:151], v[4:7]\n\t"
        "ds_read_b64_tr_b16 v[104:105], v130 offset:256\n\t"
        "buffer_load_dwordx4 v[136:139], v131, s[40:43], 0 offen offset:256 sc1\n\t"
        "v_mfma_f32_16x16x32_bf16 v[12:15], v[108:111], v[152:155], v[12:15]\n\t"
        "ds_read_b64_tr_b16 v[106:107], v130 offset:3328\n\t"
        "s_waitcnt lgkmcnt(6)\n\t"
        "v_mfma_f32_16x16x32_bf16 v[16:19], v[112:115], v[148:151], v[16:19]\n\t"
        "ds_read_b64_tr_b16 v[108:109], v130 offset:288\n\t"
        "v_mfma_f32_16x16x32_bf16 v[24:27], v[112:115], v[152:155], v[24:27]\n\t"
        "ds_read_b64_tr_b16 v[110:111], v130 offset:3360\n\t"
        "v_mfma_f32_16x16x32_bf16 v[20:23], v[116:119], v[148:151], v[20:23]\n\t"
        "ds_read_b64_tr_b16 v[112:113], v129 offset:128\n\t"
        "v_mfma_f32_16x16x32_bf16 v[28:31], v[116:119], v[152:155], v[28:31]\n\t"
        "ds_read_b64_tr_b16 v[114:115], v129 offset:3200\n\t"
        "s_waitcnt vmcnt(10) lgkmcnt(6)\n\t"
        "v_mfma_f32_16x16x32_bf16 v[64:67], v[156:159], v[96:99], v[64:67]\n\t"
        "ds_read_b64_tr_b16 v[116:117], v129 offset:160\n\t"
        "v_mfma_f32_16x16x32_bf16 v[72:75], v[160:163], v[96:99], v[72:75]\n\t"
        "ds_read_b64_tr_b16 v[118:119], v129 offset:3232\n\t"
        "v_mfma_f32_16x16x32_bf16 v[68:71], v[156:159], v[100:103], v[68:71]\n\t"
        "ds_read_b64_tr_b16 v[96:97], v130 offset:128\n\t"
        "ds_read_b32 v126, v125\n\t"
        "v_mfma_f32_16x16x32_bf16 v[76:79], v[160:163], v[100:103], v[76:79]\n\t"
        "ds_read_b64_tr_b16 v[98:99], v130 offset:3200\n\t"
        "s_waitcnt lgkmcnt(7)\n\t"
        "v_mfma_f32_16x16x32_bf16 v[80:83], v[156:159], v[104:107], v[80:83]\n\t"
        "ds_read_b64_tr_b16 v[100:101], v130 offset:160\n\t"
        "v_mfma_f32_16x16x32_bf16 v[88:91], v[160:163], v[104:107], v[88:91]\n\t"
        "ds_read_b64_tr_b16 v[102:103], v130 offset:3232\n\t"
        "v_mfma_f32_16x16x32_bf16 v[84:87], v[156:159], v[108:111], v[84:87]\n\t"
        "ds_read_b64_tr_b16 v[104:105], v129 offset:0\n\t"
        "v_add_u32 v123, s46, v121\n\t"
        "v_mfma_f32_16x16x32_bf16 v[92:95], v[160:163], v[108:111], v[92:95]\n\t"
        "ds_read_b64_tr_b16 v[106:107], v129 offset:3072\n\t"
        "s_waitcnt lgkmcnt(5)\n\t"
        "v_mfma_f32_16x16x32_bf16 v[32:35], v[112:115], v[156:159], v[32:35]\n\t"
        "ds_read_b64_tr_b16 v[108:109], v129 offset:32\n\t"
        "v_cmp_gt_u32 vcc, v128, v126\n\t"
        "v_mfma_f32_16x16x32_bf16 v[40:43], v[112:115], v[160:163], v[40:43]\n\t"
        "ds_read_b64_tr_b16 v[110:111], v129 offset:3104\n\t"
        "v_add_u32 v124, s46, v122\n\t"
        "v_mfma_f32_16x16x32_bf16 v[36:39], v[116:119], v[156:159], v[36:39]\n\t"
        "ds_read_b64_tr_b16 v[112:113], v130 offset:0\n\t"
        "buffer_load_dwordx4 v[140:143], v131, s[40:43], 0 offen offset:2048 sc1\n\t"
        "v_mfma_f32_16x16x32_bf16 v[44:47], v[116:119], v[160:163], v[44:47]\n\t"
        "ds_read_b64_tr_b16 v[114:115], v130 offset:3072\n\t"
        "s_waitcnt lgkmcnt(6)\n\t"
        "v_mfma_f32_16x16x32_bf16 v[48:51], v[96:99], v[156:159], v[48:51]\n\t"
        "ds_read_b64_tr_b16 v[116:117], v130 offset:32\n\t"
        "v_mfma_f32_16x16x32_bf16 v[56:59], v[96:99], v[160:163], v[56:59]\n\t"
        "ds_read_b64_tr_b16 v[118:119], v130 offset:3104\n\t"
        "s_cbranch_vccnz 14f\n\t"
        "15:\n\t"
        "v_mfma_f32_16x16x32_bf16 v[52:55], v[100:103], v[156:159], v[52:55]\n\t"
        "ds_read_b64_tr_b16 v[96:97], v123 offset:256\n\t"
        "s_addk_i32 s46, 12352\n\t"
        "v_mfma_f32_16x16x32_bf16 v[60:63], v[100:103], v[160:163], v[60:63]\n\t"
        "ds_read_b64_tr_b16 v[98:99], v123 offset:3328\n\t"
        "s_waitcnt lgkmcnt(6)\n\t"
        "v_mfma_f32_16x16x32_bf16 v[0:3], v[104:107], v[156:159], v[0:3]\n\t"
        "ds_read_b64_tr_b16 v[100:101], v123 offset:288\n\t"
        "s_cmp_eq_u32 s46, s47\n\t"
        "v_mfma_f32_16x16x32_bf16 v[8:11], v[104:107], v[160:163], v[8:11]\n\t"
        "ds_read_b64_tr_b16 v[102:103], v123 offset:3360\n\t"
        "s_cselect_b32 s46, 0, s46\n\t"
        "v_mfma_f32_16x16x32_bf16 v[4:7], v[108:111], v[156:159], v[4:7]\n\t"
        "ds_read_b64_tr_b16 v[104:105], v124 offset:256\n\t"
        "buffer_load_dwordx4 v[144:147], v131, s[40:43], 0 offen offset:2304 sc1\n\t"
        "v_mfma_f32_16x16x32_bf16 v[12:15], v[108:111], v[160:163], v[12:15]\n\t"
        "ds_read_b64_tr_b16 v[106:107], v124 offset:3328\n\t"
        "s_waitcnt lgkmcnt(6)\n\t"
        "v_mfma_f32_16x16x32_bf16 v[16:19], v[112:115], v[156:159], v[16:19]\n\t"
        "ds_write_b32 v127, v128\n\t"
        "ds_read_b64_tr_b16 v[108:109], v124 offset:288\n\t"
        "v_mfma_f32_16x16x32_bf16 v[24:27], v[112:115], v[160:163], v[24:27]\n\t"
        "ds_read_b64_tr_b16 v[110:111], v124 offset:3360\n\t"
        "v_add_u32 v128, 2, v128\n\t"
        "v_mfma_f32_16x16x32_bf16 v[20:23], v[116:119], v[156:159], v[20:23]\n\t"
        "ds_read_b64_tr_b16 v[112:113], v123 offset:128\n\t"
        "v_mfma_f32_16x16x32_bf16 v[28:31], v[116:119], v[160:163], v[28:31]\n\t"
        "ds_read_b64_tr_b16 v[114:115], v123 offset:3200\n\t"
        "s_waitcnt vmcnt(10) lgkmcnt(7)\n\t"
        "v_mfma_f32_16x16x32_bf16 v[64:67], v[164:167], v[96:99], v[64:67]\n\t"
        "ds_read_b64_tr_b16 v[116:117], v123 offset:160\n\t"
        "s_addk_i32 s44, 0x1000\n\t"
        "v_mfma_f32_16x16x32_bf16 v[72:75], v[168:171], v[96:99], v[72:75]\n\t"
        "ds_read_b64_tr_b16 v[118:119], v123 offset:3232\n\t"
        "s_bitset0_b32 s44, 16\n\t"
        "v_mfma_f32_16x16x32_bf16 v[68:71], v[164:167], v[100:103], v[68:71]\n\t"
        "ds_read_b64_tr_b16 v[96:97], v124 offset:128\n\t"
        "s_cmp_eq_u32 s44, s49\n\t"
        "v_mfma_f32_16x16x32_bf16 v[76:79], v[168:171], v[100:103], v[76:79]\n\t"
        "ds_read_b64_tr_b16 v[98:99], v124 offset:3200\n\t"
        "s_waitcnt lgkmcnt(6)\n\t"
        "v_mfma_f32_16x16x32_bf16 v[80:83], v[164:167], v[104:107], v[80:83]\n\t"
        "ds_read_b64_tr_b16 v[100:101], v124 offset:160\n\t"
        "s_cselect_b32 s44, s50, s44\n\t"
        "v_mfma_f32_16x16x32_bf16 v[88:91], v[168:171], v[104:107], v[88:91]\n\t"
        "ds_read_b64_tr_b16 v[102:103], v124 offset:3232\n\t"
        "v_add_u32 v131, s44, v120\n\t"
        "v_mfma_f32_16x16x32_bf16 v[84:87], v[164:167], v[108:111], v[84:87]\n\t"
        "ds_read_b64_tr_b16 v[104:105], v123 offset:0\n\t"
        "v_add_u32 v129, s46, v121\n\t"
        "v_mfma_f32_16x16x32_bf16 v[92:95], v[168:171], v[108:111], v[92:95]\n\t"
        "ds_read_b64_tr_b16 v[106:107], v123 offset:3072\n\t"
        "s_waitcnt lgkmcnt(6)\n\t"
        "v_mfma_f32_16x16x32_bf16 v[32:35], v[112:115], v[164:167], v[32:35]\n\t"
        "ds_read_b64_tr_b16 v[108:109], v123 offset:32\n\t"
        "v_mfma_f32_16x16x32_bf16 v[40:43], v[112:115], v[168:171], v[40:43]\n\t"
        "ds_read_b64_tr_b16 v[110:111], v123 offset:3104\n\t"
        "v_add_u32 v130, s46, v122\n\t"
        "v_mfma_f32_16x16x32_bf16 v[36:39], v[116:119], v[164:167], v[36:39]\n\t"
        "ds_read_b64_tr_b16 v[112:113], v124 offset:0\n\t"
        "buffer_load_dwordx4 v[148:151], v131, s[40:43], 0 offen offset:0 sc1\n\t"
        "v_mfma_f32_16x16x32_bf16 v[44:47], v[116:119], v[168:171], v[44:47]\n\t"
        "ds_read_b64_tr_b16 v[114:115], v124 offset:3072\n\t"
        "s_waitcnt lgkmcnt(6)\n\t"
        "v_mfma_f32_16x16x32_bf16 v[48:51], v[96:99], v[164:167], v[48:51]\n\t"
        "ds_read_b64_tr_b16 v[116:117], v124 offset:32\n\t"
        "v_mfma_f32_16x16x32_bf16 v[56:59], v[96:99], v[168:171], v[56:59]\n\t"
        "ds_read_b64_tr_b16 v[118:119], v124 offset:3104\n\t"
        "v_mfma_f32_16x16x32_bf16 v[52:55], v[100:103], v[164:167], v[52:55]\n\t"
        "ds_read_b64_tr_b16 v[96:97], v129 offset:256\n\t"
        "s_addk_i32 s46, 12352\n\t"
        "v_mfma_f32_16x16x32_bf16 v[60:63], v[100:103], v[168:171], v[60:63]\n\t"
        "ds_read_b64_tr_b16 v[98:99], v129 offset:3328\n\t"
        "s_waitcnt lgkmcnt(6)\n\t"
        "v_mfma_f32_16x16x32_bf16 v[0:3], v[104:107], v[164:167], v[0:3]\n\t"
        "ds_read_b64_tr_b16 v[100:101], v129 offset:288\n\t"
        "s_cmp_eq_u32 s46, s47\n\t"
        "v_mfma_f32_16x16x32_bf16 v[8:11], v[104:107], v[168:171], v[8:11]\n\t"
        "ds_read_b64_tr_b16 v[102:103], v129 offset:3360\n\t"
        "s_cselect_b32 s46, 0, s46\n\t"
        "v_mfma_f32_16x16x32_bf16 v[4:7], v[108:111], v[164:167], v[4:7]\n\t"
        "ds_read_b64_tr_b16 v[104:105], v130 offset:256\n\t"
        "buffer_load_dwordx4 v[152:155], v131, s[40:43], 0 offen offset:256 sc1\n\t"
        "v_mfma_f32_16x16x32_bf16 v[12:15], v[108:111], v[168:171], v[12:15]\n\t"
        "ds_read_b64_tr_b16 v[106:107], v130 offset:3328\n\t"
        "s_waitcnt lgkmcnt(6)\n\t"
        "v_mfma_f32_16x16x32_bf16 v[16:19], v[112:115], v[164:167], v[16:19]\n\t"
        "ds_read_b64_tr_b16 v[108:109], v130 offset:288\n\t"
        "v_mfma_f32_16x16x32_bf16 v[24:27], v[112:115], v[168:171], v[24:27]\n\t"
        "ds_read_b64_tr_b16 v[110:111], v130 offset:3360\n\t"
        "v_mfma_f32_16x16x32_bf16 v[20:23], v[116:119], v[164:167], v[20:23]\n\t"
        "ds_read_b64_tr_b16 v[112:113], v129 offset:128\n\t"
        "v_mfma_f32_16x16x32_bf16 v[28:31], v[116:119], v[168:171], v[28:31]\n\t"
        "ds_read_b64_tr_b16 v[114:115], v129 offset:3200\n\t"
        "s_waitcnt vmcnt(10) lgkmcnt(6)\n\t"
        "v_mfma_f32_16x16x32_bf16 v[64:67], v[172:175], v[96:99], v[64:67]\n\t"
        "ds_read_b64_tr_b16 v[116:117], v129 offset:160\n\t"
        "v_mfma_f32_16x16x32_bf16 v[72:75], v[176:179], v[96:99], v[72:75]\n\t"
        "ds_read_b64_tr_b16 v[118:119], v129 offset:3232\n\t"
        "v_mfma_f32_16x16x32_bf16 v[68:71], v[172:175], v[100:103], v[68:71]\n\t"
        "ds_read_b64_tr_b16 v[96:97], v130 offset:128\n\t"
        "ds_read_b32 v126, v125\n\t"
        "v_mfma_f32_16x16x32_bf16 v[76:79], v[176:179], v[100:103], v[76:79]\n\t"
        "ds_read_b64_tr_b16 v[98:99], v130 offset:3200\n\t"
        "s_waitcnt lgkmcnt(7)\n\t"
        "v_mfma_f32_16x16x32_bf16 v[80:83], v[172:175], v[104:107], v[80:83]\n\t"
        "ds_read_b64_tr_b16 v[100:101], v130 offset:160\n\t"
        "v_mfma_f32_16x16x32_bf16 v[88:91], v[176:179], v[104:107], v[88:91]\n\t"
        "ds_read_b64_tr_b16 v[102:103], v130 offset:3232\n\t"
        "v_mfma_f32_16x16x32_bf16 v[84:87], v[172:175], v[108:111], v[84:87]\n\t"
        "ds_read_b64_tr_b16 v[104:105], v129 offset:0\n\t"
        "v_add_u32 v123, s46, v121\n\t"
        "v_mfma_f32_16x16x32_bf16 v[92:95], v[176:179], v[108:111], v[92:95]\n\t"
        "ds_read_b64_tr_b16 v[106:107], v129 offset:3072\n\t"
        "s_waitcnt lgkmcnt(5)\n\t"
        "v_mfma_f32_16x16x32_bf16 v[32:35], v[112:115], v[172:175], v[32:35]\n\t"
        "ds_read_b64_tr_b16 v[108:109], v129 offset:32\n\t"
        "v_cmp_gt_u32 vcc, v128, v126\n\t"
        "v_mfma_f32_16x16x32_bf16 v[40:43], v[112:115], v[176:179], v[40:43]\n\t"
        "ds_read_b64_tr_b16 v[110:111], v129 offset:3104\n\t"
        "v_add_u32 v124, s46, v122\n\t"
        "v_mfma_f32_16x16x32_bf16 v[36:39], v[116:119], v[172:175], v[36:39]\n\t"
        "ds_read_b64_tr_b16 v[112:113], v130 offset:0\n\t"
        "buffer_load_dwordx4 v[156:159], v131, s[40:43], 0 offen offset:2048 sc1\n\t"
        "v_mfma_f32_16x16x32_bf16 v[44:47], v[116:119], v[176:179], v[44:47]\n\t"
        "ds_read_b64_tr_b16 v[114:115], v130 offset:3072\n\t"
        "s_waitcnt lgkmcnt(6)\n\t"
        "v_mfma_f32_16x16x32_bf16 v[48:51], v[96:99], v[172:175], v[48:51]\n\t"
        "ds_read_b64_tr_b16 v[116:117], v130 offset:32\n\t"
        "v_mfma_f32_16x16x32_bf16 v[56:59], v[96:99], v[176:179], v[56:59]\n\t"
        "ds_read_b64_tr_b16 v[118:119], v130 offset:3104\n\t"
        "s_cbranch_vccnz 16f\n\t"
        "17:\n\t"
        "v_mfma_f32_16x16x32_bf16 v[52:55], v[100:103], v[172:175], v[52:55]\n\t"
        "ds_read_b64_tr_b16 v[96:97], v123 offset:256\n\t"
        "s_addk_i32 s46, 12352\n\t"
        "v_mfma_f32_16x16x32_bf16 v[60:63], v[100:103], v[176:179], v[60:63]\n\t"
        "ds_read_b64_tr_b16 v[98:99], v123 offset:3328\n\t"
        "s_waitcnt lgkmcnt(6)\n\t"
        "v_mfma_f32_16x16x32_bf16 v[0:3], v[104:107], v[172:175], v[0:3]\n\t"
        "ds_read_b64_tr_b16 v[100:101], v123 offset:288\n\t"
        "s_cmp_eq_u32 s46, s47\n\t"
        "v_mfma_f32_16x16x32_bf16 v[8:11], v[104:107], v[176:179], v[8:11]\n\t"
        "ds_read_b64_tr_b16 v[102:103], v123 offset:3360\n\t"
        "s_cselect_b32 s46, 0, s46\n\t"
        "v_mfma_f32_16x16x32_bf16 v[4:7], v[108:111], v[172:175], v[4:7]\n\t"
        "ds_read_b64_tr_b16 v[104:105], v124 offset:256\n\t"
        "buffer_load_dwordx4 v[160:163], v131, s[40:43], 0 offen offset:2304 sc1\n\t"
        "v_mfma_f32_16x16x32_bf16 v[12:15], v[108:111], v[176:179], v[12:15]\n\t"
        "ds_read_b64_tr_b16 v[106:107], v124 offset:3328\n\t"
        "s_waitcnt lgkmcnt(6)\n\t"
        "v_mfma_f32_16x16x32_bf16 v[16:19], v[112:115], v[172:175], v[16:19]\n\t"
        "ds_write_b32 v127, v128\n\t"
        "ds_read_b64_tr_b16 v[108:109], v124 offset:288\n\t"
        "v_mfma_f32_16x16x32_bf16 v[24:27], v[112:115], v[176:179], v[24:27]\n\t"
        "ds_read_b64_tr_b16 v[110:111], v124 offset:3360\n\t"
        "v_add_u32 v128, 2, v128\n\t"
        "v_mfma_f32_16x16x32_bf16 v[20:23], v[116:119], v[172:175], v[20:23]\n\t"
        "ds_read_b64_tr_b16 v[112:113], v123 offset:128\n\t"
        "v_mfma_f32_16x16x32_bf16 v[28:31], v[116:119], v[176:179], v[28:31]\n\t"
        "ds_read_b64_tr_b16 v[114:115], v123 offset:3200\n\t"
        "s_waitcnt vmcnt(10) lgkmcnt(7)\n\t"
        "v_mfma_f32_16x16x32_bf16 v[64:67], v[180:183], v[96:99], v[64:67]\n\t"
        "ds_read_b64_tr_b16 v[116:117], v123 offset:160\n\t"
        "s_addk_i32 s44, 0x1000\n\t"
        "v_mfma_f32_16x16x32_bf16 v[72:75], v[184:187], v[96:99], v[72:75]\n\t"
        "ds_read_b64_tr_b16 v[118:119], v123 offset:3232\n\t"
        "s_bitset0_b32 s44, 16\n\t"
        "v_mfma_f32_16x16x32_bf16 v[68:71], v[180:183], v[100:103], v[68:71]\n\t"
        "ds_read_b64_tr_b16 v[96:97], v124 offset:128\n\t"
        "s_cmp_eq_u32 s44, s49\n\t"
        "v_mfma_f32_16x16x32_bf16 v[76:79], v[184:187], v[100:103], v[76:79]\n\t"
        "ds_read_b64_tr_b16 v[98:99], v124 offset:3200\n\t"
        "s_waitcnt lgkmcnt(6)\n\t"
        "v_mfma_f32_16x16x32_bf16 v[80:83], v[180:183], v[104:107], v[80:83]\n\t"
        "ds_read_b64_tr_b16 v[100:101], v124 offset:160\n\t"
        "s_cselect_b32 s44, s50, s44\n\t"
        "v_mfma_f32_16x16x32_bf16 v[88:91], v[184:187], v[104:107], v[88:91]\n\t"
        "ds_read_b64_tr_b16 v[102:103], v124 offset:3232\n\t"
        "v_add_u32 v131, s44, v120\n\t"
        "v_mfma_f32_16x16x32_bf16 v[84:87], v[180:183], v[108:111], v[84:87]\n\t"
        "ds_read_b64_tr_b16 v[104:105], v123 offset:0\n\t"
        "v_add_u32 v129, s46, v121\n\t"
        "v_mfma_f32_16x16x32_bf16 v[92:95], v[184:187], v[108:111], v[92:95]\n\t"
        "ds_read_b64_tr_b16 v[106:107], v123 offset:3072\n\t"
        "s_waitcnt lgkmcnt(6)\n\t"
        "v_mfma_f32_16x16x32_bf16 v[32:35], v[112:115], v[180:183], v[32:35]\n\t"
        "ds_read_b64_tr_b16 v[108:109], v123 offset:32\n\t"
        "v_mfma_f32_16x16x32_bf16 v[40:43], v[112:115], v[184:187], v[40:43]\n\t"
        "ds_read_b64_tr_b16 v[110:111], v123 offset:3104\n\t"
        "v_add_u32 v130, s46, v122\n\t"
        "v_mfma_f32_16x16x32_bf16 v[36:39], v[116:119], v[180:183], v[36:39]\n\t"
        "ds_read_b64_tr_b16 v[112:113], v124 offset:0\n\t"
        "buffer_load_dwordx4 v[164:167], v131, s[40:43], 0 offen offset:0 sc1\n\t"
        "v_mfma_f32_16x16x32_bf16 v[44:47], v[116:119], v[184:187], v[44:47]\n\t"
        "ds_read_b64_tr_b16 v[114:115], v124 offset:3072\n\t"
        "s_waitcnt lgkmcnt(6)\n\t"
        "v_mfma_f32_16x16x32_bf16 v[48:51], v[96:99], v[180:183], v[48:51]\n\t"
        "ds_read_b64_tr_b16 v[116:117], v124 offset:32\n\t"
        "v_mfma_f32_16x16x32_bf16 v[56:59], v[96:99], v[184:187], v[56:59]\n\t"
        "ds_read_b64_tr_b16 v[118:119], v124 offset:3104\n\t"
        "v_mfma_f32_16x16x32_bf16 v[52:55], v[100:103], v[180:183], v[52:55]\n\t"
        "ds_read_b64_tr_b16 v[96:97], v129 offset:256\n\t"
        "s_addk_i32 s46, 12352\n\t"
        "v_mfma_f32_16x16x32_bf16 v[60:63], v[100:103], v[184:187], v[60:63]\n\t"
        "ds_read_b64_tr_b16 v[98:99], v129 offset:3328\n\t"
        "s_waitcnt lgkmcnt(6)\n\t"
        "v_mfma_f32_16x16x32_bf16 v[0:3], v[104:107], v[180:183], v[0:3]\n\t"
        "ds_read_b64_tr_b16 v[100:101], v129 offset:288\n\t"
        "s_cmp_eq_u32 s46, s47\n\t"
        "v_mfma_f32_16x16x32_bf16 v[8:11], v[104:107], v[184:187], v[8:11]\n\t"
        "ds_read_b64_tr_b16 v[102:103], v129 offset:3360\n\t"
        "s_cselect_b32 s46, 0, s46\n\t"
        "v_mfma_f32_16x16x32_bf16 v[4:7], v[108:111], v[180:183], v[4:7]\n\t"
        "ds_read_b64_tr_b16 v[104:105], v130 offset:256\n\t"
        "buffer_load_dwordx4 v[168:171], v131, s[40:43], 0 offen offset:256 sc1\n\t"
        "v_mfma_f32_16x16x32_bf16 v[12:15], v[108:111], v[184:187], v[12:15]\n\t"
        "ds_read_b64_tr_b16 v[106:107], v130 offset:3328\n\t"
        "s_waitcnt lgkmcnt(6)\n\t"
        "v_mfma_f32_16x16x32_bf16 v[16:19], v[112:115], v[180:183], v[16:19]\n\t"
        "ds_read_b64_tr_b16 v[108:109], v130 offset:288\n\t"
        "v_mfma_f32_16x16x32_bf16 v[24:27], v[112:115], v[184:187], v[24:27]\n\t"
        "ds_read_b64_tr_b16 v[110:111], v130 offset:3360\n\t"
        "v_mfma_f32_16x16x32_bf16 v[20:23], v[116:119], v[180:183], v[20:23]\n\t"
        "ds_read_b64_tr_b16 v[112:113], v129 offset:128\n\t"
        "v_mfma_f32_16x16x32_bf16 v[28:31], v[116:119], v[184:187], v[28:31]\n\t"
        "ds_read_b64_tr_b16 v[114:115], v129 offset:3200\n\t"
        "s_waitcnt vmcnt(10) lgkmcnt(6)\n\t"
        "v_mfma_f32_16x16x32_bf16 v[64:67], v[188:191], v[96:99], v[64:67]\n\t"
        "ds_read_b64_tr_b16 v[116:117], v129 offset:160\n\t"
        "v_mfma_f32_16x16x32_bf16 v[72:75], v[192:195], v[96:99], v[72:75]\n\t"
        "ds_read_b64_tr_b16 v[118:119], v129 offset:3232\n\t"
        "v_mfma_f32_16x16x32_bf16 v[68:71], v[188:191], v[100:103], v[68:71]\n\t"
        "ds_read_b64_tr_b16 v[96:97], v130 offset:128\n\t"
        "ds_read_b32 v126, v125\n\t"
        "v_mfma_f32_16x16x32_bf16 v[76:79], v[192:195], v[100:103], v[76:79]\n\t"
        "ds_read_b64_tr_b16 v[98:99], v130 offset:3200\n\t"
        "s_waitcnt lgkmcnt(7)\n\t"
        "v_mfma_f32_16x16x32_bf16 v[80:83], v[188:191], v[104:107], v[80:83]\n\t"
        "ds_read_b64_tr_b16 v[100:101], v130 offset:160\n\t"
        "v_mfma_f32_16x16x32_bf16 v[88:91], v[192:195], v[104:107], v[88:91]\n\t"
        "ds_read_b64_tr_b16 v[102:103], v130 offset:3232\n\t"
        "v_mfma_f32_16x16x32_bf16 v[84:87], v[188:191], v[108:111], v[84:87]\n\t"
        "ds_read_b64_tr_b16 v[104:105], v129 offset:0\n\t"
        "v_add_u32 v123, s46, v121\n\t"
        "v_mfma_f32_16x16x32_bf16 v[92:95], v[192:195], v[108:111], v[92:95]\n\t"
        "ds_read_b64_tr_b16 v[106:107], v129 offset:3072\n\t"
        "s_waitcnt lgkmcnt(5)\n\t"
        "v_mfma_f32_16x16x32_bf16 v[32:35], v[112:115], v[188:191], v[32:35]\n\t"
        "ds_read_b64_tr_b16 v[108:109], v129 offset:32\n\t"
        "v_cmp_gt_u32 vcc, v128, v126\n\t"
        "v_mfma_f32_16x16x32_bf16 v[40:43], v[112:115], v[192:195], v[40:43]\n\t"
        "ds_read_b64_tr_b16 v[110:111], v129 offset:3104\n\t"
        "v_add_u32 v124, s46, v122\n\t"
        "v_mfma_f32_16x16x32_bf16 v[36:39], v[116:119], v[188:191], v[36:39]\n\t"
        "ds_read_b64_tr_b16 v[112:113], v130 offset:0\n\t"
        "buffer_load_dwordx4 v[172:175], v131, s[40:43], 0 offen offset:2048 sc1\n\t"
        "v_mfma_f32_16x16x32_bf16 v[44:47], v[116:119], v[192:195], v[44:47]\n\t"
        "ds_read_b64_tr_b16 v[114:115], v130 offset:3072\n\t"
        "s_waitcnt lgkmcnt(6)\n\t"
        "v_mfma_f32_16x16x32_bf16 v[48:51], v[96:99], v[188:191], v[48:51]\n\t"
        "ds_read_b64_tr_b16 v[116:117], v130 offset:32\n\t"
        "v_mfma_f32_16x16x32_bf16 v[56:59], v[96:99], v[192:195], v[56:59]\n\t"
        "ds_read_b64_tr_b16 v[118:119], v130 offset:3104\n\t"
        "s_cbranch_vccnz 18f\n\t"
        "19:\n\t"
        "v_mfma_f32_16x16x32_bf16 v[52:55], v[100:103], v[188:191], v[52:55]\n\t"
        "ds_read_b64_tr_b16 v[96:97], v123 offset:256\n\t"
        "s_addk_i32 s46, 12352\n\t"
        "v_mfma_f32_16x16x32_bf16 v[60:63], v[100:103], v[192:195], v[60:63]\n\t"
        "ds_read_b64_tr_b16 v[98:99], v123 offset:3328\n\t"
        "s_waitcnt lgkmcnt(6)\n\t"
        "v_mfma_f32_16x16x32_bf16 v[0:3], v[104:107], v[188:191], v[0:3]\n\t"
        "ds_read_b64_tr_b16 v[100:101], v123 offset:288\n\t"
        "s_cmp_eq_u32 s46, s47\n\t"
        "v_mfma_f32_16x16x32_bf16 v[8:11], v[104:107], v[192:195], v[8:11]\n\t"
        "ds_read_b64_tr_b16 v[102:103], v123 offset:3360\n\t"
        "s_cselect_b32 s46, 0, s46\n\t"
        "v_mfma_f32_16x16x32_bf16 v[4:7], v[108:111], v[188:191], v[4:7]\n\t"
        "ds_read_b64_tr_b16 v[104:105], v124 offset:256\n\t"
        "buffer_load_dwordx4 v[176:179], v131, s[40:43], 0 offen offset:2304 sc1\n\t"
        "v_mfma_f32_16x16x32_bf16 v[12:15], v[108:111], v[192:195], v[12:15]\n\t"
        "ds_read_b64_tr_b16 v[106:107], v124 offset:3328\n\t"
        "s_waitcnt lgkmcnt(6)\n\t"
        "v_mfma_f32_16x16x32_bf16 v[16:19], v[112:115], v[188:191], v[16:19]\n\t"
        "ds_write_b32 v127, v128\n\t"
        "ds_read_b64_tr_b16 v[108:109], v124 offset:288\n\t"
        "v_mfma_f32_16x16x32_bf16 v[24:27], v[112:115], v[192:195], v[24:27]\n\t"
        "ds_read_b64_tr_b16 v[110:111], v124 offset:3360\n\t"
        "v_add_u32 v128, 2, v128\n\t"
        "v_mfma_f32_16x16x32_bf16 v[20:23], v[116:119], v[188:191], v[20:23]\n\t"
        "ds_read_b64_tr_b16 v[112:113], v123 offset:128\n\t"
        "v_mfma_f32_16x16x32_bf16 v[28:31], v[116:119], v[192:195], v[28:31]\n\t"
        "ds_read_b64_tr_b16 v[114:115], v123 offset:3200\n\t"
        "s_waitcnt vmcnt(10) lgkmcnt(7)\n\t"
        "v_mfma_f32_16x16x32_bf16 v[64:67], v[132:135], v[96:99], v[64:67]\n\t"
        "ds_read_b64_tr_b16 v[116:117], v123 offset:160\n\t"
        "s_addk_i32 s44, 0x1000\n\t"
        "v_mfma_f32_16x16x32_bf16 v[72:75], v[136:139], v[96:99], v[72:75]\n\t"
        "ds_read_b64_tr_b16 v[118:119], v123 offset:3232\n\t"
        "s_bitset0_b32 s44, 16\n\t"
        "v_mfma_f32_16x16x32_bf16 v[68:71], v[132:135], v[100:103], v[68:71]\n\t"
        "ds_read_b64_tr_b16 v[96:97], v124 offset:128\n\t"
        "s_cmp_eq_u32 s44, s49\n\t"
        "v_mfma_f32_16x16x32_bf16 v[76:79], v[136:139], v[100:103], v[76:79]\n\t"
        "ds_read_b64_tr_b16 v[98:99], v124 offset:3200\n\t"
        "s_waitcnt lgkmcnt(6)\n\t"
        "v_mfma_f32_16x16x32_bf16 v[80:83], v[132:135], v[104:107], v[80:83]\n\t"
        "ds_read_b64_tr_b16 v[100:101], v124 offset:160\n\t"
        "s_cselect_b32 s44, s50, s44\n\t"
        "v_mfma_f32_16x16x32_bf16 v[88:91], v[136:139], v[104:107], v[88:91]\n\t"
        "ds_read_b64_tr_b16 v[102:103], v124 offset:3232\n\t"
        "v_add_u32 v131, s44, v120\n\t"
        "v_mfma_f32_16x16x32_bf16 v[84:87], v[132:135], v[108:111], v[84:87]\n\t"
        "ds_read_b64_tr_b16 v[104:105], v123 offset:0\n\t"
        "v_add_u32 v129, s46, v121\n\t"
        "v_mfma_f32_16x16x32_bf16 v[92:95], v[136:139], v[108:111], v[92:95]\n\t"
        "ds_read_b64_tr_b16 v[106:107], v123 offset:3072\n\t"
        "s_waitcnt lgkmcnt(6)\n\t"
        "v_mfma_f32_16x16x32_bf16 v[32:35], v[112:115], v[132:135], v[32:35]\n\t"
        "ds_read_b64_tr_b16 v[108:109], v123 offset:32\n\t"
        "v_mfma_f32_16x16x32_bf16 v[40:43], v[112:115], v[136:139], v[40:43]\n\t"
        "ds_read_b64_tr_b16 v[110:111], v123 offset:3104\n\t"
        "v_add_u32 v130, s46, v122\n\t"
        "v_mfma_f32_16x16x32_bf16 v[36:39], v[116:119], v[132:135], v[36:39]\n\t"
        "ds_read_b64_tr_b16 v[112:113], v124 offset:0\n\t"
        "buffer_load_dwordx4 v[180:183], v131, s[40:43], 0 offen offset:0 sc1\n\t"
        "v_mfma_f32_16x16x32_bf16 v[44:47], v[116:119], v[136:139], v[44:47]\n\t"
        "ds_read_b64_tr_b16 v[114:115], v124 offset:3072\n\t"
        "s_waitcnt lgkmcnt(6)\n\t"
        "v_mfma_f32_16x16x32_bf16 v[48:51], v[96:99], v[132:135], v[48:51]\n\t"
        "ds_read_b64_tr_b16 v[116:117], v124 offset:32\n\t"
        "v_mfma_f32_16x16x32_bf16 v[56:59], v[96:99], v[136:139], v[56:59]\n\t"
        "ds_read_b64_tr_b16 v[118:119], v124 offset:3104\n\t"
        "v_mfma_f32_16x16x32_bf16 v[52:55], v[100:103], v[132:135], v[52:55]\n\t"
        "ds_read_b64_tr_b16 v[96:97], v129 offset:256\n\t"
        "s_addk_i32 s46, 12352\n\t"
        "v_mfma_f32_16x16x32_bf16 v[60:63], v[100:103], v[136:139], v[60:63]\n\t"
        "ds_read_b64_tr_b16 v[98:99], v129 offset:3328\n\t"
        "s_waitcnt lgkmcnt(6)\n\t"
        "v_mfma_f32_16x16x32_bf16 v[0:3], v[104:107], v[132:135], v[0:3]\n\t"
        "ds_read_b64_tr_b16 v[100:101], v129 offset:288\n\t"
        "s_cmp_eq_u32 s46, s47\n\t"
        "v_mfma_f32_16x16x32_bf16 v[8:11], v[104:107], v[136:139], v[8:11]\n\t"
        "ds_read_b64_tr_b16 v[102:103], v129 offset:3360\n\t"
        "s_cselect_b32 s46, 0, s46\n\t"
        "v_mfma_f32_16x16x32_bf16 v[4:7], v[108:111], v[132:135], v[4:7]\n\t"
        "ds_read_b64_tr_b16 v[104:105], v130 offset:256\n\t"
        "buffer_load_dwordx4 v[184:187], v131, s[40:43], 0 offen offset:256 sc1\n\t"
        "v_mfma_f32_16x16x32_bf16 v[12:15], v[108:111], v[136:139], v[12:15]\n\t"
        "ds_read_b64_tr_b16 v[106:107], v130 offset:3328\n\t"
        "s_waitcnt lgkmcnt(6)\n\t"
        "v_mfma_f32_16x16x32_bf16 v[16:19], v[112:115], v[132:135], v[16:19]\n\t"
        "ds_read_b64_tr_b16 v[108:109], v130 offset:288\n\t"
        "v_mfma_f32_16x16x32_bf16 v[24:27], v[112:115], v[136:139], v[24:27]\n\t"
        "ds_read_b64_tr_b16 v[110:111], v130 offset:3360\n\t"
        "v_mfma_f32_16x16x32_bf16 v[20:23], v[116:119], v[132:135], v[20:23]\n\t"
        "ds_read_b64_tr_b16 v[112:113], v129 offset:128\n\t"
        "v_mfma_f32_16x16x32_bf16 v[28:31], v[116:119], v[136:139], v[28:31]\n\t"
        "ds_read_b64_tr_b16 v[114:115], v129 offset:3200\n\t"
        "s_waitcnt vmcnt(10) lgkmcnt(6)\n\t"
        "v_mfma_f32_16x16x32_bf16 v[64:67], v[140:143], v[96:99], v[64:67]\n\t"
        "ds_read_b64_tr_b16 v[116:117], v129 offset:160\n\t"
        "v_mfma_f32_16x16x32_bf16 v[72:75], v[144:147], v[96:99], v[72:75]\n\t"
        "ds_read_b64_tr_b16 v[118:119], v129 offset:3232\n\t"
        "v_mfma_f32_16x16x32_bf16 v[68:71], v[140:143], v[100:103], v[68:71]\n\t"
        "ds_read_b64_tr_b16 v[96:97], v130 offset:128\n\t"
        "ds_read_b32 v126, v125\n\t"
        "v_mfma_f32_16x16x32_bf16 v[76:79], v[144:147], v[100:103], v[76:79]\n\t"
        "ds_read_b64_tr_b16 v[98:99], v130 offset:3200\n\t"
        "s_waitcnt lgkmcnt(7)\n\t"
        "v_mfma_f32_16x16x32_bf16 v[80:83], v[140:143], v[104:107], v[80:83]\n\t"
        "ds_read_b64_tr_b16 v[100:101], v130 offset:160\n\t"
        "v_mfma_f32_16x16x32_bf16 v[88:91], v[144:147], v[104:107], v[88:91]\n\t"
        "ds_read_b64_tr_b16 v[102:103], v130 offset:3232\n\t"
        "v_mfma_f32_16x16x32_bf16 v[84:87], v[140:143], v[108:111], v[84:87]\n\t"
        "ds_read_b64_tr_b16 v[104:105], v129 offset:0\n\t"
        "v_add_u32 v123, s46, v121\n\t"
        "v_mfma_f32_16x16x32_bf16 v[92:95], v[144:147], v[108:111], v[92:95]\n\t"
        "ds_read_b64_tr_b16 v[106:107], v129 offset:3072\n\t"
        "s_waitcnt lgkmcnt(5)\n\t"
        "v_mfma_f32_16x16x32_bf16 v[32:35], v[112:115], v[140:143], v[32:35]\n\t"
        "ds_read_b64_tr_b16 v[108:109], v129 offset:32\n\t"
        "v_cmp_gt_u32 vcc, v128, v126\n\t"
        "v_mfma_f32_16x16x32_bf16 v[40:43], v[112:115], v[144:147], v[40:43]\n\t"
        "ds_read_b64_tr_b16 v[110:111], v129 offset:3104\n\t"
        "v_add_u32 v124, s46, v122\n\t"
        "v_mfma_f32_16x16x32_bf16 v[36:39], v[116:119], v[140:143], v[36:39]\n\t"
        "ds_read_b64_tr_b16 v[112:113], v130 offset:0\n\t"
        "buffer_load_dwordx4 v[188:191], v131, s[40:43], 0 offen offset:2048 sc1\n\t"
        "v_mfma_f32_16x16x32_bf16 v[44:47], v[116:119], v[144:147], v[44:47]\n\t"
        "ds_read_b64_tr_b16 v[114:115], v130 offset:3072\n\t"
        "s_waitcnt lgkmcnt(6)\n\t"
        "v_mfma_f32_16x16x32_bf16 v[48:51], v[96:99], v[140:143], v[48:51]\n\t"
        "ds_read_b64_tr_b16 v[116:117], v130 offset:32\n\t"
        "v_mfma_f32_16x16x32_bf16 v[56:59], v[96:99], v[144:147], v[56:59]\n\t"
        "ds_read_b64_tr_b16 v[118:119], v130 offset:3104\n\t"
        "s_cbranch_vccnz 20f\n\t"
        "21:\n\t"
        "v_mfma_f32_16x16x32_bf16 v[52:55], v[100:103], v[140:143], v[52:55]\n\t"
        "ds_read_b64_tr_b16 v[96:97], v123 offset:256\n\t"
        "s_addk_i32 s46, 12352\n\t"
        "v_mfma_f32_16x16x32_bf16 v[60:63], v[100:103], v[144:147], v[60:63]\n\t"
        "ds_read_b64_tr_b16 v[98:99], v123 offset:3328\n\t"
        "s_waitcnt lgkmcnt(6)\n\t"
        "v_mfma_f32_16x16x32_bf16 v[0:3], v[104:107], v[140:143], v[0:3]\n\t"
        "ds_read_b64_tr_b16 v[100:101], v123 offset:288\n\t"
        "s_cmp_eq_u32 s46, s47\n\t"
        "v_mfma_f32_16x16x32_bf16 v[8:11], v[104:107], v[144:147], v[8:11]\n\t"
        "ds_read_b64_tr_b16 v[102:103], v123 offset:3360\n\t"
        "s_cselect_b32 s46, 0, s46\n\t"
        "v_mfma_f32_16x16x32_bf16 v[4:7], v[108:111], v[140:143], v[4:7]\n\t"
        "ds_read_b64_tr_b16 v[104:105], v124 offset:256\n\t"
        "buffer_load_dwordx4 v[192:195], v131, s[40:43], 0 offen offset:2304 sc1\n\t"
        "v_mfma_f32_16x16x32_bf16 v[12:15], v[108:111], v[144:147], v[12:15]\n\t"
        "ds_read_b64_tr_b16 v[106:107], v124 offset:3328\n\t"
        "s_waitcnt lgkmcnt(6)\n\t"
        "v_mfma_f32_16x16x32_bf16 v[16:19], v[112:115], v[140:143], v[16:19]\n\t"
        "ds_write_b32 v127, v128\n\t"
        "ds_read_b64_tr_b16 v[108:109], v124 offset:288\n\t"
        "v_mfma_f32_16x16x32_bf16 v[24:27], v[112:115], v[144:147], v[24:27]\n\t"
        "ds_read_b64_tr_b16 v[110:111], v124 offset:3360\n\t"
        "v_add_u32 v128, 2, v128\n\t"
        "v_mfma_f32_16x16x32_bf16 v[20:23], v[116:119], v[140:143], v[20:23]\n\t"
        "ds_read_b64_tr_b16 v[112:113], v123 offset:128\n\t"
        "s_sub_u32 s45, s45, 1\n\t"
        "v_mfma_f32_16x16x32_bf16 v[28:31], v[116:119], v[144:147], v[28:31]\n\t"
        "ds_read_b64_tr_b16 v[114:115], v123 offset:3200\n\t"
        "s_cbranch_scc0 1b\n\t"
        "s_waitcnt vmcnt(10) lgkmcnt(7)\n\t"
        "v_mfma_f32_16x16x32_bf16 v[64:67], v[148:151], v[96:99], v[64:67]\n\t"
        "ds_read_b64_tr_b16 v[116:117], v121 offset:256\n\t"
        "ds_read_b32 v126, v125\n\t"
        "v_mfma_f32_16x16x32_bf16 v[72:75], v[152:155], v[96:99], v[72:75]\n\t"
        "ds_read_b64_tr_b16 v[118:119], v121 offset:3328\n\t"
        "v_add_u32 v128, 2, v128\n\t"
        "v_mfma_f32_16x16x32_bf16 v[68:71], v[148:151], v[100:103], v[68:71]\n\t"
        "ds_read_b64_tr_b16 v[96:97], v121 offset:288\n\t"
        "v_mfma_f32_16x16x32_bf16 v[76:79], v[152:155], v[100:103], v[76:79]\n\t"
        "ds_read_b64_tr_b16 v[98:99], v121 offset:3360\n\t"
        "s_waitcnt lgkmcnt(3)\n\t"
        "v_mfma_f32_16x16x32_bf16 v[80:83], v[148:151], v[104:107], v[80:83]\n\t"
        "ds_read_b64_tr_b16 v[100:101], v122 offset:256\n\t"
        "v_cmp_gt_u32 vcc, v128, v126\n\t"
        "v_mfma_f32_16x16x32_bf16 v[88:91], v[152:155], v[104:107], v[88:91]\n\t"
        "ds_read_b64_tr_b16 v[102:103], v122 offset:3328\n\t"
        "v_mfma_f32_16x16x32_bf16 v[84:87], v[148:151], v[108:111], v[84:87]\n\t"
        "ds_read_b64_tr_b16 v[104:105], v122 offset:288\n\t"
        "v_mfma_f32_16x16x32_bf16 v[92:95], v[152:155], v[108:111], v[92:95]\n\t"
        "ds_read_b64_tr_b16 v[106:107], v122 offset:3360\n\t"
        "s_cbranch_vccnz 38f\n\t"
        "39:\n\t"
        "s_waitcnt vmcnt(8) lgkmcnt(6)\n\t"
        "v_mfma_f32_16x16x32_bf16 v[32:35], v[112:115], v[148:151], v[32:35]\n\t"
        "ds_read_b64_tr_b16 v[108:109], v121 offset:12608\n\t"
        "v_mfma_f32_16x16x32_bf16 v[40:43], v[112:115], v[152:155], v[40:43]\n\t"
        "ds_read_b64_tr_b16 v[110:111], v121 offset:15680\n\t"
        "v_mfma_f32_16x16x32_bf16 v[64:67], v[156:159], v[116:119], v[64:67]\n\t"
        "ds_read_b64_tr_b16 v[112:113], v121 offset:12640\n\t"
        "v_mfma_f32_16x16x32_bf16 v[72:75], v[160:163], v[116:119], v[72:75]\n\t"
        "ds_read_b64_tr_b16 v[114:115], v121 offset:15712\n\t"
        "s_waitcnt lgkmcnt(6)\n\t"
        "v_mfma_f32_16x16x32_bf16 v[68:71], v[156:159], v[96:99], v[68:71]\n\t"
        "ds_read_b64_tr_b16 v[116:117], v122 offset:12608\n\t"
        "ds_read_b128 v[196:199], v215 offset:0\n\t"
        "v_mfma_f32_16x16x32_bf16 v[76:79], v[160:163], v[96:99], v[76:79]\n\t"
        "ds_read_b64_tr_b16 v[118:119], v122 offset:15680\n\t"
        "v_mfma_f32_16x16x32_bf16 v[80:83], v[156:159], v[100:103], v[80:83]\n\t"
        "ds_read_b64_tr_b16 v[96:97], v122 offset:12640\n\t"
        "v_mfma_f32_16x16x32_bf16 v[88:91], v[160:163], v[100:103], v[88:91]\n\t"
        "ds_read_b64_tr_b16 v[98:99], v122 offset:15712\n\t"
        "s_waitcnt vmcnt(6) lgkmcnt(7)\n\t"
        "v_mfma_f32_16x16x32_bf16 v[84:87], v[156:159], v[104:107], v[84:87]\n\t"
        "ds_read_b64_tr_b16 v[100:101], v121 offset:24960\n\t"
        "ds_read_b128 v[200:203], v215 offset:64\n\t"
        "v_mfma_f32_16x16x32_bf16 v[92:95], v[160:163], v[104:107], v[92:95]\n\t"
        "ds_read_b64_tr_b16 v[102:103], v121 offset:28032\n\t"
        "v_mfma_f32_16x16x32_bf16 v[64:67], v[164:167], v[108:111], v[64:67]\n\t"
        "ds_read_b64_tr_b16 v[104:105], v121 offset:24992\n\t"
        "v_mfma_f32_16x16x32_bf16 v[72:75], v[168:171], v[108:111], v[72:75]\n\t"
        "ds_read_b64_tr_b16 v[106:107], v121 offset:28064\n\t"
        "s_waitcnt lgkmcnt(7)\n\t"
        "v_mfma_f32_16x16x32_bf16 v[68:71], v[164:167], v[112:115], v[68:71]\n\t"
        "ds_read_b64_tr_b16 v[108:109], v122 offset:24960\n\t"
        "v_mfma_f32_16x16x32_bf16 v[76:79], v[168:171], v[112:115], v[76:79]\n\t"
        "ds_read_b64_tr_b16 v[110:111], v122 offset:28032\n\t"
        "v_mfma_f32_16x16x32_bf16 v[80:83], v[164:167], v[116:119], v[80:83]\n\t"
        "ds_read_b64_tr_b16 v[112:113], v122 offset:24992\n\t"
        "v_mfma_f32_16x16x32_bf16 v[88:91], v[168:171], v[116:119], v[88:91]\n\t"
        "ds_read_b64_tr_b16 v[114:115], v122 offset:28064\n\t"
        "s_waitcnt vmcnt(4) lgkmcnt(6)\n\t"
        "v_mfma_f32_16x16x32_bf16 v[84:87], v[164:167], v[96:99], v[84:87]\n\t"
        "ds_read_b64_tr_b16 v[116:117], v121 offset:37312\n\t"
        "v_mfma_f32_16x16x32_bf16 v[92:95], v[168:171], v[96:99], v[92:95]\n\t"
        "ds_read_b64_tr_b16 v[118:119], v121 offset:40384\n\t"
        "v_mfma_f32_16x16x32_bf16 v[64:67], v[172:175], v[100:103], v[64:67]\n\t"
        "ds_read_b64_tr_b16 v[96:97], v121 offset:37344\n\t"
        "v_mfma_f32_16x16x32_bf16 v[72:75], v[176:179], v[100:103], v[72:75]\n\t"
        "ds_read_b64_tr_b16 v[98:99], v121 offset:40416\n\t"
        "s_waitcnt lgkmcnt(6)\n\t"
        "v_mfma_f32_16x16x32_bf16 v[68:71], v[172:175], v[104:107], v[68:71]\n\t"
        "ds_read_b64_tr_b16 v[100:101], v122 offset:37312\n\t"
        "v_mfma_f32_16x16x32_bf16 v[76:79], v[176:179], v[104:107], v[76:79]\n\t"
        "ds_read_b64_tr_b16 v[102:103], v122 offset:40384\n\t"
        "v_mfma_f32_16x16x32_bf16 v[80:83], v[172:175], v[108:111], v[80:83]\n\t"
        "ds_read_b64_tr_b16 v[104:105], v122 offset:37344\n\t"
        "v_mfma_f32_16x16x32_bf16 v[88:91], v[176:179], v[108:111], v[88:91]\n\t"
        "ds_read_b64_tr_b16 v[106:107], v122 offset:40416\n\t"
        "s_waitcnt vmcnt(2) lgkmcnt(6)\n\t"
        "v_mfma_f32_16x16x32_bf16 v[84:87], v[172:175], v[112:115], v[84:87]\n\t"
        "ds_read_b64_tr_b16 v[108:109], v121 offset:49664\n\t"
        "v_mfma_f32_16x16x32_bf16 v[92:95], v[176:179], v[112:115], v[92:95]\n\t"
        "ds_read_b64_tr_b16 v[110:111], v121 offset:52736\n\t"
        "v_mfma_f32_16x16x32_bf16 v[64:67], v[180:183], v[116:119], v[64:67]\n\t"
        "ds_read_b64_tr_b16 v[112:113], v121 offset:49696\n\t"
        "v_mfma_f32_16x16x32_bf16 v[72:75], v[184:187], v[116:119], v[72:75]\n\t"
        "ds_read_b64_tr_b16 v[114:115], v121 offset:52768\n\t"
        "s_waitcnt lgkmcnt(6)\n\t"
        "v_mfma_f32_16x16x32_bf16 v[68:71], v[180:183], v[96:99], v[68:71]\n\t"
        "ds_read_b64_tr_b16 v[116:117], v122 offset:49664\n\t"
        "v_mfma_f32_16x16x32_bf16 v[76:79], v[184:187], v[96:99], v[76:79]\n\t"
        "ds_read_b64_tr_b16 v[118:119], v122 offset:52736\n\t"
        "v_mfma_f32_16x16x32_bf16 v[80:83], v[180:183], v[100:103], v[80:83]\n\t"
        "ds_read_b64_tr_b16 v[96:97], v122 offset:49696\n\t"
        "v_mfma_f32_16x16x32_bf16 v[88:91], v[184:187], v[100:103], v[88:91]\n\t"
        "ds_read_b64_tr_b16 v[98:99], v122 offset:52768\n\t"
        "s_waitcnt vmcnt(0) lgkmcnt(6)\n\t"
        "v_mfma_f32_16x16x32_bf16 v[84:87], v[180:183], v[104:107], v[84:87]\n\t"
        "ds_read_b64_tr_b16 v[100:101], v123 offset:160\n\t"
        "v_mfma_f32_16x16x32_bf16 v[92:95], v[184:187], v[104:107], v[92:95]\n\t"
        "ds_read_b64_tr_b16 v[102:103], v123 offset:3232\n\t"
        "v_mfma_f32_16x16x32_bf16 v[64:67], v[188:191], v[108:111], v[64:67]\n\t"
        "ds_read_b64_tr_b16 v[104:105], v124 offset:128\n\t"
        "v_mfma_f32_16x16x32_bf16 v[72:75], v[192:195], v[108:111], v[72:75]\n\t"
        "ds_read_b64_tr_b16 v[106:107], v124 offset:3200\n\t"
        "s_waitcnt lgkmcnt(6)\n\t"
        "v_mfma_f32_16x16x32_bf16 v[68:71], v[188:191], v[112:115], v[68:71]\n\t"
        "ds_read_b64_tr_b16 v[108:109], v124 offset:160\n\t"
        "v_mfma_f32_16x16x32_bf16 v[76:79], v[192:195], v[112:115], v[76:79]\n\t"
        "ds_read_b64_tr_b16 v[110:111], v124 offset:3232\n\t"
        "v_mfma_f32_16x16x32_bf16 v[80:83], v[188:191], v[116:119], v[80:83]\n\t"
        "ds_read_b64_tr_b16 v[112:113], v121 offset:128\n\t"
        "v_mfma_f32_16x16x32_bf16 v[88:91], v[192:195], v[116:119], v[88:91]\n\t"
        "ds_read_b64_tr_b16 v[114:115], v121 offset:3200\n\t"
        "s_waitcnt lgkmcnt(6)\n\t"
        "v_mfma_f32_16x16x32_bf16 v[84:87], v[188:191], v[96:99], v[84:87]\n\t"
        "ds_read_b64_tr_b16 v[116:117], v121 offset:160\n\t"
        "v_mfma_f32_16x16x32_bf16 v[92:95], v[192:195], v[96:99], v[92:95]\n\t"
        "ds_read_b64_tr_b16 v[118:119], v121 offset:3232\n\t"
        "v_mfma_f32_16x16x32_bf16 v[36:39], v[100:103], v[148:151], v[36:39]\n\t"
        "ds_read_b64_tr_b16 v[96:97], v122 offset:128\n\t"
        "v_mfma_f32_16x16x32_bf16 v[44:47], v[100:103], v[152:155], v[44:47]\n\t"
        "ds_read_b64_tr_b16 v[98:99], v122 offset:3200\n\t"
        "s_waitcnt lgkmcnt(6)\n\t"
        "v_mfma_f32_16x16x32_bf16 v[48:51], v[104:107], v[148:151], v[48:51]\n\t"
        "ds_read_b64_tr_b16 v[100:101], v122 offset:160\n\t"
        "v_mul_f32 v64, v196, v64\n\t"
        "v_mul_f32 v65, v197, v65\n\t"
        "v_mfma_f32_16x16x32_bf16 v[56:59], v[104:107], v[152:155], v[56:59]\n\t"
        "ds_read_b64_tr_b16 v[102:103], v122 offset:3232\n\t"
        "v_mul_f32 v66, v198, v66\n\t"
        "v_mul_f32 v67, v199, v67\n\t"
        "v_mfma_f32_16x16x32_bf16 v[52:55], v[108:111], v[148:151], v[52:55]\n\t"
        "ds_read_b64_tr_b16 v[104:105], v121 offset:12480\n\t"
        "v_mul_f32 v68, v196, v68\n\t"
        "v_mul_f32 v69, v197, v69\n\t"
        "v_mul_f32 v70, v198, v70\n\t"
        "v_mfma_f32_16x16x32_bf16 v[60:63], v[108:111], v[152:155], v[60:63]\n\t"
        "ds_read_b64_tr_b16 v[106:107], v121 offset:15552\n\t"
        "v_mul_f32 v71, v199, v71\n\t"
        "v_cvt_pk_bf16_f32 v64, v64, v65\n\t"
        "s_waitcnt lgkmcnt(6)\n\t"
        "v_mfma_f32_16x16x32_bf16 v[32:35], v[112:115], v[156:159], v[32:35]\n\t"
        "ds_read_b64_tr_b16 v[108:109], v121 offset:12512\n\t"
        "v_cvt_pk_bf16_f32 v65, v66, v67\n\t"
        "v_cvt_pk_bf16_f32 v66, v68, v69\n\t"
        "v_mfma_f32_16x16x32_bf16 v[40:43], v[112:115], v[160:163], v[40:43]\n\t"
        "ds_read_b64_tr_b16 v[110:111], v121 offset:15584\n\t"
        "v_cvt_pk_bf16_f32 v67, v70, v71\n\t"
        "v_mul_f32 v72, v200, v72\n\t"
        "v_mfma_f32_16x16x32_bf16 v[36:39], v[116:119], v[156:159], v[36:39]\n\t"
        "ds_read_b64_tr_b16 v[112:113], v122 offset:12480\n\t"
        "v_mul_f32 v73, v201, v73\n\t"
        "v_mul_f32 v74, v202, v74\n\t"
        "v_mul_f32 v75, v203, v75\n\t"
        "v_mfma_f32_16x16x32_bf16 v[44:47], v[116:119], v[160:163], v[44:47]\n\t"
        "ds_read_b64_tr_b16 v[114:115], v122 offset:15552\n\t"
        "v_mul_f32 v76, v200, v76\n\t"
        "v_mul_f32 v77, v201, v77\n\t"
        "s_waitcnt lgkmcnt(6)\n\t"
        "v_mfma_f32_16x16x32_bf16 v[48:51], v[96:99], v[156:159], v[48:51]\n\t"
        "ds_read_b64_tr_b16 v[116:117], v122 offset:12512\n\t"
        "v_mul_f32 v78, v202, v78\n\t"
        "v_mul_f32 v79, v203, v79\n\t"
        "v_mfma_f32_16x16x32_bf16 v[56:59], v[96:99], v[160:163], v[56:59]\n\t"
        "ds_read_b64_tr_b16 v[118:119], v122 offset:15584\n\t"
        "v_cvt_pk_bf16_f32 v68, v72, v73\n\t"
        "v_cvt_pk_bf16_f32 v69, v74, v75\n\t"
        "v_mfma_f32_16x16x32_bf16 v[52:55], v[100:103], v[156:159], v[52:55]\n\t"
        "ds_read_b64_tr_b16 v[96:97], v121 offset:24832\n\t"
        "v_cvt_pk_bf16_f32 v70, v76, v77\n\t"
        "v_cvt_pk_bf16_f32 v71, v78, v79\n\t"
        "v_permlane16_swap_b32 v64, v66\n\t"
        "v_mfma_f32_16x16x32_bf16 v[60:63], v[100:103], v[160:163], v[60:63]\n\t"
        "ds_read_b64_tr_b16 v[98:99], v121 offset:27904\n\t"
        "v_permlane16_swap_b32 v65, v67\n\t"
        "v_permlane16_swap_b32 v68, v70\n\t"
        "s_waitcnt lgkmcnt(6)\n\t"
        "v_mfma_f32_16x16x32_bf16 v[32:35], v[104:107], v[164:167], v[32:35]\n\t"
        "ds_read_b64_tr_b16 v[100:101], v121 offset:24864\n\t"
        "v_permlane16_swap_b32 v69, v71\n\t"
        "buffer_store_dwordx4 v[64:67], v213, s[40:43], 0 offen offset:2048 sc1\n\t"
        "v_mfma_f32_16x16x32_bf16 v[40:43], v[104:107], v[168:171], v[40:43]\n\t"
        "ds_read_b64_tr_b16 v[102:103], v121 offset:27936\n\t"
        "buffer_store_dwordx4 v[68:71], v213, s[40:43], 0 offen offset:3072 sc1\n\t"
        "v_mul_f32 v80, v196, v80\n\t"
        "v_mfma_f32_16x16x32_bf16 v[36:39], v[108:111], v[164:167], v[36:39]\n\t"
        "ds_read_b64_tr_b16 v[104:105], v122 offset:24832\n\t"
        "v_mul_f32 v81, v197, v81\n\t"
        "v_mul_f32 v82, v198, v82\n\t"
        "v_mul_f32 v83, v199, v83\n\t"
        "v_mfma_f32_16x16x32_bf16 v[44:47], v[108:111], v[168:171], v[44:47]\n\t"
        "ds_read_b64_tr_b16 v[106:107], v122 offset:27904\n\t"
        "v_mul_f32 v84, v196, v84\n\t"
        "v_mul_f32 v85, v197, v85\n\t"
        "s_waitcnt lgkmcnt(6)\n\t"
        "v_mfma_f32_16x16x32_bf16 v[48:51], v[112:115], v[164:167], v[48:51]\n\t"
        "ds_read_b64_tr_b16 v[108:109], v122 offset:24864\n\t"
        "v_mul_f32 v86, v198, v86\n\t"
        "v_mul_f32 v87, v199, v87\n\t"
        "v_mfma_f32_16x16x32_bf16 v[56:59], v[112:115], v[168:171], v[56:59]\n\t"
        "ds_read_b64_tr_b16 v[110:111], v122 offset:27936\n\t"
        "v_cvt_pk_bf16_f32 v80, v80, v81\n\t"
        "v_cvt_pk_bf16_f32 v81, v82, v83\n\t"
        "v_mfma_f32_16x16x32_bf16 v[52:55], v[116:119], v[164:167], v[52:55]\n\t"
        "ds_read_b64_tr_b16 v[112:113], v121 offset:37184\n\t"
        "v_cvt_pk_bf16_f32 v82, v84, v85\n\t"
        "v_cvt_pk_bf16_f32 v83, v86, v87\n\t"
        "v_mul_f32 v88, v200, v88\n\t"
        "v_mfma_f32_16x16x32_bf16 v[60:63], v[116:119], v[168:171], v[60:63]\n\t"
        "ds_read_b64_tr_b16 v[114:115], v121 offset:40256\n\t"
        "v_mul_f32 v89, v201, v89\n\t"
        "v_mul_f32 v90, v202, v90\n\t"
        "s_waitcnt lgkmcnt(6)\n\t"
        "v_mfma_f32_16x16x32_bf16 v[32:35], v[96:99], v[172:175], v[32:35]\n\t"
        "ds_read_b64_tr_b16 v[116:117], v121 offset:37216\n\t"
        "v_mul_f32 v91, v203, v91\n\t"
        "v_mul_f32 v92, v200, v92\n\t"
        "v_mfma_f32_16x16x32_bf16 v[40:43], v[96:99], v[176:179], v[40:43]\n\t"
        "ds_read_b64_tr_b16 v[118:119], v121 offset:40288\n\t"
        "v_mul_f32 v93, v201, v93\n\t"
        "v_mul_f32 v94, v202, v94\n\t"
        "v_mfma_f32_16x16x32_bf16 v[36:39], v[100:103], v[172:175], v[36:39]\n\t"
        "ds_read_b64_tr_b16 v[96:97], v122 offset:37184\n\t"
        "v_mul_f32 v95, v203, v95\n\t"
        "v_cvt_pk_bf16_f32 v84, v88, v89\n\t"
        "v_cvt_pk_bf16_f32 v85, v90, v91\n\t"
        "v_mfma_f32_16x16x32_bf16 v[44:47], v[100:103], v[176:179], v[44:47]\n\t"
        "ds_read_b64_tr_b16 v[98:99], v122 offset:40256\n\t"
        "v_cvt_pk_bf16_f32 v86, v92, v93\n\t"
        "v_cvt_pk_bf16_f32 v87, v94, v95\n\t"
        "s_waitcnt lgkmcnt(6)\n\t"
        "v_mfma_f32_16x16x32_bf16 v[48:51], v[104:107], v[172:175], v[48:51]\n\t"
        "ds_read_b64_tr_b16 v[100:101], v122 offset:37216\n\t"
        "v_permlane16_swap_b32 v80, v82\n\t"
        "v_permlane16_swap_b32 v81, v83\n\t"
        "v_mfma_f32_16x16x32_bf16 v[56:59], v[104:107], v[176:179], v[56:59]\n\t"
        "ds_read_b64_tr_b16 v[102:103], v122 offset:40288\n\t"
        "v_permlane16_swap_b32 v84, v86\n\t"
        "v_permlane16_swap_b32 v85, v87\n\t"
        "v_mfma_f32_16x16x32_bf16 v[52:55], v[108:111], v[172:175], v[52:55]\n\t"
        "ds_read_b64_tr_b16 v[104:105], v121 offset:49536\n\t"
        "buffer_store_dwordx4 v[80:83], v214, s[40:43], 0 offen offset:2048 sc1\n\t"
        "buffer_store_dwordx4 v[84:87], v214, s[40:43], 0 offen offset:3072 sc1\n\t"
        "ds_write_b128 v212, v[80:83] offset:2048\n\t"
        "v_mfma_f32_16x16x32_bf16 v[60:63], v[108:111], v[176:179], v[60:63]\n\t"
        "ds_read_b64_tr_b16 v[106:107], v121 offset:52608\n\t"
        "ds_write_b128 v212, v[84:87] offset:3072\n\t"
        "s_waitcnt lgkmcnt(8)\n\t"
        "v_mfma_f32_16x16x32_bf16 v[32:35], v[112:115], v[180:183], v[32:35]\n\t"
        "ds_read_b64_tr_b16 v[108:109], v121 offset:49568\n\t"
        "v_mfma_f32_16x16x32_bf16 v[40:43], v[112:115], v[184:187], v[40:43]\n\t"
        "ds_read_b64_tr_b16 v[110:111], v121 offset:52640\n\t"
        "v_mfma_f32_16x16x32_bf16 v[36:39], v[116:119], v[180:183], v[36:39]\n\t"
        "ds_read_b64_tr_b16 v[112:113], v122 offset:49536\n\t"
        "v_mfma_f32_16x16x32_bf16 v[44:47], v[116:119], v[184:187], v[44:47]\n\t"
        "ds_read_b64_tr_b16 v[114:115], v122 offset:52608\n\t"
        "s_waitcnt lgkmcnt(8)\n\t"
        "v_mfma_f32_16x16x32_bf16 v[48:51], v[96:99], v[180:183], v[48:51]\n\t"
        "ds_read_b64_tr_b16 v[116:117], v122 offset:49568\n\t"
        "v_mfma_f32_16x16x32_bf16 v[56:59], v[96:99], v[184:187], v[56:59]\n\t"
        "ds_read_b64_tr_b16 v[118:119], v122 offset:52640\n\t"
        "v_mfma_f32_16x16x32_bf16 v[52:55], v[100:103], v[180:183], v[52:55]\n\t"
        "ds_read_b64_tr_b16 v[96:97], v123 offset:0\n\t"
        "v_mfma_f32_16x16x32_bf16 v[60:63], v[100:103], v[184:187], v[60:63]\n\t"
        "ds_read_b64_tr_b16 v[98:99], v123 offset:3072\n\t"
        "s_waitcnt lgkmcnt(6)\n\t"
        "v_mfma_f32_16x16x32_bf16 v[32:35], v[104:107], v[188:191], v[32:35]\n\t"
        "ds_read_b64_tr_b16 v[100:101], v123 offset:32\n\t"
        "v_mfma_f32_16x16x32_bf16 v[40:43], v[104:107], v[192:195], v[40:43]\n\t"
        "ds_read_b64_tr_b16 v[102:103], v123 offset:3104\n\t"
        "v_mfma_f32_16x16x32_bf16 v[36:39], v[108:111], v[188:191], v[36:39]\n\t"
        "ds_read_b64_tr_b16 v[104:105], v124 offset:0\n\t"
        "v_mfma_f32_16x16x32_bf16 v[44:47], v[108:111], v[192:195], v[44:47]\n\t"
        "ds_read_b64_tr_b16 v[106:107], v124 offset:3072\n\t"
        "s_waitcnt lgkmcnt(6)\n\t"
        "v_mfma_f32_16x16x32_bf16 v[48:51], v[112:115], v[188:191], v[48:51]\n\t"
        "ds_read_b64_tr_b16 v[108:109], v124 offset:32\n\t"
        "v_mfma_f32_16x16x32_bf16 v[56:59], v[112:115], v[192:195], v[56:59]\n\t"
        "ds_read_b64_tr_b16 v[110:111], v124 offset:3104\n\t"
        "v_mfma_f32_16x16x32_bf16 v[52:55], v[116:119], v[188:191], v[52:55]\n\t"
        "ds_read_b64_tr_b16 v[112:113], v121 offset:0\n\t"
        "v_mfma_f32_16x16x32_bf16 v[60:63], v[116:119], v[192:195], v[60:63]\n\t"
        "ds_read_b64_tr_b16 v[114:115], v121 offset:3072\n\t"
        "s_waitcnt lgkmcnt(6)\n\t"
        "v_mfma_f32_16x16x32_bf16 v[0:3], v[96:99], v[148:151], v[0:3]\n\t"
        "ds_read_b64_tr_b16 v[116:117], v121 offset:32\n\t"
        "v_mfma_f32_16x16x32_bf16 v[8:11], v[96:99], v[152:155], v[8:11]\n\t"
        "ds_read_b64_tr_b16 v[118:119], v121 offset:3104\n\t"
        "v_mfma_f32_16x16x32_bf16 v[4:7], v[100:103], v[148:151], v[4:7]\n\t"
        "ds_read_b64_tr_b16 v[96:97], v122 offset:0\n\t"
        "v_mul_f32 v32, v210, v32\n\t"
        "v_mul_f32 v33, v210, v33\n\t"
        "v_mul_f32 v34, v210, v34\n\t"
        "v_mfma_f32_16x16x32_bf16 v[12:15], v[100:103], v[152:155], v[12:15]\n\t"
        "ds_read_b64_tr_b16 v[98:99], v122 offset:3072\n\t"
        "v_mul_f32 v35, v210, v35\n\t"
        "v_mul_f32 v36, v210, v36\n\t"
        "s_waitcnt lgkmcnt(6)\n\t"
        "v_mfma_f32_16x16x32_bf16 v[16:19], v[104:107], v[148:151], v[16:19]\n\t"
        "ds_read_b64_tr_b16 v[100:101], v122 offset:32\n\t"
        "v_mul_f32 v37, v210, v37\n\t"
        "v_mul_f32 v38, v210, v38\n\t"
        "v_mfma_f32_16x16x32_bf16 v[24:27], v[104:107], v[152:155], v[24:27]\n\t"
        "ds_read_b64_tr_b16 v[102:103], v122 offset:3104\n\t"
        "v_mul_f32 v39, v210, v39\n\t"
        "v_cvt_pk_bf16_f32 v32, v32, v33\n\t"
        "v_mfma_f32_16x16x32_bf16 v[20:23], v[108:111], v[148:151], v[20:23]\n\t"
        "ds_read_b64_tr_b16 v[104:105], v121 offset:12352\n\t"
        "v_cvt_pk_bf16_f32 v33, v34, v35\n\t"
        "v_cvt_pk_bf16_f32 v34, v36, v37\n\t"
        "v_cvt_pk_bf16_f32 v35, v38, v39\n\t"
        "v_mfma_f32_16x16x32_bf16 v[28:31], v[108:111], v[152:155], v[28:31]\n\t"
        "ds_read_b64_tr_b16 v[106:107], v121 offset:15424\n\t"
        "v_mul_f32 v40, v211, v40\n\t"
        "v_mul_f32 v41, v211, v41\n\t"
        "s_waitcnt lgkmcnt(6)\n\t"
        "v_mfma_f32_16x16x32_bf16 v[0:3], v[112:115], v[156:159], v[0:3]\n\t"
        "ds_read_b64_tr_b16 v[108:109], v121 offset:12384\n\t"
        "v_mul_f32 v42, v211, v42\n\t"
        "v_mul_f32 v43, v211, v43\n\t"
        "v_mfma_f32_16x16x32_bf16 v[8:11], v[112:115], v[160:163], v[8:11]\n\t"
        "ds_read_b64_tr_b16 v[110:111], v121 offset:15456\n\t"
        "v_mul_f32 v44, v211, v44\n\t"
        "v_mul_f32 v45, v211, v45\n\t"
        "v_mfma_f32_16x16x32_bf16 v[4:7], v[116:119], v[156:159], v[4:7]\n\t"
        "ds_read_b64_tr_b16 v[112:113], v122 offset:12352\n\t"
        "v_mul_f32 v46, v211, v46\n\t"
        "v_mul_f32 v47, v211, v47\n\t"
        "v_cvt_pk_bf16_f32 v36, v40, v41\n\t"
        "v_mfma_f32_16x16x32_bf16 v[12:15], v[116:119], v[160:163], v[12:15]\n\t"
        "ds_read_b64_tr_b16 v[114:115], v122 offset:15424\n\t"
        "v_cvt_pk_bf16_f32 v37, v42, v43\n\t"
        "v_cvt_pk_bf16_f32 v38, v44, v45\n\t"
        "s_waitcnt lgkmcnt(6)\n\t"
        "v_mfma_f32_16x16x32_bf16 v[16:19], v[96:99], v[156:159], v[16:19]\n\t"
        "ds_read_b64_tr_b16 v[116:117], v122 offset:12384\n\t"
        "v_cvt_pk_bf16_f32 v39, v46, v47\n\t"
        "v_permlane16_swap_b32 v32, v36\n\t"
        "v_mfma_f32_16x16x32_bf16 v[24:27], v[96:99], v[160:163], v[24:27]\n\t"
        "ds_read_b64_tr_b16 v[118:119], v122 offset:15456\n\t"
        "v_permlane16_swap_b32 v33, v37\n\t"
        "v_permlane16_swap_b32 v34, v38\n\t"
        "v_mfma_f32_16x16x32_bf16 v[20:23], v[100:103], v[156:159], v[20:23]\n\t"
        "ds_read_b64_tr_b16 v[96:97], v121 offset:24704\n\t"
        "v_permlane16_swap_b32 v35, v39\n\t"
        "buffer_store_dwordx4 v[32:35], v213, s[40:43], 0 offen offset:0 sc1\n\t"
        "buffer_store_dwordx4 v[36:39], v213, s[40:43], 0 offen offset:1024 sc1\n\t"
        "v_mfma_f32_16x16x32_bf16 v[28:31], v[100:103], v[160:163], v[28:31]\n\t"
        "ds_read_b64_tr_b16 v[98:99], v121 offset:27776\n\t"
        "v_mul_f32 v48, v210, v48\n\t"
        "v_mul_f32 v49, v210, v49\n\t"
        "s_waitcnt lgkmcnt(6)\n\t"
        "v_mfma_f32_16x16x32_bf16 v[0:3], v[104:107], v[164:167], v[0:3]\n\t"
        "ds_read_b64_tr_b16 v[100:101], v121 offset:24736\n\t"
        "v_mul_f32 v50, v210, v50\n\t"
        "v_mul_f32 v51, v210, v51\n\t"
        "v_mfma_f32_16x16x32_bf16 v[8:11], v[104:107], v[168:171], v[8:11]\n\t"
        "ds_read_b64_tr_b16 v[102:103], v121 offset:27808\n\t"
        "v_mul_f32 v52, v210, v52\n\t"
        "v_mul_f32 v53, v210, v53\n\t"
        "v_mfma_f32_16x16x32_bf16 v[4:7], v[108:111], v[164:167], v[4:7]\n\t"
        "ds_read_b64_tr_b16 v[104:105], v122 offset:24704\n\t"
        "v_mul_f32 v54, v210, v54\n\t"
        "v_mul_f32 v55, v210, v55\n\t"
        "v_cvt_pk_bf16_f32 v48, v48, v49\n\t"
        "v_mfma_f32_16x16x32_bf16 v[12:15], v[108:111], v[168:171], v[12:15]\n\t"
        "ds_read_b64_tr_b16 v[106:107], v122 offset:27776\n\t"
        "v_cvt_pk_bf16_f32 v49, v50, v51\n\t"
        "v_cvt_pk_bf16_f32 v50, v52, v53\n\t"
        "s_waitcnt lgkmcnt(6)\n\t"
        "v_mfma_f32_16x16x32_bf16 v[16:19], v[112:115], v[164:167], v[16:19]\n\t"
        "ds_read_b64_tr_b16 v[108:109], v122 offset:24736\n\t"
        "v_cvt_pk_bf16_f32 v51, v54, v55\n\t"
        "v_mul_f32 v56, v211, v56\n\t"
        "v_mfma_f32_16x16x32_bf16 v[24:27], v[112:115], v[168:171], v[24:27]\n\t"
        "ds_read_b64_tr_b16 v[110:111], v122 offset:27808\n\t"
        "v_mul_f32 v57, v211, v57\n\t"
        "v_mul_f32 v58, v211, v58\n\t"
        "v_mfma_f32_16x16x32_bf16 v[20:23], v[116:119], v[164:167], v[20:23]\n\t"
        "ds_read_b64_tr_b16 v[112:113], v121 offset:37056\n\t"
        "v_mul_f32 v59, v211, v59\n\t"
        "v_mul_f32 v60, v211, v60\n\t"
        "v_mul_f32 v61, v211, v61\n\t"
        "v_mfma_f32_16x16x32_bf16 v[28:31], v[116:119], v[168:171], v[28:31]\n\t"
        "ds_read_b64_tr_b16 v[114:115], v121 offset:40128\n\t"
        "v_mul_f32 v62, v211, v62\n\t"
        "v_mul_f32 v63, v211, v63\n\t"
        "s_waitcnt lgkmcnt(6)\n\t"
        "v_mfma_f32_16x16x32_bf16 v[0:3], v[96:99], v[172:175], v[0:3]\n\t"
        "ds_read_b64_tr_b16 v[116:117], v121 offset:37088\n\t"
        "v_cvt_pk_bf16_f32 v52, v56, v57\n\t"
        "v_cvt_pk_bf16_f32 v53, v58, v59\n\t"
        "v_mfma_f32_16x16x32_bf16 v[8:11], v[96:99], v[176:179], v[8:11]\n\t"
        "ds_read_b64_tr_b16 v[118:119], v121 offset:40160\n\t"
        "v_cvt_pk_bf16_f32 v54, v60, v61\n\t"
        "v_cvt_pk_bf16_f32 v55, v62, v63\n\t"
        "v_mfma_f32_16x16x32_bf16 v[4:7], v[100:103], v[172:175], v[4:7]\n\t"
        "ds_read_b64_tr_b16 v[96:97], v122 offset:37056\n\t"
        "v_permlane16_swap_b32 v48, v52\n\t"
        "v_permlane16_swap_b32 v49, v53\n\t"
        "v_permlane16_swap_b32 v50, v54\n\t"
        "v_mfma_f32_16x16x32_bf16 v[12:15], v[100:103], v[176:179], v[12:15]\n\t"
        "ds_read_b64_tr_b16 v[98:99], v122 offset:40128\n\t"
        "v_permlane16_swap_b32 v51, v55\n\t"
        "buffer_store_dwordx4 v[48:51], v214, s[40:43], 0 offen offset:0 sc1\n\t"
        "s_waitcnt lgkmcnt(6)\n\t"
        "v_mfma_f32_16x16x32_bf16 v[16:19], v[104:107], v[172:175], v[16:19]\n\t"
        "ds_read_b64_tr_b16 v[100:101], v122 offset:37088\n\t"
        "buffer_store_dwordx4 v[52:55], v214, s[40:43], 0 offen offset:1024 sc1\n\t"
        "ds_write_b128 v212, v[48:51] offset:0\n\t"
        "v_mfma_f32_16x16x32_bf16 v[24:27], v[104:107], v[176:179], v[24:27]\n\t"
        "ds_read_b64_tr_b16 v[102:103], v122 offset:40160\n\t"
        "ds_write_b128 v212, v[52:55] offset:1024\n\t"
        "v_mfma_f32_16x16x32_bf16 v[20:23], v[108:111], v[172:175], v[20:23]\n\t"
        "ds_read_b64_tr_b16 v[104:105], v121 offset:49408\n\t"
        "v_mfma_f32_16x16x32_bf16 v[28:31], v[108:111], v[176:179], v[28:31]\n\t"
        "ds_read_b64_tr_b16 v[106:107], v121 offset:52480\n\t"
        "s_waitcnt lgkmcnt(8)\n\t"
        "v_mfma_f32_16x16x32_bf16 v[0:3], v[112:115], v[180:183], v[0:3]\n\t"
        "ds_read_b64_tr_b16 v[108:109], v121 offset:49440\n\t"
        "v_mfma_f32_16x16x32_bf16 v[8:11], v[112:115], v[184:187], v[8:11]\n\t"
        "ds_read_b64_tr_b16 v[110:111], v121 offset:52512\n\t"
        "v_mfma_f32_16x16x32_bf16 v[4:7], v[116:119], v[180:183], v[4:7]\n\t"
        "ds_read_b64_tr_b16 v[112:113], v122 offset:49408\n\t"
        "v_mfma_f32_16x16x32_bf16 v[12:15], v[116:119], v[184:187], v[12:15]\n\t"
        "ds_read_b64_tr_b16 v[114:115], v122 offset:52480\n\t"
        "s_waitcnt lgkmcnt(7)\n\t"
        "v_mfma_f32_16x16x32_bf16 v[16:19], v[96:99], v[180:183], v[16:19]\n\t"
        "ds_read_b64_tr_b16 v[116:117], v122 offset:49440\n\t"
        "v_mfma_f32_16x16x32_bf16 v[24:27], v[96:99], v[184:187], v[24:27]\n\t"
        "ds_read_b64_tr_b16 v[118:119], v122 offset:52512\n\t"
        "v_mfma_f32_16x16x32_bf16 v[20:23], v[100:103], v[180:183], v[20:23]\n\t"
        "v_mfma_f32_16x16x32_bf16 v[28:31], v[100:103], v[184:187], v[28:31]\n\t"
        "s_waitcnt lgkmcnt(4)\n\t"
        "v_mfma_f32_16x16x32_bf16 v[0:3], v[104:107], v[188:191], v[0:3]\n\t"
        "v_mfma_f32_16x16x32_bf16 v[8:11], v[104:107], v[192:195], v[8:11]\n\t"
        "v_mfma_f32_16x16x32_bf16 v[4:7], v[108:111], v[188:191], v[4:7]\n\t"
        "v_mfma_f32_16x16x32_bf16 v[12:15], v[108:111], v[192:195], v[12:15]\n\t"
        "s_waitcnt lgkmcnt(0)\n\t"
        "v_mfma_f32_16x16x32_bf16 v[16:19], v[112:115], v[188:191], v[16:19]\n\t"
        "v_mfma_f32_16x16x32_bf16 v[24:27], v[112:115], v[192:195], v[24:27]\n\t"
        "v_mfma_f32_16x16x32_bf16 v[20:23], v[116:119], v[188:191], v[20:23]\n\t"
        "v_mfma_f32_16x16x32_bf16 v[28:31], v[116:119], v[192:195], v[28:31]\n\t"
        "s_nop 7\n\t"
        "s_branch 9f\n\t"
        "12:\n\t"
        "s_mov_b32 s48, 0x10000\n\t"
        "8:\n\t"
        "ds_read_b32 v126, v125\n\t"
        "s_sub_u32 s48, s48, 1\n\t"
        "s_waitcnt lgkmcnt(0)\n\t"
        "s_cbranch_scc1 13b\n\t"
        "v_cmp_gt_u32 vcc, v128, v126\n\t"
        "s_cbranch_vccnz 8b\n\t"
        "s_branch 13b\n\t"
        "14:\n\t"
        "s_mov_b32 s48, 0x10000\n\t"
        "8:\n\t"
        "ds_read_b32 v126, v125\n\t"
        "s_sub_u32 s48, s48, 1\n\t"
        "s_waitcnt lgkmcnt(0)\n\t"
        "s_cbranch_scc1 15b\n\t"
        "v_cmp_gt_u32 vcc, v128, v126\n\t"
        "s_cbranch_vccnz 8b\n\t"
        "s_branch 15b\n\t"
        "16:\n\t"
        "s_mov_b32 s48, 0x10000\n\t"
        "8:\n\t"
        "ds_read_b32 v126, v125\n\t"
        "s_sub_u32 s48, s48, 1\n\t"
        "s_waitcnt lgkmcnt(0)\n\t"
        "s_cbranch_scc1 17b\n\t"
        "v_cmp_gt_u32 vcc, v128, v126\n\t"
        "s_cbranch_vccnz 8b\n\t"
        "s_branch 17b\n\t"
        "18:\n\t"
        "s_mov_b32 s48, 0x10000\n\t"
        "8:\n\t"
        "ds_read_b32 v126, v125\n\t"
        "s_sub_u32 s48, s48, 1\n\t"
        "s_waitcnt lgkmcnt(0)\n\t"
        "s_cbranch_scc1 19b\n\t"
        "v_cmp_gt_u32 vcc, v128, v126\n\t"
        "s_cbranch_vccnz 8b\n\t"
        "s_branch 19b\n\t"
        "20:\n\t"
        "s_mov_b32 s48, 0x10000\n\t"
        "8:\n\t"
        "ds_read_b32 v126, v125\n\t"
        "s_sub_u32 s48, s48, 1\n\t"
        "s_waitcnt lgkmcnt(0)\n\t"
        "s_cbranch_scc1 21b\n\t"
        "v_cmp_gt_u32 vcc, v128, v126\n\t"
        "s_cbranch_vccnz 8b\n\t"
        "s_branch 21b\n\t"
        "38:\n\t"
        "s_mov_b32 s48, 0x10000\n\t"
        "8:\n\t"
        "ds_read_b32 v126, v125\n\t"
        "s_sub_u32 s48, s48, 1\n\t"
        "s_waitcnt lgkmcnt(0)\n\t"
        "s_cbranch_scc1 39b\n\t"
        "v_cmp_gt_u32 vcc, v128, v126\n\t"
        "s_cbranch_vccnz 8b\n\t"
        "s_branch 39b\n\t"
        "9:\n\t"
        : "+{v[0:15]}"(acc[0]), "+{v[16:31]}"(acc[1]), "+{v[32:47]}"(acc[2]), "+{v[48:63]}"(acc[3]), "+{v[64:79]}"(acc[4]), "+{v[80:95]}"(acc[5]),
          "+{v[96:111]}"(stA), "+{v[112:127]}"(stB), "+{v[128:131]}"(stC), "+{v[148:163]}"(xr1), "+{v[164:179]}"(xr2), "+{v[180:195]}"(xr3),
          "+{s44}"(soff), "+{s46}"(slot), "+{v[140:147]}"(xrb)
        : "{s[40:43]}"(srd), KSKIP, "{v213}"(ep[0]), "{v214}"(ep[1]), "{v215}"(ep[2]), "{v212}"(ep[3]), "{v[210:211]}"(al2) : "v196", "v197", "v198", "v199", "v200", "v201", "v202", "v203", "v132", "v133", "v134", "v135", "v136", "v137", "v138", "v139", "s45", "s47", "s48", "vcc", "scc", "memory");
      STAMPV(4 + 16 * L);
      PHASE_VARS;
      u32x4 kv[4][2];
#pragma unroll
      for (int j = 0; j < 4; ++j)
#pragma unroll
        for (int e = 0; e < 8; ++e) kv[j][e >> 2][e & 3] = __float_as_uint(acc[2 + j][e]);
      u32x4 qo[2][2];
      auto drained = [&]() { asm volatile("s_waitcnt vmcnt(0)\n\tds_write_b32 %0, %1" :: "v"(lbase + L_W + 80 + 4 * wid), "v"(2 * L + 1) : "memory"); };
      if (L == 0) {
#pragma unroll
        for (int e = 0; e < 16; ++e) {
          if (e == 8) { SBAR(); drained(); SBAR(); }
          const unsigned w = pack2(acc[e >> 3][2 * (e & 7)], acc[e >> 3][2 * (e & 7) + 1]);
          qo[e >> 3][(e >> 2) & 1][e & 3] = pack2(lo16(w) * QS, hi16(w) * QS);
        }
        row_swap(qo[0][0], qo[0][1]); row_swap(qo[1][0], qo[1][1]);
      } else {
        lds_wait_ge<0>(lbase + L_W + 4 + 4 * (lane & 1), 1);
        const unsigned idxv = words[3];
        const f32x2 aq = al2 * QS;
#pragma unroll
        for (int e = 0; e < 16; ++e) qo[e >> 3][(e >> 2) & 1][e & 3] = pack2(acc[e >> 3][2 * (e & 7)] * aq[(e >> 2) & 1], acc[e >> 3][2 * (e & 7) + 1] * aq[(e >> 2) & 1]);
        row_swap(qo[0][0], qo[0][1]); row_swap(qo[1][0], qo[1][1]);
        SBAR();
        const int idx = (int)__builtin_amdgcn_readfirstlane(idxv);
        if ((idx >> 5) == tile) {
          const int rel = (idx & 31) - 8 * hi, dfo = ((r32 >> 2) & 1) * 16 + ((r32 >> 3) & 1) * 8 + (r32 >> 4) * 4 + (r32 & 3);
          unsigned pv[2];
#pragma unroll
          for (int j = 0; j < 2; ++j) pv[j] = reinterpret_cast<const u16*>(smem + L_PATCH)[128 + (j ^ hh) * 32 + dfo] * 0x10001u;
          DRAIN();
          if (r32 == (idx & 31)) {
#pragma unroll
            for (int i = 0; i < 4; ++i) {
              const char* p = smem + L_PATCH + ((i >> 1) ^ hh) * 64 + (i & 1) * 32 + hi * 16;
              qo[i >> 1][i & 1] = *reinterpret_cast<const u32x4*>(p);
              kv[i >> 1][i & 1] = *reinterpret_cast<const u32x4*>(p + 128);
              x1p[i] = *reinterpret_cast<const u32x4*>(smem + L_X2P + (i * 2 + hi) * 16);
            }
            reinterpret_cast<float*>(smem + L_AB)[256 + wid * 64 + lane] = hi ? 1.0f : 0.0f;
#pragma unroll
            for (int i = 0; i < 4; ++i) stg(rs, ep[i >> 1] + (i & 1) * 1024, kv[i >> 1][i & 1]);
          }
#pragma unroll
          for (int i = 0; i < 4; ++i) {
#pragma unroll
            for (int e = 0; e < 4; ++e) {
              const int t0 = 16 * (i & 1) + 2 * e;
              const unsigned mask = rel == t0 ? 0xffffu : rel == t0 + 1 ? 0xffff0000u : 0u;
              kv[2 + (i >> 1)][i & 1][e] = (kv[2 + (i >> 1)][i & 1][e] & ~mask) | (pv[i >> 1] & mask);
            }
            stg(rs, (rel >> 3) == 2 * (i & 1) ? ep[i >> 1] + 2048 + (i & 1) * 1024 : OOB, kv[2 + (i >> 1)][i & 1]);
          }
#pragma unroll
          for (int i = 0; i < 4; ++i) *reinterpret_cast<u32x4*>(smem + L_KX + wid * 4096 + i * 1024 + lane * 16) = kv[(i & 2) + 1][i & 1];
        }
        if (8 < 16) drained();
      }
      SBAR();
#pragma unroll
      for (int j = 0; j < 2; ++j) {
        char* p = smem + L_QX + ((j ^ hh) * 4 + wid) * 2048 + lane * 16;
        *reinterpret_cast<u32x4*>(p) = qo[j][0]; *reinterpret_cast<u32x4*>(p + 1024) = qo[j][1];
      }
      if (8 >= 16) drained();
      STAMPV(41 + 4 * L);
      BAR_LGKM();
      STAMPV(43 + 4 * L);
      kva = cat4(kv[0][0], kv[0][1], kv[2][0], kv[2][1]);
      const u32x4* pk = reinterpret_cast<const u32x4*>(smem + L_KX + (wid ^ 1) * 4096 + lane * 16);
#pragma unroll
      for (int u = 0; u < 4; ++u) {
        const u32x4* p = reinterpret_cast<const u32x4*>(smem + L_QX + (hh * 4 + ((2 * kh + 2 + u) & 3)) * 2048 + lane * 16);
        q[u] = __builtin_shufflevector(p[0], p[64], 0, 1, 2, 3, 4, 5, 6, 7);
      }
      kvb = cat4(pk[0], pk[64], pk[128], pk[192]);
      SBAR();
      asm volatile(
        "v_mov_b64 v[0:1], 0\n\t"
        "v_mov_b64 v[2:3], 0\n\t"
        "v_mov_b64 v[4:5], 0\n\t"
        "v_mov_b64 v[6:7], 0\n\t"
        "v_mov_b64 v[8:9], 0\n\t"
        "v_mov_b64 v[10:11], 0\n\t"
        "v_mov_b64 v[12:13], 0\n\t"
        "v_mov_b64 v[14:15], 0\n\t"
        "v_mov_b64 v[16:17], 0\n\t"
        "v_mov_b64 v[18:19], 0\n\t"
        "v_mov_b64 v[20:21], 0\n\t"
        "v_mov_b64 v[22:23], 0\n\t"
        "v_mov_b64 v[24:25], 0\n\t"
        "v_mov_b64 v[26:27], 0\n\t"
        "v_mov_b64 v[28:29], 0\n\t"
        "v_mov_b64 v[30:31], 0\n\t"
        "v_mov_b64 v[32:33], 0\n\t"
        "v_mov_b64 v[34:35], 0\n\t"
        "v_mov_b64 v[36:37], 0\n\t"
        "v_mov_b64 v[38:39], 0\n\t"
        "v_mov_b64 v[40:41], 0\n\t"
        "v_mov_b64 v[42:43], 0\n\t"
        "v_mov_b64 v[44:45], 0\n\t"
        "v_mov_b64 v[46:47], 0\n\t"
        "v_mov_b64 v[48:49], 0\n\t"
        "v_mov_b64 v[50:51], 0\n\t"
        "v_mov_b64 v[52:53], 0\n\t"
        "v_mov_b64 v[54:55], 0\n\t"
        "v_mov_b64 v[56:57], 0\n\t"
        "v_mov_b64 v[58:59], 0\n\t"
        "v_mov_b64 v[60:61], 0\n\t"
        "v_mov_b64 v[62:63], 0\n\t"
        "v_mov_b64 v[128:129], 0\n\t"
        "v_mov_b64 v[130:131], 0\n\t"
        "v_mov_b64 v[132:133], 0\n\t"
        "v_mov_b64 v[134:135], 0\n\t"
        "v_mov_b64 v[136:137], 0\n\t"
        "v_mov_b64 v[138:139], 0\n\t"
        "v_mov_b64 v[140:141], 0\n\t"
        "v_mov_b64 v[142:143], 0\n\t"
        : "={v[0:15]}"(oz[0]), "={v[16:31]}"(oz[1]), "={v[32:47]}"(oz[2]), "={v[48:63]}"(oz[3]), "={v[128:143]}"(oz[4]));
    }
    STAMPV(5 + 16 * L);
    {
      u32x4 ones;
      unsigned kvoff;
      u32x8 fv, cv;
      asm volatile("" : "={v[184:191]}"(fv), "={v[216:223]}"(cv));
      { PHASE_VARS;
        const unsigned v = (lane & 3) == 0 ? 0x3F803F80u : 0u; const bool up = (lane & 4) != 0; ones[0] = ones[1] = up ? 0u : v; ones[2] = ones[3] = up ? v : 0u;
        kvoff = OFF_KV + L * (10 * MBY) + (tn * 2 + hh) * HS + kh * 8192 + lane * 16;
        fv[1] = lbase + L_FREE + wid * 256 + lane * 4; fv[2] = 32 * L + 32;
        cv[0] = lbase + L_XCH + wid * 8192 + lane * 16; cv[1] = lbase + L_XCH + (wid ^ 2) * 8192 + lane * 16;
        cv[2] = lbase + L_XCH + 32768 + wid * 512 + lane * 4; cv[3] = lbase + L_XCH + 32768 + (wid ^ 2) * 512 + lane * 4;
        cv[4] = lbase + L_W + 48 + wid * 4; cv[5] = lbase + L_W + 48 + (wid ^ 2) * 4; cv[6] = L + 1;
        cv[7] = ((tile * 32 + r32) * D_ + tn * 64 + hi * 8) * 2; }
      f32x16 o0 = oz[0], o1 = oz[1], o2 = oz[2], o3 = oz[3], lv = oz[4];
      unsigned soff = (tm | 7) * 16384;
      STAMP_DECL;
      asm volatile(
        "s_nop 1\n\t"
        "v_mfma_f32_32x32x16_bf16 v[96:111], v[152:155], v[64:67], 0\n\t"
        "v_mfma_f32_32x32x16_bf16 v[96:111], v[156:159], v[68:71], v[96:111]\n\t"
        "s_mov_b32 s46, 0\n\t"
        "s_mov_b64 s[54:55], s[42:43]\n\t"
        "ds_write_b32 v185, v186\n\t"
        "s_nop 8\n\t"
        "v_exp_f32 v96, v96\n\t"
        "v_exp_f32 v97, v97\n\t"
        "v_exp_f32 v98, v98\n\t"
        "v_exp_f32 v99, v99\n\t"
        "v_exp_f32 v100, v100\n\t"
        "v_exp_f32 v101, v101\n\t"
        "v_exp_f32 v102, v102\n\t"
        "v_mfma_f32_32x32x16_bf16 v[112:127], v[152:155], v[72:75], 0\n\t"
        "v_exp_f32 v103, v103\n\t"
        "v_exp_f32 v104, v104\n\t"
        "v_exp_f32 v105, v105\n\t"
        "v_mfma_f32_32x32x16_bf16 v[112:127], v[156:159], v[76:79], v[112:127]\n\t"
        "v_exp_f32 v106, v106\n\t"
        "v_exp_f32 v107, v107\n\t"
        "v_exp_f32 v108, v108\n\t"
        "v_smfmac_f32_32x32x32_bf16 v[0:15], v[160:163], v[96:103], v148\n\t"
        "v_exp_f32 v109, v109\n\t"
        "v_exp_f32 v110, v110\n\t"
        "v_exp_f32 v111, v111\n\t"
        "v_smfmac_f32_16x16x64_bf16 v[128:131], v[144:147], v[96:103], v148\n\t"
        "v_exp_f32 v112, v112\n\t"
        "v_exp_f32 v113, v113\n\t"
        "v_smfmac_f32_32x32x32_bf16 v[0:15], v[164:167], v[104:111], v148\n\t"
        "v_exp_f32 v114, v114\n\t"
        "v_exp_f32 v115, v115\n\t"
        "v_exp_f32 v116, v116\n\t"
        "v_smfmac_f32_16x16x64_bf16 v[128:131], v[144:147], v[104:111], v148\n\t"
        "v_exp_f32 v117, v117\n\t"
        "v_exp_f32 v118, v118\n\t"
        "v_mfma_f32_32x32x16_bf16 v[96:111], v[152:155], v[80:83], 0\n\t"
        "v_exp_f32 v119, v119\n\t"
        "v_exp_f32 v120, v120\n\t"
        "v_exp_f32 v121, v121\n\t"
        "v_mfma_f32_32x32x16_bf16 v[96:111], v[156:159], v[84:87], v[96:111]\n\t"
        "ds_read_b32 v151, v224\n\t"
        "v_exp_f32 v122, v122\n\t"
        "v_exp_f32 v123, v123\n\t"
        "v_exp_f32 v124, v124\n\t"
        "v_smfmac_f32_32x32x32_bf16 v[16:31], v[160:163], v[112:119], v148\n\t"
        "v_exp_f32 v125, v125\n\t"
        "v_exp_f32 v126, v126\n\t"
        "v_exp_f32 v127, v127\n\t"
        "v_smfmac_f32_16x16x64_bf16 v[132:135], v[144:147], v[112:119], v148\n\t"
        "s_cmp_lg_u32 s46, 0\n\t"
        "s_cbranch_scc1 25f\n\t"
        "s_waitcnt lgkmcnt(0)\n\t"
        "v_cmp_ne_u32 vcc, v222, v151\n\t"
        "s_cbranch_vccz 24f\n\t"
        "25:\n\t"
        "v_exp_f32 v96, v96\n\t"
        "v_exp_f32 v97, v97\n\t"
        "v_smfmac_f32_32x32x32_bf16 v[16:31], v[164:167], v[120:127], v148\n\t"
        "v_exp_f32 v98, v98\n\t"
        "v_exp_f32 v99, v99\n\t"
        "v_exp_f32 v100, v100\n\t"
        "v_smfmac_f32_16x16x64_bf16 v[132:135], v[144:147], v[120:127], v148\n\t"
        "v_exp_f32 v101, v101\n\t"
        "v_exp_f32 v102, v102\n\t"
        "v_mfma_f32_32x32x16_bf16 v[112:127], v[152:155], v[88:91], 0\n\t"
        "v_exp_f32 v103, v103\n\t"
        "v_exp_f32 v104, v104\n\t"
        "v_exp_f32 v105, v105\n\t"
        "v_mfma_f32_32x32x16_bf16 v[112:127], v[156:159], v[92:95], v[112:127]\n\t"
        "ds_read_b32 v151, v224\n\t"
        "v_exp_f32 v106, v106\n\t"
        "v_exp_f32 v107, v107\n\t"
        "v_exp_f32 v108, v108\n\t"
        "v_smfmac_f32_32x32x32_bf16 v[32:47], v[160:163], v[96:103], v148\n\t"
        "v_exp_f32 v109, v109\n\t"
        "v_exp_f32 v110, v110\n\t"
        "v_exp_f32 v111, v111\n\t"
        "v_smfmac_f32_16x16x64_bf16 v[136:139], v[144:147], v[96:103], v148\n\t"
        "s_cmp_lg_u32 s46, 0\n\t"
        "s_cbranch_scc1 27f\n\t"
        "s_waitcnt lgkmcnt(0)\n\t"
        "v_cmp_ne_u32 vcc, v222, v151\n\t"
        "s_cbranch_vccz 26f\n\t"
        "27:\n\t"
        "v_exp_f32 v112, v112\n\t"
        "v_exp_f32 v113, v113\n\t"
        "v_smfmac_f32_32x32x32_bf16 v[32:47], v[164:167], v[104:111], v148\n\t"
        "v_exp_f32 v114, v114\n\t"
        "v_exp_f32 v115, v115\n\t"
        "v_exp_f32 v116, v116\n\t"
        "v_smfmac_f32_16x16x64_bf16 v[136:139], v[144:147], v[104:111], v148\n\t"
        "v_exp_f32 v117, v117\n\t"
        "v_exp_f32 v118, v118\n\t"
        "v_mfma_f32_32x32x16_bf16 v[96:111], v[168:171], v[64:67], 0\n\t"
        "v_exp_f32 v119, v119\n\t"
        "v_exp_f32 v120, v120\n\t"
        "v_exp_f32 v121, v121\n\t"
        "v_mfma_f32_32x32x16_bf16 v[96:111], v[172:175], v[68:71], v[96:111]\n\t"
        "ds_read_b32 v151, v224\n\t"
        "v_exp_f32 v122, v122\n\t"
        "v_exp_f32 v123, v123\n\t"
        "v_exp_f32 v124, v124\n\t"
        "v_smfmac_f32_32x32x32_bf16 v[48:63], v[160:163], v[112:119], v148\n\t"
        "v_exp_f32 v125, v125\n\t"
        "v_exp_f32 v126, v126\n\t"
        "v_exp_f32 v127, v127\n\t"
        "v_smfmac_f32_16x16x64_bf16 v[140:143], v[144:147], v[112:119], v148\n\t"
        "s_cmp_lg_u32 s46, 0\n\t"
        "s_cbranch_scc1 29f\n\t"
        "s_waitcnt lgkmcnt(0)\n\t"
        "v_cmp_ne_u32 vcc, v222, v151\n\t"
        "s_cbranch_vccz 28f\n\t"
        "29:\n\t"
        "v_exp_f32 v96, v96\n\t"
        "v_exp_f32 v97, v97\n\t"
        "v_smfmac_f32_32x32x32_bf16 v[48:63], v[164:167], v[120:127], v148\n\t"
        "v_exp_f32 v98, v98\n\t"
        "v_exp_f32 v99, v99\n\t"
        "v_exp_f32 v100, v100\n\t"
        "v_smfmac_f32_16x16x64_bf16 v[140:143], v[144:147], v[120:127], v148\n\t"
        "v_exp_f32 v101, v101\n\t"
        "v_exp_f32 v102, v102\n\t"
        "v_mfma_f32_32x32x16_bf16 v[112:127], v[168:171], v[72:75], 0\n\t"
        "v_exp_f32 v103, v103\n\t"
        "v_exp_f32 v104, v104\n\t"
        "v_exp_f32 v105, v105\n\t"
        "v_mfma_f32_32x32x16_bf16 v[112:127], v[172:175], v[76:79], v[112:127]\n\t"
        "ds_read_b32 v151, v224\n\t"
        "v_exp_f32 v106, v106\n\t"
        "v_exp_f32 v107, v107\n\t"
        "v_exp_f32 v108, v108\n\t"
        "v_smfmac_f32_32x32x32_bf16 v[0:15], v[176:179], v[96:103], v148\n\t"
        "v_exp_f32 v109, v109\n\t"
        "v_exp_f32 v110, v110\n\t"
        "v_exp_f32 v111, v111\n\t"
        "v_smfmac_f32_16x16x64_bf16 v[128:131], v[144:147], v[96:103], v148\n\t"
        "s_cmp_lg_u32 s46, 0\n\t"
        "s_cbranch_scc1 31f\n\t"
        "s_waitcnt lgkmcnt(0)\n\t"
        "v_cmp_ne_u32 vcc, v222, v151\n\t"
        "s_cbranch_vccz 30f\n\t"
        "31:\n\t"
        "v_exp_f32 v112, v112\n\t"
        "v_exp_f32 v113, v113\n\t"
        "v_smfmac_f32_32x32x32_bf16 v[0:15], v[180:183], v[104:111], v148\n\t"
        "v_exp_f32 v114, v114\n\t"
        "v_exp_f32 v115, v115\n\t"
        "v_exp_f32 v116, v116\n\t"
        "v_smfmac_f32_16x16x64_bf16 v[128:131], v[144:147], v[104:111], v148\n\t"
        "v_exp_f32 v117, v117\n\t"
        "v_exp_f32 v118, v118\n\t"
        "v_mfma_f32_32x32x16_bf16 v[96:111], v[168:171], v[80:83], 0\n\t"
        "v_exp_f32 v119, v119\n\t"
        "v_exp_f32 v120, v120\n\t"
        "v_exp_f32 v121, v121\n\t"
        "v_mfma_f32_32x32x16_bf16 v[96:111], v[172:175], v[84:87], v[96:111]\n\t"
        "ds_read_b32 v151, v224\n\t"
        "v_exp_f32 v122, v122\n\t"
        "v_exp_f32 v123, v123\n\t"
        "v_exp_f32 v124, v124\n\t"
        "v_smfmac_f32_32x32x32_bf16 v[16:31], v[176:179], v[112:119], v148\n\t"
        "v_exp_f32 v125, v125\n\t"
        "v_exp_f32 v126, v126\n\t"
        "v_exp_f32 v127, v127\n\t"
        "v_smfmac_f32_16x16x64_bf16 v[132:135], v[144:147], v[112:119], v148\n\t"
        "s_cmp_lg_u32 s46, 0\n\t"
        "s_cbranch_scc1 33f\n\t"
        "s_waitcnt lgkmcnt(0)\n\t"
        "v_cmp_ne_u32 vcc, v222, v151\n\t"
        "s_cbranch_vccz 32f\n\t"
        "33:\n\t"
        "v_exp_f32 v96, v96\n\t"
        "v_exp_f32 v97, v97\n\t"
        "v_smfmac_f32_32x32x32_bf16 v[16:31], v[180:183], v[120:127], v148\n\t"
        "v_exp_f32 v98, v98\n\t"
        "v_exp_f32 v99, v99\n\t"
        "v_exp_f32 v100, v100\n\t"
        "v_smfmac_f32_16x16x64_bf16 v[132:135], v[144:147], v[120:127], v148\n\t"
        "v_exp_f32 v101, v101\n\t"
        "v_exp_f32 v102, v102\n\t"
        "v_mfma_f32_32x32x16_bf16 v[112:127], v[168:171], v[88:91], 0\n\t"
        "v_exp_f32 v103, v103\n\t"
        "v_exp_f32 v104, v104\n\t"
        "v_exp_f32 v105, v105\n\t"
        "v_mfma_f32_32x32x16_bf16 v[112:127], v[172:175], v[92:95], v[112:127]\n\t"
        "v_exp_f32 v106, v106\n\t"
        "v_exp_f32 v107, v107\n\t"
        "v_exp_f32 v108, v108\n\t"
        "v_smfmac_f32_32x32x32_bf16 v[32:47], v[176:179], v[96:103], v148\n\t"
        "v_exp_f32 v109, v109\n\t"
        "v_exp_f32 v110, v110\n\t"
        "v_exp_f32 v111, v111\n\t"
        "v_smfmac_f32_16x16x64_bf16 v[136:139], v[144:147], v[96:103], v148\n\t"
        "s_cmp_lg_u32 s46, 0\n\t"
        "s_cbranch_scc1 5f\n\t"
        "s_mov_b32 s45, 0x200000\n\t"
        "2:\n\t"
        "ds_read_b32 v151, v224\n\t"
        "s_sub_u32 s45, s45, 1\n\t"
        "s_waitcnt lgkmcnt(0)\n\t"
        "s_cbranch_scc1 3f\n\t"
        "v_cmp_ne_u32 vcc, v222, v151\n\t"
        "s_cbranch_vccnz 2b\n\t"
        "3:\n\t"
        "s_add_u32 s44, s44, 0x4000\n\t"
        "s_and_b32 s44, s44, 0x3ffff\n\t"
        "s_cmp_eq_u32 s44, s49\n\t"
        "s_cselect_b32 s44, s50, s44\n\t"
        "s_add_u32 s52, s40, s44\n\t"
        "s_addc_u32 s53, s41, 0\n\t"
        "buffer_load_dwordx4 v[184:187], v149, s[52:55], 0 offen offset:0 sc1\n\t"
        "buffer_load_dwordx4 v[188:191], v149, s[52:55], 0 offen offset:1024 sc1\n\t"
        "buffer_load_dwordx4 v[192:195], v149, s[52:55], 0 offen offset:2048 sc1\n\t"
        "buffer_load_dwordx4 v[196:199], v149, s[52:55], 0 offen offset:3072 sc1\n\t"
        "buffer_load_dwordx4 v[200:203], v150, s[52:55], 0 offen offset:0 sc1\n\t"
        "buffer_load_dwordx4 v[204:207], v150, s[52:55], 0 offen offset:1024 sc1\n\t"
        "buffer_load_dwordx4 v[208:211], v150, s[52:55], 0 offen offset:2048 sc1\n\t"
        "buffer_load_dwordx4 v[212:215], v150, s[52:55], 0 offen offset:3072 sc1\n\t"
        "5:\n\t"
        "s_add_u32 s44, s44, 0x4000\n\t"
        "s_and_b32 s44, s44, 0x3ffff\n\t"
        "s_cmp_eq_u32 s44, s49\n\t"
        "s_cselect_b32 s44, s50, s44\n\t"
        "s_add_u32 s52, s40, s44\n\t"
        "s_addc_u32 s53, s41, 0\n\t"
        "buffer_load_dwordx4 v[152:155], v149, s[52:55], 0 offen offset:0 sc1\n\t"
        "buffer_load_dwordx4 v[156:159], v149, s[52:55], 0 offen offset:1024 sc1\n\t"
        "buffer_load_dwordx4 v[160:163], v149, s[52:55], 0 offen offset:2048 sc1\n\t"
        "buffer_load_dwordx4 v[164:167], v149, s[52:55], 0 offen offset:3072 sc1\n\t"
        "s_mov_b32 s45, 6\n\t"
        "1:\n\t"
        "v_exp_f32 v112, v112\n\t"
        "v_exp_f32 v113, v113\n\t"
        "v_smfmac_f32_32x32x32_bf16 v[32:47], v[180:183], v[104:111], v148\n\t"
        "v_exp_f32 v114, v114\n\t"
        "v_exp_f32 v115, v115\n\t"
        "v_exp_f32 v116, v116\n\t"
        "v_smfmac_f32_16x16x64_bf16 v[136:139], v[144:147], v[104:111], v148\n\t"
        "s_waitcnt vmcnt(8)\n\t"
        "v_exp_f32 v117, v117\n\t"
        "v_exp_f32 v118, v118\n\t"
        "v_mfma_f32_32x32x16_bf16 v[96:111], v[184:187], v[64:67], 0\n\t"
        "v_exp_f32 v119, v119\n\t"
        "v_exp_f32 v120, v120\n\t"
        "v_exp_f32 v121, v121\n\t"
        "v_mfma_f32_32x32x16_bf16 v[96:111], v[188:191], v[68:71], v[96:111]\n\t"
        "v_exp_f32 v122, v122\n\t"
        "v_exp_f32 v123, v123\n\t"
        "v_exp_f32 v124, v124\n\t"
        "v_smfmac_f32_32x32x32_bf16 v[48:63], v[176:179], v[112:119], v148\n\t"
        "v_exp_f32 v125, v125\n\t"
        "v_exp_f32 v126, v126\n\t"
        "v_exp_f32 v127, v127\n\t"
        "v_smfmac_f32_16x16x64_bf16 v[140:143], v[144:147], v[112:119], v148\n\t"
        "buffer_load_dwordx4 v[168:171], v150, s[52:55], 0 offen offset:0 sc1\n\t"
        "v_exp_f32 v96, v96\n\t"
        "v_exp_f32 v97, v97\n\t"
        "v_smfmac_f32_32x32x32_bf16 v[48:63], v[180:183], v[120:127], v148\n\t"
        "v_exp_f32 v98, v98\n\t"
        "v_exp_f32 v99, v99\n\t"
        "v_exp_f32 v100, v100\n\t"
        "v_smfmac_f32_16x16x64_bf16 v[140:143], v[144:147], v[120:127], v148\n\t"
        "v_exp_f32 v101, v101\n\t"
        "v_exp_f32 v102, v102\n\t"
        "v_mfma_f32_32x32x16_bf16 v[112:127], v[184:187], v[72:75], 0\n\t"
        "v_exp_f32 v103, v103\n\t"
        "v_exp_f32 v104, v104\n\t"
        "v_exp_f32 v105, v105\n\t"
        "v_mfma_f32_32x32x16_bf16 v[112:127], v[188:191], v[76:79], v[112:127]\n\t"
        "v_exp_f32 v106, v106\n\t"
        "v_exp_f32 v107, v107\n\t"
        "v_exp_f32 v108, v108\n\t"
        "v_smfmac_f32_32x32x32_bf16 v[0:15], v[192:195], v[96:103], v148\n\t"
        "v_exp_f32 v109, v109\n\t"
        "v_exp_f32 v110, v110\n\t"
        "v_exp_f32 v111, v111\n\t"
        "v_smfmac_f32_16x16x64_bf16 v[128:131], v[144:147], v[96:103], v148\n\t"
        "buffer_load_dwordx4 v[172:175], v150, s[52:55], 0 offen offset:1024 sc1\n\t"
        "v_exp_f32 v112, v112\n\t"
        "v_exp_f32 v113, v113\n\t"
        "v_smfmac_f32_32x32x32_bf16 v[0:15], v[196:199], v[104:111], v148\n\t"
        "v_exp_f32 v114, v114\n\t"
        "v_exp_f32 v115, v115\n\t"
        "v_exp_f32 v116, v116\n\t"
        "v_smfmac_f32_16x16x64_bf16 v[128:131], v[144:147], v[104:111], v148\n\t"
        "v_exp_f32 v117, v117\n\t"
        "v_exp_f32 v118, v118\n\t"
        "v_mfma_f32_32x32x16_bf16 v[96:111], v[184:187], v[80:83], 0\n\t"
        "v_exp_f32 v119, v119\n\t"
        "v_exp_f32 v120, v120\n\t"
        "v_exp_f32 v121, v121\n\t"
        "v_mfma_f32_32x32x16_bf16 v[96:111], v[188:191], v[84:87], v[96:111]\n\t"
        "v_exp_f32 v122, v122\n\t"
        "v_exp_f32 v123, v123\n\t"
        "v_exp_f32 v124, v124\n\t"
        "v_smfmac_f32_32x32x32_bf16 v[16:31], v[192:195], v[112:119], v148\n\t"
        "v_exp_f32 v125, v125\n\t"
        "v_exp_f32 v126, v126\n\t"
        "v_exp_f32 v127, v127\n\t"
        "v_smfmac_f32_16x16x64_bf16 v[132:135], v[144:147], v[112:119], v148\n\t"
        "buffer_load_dwordx4 v[176:179], v150, s[52:55], 0 offen offset:2048 sc1\n\t"
        "v_exp_f32 v96, v96\n\t"
        "v_exp_f32 v97, v97\n\t"
        "v_smfmac_f32_32x32x32_bf16 v[16:31], v[196:199], v[120:127], v148\n\t"
        "v_exp_f32 v98, v98\n\t"
        "v_exp_f32 v99, v99\n\t"
        "v_exp_f32 v100, v100\n\t"
        "v_smfmac_f32_16x16x64_bf16 v[132:135], v[144:147], v[120:127], v148\n\t"
        "v_exp_f32 v101, v101\n\t"
        "v_exp_f32 v102, v102\n\t"
        "v_mfma_f32_32x32x16_bf16 v[112:127], v[184:187], v[88:91], 0\n\t"
        "v_exp_f32 v103, v103\n\t"
        "v_exp_f32 v104, v104\n\t"
        "v_exp_f32 v105, v105\n\t"
        "v_mfma_f32_32x32x16_bf16 v[112:127], v[188:191], v[92:95], v[112:127]\n\t"
        "v_exp_f32 v106, v106\n\t"
        "v_exp_f32 v107, v107\n\t"
        "v_exp_f32 v108, v108\n\t"
        "v_smfmac_f32_32x32x32_bf16 v[32:47], v[192:195], v[96:103], v148\n\t"
        "v_exp_f32 v109, v109\n\t"
        "v_exp_f32 v110, v110\n\t"
        "v_exp_f32 v111, v111\n\t"
        "v_smfmac_f32_16x16x64_bf16 v[136:139], v[144:147], v[96:103], v148\n\t"
        "buffer_load_dwordx4 v[180:183], v150, s[52:55], 0 offen offset:3072 sc1\n\t"
        "v_exp_f32 v112, v112\n\t"
        "v_exp_f32 v113, v113\n\t"
        "v_smfmac_f32_32x32x32_bf16 v[32:47], v[196:199], v[104:111], v148\n\t"
        "s_add_u32 s44, s44, 0x4000\n\t"
        "s_and_b32 s44, s44, 0x3ffff\n\t"
        "v_exp_f32 v114, v114\n\t"
        "v_exp_f32 v115, v115\n\t"
        "v_exp_f32 v116, v116\n\t"
        "v_smfmac_f32_16x16x64_bf16 v[136:139], v[144:147], v[104:111], v148\n\t"
        "s_waitcnt vmcnt(8)\n\t"
        "v_exp_f32 v117, v117\n\t"
        "v_exp_f32 v118, v118\n\t"
        "v_mfma_f32_32x32x16_bf16 v[96:111], v[200:203], v[64:67], 0\n\t"
        "s_cmp_eq_u32 s44, s49\n\t"
        "v_exp_f32 v119, v119\n\t"
        "v_exp_f32 v120, v120\n\t"
        "v_exp_f32 v121, v121\n\t"
        "v_mfma_f32_32x32x16_bf16 v[96:111], v[204:207], v[68:71], v[96:111]\n\t"
        "s_cselect_b32 s44, s50, s44\n\t"
        "v_exp_f32 v122, v122\n\t"
        "v_exp_f32 v123, v123\n\t"
        "v_exp_f32 v124, v124\n\t"
        "v_smfmac_f32_32x32x32_bf16 v[48:63], v[192:195], v[112:119], v148\n\t"
        "s_add_u32 s52, s40, s44\n\t"
        "s_addc_u32 s53, s41, 0\n\t"
        "v_exp_f32 v125, v125\n\t"
        "v_exp_f32 v126, v126\n\t"
        "v_exp_f32 v127, v127\n\t"
        "v_smfmac_f32_16x16x64_bf16 v[140:143], v[144:147], v[112:119], v148\n\t"
        "buffer_load_dwordx4 v[184:187], v149, s[52:55], 0 offen offset:0 sc1\n\t"
        "v_exp_f32 v96, v96\n\t"
        "v_exp_f32 v97, v97\n\t"
        "v_smfmac_f32_32x32x32_bf16 v[48:63], v[196:199], v[120:127], v148\n\t"
        "v_exp_f32 v98, v98\n\t"
        "v_exp_f32 v99, v99\n\t"
        "v_exp_f32 v100, v100\n\t"
        "v_smfmac_f32_16x16x64_bf16 v[140:143], v[144:147], v[120:127], v148\n\t"
        "v_exp_f32 v101, v101\n\t"
        "v_exp_f32 v102, v102\n\t"
        "v_mfma_f32_32x32x16_bf16 v[112:127], v[200:203], v[72:75], 0\n\t"
        "v_exp_f32 v103, v103\n\t"
        "v_exp_f32 v104, v104\n\t"
        "v_exp_f32 v105, v105\n\t"
        "v_mfma_f32_32x32x16_bf16 v[112:127], v[204:207], v[76:79], v[112:127]\n\t"
        "v_exp_f32 v106, v106\n\t"
        "v_exp_f32 v107, v107\n\t"
        "v_exp_f32 v108, v108\n\t"
        "v_smfmac_f32_32x32x32_bf16 v[0:15], v[208:211], v[96:103], v148\n\t"
        "v_exp_f32 v109, v109\n\t"
        "v_exp_f32 v110, v110\n\t"
        "v_exp_f32 v111, v111\n\t"
        "v_smfmac_f32_16x16x64_bf16 v[128:131], v[144:147], v[96:103], v148\n\t"
        "buffer_load_dwordx4 v[188:191], v149, s[52:55], 0 offen offset:1024 sc1\n\t"
        "v_exp_f32 v112, v112\n\t"
        "v_exp_f32 v113, v113\n\t"
        "v_smfmac_f32_32x32x32_bf16 v[0:15], v[212:215], v[104:111], v148\n\t"
        "v_exp_f32 v114, v114\n\t"
        "v_exp_f32 v115, v115\n\t"
        "v_exp_f32 v116, v116\n\t"
        "v_smfmac_f32_16x16x64_bf16 v[128:131], v[144:147], v[104:111], v148\n\t"
        "v_exp_f32 v117, v117\n\t"
        "v_exp_f32 v118, v118\n\t"
        "v_mfma_f32_32x32x16_bf16 v[96:111], v[200:203], v[80:83], 0\n\t"
        "v_exp_f32 v119, v119\n\t"
        "v_exp_f32 v120, v120\n\t"
        "v_exp_f32 v121, v121\n\t"
        "v_mfma_f32_32x32x16_bf16 v[96:111], v[204:207], v[84:87], v[96:111]\n\t"
        "v_exp_f32 v122, v122\n\t"
        "v_exp_f32 v123, v123\n\t"
        "v_exp_f32 v124, v124\n\t"
        "v_smfmac_f32_32x32x32_bf16 v[16:31], v[208:211], v[112:119], v148\n\t"
        "v_exp_f32 v125, v125\n\t"
        "v_exp_f32 v126, v126\n\t"
        "v_exp_f32 v127, v127\n\t"
        "v_smfmac_f32_16x16x64_bf16 v[132:135], v[144:147], v[112:119], v148\n\t"
        "buffer_load_dwordx4 v[192:195], v149, s[52:55], 0 offen offset:2048 sc1\n\t"
        "v_exp_f32 v96, v96\n\t"
        "v_exp_f32 v97, v97\n\t"
        "v_smfmac_f32_32x32x32_bf16 v[16:31], v[212:215], v[120:127], v148\n\t"
        "v_exp_f32 v98, v98\n\t"
        "v_exp_f32 v99, v99\n\t"
        "v_exp_f32 v100, v100\n\t"
        "v_smfmac_f32_16x16x64_bf16 v[132:135], v[144:147], v[120:127], v148\n\t"
        "v_exp_f32 v101, v101\n\t"
        "v_exp_f32 v102, v102\n\t"
        "v_mfma_f32_32x32x16_bf16 v[112:127], v[200:203], v[88:91], 0\n\t"
        "v_exp_f32 v103, v103\n\t"
        "v_exp_f32 v104, v104\n\t"
        "v_exp_f32 v105, v105\n\t"
        "v_mfma_f32_32x32x16_bf16 v[112:127], v[204:207], v[92:95], v[112:127]\n\t"
        "v_exp_f32 v106, v106\n\t"
        "v_exp_f32 v107, v107\n\t"
        "v_exp_f32 v108, v108\n\t"
        "v_smfmac_f32_32x32x32_bf16 v[32:47], v[208:211], v[96:103], v148\n\t"
        "v_exp_f32 v109, v109\n\t"
        "v_exp_f32 v110, v110\n\t"
        "v_exp_f32 v111, v111\n\t"
        "v_smfmac_f32_16x16x64_bf16 v[136:139], v[144:147], v[96:103], v148\n\t"
        "buffer_load_dwordx4 v[196:199], v149, s[52:55], 0 offen offset:3072 sc1\n\t"
        "v_exp_f32 v112, v112\n\t"
        "v_exp_f32 v113, v113\n\t"
        "v_smfmac_f32_32x32x32_bf16 v[32:47], v[212:215], v[104:111], v148\n\t"
        "v_exp_f32 v114, v114\n\t"
        "v_exp_f32 v115, v115\n\t"
        "v_exp_f32 v116, v116\n\t"
        "v_smfmac_f32_16x16x64_bf16 v[136:139], v[144:147], v[104:111], v148\n\t"
        "s_waitcnt vmcnt(8)\n\t"
        "v_exp_f32 v117, v117\n\t"
        "v_exp_f32 v118, v118\n\t"
        "v_mfma_f32_32x32x16_bf16 v[96:111], v[152:155], v[64:67], 0\n\t"
        "v_exp_f32 v119, v119\n\t"
        "v_exp_f32 v120, v120\n\t"
        "v_exp_f32 v121, v121\n\t"
        "v_mfma_f32_32x32x16_bf16 v[96:111], v[156:159], v[68:71], v[96:111]\n\t"
        "v_exp_f32 v122, v122\n\t"
        "v_exp_f32 v123, v123\n\t"
        "v_exp_f32 v124, v124\n\t"
        "v_smfmac_f32_32x32x32_bf16 v[48:63], v[208:211], v[112:119], v148\n\t"
        "v_exp_f32 v125, v125\n\t"
        "v_exp_f32 v126, v126\n\t"
        "v_exp_f32 v127, v127\n\t"
        "v_smfmac_f32_16x16x64_bf16 v[140:143], v[144:147], v[112:119], v148\n\t"
        "buffer_load_dwordx4 v[200:203], v150, s[52:55], 0 offen offset:0 sc1\n\t"
        "v_exp_f32 v96, v96\n\t"
        "v_exp_f32 v97, v97\n\t"
        "v_smfmac_f32_32x32x32_bf16 v[48:63], v[212:215], v[120:127], v148\n\t"
        "v_exp_f32 v98, v98\n\t"
        "v_exp_f32 v99, v99\n\t"
        "v_exp_f32 v100, v100\n\t"
        "v_smfmac_f32_16x16x64_bf16 v[140:143], v[144:147], v[120:127], v148\n\t"
        "v_exp_f32 v101, v101\n\t"
        "v_exp_f32 v102, v102\n\t"
        "v_mfma_f32_32x32x16_bf16 v[112:127], v[152:155], v[72:75], 0\n\t"
        "v_exp_f32 v103, v103\n\t"
        "v_exp_f32 v104, v104\n\t"
        "v_exp_f32 v105, v105\n\t"
        "v_mfma_f32_32x32x16_bf16 v[112:127], v[156:159], v[76:79], v[112:127]\n\t"
        "v_exp_f32 v106, v106\n\t"
        "v_exp_f32 v107, v107\n\t"
        "v_exp_f32 v108, v108\n\t"
        "v_smfmac_f32_32x32x32_bf16 v[0:15], v[160:163], v[96:103], v148\n\t"
        "v_exp_f32 v109, v109\n\t"
        "v_exp_f32 v110, v110\n\t"
        "v_exp_f32 v111, v111\n\t"
        "v_smfmac_f32_16x16x64_bf16 v[128:131], v[144:147], v[96:103], v148\n\t"
        "buffer_load_dwordx4 v[204:207], v150, s[52:55], 0 offen offset:1024 sc1\n\t"
        "v_exp_f32 v112, v112\n\t"
        "v_exp_f32 v113, v113\n\t"
        "v_smfmac_f32_32x32x32_bf16 v[0:15], v[164:167], v[104:111], v148\n\t"
        "v_exp_f32 v114, v114\n\t"
        "v_exp_f32 v115, v115\n\t"
        "v_exp_f32 v116, v116\n\t"
        "v_smfmac_f32_16x16x64_bf16 v[128:131], v[144:147], v[104:111], v148\n\t"
        "v_exp_f32 v117, v117\n\t"
        "v_exp_f32 v118, v118\n\t"
        "v_mfma_f32_32x32x16_bf16 v[96:111], v[152:155], v[80:83], 0\n\t"
        "v_exp_f32 v119, v119\n\t"
        "v_exp_f32 v120, v120\n\t"
        "v_exp_f32 v121, v121\n\t"
        "v_mfma_f32_32x32x16_bf16 v[96:111], v[156:159], v[84:87], v[96:111]\n\t"
        "v_exp_f32 v122, v122\n\t"
        "v_exp_f32 v123, v123\n\t"
        "v_exp_f32 v124, v124\n\t"
        "v_smfmac_f32_32x32x32_bf16 v[16:31], v[160:163], v[112:119], v148\n\t"
        "v_exp_f32 v125, v125\n\t"
        "v_exp_f32 v126, v126\n\t"
        "v_exp_f32 v127, v127\n\t"
        "v_smfmac_f32_16x16x64_bf16 v[132:135], v[144:147], v[112:119], v148\n\t"
        "buffer_load_dwordx4 v[208:211], v150, s[52:55], 0 offen offset:2048 sc1\n\t"
        "v_exp_f32 v96, v96\n\t"
        "v_exp_f32 v97, v97\n\t"
        "v_smfmac_f32_32x32x32_bf16 v[16:31], v[164:167], v[120:127], v148\n\t"
        "v_exp_f32 v98, v98\n\t"
        "v_exp_f32 v99, v99\n\t"
        "v_exp_f32 v100, v100\n\t"
        "v_smfmac_f32_16x16x64_bf16 v[132:135], v[144:147], v[120:127], v148\n\t"
        "v_exp_f32 v101, v101\n\t"
        "v_exp_f32 v102, v102\n\t"
        "v_mfma_f32_32x32x16_bf16 v[112:127], v[152:155], v[88:91], 0\n\t"
        "v_exp_f32 v103, v103\n\t"
        "v_exp_f32 v104, v104\n\t"
        "v_exp_f32 v105, v105\n\t"
        "v_mfma_f32_32x32x16_bf16 v[112:127], v[156:159], v[92:95], v[112:127]\n\t"
        "v_exp_f32 v106, v106\n\t"
        "v_exp_f32 v107, v107\n\t"
        "v_exp_f32 v108, v108\n\t"
        "v_smfmac_f32_32x32x32_bf16 v[32:47], v[160:163], v[96:103], v148\n\t"
        "v_exp_f32 v109, v109\n\t"
        "v_exp_f32 v110, v110\n\t"
        "v_exp_f32 v111, v111\n\t"
        "v_smfmac_f32_16x16x64_bf16 v[136:139], v[144:147], v[96:103], v148\n\t"
        "buffer_load_dwordx4 v[212:215], v150, s[52:55], 0 offen offset:3072 sc1\n\t"
        "v_exp_f32 v112, v112\n\t"
        "v_exp_f32 v113, v113\n\t"
        "v_smfmac_f32_32x32x32_bf16 v[32:47], v[164:167], v[104:111], v148\n\t"
        "s_add_u32 s44, s44, 0x4000\n\t"
        "s_and_b32 s44, s44, 0x3ffff\n\t"
        "v_exp_f32 v114, v114\n\t"
        "v_exp_f32 v115, v115\n\t"
        "v_exp_f32 v116, v116\n\t"
        "v_smfmac_f32_16x16x64_bf16 v[136:139], v[144:147], v[104:111], v148\n\t"
        "s_waitcnt vmcnt(8)\n\t"
        "v_exp_f32 v117, v117\n\t"
        "v_exp_f32 v118, v118\n\t"
        "v_mfma_f32_32x32x16_bf16 v[96:111], v[168:171], v[64:67], 0\n\t"
        "s_cmp_eq_u32 s44, s49\n\t"
        "v_exp_f32 v119, v119\n\t"
        "v_exp_f32 v120, v120\n\t"
        "v_exp_f32 v121, v121\n\t"
        "v_mfma_f32_32x32x16_bf16 v[96:111], v[172:175], v[68:71], v[96:111]\n\t"
        "s_cselect_b32 s44, s50, s44\n\t"
        "v_exp_f32 v122, v122\n\t"
        "v_exp_f32 v123, v123\n\t"
        "v_exp_f32 v124, v124\n\t"
        "v_smfmac_f32_32x32x32_bf16 v[48:63], v[160:163], v[112:119], v148\n\t"
        "s_add_u32 s52, s40, s44\n\t"
        "s_addc_u32 s53, s41, 0\n\t"
        "v_exp_f32 v125, v125\n\t"
        "v_exp_f32 v126, v126\n\t"
        "v_exp_f32 v127, v127\n\t"
        "v_smfmac_f32_16x16x64_bf16 v[140:143], v[144:147], v[112:119], v148\n\t"
        "buffer_load_dwordx4 v[152:155], v149, s[52:55], 0 offen offset:0 sc1\n\t"
        "v_exp_f32 v96, v96\n\t"
        "v_exp_f32 v97, v97\n\t"
        "v_smfmac_f32_32x32x32_bf16 v[48:63], v[164:167], v[120:127], v148\n\t"
        "v_exp_f32 v98, v98\n\t"
        "v_exp_f32 v99, v99\n\t"
        "v_exp_f32 v100, v100\n\t"
        "v_smfmac_f32_16x16x64_bf16 v[140:143], v[144:147], v[120:127], v148\n\t"
        "v_exp_f32 v101, v101\n\t"
        "v_exp_f32 v102, v102\n\t"
        "v_mfma_f32_32x32x16_bf16 v[112:127], v[168:171], v[72:75], 0\n\t"
        "v_exp_f32 v103, v103\n\t"
        "v_exp_f32 v104, v104\n\t"
        "v_exp_f32 v105, v105\n\t"
        "v_mfma_f32_32x32x16_bf16 v[112:127], v[172:175], v[76:79], v[112:127]\n\t"
        "v_exp_f32 v106, v106\n\t"
        "v_exp_f32 v107, v107\n\t"
        "v_exp_f32 v108, v108\n\t"
        "v_smfmac_f32_32x32x32_bf16 v[0:15], v[176:179], v[96:103], v148\n\t"
        "v_exp_f32 v109, v109\n\t"
        "v_exp_f32 v110, v110\n\t"
        "v_exp_f32 v111, v111\n\t"
        "v_smfmac_f32_16x16x64_bf16 v[128:131], v[144:147], v[96:103], v148\n\t"
        "buffer_load_dwordx4 v[156:159], v149, s[52:55], 0 offen offset:1024 sc1\n\t"
        "v_exp_f32 v112, v112\n\t"
        "v_exp_f32 v113, v113\n\t"
        "v_smfmac_f32_32x32x32_bf16 v[0:15], v[180:183], v[104:111], v148\n\t"
        "v_exp_f32 v114, v114\n\t"
        "v_exp_f32 v115, v115\n\t"
        "v_exp_f32 v116, v116\n\t"
        "v_smfmac_f32_16x16x64_bf16 v[128:131], v[144:147], v[104:111], v148\n\t"
        "v_exp_f32 v117, v117\n\t"
        "v_exp_f32 v118, v118\n\t"
        "v_mfma_f32_32x32x16_bf16 v[96:111], v[168:171], v[80:83], 0\n\t"
        "v_exp_f32 v119, v119\n\t"
        "v_exp_f32 v120, v120\n\t"
        "v_exp_f32 v121, v121\n\t"
        "v_mfma_f32_32x32x16_bf16 v[96:111], v[172:175], v[84:87], v[96:111]\n\t"
        "v_exp_f32 v122, v122\n\t"
        "v_exp_f32 v123, v123\n\t"
        "v_exp_f32 v124, v124\n\t"
        "v_smfmac_f32_32x32x32_bf16 v[16:31], v[176:179], v[112:119], v148\n\t"
        "v_exp_f32 v125, v125\n\t"
        "v_exp_f32 v126, v126\n\t"
        "v_exp_f32 v127, v127\n\t"
        "v_smfmac_f32_16x16x64_bf16 v[132:135], v[144:147], v[112:119], v148\n\t"
        "buffer_load_dwordx4 v[160:163], v149, s[52:55], 0 offen offset:2048 sc1\n\t"
        "v_exp_f32 v96, v96\n\t"
        "v_exp_f32 v97, v97\n\t"
        "v_smfmac_f32_32x32x32_bf16 v[16:31], v[180:183], v[120:127], v148\n\t"
        "v_exp_f32 v98, v98\n\t"
        "v_exp_f32 v99, v99\n\t"
        "v_exp_f32 v100, v100\n\t"
        "v_smfmac_f32_16x16x64_bf16 v[132:135], v[144:147], v[120:127], v148\n\t"
        "v_exp_f32 v101, v101\n\t"
        "v_exp_f32 v102, v102\n\t"
        "v_mfma_f32_32x32x16_bf16 v[112:127], v[168:171], v[88:91], 0\n\t"
        "v_exp_f32 v103, v103\n\t"
        "v_exp_f32 v104, v104\n\t"
        "v_exp_f32 v105, v105\n\t"
        "v_mfma_f32_32x32x16_bf16 v[112:127], v[172:175], v[92:95], v[112:127]\n\t"
        "v_exp_f32 v106, v106\n\t"
        "v_exp_f32 v107, v107\n\t"
        "v_exp_f32 v108, v108\n\t"
        "v_smfmac_f32_32x32x32_bf16 v[32:47], v[176:179], v[96:103], v148\n\t"
        "s_sub_u32 s45, s45, 1\n\t"
        "v_exp_f32 v109, v109\n\t"
        "v_exp_f32 v110, v110\n\t"
        "v_exp_f32 v111, v111\n\t"
        "v_smfmac_f32_16x16x64_bf16 v[136:139], v[144:147], v[96:103], v148\n\t"
        "buffer_load_dwordx4 v[164:167], v149, s[52:55], 0 offen offset:3072 sc1\n\t"
        "s_cbranch_scc0 1b\n\t"
        "v_exp_f32 v112, v112\n\t"
        "v_exp_f32 v113, v113\n\t"
        "v_smfmac_f32_32x32x32_bf16 v[32:47], v[180:183], v[104:111], v148\n\t"
        "v_exp_f32 v114, v114\n\t"
        "v_exp_f32 v115, v115\n\t"
        "v_exp_f32 v116, v116\n\t"
        "v_smfmac_f32_16x16x64_bf16 v[136:139], v[144:147], v[104:111], v148\n\t"
        "s_waitcnt vmcnt(8)\n\t"
        "v_exp_f32 v117, v117\n\t"
        "v_exp_f32 v118, v118\n\t"
        "v_mfma_f32_32x32x16_bf16 v[96:111], v[184:187], v[64:67], 0\n\t"
        "v_exp_f32 v119, v119\n\t"
        "v_exp_f32 v120, v120\n\t"
        "v_exp_f32 v121, v121\n\t"
        "v_mfma_f32_32x32x16_bf16 v[96:111], v[188:191], v[68:71], v[96:111]\n\t"
        "v_exp_f32 v122, v122\n\t"
        "v_exp_f32 v123, v123\n\t"
        "v_exp_f32 v124, v124\n\t"
        "v_smfmac_f32_32x32x32_bf16 v[48:63], v[176:179], v[112:119], v148\n\t"
        "v_exp_f32 v125, v125\n\t"
        "v_exp_f32 v126, v126\n\t"
        "v_exp_f32 v127, v127\n\t"
        "v_smfmac_f32_16x16x64_bf16 v[140:143], v[144:147], v[112:119], v148\n\t"
        "v_exp_f32 v96, v96\n\t"
        "v_exp_f32 v97, v97\n\t"
        "v_smfmac_f32_32x32x32_bf16 v[48:63], v[180:183], v[120:127], v148\n\t"
        "v_exp_f32 v98, v98\n\t"
        "v_exp_f32 v99, v99\n\t"
        "v_exp_f32 v100, v100\n\t"
        "v_smfmac_f32_16x16x64_bf16 v[140:143], v[144:147], v[120:127], v148\n\t"
        "v_exp_f32 v101, v101\n\t"
        "v_exp_f32 v102, v102\n\t"
        "v_mfma_f32_32x32x16_bf16 v[112:127], v[184:187], v[72:75], 0\n\t"
        "v_exp_f32 v103, v103\n\t"
        "v_exp_f32 v104, v104\n\t"
        "v_exp_f32 v105, v105\n\t"
        "v_mfma_f32_32x32x16_bf16 v[112:127], v[188:191], v[76:79], v[112:127]\n\t"
        "v_exp_f32 v106, v106\n\t"
        "v_exp_f32 v107, v107\n\t"
        "v_exp_f32 v108, v108\n\t"
        "v_smfmac_f32_32x32x32_bf16 v[0:15], v[192:195], v[96:103], v148\n\t"
        "v_exp_f32 v109, v109\n\t"
        "v_exp_f32 v110, v110\n\t"
        "v_exp_f32 v111, v111\n\t"
        "v_smfmac_f32_16x16x64_bf16 v[128:131], v[144:147], v[96:103], v148\n\t"
        "v_exp_f32 v112, v112\n\t"
        "v_exp_f32 v113, v113\n\t"
        "v_smfmac_f32_32x32x32_bf16 v[0:15], v[196:199], v[104:111], v148\n\t"
        "v_exp_f32 v114, v114\n\t"
        "v_exp_f32 v115, v115\n\t"
        "v_exp_f32 v116, v116\n\t"
        "v_smfmac_f32_16x16x64_bf16 v[128:131], v[144:147], v[104:111], v148\n\t"
        "s_waitcnt vmcnt(4)\n\t"
        "v_exp_f32 v117, v117\n\t"
        "v_exp_f32 v118, v118\n\t"
        "v_mfma_f32_32x32x16_bf16 v[96:111], v[200:203], v[64:67], 0\n\t"
        "v_exp_f32 v119, v119\n\t"
        "v_exp_f32 v120, v120\n\t"
        "v_exp_f32 v121, v121\n\t"
        "v_mfma_f32_32x32x16_bf16 v[96:111], v[204:207], v[68:71], v[96:111]\n\t"
        "v_exp_f32 v122, v122\n\t"
        "v_exp_f32 v123, v123\n\t"
        "v_exp_f32 v124, v124\n\t"
        "v_smfmac_f32_32x32x32_bf16 v[16:31], v[192:195], v[112:119], v148\n\t"
        "v_exp_f32 v125, v125\n\t"
        "v_exp_f32 v126, v126\n\t"
        "v_exp_f32 v127, v127\n\t"
        "v_smfmac_f32_16x16x64_bf16 v[132:135], v[144:147], v[112:119], v148\n\t"
        "s_cmp_lg_u32 s47, 0\n\t"
        "s_cbranch_scc1 4f\n\t"
        "global_load_dwordx4 %[r0], v223, %[xb] offset:0\n\t"
        "global_load_dwordx4 %[r1], v223, %[xb] offset:32\n\t"
        "global_load_dwordx4 %[r2], v223, %[xb] offset:64\n\t"
        "global_load_dwordx4 %[r3], v223, %[xb] offset:96\n\t"
        "4:\n\t"
        "v_exp_f32 v96, v96\n\t"
        "v_exp_f32 v97, v97\n\t"
        "v_smfmac_f32_32x32x32_bf16 v[16:31], v[196:199], v[120:127], v148\n\t"
        "v_exp_f32 v98, v98\n\t"
        "v_exp_f32 v99, v99\n\t"
        "v_exp_f32 v100, v100\n\t"
        "v_smfmac_f32_16x16x64_bf16 v[132:135], v[144:147], v[120:127], v148\n\t"
        "v_exp_f32 v101, v101\n\t"
        "v_exp_f32 v102, v102\n\t"
        "v_mfma_f32_32x32x16_bf16 v[112:127], v[200:203], v[72:75], 0\n\t"
        "v_exp_f32 v103, v103\n\t"
        "v_exp_f32 v104, v104\n\t"
        "v_exp_f32 v105, v105\n\t"
        "v_mfma_f32_32x32x16_bf16 v[112:127], v[204:207], v[76:79], v[112:127]\n\t"
        "v_exp_f32 v106, v106\n\t"
        "v_exp_f32 v107, v107\n\t"
        "v_exp_f32 v108, v108\n\t"
        "v_smfmac_f32_32x32x32_bf16 v[0:15], v[208:211], v[96:103], v148\n\t"
        "v_exp_f32 v109, v109\n\t"
        "v_exp_f32 v110, v110\n\t"
        "v_exp_f32 v111, v111\n\t"
        "v_smfmac_f32_16x16x64_bf16 v[128:131], v[144:147], v[96:103], v148\n\t"
        "v_exp_f32 v112, v112\n\t"
        "v_exp_f32 v113, v113\n\t"
        "v_smfmac_f32_32x32x32_bf16 v[0:15], v[212:215], v[104:111], v148\n\t"
        "v_exp_f32 v114, v114\n\t"
        "v_exp_f32 v115, v115\n\t"
        "v_exp_f32 v116, v116\n\t"
        "v_smfmac_f32_16x16x64_bf16 v[128:131], v[144:147], v[104:111], v148\n\t"
        "v_exp_f32 v117, v117\n\t"
        "v_exp_f32 v118, v118\n\t"
        "v_mfma_f32_32x32x16_bf16 v[96:111], v[184:187], v[80:83], 0\n\t"
        "v_exp_f32 v119, v119\n\t"
        "v_exp_f32 v120, v120\n\t"
        "v_exp_f32 v121, v121\n\t"
        "v_mfma_f32_32x32x16_bf16 v[96:111], v[188:191], v[84:87], v[96:111]\n\t"
        "ds_write_b128 v216, v[0:3] offset:0\n\t"
        "ds_write_b128 v216, v[4:7] offset:1024\n\t"
        "v_exp_f32 v122, v122\n\t"
        "v_exp_f32 v123, v123\n\t"
        "v_exp_f32 v124, v124\n\t"
        "v_smfmac_f32_32x32x32_bf16 v[16:31], v[208:211], v[112:119], v148\n\t"
        "ds_write_b128 v216, v[8:11] offset:2048\n\t"
        "ds_write_b128 v216, v[12:15] offset:3072\n\t"
        "v_exp_f32 v125, v125\n\t"
        "v_exp_f32 v126, v126\n\t"
        "v_exp_f32 v127, v127\n\t"
        "v_smfmac_f32_16x16x64_bf16 v[132:135], v[144:147], v[112:119], v148\n\t"
        "ds_write_b32 v218, v128 offset:0\n\t"
        "v_exp_f32 v96, v96\n\t"
        "v_exp_f32 v97, v97\n\t"
        "v_smfmac_f32_32x32x32_bf16 v[16:31], v[212:215], v[120:127], v148\n\t"
        "v_exp_f32 v98, v98\n\t"
        "v_exp_f32 v99, v99\n\t"
        "v_exp_f32 v100, v100\n\t"
        "v_smfmac_f32_16x16x64_bf16 v[132:135], v[144:147], v[120:127], v148\n\t"
        "v_exp_f32 v101, v101\n\t"
        "v_exp_f32 v102, v102\n\t"
        "v_mfma_f32_32x32x16_bf16 v[112:127], v[184:187], v[88:91], 0\n\t"
        "v_exp_f32 v103, v103\n\t"
        "v_exp_f32 v104, v104\n\t"
        "v_exp_f32 v105, v105\n\t"
        "v_mfma_f32_32x32x16_bf16 v[112:127], v[188:191], v[92:95], v[112:127]\n\t"
        "ds_write_b128 v216, v[16:19] offset:4096\n\t"
        "ds_write_b128 v216, v[20:23] offset:5120\n\t"
        "v_exp_f32 v106, v106\n\t"
        "v_exp_f32 v107, v107\n\t"
        "v_exp_f32 v108, v108\n\t"
        "v_smfmac_f32_32x32x32_bf16 v[32:47], v[192:195], v[96:103], v148\n\t"
        "ds_write_b128 v216, v[24:27] offset:6144\n\t"
        "ds_write_b128 v216, v[28:31] offset:7168\n\t"
        "v_exp_f32 v109, v109\n\t"
        "v_exp_f32 v110, v110\n\t"
        "v_exp_f32 v111, v111\n\t"
        "v_smfmac_f32_16x16x64_bf16 v[136:139], v[144:147], v[96:103], v148\n\t"
        "ds_write_b32 v218, v132 offset:256\n\t"
        "ds_write_b32 v220, v222\n\t"
        "v_exp_f32 v112, v112\n\t"
        "v_exp_f32 v113, v113\n\t"
        "v_smfmac_f32_32x32x32_bf16 v[32:47], v[196:199], v[104:111], v148\n\t"
        "v_exp_f32 v114, v114\n\t"
        "v_exp_f32 v115, v115\n\t"
        "v_exp_f32 v116, v116\n\t"
        "v_smfmac_f32_16x16x64_bf16 v[136:139], v[144:147], v[104:111], v148\n\t"
        "v_exp_f32 v117, v117\n\t"
        "v_exp_f32 v118, v118\n\t"
        "v_mfma_f32_32x32x16_bf16 v[96:111], v[200:203], v[80:83], 0\n\t"
        "v_exp_f32 v119, v119\n\t"
        "v_exp_f32 v120, v120\n\t"
        "v_exp_f32 v121, v121\n\t"
        "v_mfma_f32_32x32x16_bf16 v[96:111], v[204:207], v[84:87], v[96:111]\n\t"
        "v_exp_f32 v122, v122\n\t"
        "v_exp_f32 v123, v123\n\t"
        "v_exp_f32 v124, v124\n\t"
        "v_smfmac_f32_32x32x32_bf16 v[48:63], v[192:195], v[112:119], v148\n\t"
        "v_exp_f32 v125, v125\n\t"
        "v_exp_f32 v126, v126\n\t"
        "v_exp_f32 v127, v127\n\t"
        "v_smfmac_f32_16x16x64_bf16 v[140:143], v[144:147], v[112:119], v148\n\t"
        "v_exp_f32 v96, v96\n\t"
        "v_exp_f32 v97, v97\n\t"
        "v_smfmac_f32_32x32x32_bf16 v[48:63], v[196:199], v[120:127], v148\n\t"
        "v_exp_f32 v98, v98\n\t"
        "v_exp_f32 v99, v99\n\t"
        "v_exp_f32 v100, v100\n\t"
        "v_smfmac_f32_16x16x64_bf16 v[140:143], v[144:147], v[120:127], v148\n\t"
        "v_exp_f32 v101, v101\n\t"
        "v_exp_f32 v102, v102\n\t"
        "v_mfma_f32_32x32x16_bf16 v[112:127], v[200:203], v[88:91], 0\n\t"
        "v_exp_f32 v103, v103\n\t"
        "v_exp_f32 v104, v104\n\t"
        "v_exp_f32 v105, v105\n\t"
        "v_mfma_f32_32x32x16_bf16 v[112:127], v[204:207], v[92:95], v[112:127]\n\t"
        "ds_read_b32 v151, v221\n\t"
        "v_exp_f32 v106, v106\n\t"
        "v_exp_f32 v107, v107\n\t"
        "v_exp_f32 v108, v108\n\t"
        "v_smfmac_f32_32x32x32_bf16 v[32:47], v[208:211], v[96:103], v148\n\t"
        "v_exp_f32 v109, v109\n\t"
        "v_exp_f32 v110, v110\n\t"
        "v_exp_f32 v111, v111\n\t"
        "v_smfmac_f32_16x16x64_bf16 v[136:139], v[144:147], v[96:103], v148\n\t"
        "s_waitcnt lgkmcnt(0)\n\t"
        "v_cmp_ne_u32 vcc, v222, v151\n\t"
        "s_cbranch_vccnz 40f\n\t"
        "41:\n\t"
        "v_exp_f32 v112, v112\n\t"
        "v_exp_f32 v113, v113\n\t"
        "v_smfmac_f32_32x32x32_bf16 v[32:47], v[212:215], v[104:111], v148\n\t"
        "ds_read_b128 v[0:3], v217 offset:0\n\t"
        "ds_read_b128 v[4:7], v217 offset:1024\n\t"
        "ds_read_b128 v[8:11], v217 offset:2048\n\t"
        "ds_read_b128 v[12:15], v217 offset:3072\n\t"
        "v_exp_f32 v114, v114\n\t"
        "v_exp_f32 v115, v115\n\t"
        "v_exp_f32 v116, v116\n\t"
        "v_smfmac_f32_16x16x64_bf16 v[136:139], v[144:147], v[104:111], v148\n\t"
        "ds_read_b32 v128, v219 offset:0\n\t"
        "ds_read_b128 v[16:19], v217 offset:4096\n\t"
        "ds_read_b128 v[20:23], v217 offset:5120\n\t"
        "v_exp_f32 v117, v117\n\t"
        "v_exp_f32 v118, v118\n\t"
        "v_exp_f32 v119, v119\n\t"
        "v_exp_f32 v120, v120\n\t"
        "v_exp_f32 v121, v121\n\t"
        "v_exp_f32 v122, v122\n\t"
        "v_exp_f32 v123, v123\n\t"
        "v_exp_f32 v124, v124\n\t"
        "v_smfmac_f32_32x32x32_bf16 v[48:63], v[208:211], v[112:119], v148\n\t"
        "ds_read_b128 v[24:27], v217 offset:6144\n\t"
        "ds_read_b128 v[28:31], v217 offset:7168\n\t"
        "ds_read_b32 v132, v219 offset:256\n\t"
        "v_exp_f32 v125, v125\n\t"
        "v_exp_f32 v126, v126\n\t"
        "v_exp_f32 v127, v127\n\t"
        "v_smfmac_f32_16x16x64_bf16 v[140:143], v[144:147], v[112:119], v148\n\t"
        "s_nop 1\n\t"
        "v_smfmac_f32_32x32x32_bf16 v[48:63], v[212:215], v[120:127], v148\n\t"
        "v_smfmac_f32_16x16x64_bf16 v[140:143], v[144:147], v[120:127], v148\n\t"
        "s_waitcnt vmcnt(0) lgkmcnt(0)\n\t"
        "s_nop 6\n\t"
        "s_branch 9f\n\t"
        "24:\n\t"
        "s_add_u32 s44, s44, 0x4000\n\t"
        "s_and_b32 s44, s44, 0x3ffff\n\t"
        "s_cmp_eq_u32 s44, s49\n\t"
        "s_cselect_b32 s44, s50, s44\n\t"
        "s_add_u32 s52, s40, s44\n\t"
        "s_addc_u32 s53, s41, 0\n\t"
        "buffer_load_dwordx4 v[184:187], v149, s[52:55], 0 offen offset:0 sc1\n\t"
        "buffer_load_dwordx4 v[188:191], v149, s[52:55], 0 offen offset:1024 sc1\n\t"
        "buffer_load_dwordx4 v[192:195], v149, s[52:55], 0 offen offset:2048 sc1\n\t"
        "buffer_load_dwordx4 v[196:199], v149, s[52:55], 0 offen offset:3072 sc1\n\t"
        "buffer_load_dwordx4 v[200:203], v150, s[52:55], 0 offen offset:0 sc1\n\t"
        "buffer_load_dwordx4 v[204:207], v150, s[52:55], 0 offen offset:1024 sc1\n\t"
        "buffer_load_dwordx4 v[208:211], v150, s[52:55], 0 offen offset:2048 sc1\n\t"
        "buffer_load_dwordx4 v[212:215], v150, s[52:55], 0 offen offset:3072 sc1\n\t"
        "s_mov_b32 s46, 1\n\t"
        "s_branch 25b\n\t"
        "26:\n\t"
        "s_add_u32 s44, s44, 0x4000\n\t"
        "s_and_b32 s44, s44, 0x3ffff\n\t"
        "s_cmp_eq_u32 s44, s49\n\t"
        "s_cselect_b32 s44, s50, s44\n\t"
        "s_add_u32 s52, s40, s44\n\t"
        "s_addc_u32 s53, s41, 0\n\t"
        "buffer_load_dwordx4 v[184:187], v149, s[52:55], 0 offen offset:0 sc1\n\t"
        "buffer_load_dwordx4 v[188:191], v149, s[52:55], 0 offen offset:1024 sc1\n\t"
        "buffer_load_dwordx4 v[192:195], v149, s[52:55], 0 offen offset:2048 sc1\n\t"
        "buffer_load_dwordx4 v[196:199], v149, s[52:55], 0 offen offset:3072 sc1\n\t"
        "buffer_load_dwordx4 v[200:203], v150, s[52:55], 0 offen offset:0 sc1\n\t"
        "buffer_load_dwordx4 v[204:207], v150, s[52:55], 0 offen offset:1024 sc1\n\t"
        "buffer_load_dwordx4 v[208:211], v150, s[52:55], 0 offen offset:2048 sc1\n\t"
        "buffer_load_dwordx4 v[212:215], v150, s[52:55], 0 offen offset:3072 sc1\n\t"
        "s_mov_b32 s46, 1\n\t"
        "s_branch 27b\n\t"
        "28:\n\t"
        "s_add_u32 s44, s44, 0x4000\n\t"
        "s_and_b32 s44, s44, 0x3ffff\n\t"
        "s_cmp_eq_u32 s44, s49\n\t"
        "s_cselect_b32 s44, s50, s44\n\t"
        "s_add_u32 s52, s40, s44\n\t"
        "s_addc_u32 s53, s41, 0\n\t"
        "buffer_load_dwordx4 v[184:187], v149, s[52:55], 0 offen offset:0 sc1\n\t"
        "buffer_load_dwordx4 v[188:191], v149, s[52:55], 0 offen offset:1024 sc1\n\t"
        "buffer_load_dwordx4 v[192:195], v149, s[52:55], 0 offen offset:2048 sc1\n\t"
        "buffer_load_dwordx4 v[196:199], v149, s[52:55], 0 offen offset:3072 sc1\n\t"
        "buffer_load_dwordx4 v[200:203], v150, s[52:55], 0 offen offset:0 sc1\n\t"
        "buffer_load_dwordx4 v[204:207], v150, s[52:55], 0 offen offset:1024 sc1\n\t"
        "buffer_load_dwordx4 v[208:211], v150, s[52:55], 0 offen offset:2048 sc1\n\t"
        "buffer_load_dwordx4 v[212:215], v150, s[52:55], 0 offen offset:3072 sc1\n\t"
        "s_mov_b32 s46, 1\n\t"
        "s_branch 29b\n\t"
        "30:\n\t"
        "s_add_u32 s44, s44, 0x4000\n\t"
        "s_and_b32 s44, s44, 0x3ffff\n\t"
        "s_cmp_eq_u32 s44, s49\n\t"
        "s_cselect_b32 s44, s50, s44\n\t"
        "s_add_u32 s52, s40, s44\n\t"
        "s_addc_u32 s53, s41, 0\n\t"
        "buffer_load_dwordx4 v[184:187], v149, s[52:55], 0 offen offset:0 sc1\n\t"
        "buffer_load_dwordx4 v[188:191], v149, s[52:55], 0 offen offset:1024 sc1\n\t"
        "buffer_load_dwordx4 v[192:195], v149, s[52:55], 0 offen offset:2048 sc1\n\t"
        "buffer_load_dwordx4 v[196:199], v149, s[52:55], 0 offen offset:3072 sc1\n\t"
        "buffer_load_dwordx4 v[200:203], v150, s[52:55], 0 offen offset:0 sc1\n\t"
        "buffer_load_dwordx4 v[204:207], v150, s[52:55], 0 offen offset:1024 sc1\n\t"
        "buffer_load_dwordx4 v[208:211], v150, s[52:55], 0 offen offset:2048 sc1\n\t"
        "buffer_load_dwordx4 v[212:215], v150, s[52:55], 0 offen offset:3072 sc1\n\t"
        "s_mov_b32 s46, 1\n\t"
        "s_branch 31b\n\t"
        "32:\n\t"
        "s_add_u32 s44, s44, 0x4000\n\t"
        "s_and_b32 s44, s44, 0x3ffff\n\t"
        "s_cmp_eq_u32 s44, s49\n\t"
        "s_cselect_b32 s44, s50, s44\n\t"
        "s_add_u32 s52, s40, s44\n\t"
        "s_addc_u32 s53, s41, 0\n\t"
        "buffer_load_dwordx4 v[184:187], v149, s[52:55], 0 offen offset:0 sc1\n\t"
        "buffer_load_dwordx4 v[188:191], v149, s[52:55], 0 offen offset:1024 sc1\n\t"
        "buffer_load_dwordx4 v[192:195], v149, s[52:55], 0 offen offset:2048 sc1\n\t"
        "buffer_load_dwordx4 v[196:199], v149, s[52:55], 0 offen offset:3072 sc1\n\t"
        "buffer_load_dwordx4 v[200:203], v150, s[52:55], 0 offen offset:0 sc1\n\t"
        "buffer_load_dwordx4 v[204:207], v150, s[52:55], 0 offen offset:1024 sc1\n\t"
        "buffer_load_dwordx4 v[208:211], v150, s[52:55], 0 offen offset:2048 sc1\n\t"
        "buffer_load_dwordx4 v[212:215], v150, s[52:55], 0 offen offset:3072 sc1\n\t"
        "s_mov_b32 s46, 1\n\t"
        "s_branch 33b\n\t"
        "40:\n\t"
        "s_mov_b32 s45, 0x100000\n\t"
        "42:\n\t"
        "ds_read_b32 v151, v221\n\t"
        "s_sub_u32 s45, s45, 1\n\t"
        "s_waitcnt lgkmcnt(0)\n\t"
        "s_cbranch_scc1 41b\n\t"
        "v_cmp_ne_u32 vcc, v222, v151\n\t"
        "s_cbranch_vccnz 42b\n\t"
        "s_branch 41b\n\t"
        "9:\n\t"
        : "+{v[0:15]}"(o0), "+{v[16:31]}"(o1), "+{v[32:47]}"(o2), "+{v[48:63]}"(o3), "+{v[128:143]}"(lv), "+{s44}"(soff), "+{v[152:167]}"(kva), "+{v[168:183]}"(kvb), "+{v[184:191]}"(fv),
          [r0] "+v"(x1p[0]), [r1] "+v"(x1p[1]), [r2] "+v"(x1p[2]), [r3] "+v"(x1p[3]) STAMP_OUTS
        : "{v[64:71]}"(q[0]), "{v[72:79]}"(q[1]), "{v[80:87]}"(q[2]), "{v[88:95]}"(q[3]), "{v[144:147]}"(ones), "{v148}"(0xddddu), "{v149}"(kvoff), "{v150}"(kvoff + 4096),
          "{v[216:223]}"(cv), "{v224}"(lbase + L_W + 16), "{s[40:43]}"(srd), "{s47}"(L), "{s49}"(tm * 16384), "{s50}"(((tm + 1) & 15) * 16384), [xb] "s"(x0)
        : "v96", "v97", "v98", "v99", "v100", "v101", "v102", "v103", "v104", "v105", "v106", "v107", "v108", "v109", "v110", "v111", "v112", "v113", "v114", "v115", "v116", "v117", "v118", "v119", "v120", "v121", "v122", "v123", "v124", "v125", "v126", "v127", "v151", "v192", "v193", "v194", "v195", "v196", "v197", "v198", "v199", "v200", "v201", "v202", "v203", "v204", "v205", "v206", "v207", "v208", "v209", "v210", "v211", "v212", "v213", "v214", "v215", "s45", "s46", "s52", "s53", "s54", "s55", "vcc", "scc", "memory");
      STAMP_PUT(6 + 16 * L);
      STAMPV(9 + 16 * L);
      PHASE_VARS;
      const unsigned dst = OFF_OF + L * (5 * MBY) + (tm * 4 + 2 * kh) * TS + (tn * 2 + hh) * 2048 + lane * 16;
      auto put = [&](int u, const f32x16& o) {
#pragma unroll
        for (int c = 0; c < 2; ++c) {
          u32x4 w;
#pragma unroll
          for (int e = 0; e < 4; ++e) w[e] = pack2(o[8 * c + 2 * e], o[8 * c + 2 * e + 1]);
          stg(rs, dst + u * TS + c * 1024, w);
        }
      };
      const float la = lv[8] + lv[0], lb = lv[12] + lv[4];
      o2 = (o2 + o0) * RCP(la); put(0, o2);
      SBAR();
      o3 = (o3 + o1) * RCP(lb); put(1, o3);
      SBAR();
      {
        const f32x16 t = o2 + o3;
        float sum = 0.f;
#pragma unroll
        for (int e = 0; e < 16; e += 4) sum += (t[e] + t[e + 1]) + (t[e + 2] + t[e + 3]);
        if (__any(!(fabsf(sum) < 1e37f) | !(fmaxf(la, lb) < 1e27f) | !(fminf(la, lb) > 1e-9f) | !(la + lb == la + lb))) {
          DRAIN();
          attn_safe(rs, kvoff - kh * 8192, q[2], ones, o2); put(0, o2);
          attn_safe(rs, kvoff - kh * 8192, q[3], ones, o3); put(1, o3);
        }
      }
    }
    STAMPV(10 + 16 * L);
    {
      f32x16 acc[8];
      u32x16 wrc;
      u32x16 fl0, fl1, fl2;
      STAMP_DECL;
      { PHASE_VARS;
      f32x2 r0 = {0.f, 0.f}, r1 = {1.f, 1.f};
      if (L == 0) {
#pragma unroll
        for (int i = 0; i < 4; ++i) x1p[i] = fo_swap(x1p[i]);
      } else {
        const float* p = reinterpret_cast<const float*>(smem + L_AB) + 256 + wid * 64 + (lane & 15);
        r0 = f32x2{p[0], p[16]}; r1 = f32x2{p[32], p[48]};
      }
      row_swap(x1p[0], x1p[1]); row_swap(x1p[2], x1p[3]);
#pragma unroll
      for (int e = 0; e < 16; ++e) {
        acc[e >> 3][2 * (e & 7)] = lo16(x1p[e >> 2][e & 3]) * r1[(e >> 2) & 1] + r0[(e >> 2) & 1];
        acc[e >> 3][2 * (e & 7) + 1] = hi16(x1p[e >> 2][e & 3]) * r1[(e >> 2) & 1] + r0[(e >> 2) & 1];
      }
      PIN(acc[0]); PIN(acc[1]);
      asm volatile("s_waitcnt vmcnt(0)\n\tds_write_b32 %0, %1" :: "v"(lbase + L_W + 80 + 4 * wid), "v"(2 * L + 2) : "memory");
      STAMPV(11 + 16 * L);
      u32x8 wa;
#pragma unroll
      for (int f = 0; f < 8; ++f) wa[f] = lbase + (f >> 2) * 65536 + (16 * tblk + wrow) * 128 + (((4 * ((f >> 1) & 1) + 2 * (tp & 1) + (f & 1)) ^ wsw ^ tblk) << 4) + 8 * (tp >> 1);
      asm volatile(
        "s_mov_b32 s46, 0\n\t"
        "ostart_%=:\n\t"
        "s_mov_b32 s48, 0x200000\n\t"
        "2:\n\t"
        "ds_read_b32 v239, v238\n\t"
        "s_sub_u32 s48, s48, 1\n\t"
        "s_waitcnt lgkmcnt(0)\n\t"
        "s_cselect_b32 s47, 15, 0\n\t"
        "v_readfirstlane_b32 s51, v239\n\t"
        "s_nop 3\n\t"
        "s_or_b32 s51, s51, s47\n\t"
        "s_andn2_b32 s47, s51, s46\n\t"
        "s_cbranch_scc0 2b\n\t"
        "s_andn2_b32 s51, s51, s46\n\t"
        "s_ff1_i32_b32 s47, s51\n\t"
        "s_add_u32 s45, s47, s49\n\t"
        "s_and_b32 s45, s45, 3\n\t"
        "s_lshl_b32 s45, s45, 14\n\t"
        "s_lshr_b32 s51, s47, 31\n\t"
        "s_lshl_b32 s51, s51, 31\n\t"
        "v_or_b32 v225, s51, v224\n\t"
        "s_and_b32 s51, s47, 3\n\t"
        "s_lshl_b32 s51, s51, 15\n\t"
        "v_add_u32 v234, s51, v226\n\t"
        "v_add_u32 v235, s51, v227\n\t"
        "v_add_u32 v236, s51, v228\n\t"
        "v_add_u32 v237, s51, v229\n\t"
        "buffer_load_dwordx4 v[160:163], v225, s[40:43], s45 offen offset:0 sc1\n\t"
        "buffer_load_dwordx4 v[164:167], v225, s[40:43], s45 offen offset:256 sc1\n\t"
        "buffer_load_dwordx4 v[168:171], v225, s[40:43], s45 offen offset:2048 sc1\n\t"
        "buffer_load_dwordx4 v[172:175], v225, s[40:43], s45 offen offset:2304 sc1\n\t"
        "s_addk_i32 s45, 0x1000\n\t"
        "buffer_load_dwordx4 v[176:179], v225, s[40:43], s45 offen offset:0 sc1\n\t"
        "buffer_load_dwordx4 v[180:183], v225, s[40:43], s45 offen offset:256 sc1\n\t"
        "buffer_load_dwordx4 v[184:187], v225, s[40:43], s45 offen offset:2048 sc1\n\t"
        "buffer_load_dwordx4 v[188:191], v225, s[40:43], s45 offen offset:2304 sc1\n\t"
        "s_addk_i32 s45, 0x1000\n\t"
        "buffer_load_dwordx4 v[192:195], v225, s[40:43], s45 offen offset:0 sc1\n\t"
        "buffer_load_dwordx4 v[196:199], v225, s[40:43], s45 offen offset:256 sc1\n\t"
        "buffer_load_dwordx4 v[200:203], v225, s[40:43], s45 offen offset:2048 sc1\n\t"
        "buffer_load_dwordx4 v[204:207], v225, s[40:43], s45 offen offset:2304 sc1\n\t"
        "ds_read_b64_tr_b16 v[128:129], v234 offset:0\n\t"
        "ds_read_b64_tr_b16 v[130:131], v234 offset:1024\n\t"
        "ds_read_b64_tr_b16 v[132:133], v235 offset:0\n\t"
        "ds_read_b64_tr_b16 v[134:135], v235 offset:1024\n\t"
        "ds_read_b64_tr_b16 v[136:137], v236 offset:0\n\t"
        "ds_read_b64_tr_b16 v[138:139], v236 offset:1024\n\t"
        "ds_read_b64_tr_b16 v[140:141], v237 offset:0\n\t"
        "ds_read_b64_tr_b16 v[142:143], v237 offset:1024\n\t"
        "ds_read_b64_tr_b16 v[144:145], v234 offset:4096\n\t"
        "ds_read_b64_tr_b16 v[146:147], v234 offset:5120\n\t"
        "ds_read_b64_tr_b16 v[148:149], v235 offset:4096\n\t"
        "ds_read_b64_tr_b16 v[150:151], v235 offset:5120\n\t"
        "ds_read_b64_tr_b16 v[152:153], v236 offset:4096\n\t"
        "ds_read_b64_tr_b16 v[154:155], v236 offset:5120\n\t"
        "s_mov_b32 s44, s45\n\t"
        "s_cmp_eq_u32 s47, 0\n\t"
        "s_cbranch_scc1 oseg0_%=\n\t"
        "s_cmp_eq_u32 s47, 1\n\t"
        "s_cbranch_scc1 oseg1_%=\n\t"
        "s_cmp_eq_u32 s47, 2\n\t"
        "s_cbranch_scc1 oseg2_%=\n\t"
        "s_cmp_eq_u32 s47, 3\n\t"
        "s_cbranch_scc1 oseg3_%=\n\t"
        "oseg0_%=:\n\t"
        "s_or_b32 s46, s46, 1\n\t"
        "s_waitcnt vmcnt(10) lgkmcnt(10)\n\t"
        "v_mfma_f32_16x16x32_bf16 v[0:3], v[128:131], v[160:163], v[0:3]\n\t"
        "ds_read_b64_tr_b16 v[156:157], v229 offset:4096\n\t"
        "ds_read_b32 v239, v238\n\t"
        "v_mfma_f32_16x16x32_bf16 v[8:11], v[128:131], v[164:167], v[8:11]\n\t"
        "ds_read_b64_tr_b16 v[158:159], v229 offset:5120\n\t"
        "v_mfma_f32_16x16x32_bf16 v[4:7], v[132:135], v[160:163], v[4:7]\n\t"
        "ds_read_b64_tr_b16 v[128:129], v226 offset:8192\n\t"
        "s_addk_i32 s44, 0x1000\n\t"
        "buffer_load_dwordx4 v[208:211], v224, s[40:43], s44 offen offset:0 sc1\n\t"
        "v_mfma_f32_16x16x32_bf16 v[12:15], v[132:135], v[164:167], v[12:15]\n\t"
        "ds_read_b64_tr_b16 v[130:131], v226 offset:9216\n\t"
        "s_waitcnt lgkmcnt(3)\n\t"
        "v_mfma_f32_16x16x32_bf16 v[16:19], v[136:139], v[160:163], v[16:19]\n\t"
        "ds_read_b64_tr_b16 v[132:133], v227 offset:8192\n\t"
        "v_readfirstlane_b32 s51, v239\n\t"
        "v_mfma_f32_16x16x32_bf16 v[24:27], v[136:139], v[164:167], v[24:27]\n\t"
        "ds_read_b64_tr_b16 v[134:135], v227 offset:9216\n\t"
        "v_mfma_f32_16x16x32_bf16 v[20:23], v[140:143], v[160:163], v[20:23]\n\t"
        "ds_read_b64_tr_b16 v[136:137], v228 offset:8192\n\t"
        "buffer_load_dwordx4 v[212:215], v224, s[40:43], s44 offen offset:256 sc1\n\t"
        "v_mfma_f32_16x16x32_bf16 v[28:31], v[140:143], v[164:167], v[28:31]\n\t"
        "ds_read_b64_tr_b16 v[138:139], v228 offset:9216\n\t"
        "s_andn2_b32 s51, s51, s46\n\t"
        "s_waitcnt vmcnt(10) lgkmcnt(11)\n\t"
        "v_mfma_f32_16x16x32_bf16 v[0:3], v[144:147], v[168:171], v[0:3]\n\t"
        "ds_read_b64_tr_b16 v[140:141], v229 offset:8192\n\t"
        "s_ff1_i32_b32 s47, s51\n\t"
        "v_mfma_f32_16x16x32_bf16 v[8:11], v[144:147], v[172:175], v[8:11]\n\t"
        "ds_read_b64_tr_b16 v[142:143], v229 offset:9216\n\t"
        "s_add_u32 s45, s47, s49\n\t"
        "v_mfma_f32_16x16x32_bf16 v[4:7], v[148:151], v[168:171], v[4:7]\n\t"
        "ds_read_b64_tr_b16 v[144:145], v226 offset:12288\n\t"
        "buffer_load_dwordx4 v[216:219], v224, s[40:43], s44 offen offset:2048 sc1\n\t"
        "v_mfma_f32_16x16x32_bf16 v[12:15], v[148:151], v[172:175], v[12:15]\n\t"
        "ds_read_b64_tr_b16 v[146:147], v226 offset:13312\n\t"
        "s_and_b32 s45, s45, 3\n\t"
        "s_waitcnt lgkmcnt(10)\n\t"
        "v_mfma_f32_16x16x32_bf16 v[16:19], v[152:155], v[168:171], v[16:19]\n\t"
        "ds_read_b64_tr_b16 v[148:149], v227 offset:12288\n\t"
        "s_lshl_b32 s45, s45, 14\n\t"
        "v_mfma_f32_16x16x32_bf16 v[24:27], v[152:155], v[172:175], v[24:27]\n\t"
        "ds_read_b64_tr_b16 v[150:151], v227 offset:13312\n\t"
        "s_lshr_b32 s51, s47, 31\n\t"
        "v_mfma_f32_16x16x32_bf16 v[20:23], v[156:159], v[168:171], v[20:23]\n\t"
        "ds_read_b64_tr_b16 v[152:153], v228 offset:12288\n\t"
        "buffer_load_dwordx4 v[220:223], v224, s[40:43], s44 offen offset:2304 sc1\n\t"
        "v_mfma_f32_16x16x32_bf16 v[28:31], v[156:159], v[172:175], v[28:31]\n\t"
        "ds_read_b64_tr_b16 v[154:155], v228 offset:13312\n\t"
        "s_lshl_b32 s51, s51, 31\n\t"
        "s_waitcnt vmcnt(10) lgkmcnt(10)\n\t"
        "v_mfma_f32_16x16x32_bf16 v[0:3], v[128:131], v[176:179], v[0:3]\n\t"
        "ds_read_b64_tr_b16 v[156:157], v229 offset:12288\n\t"
        "v_or_b32 v225, s51, v224\n\t"
        "v_mfma_f32_16x16x32_bf16 v[8:11], v[128:131], v[180:183], v[8:11]\n\t"
        "ds_read_b64_tr_b16 v[158:159], v229 offset:13312\n\t"
        "s_and_b32 s51, s47, 3\n\t"
        "v_mfma_f32_16x16x32_bf16 v[4:7], v[132:135], v[176:179], v[4:7]\n\t"
        "ds_read_b64_tr_b16 v[128:129], v226 offset:16384\n\t"
        "buffer_load_dwordx4 v[160:163], v225, s[40:43], s45 offen offset:0 sc1\n\t"
        "v_mfma_f32_16x16x32_bf16 v[12:15], v[132:135], v[180:183], v[12:15]\n\t"
        "ds_read_b64_tr_b16 v[130:131], v226 offset:17408\n\t"
        "s_lshl_b32 s51, s51, 15\n\t"
        "s_waitcnt lgkmcnt(10)\n\t"
        "v_mfma_f32_16x16x32_bf16 v[16:19], v[136:139], v[176:179], v[16:19]\n\t"
        "ds_read_b64_tr_b16 v[132:133], v227 offset:16384\n\t"
        "v_add_u32 v234, s51, v226\n\t"
        "v_mfma_f32_16x16x32_bf16 v[24:27], v[136:139], v[180:183], v[24:27]\n\t"
        "ds_read_b64_tr_b16 v[134:135], v227 offset:17408\n\t"
        "v_add_u32 v235, s51, v227\n\t"
        "v_mfma_f32_16x16x32_bf16 v[20:23], v[140:143], v[176:179], v[20:23]\n\t"
        "ds_read_b64_tr_b16 v[136:137], v228 offset:16384\n\t"
        "buffer_load_dwordx4 v[164:167], v225, s[40:43], s45 offen offset:256 sc1\n\t"
        "v_mfma_f32_16x16x32_bf16 v[28:31], v[140:143], v[180:183], v[28:31]\n\t"
        "ds_read_b64_tr_b16 v[138:139], v228 offset:17408\n\t"
        "v_add_u32 v236, s51, v228\n\t"
        "s_waitcnt vmcnt(10) lgkmcnt(10)\n\t"
        "v_mfma_f32_16x16x32_bf16 v[0:3], v[144:147], v[184:187], v[0:3]\n\t"
        "ds_read_b64_tr_b16 v[140:141], v229 offset:16384\n\t"
        "v_add_u32 v237, s51, v229\n\t"
        "v_mfma_f32_16x16x32_bf16 v[8:11], v[144:147], v[188:191], v[8:11]\n\t"
        "ds_read_b64_tr_b16 v[142:143], v229 offset:17408\n\t"
        "v_mfma_f32_16x16x32_bf16 v[4:7], v[148:151], v[184:187], v[4:7]\n\t"
        "ds_read_b64_tr_b16 v[144:145], v226 offset:20480\n\t"
        "buffer_load_dwordx4 v[168:171], v225, s[40:43], s45 offen offset:2048 sc1\n\t"
        "v_mfma_f32_16x16x32_bf16 v[12:15], v[148:151], v[188:191], v[12:15]\n\t"
        "ds_read_b64_tr_b16 v[146:147], v226 offset:21504\n\t"
        "s_waitcnt lgkmcnt(10)\n\t"
        "v_mfma_f32_16x16x32_bf16 v[16:19], v[152:155], v[184:187], v[16:19]\n\t"
        "ds_read_b64_tr_b16 v[148:149], v227 offset:20480\n\t"
        "v_mfma_f32_16x16x32_bf16 v[24:27], v[152:155], v[188:191], v[24:27]\n\t"
        "ds_read_b64_tr_b16 v[150:151], v227 offset:21504\n\t"
        "v_mfma_f32_16x16x32_bf16 v[20:23], v[156:159], v[184:187], v[20:23]\n\t"
        "ds_read_b64_tr_b16 v[152:153], v228 offset:20480\n\t"
        "buffer_load_dwordx4 v[172:175], v225, s[40:43], s45 offen offset:2304 sc1\n\t"
        "v_mfma_f32_16x16x32_bf16 v[28:31], v[156:159], v[188:191], v[28:31]\n\t"
        "ds_read_b64_tr_b16 v[154:155], v228 offset:21504\n\t"
        "s_waitcnt vmcnt(10) lgkmcnt(10)\n\t"
        "v_mfma_f32_16x16x32_bf16 v[0:3], v[128:131], v[192:195], v[0:3]\n\t"
        "ds_read_b64_tr_b16 v[156:157], v229 offset:20480\n\t"
        "v_mfma_f32_16x16x32_bf16 v[8:11], v[128:131], v[196:199], v[8:11]\n\t"
        "ds_read_b64_tr_b16 v[158:159], v229 offset:21504\n\t"
        "v_mfma_f32_16x16x32_bf16 v[4:7], v[132:135], v[192:195], v[4:7]\n\t"
        "ds_read_b64_tr_b16 v[128:129], v226 offset:24576\n\t"
        "s_addk_i32 s45, 0x1000\n\t"
        "buffer_load_dwordx4 v[176:179], v225, s[40:43], s45 offen offset:0 sc1\n\t"
        "v_mfma_f32_16x16x32_bf16 v[12:15], v[132:135], v[196:199], v[12:15]\n\t"
        "ds_read_b64_tr_b16 v[130:131], v226 offset:25600\n\t"
        "s_waitcnt lgkmcnt(10)\n\t"
        "v_mfma_f32_16x16x32_bf16 v[16:19], v[136:139], v[192:195], v[16:19]\n\t"
        "ds_read_b64_tr_b16 v[132:133], v227 offset:24576\n\t"
        "v_mfma_f32_16x16x32_bf16 v[24:27], v[136:139], v[196:199], v[24:27]\n\t"
        "ds_read_b64_tr_b16 v[134:135], v227 offset:25600\n\t"
        "v_mfma_f32_16x16x32_bf16 v[20:23], v[140:143], v[192:195], v[20:23]\n\t"
        "ds_read_b64_tr_b16 v[136:137], v228 offset:24576\n\t"
        "buffer_load_dwordx4 v[180:183], v225, s[40:43], s45 offen offset:256 sc1\n\t"
        "v_mfma_f32_16x16x32_bf16 v[28:31], v[140:143], v[196:199], v[28:31]\n\t"
        "ds_read_b64_tr_b16 v[138:139], v228 offset:25600\n\t"
        "s_waitcnt vmcnt(10) lgkmcnt(10)\n\t"
        "v_mfma_f32_16x16x32_bf16 v[0:3], v[144:147], v[200:203], v[0:3]\n\t"
        "ds_read_b64_tr_b16 v[140:141], v229 offset:24576\n\t"
        "v_mfma_f32_16x16x32_bf16 v[8:11], v[144:147], v[204:207], v[8:11]\n\t"
        "ds_read_b64_tr_b16 v[142:143], v229 offset:25600\n\t"
        "v_mfma_f32_16x16x32_bf16 v[4:7], v[148:151], v[200:203], v[4:7]\n\t"
        "ds_read_b64_tr_b16 v[144:145], v226 offset:28672\n\t"
        "buffer_load_dwordx4 v[184:187], v225, s[40:43], s45 offen offset:2048 sc1\n\t"
        "v_mfma_f32_16x16x32_bf16 v[12:15], v[148:151], v[204:207], v[12:15]\n\t"
        "ds_read_b64_tr_b16 v[146:147], v226 offset:29696\n\t"
        "s_waitcnt lgkmcnt(10)\n\t"
        "v_mfma_f32_16x16x32_bf16 v[16:19], v[152:155], v[200:203], v[16:19]\n\t"
        "ds_read_b64_tr_b16 v[148:149], v227 offset:28672\n\t"
        "v_mfma_f32_16x16x32_bf16 v[24:27], v[152:155], v[204:207], v[24:27]\n\t"
        "ds_read_b64_tr_b16 v[150:151], v227 offset:29696\n\t"
        "v_mfma_f32_16x16x32_bf16 v[20:23], v[156:159], v[200:203], v[20:23]\n\t"
        "ds_read_b64_tr_b16 v[152:153], v228 offset:28672\n\t"
        "buffer_load_dwordx4 v[188:191], v225, s[40:43], s45 offen offset:2304 sc1\n\t"
        "v_mfma_f32_16x16x32_bf16 v[28:31], v[156:159], v[204:207], v[28:31]\n\t"
        "ds_read_b64_tr_b16 v[154:155], v228 offset:29696\n\t"
        "s_waitcnt vmcnt(10) lgkmcnt(10)\n\t"
        "v_mfma_f32_16x16x32_bf16 v[0:3], v[128:131], v[208:211], v[0:3]\n\t"
        "ds_read_b64_tr_b16 v[156:157], v229 offset:28672\n\t"
        "v_mfma_f32_16x16x32_bf16 v[8:11], v[128:131], v[212:215], v[8:11]\n\t"
        "ds_read_b64_tr_b16 v[158:159], v229 offset:29696\n\t"
        "v_mfma_f32_16x16x32_bf16 v[4:7], v[132:135], v[208:211], v[4:7]\n\t"
        "ds_read_b64_tr_b16 v[128:129], v234 offset:0\n\t"
        "s_addk_i32 s45, 0x1000\n\t"
        "buffer_load_dwordx4 v[192:195], v225, s[40:43], s45 offen offset:0 sc1\n\t"
        "v_mfma_f32_16x16x32_bf16 v[12:15], v[132:135], v[212:215], v[12:15]\n\t"
        "ds_read_b64_tr_b16 v[130:131], v234 offset:1024\n\t"
        "s_waitcnt lgkmcnt(10)\n\t"
        "v_mfma_f32_16x16x32_bf16 v[16:19], v[136:139], v[208:211], v[16:19]\n\t"
        "ds_read_b64_tr_b16 v[132:133], v235 offset:0\n\t"
        "v_mfma_f32_16x16x32_bf16 v[24:27], v[136:139], v[212:215], v[24:27]\n\t"
        "ds_read_b64_tr_b16 v[134:135], v235 offset:1024\n\t"
        "v_mfma_f32_16x16x32_bf16 v[20:23], v[140:143], v[208:211], v[20:23]\n\t"
        "ds_read_b64_tr_b16 v[136:137], v236 offset:0\n\t"
        "buffer_load_dwordx4 v[196:199], v225, s[40:43], s45 offen offset:256 sc1\n\t"
        "v_mfma_f32_16x16x32_bf16 v[28:31], v[140:143], v[212:215], v[28:31]\n\t"
        "ds_read_b64_tr_b16 v[138:139], v236 offset:1024\n\t"
        "s_waitcnt vmcnt(10) lgkmcnt(10)\n\t"
        "v_mfma_f32_16x16x32_bf16 v[0:3], v[144:147], v[216:219], v[0:3]\n\t"
        "ds_read_b64_tr_b16 v[140:141], v237 offset:0\n\t"
        "v_mfma_f32_16x16x32_bf16 v[8:11], v[144:147], v[220:223], v[8:11]\n\t"
        "ds_read_b64_tr_b16 v[142:143], v237 offset:1024\n\t"
        "v_mfma_f32_16x16x32_bf16 v[4:7], v[148:151], v[216:219], v[4:7]\n\t"
        "ds_read_b64_tr_b16 v[144:145], v234 offset:4096\n\t"
        "buffer_load_dwordx4 v[200:203], v225, s[40:43], s45 offen offset:2048 sc1\n\t"
        "v_mfma_f32_16x16x32_bf16 v[12:15], v[148:151], v[220:223], v[12:15]\n\t"
        "ds_read_b64_tr_b16 v[146:147], v234 offset:5120\n\t"
        "s_waitcnt lgkmcnt(10)\n\t"
        "v_mfma_f32_16x16x32_bf16 v[16:19], v[152:155], v[216:219], v[16:19]\n\t"
        "ds_read_b64_tr_b16 v[148:149], v235 offset:4096\n\t"
        "v_mfma_f32_16x16x32_bf16 v[24:27], v[152:155], v[220:223], v[24:27]\n\t"
        "ds_read_b64_tr_b16 v[150:151], v235 offset:5120\n\t"
        "v_mfma_f32_16x16x32_bf16 v[20:23], v[156:159], v[216:219], v[20:23]\n\t"
        "ds_read_b64_tr_b16 v[152:153], v236 offset:4096\n\t"
        "buffer_load_dwordx4 v[204:207], v225, s[40:43], s45 offen offset:2304 sc1\n\t"
        "v_mfma_f32_16x16x32_bf16 v[28:31], v[156:159], v[220:223], v[28:31]\n\t"
        "ds_read_b64_tr_b16 v[154:155], v236 offset:5120\n\t"
        "s_mov_b32 s44, s45\n\t"
        "s_cmp_eq_u32 s47, 0\n\t"
        "s_cbranch_scc1 oseg0_%=\n\t"
        "s_cmp_eq_u32 s47, 1\n\t"
        "s_cbranch_scc1 oseg1_%=\n\t"
        "s_cmp_eq_u32 s47, 2\n\t"
        "s_cbranch_scc1 oseg2_%=\n\t"
        "s_cmp_eq_u32 s47, 3\n\t"
        "s_cbranch_scc1 oseg3_%=\n\t"
        "s_cmp_lg_u32 s46, 15\n\t"
        "s_cbranch_scc1 ostart_%=\n\t"
        "s_branch oend_%=\n\t"
        "oseg1_%=:\n\t"
        "s_or_b32 s46, s46, 2\n\t"
        "s_waitcnt vmcnt(10) lgkmcnt(10)\n\t"
        "v_mfma_f32_16x16x32_bf16 v[32:35], v[128:131], v[160:163], 0\n\t"
        "ds_read_b64_tr_b16 v[156:157], v229 offset:36864\n\t"
        "ds_read_b32 v239, v238\n\t"
        "v_mfma_f32_16x16x32_bf16 v[40:43], v[128:131], v[164:167], 0\n\t"
        "ds_read_b64_tr_b16 v[158:159], v229 offset:37888\n\t"
        "v_mfma_f32_16x16x32_bf16 v[36:39], v[132:135], v[160:163], 0\n\t"
        "ds_read_b64_tr_b16 v[128:129], v226 offset:40960\n\t"
        "s_addk_i32 s44, 0x1000\n\t"
        "buffer_load_dwordx4 v[208:211], v224, s[40:43], s44 offen offset:0 sc1\n\t"
        "v_mfma_f32_16x16x32_bf16 v[44:47], v[132:135], v[164:167], 0\n\t"
        "ds_read_b64_tr_b16 v[130:131], v226 offset:41984\n\t"
        "s_waitcnt lgkmcnt(3)\n\t"
        "v_mfma_f32_16x16x32_bf16 v[48:51], v[136:139], v[160:163], 0\n\t"
        "ds_read_b64_tr_b16 v[132:133], v227 offset:40960\n\t"
        "v_readfirstlane_b32 s51, v239\n\t"
        "v_mfma_f32_16x16x32_bf16 v[56:59], v[136:139], v[164:167], 0\n\t"
        "ds_read_b64_tr_b16 v[134:135], v227 offset:41984\n\t"
        "v_mfma_f32_16x16x32_bf16 v[52:55], v[140:143], v[160:163], 0\n\t"
        "ds_read_b64_tr_b16 v[136:137], v228 offset:40960\n\t"
        "buffer_load_dwordx4 v[212:215], v224, s[40:43], s44 offen offset:256 sc1\n\t"
        "v_mfma_f32_16x16x32_bf16 v[60:63], v[140:143], v[164:167], 0\n\t"
        "ds_read_b64_tr_b16 v[138:139], v228 offset:41984\n\t"
        "s_andn2_b32 s51, s51, s46\n\t"
        "s_waitcnt vmcnt(10) lgkmcnt(11)\n\t"
        "v_mfma_f32_16x16x32_bf16 v[32:35], v[144:147], v[168:171], v[32:35]\n\t"
        "ds_read_b64_tr_b16 v[140:141], v229 offset:40960\n\t"
        "s_ff1_i32_b32 s47, s51\n\t"
        "v_mfma_f32_16x16x32_bf16 v[40:43], v[144:147], v[172:175], v[40:43]\n\t"
        "ds_read_b64_tr_b16 v[142:143], v229 offset:41984\n\t"
        "s_add_u32 s45, s47, s49\n\t"
        "v_mfma_f32_16x16x32_bf16 v[36:39], v[148:151], v[168:171], v[36:39]\n\t"
        "ds_read_b64_tr_b16 v[144:145], v226 offset:45056\n\t"
        "buffer_load_dwordx4 v[216:219], v224, s[40:43], s44 offen offset:2048 sc1\n\t"
        "v_mfma_f32_16x16x32_bf16 v[44:47], v[148:151], v[172:175], v[44:47]\n\t"
        "ds_read_b64_tr_b16 v[146:147], v226 offset:46080\n\t"
        "s_and_b32 s45, s45, 3\n\t"
        "s_waitcnt lgkmcnt(10)\n\t"
        "v_mfma_f32_16x16x32_bf16 v[48:51], v[152:155], v[168:171], v[48:51]\n\t"
        "ds_read_b64_tr_b16 v[148:149], v227 offset:45056\n\t"
        "s_lshl_b32 s45, s45, 14\n\t"
        "v_mfma_f32_16x16x32_bf16 v[56:59], v[152:155], v[172:175], v[56:59]\n\t"
        "ds_read_b64_tr_b16 v[150:151], v227 offset:46080\n\t"
        "s_lshr_b32 s51, s47, 31\n\t"
        "v_mfma_f32_16x16x32_bf16 v[52:55], v[156:159], v[168:171], v[52:55]\n\t"
        "ds_read_b64_tr_b16 v[152:153], v228 offset:45056\n\t"
        "buffer_load_dwordx4 v[220:223], v224, s[40:43], s44 offen offset:2304 sc1\n\t"
        "v_mfma_f32_16x16x32_bf16 v[60:63], v[156:159], v[172:175], v[60:63]\n\t"
        "ds_read_b64_tr_b16 v[154:155], v228 offset:46080\n\t"
        "s_lshl_b32 s51, s51, 31\n\t"
        "s_waitcnt vmcnt(10) lgkmcnt(10)\n\t"
        "v_mfma_f32_16x16x32_bf16 v[32:35], v[128:131], v[176:179], v[32:35]\n\t"
        "ds_read_b64_tr_b16 v[156:157], v229 offset:45056\n\t"
        "v_or_b32 v225, s51, v224\n\t"
        "v_mfma_f32_16x16x32_bf16 v[40:43], v[128:131], v[180:183], v[40:43]\n\t"
        "ds_read_b64_tr_b16 v[158:159], v229 offset:46080\n\t"
        "s_and_b32 s51, s47, 3\n\t"
        "v_mfma_f32_16x16x32_bf16 v[36:39], v[132:135], v[176:179], v[36:39]\n\t"
        "ds_read_b64_tr_b16 v[128:129], v226 offset:49152\n\t"
        "buffer_load_dwordx4 v[160:163], v225, s[40:43], s45 offen offset:0 sc1\n\t"
        "v_mfma_f32_16x16x32_bf16 v[44:47], v[132:135], v[180:183], v[44:47]\n\t"
        "ds_read_b64_tr_b16 v[130:131], v226 offset:50176\n\t"
        "s_lshl_b32 s51, s51, 15\n\t"
        "s_waitcnt lgkmcnt(10)\n\t"
        "v_mfma_f32_16x16x32_bf16 v[48:51], v[136:139], v[176:179], v[48:51]\n\t"
        "ds_read_b64_tr_b16 v[132:133], v227 offset:49152\n\t"
        "v_add_u32 v234, s51, v226\n\t"
        "v_mfma_f32_16x16x32_bf16 v[56:59], v[136:139], v[180:183], v[56:59]\n\t"
        "ds_read_b64_tr_b16 v[134:135], v227 offset:50176\n\t"
        "v_add_u32 v235, s51, v227\n\t"
        "v_mfma_f32_16x16x32_bf16 v[52:55], v[140:143], v[176:179], v[52:55]\n\t"
        "ds_read_b64_tr_b16 v[136:137], v228 offset:49152\n\t"
        "buffer_load_dwordx4 v[164:167], v225, s[40:43], s45 offen offset:256 sc1\n\t"
        "v_mfma_f32_16x16x32_bf16 v[60:63], v[140:143], v[180:183], v[60:63]\n\t"
        "ds_read_b64_tr_b16 v[138:139], v228 offset:50176\n\t"
        "v_add_u32 v236, s51, v228\n\t"
        "s_waitcnt vmcnt(10) lgkmcnt(10)\n\t"
        "v_mfma_f32_16x16x32_bf16 v[32:35], v[144:147], v[184:187], v[32:35]\n\t"
        "ds_read_b64_tr_b16 v[140:141], v229 offset:49152\n\t"
        "v_add_u32 v237, s51, v229\n\t"
        "v_mfma_f32_16x16x32_bf16 v[40:43], v[144:147], v[188:191], v[40:43]\n\t"
        "ds_read_b64_tr_b16 v[142:143], v229 offset:50176\n\t"
        "v_mfma_f32_16x16x32_bf16 v[36:39], v[148:151], v[184:187], v[36:39]\n\t"
        "ds_read_b64_tr_b16 v[144:145], v226 offset:53248\n\t"
        "buffer_load_dwordx4 v[168:171], v225, s[40:43], s45 offen offset:2048 sc1\n\t"
        "v_mfma_f32_16x16x32_bf16 v[44:47], v[148:151], v[188:191], v[44:47]\n\t"
        "ds_read_b64_tr_b16 v[146:147], v226 offset:54272\n\t"
        "s_waitcnt lgkmcnt(10)\n\t"
        "v_mfma_f32_16x16x32_bf16 v[48:51], v[152:155], v[184:187], v[48:51]\n\t"
        "ds_read_b64_tr_b16 v[148:149], v227 offset:53248\n\t"
        "v_mfma_f32_16x16x32_bf16 v[56:59], v[152:155], v[188:191], v[56:59]\n\t"
        "ds_read_b64_tr_b16 v[150:151], v227 offset:54272\n\t"
        "v_mfma_f32_16x16x32_bf16 v[52:55], v[156:159], v[184:187], v[52:55]\n\t"
        "ds_read_b64_tr_b16 v[152:153], v228 offset:53248\n\t"
        "buffer_load_dwordx4 v[172:175], v225, s[40:43], s45 offen offset:2304 sc1\n\t"
        "v_mfma_f32_16x16x32_bf16 v[60:63], v[156:159], v[188:191], v[60:63]\n\t"
        "ds_read_b64_tr_b16 v[154:155], v228 offset:54272\n\t"
        "s_waitcnt vmcnt(10) lgkmcnt(10)\n\t"
        "v_mfma_f32_16x16x32_bf16 v[32:35], v[128:131], v[192:195], v[32:35]\n\t"
        "ds_read_b64_tr_b16 v[156:157], v229 offset:53248\n\t"
        "v_mfma_f32_16x16x32_bf16 v[40:43], v[128:131], v[196:199], v[40:43]\n\t"
        "ds_read_b64_tr_b16 v[158:159], v229 offset:54272\n\t"
        "v_mfma_f32_16x16x32_bf16 v[36:39], v[132:135], v[192:195], v[36:39]\n\t"
        "ds_read_b64_tr_b16 v[128:129], v226 offset:57344\n\t"
        "s_addk_i32 s45, 0x1000\n\t"
        "buffer_load_dwordx4 v[176:179], v225, s[40:43], s45 offen offset:0 sc1\n\t"
        "v_mfma_f32_16x16x32_bf16 v[44:47], v[132:135], v[196:199], v[44:47]\n\t"
        "ds_read_b64_tr_b16 v[130:131], v226 offset:58368\n\t"
        "s_waitcnt lgkmcnt(10)\n\t"
        "v_mfma_f32_16x16x32_bf16 v[48:51], v[136:139], v[192:195], v[48:51]\n\t"
        "ds_read_b64_tr_b16 v[132:133], v227 offset:57344\n\t"
        "v_mfma_f32_16x16x32_bf16 v[56:59], v[136:139], v[196:199], v[56:59]\n\t"
        "ds_read_b64_tr_b16 v[134:135], v227 offset:58368\n\t"
        "v_mfma_f32_16x16x32_bf16 v[52:55], v[140:143], v[192:195], v[52:55]\n\t"
        "ds_read_b64_tr_b16 v[136:137], v228 offset:57344\n\t"
        "buffer_load_dwordx4 v[180:183], v225, s[40:43], s45 offen offset:256 sc1\n\t"
        "v_mfma_f32_16x16x32_bf16 v[60:63], v[140:143], v[196:199], v[60:63]\n\t"
        "ds_read_b64_tr_b16 v[138:139], v228 offset:58368\n\t"
        "s_waitcnt vmcnt(10) lgkmcnt(10)\n\t"
        "v_mfma_f32_16x16x32_bf16 v[32:35], v[144:147], v[200:203], v[32:35]\n\t"
        "ds_read_b64_tr_b16 v[140:141], v229 offset:57344\n\t"
        "v_mfma_f32_16x16x32_bf16 v[40:43], v[144:147], v[204:207], v[40:43]\n\t"
        "ds_read_b64_tr_b16 v[142:143], v229 offset:58368\n\t"
        "v_mfma_f32_16x16x32_bf16 v[36:39], v[148:151], v[200:203], v[36:39]\n\t"
        "ds_read_b64_tr_b16 v[144:145], v226 offset:61440\n\t"
        "buffer_load_dwordx4 v[184:187], v225, s[40:43], s45 offen offset:2048 sc1\n\t"
        "v_mfma_f32_16x16x32_bf16 v[44:47], v[148:151], v[204:207], v[44:47]\n\t"
        "ds_read_b64_tr_b16 v[146:147], v226 offset:62464\n\t"
        "s_waitcnt lgkmcnt(10)\n\t"
        "v_mfma_f32_16x16x32_bf16 v[48:51], v[152:155], v[200:203], v[48:51]\n\t"
        "ds_read_b64_tr_b16 v[148:149], v227 offset:61440\n\t"
        "v_mfma_f32_16x16x32_bf16 v[56:59], v[152:155], v[204:207], v[56:59]\n\t"
        "ds_read_b64_tr_b16 v[150:151], v227 offset:62464\n\t"
        "v_mfma_f32_16x16x32_bf16 v[52:55], v[156:159], v[200:203], v[52:55]\n\t"
        "ds_read_b64_tr_b16 v[152:153], v228 offset:61440\n\t"
        "buffer_load_dwordx4 v[188:191], v225, s[40:43], s45 offen offset:2304 sc1\n\t"
        "v_mfma_f32_16x16x32_bf16 v[60:63], v[156:159], v[204:207], v[60:63]\n\t"
        "ds_read_b64_tr_b16 v[154:155], v228 offset:62464\n\t"
        "s_waitcnt vmcnt(10) lgkmcnt(10)\n\t"
        "v_mfma_f32_16x16x32_bf16 v[32:35], v[128:131], v[208:211], v[32:35]\n\t"
        "ds_read_b64_tr_b16 v[156:157], v229 offset:61440\n\t"
        "v_mfma_f32_16x16x32_bf16 v[40:43], v[128:131], v[212:215], v[40:43]\n\t"
        "ds_read_b64_tr_b16 v[158:159], v229 offset:62464\n\t"
        "v_mfma_f32_16x16x32_bf16 v[36:39], v[132:135], v[208:211], v[36:39]\n\t"
        "ds_read_b64_tr_b16 v[128:129], v234 offset:0\n\t"
        "s_addk_i32 s45, 0x1000\n\t"
        "buffer_load_dwordx4 v[192:195], v225, s[40:43], s45 offen offset:0 sc1\n\t"
        "v_mfma_f32_16x16x32_bf16 v[44:47], v[132:135], v[212:215], v[44:47]\n\t"
        "ds_read_b64_tr_b16 v[130:131], v234 offset:1024\n\t"
        "s_waitcnt lgkmcnt(10)\n\t"
        "v_mfma_f32_16x16x32_bf16 v[48:51], v[136:139], v[208:211], v[48:51]\n\t"
        "ds_read_b64_tr_b16 v[132:133], v235 offset:0\n\t"
        "v_mfma_f32_16x16x32_bf16 v[56:59], v[136:139], v[212:215], v[56:59]\n\t"
        "ds_read_b64_tr_b16 v[134:135], v235 offset:1024\n\t"
        "v_mfma_f32_16x16x32_bf16 v[52:55], v[140:143], v[208:211], v[52:55]\n\t"
        "ds_read_b64_tr_b16 v[136:137], v236 offset:0\n\t"
        "buffer_load_dwordx4 v[196:199], v225, s[40:43], s45 offen offset:256 sc1\n\t"
        "v_mfma_f32_16x16x32_bf16 v[60:63], v[140:143], v[212:215], v[60:63]\n\t"
        "ds_read_b64_tr_b16 v[138:139], v236 offset:1024\n\t"
        "s_waitcnt vmcnt(10) lgkmcnt(10)\n\t"
        "v_mfma_f32_16x16x32_bf16 v[32:35], v[144:147], v[216:219], v[32:35]\n\t"
        "ds_read_b64_tr_b16 v[140:141], v237 offset:0\n\t"
        "v_mfma_f32_16x16x32_bf16 v[40:43], v[144:147], v[220:223], v[40:43]\n\t"
        "ds_read_b64_tr_b16 v[142:143], v237 offset:1024\n\t"
        "v_mfma_f32_16x16x32_bf16 v[36:39], v[148:151], v[216:219], v[36:39]\n\t"
        "ds_read_b64_tr_b16 v[144:145], v234 offset:4096\n\t"
        "buffer_load_dwordx4 v[200:203], v225, s[40:43], s45 offen offset:2048 sc1\n\t"
        "v_mfma_f32_16x16x32_bf16 v[44:47], v[148:151], v[220:223], v[44:47]\n\t"
        "ds_read_b64_tr_b16 v[146:147], v234 offset:5120\n\t"
        "s_waitcnt lgkmcnt(10)\n\t"
        "v_mfma_f32_16x16x32_bf16 v[48:51], v[152:155], v[216:219], v[48:51]\n\t"
        "ds_read_b64_tr_b16 v[148:149], v235 offset:4096\n\t"
        "v_mfma_f32_16x16x32_bf16 v[56:59], v[152:155], v[220:223], v[56:59]\n\t"
        "ds_read_b64_tr_b16 v[150:151], v235 offset:5120\n\t"
        "v_mfma_f32_16x16x32_bf16 v[52:55], v[156:159], v[216:219], v[52:55]\n\t"
        "ds_read_b64_tr_b16 v[152:153], v236 offset:4096\n\t"
        "buffer_load_dwordx4 v[204:207], v225, s[40:43], s45 offen offset:2304 sc1\n\t"
        "v_mfma_f32_16x16x32_bf16 v[60:63], v[156:159], v[220:223], v[60:63]\n\t"
        "ds_read_b64_tr_b16 v[154:155], v236 offset:5120\n\t"
        "s_mov_b32 s44, s45\n\t"
        "s_cmp_eq_u32 s47, 0\n\t"
        "s_cbranch_scc1 oseg0_%=\n\t"
        "s_cmp_eq_u32 s47, 1\n\t"
        "s_cbranch_scc1 oseg1_%=\n\t"
        "s_cmp_eq_u32 s47, 2\n\t"
        "s_cbranch_scc1 oseg2_%=\n\t"
        "s_cmp_eq_u32 s47, 3\n\t"
        "s_cbranch_scc1 oseg3_%=\n\t"
        "s_cmp_lg_u32 s46, 15\n\t"
        "s_cbranch_scc1 ostart_%=\n\t"
        "s_branch oend_%=\n\t"
        "oseg2_%=:\n\t"
        "s_or_b32 s46, s46, 4\n\t"
        "s_waitcnt vmcnt(10) lgkmcnt(10)\n\t"
        "v_mfma_f32_16x16x32_bf16 v[64:67], v[128:131], v[160:163], 0\n\t"
        "ds_read_b64_tr_b16 v[156:157], v233 offset:4096\n\t"
        "ds_read_b32 v239, v238\n\t"
        "v_mfma_f32_16x16x32_bf16 v[72:75], v[128:131], v[164:167], 0\n\t"
        "ds_read_b64_tr_b16 v[158:159], v233 offset:5120\n\t"
        "v_mfma_f32_16x16x32_bf16 v[68:71], v[132:135], v[160:163], 0\n\t"
        "ds_read_b64_tr_b16 v[128:129], v230 offset:8192\n\t"
        "s_addk_i32 s44, 0x1000\n\t"
        "buffer_load_dwordx4 v[208:211], v224, s[40:43], s44 offen offset:0 sc1\n\t"
        "v_mfma_f32_16x16x32_bf16 v[76:79], v[132:135], v[164:167], 0\n\t"
        "ds_read_b64_tr_b16 v[130:131], v230 offset:9216\n\t"
        "s_waitcnt lgkmcnt(3)\n\t"
        "v_mfma_f32_16x16x32_bf16 v[80:83], v[136:139], v[160:163], 0\n\t"
        "ds_read_b64_tr_b16 v[132:133], v231 offset:8192\n\t"
        "v_readfirstlane_b32 s51, v239\n\t"
        "v_mfma_f32_16x16x32_bf16 v[88:91], v[136:139], v[164:167], 0\n\t"
        "ds_read_b64_tr_b16 v[134:135], v231 offset:9216\n\t"
        "v_mfma_f32_16x16x32_bf16 v[84:87], v[140:143], v[160:163], 0\n\t"
        "ds_read_b64_tr_b16 v[136:137], v232 offset:8192\n\t"
        "buffer_load_dwordx4 v[212:215], v224, s[40:43], s44 offen offset:256 sc1\n\t"
        "v_mfma_f32_16x16x32_bf16 v[92:95], v[140:143], v[164:167], 0\n\t"
        "ds_read_b64_tr_b16 v[138:139], v232 offset:9216\n\t"
        "s_andn2_b32 s51, s51, s46\n\t"
        "s_waitcnt vmcnt(10) lgkmcnt(11)\n\t"
        "v_mfma_f32_16x16x32_bf16 v[64:67], v[144:147], v[168:171], v[64:67]\n\t"
        "ds_read_b64_tr_b16 v[140:141], v233 offset:8192\n\t"
        "s_ff1_i32_b32 s47, s51\n\t"
        "v_mfma_f32_16x16x32_bf16 v[72:75], v[144:147], v[172:175], v[72:75]\n\t"
        "ds_read_b64_tr_b16 v[142:143], v233 offset:9216\n\t"
        "s_add_u32 s45, s47, s49\n\t"
        "v_mfma_f32_16x16x32_bf16 v[68:71], v[148:151], v[168:171], v[68:71]\n\t"
        "ds_read_b64_tr_b16 v[144:145], v230 offset:12288\n\t"
        "buffer_load_dwordx4 v[216:219], v224, s[40:43], s44 offen offset:2048 sc1\n\t"
        "v_mfma_f32_16x16x32_bf16 v[76:79], v[148:151], v[172:175], v[76:79]\n\t"
        "ds_read_b64_tr_b16 v[146:147], v230 offset:13312\n\t"
        "s_and_b32 s45, s45, 3\n\t"
        "s_waitcnt lgkmcnt(10)\n\t"
        "v_mfma_f32_16x16x32_bf16 v[80:83], v[152:155], v[168:171], v[80:83]\n\t"
        "ds_read_b64_tr_b16 v[148:149], v231 offset:12288\n\t"
        "s_lshl_b32 s45, s45, 14\n\t"
        "v_mfma_f32_16x16x32_bf16 v[88:91], v[152:155], v[172:175], v[88:91]\n\t"
        "ds_read_b64_tr_b16 v[150:151], v231 offset:13312\n\t"
        "s_lshr_b32 s51, s47, 31\n\t"
        "v_mfma_f32_16x16x32_bf16 v[84:87], v[156:159], v[168:171], v[84:87]\n\t"
        "ds_read_b64_tr_b16 v[152:153], v232 offset:12288\n\t"
        "buffer_load_dwordx4 v[220:223], v224, s[40:43], s44 offen offset:2304 sc1\n\t"
        "v_mfma_f32_16x16x32_bf16 v[92:95], v[156:159], v[172:175], v[92:95]\n\t"
        "ds_read_b64_tr_b16 v[154:155], v232 offset:13312\n\t"
        "s_lshl_b32 s51, s51, 31\n\t"
        "s_waitcnt vmcnt(10) lgkmcnt(10)\n\t"
        "v_mfma_f32_16x16x32_bf16 v[64:67], v[128:131], v[176:179], v[64:67]\n\t"
        "ds_read_b64_tr_b16 v[156:157], v233 offset:12288\n\t"
        "v_or_b32 v225, s51, v224\n\t"
        "v_mfma_f32_16x16x32_bf16 v[72:75], v[128:131], v[180:183], v[72:75]\n\t"
        "ds_read_b64_tr_b16 v[158:159], v233 offset:13312\n\t"
        "s_and_b32 s51, s47, 3\n\t"
        "v_mfma_f32_16x16x32_bf16 v[68:71], v[132:135], v[176:179], v[68:71]\n\t"
        "ds_read_b64_tr_b16 v[128:129], v230 offset:16384\n\t"
        "buffer_load_dwordx4 v[160:163], v225, s[40:43], s45 offen offset:0 sc1\n\t"
        "v_mfma_f32_16x16x32_bf16 v[76:79], v[132:135], v[180:183], v[76:79]\n\t"
        "ds_read_b64_tr_b16 v[130:131], v230 offset:17408\n\t"
        "s_lshl_b32 s51, s51, 15\n\t"
        "s_waitcnt lgkmcnt(10)\n\t"
        "v_mfma_f32_16x16x32_bf16 v[80:83], v[136:139], v[176:179], v[80:83]\n\t"
        "ds_read_b64_tr_b16 v[132:133], v231 offset:16384\n\t"
        "v_add_u32 v234, s51, v226\n\t"
        "v_mfma_f32_16x16x32_bf16 v[88:91], v[136:139], v[180:183], v[88:91]\n\t"
        "ds_read_b64_tr_b16 v[134:135], v231 offset:17408\n\t"
        "v_add_u32 v235, s51, v227\n\t"
        "v_mfma_f32_16x16x32_bf16 v[84:87], v[140:143], v[176:179], v[84:87]\n\t"
        "ds_read_b64_tr_b16 v[136:137], v232 offset:16384\n\t"
        "buffer_load_dwordx4 v[164:167], v225, s[40:43], s45 offen offset:256 sc1\n\t"
        "v_mfma_f32_16x16x32_bf16 v[92:95], v[140:143], v[180:183], v[92:95]\n\t"
        "ds_read_b64_tr_b16 v[138:139], v232 offset:17408\n\t"
        "v_add_u32 v236, s51, v228\n\t"
        "s_waitcnt vmcnt(10) lgkmcnt(10)\n\t"
        "v_mfma_f32_16x16x32_bf16 v[64:67], v[144:147], v[184:187], v[64:67]\n\t"
        "ds_read_b64_tr_b16 v[140:141], v233 offset:16384\n\t"
        "v_add_u32 v237, s51, v229\n\t"
        "v_mfma_f32_16x16x32_bf16 v[72:75], v[144:147], v[188:191], v[72:75]\n\t"
        "ds_read_b64_tr_b16 v[142:143], v233 offset:17408\n\t"
        "v_mfma_f32_16x16x32_bf16 v[68:71], v[148:151], v[184:187], v[68:71]\n\t"
        "ds_read_b64_tr_b16 v[144:145], v230 offset:20480\n\t"
        "buffer_load_dwordx4 v[168:171], v225, s[40:43], s45 offen offset:2048 sc1\n\t"
        "v_mfma_f32_16x16x32_bf16 v[76:79], v[148:151], v[188:191], v[76:79]\n\t"
        "ds_read_b64_tr_b16 v[146:147], v230 offset:21504\n\t"
        "s_waitcnt lgkmcnt(10)\n\t"
        "v_mfma_f32_16x16x32_bf16 v[80:83], v[152:155], v[184:187], v[80:83]\n\t"
        "ds_read_b64_tr_b16 v[148:149], v231 offset:20480\n\t"
        "v_mfma_f32_16x16x32_bf16 v[88:91], v[152:155], v[188:191], v[88:91]\n\t"
        "ds_read_b64_tr_b16 v[150:151], v231 offset:21504\n\t"
        "v_mfma_f32_16x16x32_bf16 v[84:87], v[156:159], v[184:187], v[84:87]\n\t"
        "ds_read_b64_tr_b16 v[152:153], v232 offset:20480\n\t"
        "buffer_load_dwordx4 v[172:175], v225, s[40:43], s45 offen offset:2304 sc1\n\t"
        "v_mfma_f32_16x16x32_bf16 v[92:95], v[156:159], v[188:191], v[92:95]\n\t"
        "ds_read_b64_tr_b16 v[154:155], v232 offset:21504\n\t"
        "s_waitcnt vmcnt(10) lgkmcnt(10)\n\t"
        "v_mfma_f32_16x16x32_bf16 v[64:67], v[128:131], v[192:195], v[64:67]\n\t"
        "ds_read_b64_tr_b16 v[156:157], v233 offset:20480\n\t"
        "v_mfma_f32_16x16x32_bf16 v[72:75], v[128:131], v[196:199], v[72:75]\n\t"
        "ds_read_b64_tr_b16 v[158:159], v233 offset:21504\n\t"
        "v_mfma_f32_16x16x32_bf16 v[68:71], v[132:135], v[192:195], v[68:71]\n\t"
        "ds_read_b64_tr_b16 v[128:129], v230 offset:24576\n\t"
        "s_addk_i32 s45, 0x1000\n\t"
        "buffer_load_dwordx4 v[176:179], v225, s[40:43], s45 offen offset:0 sc1\n\t"
        "v_mfma_f32_16x16x32_bf16 v[76:79], v[132:135], v[196:199], v[76:79]\n\t"
        "ds_read_b64_tr_b16 v[130:131], v230 offset:25600\n\t"
        "s_waitcnt lgkmcnt(10)\n\t"
        "v_mfma_f32_16x16x32_bf16 v[80:83], v[136:139], v[192:195], v[80:83]\n\t"
        "ds_read_b64_tr_b16 v[132:133], v231 offset:24576\n\t"
        "v_mfma_f32_16x16x32_bf16 v[88:91], v[136:139], v[196:199], v[88:91]\n\t"
        "ds_read_b64_tr_b16 v[134:135], v231 offset:25600\n\t"
        "v_mfma_f32_16x16x32_bf16 v[84:87], v[140:143], v[192:195], v[84:87]\n\t"
        "ds_read_b64_tr_b16 v[136:137], v232 offset:24576\n\t"
        "buffer_load_dwordx4 v[180:183], v225, s[40:43], s45 offen offset:256 sc1\n\t"
        "v_mfma_f32_16x16x32_bf16 v[92:95], v[140:143], v[196:199], v[92:95]\n\t"
        "ds_read_b64_tr_b16 v[138:139], v232 offset:25600\n\t"
        "s_waitcnt vmcnt(10) lgkmcnt(10)\n\t"
        "v_mfma_f32_16x16x32_bf16 v[64:67], v[144:147], v[200:203], v[64:67]\n\t"
        "ds_read_b64_tr_b16 v[140:141], v233 offset:24576\n\t"
        "v_mfma_f32_16x16x32_bf16 v[72:75], v[144:147], v[204:207], v[72:75]\n\t"
        "ds_read_b64_tr_b16 v[142:143], v233 offset:25600\n\t"
        "v_mfma_f32_16x16x32_bf16 v[68:71], v[148:151], v[200:203], v[68:71]\n\t"
        "ds_read_b64_tr_b16 v[144:145], v230 offset:28672\n\t"
        "buffer_load_dwordx4 v[184:187], v225, s[40:43], s45 offen offset:2048 sc1\n\t"
        "v_mfma_f32_16x16x32_bf16 v[76:79], v[148:151], v[204:207], v[76:79]\n\t"
        "ds_read_b64_tr_b16 v[146:147], v230 offset:29696\n\t"
        "s_waitcnt lgkmcnt(10)\n\t"
        "v_mfma_f32_16x16x32_bf16 v[80:83], v[152:155], v[200:203], v[80:83]\n\t"
        "ds_read_b64_tr_b16 v[148:149], v231 offset:28672\n\t"
        "v_mfma_f32_16x16x32_bf16 v[88:91], v[152:155], v[204:207], v[88:91]\n\t"
        "ds_read_b64_tr_b16 v[150:151], v231 offset:29696\n\t"
        "v_mfma_f32_16x16x32_bf16 v[84:87], v[156:159], v[200:203], v[84:87]\n\t"
        "ds_read_b64_tr_b16 v[152:153], v232 offset:28672\n\t"
        "buffer_load_dwordx4 v[188:191], v225, s[40:43], s45 offen offset:2304 sc1\n\t"
        "v_mfma_f32_16x16x32_bf16 v[92:95], v[156:159], v[204:207], v[92:95]\n\t"
        "ds_read_b64_tr_b16 v[154:155], v232 offset:29696\n\t"
        "s_waitcnt vmcnt(10) lgkmcnt(10)\n\t"
        "v_mfma_f32_16x16x32_bf16 v[64:67], v[128:131], v[208:211], v[64:67]\n\t"
        "ds_read_b64_tr_b16 v[156:157], v233 offset:28672\n\t"
        "v_mfma_f32_16x16x32_bf16 v[72:75], v[128:131], v[212:215], v[72:75]\n\t"
        "ds_read_b64_tr_b16 v[158:159], v233 offset:29696\n\t"
        "v_mfma_f32_16x16x32_bf16 v[68:71], v[132:135], v[208:211], v[68:71]\n\t"
        "ds_read_b64_tr_b16 v[128:129], v234 offset:0\n\t"
        "s_addk_i32 s45, 0x1000\n\t"
        "buffer_load_dwordx4 v[192:195], v225, s[40:43], s45 offen offset:0 sc1\n\t"
        "v_mfma_f32_16x16x32_bf16 v[76:79], v[132:135], v[212:215], v[76:79]\n\t"
        "ds_read_b64_tr_b16 v[130:131], v234 offset:1024\n\t"
        "s_waitcnt lgkmcnt(10)\n\t"
        "v_mfma_f32_16x16x32_bf16 v[80:83], v[136:139], v[208:211], v[80:83]\n\t"
        "ds_read_b64_tr_b16 v[132:133], v235 offset:0\n\t"
        "v_mfma_f32_16x16x32_bf16 v[88:91], v[136:139], v[212:215], v[88:91]\n\t"
        "ds_read_b64_tr_b16 v[134:135], v235 offset:1024\n\t"
        "v_mfma_f32_16x16x32_bf16 v[84:87], v[140:143], v[208:211], v[84:87]\n\t"
        "ds_read_b64_tr_b16 v[136:137], v236 offset:0\n\t"
        "buffer_load_dwordx4 v[196:199], v225, s[40:43], s45 offen offset:256 sc1\n\t"
        "v_mfma_f32_16x16x32_bf16 v[92:95], v[140:143], v[212:215], v[92:95]\n\t"
        "ds_read_b64_tr_b16 v[138:139], v236 offset:1024\n\t"
        "s_waitcnt vmcnt(10) lgkmcnt(10)\n\t"
        "v_mfma_f32_16x16x32_bf16 v[64:67], v[144:147], v[216:219], v[64:67]\n\t"
        "ds_read_b64_tr_b16 v[140:141], v237 offset:0\n\t"
        "v_mfma_f32_16x16x32_bf16 v[72:75], v[144:147], v[220:223], v[72:75]\n\t"
        "ds_read_b64_tr_b16 v[142:143], v237 offset:1024\n\t"
        "v_mfma_f32_16x16x32_bf16 v[68:71], v[148:151], v[216:219], v[68:71]\n\t"
        "ds_read_b64_tr_b16 v[144:145], v234 offset:4096\n\t"
        "buffer_load_dwordx4 v[200:203], v225, s[40:43], s45 offen offset:2048 sc1\n\t"
        "v_mfma_f32_16x16x32_bf16 v[76:79], v[148:151], v[220:223], v[76:79]\n\t"
        "ds_read_b64_tr_b16 v[146:147], v234 offset:5120\n\t"
        "s_waitcnt lgkmcnt(10)\n\t"
        "v_mfma_f32_16x16x32_bf16 v[80:83], v[152:155], v[216:219], v[80:83]\n\t"
        "ds_read_b64_tr_b16 v[148:149], v235 offset:4096\n\t"
        "v_mfma_f32_16x16x32_bf16 v[88:91], v[152:155], v[220:223], v[88:91]\n\t"
        "ds_read_b64_tr_b16 v[150:151], v235 offset:5120\n\t"
        "v_mfma_f32_16x16x32_bf16 v[84:87], v[156:159], v[216:219], v[84:87]\n\t"
        "ds_read_b64_tr_b16 v[152:153], v236 offset:4096\n\t"
        "buffer_load_dwordx4 v[204:207], v225, s[40:43], s45 offen offset:2304 sc1\n\t"
        "v_mfma_f32_16x16x32_bf16 v[92:95], v[156:159], v[220:223], v[92:95]\n\t"
        "ds_read_b64_tr_b16 v[154:155], v236 offset:5120\n\t"
        "s_mov_b32 s44, s45\n\t"
        "s_cmp_eq_u32 s47, 0\n\t"
        "s_cbranch_scc1 oseg0_%=\n\t"
        "s_cmp_eq_u32 s47, 1\n\t"
        "s_cbranch_scc1 oseg1_%=\n\t"
        "s_cmp_eq_u32 s47, 2\n\t"
        "s_cbranch_scc1 oseg2_%=\n\t"
        "s_cmp_eq_u32 s47, 3\n\t"
        "s_cbranch_scc1 oseg3_%=\n\t"
        "s_cmp_lg_u32 s46, 15\n\t"
        "s_cbranch_scc1 ostart_%=\n\t"
        "s_branch oend_%=\n\t"
        "oseg3_%=:\n\t"
        "s_or_b32 s46, s46, 8\n\t"
        "s_waitcnt vmcnt(10) lgkmcnt(10)\n\t"
        "v_mfma_f32_16x16x32_bf16 v[96:99], v[128:131], v[160:163], 0\n\t"
        "ds_read_b64_tr_b16 v[156:157], v233 offset:36864\n\t"
        "ds_read_b32 v239, v238\n\t"
        "v_mfma_f32_16x16x32_bf16 v[104:107], v[128:131], v[164:167], 0\n\t"
        "ds_read_b64_tr_b16 v[158:159], v233 offset:37888\n\t"
        "v_mfma_f32_16x16x32_bf16 v[100:103], v[132:135], v[160:163], 0\n\t"
        "ds_read_b64_tr_b16 v[128:129], v230 offset:40960\n\t"
        "s_addk_i32 s44, 0x1000\n\t"
        "buffer_load_dwordx4 v[208:211], v224, s[40:43], s44 offen offset:0 sc1\n\t"
        "v_mfma_f32_16x16x32_bf16 v[108:111], v[132:135], v[164:167], 0\n\t"
        "ds_read_b64_tr_b16 v[130:131], v230 offset:41984\n\t"
        "s_waitcnt lgkmcnt(3)\n\t"
        "v_mfma_f32_16x16x32_bf16 v[112:115], v[136:139], v[160:163], 0\n\t"
        "ds_read_b64_tr_b16 v[132:133], v231 offset:40960\n\t"
        "v_readfirstlane_b32 s51, v239\n\t"
        "v_mfma_f32_16x16x32_bf16 v[120:123], v[136:139], v[164:167], 0\n\t"
        "ds_read_b64_tr_b16 v[134:135], v231 offset:41984\n\t"
        "v_mfma_f32_16x16x32_bf16 v[116:119], v[140:143], v[160:163], 0\n\t"
        "ds_read_b64_tr_b16 v[136:137], v232 offset:40960\n\t"
        "buffer_load_dwordx4 v[212:215], v224, s[40:43], s44 offen offset:256 sc1\n\t"
        "v_mfma_f32_16x16x32_bf16 v[124:127], v[140:143], v[164:167], 0\n\t"
        "ds_read_b64_tr_b16 v[138:139], v232 offset:41984\n\t"
        "s_andn2_b32 s51, s51, s46\n\t"
        "s_waitcnt vmcnt(10) lgkmcnt(11)\n\t"
        "v_mfma_f32_16x16x32_bf16 v[96:99], v[144:147], v[168:171], v[96:99]\n\t"
        "ds_read_b64_tr_b16 v[140:141], v233 offset:40960\n\t"
        "s_ff1_i32_b32 s47, s51\n\t"
        "v_mfma_f32_16x16x32_bf16 v[104:107], v[144:147], v[172:175], v[104:107]\n\t"
        "ds_read_b64_tr_b16 v[142:143], v233 offset:41984\n\t"
        "s_add_u32 s45, s47, s49\n\t"
        "v_mfma_f32_16x16x32_bf16 v[100:103], v[148:151], v[168:171], v[100:103]\n\t"
        "ds_read_b64_tr_b16 v[144:145], v230 offset:45056\n\t"
        "buffer_load_dwordx4 v[216:219], v224, s[40:43], s44 offen offset:2048 sc1\n\t"
        "v_mfma_f32_16x16x32_bf16 v[108:111], v[148:151], v[172:175], v[108:111]\n\t"
        "ds_read_b64_tr_b16 v[146:147], v230 offset:46080\n\t"
        "s_and_b32 s45, s45, 3\n\t"
        "s_waitcnt lgkmcnt(10)\n\t"
        "v_mfma_f32_16x16x32_bf16 v[112:115], v[152:155], v[168:171], v[112:115]\n\t"
        "ds_read_b64_tr_b16 v[148:149], v231 offset:45056\n\t"
        "s_lshl_b32 s45, s45, 14\n\t"
        "v_mfma_f32_16x16x32_bf16 v[120:123], v[152:155], v[172:175], v[120:123]\n\t"
        "ds_read_b64_tr_b16 v[150:151], v231 offset:46080\n\t"
        "s_lshr_b32 s51, s47, 31\n\t"
        "v_mfma_f32_16x16x32_bf16 v[116:119], v[156:159], v[168:171], v[116:119]\n\t"
        "ds_read_b64_tr_b16 v[152:153], v232 offset:45056\n\t"
        "buffer_load_dwordx4 v[220:223], v224, s[40:43], s44 offen offset:2304 sc1\n\t"
        "v_mfma_f32_16x16x32_bf16 v[124:127], v[156:159], v[172:175], v[124:127]\n\t"
        "ds_read_b64_tr_b16 v[154:155], v232 offset:46080\n\t"
        "s_lshl_b32 s51, s51, 31\n\t"
        "s_waitcnt vmcnt(10) lgkmcnt(10)\n\t"
        "v_mfma_f32_16x16x32_bf16 v[96:99], v[128:131], v[176:179], v[96:99]\n\t"
        "ds_read_b64_tr_b16 v[156:157], v233 offset:45056\n\t"
        "v_or_b32 v225, s51, v224\n\t"
        "v_mfma_f32_16x16x32_bf16 v[104:107], v[128:131], v[180:183], v[104:107]\n\t"
        "ds_read_b64_tr_b16 v[158:159], v233 offset:46080\n\t"
        "s_and_b32 s51, s47, 3\n\t"
        "v_mfma_f32_16x16x32_bf16 v[100:103], v[132:135], v[176:179], v[100:103]\n\t"
        "ds_read_b64_tr_b16 v[128:129], v230 offset:49152\n\t"
        "buffer_load_dwordx4 v[160:163], v225, s[40:43], s45 offen offset:0 sc1\n\t"
        "v_mfma_f32_16x16x32_bf16 v[108:111], v[132:135], v[180:183], v[108:111]\n\t"
        "ds_read_b64_tr_b16 v[130:131], v230 offset:50176\n\t"
        "s_lshl_b32 s51, s51, 15\n\t"
        "s_waitcnt lgkmcnt(10)\n\t"
        "v_mfma_f32_16x16x32_bf16 v[112:115], v[136:139], v[176:179], v[112:115]\n\t"
        "ds_read_b64_tr_b16 v[132:133], v231 offset:49152\n\t"
        "v_add_u32 v234, s51, v226\n\t"
        "v_mfma_f32_16x16x32_bf16 v[120:123], v[136:139], v[180:183], v[120:123]\n\t"
        "ds_read_b64_tr_b16 v[134:135], v231 offset:50176\n\t"
        "v_add_u32 v235, s51, v227\n\t"
        "v_mfma_f32_16x16x32_bf16 v[116:119], v[140:143], v[176:179], v[116:119]\n\t"
        "ds_read_b64_tr_b16 v[136:137], v232 offset:49152\n\t"
        "buffer_load_dwordx4 v[164:167], v225, s[40:43], s45 offen offset:256 sc1\n\t"
        "v_mfma_f32_16x16x32_bf16 v[124:127], v[140:143], v[180:183], v[124:127]\n\t"
        "ds_read_b64_tr_b16 v[138:139], v232 offset:50176\n\t"
        "v_add_u32 v236, s51, v228\n\t"
        "s_waitcnt vmcnt(10) lgkmcnt(10)\n\t"
        "v_mfma_f32_16x16x32_bf16 v[96:99], v[144:147], v[184:187], v[96:99]\n\t"
        "ds_read_b64_tr_b16 v[140:141], v233 offset:49152\n\t"
        "v_add_u32 v237, s51, v229\n\t"
        "v_mfma_f32_16x16x32_bf16 v[104:107], v[144:147], v[188:191], v[104:107]\n\t"
        "ds_read_b64_tr_b16 v[142:143], v233 offset:50176\n\t"
        "v_mfma_f32_16x16x32_bf16 v[100:103], v[148:151], v[184:187], v[100:103]\n\t"
        "ds_read_b64_tr_b16 v[144:145], v230 offset:53248\n\t"
        "buffer_load_dwordx4 v[168:171], v225, s[40:43], s45 offen offset:2048 sc1\n\t"
        "v_mfma_f32_16x16x32_bf16 v[108:111], v[148:151], v[188:191], v[108:111]\n\t"
        "ds_read_b64_tr_b16 v[146:147], v230 offset:54272\n\t"
        "s_waitcnt lgkmcnt(10)\n\t"
        "v_mfma_f32_16x16x32_bf16 v[112:115], v[152:155], v[184:187], v[112:115]\n\t"
        "ds_read_b64_tr_b16 v[148:149], v231 offset:53248\n\t"
        "v_mfma_f32_16x16x32_bf16 v[120:123], v[152:155], v[188:191], v[120:123]\n\t"
        "ds_read_b64_tr_b16 v[150:151], v231 offset:54272\n\t"
        "v_mfma_f32_16x16x32_bf16 v[116:119], v[156:159], v[184:187], v[116:119]\n\t"
        "ds_read_b64_tr_b16 v[152:153], v232 offset:53248\n\t"
        "buffer_load_dwordx4 v[172:175], v225, s[40:43], s45 offen offset:2304 sc1\n\t"
        "v_mfma_f32_16x16x32_bf16 v[124:127], v[156:159], v[188:191], v[124:127]\n\t"
        "ds_read_b64_tr_b16 v[154:155], v232 offset:54272\n\t"
        "s_waitcnt vmcnt(10) lgkmcnt(10)\n\t"
        "v_mfma_f32_16x16x32_bf16 v[96:99], v[128:131], v[192:195], v[96:99]\n\t"
        "ds_read_b64_tr_b16 v[156:157], v233 offset:53248\n\t"
        "v_mfma_f32_16x16x32_bf16 v[104:107], v[128:131], v[196:199], v[104:107]\n\t"
        "ds_read_b64_tr_b16 v[158:159], v233 offset:54272\n\t"
        "v_mfma_f32_16x16x32_bf16 v[100:103], v[132:135], v[192:195], v[100:103]\n\t"
        "ds_read_b64_tr_b16 v[128:129], v230 offset:57344\n\t"
        "s_addk_i32 s45, 0x1000\n\t"
        "buffer_load_dwordx4 v[176:179], v225, s[40:43], s45 offen offset:0 sc1\n\t"
        "v_mfma_f32_16x16x32_bf16 v[108:111], v[132:135], v[196:199], v[108:111]\n\t"
        "ds_read_b64_tr_b16 v[130:131], v230 offset:58368\n\t"
        "s_waitcnt lgkmcnt(10)\n\t"
        "v_mfma_f32_16x16x32_bf16 v[112:115], v[136:139], v[192:195], v[112:115]\n\t"
        "ds_read_b64_tr_b16 v[132:133], v231 offset:57344\n\t"
        "v_mfma_f32_16x16x32_bf16 v[120:123], v[136:139], v[196:199], v[120:123]\n\t"
        "ds_read_b64_tr_b16 v[134:135], v231 offset:58368\n\t"
        "v_mfma_f32_16x16x32_bf16 v[116:119], v[140:143], v[192:195], v[116:119]\n\t"
        "ds_read_b64_tr_b16 v[136:137], v232 offset:57344\n\t"
        "buffer_load_dwordx4 v[180:183], v225, s[40:43], s45 offen offset:256 sc1\n\t"
        "v_mfma_f32_16x16x32_bf16 v[124:127], v[140:143], v[196:199], v[124:127]\n\t"
        "ds_read_b64_tr_b16 v[138:139], v232 offset:58368\n\t"
        "s_waitcnt vmcnt(10) lgkmcnt(10)\n\t"
        "v_mfma_f32_16x16x32_bf16 v[96:99], v[144:147], v[200:203], v[96:99]\n\t"
        "ds_read_b64_tr_b16 v[140:141], v233 offset:57344\n\t"
        "v_mfma_f32_16x16x32_bf16 v[104:107], v[144:147], v[204:207], v[104:107]\n\t"
        "ds_read_b64_tr_b16 v[142:143], v233 offset:58368\n\t"
        "v_mfma_f32_16x16x32_bf16 v[100:103], v[148:151], v[200:203], v[100:103]\n\t"
        "ds_read_b64_tr_b16 v[144:145], v230 offset:61440\n\t"
        "buffer_load_dwordx4 v[184:187], v225, s[40:43], s45 offen offset:2048 sc1\n\t"
        "v_mfma_f32_16x16x32_bf16 v[108:111], v[148:151], v[204:207], v[108:111]\n\t"
        "ds_read_b64_tr_b16 v[146:147], v230 offset:62464\n\t"
        "s_waitcnt lgkmcnt(10)\n\t"
        "v_mfma_f32_16x16x32_bf16 v[112:115], v[152:155], v[200:203], v[112:115]\n\t"
        "ds_read_b64_tr_b16 v[148:149], v231 offset:61440\n\t"
        "v_mfma_f32_16x16x32_bf16 v[120:123], v[152:155], v[204:207], v[120:123]\n\t"
        "ds_read_b64_tr_b16 v[150:151], v231 offset:62464\n\t"
        "v_mfma_f32_16x16x32_bf16 v[116:119], v[156:159], v[200:203], v[116:119]\n\t"
        "ds_read_b64_tr_b16 v[152:153], v232 offset:61440\n\t"
        "buffer_load_dwordx4 v[188:191], v225, s[40:43], s45 offen offset:2304 sc1\n\t"
        "v_mfma_f32_16x16x32_bf16 v[124:127], v[156:159], v[204:207], v[124:127]\n\t"
        "ds_read_b64_tr_b16 v[154:155], v232 offset:62464\n\t"
        "s_waitcnt vmcnt(10) lgkmcnt(10)\n\t"
        "v_mfma_f32_16x16x32_bf16 v[96:99], v[128:131], v[208:211], v[96:99]\n\t"
        "ds_read_b64_tr_b16 v[156:157], v233 offset:61440\n\t"
        "v_mfma_f32_16x16x32_bf16 v[104:107], v[128:131], v[212:215], v[104:107]\n\t"
        "ds_read_b64_tr_b16 v[158:159], v233 offset:62464\n\t"
        "v_mfma_f32_16x16x32_bf16 v[100:103], v[132:135], v[208:211], v[100:103]\n\t"
        "ds_read_b64_tr_b16 v[128:129], v234 offset:0\n\t"
        "s_addk_i32 s45, 0x1000\n\t"
        "buffer_load_dwordx4 v[192:195], v225, s[40:43], s45 offen offset:0 sc1\n\t"
        "v_mfma_f32_16x16x32_bf16 v[108:111], v[132:135], v[212:215], v[108:111]\n\t"
        "ds_read_b64_tr_b16 v[130:131], v234 offset:1024\n\t"
        "s_waitcnt lgkmcnt(10)\n\t"
        "v_mfma_f32_16x16x32_bf16 v[112:115], v[136:139], v[208:211], v[112:115]\n\t"
        "ds_read_b64_tr_b16 v[132:133], v235 offset:0\n\t"
        "v_mfma_f32_16x16x32_bf16 v[120:123], v[136:139], v[212:215], v[120:123]\n\t"
        "ds_read_b64_tr_b16 v[134:135], v235 offset:1024\n\t"
        "v_mfma_f32_16x16x32_bf16 v[116:119], v[140:143], v[208:211], v[116:119]\n\t"
        "ds_read_b64_tr_b16 v[136:137], v236 offset:0\n\t"
        "buffer_load_dwordx4 v[196:199], v225, s[40:43], s45 offen offset:256 sc1\n\t"
        "v_mfma_f32_16x16x32_bf16 v[124:127], v[140:143], v[212:215], v[124:127]\n\t"
        "ds_read_b64_tr_b16 v[138:139], v236 offset:1024\n\t"
        "s_waitcnt vmcnt(10) lgkmcnt(10)\n\t"
        "v_mfma_f32_16x16x32_bf16 v[96:99], v[144:147], v[216:219], v[96:99]\n\t"
        "ds_read_b64_tr_b16 v[140:141], v237 offset:0\n\t"
        "v_mfma_f32_16x16x32_bf16 v[104:107], v[144:147], v[220:223], v[104:107]\n\t"
        "ds_read_b64_tr_b16 v[142:143], v237 offset:1024\n\t"
        "v_mfma_f32_16x16x32_bf16 v[100:103], v[148:151], v[216:219], v[100:103]\n\t"
        "ds_read_b64_tr_b16 v[144:145], v234 offset:4096\n\t"
        "buffer_load_dwordx4 v[200:203], v225, s[40:43], s45 offen offset:2048 sc1\n\t"
        "v_mfma_f32_16x16x32_bf16 v[108:111], v[148:151], v[220:223], v[108:111]\n\t"
        "ds_read_b64_tr_b16 v[146:147], v234 offset:5120\n\t"
        "s_waitcnt lgkmcnt(10)\n\t"
        "v_mfma_f32_16x16x32_bf16 v[112:115], v[152:155], v[216:219], v[112:115]\n\t"
        "ds_read_b64_tr_b16 v[148:149], v235 offset:4096\n\t"
        "v_mfma_f32_16x16x32_bf16 v[120:123], v[152:155], v[220:223], v[120:123]\n\t"
        "ds_read_b64_tr_b16 v[150:151], v235 offset:5120\n\t"
        "v_mfma_f32_16x16x32_bf16 v[116:119], v[156:159], v[216:219], v[116:119]\n\t"
        "ds_read_b64_tr_b16 v[152:153], v236 offset:4096\n\t"
        "buffer_load_dwordx4 v[204:207], v225, s[40:43], s45 offen offset:2304 sc1\n\t"
        "v_mfma_f32_16x16x32_bf16 v[124:127], v[156:159], v[220:223], v[124:127]\n\t"
        "ds_read_b64_tr_b16 v[154:155], v236 offset:5120\n\t"
        "s_mov_b32 s44, s45\n\t"
        "s_cmp_eq_u32 s47, 0\n\t"
        "s_cbranch_scc1 oseg0_%=\n\t"
        "s_cmp_eq_u32 s47, 1\n\t"
        "s_cbranch_scc1 oseg1_%=\n\t"
        "s_cmp_eq_u32 s47, 2\n\t"
        "s_cbranch_scc1 oseg2_%=\n\t"
        "s_cmp_eq_u32 s47, 3\n\t"
        "s_cbranch_scc1 oseg3_%=\n\t"
        "s_cmp_lg_u32 s46, 15\n\t"
        "s_cbranch_scc1 ostart_%=\n\t"
        "s_branch oend_%=\n\t"
        "oend_%=:\n\t"
        "ds_read_b64 v[128:129], v240 offset:0\n\t"
        "ds_read_b64 v[130:131], v240 offset:16\n\t"
        "ds_read_b64 v[132:133], v240 offset:32\n\t"
        "ds_read_b64 v[134:135], v240 offset:48\n\t"
        "ds_read_b64 v[136:137], v240 offset:64\n\t"
        "ds_read_b64 v[138:139], v240 offset:80\n\t"
        "ds_read_b64 v[140:141], v240 offset:96\n\t"
        "ds_read_b64 v[142:143], v240 offset:112\n\t"
        "s_waitcnt lgkmcnt(0)\n\t"
        "s_barrier\n\t"
        : "+{v[0:15]}"(acc[0]), "+{v[16:31]}"(acc[1]), "={v[32:47]}"(acc[2]), "={v[48:63]}"(acc[3]), "={v[64:79]}"(acc[4]), "={v[80:95]}"(acc[5]),
          "={v[96:111]}"(acc[6]), "={v[112:127]}"(acc[7]), "={v[128:143]}"(wrc),
          "={v[160:175]}"(fl0), "={v[176:191]}"(fl1), "={v[192:207]}"(fl2) STAMP_OUTS
        : "{v224}"(OFF_OF + L * (5 * MBY) + tile * TS + tblk * 1024 + (hi * 32 + (lane & 15)) * 16), "{v[226:233]}"(wa),
          "{v238}"(lbase + L_W + 20 + 8 * L), "{v240}"(lbase + L_WR + hi * 8), "{s49}"((tn >> 2) + 1), "{s[40:43]}"(srd)
        : "v144", "v145", "v146", "v147", "v148", "v149", "v150", "v151", "v152", "v153", "v154", "v155", "v156", "v157", "v158", "v159", "v208", "v209", "v210", "v211", "v212", "v213", "v214", "v215", "v216", "v217", "v218", "v219", "v220", "v221", "v222", "v223", "v225", "v234", "v235", "v236", "v237", "v239", "s44", "s45", "s46", "s47", "s48", "s51", "vcc", "scc", "memory");
      }
      PHASE_VARS;
      STAMP_PUT(12 + 16 * L);
      STAMPV(15 + 16 * L);
      float s = 0.f, sq = 0.f, d = 0.f;
      const unsigned xdst = OFF_X1 + tile * TS + tn * 4096 + lane * 16;
#pragma unroll
      for (int i = 0; i < 4; ++i) {
#pragma unroll
        for (int e = 0; e < 4; ++e) {
          const int j = i >> 1, k = 8 * (i & 1) + 2 * e;
          x1p[i][e] = pack2((acc[j][k] + acc[2 + j][k]) + (acc[4 + j][k] + acc[6 + j][k]), (acc[j][k + 1] + acc[2 + j][k + 1]) + (acc[4 + j][k + 1] + acc[6 + j][k + 1]));
        }
        if (i & 1) {
          row_swap(x1p[i - 1], x1p[i]);
          if (L == 0) { stg(rs, xdst + (i - 1) * 1024, x1p[i - 1]); stg(rs, xdst + i * 1024, x1p[i]); }
          SBAR();
        }
      }
#pragma unroll
      for (int g = 0; g < 8; ++g)
#pragma unroll
        for (int e = 0; e < 2; ++e) {
          const unsigned o = x1p[g >> 1][2 * (g & 1) + e];
          s = dot2(o, 0x3f803f80u, s); sq = dot2(o, o, sq); d = dot2(o, wrc[2 * g + e], d);
        }
      const u32x4 pst = {nzf(half_swap_sum(s)), nzf(half_swap_sum(sq)), nzf(half_swap_sum(d)), 0u};
      asm volatile("buffer_store_dwordx4 %0, %1, %2, 0 offen sc1\n\ts_waitcnt vmcnt(1)\n\tds_write_b32 %3, %4"
                   :: "v"(pst), "v"(stats_dst(sx, tile, tn, lane)), "s"(srd), "v"(lbase + L_W + 64 + 4 * wid), "v"(3),
                      "{v[160:175]}"(fl0), "{v[176:191]}"(fl1), "{v[192:207]}"(fl2) : "memory");
    }
    STAMPV(16 + 16 * L);
  }
  {
    PHASE_VARS;
    float S, Q, Dd, mean, var, mb1, rd1;
    __builtin_amdgcn_s_sleep(13);
    row_stats_sum(rs, OFF_SX + SX_LAYER, tile, lane, S, Q, Dd);
    STAMPV(33);
    row_stats_fin(rs, OFF_SX + SX_LAYER + SX_CAND + (tn * 16 + tm) * 128 + wid * 4, smem, 2, tile, lane, S, Q, Dd, U2F(words[0]), x1p, mean, var, mb1, rd1);
    STAMPV(34);
    u32x4 xo[4];
#pragma unroll
    for (int i = 0; i < 4; ++i) {
#pragma unroll
      for (int e = 0; e < 4; ++e) {
        const unsigned h2 = ln_pair(x1p[i][e], mb1, rd1);
        xo[i][e] = pack2(lo16(x1p[i][e]) + lo16(h2), hi16(x1p[i][e]) + hi16(h2));
      }
      *reinterpret_cast<u32x4*>(smem + wid * 4096 + r32 * 128 + ((2 * i + hi) ^ (r32 & 7)) * 16) = fo_swap(xo[i]);
    }
    asm volatile("s_waitcnt lgkmcnt(0)" ::: "memory");
#pragma unroll
    for (int i = 0; i < 4; ++i) { xo[i] = *reinterpret_cast<const u32x4*>(smem + wid * 4096 + i * 1024 + (lane >> 3) * 128 + ((lane ^ (lane >> 3)) & 7) * 16); PIN(xo[i]); }
    u16* dst = out + (size_t)(tile * 32 + (lane >> 3)) * D_ + tn * 64 + (lane & 7) * 8;
    lds_wait_ge(lbase + L_W + 4 + 4 * (lane & 1), 2);
    const int rel = (int)words[3] - tile * 32 - (lane >> 3);
#pragma unroll
    for (int i = 0; i < 4; ++i)
      if (rel != 8 * i) st16(dst + (size_t)i * 8 * D_, xo[i]);
    STAMPV(35);
    STAMP_END;
  }
}

extern "C" __global__ void end_marker() {}

extern "C" void kernel_launch(void* const* d_in, const int* in_sizes, int n_in, void* d_out, int out_size,
                              void* d_ws, size_t ws_size, hipStream_t stream) {
  char* ws = (char*)d_ws;
  mega_kernel<<<256, 512, 0, stream>>>((const u16*)d_in[0], (const u16*)d_in[1], (const u16*)d_in[2], (const u16*)d_in[4], (const u16*)d_in[5],
                                       (const u16*)d_in[6], (const u16*)d_in[7], ws, (u16*)d_out);
}
```

```cpp
#include <hip/hip_runtime.h>
#include <hip/hip_bf16.h>
#include <stdint.h>

typedef unsigned short u16;
typedef short s16x4 __attribute__((ext_vector_type(4)));
typedef short s16x8 __attribute__((ext_vector_type(8)));
typedef float f32x16 __attribute__((ext_vector_type(16)));
typedef float f32x4 __attribute__((ext_vector_type(4)));
typedef float f32x2 __attribute__((ext_vector_type(2)));
typedef unsigned int u32x16 __attribute__((ext_vector_type(16)));
typedef unsigned int u32x8 __attribute__((ext_vector_type(8)));
typedef unsigned int u32x4 __attribute__((ext_vector_type(4)));
typedef unsigned int u32x2 __attribute__((ext_vector_type(2)));
typedef __bf16 bf16x2 __attribute__((ext_vector_type(2)));
typedef __bf16 bf16x8 __attribute__((ext_vector_type(8)));
typedef __bf16 bf16x16 __attribute__((ext_vector_type(16)));

#define DI __device__ __forceinline__
#define LDS_PTR(T, p) ((__attribute__((address_space(3))) T*)(p))
#define BARRIER() asm volatile("s_barrier" ::: "memory")
#define BAR_LGKM() asm volatile("s_waitcnt lgkmcnt(0)\n\ts_barrier" ::: "memory")
#define BAR_ALL() asm volatile("s_waitcnt vmcnt(0) lgkmcnt(0)\n\ts_barrier" ::: "memory")
#define DRAIN() asm volatile("s_waitcnt vmcnt(0)" ::: "memory")
#define MFMA32(a, b, c) __builtin_amdgcn_mfma_f32_32x32x16_bf16(a, b, c, 0, 0, 0)
#define MFMA16(a, b, c) __builtin_amdgcn_mfma_f32_16x16x32_bf16(a, b, c, 0, 0, 0)
#define PIN(x) asm volatile("" : "+v"(x))
#define EX2(x) __builtin_amdgcn_exp2f(x)
#define RCP(x) __builtin_amdgcn_rcpf(x)
#define SBAR() __builtin_amdgcn_sched_barrier(0)

#define STAMP_INIT
#define STAMPV(i)
#define STAMP_END
#define STAMP_DECL
#define STAMP_OUTS
#define STAMP_PUT(i)
#define JIT()
constexpr int D_ = 1024, F_ = 3584;
constexpr float QS = 0.17677669529663687f * 1.4426950408889634f;

constexpr unsigned MBY = 1u << 20;
constexpr unsigned TS = 65536 + 4096, HS = 262144 + 4096;
constexpr unsigned OFF_HF = 0, OFF_X1 = 5 * MBY;
constexpr unsigned OFF_OF = 10 * MBY;
constexpr unsigned OFF_KV = 20 * MBY;
constexpr unsigned OFF_FL = 40 * MBY;
constexpr unsigned FL_EPOCH = OFF_FL;
constexpr unsigned FL_KV = OFF_FL + 4096;
constexpr unsigned FL_O = FL_KV + 65536;
constexpr unsigned FL_X = FL_O + 65536;
constexpr unsigned OFF_CS = 41 * MBY;
constexpr unsigned OFF_SX = 42 * MBY;
constexpr unsigned SX_ST = 0;
constexpr unsigned SX_ROW = 512 * 1024;
constexpr unsigned SX_CAND = SX_ROW + 256 * 1024;
constexpr unsigned SX_GP = SX_CAND + 32 * 1024;
constexpr unsigned SX_YP = SX_GP + 224 * 1024;
constexpr unsigned SX_LAYER = SX_YP + 224 * 1024;
constexpr unsigned SX_QP = 2 * SX_LAYER;
constexpr unsigned SX_BYTES = SX_QP + 512 * 1024;
static_assert(SX_BYTES <= 6 * 256 * 128 * 16, "sentinel fill");
constexpr unsigned WS_BYTES = 48 * MBY, OOB = 0x80000000u;
constexpr unsigned SENT = 0xFFFFFFFFu;

constexpr int NS = 9, DD = 3, SLOTB = 12352, HALFB = 6176;
constexpr int L_XCH = 122880;
constexpr int L_KX = L_XCH - 8192, L_QX = L_XCH + 8192;
constexpr int L_SL = L_XCH + 8192;
constexpr int L_CAND = L_XCH + 32768;
constexpr int L_AB = L_XCH + 34816;
constexpr int L_FULL = 159744, L_FREE = L_FULL + 512;
constexpr int L_CS = L_FREE + 1024, L_PATCH = L_CS + 768;
constexpr int L_GU = L_PATCH + 768, L_TAG = L_GU + 256, L_WR = L_TAG + 256;
constexpr int L_X2F = L_WR + 128, L_X2P = L_X2F + 128;
constexpr int L_W = L_X2P + 128;
constexpr int LDS_BYTES = 163840;
static_assert(L_W + 128 <= LDS_BYTES && NS * SLOTB + 256 <= L_KX && L_AB + 2048 <= L_FULL, "LDS map");

DI int fresh_lane() { int l; asm volatile("v_mbcnt_lo_u32_b32 %0, -1, 0\n\tv_mbcnt_hi_u32_b32 %0, -1, %0" : "=v"(l)); return l; }
#define LANE_VARS const int lane = fresh_lane(), r32 = lane & 31, hi = lane >> 5, tq = (lane & 15) >> 2, tp = lane & 3, tblk = (lane >> 4) & 1, \
  wrow = 4 * hi + tq, wsw = 4 * ((wrow >> 1) & 1), wch = 2 * tblk + (tp >> 1), c24 = r32 < 24 ? r32 : 23; \
  (void)r32; (void)hi; (void)wsw; (void)wch; (void)c24
DI int fresh_s(int v) { asm volatile("" : "+s"(v)); return v; }
#define PHASE_VARS const int tm = fresh_s(tm_), tn = fresh_s(tn_), wid = fresh_s(wid_), tile = tm * 4 + wid, hh = wid & 1, kh = wid >> 1; (void)tile; (void)hh; (void)kh; LANE_VARS
DI int kpos(int tm, int tn, int i) { return i == 0 ? tn : (tn + 1 + (1 * (3 - (tn & 3)) + i - 1) % 15) & 15; }
DI unsigned pack2(float lo, float hi) { f32x2 v = {lo, hi}; return __builtin_bit_cast(unsigned, __builtin_convertvector(v, bf16x2)); }
DI float rbf(float f) { return __uint_as_float(pack2(f, 0.f) << 16); }
DI float lo16(unsigned u) { return __uint_as_float(u << 16); }
DI float hi16(unsigned u) { return __uint_as_float(u & 0xffff0000u); }
DI float opq(float r) { asm("" : "+v"(r)); return r; }
DI float fadd(float a, float b) { return opq(a + b); }
DI float fmul(float a, float b) { return opq(a * b); }
DI float ffma(float a, float b, float c) { return opq(__builtin_fmaf(a, b, c)); }
DI float U2F(unsigned u) { return __uint_as_float(u); }
DI float half_swap_sum(float v) {
  auto r = __builtin_amdgcn_permlane32_swap(__float_as_uint(v), __float_as_uint(v), false, false);
  return fadd(U2F(r[0]), U2F(r[1]));
}
DI float half_swap_max(float v) {
  auto r = __builtin_amdgcn_permlane32_swap(__float_as_uint(v), __float_as_uint(v), false, false);
  return fmaxf(__uint_as_float(r[0]), __uint_as_float(r[1]));
}
template <int CTRL> DI unsigned dpp(unsigned v) { return (unsigned)__builtin_amdgcn_update_dpp(0, (int)v, CTRL, 0xf, 0xf, false); }
DI float row_swap_sum(float v) {
  auto r = __builtin_amdgcn_permlane16_swap(__float_as_uint(v), __float_as_uint(v), false, false);
  return fadd(U2F(r[0]), U2F(r[1]));
}
DI float half_sum(float v) {
  v = fadd(v, U2F(dpp<0xB1>(__float_as_uint(v)))); v = fadd(v, U2F(dpp<0x4E>(__float_as_uint(v))));
  v = fadd(v, U2F(dpp<0x141>(__float_as_uint(v)))); v = fadd(v, U2F(dpp<0x140>(__float_as_uint(v))));
  return row_swap_sum(v);
}
DI float wave_sum(float v) { return half_swap_sum(half_sum(v)); }
DI unsigned half_max_u32(unsigned v) {
  v = max(v, dpp<0xB1>(v)); v = max(v, dpp<0x4E>(v)); v = max(v, dpp<0x141>(v)); v = max(v, dpp<0x140>(v));
  auto r = __builtin_amdgcn_permlane16_swap(v, v, false, false);
  return max(r[0], r[1]);
}
DI unsigned wave_max_u32(unsigned v) {
  v = half_max_u32(v);
  auto r = __builtin_amdgcn_permlane32_swap(v, v, false, false);
  return max(r[0], r[1]);
}
DI u32x4 fo_swap(u32x4 v) {
  auto a = __builtin_amdgcn_permlane32_swap(v[0], v[2], false, false);
  auto b = __builtin_amdgcn_permlane32_swap(v[1], v[3], false, false);
  u32x4 o; o[0] = a[0]; o[1] = b[0]; o[2] = a[1]; o[3] = b[1];
  return o;
}
DI void row_swap(u32x4& a, u32x4& b) {
#pragma unroll
  for (int e = 0; e < 4; ++e) { auto r = __builtin_amdgcn_permlane16_swap(a[e], b[e], false, false); a[e] = r[0]; b[e] = r[1]; }
}
DI void glds4(const void* sb, unsigned vo, unsigned lds_dst) {
  unsigned keep;
  asm volatile("s_mov_b32 %0, m0\n\ts_mov_b32 m0, %3\n\ts_nop 0\n\tglobal_load_lds_dword %1, %2\n\ts_mov_b32 m0, %0"
               : "=&s"(keep) : "v"(vo), "s"(sb), "s"(lds_dst) : "memory");
}
#define GL_ "global_load_lds_dwordx4 "
#define GP_ "\n\ts_sleep %8\n\ts_waitcnt vmcnt(%7)\n\t"
template <int TH, int SL> DI void glds_v4t(const void* sb, const unsigned (&v)[4], unsigned m0v) {
  unsigned keep;
  asm volatile("s_mov_b32 %0, m0\n\ts_mov_b32 m0, %6\n\ts_nop 0\n\t" GL_ "%1, %5" GP_ GL_ "%2, %5 offset:1024" GP_ GL_ "%3, %5 offset:2048" GP_ GL_ "%4, %5 offset:3072" GP_ "s_mov_b32 m0, %0"
               : "=&s"(keep) : "v"(v[0]), "v"(v[1]), "v"(v[2]), "v"(v[3]), "s"(sb), "s"(m0v), "n"(TH), "n"(SL) : "memory");
}
DI void glds_v3(const void* sb, const unsigned (&v)[3], unsigned m0v) {
  unsigned keep;
  asm volatile("s_mov_b32 %0, m0\n\ts_mov_b32 m0, %5\n\ts_nop 0\n\t" GL_ "%1, %4\n\t" GL_ "%2, %4 offset:1024\n\t" GL_ "%3, %4 offset:2048\n\t"
               "s_mov_b32 m0, %0" : "=&s"(keep) : "v"(v[0]), "v"(v[1]), "v"(v[2]), "s"(sb), "s"(m0v) : "memory");
}
DI float dot2(unsigned a, unsigned b, float c) {
  return __builtin_amdgcn_fdot2_f32_bf16(__builtin_bit_cast(bf16x2, a), __builtin_bit_cast(bf16x2, b), c, false);
}
#define CODE_ADDR(sym, out) do { unsigned long long pc_; unsigned off_; \
  asm volatile("s_getpc_b64 %0\n1:\n\ts_mov_b32 %1, " #sym "-1b" : "=s"(pc_), "=s"(off_)); out = (const char*)(pc_ + (long long)(int)off_); } while (0)
DI void st16(void* p, u32x4 v) { asm volatile("global_store_dwordx4 %0, %1, off sc0 sc1\n\ts_nop 1" :: "v"(p), "v"(v) : "memory"); }
DI void ln_scalars(float mean, float var, float& mb, float& den) { mb = rbf(mean); den = rbf(__builtin_amdgcn_sqrtf(rbf(rbf(var) + 1e-5f))); }
DI unsigned ln_pair(unsigned v, float mb, float rd) {
  const unsigned c = pack2(lo16(v) - mb, hi16(v) - mb);
  return pack2(lo16(c) * rd, hi16(c) * rd);
}
DI unsigned ln_pair_s(unsigned v, float nmb, float rd) {
  const unsigned c = pack2(fadd(lo16(v), nmb), fadd(hi16(v), nmb));
  return pack2(fmul(lo16(c), rd), fmul(hi16(c), rd));
}
template <int N, int M> DI f32x4 sum4(const u32x4 (&v)[M]) {
  f32x4 t;
#pragma unroll
  for (int e = 0; e < 4; ++e) {
    t[e] = U2F(v[0][e]);
#pragma unroll
    for (int i = 1; i < N; ++i) t[e] = fadd(t[e], U2F(v[i][e]));
  }
  return t;
}
#define PERM_LO(x, y) __builtin_amdgcn_perm(y, x, 0x05040100u)
#define PERM_HI(x, y) __builtin_amdgcn_perm(y, x, 0x07060302u)
constexpr unsigned SPIN_CAP = 1u << 15;
DI unsigned nz(unsigned v) { return min(v, SENT - 1u); }
DI unsigned nzf(float v) { return nz(__float_as_uint(v)); }
DI unsigned mx4(unsigned m, const u32x4& v) { return max(max(max(m, v[0]), v[1]), max(v[2], v[3])); }
template <int N> DI bool none_sent(const u32x4 (&v)[N]) {
  unsigned m = 0u;
#pragma unroll
  for (int i = 0; i < N; ++i) m = mx4(m, v[i]);
  return m != SENT;
}
typedef __amdgpu_buffer_rsrc_t rsrc_t;
DI u32x4 ldg(rsrc_t rs, unsigned off) { return __builtin_amdgcn_raw_buffer_load_b128(rs, (int)off, 0, 16); }
DI void stg(rsrc_t rs, unsigned off, u32x4 v) { __builtin_amdgcn_raw_buffer_store_b128(v, rs, (int)off, 0, 16); }
DI void poll_pause(bool slow) { if (slow) __builtin_amdgcn_s_sleep(4); asm volatile("s_sleep 1" ::: "memory"); }
template <int N, class LD, class OKF> DI void poll_all(u32x4 (&out)[N], bool slow, LD ld, OKF okf) {
  for (unsigned spins = 0;; ++spins) {
    ld(out);
    SBAR();
    if (__all(okf(out)) || spins > SPIN_CAP) break;
    poll_pause(slow);
  }
}
DI void lds_set(unsigned addr, unsigned v) { asm volatile("s_waitcnt lgkmcnt(0)\n\tds_write_b32 %0, %1" :: "v"(addr), "v"(v) : "memory"); }
template <int SLEEP = 1> DI void lds_wait_ge(unsigned addr, unsigned need) {
  unsigned spin = 0x100000, v;
  asm volatile("1:\n\tds_read_b32 %0, %2\n\ts_sub_u32 %1, %1, 1\n\ts_waitcnt lgkmcnt(0)\n\ts_cbranch_scc1 2f\n\tv_cmp_gt_u32 vcc, %3, %0\n\ts_cbranch_vccz 2f\n\ts_sleep %4\n\ts_branch 1b\n2:"
               : "=&v"(v), "+&s"(spin) : "v"(addr), "s"(need), "n"(SLEEP) : "vcc", "scc", "memory");
}
DI s16x8 tr2(const char* p, int second) {
  s16x4 lo = __builtin_amdgcn_ds_read_tr16_b64_v4i16(LDS_PTR(s16x4, p)), hi = __builtin_amdgcn_ds_read_tr16_b64_v4i16(LDS_PTR(s16x4, p + second));
  return __builtin_shufflevector(lo, hi, 0, 1, 2, 3, 4, 5, 6, 7);
}
DI u32x16 cat4(const u32x4& a, const u32x4& b, const u32x4& c, const u32x4& d) {
  const u32x8 x = __builtin_shufflevector(a, b, 0, 1, 2, 3, 4, 5, 6, 7), y = __builtin_shufflevector(c, d, 0, 1, 2, 3, 4, 5, 6, 7);
  return __builtin_shufflevector(x, y, 0, 1, 2, 3, 4, 5, 6, 7, 8, 9, 10, 11, 12, 13, 14, 15);
}

DI unsigned stats_dst(unsigned sx, int tile, int tn, int lane) { return lane < 32 ? sx + SX_ST + tile * 8192 + tn * 512 + lane * 16 : OOB; }
DI void row_stats_sum(rsrc_t rs, unsigned sx, int tile, int lane, float& S, float& Q, float& Dd) {
  const unsigned exb = sx + SX_ST + tile * 8192 + (lane & 31) * 16;
  u32x4 pv[8];
  poll_all<8>(pv, false, [&](u32x4 (&v)[8]) {
#pragma unroll
    for (int i = 0; i < 8; ++i) v[i] = ldg(rs, exb + ((lane >> 5) * 8 + i) * 512);
  }, [&](const u32x4 (&v)[8]) { return none_sent(v); });
  S = 0.f; Q = 0.f; Dd = 0.f;
#pragma unroll
  for (int i = 0; i < 8; ++i) { S += __uint_as_float(pv[i][0]); Q += __uint_as_float(pv[i][1]); Dd += __uint_as_float(pv[i][2]); }
}
DI void row_stats_fin(rsrc_t rs, unsigned kdst, char* smem, int epoch, int tile, int lane, float S, float Q, float Dd, float wsum, const u32x4 (&x1p)[4],
                      float& mean, float& var, float& mb, float& rd) {
  S = half_swap_sum(S); Q = half_swap_sum(Q); Dd = half_swap_sum(Dd);
  mean = S * (1.0f / 1024.0f); var = fmaxf(Q * (1.0f / 1024.0f) - mean * mean, 0.f);
  float den;
  ln_scalars(mean, var, mb, den);
  rd = RCP(den);
  const float sc = (Dd - mb * wsum) * rd;
  const unsigned sb = __float_as_uint(sc), rb = pack2(0.f, sc), neg = (unsigned)((int)rb >> 31) | 0x80000000u;
  const unsigned fine = min((((sb ^ neg) - (rb ^ neg)) + 0x8000u) >> 11, 30u);
  const unsigned mine = ((rb ^ neg) & 0xffff0000u) | (fine << 11) | (unsigned)(2047 - (tile * 32 + (lane & 31)));
  const bool best = half_max_u32(mine) == mine;
  __builtin_amdgcn_raw_buffer_store_b32(mine, rs, (best && lane < 32) ? kdst : OOB, 0, 16);
  if (best) {
    char* c = smem + L_CAND + (tile & 3) * 144;
#pragma unroll
    for (int i = 0; i < 4; ++i) *reinterpret_cast<u32x4*>(c + (2 * i + (lane >> 5)) * 16) = x1p[i];
    *reinterpret_cast<u32x4*>(c + 128) = u32x4{mine, __float_as_uint(mb), __float_as_uint(rd), 0u};
  }
  lds_set((unsigned)(uintptr_t)smem + L_W + 32 + (tile & 3) * 4, epoch);
}

DI void attn_safe(rsrc_t rs, unsigned kvoff, const u32x8& q, const u32x4& ones, f32x16& o) {
  const s16x8 q0 = __builtin_bit_cast(s16x8, __builtin_shufflevector(q, q, 0, 1, 2, 3)), q1 = __builtin_bit_cast(s16x8, __builtin_shufflevector(q, q, 4, 5, 6, 7));
  f32x4 l = {0.f, 0.f, 0.f, 0.f};
#pragma unroll
  for (int e = 0; e < 16; ++e) o[e] = 0.f;
  float m = -INFINITY;
#pragma unroll 1
  for (int kt = 0; kt < 64; ++kt) {
    const unsigned p = kvoff + kt * 4096;
    const u32x4 k0 = ldg(rs, p), k1 = ldg(rs, p + 1024), v0 = ldg(rs, p + 2048), v1 = ldg(rs, p + 3072);
    f32x16 s = {};
    s = MFMA32(__builtin_bit_cast(s16x8, k0), q0, s);
    s = MFMA32(__builtin_bit_cast(s16x8, k1), q1, s);
    float mt = fmaxf(s[0], s[1]);
#pragma unroll
    for (int e = 2; e < 16; ++e) mt = fmaxf(mt, s[e]);
    const float mn = fmaxf(m, half_swap_max(mt));
    const float alpha = EX2(m - mn);
    m = mn;
#pragma unroll
    for (int e = 0; e < 16; ++e) { s[e] = EX2(s[e] - mn); o[e] *= alpha; }
    l[0] *= alpha;
    const bf16x16 pa = __builtin_bit_cast(bf16x16, __builtin_shufflevector(s, s, 0, 1, 2, 3, 4, 5, 6, 7));
    const bf16x16 pb = __builtin_bit_cast(bf16x16, __builtin_shufflevector(s, s, 8, 9, 10, 11, 12, 13, 14, 15));
    o = __builtin_amdgcn_smfmac_f32_32x32x32_bf16(__builtin_bit_cast(bf16x8, v0), pa, o, 0xdddd, 0, 0);
    l = __builtin_amdgcn_smfmac_f32_16x16x64_bf16(__builtin_bit_cast(bf16x8, ones), pa, l, 0xdddd, 0, 0);
    o = __builtin_amdgcn_smfmac_f32_32x32x32_bf16(__builtin_bit_cast(bf16x8, v1), pb, o, 0xdddd, 0, 0);
    l = __builtin_amdgcn_smfmac_f32_16x16x64_bf16(__builtin_bit_cast(bf16x8, ones), pb, l, 0xdddd, 0, 0);
  }
  o *= 1.0f / l[0];
}

extern "C" __global__ __launch_bounds__(512, 2) void mega_kernel(const u16* __restrict__ x0, const u16* __restrict__ wqkv, const u16* __restrict__ wo,
                                                              const u16* __restrict__ w_r, const u16* __restrict__ wg, const u16* __restrict__ wu,
                                                              const u16* __restrict__ wd, char* ws, u16* __restrict__ out) {
  __shared__ __attribute__((aligned(16))) char smem[LDS_BYTES];
  const int wid = __builtin_amdgcn_readfirstlane(threadIdx.x >> 6);
  STAMP_INIT;
  STAMPV(0);
  const int bid = blockIdx.x, xcd = bid & 7, loc = bid >> 3;
  const int tm = 8 * (xcd >> 2) + (loc >> 2), tn = 4 * (xcd & 3) + (loc & 3);
  const unsigned lbase = (unsigned)(uintptr_t)smem;
  const rsrc_t rs = __builtin_amdgcn_make_buffer_rsrc(ws, 0, (int)WS_BYTES, 0x00020000);
  volatile __attribute__((address_space(3))) unsigned* words = LDS_PTR(volatile unsigned, smem + L_W);
  u32x4 v[4];
  if (wid < 4)
    asm volatile("global_load_dwordx4 %0, %4, %5\n\tglobal_load_dwordx4 %1, %4, %5 offset:512\n\tglobal_load_dwordx4 %2, %4, %5 offset:1024\n\tglobal_load_dwordx4 %3, %4, %5 offset:1536"
                 : "=&v"(v[0]), "=&v"(v[1]), "=&v"(v[2]), "=&v"(v[3])
                 : "v"((threadIdx.x >> 5) * (D_ * 2) + (threadIdx.x & 31) * 16), "s"(x0 + (size_t)(tm * 128 + tn * 8) * D_) : "memory");
  else {
    asm volatile("" : "=v"(v[0]), "=v"(v[1]), "=v"(v[2]), "=v"(v[3]));
    if (wid == 6) {
      const char *c0, *c1;
      CODE_ADDR(mega_kernel, c0); CODE_ADDR(end_marker, c1);
      const unsigned o = (loc * 64 + (threadIdx.x & 63)) * 32;
      glds4(c0, (long long)o + 4 <= c1 - c0 ? o : 0u, lbase + NS * SLOTB);
    }
  }
  STAMPV(48);
  *reinterpret_cast<unsigned*>(smem + L_FULL + threadIdx.x * 4) = 0u;
  words[threadIdx.x & 31] = 0u;
  BAR_LGKM();
  if (wid < 4) {
    const int tid = threadIdx.x, lane = tid & 63, r32 = lane & 31, row = 2 * wid + (lane >> 5);
    char* sm = smem + L_XCH;
    unsigned dst = OFF_HF + (tm * 4 + (tn >> 2)) * TS + (tn & 3) * 128 + (tid >> 3) * 512 + (tid & 7) * 16;
    asm volatile("s_waitcnt vmcnt(0)" : "+v"(v[0]), "+v"(v[1]), "+v"(v[2]), "+v"(v[3]), "+v"(dst) :: "memory");
    STAMPV(49);
    float s = 0.f, s2 = 0.f;
#pragma unroll
    for (int i = 0; i < 4; ++i)
#pragma unroll
      for (int e = 0; e < 4; e += 2) { s = dot2(v[i][e], 0x3f803f80u, s); s2 = dot2(v[i][e + 1], 0x3f803f80u, s2); }
    const float mean = half_sum(s + s2) * (1.0f / 1024.0f);
    f32x2 m2 = {0.f, 0.f};
#pragma unroll
    for (int i = 0; i < 4; ++i)
#pragma unroll
      for (int e = 0; e < 4; ++e) { const f32x2 d = f32x2{lo16(v[i][e]), hi16(v[i][e])} - mean; m2 += d * d; }
    float mb, den;
    ln_scalars(mean, half_sum(m2[0] + m2[1]) * (1.0f / 1024.0f), mb, den);
    const float rd = 1.0f / den;
#pragma unroll
    for (int i = 0; i < 4; ++i) {
      char* p = sm + ((16 * i + (r32 >> 1)) * 2) * 144 + row * 16 + (r32 & 1) * 8;
      *reinterpret_cast<u32x2*>(p) = u32x2{ln_pair(v[i][0], mb, rd), ln_pair(v[i][1], mb, rd)};
      *reinterpret_cast<u32x2*>(p + 144) = u32x2{ln_pair(v[i][2], mb, rd), ln_pair(v[i][3], mb, rd)};
    }
    STAMPV(37);
    lds_set(lbase + L_W + 64 + 4 * wid, 1);
    lds_wait_ge<0>(lbase + L_W + 64 + 4 * (lane & 3), 1);
    STAMPV(50);
    u32x4 t[4];
#pragma unroll
    for (int i = 0; i < 4; ++i) t[i] = *reinterpret_cast<const u32x4*>(sm + (i * 32 + (tid >> 3)) * 144 + (tid & 7) * 16);
    SBAR();
#pragma unroll
    for (int i = 0; i < 4; ++i) stg(rs, dst + i * 16384, t[i]);
    STAMPV(51);
    DRAIN();
    lds_set(lbase + L_W + 64 + 4 * wid, 2);
    STAMPV(38);
  }
  const int lw = wid & 1;
  const char* wb = reinterpret_cast<const char*>(wqkv + (size_t)(16 * lw) * (3 * D_) + tn * 64);
  auto stage_lanes = [](unsigned (&wv)[3]) {
    const int lane = fresh_lane();
#pragma unroll
    for (int i = 0; i < 3; ++i) {
      const int li = lane + 64 * i, k = li / 24, cp = li % 24, c = cp ^ (4 * ((k >> 1) & 1));
      wv[i] = (unsigned)(k * (3 * D_ * 2) + (c >> 3) * (D_ * 2) + (c & 7) * 16 - i * 1024);
    }
  };
  auto stage = [&](const unsigned (&wv)[3], int s, int slot) {
    const char* p = wb + (size_t)(2 * kpos(tm, tn, s >> 1) + (s & 1)) * (32 * 3 * D_ * 2);
    glds_v3(p, wv, lbase + slot * SLOTB + lw * HALFB);
    glds_v3(p + 8 * (3 * D_ * 2), wv, lbase + slot * SLOTB + lw * HALFB + 3072);
  };
  auto exchange = [&](const u32x2& tag, unsigned line0, int me, bool bar = false) {
    const int lane = fresh_lane();
    __builtin_amdgcn_raw_buffer_store_b64(tag, rs, lane == 0 ? line0 + me * 128 : OOB, 0, 16);
    if (bar) BARRIER();
    JIT();
    unsigned po = lane < 16 ? line0 + lane * 128 : OOB;
    const u32x4 srd = {(unsigned)(uintptr_t)ws, (unsigned)((uintptr_t)ws >> 32) & 0xffffu, WS_BYTES, 0x00020000u};
    for (unsigned spins = 0;; ++spins) {
      u32x2 v = {0u, 0u};
      asm volatile("buffer_atomic_or_x2 %0, %1, %2, 0 offen sc0 sc1\n\ts_waitcnt vmcnt(0)" : "+v"(v) : "v"(po), "s"(srd) : "memory");
      po = ((v[0] == tag[0]) & (v[1] == tag[1])) ? OOB : po;
      if (__all(po == OOB) || spins > SPIN_CAP) break;
    }
  };
  if ((wid >> 1) == 2) {
    unsigned wv[3];
    stage_lanes(wv);
    const int r = tm & 7;
#pragma unroll 1
    for (int i = 0; i < NS; ++i) {
      const int s = i == 0 ? r : i <= r ? i - 1 : i;
      stage(wv, s, s);
      const int j = i - 1, full = j < 1 ? (j == 0 && r == 0) : j < r ? j : j + 1;
      asm volatile("s_waitcnt vmcnt(%2)\n\tds_write_b32 %0, %1" :: "v"(lbase + L_FULL + lw * 256), "v"(full), "n"(6 * 1) : "memory");
    }
    asm volatile("s_waitcnt vmcnt(0)\n\tds_write_b32 %0, %1" :: "v"(lbase + L_FULL + lw * 256), "v"(NS) : "memory");
    STAMPV(1);
  }
  if (wid == 7) {
    u32x2 tag = __builtin_amdgcn_raw_buffer_load_b64(rs, FL_EPOCH, 0, 16);
    tag[0] += 1u; tag[1] += 0x9E3779B9u;
    *reinterpret_cast<u32x2*>(smem + L_TAG) = tag;
    lds_wait_ge<0>(lbase + L_W + 64 + 4 * (fresh_lane() & 3), 2);
    exchange(tag, FL_X + tm * 2048, tn);
    lds_set(lbase + L_W + 24, 1);
  }
  if (wid >= 4) {
    auto wo_rows = [&](int g0, int n, int gs) {
      const int lane = fresh_lane();
      unsigned wov[4];
#pragma unroll
      for (int i = 0; i < 4; ++i) wov[i] = (unsigned)((8 * i + (lane >> 3)) * (D_ * 2) + ((lane & 7) ^ (4 * ((lane >> 4) & 1)) ^ (i >> 1)) * 16 - i * 1024);
#pragma unroll 1
      for (int i = 0, k = (0 * (tm & 7)) % n; i < n; ++i, k = k + 1 == n ? 0 : k + 1) {
        const int g = g0 + gs * k;
        glds_v4t<3, 1>(reinterpret_cast<const char*>(wo) + (size_t)((((tn >> 2) + 1) * 256 + 32 * g) & 1023) * (D_ * 2) + tn * 128, wov, lbase + g * 4096);
      }
    };
    if (wid < 6) {
      const int lane = fresh_lane();
      unsigned wv[3];
      stage_lanes(wv);
      __builtin_amdgcn_s_setprio(3);
      const unsigned fra = lbase + L_FREE + (lane & 3) * 256, fla = lbase + L_FULL + lw * 256 + lane * 4;
      u32x4 pre[NS * 6];
#pragma unroll
      for (int i = 0; i < NS * 6; ++i) pre[i] = *reinterpret_cast<const u32x4*>(smem + lw * HALFB + lane * 16 + (i / 6) * SLOTB + (i % 6) * 1024);
#pragma unroll
      for (int i = 0; i < NS * 6; ++i) PIN(pre[i]);
#pragma unroll 1
      for (int L = 0; L < 2; ++L) {
        {
          constexpr unsigned STG = 32 * 3 * D_ * 2;
          unsigned off = (2 * kpos(tm, tn, NS >> 1) + (NS & 1)) * STG, dst = lbase + lw * HALFB, need = 32 * L + 3, cnt = 31 - NS, pub = 32 * L + NS - DD, keep, spin, t0, t1;
          asm volatile(
            "s_mov_b32 %[keep], m0\n\ts_branch 3f\n"
            "1:\n\ts_cbranch_vccnz 3f\n"
            "2:\n\ts_add_u32 s56, %[blo], %[off]\n\ts_addc_u32 s57, %[bhi], 0\n\ts_mov_b32 m0, %[dst]\n\ts_add_u32 s58, s56, %[half]\n\t"
            GL_ "%[w0], s[56:57]\n\ts_nop 7\n\t" GL_ "%[w1], s[56:57] offset:1024\n\ts_nop 7\n\t" GL_ "%[w2], s[56:57] offset:2048\n\ts_nop 7\n\t"
            "s_addc_u32 s59, s57, 0\n\ts_add_u32 m0, %[dst], 3072\n\ts_add_u32 %[off], %[off], %[stg]\n\t"
            GL_ "%[w0], s[58:59]\n\ts_nop 7\n\t" GL_ "%[w1], s[58:59] offset:1024\n\ts_nop 7\n\t" GL_ "%[w2], s[58:59] offset:2048\n\ts_nop 7\n\t"
            "ds_read_b32 %[t0], %[fra]\n\t"
            "s_cmp_eq_u32 %[off], %[wrap]\n\ts_cselect_b32 %[off], 0, %[off]\n\ts_cmp_eq_u32 %[off], %[own]\n\ts_cselect_b32 %[off], %[own2], %[off]\n\t"
            "s_add_u32 %[dst], %[dst], %[slot]\n\ts_cmp_eq_u32 %[dst], %[dend]\n\ts_cselect_b32 %[dst], %[dbeg], %[dst]\n\t"
            "v_add_u32 %[pub], 1, %[pub]\n\ts_add_u32 %[need], %[need], 1\n\tv_max_u32 %[t1], %[pmin], %[pub]\n\t"
            "s_waitcnt vmcnt(%[vm]) lgkmcnt(0)\n\tds_write_b32 %[fla], %[t1]\n\tv_cmp_gt_u32 vcc, %[need], %[t0]\n\t"
            "s_sub_u32 %[cnt], %[cnt], 1\n\ts_cbranch_scc0 1b\n\ts_branch 5f\n"
            "3:\n\ts_mov_b32 %[spin], 0x100000\n"
            "4:\n\tds_read_b32 %[t0], %[fra]\n\ts_sub_u32 %[spin], %[spin], 1\n\ts_waitcnt lgkmcnt(0)\n\ts_cbranch_scc1 2b\n\t"
            "v_cmp_gt_u32 vcc, %[need], %[t0]\n\ts_cbranch_vccz 2b\n\ts_sleep 1\n\ts_branch 4b\n"
            "5:\n\ts_mov_b32 m0, %[keep]"
            : [off] "+&s"(off), [dst] "+&s"(dst), [need] "+&s"(need), [cnt] "+&s"(cnt), [pub] "+&v"(pub), [keep] "=&s"(keep), [spin] "=&s"(spin), [t0] "=&v"(t0), [t1] "=&v"(t1)
            : [blo] "s"((unsigned)(uintptr_t)wb), [bhi] "s"((unsigned)((uintptr_t)wb >> 32)), [half] "s"(STG / 4), [stg] "s"(STG), [wrap] "s"(32 * STG), [own] "s"(2 * tn * STG),
              [own2] "s"(2 * ((tn + 1) & 15) * STG), [slot] "s"(SLOTB), [dbeg] "s"(lbase + lw * HALFB), [dend] "s"(lbase + lw * HALFB + NS * SLOTB), [pmin] "s"(32 * L + NS),
              [w0] "v"(wv[0]), [w1] "v"(wv[1]), [w2] "v"(wv[2]), [fra] "v"(fra), [fla] "v"(fla), [vm] "n"(DD * 6)
            : "s56", "s57", "s58", "s59", "vcc", "scc", "memory");
        }
#pragma unroll
        for (int d = DD - 1; d >= 0; --d) asm volatile("s_waitcnt vmcnt(%0)\n\tds_write_b32 %1, %2" :: "n"(d * 6), "v"(fla), "v"(32 * L + 32 - d) : "memory");
        u32x2 tag = *reinterpret_cast<const u32x2*>(smem + L_TAG);
        PIN(tag);
        if (lw == 0) {
          lds_wait_ge(lbase + L_W + 96 + 4 * (lane & 1), 1);
          lds_wait_ge<0>(lbase + L_W + 80 + 4 * (lane & 3), 2 * L + 1);
          exchange(tag, FL_KV + (L * 16 + tn) * 2048, tm, true);
          lds_wait_ge(fra, 32 * L + 32);
          lds_set(lbase + L_W + 16, L + 1);
        } else { BAR_LGKM(); lds_wait_ge<8>(lbase + L_W + 16, L + 1); }
        __builtin_amdgcn_s_sleep(24);
        wo_rows(lw, 15, 2);
        STAMPV(7 + 16 * L);
        DRAIN();
        const unsigned fo = FL_O + (L * 16 + tm) * 2048;
        if (lw) {
          lds_set(lbase + L_W + 112, 2 * L + 1);
          lds_wait_ge<4>(lbase + L_W + 48 + 4 * (lane & 3), L + 1);
          lds_wait_ge<0>(lbase + L_W + 80 + 4 * (lane & 3), 2 * L + 2);
          __builtin_amdgcn_raw_buffer_store_b64(tag, rs, lane == 0 ? fo + tn * 128 : OOB, 0, 16);
          JIT();
          wo_rows(30, 2, 1);
          DRAIN();
          lds_set(lbase + L_W + 112, 2 * L + 2);
        } else {
          lds_wait_ge<4>(lbase + L_W + 112, 2 * L + 1);
          lds_wait_ge<4>(lbase + L_W + 48 + 4 * (lane & 3), L + 1);
          unsigned po = lane < 16 ? fo + ((lane + 4 + (tn & 12)) & 15) * 128 : OOB;
          const u32x4 srd = {(unsigned)(uintptr_t)ws, (unsigned)((uintptr_t)ws >> 32) & 0xffffu, WS_BYTES, 0x00020000u};
          for (unsigned spins = 0;; ++spins) {
            u32x2 v = {0u, 0u};
            asm volatile("buffer_atomic_or_x2 %0, %1, %2, 0 offen sc0 sc1\n\ts_waitcnt vmcnt(0)" : "+v"(v) : "v"(po), "s"(srd) : "memory");
            po = ((v[0] == tag[0]) & (v[1] == tag[1])) ? OOB : po;
            unsigned m = (unsigned)__ballot(po == OOB);
            m &= m >> 1; m &= m >> 2;
            m = (m & 1u) | ((m >> 3) & 2u) | ((m >> 6) & 4u) | ((m >> 9) & 8u);
            if (m == 15u || spins > SPIN_CAP) break;
            words[5 + 2 * L] = words[28] == 2u * L + 2u ? m : m & 7u;
          }
          words[5 + 2 * L] = words[28] == 2u * L + 2u ? 15u : 7u;
          lds_wait_ge<0>(lbase + L_W + 112, 2 * L + 2);
          words[5 + 2 * L] = 15u;
          __builtin_amdgcn_raw_buffer_store_b64(tag, rs, (bid == 0 && lane == 0) ? FL_EPOCH : OOB, 0, 16);
        }
        BAR_ALL();
        if (L == 0) {
          char* d = smem + lw * HALFB + fresh_lane() * 16;
#pragma unroll
          for (int s = 0; s < NS; ++s) {
#pragma unroll
            for (int i = 0; i < 6; ++i) *reinterpret_cast<u32x4*>(d + s * SLOTB + i * 1024) = pre[s * 6 + i];
            lds_set(fla, 32 + s + 1);
            if (s == 1) lds_wait_ge(lbase + L_W + 64 + 4 * (lane & 3), 3);
          }
          STAMPV(2);
          lds_wait_ge<2>(lbase + L_W + 24, 2);
        }
      }
      STAMP_END;
      return;
    }
    const int a = wid - 6;
    const bool ffn = loc < 28;
    const int grp = xcd * 7 + (ffn ? loc >> 2 : 0), qa = (loc & 3) * 2 + a;
    const int kr = loc, cg = xcd;
    const unsigned wa = lbase + L_W;
    lds_wait_ge<2>(wa + 24, 1);
    __builtin_amdgcn_s_sleep(16);
    auto transpose8 = [](u32x4* v) {
      u32x4 t[8];
#pragma unroll
      for (int j = 0; j < 8; ++j)
#pragma unroll
        for (int e = 0; e < 4; ++e) t[j][e] = (j & 1) ? PERM_HI(v[2 * e][j >> 1], v[2 * e + 1][j >> 1]) : PERM_LO(v[2 * e][j >> 1], v[2 * e + 1][j >> 1]);
#pragma unroll
      for (int j = 0; j < 8; ++j) { v[j] = t[j]; PIN(v[j]); }
    };
    u32x4 gf[32];
    u32x4 wdf[16];
    auto wait_go = [&](int L) {
      __builtin_amdgcn_s_setprio(1);
      lds_wait_ge<8>(lbase + L_W + 16, L + 1);
      __builtin_amdgcn_s_sleep(24);
    };
    auto load_wd = [&](unsigned dv) {
      const rsrc_t rd = __builtin_amdgcn_make_buffer_rsrc(const_cast<u16*>(wd) + (size_t)grp * 64 * D_ + 128 * qa, 0, 64 * D_ * 2 - 256 * qa, 0x00020000);
#pragma unroll
      for (int i = 0; i < 16; ++i) {
        wdf[i] = __builtin_amdgcn_raw_buffer_load_b128(rd, dv, (32 * (i >> 3) + (i & 7)) * (D_ * 2), 0);
        asm volatile("s_sleep 2\n\ts_waitcnt vmcnt(2)" ::: "memory");
      }
    };
    {
      LANE_VARS;
      if (a == 0) {
        const u32x4 w0 = *reinterpret_cast<const u32x4*>(w_r + lane * 8), w1 = *reinterpret_cast<const u32x4*>(w_r + 512 + lane * 8);
        float t = 0.f;
#pragma unroll
        for (int e = 0; e < 4; ++e) t = fadd(fadd(t, fadd(lo16(w0[e]), hi16(w0[e]))), fadd(lo16(w1[e]), hi16(w1[e])));
        words[0] = __float_as_uint(wave_sum(t));
        if ((lane >> 3) == (tn & 7)) *reinterpret_cast<u32x4*>(smem + L_WR + (lane & 7) * 16) = tn < 8 ? w0 : w1;
      }
      const u16* p = wqkv + (size_t)(64 * tm + 32 * a + hi) * (3 * D_) + (c24 >> 3) * D_ + tn * 64 + (c24 & 7) * 8;
      float cs[8] = {0.f, 0.f, 0.f, 0.f, 0.f, 0.f, 0.f, 0.f};
      u32x4 v[16];
#pragma unroll
      for (int i = 0; i < 16; ++i) v[i] = *reinterpret_cast<const u32x4*>(p + (size_t)i * (2 * 3 * D_));
      SBAR();
      const u32x4 sv = {SENT, SENT, SENT, SENT};
#pragma unroll
      for (int i = 0; i < 6; ++i) {
        const unsigned o = ((i * 256 + bid) * 128 + a * 64 + lane) * 16;
        stg(rs, o < SX_BYTES ? OFF_SX + o : OOB, sv);
      }

#pragma unroll
      for (int i = 0; i < 16; ++i)
#pragma unroll
        for (int e = 0; e < 4; ++e) { cs[2 * e] = fadd(cs[2 * e], lo16(v[i][e])); cs[2 * e + 1] = fadd(cs[2 * e + 1], hi16(v[i][e])); }
      u32x4 o;
#pragma unroll
      for (int e = 0; e < 4; ++e) { const float x = half_swap_sum(cs[e]), y = half_swap_sum(cs[4 + e]); o[e] = __float_as_uint(hi ? y : x); }
      stg(rs, r32 < 24 ? OFF_CS + (tn * 32 + tm * 2 + a) * 768 + r32 * 32 + hi * 16 : OOB, o);
    }
    {
      DRAIN();
      lds_set(wa + 96 + 4 * a, 1);
      BAR_LGKM();
      LANE_VARS;
      uintptr_t gp = reinterpret_cast<uintptr_t>(((lane & 8) ? wu : wg) + (size_t)(128 * qa + 16 * hi + 4 * tblk) * F_ + 64 * grp + (lane & 7) * 8);
      unsigned dv = (lane >> 4) * (8 * D_ * 2) + (lane & 15) * 16;
      PIN(gp); PIN(dv);
      unsigned sv[3];
#pragma unroll
      for (int i = 0; i < 3; ++i) { const int li = lane + 64 * i, k = li / 24, c = li % 24; sv[i] = (unsigned)(k * (3 * D_ * 2) + (c >> 3) * (D_ * 2) + (c & 7) * 16 - i * 1024); PIN(sv[i]); }
      wait_go(0);
#pragma unroll
      for (int i = 0; i < 32; ++i) {
        gf[i] = *reinterpret_cast<const __attribute__((address_space(1))) u32x4*>(gp + (size_t)(32 * (i >> 3) + 8 * ((i >> 2) & 1) + (i & 3)) * (F_ * 2));
        asm volatile("s_sleep 2\n\ts_waitcnt vmcnt(2)" ::: "memory");
      }
      load_wd(dv);
      STAMPV(7);
      __builtin_amdgcn_s_setprio(0);
      lds_wait_ge<4>(wa + 80 + 4 * (lane & 3), 2);
#pragma unroll
      for (int g = 0; g < 4; ++g) glds_v3(wqkv + (size_t)(32 * kr + 8 * g) * (3 * D_) + (2 * cg + a) * 64, sv, lbase + L_SL + a * 12288 + g * 3072);
      transpose8(gf); transpose8(gf + 8); transpose8(gf + 16); transpose8(gf + 24);
      transpose8(wdf); transpose8(wdf + 8);
      {
        LANE_VARS;
        f32x4 t[2];
#pragma unroll
        for (int c = 0; c < 2; ++c) {
          u32x4 v[8];
#pragma unroll
          for (int i = 0; i < 8; ++i) v[i] = ldg(rs, OFF_CS + (tn * 32 + 16 * c + 2 * i + hi) * 768 + a * 384 + c24 * 16);
          SBAR();
          t[c] = sum4<8>(v);
          SBAR();
        }
#pragma unroll
        for (int e = 0; e < 4; ++e) t[0][e] = half_swap_sum(fadd(t[0][e], t[1][e]));
        if (lane < 24) *reinterpret_cast<f32x4*>(smem + L_CS + a * 384 + lane * 16) = t[0];
      }
      u32x2 tag = *reinterpret_cast<const u32x2*>(smem + L_TAG);
      PIN(tag);
      BAR_ALL();
      if (a) {
        lds_wait_ge<0>(wa + 64 + 4 * (lane & 3), 3);
        exchange(tag, FL_X + (16 + tm) * 2048, tn);
        lds_set(wa + 24, 2);
        STAMPV(12);
      }
    }
#pragma unroll 1
    for (int L = fresh_s(0);; ++L) {
      const unsigned sx = OFF_SX + L * SX_LAYER;
      STAMPV(1 + 16 * L);
      __builtin_amdgcn_s_setprio(1);
      u32x4 hf[4];
      unsigned key;
      { LANE_VARS;
      lds_wait_ge<0>(wa + 32 + 4 * tp, L + 1);
      if (a == 0) {
        const char* cp = smem + L_CAND + (r32 >> 3) * 144;
        const u32x4 c = *reinterpret_cast<const u32x4*>(cp + (lane & 7) * 16), rec = *reinterpret_cast<const u32x4*>(cp + 128);
        u32x4 o;
#pragma unroll
        for (int e = 0; e < 4; ++e) { unsigned h = ln_pair_s(c[e], -U2F(rec[1]), U2F(rec[2])); PIN(h); o[e] = nz(hi ? c[e] : h); }
        stg(rs, sx + SX_ROW + hi * 131072 + (tm * 4 + (r32 >> 3)) * 2048 + tn * 128 + (lane & 7) * 16, o);
        STAMPV(8 + 16 * L);
        lds_wait_ge<0>(wa + 104, L + 1);
        key = __builtin_amdgcn_readfirstlane(words[27]);
      } else {
        const u32x4 srd = {(unsigned)(uintptr_t)ws, (unsigned)((uintptr_t)ws >> 32) & 0xffffu, WS_BYTES, 0x00020000u};
        unsigned po = sx + SX_CAND + (tn * 16 + (lane >> 2)) * 128 + tp * 4;
        key = 0u;
        for (unsigned spins = 0;; ++spins) {
          unsigned v = 0u;
          asm volatile("buffer_atomic_or %0, %1, %2, 0 offen sc0 sc1\n\ts_waitcnt vmcnt(0)" : "+v"(v) : "v"(po), "s"(srd) : "memory");
          key = v == SENT ? key : max(key, v);
          po = v == SENT ? po : OOB;
          if (__all(po == OOB) || spins > SPIN_CAP) break;
        }
        key = __builtin_amdgcn_readfirstlane(wave_max_u32(key));
        words[27] = key;
        words[3] = 2047u - (key & 2047u);
        lds_set(wa + 104, L + 1);
        STAMPV(13 + 16 * L);
      }
      poll_all<4>(hf, false, [&](u32x4 (&t)[4]) {
#pragma unroll
        for (int i = 0; i < 4; ++i) t[i] = ldg(rs, sx + SX_ROW + ((2047 - (int)(key & 2047u)) >> 5) * 2048 + (16 * qa + 4 * i + (lane >> 4)) * 16);
      }, [&](const u32x4 (&t)[4]) { return none_sent(t); });
      if (L == 1) lds_set(wa + 4 + 4 * a, 2);
      }
      STAMPV(2 + 16 * L);
      f32x4 d[8];
      const f32x4 zero = {0.f, 0.f, 0.f, 0.f};
      SBAR();
      __builtin_amdgcn_s_setprio(3);
#pragma unroll
      for (int t = 0; t < 32; ++t) d[t & 7] = MFMA16(__builtin_bit_cast(s16x8, hf[t >> 3]), __builtin_bit_cast(s16x8, gf[t]), t < 8 ? zero : d[t & 7]);
      SBAR();
      __builtin_amdgcn_s_setprio(1);
      LANE_VARS;
      const int idx = 2047 - (int)(key & 2047u), win = idx >> 7;
      const bool owner = win == tm;
      auto pick2 = [&](const f32x4 (&d)[8]) {
        u32x2 o;
#pragma unroll
        for (int e = 0; e < 2; ++e) {
          const float x = tblk ? d[2 + e][0] : d[e][0], y = tblk ? d[6 + e][0] : d[4 + e][0];
          o[e] = nzf(hi ? y : x);
        }
        return o;
      };
      if (ffn) {
        const unsigned ppos = (lane & 15) * 32 + (lane >> 4) * 8;
        __builtin_amdgcn_raw_buffer_store_b64(pick2(d), rs, sx + SX_GP + (grp * 8 + qa) * 512 + ppos, 0, 16);
        STAMPV(9 + 16 * L);
        __builtin_amdgcn_s_sleep(16);
        u32x2 gu[8];
        for (unsigned spins = 0;; ++spins) {
#pragma unroll
          for (int i = 0; i < 8; ++i) gu[i] = __builtin_amdgcn_raw_buffer_load_b64(rs, sx + SX_GP + (grp * 8 + i) * 512 + lane * 8, 0, 16);
          SBAR();
          unsigned m = 0u;
#pragma unroll
          for (int i = 0; i < 8; ++i) m = max(max(m, gu[i][0]), gu[i][1]);
          if (__all(m != SENT) || spins > SPIN_CAP) break;
          poll_pause(L == 0);
        }
        STAMPV(10 + 16 * L);
        float t0 = opq(U2F(gu[0][0])), t1 = opq(U2F(gu[0][1]));
#pragma unroll
        for (int i = 1; i < 8; ++i) { t0 = fadd(t0, U2F(gu[i][0])); t1 = fadd(t1, U2F(gu[i][1])); }
        auto sw = __builtin_amdgcn_permlane32_swap(__float_as_uint(t0), __float_as_uint(t1), false, false);
        const float gv = rbf(U2F(sw[0])), uv = rbf(U2F(sw[1]));
        reinterpret_cast<u16*>(smem + L_GU + a * 128)[2 * r32 + hi] = (u16)pack2(fmul(rbf(fmul(gv, RCP(fadd(1.0f, __expf(-gv))))), uv), 0.f);
        asm volatile("s_waitcnt lgkmcnt(0)" ::: "memory");
        const s16x8 a0 = *reinterpret_cast<const s16x8*>(smem + L_GU + a * 128 + (lane >> 4) * 16), a1 = *reinterpret_cast<const s16x8*>(smem + L_GU + a * 128 + 64 + (lane >> 4) * 16);
        SBAR();
        __builtin_amdgcn_s_setprio(3);
#pragma unroll
        for (int t = 0; t < 16; ++t) d[t & 7] = MFMA16(t < 8 ? a0 : a1, __builtin_bit_cast(s16x8, wdf[t]), t < 8 ? zero : d[t & 7]);
        SBAR();
        __builtin_amdgcn_s_setprio(1);
        __builtin_amdgcn_raw_buffer_store_b64(pick2(d), rs, sx + SX_YP + grp * 4096 + qa * 512 + ppos, 0, 16);
      }
      STAMPV(3 + 16 * L);
      if (L == 1 && (cg != (kr & 7) || a)) { STAMP_END; __builtin_amdgcn_endpgm(); }
      s16x8 wpf[12];
#pragma unroll
      for (int t = 0; t < 12; ++t) wpf[t] = tr2(smem + L_SL + a * 12288 + (16 * hi + 4 * tblk + tq) * 384 + t * 32 + tp * 8, 8 * 384);
#pragma unroll
      for (int t = 0; t < 12; ++t) PIN(wpf[t]);
      if (L == 0 && !owner) lds_set(wa + 4 + 4 * a, 1);
      __builtin_amdgcn_s_sleep(9);
      if (L == 0 && !ffn) __builtin_amdgcn_s_sleep(64);
      const unsigned rk = key >> 16;
      float rsig = rbf(RCP(fadd(1.0f, __expf(-lo16((rk & 0x8000u) ? (rk & 0x7fffu) : (~rk & 0xffffu))))));
      PIN(rsig);
      const int n = 4 * (lane & 7) + 2 * tblk + hi;
      const int fo = ((n >> 4) * 2 + ((n >> 2) & 1)) * 8 + ((n >> 3) & 1) * 4 + (n & 3);
      unsigned odst = (L == 1 && !(lane & 8)) ? (idx * D_ + kr * 32 + n) * 2 : OOB;
      PIN(odst);
      u32x4 pv[8];
      poll_all<8>(pv, L == 0, [&](u32x4 (&t)[8]) {
#pragma unroll
        for (int i = 0; i < 7; ++i) t[i] = ldg(rs, sx + SX_YP + (8 * i + (lane >> 3)) * 4096 + kr * 128 + (lane & 7) * 16);
        t[7][0] = t[7][2] = __builtin_amdgcn_raw_buffer_load_b32(rs, sx + SX_ROW + (idx >> 5) * 2048 + kr * 64 + (fo >> 1) * 4, 0, 16);
        t[7][1] = t[7][3] = __builtin_amdgcn_raw_buffer_load_b32(rs, sx + SX_ROW + 131072 + (idx >> 5) * 2048 + kr * 64 + (fo >> 1) * 4, 0, 16);
      }, [&](const u32x4 (&t)[8]) { return none_sent(t); });
      const float hv = (fo & 1) ? hi16(pv[7][0]) : lo16(pv[7][0]), xv = (fo & 1) ? hi16(pv[7][1]) : lo16(pv[7][1]);
      STAMPV(4 + 16 * L);
      float ys;
      {
        const f32x4 t = sum4<7>(pv);
        auto p = __builtin_amdgcn_permlane32_swap(__float_as_uint(t[0]), __float_as_uint(t[1]), false, false);
        auto q = __builtin_amdgcn_permlane32_swap(__float_as_uint(t[2]), __float_as_uint(t[3]), false, false);
        auto r = __builtin_amdgcn_permlane16_swap(__float_as_uint(fadd(U2F(p[0]), U2F(p[1]))), __float_as_uint(fadd(U2F(q[0]), U2F(q[1]))), false, false);
        ys = fadd(U2F(r[0]), U2F(r[1]));
        ys = fadd(ys, U2F(dpp<0x128>(__float_as_uint(ys))));
      }
      const unsigned ob = pack2(fadd(xv, rbf(fadd(hv, rbf(fmul(rbf(ys), rsig))))), 0.f) & 0xffffu;
      __builtin_amdgcn_raw_buffer_store_b16((u16)ob, __builtin_amdgcn_make_buffer_rsrc(out, 0, 2048 * D_ * 2, 0x00020000), odst, 0, 0);
      if (L == 1) {
        STAMPV(5 + 16 * L);
        STAMP_END;
        __builtin_amdgcn_endpgm();
      }
      reinterpret_cast<u16*>(smem + L_X2F + a * 64)[fo] = (u16)ob;
      asm volatile("s_waitcnt lgkmcnt(0)" ::: "memory");
      {
        const s16x8 xa = *reinterpret_cast<const s16x8*>(smem + L_X2F + a * 64 + (lane >> 4) * 16);
        const u32x4 xf = *reinterpret_cast<const u32x4*>(smem + L_X2F + a * 64 + tp * 16);
        f32x4 z[12];
        SBAR();
        __builtin_amdgcn_s_setprio(3);
#pragma unroll
        for (int t = 0; t < 12; ++t) z[t] = MFMA16(xa, wpf[t], zero);
        SBAR();
        __builtin_amdgcn_s_setprio(1);
        const float x2 = lo16(ob), s1 = fmul(wave_sum(x2), 0.5f), s2 = fmul(wave_sum(fmul(x2, x2)), 0.5f);
        u32x4 o;
#pragma unroll
        for (int e = 0; e < 4; ++e) {
          const unsigned sp = lane < 52 ? (e == 0 ? __float_as_uint(s1) : __float_as_uint(s2)) : xf[e];
          const unsigned x = __float_as_uint(tblk ? z[4 + e][0] : z[e][0]), y = tblk ? sp : __float_as_uint(z[8 + e][0]);
          o[e] = nz(hi ? y : x);
        }
        const unsigned pos = lane < 24 ? lane * 16 : lane < 48 ? lane * 16 + 128 : lane < 52 ? (lane & 1) * 512 + 384 : 400 + tp * 16;
        stg(rs, lane < 56 ? OFF_SX + SX_QP + ((2 * cg + a) * 32 + kr) * 1024 + pos : OOB, o);
      }
      STAMPV(5 + 16 * L);
      if (owner) {
        __builtin_amdgcn_s_sleep(13);
        const int c25 = r32 < 25 ? r32 : 24, pl = 24 * a + c24, col = 64 * (pl >> 4) + (pl & 15);
        const int pfo = (col & ~15) + ((col >> 2) & 1) * 16 + ((col >> 3) & 1) * 8 + (col & 3);
        float cs[4];
#pragma unroll
        for (int e = 0; e < 4; ++e) { cs[e] = reinterpret_cast<const float*>(smem + L_CS)[col + 16 * e]; PIN(cs[e]); }
        u32x4 qv[17];
        poll_all<17>(qv, true, [&](u32x4 (&t)[17]) {
#pragma unroll
          for (int i = 0; i < 16; ++i) t[i] = ldg(rs, OFF_SX + SX_QP + (tn * 32 + 2 * i + hi) * 1024 + a * 512 + c25 * 16);
          t[16] = ldg(rs, OFF_SX + SX_QP + (tn * 34 + hi) * 1024 + 400 + tp * 16);
        }, [&](const u32x4 (&t)[17]) { return none_sent(t); });
        *reinterpret_cast<u32x4*>(smem + L_X2P + hi * 64 + tp * 16) = qv[16];
        f32x4 t = sum4<16>(qv);
#pragma unroll
        for (int e = 0; e < 4; ++e) t[e] = half_swap_sum(t[e]);
        const float mean = fmul(U2F(__builtin_amdgcn_readlane(__float_as_uint(t[0]), 24)), 1.0f / 1024.0f);
        const float var = fmaxf(ffma(-mean, mean, fmul(U2F(__builtin_amdgcn_readlane(__float_as_uint(t[1]), 24)), 1.0f / 1024.0f)), 0.f);
        float mb, den;
        ln_scalars(mean, var, mb, den);
        const float rd = RCP(den);
        if (lane < 24) {
#pragma unroll
          for (int e = 0; e < 4; ++e) reinterpret_cast<u16*>(smem + L_PATCH)[pfo + 32 * (e >> 1) + 4 * (e & 1)] = (u16)pack2(fmul(ffma(-mb, cs[e], t[e]), pl < 16 ? fmul(rd, QS) : rd), 0.f);
        }
        lds_set(wa + 4 + 4 * a, L + 1);
      }
      STAMPV(6 + 16 * L);
      BAR_ALL();
      unsigned dv = fresh_lane();
      dv = (dv >> 4) * (8 * D_ * 2) + (dv & 15) * 16;
      PIN(dv);
      wait_go(1);
      load_wd(dv);
      STAMPV(23);
      __builtin_amdgcn_s_setprio(0);
      lds_wait_ge<4>(wa + 80 + 4 * (fresh_lane() & 3), 4);
      transpose8(wdf); transpose8(wdf + 8);
      BARRIER();
    }
  }
  const int tm_ = tm, tn_ = tn, wid_ = wid;
#define KSKIP "{s49}"(tn * 4096), "{s50}"(((tn + 1) & 15) * 4096)
  const u32x4 srd = {(unsigned)(uintptr_t)ws, (unsigned)((uintptr_t)ws >> 32) & 0xffffu, WS_BYTES, 0x00020000u};
  u32x4 x1p[4];
  asm volatile("" : "=v"(x1p[0]), "=v"(x1p[1]), "=v"(x1p[2]), "=v"(x1p[3]));
#pragma unroll 1
  for (int L = 0; L < 2; ++L) {
    const unsigned sx = OFF_SX + L * SX_LAYER;
    u32x8 q[4];
    u32x16 kva, kvb;
    f32x16 oz[5];
    {
      f32x16 acc[6];
      u32x16 stA, stB, xr1, xr2, xr3;
      u32x8 xra, xrb;
      u32x4 stC;
      asm volatile("" : "={v[112:127]}"(stB), "={v[128:131]}"(stC));
      { PHASE_VARS;
      stB[8] = (L ? OFF_X1 : OFF_HF) + tile * TS + tblk * 1024 + (hi * 32 + (lane & 15)) * 16;
      stB[9] = lbase + tblk * HALFB + wrow * 384 + (((4 * hh + (tp >> 1)) ^ wsw) << 4) + 8 * (tp & 1);
      stB[10] = lbase + tblk * HALFB + wrow * 384 + (((4 * (hh ^ 1) + (tp >> 1)) ^ wsw) << 4) + 8 * (tp & 1);
      stB[13] = lbase + L_FULL + (lane & 1) * 256;
      stB[15] = lbase + L_FREE + wid * 256 + lane * 4;
      stC[0] = 32 * L + 2; }
      unsigned soff, slot;
      if (L == 0) {
        lds_wait_ge<0>(lbase + L_W + 24, 1);
        STAMPV(39);
        soff = tn * 4096;
        asm volatile(
          "v_add_u32 v131, s44, v120\n\t"
          "buffer_load_dwordx4 v[132:135], v131, s[40:43], 0 offen offset:0 sc1\n\t"
          "buffer_load_dwordx4 v[136:139], v131, s[40:43], 0 offen offset:256 sc1\n\t"
          "buffer_load_dwordx4 v[140:143], v131, s[40:43], 0 offen offset:2048 sc1\n\t"
          "buffer_load_dwordx4 v[144:147], v131, s[40:43], 0 offen offset:2304 sc1\n\t"
          "s_mov_b32 s44, s51\n\t"
          "v_add_u32 v131, s44, v120\n\t"
          "buffer_load_dwordx4 v[148:151], v131, s[40:43], 0 offen offset:0 sc1\n\t"
          "buffer_load_dwordx4 v[152:155], v131, s[40:43], 0 offen offset:256 sc1\n\t"
          "buffer_load_dwordx4 v[156:159], v131, s[40:43], 0 offen offset:2048 sc1\n\t"
          "buffer_load_dwordx4 v[160:163], v131, s[40:43], 0 offen offset:2304 sc1\n\t"
          "s_addk_i32 s44, 0x1000\n\t"
          "s_bitset0_b32 s44, 16\n\t"
          "s_cmp_eq_u32 s44, s49\n\t"
          "s_cselect_b32 s44, s50, s44\n\t"
          "v_add_u32 v131, s44, v120\n\t"
          "buffer_load_dwordx4 v[164:167], v131, s[40:43], 0 offen offset:0 sc1\n\t"
          "buffer_load_dwordx4 v[168:171], v131, s[40:43], 0 offen offset:256 sc1\n\t"
          "buffer_load_dwordx4 v[172:175], v131, s[40:43], 0 offen offset:2048 sc1\n\t"
          "buffer_load_dwordx4 v[176:179], v131, s[40:43], 0 offen offset:2304 sc1\n\t"
          "s_addk_i32 s44, 0x1000\n\t"
          "s_bitset0_b32 s44, 16\n\t"
          "s_cmp_eq_u32 s44, s49\n\t"
          "s_cselect_b32 s44, s50, s44\n\t"
          "v_add_u32 v131, s44, v120\n\t"
          "buffer_load_dwordx4 v[180:183], v131, s[40:43], 0 offen offset:0 sc1\n\t"
          "buffer_load_dwordx4 v[184:187], v131, s[40:43], 0 offen offset:256 sc1\n\t"
          "buffer_load_dwordx4 v[188:191], v131, s[40:43], 0 offen offset:2048 sc1\n\t"
          "buffer_load_dwordx4 v[192:195], v131, s[40:43], 0 offen offset:2304 sc1\n\t"
          : "={v[132:139]}"(xra), "={v[140:147]}"(xrb), "={v[148:163]}"(xr1), "={v[164:179]}"(xr2), "={v[180:195]}"(xr3), "+{s44}"(soff), "+{v[128:131]}"(stC) : "{v[112:127]}"(stB), "{s[40:43]}"(srd), KSKIP, "{s51}"(kpos(tm, tn, 1) * 4096) : "scc", "memory");
      } else {
        u32x4 t[4] = {x1p[0], x1p[1], x1p[2], x1p[3]};
        row_swap(t[0], t[1]); row_swap(t[2], t[3]);
        xra = __builtin_shufflevector(t[0], t[1], 0, 1, 2, 3, 4, 5, 6, 7); xrb = __builtin_shufflevector(t[2], t[3], 0, 1, 2, 3, 4, 5, 6, 7);
        asm volatile("" : "={v[148:163]}"(xr1), "={v[164:179]}"(xr2), "={v[180:195]}"(xr3));
      }
      STAMPV(1 + 16 * L);
      asm volatile(
        "v_mov_b32 v123, v121\n\t"
        "v_mov_b32 v124, v122\n\t"
        "s_mov_b64 vcc, -1\n\t"
        "s_cbranch_vccnz 10f\n\t"
        "11:\n\t"
        "s_movk_i32 s46, 12352\n\t"
        "s_mov_b32 s47, 111168\n\t"
        "ds_read_b64_tr_b16 v[96:97], v123 offset:256\n\t"
        "ds_read_b64_tr_b16 v[98:99], v123 offset:3328\n\t"
        "ds_read_b64_tr_b16 v[100:101], v123 offset:288\n\t"
        "ds_read_b64_tr_b16 v[102:103], v123 offset:3360\n\t"
        "ds_read_b64_tr_b16 v[104:105], v124 offset:256\n\t"
        "ds_read_b64_tr_b16 v[106:107], v124 offset:3328\n\t"
        "ds_write_b32 v127, v128\n\t"
        "ds_read_b64_tr_b16 v[108:109], v124 offset:288\n\t"
        "ds_read_b64_tr_b16 v[110:111], v124 offset:3360\n\t"
        "ds_read_b64_tr_b16 v[112:113], v123 offset:128\n\t"
        "ds_read_b64_tr_b16 v[114:115], v123 offset:3200\n\t"
        "v_add_u32 v128, 2, v128\n\t"
        "s_waitcnt vmcnt(14) lgkmcnt(7)\n\t"
        "v_mfma_f32_16x16x32_bf16 v[64:67], v[132:135], v[96:99], 0\n\t"
        "ds_read_b64_tr_b16 v[116:117], v123 offset:160\n\t"
        "v_mfma_f32_16x16x32_bf16 v[72:75], v[136:139], v[96:99], 0\n\t"
        "ds_read_b64_tr_b16 v[118:119], v123 offset:3232\n\t"
        "v_mfma_f32_16x16x32_bf16 v[68:71], v[132:135], v[100:103], 0\n\t"
        "ds_read_b64_tr_b16 v[96:97], v124 offset:128\n\t"
        "v_mfma_f32_16x16x32_bf16 v[76:79], v[136:139], v[100:103], 0\n\t"
        "ds_read_b64_tr_b16 v[98:99], v124 offset:3200\n\t"
        "s_waitcnt lgkmcnt(6)\n\t"
        "v_mfma_f32_16x16x32_bf16 v[80:83], v[132:135], v[104:107], 0\n\t"
        "ds_read_b64_tr_b16 v[100:101], v124 offset:160\n\t"
        "v_mfma_f32_16x16x32_bf16 v[88:91], v[136:139], v[104:107], 0\n\t"
        "ds_read_b64_tr_b16 v[102:103], v124 offset:3232\n\t"
        "v_mfma_f32_16x16x32_bf16 v[84:87], v[132:135], v[108:111], 0\n\t"
        "ds_read_b64_tr_b16 v[104:105], v123 offset:0\n\t"
        "v_add_u32 v129, s46, v121\n\t"
        "v_mfma_f32_16x16x32_bf16 v[92:95], v[136:139], v[108:111], 0\n\t"
        "ds_read_b64_tr_b16 v[106:107], v123 offset:3072\n\t"
        "s_waitcnt lgkmcnt(6)\n\t"
        "v_mfma_f32_16x16x32_bf16 v[32:35], v[112:115], v[132:135], 0\n\t"
        "ds_read_b64_tr_b16 v[108:109], v123 offset:32\n\t"
        "v_mfma_f32_16x16x32_bf16 v[40:43], v[112:115], v[136:139], 0\n\t"
        "ds_read_b64_tr_b16 v[110:111], v123 offset:3104\n\t"
        "v_add_u32 v130, s46, v122\n\t"
        "v_mfma_f32_16x16x32_bf16 v[36:39], v[116:119], v[132:135], 0\n\t"
        "ds_read_b64_tr_b16 v[112:113], v124 offset:0\n\t"
        "v_mfma_f32_16x16x32_bf16 v[44:47], v[116:119], v[136:139], 0\n\t"
        "ds_read_b64_tr_b16 v[114:115], v124 offset:3072\n\t"
        "s_waitcnt lgkmcnt(6)\n\t"
        "v_mfma_f32_16x16x32_bf16 v[48:51], v[96:99], v[132:135], 0\n\t"
        "ds_read_b64_tr_b16 v[116:117], v124 offset:32\n\t"
        "v_mfma_f32_16x16x32_bf16 v[56:59], v[96:99], v[136:139], 0\n\t"
        "ds_read_b64_tr_b16 v[118:119], v124 offset:3104\n\t"
        "v_mfma_f32_16x16x32_bf16 v[52:55], v[100:103], v[132:135], 0\n\t"
        "ds_read_b64_tr_b16 v[96:97], v129 offset:256\n\t"
        "s_addk_i32 s46, 12352\n\t"
        "v_mfma_f32_16x16x32_bf16 v[60:63], v[100:103], v[136:139], 0\n\t"
        "ds_read_b64_tr_b16 v[98:99], v129 offset:3328\n\t"
        "s_waitcnt lgkmcnt(6)\n\t"
        "v_mfma_f32_16x16x32_bf16 v[0:3], v[104:107], v[132:135], 0\n\t"
        "ds_read_b64_tr_b16 v[100:101], v129 offset:288\n\t"
        "s_cmp_eq_u32 s46, s47\n\t"
        "v_mfma_f32_16x16x32_bf16 v[8:11], v[104:107], v[136:139], 0\n\t"
        "ds_read_b64_tr_b16 v[102:103], v129 offset:3360\n\t"
        "s_cselect_b32 s46, 0, s46\n\t"
        "v_mfma_f32_16x16x32_bf16 v[4:7], v[108:111], v[132:135], 0\n\t"
        "ds_read_b64_tr_b16 v[104:105], v130 offset:256\n\t"
        "v_mfma_f32_16x16x32_bf16 v[12:15], v[108:111], v[136:139], 0\n\t"
        "ds_read_b64_tr_b16 v[106:107], v130 offset:3328\n\t"
        "s_waitcnt lgkmcnt(6)\n\t"
        "v_mfma_f32_16x16x32_bf16 v[16:19], v[112:115], v[132:135], 0\n\t"
        "ds_read_b64_tr_b16 v[108:109], v130 offset:288\n\t"
        "v_mfma_f32_16x16x32_bf16 v[24:27], v[112:115], v[136:139], 0\n\t"
        "ds_read_b64_tr_b16 v[110:111], v130 offset:3360\n\t"
        "v_mfma_f32_16x16x32_bf16 v[20:23], v[116:119], v[132:135], 0\n\t"
        "ds_read_b64_tr_b16 v[112:113], v129 offset:128\n\t"
        "v_mfma_f32_16x16x32_bf16 v[28:31], v[116:119], v[136:139], 0\n\t"
        "ds_read_b64_tr_b16 v[114:115], v129 offset:3200\n\t"
        "s_waitcnt lgkmcnt(0)\n\t"
        "s_nop 7\n\t"
        "s_branch 9f\n\t"
        "10:\n\t"
        "s_mov_b32 s48, 0x10000\n\t"
        "8:\n\t"
        "ds_read_b32 v126, v125\n\t"
        "s_sub_u32 s48, s48, 1\n\t"
        "s_waitcnt lgkmcnt(0)\n\t"
        "s_cbranch_scc1 11b\n\t"
        "v_cmp_gt_u32 vcc, v128, v126\n\t"
        "s_cbranch_vccnz 8b\n\t"
        "s_branch 11b\n\t"
        "9:\n\t"
        : "={v[0:15]}"(acc[0]), "={v[16:31]}"(acc[1]), "={v[32:47]}"(acc[2]), "={v[48:63]}"(acc[3]), "={v[64:79]}"(acc[4]), "={v[80:95]}"(acc[5]),
          "={v[96:111]}"(stA), "+{v[112:127]}"(stB), "+{v[128:131]}"(stC), "={s46}"(slot), "+{v[140:147]}"(xrb)
        : "{v[132:139]}"(xra) : "s47", "s48", "vcc", "scc", "memory");
      STAMPV(2 + 16 * L);
      if (L == 1) {
        PHASE_VARS;
        float S, Q, Dd;
        __builtin_amdgcn_s_sleep(0);
        row_stats_sum(rs, OFF_SX, tile, lane, S, Q, Dd);
        STAMPV(36);
        auto request_x = [&]() {
          lds_wait_ge<0>(lbase + L_W + 24, 2);
          soff = kpos(tm, tn, 1) * 4096;
          asm volatile(
          "v_add_u32 v131, s44, v120\n\t"
          "buffer_load_dwordx4 v[148:151], v131, s[40:43], 0 offen offset:0 sc1\n\t"
          "buffer_load_dwordx4 v[152:155], v131, s[40:43], 0 offen offset:256 sc1\n\t"
          "buffer_load_dwordx4 v[156:159], v131, s[40:43], 0 offen offset:2048 sc1\n\t"
          "buffer_load_dwordx4 v[160:163], v131, s[40:43], 0 offen offset:2304 sc1\n\t"
          "s_addk_i32 s44, 0x1000\n\t"
          "s_bitset0_b32 s44, 16\n\t"
          "s_cmp_eq_u32 s44, s49\n\t"
          "s_cselect_b32 s44, s50, s44\n\t"
          "v_add_u32 v131, s44, v120\n\t"
          "buffer_load_dwordx4 v[164:167], v131, s[40:43], 0 offen offset:0 sc1\n\t"
          "buffer_load_dwordx4 v[168:171], v131, s[40:43], 0 offen offset:256 sc1\n\t"
          "buffer_load_dwordx4 v[172:175], v131, s[40:43], 0 offen offset:2048 sc1\n\t"
          "buffer_load_dwordx4 v[176:179], v131, s[40:43], 0 offen offset:2304 sc1\n\t"
          "s_addk_i32 s44, 0x1000\n\t"
          "s_bitset0_b32 s44, 16\n\t"
          "s_cmp_eq_u32 s44, s49\n\t"
          "s_cselect_b32 s44, s50, s44\n\t"
          "v_add_u32 v131, s44, v120\n\t"
          "buffer_load_dwordx4 v[180:183], v131, s[40:43], 0 offen offset:0 sc1\n\t"
          "buffer_load_dwordx4 v[184:187], v131, s[40:43], 0 offen offset:256 sc1\n\t"
          "buffer_load_dwordx4 v[188:191], v131, s[40:43], 0 offen offset:2048 sc1\n\t"
          "buffer_load_dwordx4 v[192:195], v131, s[40:43], 0 offen offset:2304 sc1\n\t"
            : "={v[148:163]}"(xr1), "={v[164:179]}"(xr2), "={v[180:195]}"(xr3), "+{s44}"(soff), "+{v[128:131]}"(stC) : "{v[112:127]}"(stB), "{s[40:43]}"(srd), KSKIP : "scc", "memory");
          SBAR();
        };
        float mean, var, mb1, rd1, mb2, den2;
        row_stats_fin(rs, OFF_SX + SX_CAND + (tn * 16 + tm) * 128 + wid * 4, smem, 1, tile, lane, S, Q, Dd, U2F(words[0]), x1p, mean, var, mb1, rd1);
        ln_scalars(mean + (mean - mb1) * rd1, var * (1.0f + rd1) * (1.0f + rd1), mb2, den2);
        const float rd2 = RCP(den2);
        const float al = (1.0f + rd1) * rd2;
        const float be = -(mb1 * rd1 + mb2) * RCP(1.0f + rd1);
        reinterpret_cast<float*>(smem + L_AB)[wid * 64 + lane] = hi ? be : al;
        reinterpret_cast<float*>(smem + L_AB)[256 + wid * 64 + lane] = hi ? 1.0f + rd1 : -mb1 * rd1;
        const float* ab = reinterpret_cast<const float*>(smem + L_AB) + wid * 64 + 32;
        const float bt[2] = {ab[lane & 15], ab[16 + (lane & 15)]};
#pragma unroll
        for (int jj = 0; jj < 2; ++jj) {
          if (jj == 0) request_x();
          f32x4 cs[2][2], bv[2];
          float cv[2];
#pragma unroll
          for (int i = 0; i < 2; ++i) {
            const float* p = reinterpret_cast<const float*>(smem + L_CS) + (jj ^ hh) * 32 + 16 * i;
            bv[i] = *reinterpret_cast<const f32x4*>(ab + 16 * i + 4 * (lane >> 4));
            cs[0][i] = *reinterpret_cast<const f32x4*>(p + 4 * (lane >> 4)); cs[1][i] = *reinterpret_cast<const f32x4*>(p + 64 + 4 * (lane >> 4));
            cv[i] = p[128 + (lane & 15)];
          }
          SBAR();
#pragma unroll
          for (int j = 0; j < 3; ++j)
#pragma unroll
            for (int e = 0; e < 16; ++e) acc[2 * j + jj][e] += j < 2 ? bt[e >> 3] * cs[j & 1][(e >> 2) & 1][e & 3] : bv[e >> 3][e & 3] * cv[(e >> 2) & 1];
          SBAR();
        }
        if (0 == 2) request_x();
      }
      unsigned ep[4];
      f32x2 al2;
      { PHASE_VARS;
        float* ab = reinterpret_cast<float*>(smem + L_AB) + wid * 64;
        if (L == 0) ab[lane] = 1.0f;
        al2 = f32x2{ab[lane & 15], ab[16 + (lane & 15)]};
        const unsigned dst = OFF_KV + L * (10 * MBY) + tn * 2 * HS + tile * 4096 + lane * 16;
        ep[0] = dst + hh * HS; ep[1] = dst + (hh ^ 1) * HS; ep[2] = lbase + L_AB + wid * 256 + (lane >> 4) * 16; ep[3] = lbase + L_KX + wid * 4096 + lane * 16; }
      STAMPV(3 + 16 * L);
      soff = kpos(tm, tn, 1 + 2) * 4096;
      asm volatile(
        "s_mov_b32 s47, 111168\n\t"
        "v_add_u32 v131, s44, v120\n\t"
        "s_waitcnt vmcnt(12) lgkmcnt(6)\n\t"
        "v_mfma_f32_16x16x32_bf16 v[64:67], v[140:143], v[96:99], v[64:67]\n\t"
        "ds_read_b64_tr_b16 v[116:117], v129 offset:160\n\t"
        "v_mfma_f32_16x16x32_bf16 v[72:75], v[144:147], v[96:99], v[72:75]\n\t"
        "ds_read_b64_tr_b16 v[118:119], v129 offset:3232\n\t"
        "v_mfma_f32_16x16x32_bf16 v[68:71], v[140:143], v[100:103], v[68:71]\n\t"
        "ds_read_b64_tr_b16 v[96:97], v130 offset:128\n\t"
        "ds_read_b32 v126, v125\n\t"
        "v_mfma_f32_16x16x32_bf16 v[76:79], v[144:147], v[100:103], v[76:79]\n\t"
        "ds_read_b64_tr_b16 v[98:99], v130 offset:3200\n\t"
        "s_waitcnt lgkmcnt(7)\n\t"
        "v_mfma_f32_16x16x32_bf16 v[80:83], v[140:143], v[104:107], v[80:83]\n\t"
        "ds_read_b64_tr_b16 v[100:101], v130 offset:160\n\t"
        "v_mfma_f32_16x16x32_bf16 v[88:91], v[144:147], v[104:107], v[88:91]\n\t"
        "ds_read_b64_tr_b16 v[102:103], v130 offset:3232\n\t"
        "v_mfma_f32_16x16x32_bf16 v[84:87], v[140:143], v[108:111], v[84:87]\n\t"
        "ds_read_b64_tr_b16 v[104:105], v129 offset:0\n\t"
        "v_add_u32 v123, s46, v121\n\t"
        "v_mfma_f32_16x16x32_bf16 v[92:95], v[144:147], v[108:111], v[92:95]\n\t"
        "ds_read_b64_tr_b16 v[106:107], v129 offset:3072\n\t"
        "s_waitcnt lgkmcnt(5)\n\t"
        "v_mfma_f32_16x16x32_bf16 v[32:35], v[112:115], v[140:143], v[32:35]\n\t"
        "ds_read_b64_tr_b16 v[108:109], v129 offset:32\n\t"
        "v_cmp_gt_u32 vcc, v128, v126\n\t"
        "v_mfma_f32_16x16x32_bf16 v[40:43], v[112:115], v[144:147], v[40:43]\n\t"
        "ds_read_b64_tr_b16 v[110:111], v129 offset:3104\n\t"
        "v_add_u32 v124, s46, v122\n\t"
        "v_mfma_f32_16x16x32_bf16 v[36:39], v[116:119], v[140:143], v[36:39]\n\t"
        "ds_read_b64_tr_b16 v[112:113], v130 offset:0\n\t"
        "v_mfma_f32_16x16x32_bf16 v[44:47], v[116:119], v[144:147], v[44:47]\n\t"
        "ds_read_b64_tr_b16 v[114:115], v130 offset:3072\n\t"
        "s_waitcnt lgkmcnt(6)\n\t"
        "v_mfma_f32_16x16x32_bf16 v[48:51], v[96:99], v[140:143], v[48:51]\n\t"
        "ds_read_b64_tr_b16 v[116:117], v130 offset:32\n\t"
        "v_mfma_f32_16x16x32_bf16 v[56:59], v[96:99], v[144:147], v[56:59]\n\t"
        "ds_read_b64_tr_b16 v[118:119], v130 offset:3104\n\t"
        "s_cbranch_vccnz 12f\n\t"
        "13:\n\t"
        "v_mfma_f32_16x16x32_bf16 v[52:55], v[100:103], v[140:143], v[52:55]\n\t"
        "ds_read_b64_tr_b16 v[96:97], v123 offset:256\n\t"
        "s_addk_i32 s46, 12352\n\t"
        "v_mfma_f32_16x16x32_bf16 v[60:63], v[100:103], v[144:147], v[60:63]\n\t"
        "ds_read_b64_tr_b16 v[98:99], v123 offset:3328\n\t"
        "s_waitcnt lgkmcnt(6)\n\t"
        "v_mfma_f32_16x16x32_bf16 v[0:3], v[104:107], v[140:143], v[0:3]\n\t"
        "ds_read_b64_tr_b16 v[100:101], v123 offset:288\n\t"
        "s_cmp_eq_u32 s46, s47\n\t"
        "v_mfma_f32_16x16x32_bf16 v[8:11], v[104:107], v[144:147], v[8:11]\n\t"
        "ds_read_b64_tr_b16 v[102:103], v123 offset:3360\n\t"
        "s_cselect_b32 s46, 0, s46\n\t"
        "v_mfma_f32_16x16x32_bf16 v[4:7], v[108:111], v[140:143], v[4:7]\n\t"
        "ds_read_b64_tr_b16 v[104:105], v124 offset:256\n\t"
        "v_mfma_f32_16x16x32_bf16 v[12:15], v[108:111], v[144:147], v[12:15]\n\t"
        "ds_read_b64_tr_b16 v[106:107], v124 offset:3328\n\t"
        "s_waitcnt lgkmcnt(6)\n\t"
        "v_mfma_f32_16x16x32_bf16 v[16:19], v[112:115], v[140:143], v[16:19]\n\t"
        "ds_write_b32 v127, v128\n\t"
        "ds_read_b64_tr_b16 v[108:109], v124 offset:288\n\t"
        "v_mfma_f32_16x16x32_bf16 v[24:27], v[112:115], v[144:147], v[24:27]\n\t"
        "ds_read_b64_tr_b16 v[110:111], v124 offset:3360\n\t"
        "v_add_u32 v128, 2, v128\n\t"
        "v_mfma_f32_16x16x32_bf16 v[20:23], v[116:119], v[140:143], v[20:23]\n\t"
        "ds_read_b64_tr_b16 v[112:113], v123 offset:128\n\t"
        "v_mfma_f32_16x16x32_bf16 v[28:31], v[116:119], v[144:147], v[28:31]\n\t"
        "ds_read_b64_tr_b16 v[114:115], v123 offset:3200\n\t"
        "s_mov_b32 s45, 2\n\t"
        "1:\n\t"
        "s_waitcnt vmcnt(10) lgkmcnt(7)\n\t"
        "v_mfma_f32_16x16x32_bf16 v[64:67], v[148:151], v[96:99], v[64:67]\n\t"
        "ds_read_b64_tr_b16 v[116:117], v123 offset:160\n\t"
        "s_addk_i32 s44, 0x1000\n\t"
        "v_mfma_f32_16x16x32_bf16 v[72:75], v[152:155], v[96:99], v[72:75]\n\t"
        "ds_read_b64_tr_b16 v[118:119], v123 offset:3232\n\t"
        "s_bitset0_b32 s44, 16\n\t"
        "v_mfma_f32_16x16x32_bf16 v[68:71], v[148:151], v[100:103], v[68:71]\n\t"
        "ds_read_b64_tr_b16 v[96:97], v124 offset:128\n\t"
        "s_cmp_eq_u32 s44, s49\n\t"
        "v_mfma_f32_16x16x32_bf16 v[76:79], v[152:155], v[100:103], v[76:79]\n\t"
        "ds_read_b64_tr_b16 v[98:99], v124 offset:3200\n\t"
        "s_waitcnt lgkmcnt(6)\n\t"
        "v_mfma_f32_16x16x32_bf16 v[80:83], v[148:151], v[104:107], v[80:83]\n\t"
        "ds_read_b64_tr_b16 v[100:101], v124 offset:160\n\t"
        "s_cselect_b32 s44, s50, s44\n\t"
        "v_mfma_f32_16x16x32_bf16 v[88:91], v[152:155], v[104:107], v[88:91]\n\t"
        "ds_read_b64_tr_b16 v[102:103], v124 offset:3232\n\t"
        "v_add_u32 v131, s44, v120\n\t"
        "v_mfma_f32_16x16x32_bf16 v[84:87], v[148:151], v[108:111], v[84:87]\n\t"
        "ds_read_b64_tr_b16 v[104:105], v123 offset:0\n\t"
        "v_add_u32 v129, s46, v121\n\t"
        "v_mfma_f32_16x16x32_bf16 v[92:95], v[152:155], v[108:111], v[92:95]\n\t"
        "ds_read_b64_tr_b16 v[106:107], v123 offset:3072\n\t"
        "s_waitcnt lgkmcnt(6)\n\t"
        "v_mfma_f32_16x16x32_bf16 v[32:35], v[112:115], v[148:151], v[32:35]\n\t"
        "ds_read_b64_tr_b16 v[108:109], v123 offset:32\n\t"
        "v_mfma_f32_16x16x32_bf16 v[40:43], v[112:115], v[152:155], v[40:43]\n\t"
        "ds_read_b64_tr_b16 v[110:111], v123 offset:3104\n\t"
        "v_add_u32 v130, s46, v122\n\t"
        "v_mfma_f32_16x16x32_bf16 v[36:39], v[116:119], v[148:151], v[36:39]\n\t"
        "ds_read_b64_tr_b16 v[112:113], v124 offset:0\n\t"
        "buffer_load_dwordx4 v[132:135], v131, s[40:43], 0 offen offset:0 sc1\n\t"
        "v_mfma_f32_16x16x32_bf16 v[44:47], v[116:119], v[152:155], v[44:47]\n\t"
        "ds_read_b64_tr_b16 v[114:115], v124 offset:3072\n\t"
        "s_waitcnt lgkmcnt(6)\n\t"
        "v_mfma_f32_16x16x32_bf16 v[48:51], v[96:99], v[148:151], v[48:51]\n\t"
        "ds_read_b64_tr_b16 v[116:117], v124 offset:32\n\t"
        "v_mfma_f32_16x16x32_bf16 v[56:59], v[96:99], v[152:155], v[56:59]\n\t"
        "ds_read_b64_tr_b16 v[118:119], v124 offset:3104\n\t"
        "v_mfma_f32_16x16x32_bf16 v[52:55], v[100:103], v[148:151], v[52:55]\n\t"
        "ds_read_b64_tr_b16 v[96:97], v129 offset:256\n\t"
        "s_addk_i32 s46, 12352\n\t"
        "v_mfma_f32_16x16x32_bf16 v[60:63], v[100:103], v[152:155], v[60:63]\n\t"
        "ds_read_b64_tr_b16 v[98:99], v129 offset:3328\n\t"
        "s_waitcnt lgkmcnt(6)\n\t"
        "v_mfma_f32_16x16x32_bf16 v[0:3], v[104:107], v[148:151], v[0:3]\n\t"
        "ds_read_b64_tr_b16 v[100:101], v129 offset:288\n\t"
        "s_cmp_eq_u32 s46, s47\n\t"
        "v_mfma_f32_16x16x32_bf16 v[8:11], v[104:107], v[152:155], v[8:11]\n\t"
        "ds_read_b64_tr_b16 v[102:103], v129 offset:3360\n\t"
        "s_cselect_b32 s46, 0, s46\n\t"
        "v_mfma_f32_16x16x32_bf16 v[4:7], v[108:111], v[148:151], v[4:7]\n\t"
        "ds_read_b64_tr_b16 v[104:105], v130 offset:256\n\t"
        "buffer_load_dwordx4 v[136:139], v131, s[40:43], 0 offen offset:256 sc1\n\t"
        "v_mfma_f32_16x16x32_bf16 v[12:15], v[108:111], v[152:155], v[12:15]\n\t"
        "ds_read_b64_tr_b16 v[106:107], v130 offset:3328\n\t"
        "s_waitcnt lgkmcnt(6)\n\t"
        "v_mfma_f32_16x16x32_bf16 v[16:19], v[112:115], v[148:151], v[16:19]\n\t"
        "ds_read_b64_tr_b16 v[108:109], v130 offset:288\n\t"
        "v_mfma_f32_16x16x32_bf16 v[24:27], v[112:115], v[152:155], v[24:27]\n\t"
        "ds_read_b64_tr_b16 v[110:111], v130 offset:3360\n\t"
        "v_mfma_f32_16x16x32_bf16 v[20:23], v[116:119], v[148:151], v[20:23]\n\t"
        "ds_read_b64_tr_b16 v[112:113], v129 offset:128\n\t"
        "v_mfma_f32_16x16x32_bf16 v[28:31], v[116:119], v[152:155], v[28:31]\n\t"
        "ds_read_b64_tr_b16 v[114:115], v129 offset:3200\n\t"
        "s_waitcnt vmcnt(10) lgkmcnt(6)\n\t"
        "v_mfma_f32_16x16x32_bf16 v[64:67], v[156:159], v[96:99], v[64:67]\n\t"
        "ds_read_b64_tr_b16 v[116:117], v129 offset:160\n\t"
        "v_mfma_f32_16x16x32_bf16 v[72:75], v[160:163], v[96:99], v[72:75]\n\t"
        "ds_read_b64_tr_b16 v[118:119], v129 offset:3232\n\t"
        "v_mfma_f32_16x16x32_bf16 v[68:71], v[156:159], v[100:103], v[68:71]\n\t"
        "ds_read_b64_tr_b16 v[96:97], v130 offset:128\n\t"
        "ds_read_b32 v126, v125\n\t"
        "v_mfma_f32_16x16x32_bf16 v[76:79], v[160:163], v[100:103], v[76:79]\n\t"
        "ds_read_b64_tr_b16 v[98:99], v130 offset:3200\n\t"
        "s_waitcnt lgkmcnt(7)\n\t"
        "v_mfma_f32_16x16x32_bf16 v[80:83], v[156:159], v[104:107], v[80:83]\n\t"
        "ds_read_b64_tr_b16 v[100:101], v130 offset:160\n\t"
        "v_mfma_f32_16x16x32_bf16 v[88:91], v[160:163], v[104:107], v[88:91]\n\t"
        "ds_read_b64_tr_b16 v[102:103], v130 offset:3232\n\t"
        "v_mfma_f32_16x16x32_bf16 v[84:87], v[156:159], v[108:111], v[84:87]\n\t"
        "ds_read_b64_tr_b16 v[104:105], v129 offset:0\n\t"
        "v_add_u32 v123, s46, v121\n\t"
        "v_mfma_f32_16x16x32_bf16 v[92:95], v[160:163], v[108:111], v[92:95]\n\t"
        "ds_read_b64_tr_b16 v[106:107], v129 offset:3072\n\t"
        "s_waitcnt lgkmcnt(5)\n\t"
        "v_mfma_f32_16x16x32_bf16 v[32:35], v[112:115], v[156:159], v[32:35]\n\t"
        "ds_read_b64_tr_b16 v[108:109], v129 offset:32\n\t"
        "v_cmp_gt_u32 vcc, v128, v126\n\t"
        "v_mfma_f32_16x16x32_bf16 v[40:43], v[112:115], v[160:163], v[40:43]\n\t"
        "ds_read_b64_tr_b16 v[110:111], v129 offset:3104\n\t"
        "v_add_u32 v124, s46, v122\n\t"
        "v_mfma_f32_16x16x32_bf16 v[36:39], v[116:119], v[156:159], v[36:39]\n\t"
        "ds_read_b64_tr_b16 v[112:113], v130 offset:0\n\t"
        "buffer_load_dwordx4 v[140:143], v131, s[40:43], 0 offen offset:2048 sc1\n\t"
        "v_mfma_f32_16x16x32_bf16 v[44:47], v[116:119], v[160:163], v[44:47]\n\t"
        "ds_read_b64_tr_b16 v[114:115], v130 offset:3072\n\t"
        "s_waitcnt lgkmcnt(6)\n\t"
        "v_mfma_f32_16x16x32_bf16 v[48:51], v[96:99], v[156:159], v[48:51]\n\t"
        "ds_read_b64_tr_b16 v[116:117], v130 offset:32\n\t"
        "v_mfma_f32_16x16x32_bf16 v[56:59], v[96:99], v[160:163], v[56:59]\n\t"
        "ds_read_b64_tr_b16 v[118:119], v130 offset:3104\n\t"
        "s_cbranch_vccnz 14f\n\t"
        "15:\n\t"
        "v_mfma_f32_16x16x32_bf16 v[52:55], v[100:103], v[156:159], v[52:55]\n\t"
        "ds_read_b64_tr_b16 v[96:97], v123 offset:256\n\t"
        "s_addk_i32 s46, 12352\n\t"
        "v_mfma_f32_16x16x32_bf16 v[60:63], v[100:103], v[160:163], v[60:63]\n\t"
        "ds_read_b64_tr_b16 v[98:99], v123 offset:3328\n\t"
        "s_waitcnt lgkmcnt(6)\n\t"
        "v_mfma_f32_16x16x32_bf16 v[0:3], v[104:107], v[156:159], v[0:3]\n\t"
        "ds_read_b64_tr_b16 v[100:101], v123 offset:288\n\t"
        "s_cmp_eq_u32 s46, s47\n\t"
        "v_mfma_f32_16x16x32_bf16 v[8:11], v[104:107], v[160:163], v[8:11]\n\t"
        "ds_read_b64_tr_b16 v[102:103], v123 offset:3360\n\t"
        "s_cselect_b32 s46, 0, s46\n\t"
        "v_mfma_f32_16x16x32_bf16 v[4:7], v[108:111], v[156:159], v[4:7]\n\t"
        "ds_read_b64_tr_b16 v[104:105], v124 offset:256\n\t"
        "buffer_load_dwordx4 v[144:147], v131, s[40:43], 0 offen offset:2304 sc1\n\t"
        "v_mfma_f32_16x16x32_bf16 v[12:15], v[108:111], v[160:163], v[12:15]\n\t"
        "ds_read_b64_tr_b16 v[106:107], v124 offset:3328\n\t"
        "s_waitcnt lgkmcnt(6)\n\t"
        "v_mfma_f32_16x16x32_bf16 v[16:19], v[112:115], v[156:159], v[16:19]\n\t"
        "ds_write_b32 v127, v128\n\t"
        "ds_read_b64_tr_b16 v[108:109], v124 offset:288\n\t"
        "v_mfma_f32_16x16x32_bf16 v[24:27], v[112:115], v[160:163], v[24:27]\n\t"
        "ds_read_b64_tr_b16 v[110:111], v124 offset:3360\n\t"
        "v_add_u32 v128, 2, v128\n\t"
        "v_mfma_f32_16x16x32_bf16 v[20:23], v[116:119], v[156:159], v[20:23]\n\t"
        "ds_read_b64_tr_b16 v[112:113], v123 offset:128\n\t"
        "v_mfma_f32_16x16x32_bf16 v[28:31], v[116:119], v[160:163], v[28:31]\n\t"
        "ds_read_b64_tr_b16 v[114:115], v123 offset:3200\n\t"
        "s_waitcnt vmcnt(10) lgkmcnt(7)\n\t"
        "v_mfma_f32_16x16x32_bf16 v[64:67], v[164:167], v[96:99], v[64:67]\n\t"
        "ds_read_b64_tr_b16 v[116:117], v123 offset:160\n\t"
        "s_addk_i32 s44, 0x1000\n\t"
        "v_mfma_f32_16x16x32_bf16 v[72:75], v[168:171], v[96:99], v[72:75]\n\t"
        "ds_read_b64_tr_b16 v[118:119], v123 offset:3232\n\t"
        "s_bitset0_b32 s44, 16\n\t"
        "v_mfma_f32_16x16x32_bf16 v[68:71], v[164:167], v[100:103], v[68:71]\n\t"
        "ds_read_b64_tr_b16 v[96:97], v124 offset:128\n\t"
        "s_cmp_eq_u32 s44, s49\n\t"
        "v_mfma_f32_16x16x32_bf16 v[76:79], v[168:171], v[100:103], v[76:79]\n\t"
        "ds_read_b64_tr_b16 v[98:99], v124 offset:3200\n\t"
        "s_waitcnt lgkmcnt(6)\n\t"
        "v_mfma_f32_16x16x32_bf16 v[80:83], v[164:167], v[104:107], v[80:83]\n\t"
        "ds_read_b64_tr_b16 v[100:101], v124 offset:160\n\t"
        "s_cselect_b32 s44, s50, s44\n\t"
        "v_mfma_f32_16x16x32_bf16 v[88:91], v[168:171], v[104:107], v[88:91]\n\t"
        "ds_read_b64_tr_b16 v[102:103], v124 offset:3232\n\t"
        "v_add_u32 v131, s44, v120\n\t"
        "v_mfma_f32_16x16x32_bf16 v[84:87], v[164:167], v[108:111], v[84:87]\n\t"
        "ds_read_b64_tr_b16 v[104:105], v123 offset:0\n\t"
        "v_add_u32 v129, s46, v121\n\t"
        "v_mfma_f32_16x16x32_bf16 v[92:95], v[168:171], v[108:111], v[92:95]\n\t"
        "ds_read_b64_tr_b16 v[106:107], v123 offset:3072\n\t"
        "s_waitcnt lgkmcnt(6)\n\t"
        "v_mfma_f32_16x16x32_bf16 v[32:35], v[112:115], v[164:167], v[32:35]\n\t"
        "ds_read_b64_tr_b16 v[108:109], v123 offset:32\n\t"
        "v_mfma_f32_16x16x32_bf16 v[40:43], v[112:115], v[168:171], v[40:43]\n\t"
        "ds_read_b64_tr_b16 v[110:111], v123 offset:3104\n\t"
        "v_add_u32 v130, s46, v122\n\t"
        "v_mfma_f32_16x16x32_bf16 v[36:39], v[116:119], v[164:167], v[36:39]\n\t"
        "ds_read_b64_tr_b16 v[112:113], v124 offset:0\n\t"
        "buffer_load_dwordx4 v[148:151], v131, s[40:43], 0 offen offset:0 sc1\n\t"
        "v_mfma_f32_16x16x32_bf16 v[44:47], v[116:119], v[168:171], v[44:47]\n\t"
        "ds_read_b64_tr_b16 v[114:115], v124 offset:3072\n\t"
        "s_waitcnt lgkmcnt(6)\n\t"
        "v_mfma_f32_16x16x32_bf16 v[48:51], v[96:99], v[164:167], v[48:51]\n\t"
        "ds_read_b64_tr_b16 v[116:117], v124 offset:32\n\t"
        "v_mfma_f32_16x16x32_bf16 v[56:59], v[96:99], v[168:171], v[56:59]\n\t"
        "ds_read_b64_tr_b16 v[118:119], v124 offset:3104\n\t"
        "v_mfma_f32_16x16x32_bf16 v[52:55], v[100:103], v[164:167], v[52:55]\n\t"
        "ds_read_b64_tr_b16 v[96:97], v129 offset:256\n\t"
        "s_addk_i32 s46, 12352\n\t"
        "v_mfma_f32_16x16x32_bf16 v[60:63], v[100:103], v[168:171], v[60:63]\n\t"
        "ds_read_b64_tr_b16 v[98:99], v129 offset:3328\n\t"
        "s_waitcnt lgkmcnt(6)\n\t"
        "v_mfma_f32_16x16x32_bf16 v[0:3], v[104:107], v[164:167], v[0:3]\n\t"
        "ds_read_b64_tr_b16 v[100:101], v129 offset:288\n\t"
        "s_cmp_eq_u32 s46, s47\n\t"
        "v_mfma_f32_16x16x32_bf16 v[8:11], v[104:107], v[168:171], v[8:11]\n\t"
        "ds_read_b64_tr_b16 v[102:103], v129 offset:3360\n\t"
        "s_cselect_b32 s46, 0, s46\n\t"
        "v_mfma_f32_16x16x32_bf16 v[4:7], v[108:111], v[164:167], v[4:7]\n\t"
        "ds_read_b64_tr_b16 v[104:105], v130 offset:256\n\t"
        "buffer_load_dwordx4 v[152:155], v131, s[40:43], 0 offen offset:256 sc1\n\t"
        "v_mfma_f32_16x16x32_bf16 v[12:15], v[108:111], v[168:171], v[12:15]\n\t"
        "ds_read_b64_tr_b16 v[106:107], v130 offset:3328\n\t"
        "s_waitcnt lgkmcnt(6)\n\t"
        "v_mfma_f32_16x16x32_bf16 v[16:19], v[112:115], v[164:167], v[16:19]\n\t"
        "ds_read_b64_tr_b16 v[108:109], v130 offset:288\n\t"
        "v_mfma_f32_16x16x32_bf16 v[24:27], v[112:115], v[168:171], v[24:27]\n\t"
        "ds_read_b64_tr_b16 v[110:111], v130 offset:3360\n\t"
        "v_mfma_f32_16x16x32_bf16 v[20:23], v[116:119], v[164:167], v[20:23]\n\t"
        "ds_read_b64_tr_b16 v[112:113], v129 offset:128\n\t"
        "v_mfma_f32_16x16x32_bf16 v[28:31], v[116:119], v[168:171], v[28:31]\n\t"
        "ds_read_b64_tr_b16 v[114:115], v129 offset:3200\n\t"
        "s_waitcnt vmcnt(10) lgkmcnt(6)\n\t"
        "v_mfma_f32_16x16x32_bf16 v[64:67], v[172:175], v[96:99], v[64:67]\n\t"
        "ds_read_b64_tr_b16 v[116:117], v129 offset:160\n\t"
        "v_mfma_f32_16x16x32_bf16 v[72:75], v[176:179], v[96:99], v[72:75]\n\t"
        "ds_read_b64_tr_b16 v[118:119], v129 offset:3232\n\t"
        "v_mfma_f32_16x16x32_bf16 v[68:71], v[172:175], v[100:103], v[68:71]\n\t"
        "ds_read_b64_tr_b16 v[96:97], v130 offset:128\n\t"
        "ds_read_b32 v126, v125\n\t"
        "v_mfma_f32_16x16x32_bf16 v[76:79], v[176:179], v[100:103], v[76:79]\n\t"
        "ds_read_b64_tr_b16 v[98:99], v130 offset:3200\n\t"
        "s_waitcnt lgkmcnt(7)\n\t"
        "v_mfma_f32_16x16x32_bf16 v[80:83], v[172:175], v[104:107], v[80:83]\n\t"
        "ds_read_b64_tr_b16 v[100:101], v130 offset:160\n\t"
        "v_mfma_f32_16x16x32_bf16 v[88:91], v[176:179], v[104:107], v[88:91]\n\t"
        "ds_read_b64_tr_b16 v[102:103], v130 offset:3232\n\t"
        "v_mfma_f32_16x16x32_bf16 v[84:87], v[172:175], v[108:111], v[84:87]\n\t"
        "ds_read_b64_tr_b16 v[104:105], v129 offset:0\n\t"
        "v_add_u32 v123, s46, v121\n\t"
        "v_mfma_f32_16x16x32_bf16 v[92:95], v[176:179], v[108:111], v[92:95]\n\t"
        "ds_read_b64_tr_b16 v[106:107], v129 offset:3072\n\t"
        "s_waitcnt lgkmcnt(5)\n\t"
        "v_mfma_f32_16x16x32_bf16 v[32:35], v[112:115], v[172:175], v[32:35]\n\t"
        "ds_read_b64_tr_b16 v[108:109], v129 offset:32\n\t"
        "v_cmp_gt_u32 vcc, v128, v126\n\t"
        "v_mfma_f32_16x16x32_bf16 v[40:43], v[112:115], v[176:179], v[40:43]\n\t"
        "ds_read_b64_tr_b16 v[110:111], v129 offset:3104\n\t"
        "v_add_u32 v124, s46, v122\n\t"
        "v_mfma_f32_16x16x32_bf16 v[36:39], v[116:119], v[172:175], v[36:39]\n\t"
        "ds_read_b64_tr_b16 v[112:113], v130 offset:0\n\t"
        "buffer_load_dwordx4 v[156:159], v131, s[40:43], 0 offen offset:2048 sc1\n\t"
        "v_mfma_f32_16x16x32_bf16 v[44:47], v[116:119], v[176:179], v[44:47]\n\t"
        "ds_read_b64_tr_b16 v[114:115], v130 offset:3072\n\t"
        "s_waitcnt lgkmcnt(6)\n\t"
        "v_mfma_f32_16x16x32_bf16 v[48:51], v[96:99], v[172:175], v[48:51]\n\t"
        "ds_read_b64_tr_b16 v[116:117], v130 offset:32\n\t"
        "v_mfma_f32_16x16x32_bf16 v[56:59], v[96:99], v[176:179], v[56:59]\n\t"
        "ds_read_b64_tr_b16 v[118:119], v130 offset:3104\n\t"
        "s_cbranch_vccnz 16f\n\t"
        "17:\n\t"
        "v_mfma_f32_16x16x32_bf16 v[52:55], v[100:103], v[172:175], v[52:55]\n\t"
        "ds_read_b64_tr_b16 v[96:97], v123 offset:256\n\t"
        "s_addk_i32 s46, 12352\n\t"
        "v_mfma_f32_16x16x32_bf16 v[60:63], v[100:103], v[176:179], v[60:63]\n\t"
        "ds_read_b64_tr_b16 v[98:99], v123 offset:3328\n\t"
        "s_waitcnt lgkmcnt(6)\n\t"
        "v_mfma_f32_16x16x32_bf16 v[0:3], v[104:107], v[172:175], v[0:3]\n\t"
        "ds_read_b64_tr_b16 v[100:101], v123 offset:288\n\t"
        "s_cmp_eq_u32 s46, s47\n\t"
        "v_mfma_f32_16x16x32_bf16 v[8:11], v[104:107], v[176:179], v[8:11]\n\t"
        "ds_read_b64_tr_b16 v[102:103], v123 offset:3360\n\t"
        "s_cselect_b32 s46, 0, s46\n\t"
        "v_mfma_f32_16x16x32_bf16 v[4:7], v[108:111], v[172:175], v[4:7]\n\t"
        "ds_read_b64_tr_b16 v[104:105], v124 offset:256\n\t"
        "buffer_load_dwordx4 v[160:163], v131, s[40:43], 0 offen offset:2304 sc1\n\t"
        "v_mfma_f32_16x16x32_bf16 v[12:15], v[108:111], v[176:179], v[12:15]\n\t"
        "ds_read_b64_tr_b16 v[106:107], v124 offset:3328\n\t"
        "s_waitcnt lgkmcnt(6)\n\t"
        "v_mfma_f32_16x16x32_bf16 v[16:19], v[112:115], v[172:175], v[16:19]\n\t"
        "ds_write_b32 v127, v128\n\t"
        "ds_read_b64_tr_b16 v[108:109], v124 offset:288\n\t"
        "v_mfma_f32_16x16x32_bf16 v[24:27], v[112:115], v[176:179], v[24:27]\n\t"
        "ds_read_b64_tr_b16 v[110:111], v124 offset:3360\n\t"
        "v_add_u32 v128, 2, v128\n\t"
        "v_mfma_f32_16x16x32_bf16 v[20:23], v[116:119], v[172:175], v[20:23]\n\t"
        "ds_read_b64_tr_b16 v[112:113], v123 offset:128\n\t"
        "v_mfma_f32_16x16x32_bf16 v[28:31], v[116:119], v[176:179], v[28:31]\n\t"
        "ds_read_b64_tr_b16 v[114:115], v123 offset:3200\n\t"
        "s_waitcnt vmcnt(10) lgkmcnt(7)\n\t"
        "v_mfma_f32_16x16x32_bf16 v[64:67], v[180:183], v[96:99], v[64:67]\n\t"
        "ds_read_b64_tr_b16 v[116:117], v123 offset:160\n\t"
        "s_addk_i32 s44, 0x1000\n\t"
        "v_mfma_f32_16x16x32_bf16 v[72:75], v[184:187], v[96:99], v[72:75]\n\t"
        "ds_read_b64_tr_b16 v[118:119], v123 offset:3232\n\t"
        "s_bitset0_b32 s44, 16\n\t"
        "v_mfma_f32_16x16x32_bf16 v[68:71], v[180:183], v[100:103], v[68:71]\n\t"
        "ds_read_b64_tr_b16 v[96:97], v124 offset:128\n\t"
        "s_cmp_eq_u32 s44, s49\n\t"
        "v_mfma_f32_16x16x32_bf16 v[76:79], v[184:187], v[100:103], v[76:79]\n\t"
        "ds_read_b64_tr_b16 v[98:99], v124 offset:3200\n\t"
        "s_waitcnt lgkmcnt(6)\n\t"
        "v_mfma_f32_16x16x32_bf16 v[80:83], v[180:183], v[104:107], v[80:83]\n\t"
        "ds_read_b64_tr_b16 v[100:101], v124 offset:160\n\t"
        "s_cselect_b32 s44, s50, s44\n\t"
        "v_mfma_f32_16x16x32_bf16 v[88:91], v[184:187], v[104:107], v[88:91]\n\t"
        "ds_read_b64_tr_b16 v[102:103], v124 offset:3232\n\t"
        "v_add_u32 v131, s44, v120\n\t"
        "v_mfma_f32_16x16x32_bf16 v[84:87], v[180:183], v[108:111], v[84:87]\n\t"
        "ds_read_b64_tr_b16 v[104:105], v123 offset:0\n\t"
        "v_add_u32 v129, s46, v121\n\t"
        "v_mfma_f32_16x16x32_bf16 v[92:95], v[184:187], v[108:111], v[92:95]\n\t"
        "ds_read_b64_tr_b16 v[106:107], v123 offset:3072\n\t"
        "s_waitcnt lgkmcnt(6)\n\t"
        "v_mfma_f32_16x16x32_bf16 v[32:35], v[112:115], v[180:183], v[32:35]\n\t"
        "ds_read_b64_tr_b16 v[108:109], v123 offset:32\n\t"
        "v_mfma_f32_16x16x32_bf16 v[40:43], v[112:115], v[184:187], v[40:43]\n\t"
        "ds_read_b64_tr_b16 v[110:111], v123 offset:3104\n\t"
        "v_add_u32 v130, s46, v122\n\t"
        "v_mfma_f32_16x16x32_bf16 v[36:39], v[116:119], v[180:183], v[36:39]\n\t"
        "ds_read_b64_tr_b16 v[112:113], v124 offset:0\n\t"
        "buffer_load_dwordx4 v[164:167], v131, s[40:43], 0 offen offset:0 sc1\n\t"
        "v_mfma_f32_16x16x32_bf16 v[44:47], v[116:119], v[184:187], v[44:47]\n\t"
        "ds_read_b64_tr_b16 v[114:115], v124 offset:3072\n\t"
        "s_waitcnt lgkmcnt(6)\n\t"
        "v_mfma_f32_16x16x32_bf16 v[48:51], v[96:99], v[180:183], v[48:51]\n\t"
        "ds_read_b64_tr_b16 v[116:117], v124 offset:32\n\t"
        "v_mfma_f32_16x16x32_bf16 v[56:59], v[96:99], v[184:187], v[56:59]\n\t"
        "ds_read_b64_tr_b16 v[118:119], v124 offset:3104\n\t"
        "v_mfma_f32_16x16x32_bf16 v[52:55], v[100:103], v[180:183], v[52:55]\n\t"
        "ds_read_b64_tr_b16 v[96:97], v129 offset:256\n\t"
        "s_addk_i32 s46, 12352\n\t"
        "v_mfma_f32_16x16x32_bf16 v[60:63], v[100:103], v[184:187], v[60:63]\n\t"
        "ds_read_b64_tr_b16 v[98:99], v129 offset:3328\n\t"
        "s_waitcnt lgkmcnt(6)\n\t"
        "v_mfma_f32_16x16x32_bf16 v[0:3], v[104:107], v[180:183], v[0:3]\n\t"
        "ds_read_b64_tr_b16 v[100:101], v129 offset:288\n\t"
        "s_cmp_eq_u32 s46, s47\n\t"
        "v_mfma_f32_16x16x32_bf16 v[8:11], v[104:107], v[184:187], v[8:11]\n\t"
        "ds_read_b64_tr_b16 v[102:103], v129 offset:3360\n\t"
        "s_cselect_b32 s46, 0, s46\n\t"
        "v_mfma_f32_16x16x32_bf16 v[4:7], v[108:111], v[180:183], v[4:7]\n\t"
        "ds_read_b64_tr_b16 v[104:105], v130 offset:256\n\t"
        "buffer_load_dwordx4 v[168:171], v131, s[40:43], 0 offen offset:256 sc1\n\t"
        "v_mfma_f32_16x16x32_bf16 v[12:15], v[108:111], v[184:187], v[12:15]\n\t"
        "ds_read_b64_tr_b16 v[106:107], v130 offset:3328\n\t"
        "s_waitcnt lgkmcnt(6)\n\t"
        "v_mfma_f32_16x16x32_bf16 v[16:19], v[112:115], v[180:183], v[16:19]\n\t"
        "ds_read_b64_tr_b16 v[108:109], v130 offset:288\n\t"
        "v_mfma_f32_16x16x32_bf16 v[24:27], v[112:115], v[184:187], v[24:27]\n\t"
        "ds_read_b64_tr_b16 v[110:111], v130 offset:3360\n\t"
        "v_mfma_f32_16x16x32_bf16 v[20:23], v[116:119], v[180:183], v[20:23]\n\t"
        "ds_read_b64_tr_b16 v[112:113], v129 offset:128\n\t"
        "v_mfma_f32_16x16x32_bf16 v[28:31], v[116:119], v[184:187], v[28:31]\n\t"
        "ds_read_b64_tr_b16 v[114:115], v129 offset:3200\n\t"
        "s_waitcnt vmcnt(10) lgkmcnt(6)\n\t"
        "v_mfma_f32_16x16x32_bf16 v[64:67], v[188:191], v[96:99], v[64:67]\n\t"
        "ds_read_b64_tr_b16 v[116:117], v129 offset:160\n\t"
        "v_mfma_f32_16x16x32_bf16 v[72:75], v[192:195], v[96:99], v[72:75]\n\t"
        "ds_read_b64_tr_b16 v[118:119], v129 offset:3232\n\t"
        "v_mfma_f32_16x16x32_bf16 v[68:71], v[188:191], v[100:103], v[68:71]\n\t"
        "ds_read_b64_tr_b16 v[96:97], v130 offset:128\n\t"
        "ds_read_b32 v126, v125\n\t"
        "v_mfma_f32_16x16x32_bf16 v[76:79], v[192:195], v[100:103], v[76:79]\n\t"
        "ds_read_b64_tr_b16 v[98:99], v130 offset:3200\n\t"
        "s_waitcnt lgkmcnt(7)\n\t"
        "v_mfma_f32_16x16x32_bf16 v[80:83], v[188:191], v[104:107], v[80:83]\n\t"
        "ds_read_b64_tr_b16 v[100:101], v130 offset:160\n\t"
        "v_mfma_f32_16x16x32_bf16 v[88:91], v[192:195], v[104:107], v[88:91]\n\t"
        "ds_read_b64_tr_b16 v[102:103], v130 offset:3232\n\t"
        "v_mfma_f32_16x16x32_bf16 v[84:87], v[188:191], v[108:111], v[84:87]\n\t"
        "ds_read_b64_tr_b16 v[104:105], v129 offset:0\n\t"
        "v_add_u32 v123, s46, v121\n\t"
        "v_mfma_f32_16x16x32_bf16 v[92:95], v[192:195], v[108:111], v[92:95]\n\t"
        "ds_read_b64_tr_b16 v[106:107], v129 offset:3072\n\t"
        "s_waitcnt lgkmcnt(5)\n\t"
        "v_mfma_f32_16x16x32_bf16 v[32:35], v[112:115], v[188:191], v[32:35]\n\t"
        "ds_read_b64_tr_b16 v[108:109], v129 offset:32\n\t"
        "v_cmp_gt_u32 vcc, v128, v126\n\t"
        "v_mfma_f32_16x16x32_bf16 v[40:43], v[112:115], v[192:195], v[40:43]\n\t"
        "ds_read_b64_tr_b16 v[110:111], v129 offset:3104\n\t"
        "v_add_u32 v124, s46, v122\n\t"
        "v_mfma_f32_16x16x32_bf16 v[36:39], v[116:119], v[188:191], v[36:39]\n\t"
        "ds_read_b64_tr_b16 v[112:113], v130 offset:0\n\t"
        "buffer_load_dwordx4 v[172:175], v131, s[40:43], 0 offen offset:2048 sc1\n\t"
        "v_mfma_f32_16x16x32_bf16 v[44:47], v[116:119], v[192:195], v[44:47]\n\t"
        "ds_read_b64_tr_b16 v[114:115], v130 offset:3072\n\t"
        "s_waitcnt lgkmcnt(6)\n\t"
        "v_mfma_f32_16x16x32_bf16 v[48:51], v[96:99], v[188:191], v[48:51]\n\t"
        "ds_read_b64_tr_b16 v[116:117], v130 offset:32\n\t"
        "v_mfma_f32_16x16x32_bf16 v[56:59], v[96:99], v[192:195], v[56:59]\n\t"
        "ds_read_b64_tr_b16 v[118:119], v130 offset:3104\n\t"
        "s_cbranch_vccnz 18f\n\t"
        "19:\n\t"
        "v_mfma_f32_16x16x32_bf16 v[52:55], v[100:103], v[188:191], v[52:55]\n\t"
        "ds_read_b64_tr_b16 v[96:97], v123 offset:256\n\t"
        "s_addk_i32 s46, 12352\n\t"
        "v_mfma_f32_16x16x32_bf16 v[60:63], v[100:103], v[192:195], v[60:63]\n\t"
        "ds_read_b64_tr_b16 v[98:99], v123 offset:3328\n\t"
        "s_waitcnt lgkmcnt(6)\n\t"
        "v_mfma_f32_16x16x32_bf16 v[0:3], v[104:107], v[188:191], v[0:3]\n\t"
        "ds_read_b64_tr_b16 v[100:101], v123 offset:288\n\t"
        "s_cmp_eq_u32 s46, s47\n\t"
        "v_mfma_f32_16x16x32_bf16 v[8:11], v[104:107], v[192:195], v[8:11]\n\t"
        "ds_read_b64_tr_b16 v[102:103], v123 offset:3360\n\t"
        "s_cselect_b32 s46, 0, s46\n\t"
        "v_mfma_f32_16x16x32_bf16 v[4:7], v[108:111], v[188:191], v[4:7]\n\t"
        "ds_read_b64_tr_b16 v[104:105], v124 offset:256\n\t"
        "buffer_load_dwordx4 v[176:179], v131, s[40:43], 0 offen offset:2304 sc1\n\t"
        "v_mfma_f32_16x16x32_bf16 v[12:15], v[108:111], v[192:195], v[12:15]\n\t"
        "ds_read_b64_tr_b16 v[106:107], v124 offset:3328\n\t"
        "s_waitcnt lgkmcnt(6)\n\t"
        "v_mfma_f32_16x16x32_bf16 v[16:19], v[112:115], v[188:191], v[16:19]\n\t"
        "ds_write_b32 v127, v128\n\t"
        "ds_read_b64_tr_b16 v[108:109], v124 offset:288\n\t"
        "v_mfma_f32_16x16x32_bf16 v[24:27], v[112:115], v[192:195], v[24:27]\n\t"
        "ds_read_b64_tr_b16 v[110:111], v124 offset:3360\n\t"
        "v_add_u32 v128, 2, v128\n\t"
        "v_mfma_f32_16x16x32_bf16 v[20:23], v[116:119], v[188:191], v[20:23]\n\t"
        "ds_read_b64_tr_b16 v[112:113], v123 offset:128\n\t"
        "v_mfma_f32_16x16x32_bf16 v[28:31], v[116:119], v[192:195], v[28:31]\n\t"
        "ds_read_b64_tr_b16 v[114:115], v123 offset:3200\n\t"
        "s_waitcnt vmcnt(10) lgkmcnt(7)\n\t"
        "v_mfma_f32_16x16x32_bf16 v[64:67], v[132:135], v[96:99], v[64:67]\n\t"
        "ds_read_b64_tr_b16 v[116:117], v123 offset:160\n\t"
        "s_addk_i32 s44, 0x1000\n\t"
        "v_mfma_f32_16x16x32_bf16 v[72:75], v[136:139], v[96:99], v[72:75]\n\t"
        "ds_read_b64_tr_b16 v[118:119], v123 offset:3232\n\t"
        "s_bitset0_b32 s44, 16\n\t"
        "v_mfma_f32_16x16x32_bf16 v[68:71], v[132:135], v[100:103], v[68:71]\n\t"
        "ds_read_b64_tr_b16 v[96:97], v124 offset:128\n\t"
        "s_cmp_eq_u32 s44, s49\n\t"
        "v_mfma_f32_16x16x32_bf16 v[76:79], v[136:139], v[100:103], v[76:79]\n\t"
        "ds_read_b64_tr_b16 v[98:99], v124 offset:3200\n\t"
        "s_waitcnt lgkmcnt(6)\n\t"
        "v_mfma_f32_16x16x32_bf16 v[80:83], v[132:135], v[104:107], v[80:83]\n\t"
        "ds_read_b64_tr_b16 v[100:101], v124 offset:160\n\t"
        "s_cselect_b32 s44, s50, s44\n\t"
        "v_mfma_f32_16x16x32_bf16 v[88:91], v[136:139], v[104:107], v[88:91]\n\t"
        "ds_read_b64_tr_b16 v[102:103], v124 offset:3232\n\t"
        "v_add_u32 v131, s44, v120\n\t"
        "v_mfma_f32_16x16x32_bf16 v[84:87], v[132:135], v[108:111], v[84:87]\n\t"
        "ds_read_b64_tr_b16 v[104:105], v123 offset:0\n\t"
        "v_add_u32 v129, s46, v121\n\t"
        "v_mfma_f32_16x16x32_bf16 v[92:95], v[136:139], v[108:111], v[92:95]\n\t"
        "ds_read_b64_tr_b16 v[106:107], v123 offset:3072\n\t"
        "s_waitcnt lgkmcnt(6)\n\t"
        "v_mfma_f32_16x16x32_bf16 v[32:35], v[112:115], v[132:135], v[32:35]\n\t"
        "ds_read_b64_tr_b16 v[108:109], v123 offset:32\n\t"
        "v_mfma_f32_16x16x32_bf16 v[40:43], v[112:115], v[136:139], v[40:43]\n\t"
        "ds_read_b64_tr_b16 v[110:111], v123 offset:3104\n\t"
        "v_add_u32 v130, s46, v122\n\t"
        "v_mfma_f32_16x16x32_bf16 v[36:39], v[116:119], v[132:135], v[36:39]\n\t"
        "ds_read_b64_tr_b16 v[112:113], v124 offset:0\n\t"
        "buffer_load_dwordx4 v[180:183], v131, s[40:43], 0 offen offset:0 sc1\n\t"
        "v_mfma_f32_16x16x32_bf16 v[44:47], v[116:119], v[136:139], v[44:47]\n\t"
        "ds_read_b64_tr_b16 v[114:115], v124 offset:3072\n\t"
        "s_waitcnt lgkmcnt(6)\n\t"
        "v_mfma_f32_16x16x32_bf16 v[48:51], v[96:99], v[132:135], v[48:51]\n\t"
        "ds_read_b64_tr_b16 v[116:117], v124 offset:32\n\t"
        "v_mfma_f32_16x16x32_bf16 v[56:59], v[96:99], v[136:139], v[56:59]\n\t"
        "ds_read_b64_tr_b16 v[118:119], v124 offset:3104\n\t"
        "v_mfma_f32_16x16x32_bf16 v[52:55], v[100:103], v[132:135], v[52:55]\n\t"
        "ds_read_b64_tr_b16 v[96:97], v129 offset:256\n\t"
        "s_addk_i32 s46, 12352\n\t"
        "v_mfma_f32_16x16x32_bf16 v[60:63], v[100:103], v[136:139], v[60:63]\n\t"
        "ds_read_b64_tr_b16 v[98:99], v129 offset:3328\n\t"
        "s_waitcnt lgkmcnt(6)\n\t"
        "v_mfma_f32_16x16x32_bf16 v[0:3], v[104:107], v[132:135], v[0:3]\n\t"
        "ds_read_b64_tr_b16 v[100:101], v129 offset:288\n\t"
        "s_cmp_eq_u32 s46, s47\n\t"
        "v_mfma_f32_16x16x32_bf16 v[8:11], v[104:107], v[136:139], v[8:11]\n\t"
        "ds_read_b64_tr_b16 v[102:103], v129 offset:3360\n\t"
        "s_cselect_b32 s46, 0, s46\n\t"
        "v_mfma_f32_16x16x32_bf16 v[4:7], v[108:111], v[132:135], v[4:7]\n\t"
        "ds_read_b64_tr_b16 v[104:105], v130 offset:256\n\t"
        "buffer_load_dwordx4 v[184:187], v131, s[40:43], 0 offen offset:256 sc1\n\t"
        "v_mfma_f32_16x16x32_bf16 v[12:15], v[108:111], v[136:139], v[12:15]\n\t"
        "ds_read_b64_tr_b16 v[106:107], v130 offset:3328\n\t"
        "s_waitcnt lgkmcnt(6)\n\t"
        "v_mfma_f32_16x16x32_bf16 v[16:19], v[112:115], v[132:135], v[16:19]\n\t"
        "ds_read_b64_tr_b16 v[108:109], v130 offset:288\n\t"
        "v_mfma_f32_16x16x32_bf16 v[24:27], v[112:115], v[136:139], v[24:27]\n\t"
        "ds_read_b64_tr_b16 v[110:111], v130 offset:3360\n\t"
        "v_mfma_f32_16x16x32_bf16 v[20:23], v[116:119], v[132:135], v[20:23]\n\t"
        "ds_read_b64_tr_b16 v[112:113], v129 offset:128\n\t"
        "v_mfma_f32_16x16x32_bf16 v[28:31], v[116:119], v[136:139], v[28:31]\n\t"
        "ds_read_b64_tr_b16 v[114:115], v129 offset:3200\n\t"
        "s_waitcnt vmcnt(10) lgkmcnt(6)\n\t"
        "v_mfma_f32_16x16x32_bf16 v[64:67], v[140:143], v[96:99], v[64:67]\n\t"
        "ds_read_b64_tr_b16 v[116:117], v129 offset:160\n\t"
        "v_mfma_f32_16x16x32_bf16 v[72:75], v[144:147], v[96:99], v[72:75]\n\t"
        "ds_read_b64_tr_b16 v[118:119], v129 offset:3232\n\t"
        "v_mfma_f32_16x16x32_bf16 v[68:71], v[140:143], v[100:103], v[68:71]\n\t"
        "ds_read_b64_tr_b16 v[96:97], v130 offset:128\n\t"
        "ds_read_b32 v126, v125\n\t"
        "v_mfma_f32_16x16x32_bf16 v[76:79], v[144:147], v[100:103], v[76:79]\n\t"
        "ds_read_b64_tr_b16 v[98:99], v130 offset:3200\n\t"
        "s_waitcnt lgkmcnt(7)\n\t"
        "v_mfma_f32_16x16x32_bf16 v[80:83], v[140:143], v[104:107], v[80:83]\n\t"
        "ds_read_b64_tr_b16 v[100:101], v130 offset:160\n\t"
        "v_mfma_f32_16x16x32_bf16 v[88:91], v[144:147], v[104:107], v[88:91]\n\t"
        "ds_read_b64_tr_b16 v[102:103], v130 offset:3232\n\t"
        "v_mfma_f32_16x16x32_bf16 v[84:87], v[140:143], v[108:111], v[84:87]\n\t"
        "ds_read_b64_tr_b16 v[104:105], v129 offset:0\n\t"
        "v_add_u32 v123, s46, v121\n\t"
        "v_mfma_f32_16x16x32_bf16 v[92:95], v[144:147], v[108:111], v[92:95]\n\t"
        "ds_read_b64_tr_b16 v[106:107], v129 offset:3072\n\t"
        "s_waitcnt lgkmcnt(5)\n\t"
        "v_mfma_f32_16x16x32_bf16 v[32:35], v[112:115], v[140:143], v[32:35]\n\t"
        "ds_read_b64_tr_b16 v[108:109], v129 offset:32\n\t"
        "v_cmp_gt_u32 vcc, v128, v126\n\t"
        "v_mfma_f32_16x16x32_bf16 v[40:43], v[112:115], v[144:147], v[40:43]\n\t"
        "ds_read_b64_tr_b16 v[110:111], v129 offset:3104\n\t"
        "v_add_u32 v124, s46, v122\n\t"
        "v_mfma_f32_16x16x32_bf16 v[36:39], v[116:119], v[140:143], v[36:39]\n\t"
        "ds_read_b64_tr_b16 v[112:113], v130 offset:0\n\t"
        "buffer_load_dwordx4 v[188:191], v131, s[40:43], 0 offen offset:2048 sc1\n\t"
        "v_mfma_f32_16x16x32_bf16 v[44:47], v[116:119], v[144:147], v[44:47]\n\t"
        "ds_read_b64_tr_b16 v[114:115], v130 offset:3072\n\t"
        "s_waitcnt lgkmcnt(6)\n\t"
        "v_mfma_f32_16x16x32_bf16 v[48:51], v[96:99], v[140:143], v[48:51]\n\t"
        "ds_read_b64_tr_b16 v[116:117], v130 offset:32\n\t"
        "v_mfma_f32_16x16x32_bf16 v[56:59], v[96:99], v[144:147], v[56:59]\n\t"
        "ds_read_b64_tr_b16 v[118:119], v130 offset:3104\n\t"
        "s_cbranch_vccnz 20f\n\t"
        "21:\n\t"
        "v_mfma_f32_16x16x32_bf16 v[52:55], v[100:103], v[140:143], v[52:55]\n\t"
        "ds_read_b64_tr_b16 v[96:97], v123 offset:256\n\t"
        "s_addk_i32 s46, 12352\n\t"
        "v_mfma_f32_16x16x32_bf16 v[60:63], v[100:103], v[144:147], v[60:63]\n\t"
        "ds_read_b64_tr_b16 v[98:99], v123 offset:3328\n\t"
        "s_waitcnt lgkmcnt(6)\n\t"
        "v_mfma_f32_16x16x32_bf16 v[0:3], v[104:107], v[140:143], v[0:3]\n\t"
        "ds_read_b64_tr_b16 v[100:101], v123 offset:288\n\t"
        "s_cmp_eq_u32 s46, s47\n\t"
        "v_mfma_f32_16x16x32_bf16 v[8:11], v[104:107], v[144:147], v[8:11]\n\t"
        "ds_read_b64_tr_b16 v[102:103], v123 offset:3360\n\t"
        "s_cselect_b32 s46, 0, s46\n\t"
        "v_mfma_f32_16x16x32_bf16 v[4:7], v[108:111], v[140:143], v[4:7]\n\t"
        "ds_read_b64_tr_b16 v[104:105], v124 offset:256\n\t"
        "buffer_load_dwordx4 v[192:195], v131, s[40:43], 0 offen offset:2304 sc1\n\t"
        "v_mfma_f32_16x16x32_bf16 v[12:15], v[108:111], v[144:147], v[12:15]\n\t"
        "ds_read_b64_tr_b16 v[106:107], v124 offset:3328\n\t"
        "s_waitcnt lgkmcnt(6)\n\t"
        "v_mfma_f32_16x16x32_bf16 v[16:19], v[112:115], v[140:143], v[16:19]\n\t"
        "ds_write_b32 v127, v128\n\t"
        "ds_read_b64_tr_b16 v[108:109], v124 offset:288\n\t"
        "v_mfma_f32_16x16x32_bf16 v[24:27], v[112:115], v[144:147], v[24:27]\n\t"
        "ds_read_b64_tr_b16 v[110:111], v124 offset:3360\n\t"
        "v_add_u32 v128, 2, v128\n\t"
        "v_mfma_f32_16x16x32_bf16 v[20:23], v[116:119], v[140:143], v[20:23]\n\t"
        "ds_read_b64_tr_b16 v[112:113], v123 offset:128\n\t"
        "s_sub_u32 s45, s45, 1\n\t"
        "v_mfma_f32_16x16x32_bf16 v[28:31], v[116:119], v[144:147], v[28:31]\n\t"
        "ds_read_b64_tr_b16 v[114:115], v123 offset:3200\n\t"
        "s_cbranch_scc0 1b\n\t"
        "s_waitcnt vmcnt(10) lgkmcnt(7)\n\t"
        "v_mfma_f32_16x16x32_bf16 v[64:67], v[148:151], v[96:99], v[64:67]\n\t"
        "ds_read_b64_tr_b16 v[116:117], v121 offset:256\n\t"
        "ds_read_b32 v126, v125\n\t"
        "v_mfma_f32_16x16x32_bf16 v[72:75], v[152:155], v[96:99], v[72:75]\n\t"
        "ds_read_b64_tr_b16 v[118:119], v121 offset:3328\n\t"
        "v_add_u32 v128, 2, v128\n\t"
        "v_mfma_f32_16x16x32_bf16 v[68:71], v[148:151], v[100:103], v[68:71]\n\t"
        "ds_read_b64_tr_b16 v[96:97], v121 offset:288\n\t"
        "v_mfma_f32_16x16x32_bf16 v[76:79], v[152:155], v[100:103], v[76:79]\n\t"
        "ds_read_b64_tr_b16 v[98:99], v121 offset:3360\n\t"
        "s_waitcnt lgkmcnt(3)\n\t"
        "v_mfma_f32_16x16x32_bf16 v[80:83], v[148:151], v[104:107], v[80:83]\n\t"
        "ds_read_b64_tr_b16 v[100:101], v122 offset:256\n\t"
        "v_cmp_gt_u32 vcc, v128, v126\n\t"
        "v_mfma_f32_16x16x32_bf16 v[88:91], v[152:155], v[104:107], v[88:91]\n\t"
        "ds_read_b64_tr_b16 v[102:103], v122 offset:3328\n\t"
        "v_mfma_f32_16x16x32_bf16 v[84:87], v[148:151], v[108:111], v[84:87]\n\t"
        "ds_read_b64_tr_b16 v[104:105], v122 offset:288\n\t"
        "v_mfma_f32_16x16x32_bf16 v[92:95], v[152:155], v[108:111], v[92:95]\n\t"
        "ds_read_b64_tr_b16 v[106:107], v122 offset:3360\n\t"
        "s_cbranch_vccnz 38f\n\t"
        "39:\n\t"
        "s_waitcnt vmcnt(8) lgkmcnt(6)\n\t"
        "v_mfma_f32_16x16x32_bf16 v[32:35], v[112:115], v[148:151], v[32:35]\n\t"
        "ds_read_b64_tr_b16 v[108:109], v121 offset:12608\n\t"
        "v_mfma_f32_16x16x32_bf16 v[40:43], v[112:115], v[152:155], v[40:43]\n\t"
        "ds_read_b64_tr_b16 v[110:111], v121 offset:15680\n\t"
        "v_mfma_f32_16x16x32_bf16 v[64:67], v[156:159], v[116:119], v[64:67]\n\t"
        "ds_read_b64_tr_b16 v[112:113], v121 offset:12640\n\t"
        "v_mfma_f32_16x16x32_bf16 v[72:75], v[160:163], v[116:119], v[72:75]\n\t"
        "ds_read_b64_tr_b16 v[114:115], v121 offset:15712\n\t"
        "s_waitcnt lgkmcnt(6)\n\t"
        "v_mfma_f32_16x16x32_bf16 v[68:71], v[156:159], v[96:99], v[68:71]\n\t"
        "ds_read_b64_tr_b16 v[116:117], v122 offset:12608\n\t"
        "ds_read_b128 v[196:199], v215 offset:0\n\t"
        "v_mfma_f32_16x16x32_bf16 v[76:79], v[160:163], v[96:99], v[76:79]\n\t"
        "ds_read_b64_tr_b16 v[118:119], v122 offset:15680\n\t"
        "v_mfma_f32_16x16x32_bf16 v[80:83], v[156:159], v[100:103], v[80:83]\n\t"
        "ds_read_b64_tr_b16 v[96:97], v122 offset:12640\n\t"
        "v_mfma_f32_16x16x32_bf16 v[88:91], v[160:163], v[100:103], v[88:91]\n\t"
        "ds_read_b64_tr_b16 v[98:99], v122 offset:15712\n\t"
        "s_waitcnt vmcnt(6) lgkmcnt(7)\n\t"
        "v_mfma_f32_16x16x32_bf16 v[84:87], v[156:159], v[104:107], v[84:87]\n\t"
        "ds_read_b64_tr_b16 v[100:101], v121 offset:24960\n\t"
        "ds_read_b128 v[200:203], v215 offset:64\n\t"
        "v_mfma_f32_16x16x32_bf16 v[92:95], v[160:163], v[104:107], v[92:95]\n\t"
        "ds_read_b64_tr_b16 v[102:103], v121 offset:28032\n\t"
        "v_mfma_f32_16x16x32_bf16 v[64:67], v[164:167], v[108:111], v[64:67]\n\t"
        "ds_read_b64_tr_b16 v[104:105], v121 offset:24992\n\t"
        "v_mfma_f32_16x16x32_bf16 v[72:75], v[168:171], v[108:111], v[72:75]\n\t"
        "ds_read_b64_tr_b16 v[106:107], v121 offset:28064\n\t"
        "s_waitcnt lgkmcnt(7)\n\t"
        "v_mfma_f32_16x16x32_bf16 v[68:71], v[164:167], v[112:115], v[68:71]\n\t"
        "ds_read_b64_tr_b16 v[108:109], v122 offset:24960\n\t"
        "v_mfma_f32_16x16x32_bf16 v[76:79], v[168:171], v[112:115], v[76:79]\n\t"
        "ds_read_b64_tr_b16 v[110:111], v122 offset:28032\n\t"
        "v_mfma_f32_16x16x32_bf16 v[80:83], v[164:167], v[116:119], v[80:83]\n\t"
        "ds_read_b64_tr_b16 v[112:113], v122 offset:24992\n\t"
        "v_mfma_f32_16x16x32_bf16 v[88:91], v[168:171], v[116:119], v[88:91]\n\t"
        "ds_read_b64_tr_b16 v[114:115], v122 offset:28064\n\t"
        "s_waitcnt vmcnt(4) lgkmcnt(6)\n\t"
        "v_mfma_f32_16x16x32_bf16 v[84:87], v[164:167], v[96:99], v[84:87]\n\t"
        "ds_read_b64_tr_b16 v[116:117], v121 offset:37312\n\t"
        "v_mfma_f32_16x16x32_bf16 v[92:95], v[168:171], v[96:99], v[92:95]\n\t"
        "ds_read_b64_tr_b16 v[118:119], v121 offset:40384\n\t"
        "v_mfma_f32_16x16x32_bf16 v[64:67], v[172:175], v[100:103], v[64:67]\n\t"
        "ds_read_b64_tr_b16 v[96:97], v121 offset:37344\n\t"
        "v_mfma_f32_16x16x32_bf16 v[72:75], v[176:179], v[100:103], v[72:75]\n\t"
        "ds_read_b64_tr_b16 v[98:99], v121 offset:40416\n\t"
        "s_waitcnt lgkmcnt(6)\n\t"
        "v_mfma_f32_16x16x32_bf16 v[68:71], v[172:175], v[104:107], v[68:71]\n\t"
        "ds_read_b64_tr_b16 v[100:101], v122 offset:37312\n\t"
        "v_mfma_f32_16x16x32_bf16 v[76:79], v[176:179], v[104:107], v[76:79]\n\t"
        "ds_read_b64_tr_b16 v[102:103], v122 offset:40384\n\t"
        "v_mfma_f32_16x16x32_bf16 v[80:83], v[172:175], v[108:111], v[80:83]\n\t"
        "ds_read_b64_tr_b16 v[104:105], v122 offset:37344\n\t"
        "v_mfma_f32_16x16x32_bf16 v[88:91], v[176:179], v[108:111], v[88:91]\n\t"
        "ds_read_b64_tr_b16 v[106:107], v122 offset:40416\n\t"
        "s_waitcnt vmcnt(2) lgkmcnt(6)\n\t"
        "v_mfma_f32_16x16x32_bf16 v[84:87], v[172:175], v[112:115], v[84:87]\n\t"
        "ds_read_b64_tr_b16 v[108:109], v121 offset:49664\n\t"
        "v_mfma_f32_16x16x32_bf16 v[92:95], v[176:179], v[112:115], v[92:95]\n\t"
        "ds_read_b64_tr_b16 v[110:111], v121 offset:52736\n\t"
        "v_mfma_f32_16x16x32_bf16 v[64:67], v[180:183], v[116:119], v[64:67]\n\t"
        "ds_read_b64_tr_b16 v[112:113], v121 offset:49696\n\t"
        "v_mfma_f32_16x16x32_bf16 v[72:75], v[184:187], v[116:119], v[72:75]\n\t"
        "ds_read_b64_tr_b16 v[114:115], v121 offset:52768\n\t"
        "s_waitcnt lgkmcnt(6)\n\t"
        "v_mfma_f32_16x16x32_bf16 v[68:71], v[180:183], v[96:99], v[68:71]\n\t"
        "ds_read_b64_tr_b16 v[116:117], v122 offset:49664\n\t"
        "v_mfma_f32_16x16x32_bf16 v[76:79], v[184:187], v[96:99], v[76:79]\n\t"
        "ds_read_b64_tr_b16 v[118:119], v122 offset:52736\n\t"
        "v_mfma_f32_16x16x32_bf16 v[80:83], v[180:183], v[100:103], v[80:83]\n\t"
        "ds_read_b64_tr_b16 v[96:97], v122 offset:49696\n\t"
        "v_mfma_f32_16x16x32_bf16 v[88:91], v[184:187], v[100:103], v[88:91]\n\t"
        "ds_read_b64_tr_b16 v[98:99], v122 offset:52768\n\t"
        "s_waitcnt vmcnt(0) lgkmcnt(6)\n\t"
        "v_mfma_f32_16x16x32_bf16 v[84:87], v[180:183], v[104:107], v[84:87]\n\t"
        "ds_read_b64_tr_b16 v[100:101], v123 offset:160\n\t"
        "v_mfma_f32_16x16x32_bf16 v[92:95], v[184:187], v[104:107], v[92:95]\n\t"
        "ds_read_b64_tr_b16 v[102:103], v123 offset:3232\n\t"
        "v_mfma_f32_16x16x32_bf16 v[64:67], v[188:191], v[108:111], v[64:67]\n\t"
        "ds_read_b64_tr_b16 v[104:105], v124 offset:128\n\t"
        "v_mfma_f32_16x16x32_bf16 v[72:75], v[192:195], v[108:111], v[72:75]\n\t"
        "ds_read_b64_tr_b16 v[106:107], v124 offset:3200\n\t"
        "s_waitcnt lgkmcnt(6)\n\t"
        "v_mfma_f32_16x16x32_bf16 v[68:71], v[188:191], v[112:115], v[68:71]\n\t"
        "ds_read_b64_tr_b16 v[108:109], v124 offset:160\n\t"
        "v_mfma_f32_16x16x32_bf16 v[76:79], v[192:195], v[112:115], v[76:79]\n\t"
        "ds_read_b64_tr_b16 v[110:111], v124 offset:3232\n\t"
        "v_mfma_f32_16x16x32_bf16 v[80:83], v[188:191], v[116:119], v[80:83]\n\t"
        "ds_read_b64_tr_b16 v[112:113], v121 offset:128\n\t"
        "v_mfma_f32_16x16x32_bf16 v[88:91], v[192:195], v[116:119], v[88:91]\n\t"
        "ds_read_b64_tr_b16 v[114:115], v121 offset:3200\n\t"
        "s_waitcnt lgkmcnt(6)\n\t"
        "v_mfma_f32_16x16x32_bf16 v[84:87], v[188:191], v[96:99], v[84:87]\n\t"
        "ds_read_b64_tr_b16 v[116:117], v121 offset:160\n\t"
        "v_mfma_f32_16x16x32_bf16 v[92:95], v[192:195], v[96:99], v[92:95]\n\t"
        "ds_read_b64_tr_b16 v[118:119], v121 offset:3232\n\t"
        "v_mfma_f32_16x16x32_bf16 v[36:39], v[100:103], v[148:151], v[36:39]\n\t"
        "ds_read_b64_tr_b16 v[96:97], v122 offset:128\n\t"
        "v_mfma_f32_16x16x32_bf16 v[44:47], v[100:103], v[152:155], v[44:47]\n\t"
        "ds_read_b64_tr_b16 v[98:99], v122 offset:3200\n\t"
        "s_waitcnt lgkmcnt(6)\n\t"
        "v_mfma_f32_16x16x32_bf16 v[48:51], v[104:107], v[148:151], v[48:51]\n\t"
        "ds_read_b64_tr_b16 v[100:101], v122 offset:160\n\t"
        "v_mul_f32 v64, v196, v64\n\t"
        "v_mul_f32 v65, v197, v65\n\t"
        "v_mfma_f32_16x16x32_bf16 v[56:59], v[104:107], v[152:155], v[56:59]\n\t"
        "ds_read_b64_tr_b16 v[102:103], v122 offset:3232\n\t"
        "v_mul_f32 v66, v198, v66\n\t"
        "v_mul_f32 v67, v199, v67\n\t"
        "v_mfma_f32_16x16x32_bf16 v[52:55], v[108:111], v[148:151], v[52:55]\n\t"
        "ds_read_b64_tr_b16 v[104:105], v121 offset:12480\n\t"
        "v_mul_f32 v68, v196, v68\n\t"
        "v_mul_f32 v69, v197, v69\n\t"
        "v_mul_f32 v70, v198, v70\n\t"
        "v_mfma_f32_16x16x32_bf16 v[60:63], v[108:111], v[152:155], v[60:63]\n\t"
        "ds_read_b64_tr_b16 v[106:107], v121 offset:15552\n\t"
        "v_mul_f32 v71, v199, v71\n\t"
        "v_cvt_pk_bf16_f32 v64, v64, v65\n\t"
        "s_waitcnt lgkmcnt(6)\n\t"
        "v_mfma_f32_16x16x32_bf16 v[32:35], v[112:115], v[156:159], v[32:35]\n\t"
        "ds_read_b64_tr_b16 v[108:109], v121 offset:12512\n\t"
        "v_cvt_pk_bf16_f32 v65, v66, v67\n\t"
        "v_cvt_pk_bf16_f32 v66, v68, v69\n\t"
        "v_mfma_f32_16x16x32_bf16 v[40:43], v[112:115], v[160:163], v[40:43]\n\t"
        "ds_read_b64_tr_b16 v[110:111], v121 offset:15584\n\t"
        "v_cvt_pk_bf16_f32 v67, v70, v71\n\t"
        "v_mul_f32 v72, v200, v72\n\t"
        "v_mfma_f32_16x16x32_bf16 v[36:39], v[116:119], v[156:159], v[36:39]\n\t"
        "ds_read_b64_tr_b16 v[112:113], v122 offset:12480\n\t"
        "v_mul_f32 v73, v201, v73\n\t"
        "v_mul_f32 v74, v202, v74\n\t"
        "v_mul_f32 v75, v203, v75\n\t"
        "v_mfma_f32_16x16x32_bf16 v[44:47], v[116:119], v[160:163], v[44:47]\n\t"
        "ds_read_b64_tr_b16 v[114:115], v122 offset:15552\n\t"
        "v_mul_f32 v76, v200, v76\n\t"
        "v_mul_f32 v77, v201, v77\n\t"
        "s_waitcnt lgkmcnt(6)\n\t"
        "v_mfma_f32_16x16x32_bf16 v[48:51], v[96:99], v[156:159], v[48:51]\n\t"
        "ds_read_b64_tr_b16 v[116:117], v122 offset:12512\n\t"
        "v_mul_f32 v78, v202, v78\n\t"
        "v_mul_f32 v79, v203, v79\n\t"
        "v_mfma_f32_16x16x32_bf16 v[56:59], v[96:99], v[160:163], v[56:59]\n\t"
        "ds_read_b64_tr_b16 v[118:119], v122 offset:15584\n\t"
        "v_cvt_pk_bf16_f32 v68, v72, v73\n\t"
        "v_cvt_pk_bf16_f32 v69, v74, v75\n\t"
        "v_mfma_f32_16x16x32_bf16 v[52:55], v[100:103], v[156:159], v[52:55]\n\t"
        "ds_read_b64_tr_b16 v[96:97], v121 offset:24832\n\t"
        "v_cvt_pk_bf16_f32 v70, v76, v77\n\t"
        "v_cvt_pk_bf16_f32 v71, v78, v79\n\t"
        "v_permlane16_swap_b32 v64, v66\n\t"
        "v_mfma_f32_16x16x32_bf16 v[60:63], v[100:103], v[160:163], v[60:63]\n\t"
        "ds_read_b64_tr_b16 v[98:99], v121 offset:27904\n\t"
        "v_permlane16_swap_b32 v65, v67\n\t"
        "v_permlane16_swap_b32 v68, v70\n\t"
        "s_waitcnt lgkmcnt(6)\n\t"
        "v_mfma_f32_16x16x32_bf16 v[32:35], v[104:107], v[164:167], v[32:35]\n\t"
        "ds_read_b64_tr_b16 v[100:101], v121 offset:24864\n\t"
        "v_permlane16_swap_b32 v69, v71\n\t"
        "buffer_store_dwordx4 v[64:67], v213, s[40:43], 0 offen offset:2048 sc1\n\t"
        "v_mfma_f32_16x16x32_bf16 v[40:43], v[104:107], v[168:171], v[40:43]\n\t"
        "ds_read_b64_tr_b16 v[102:103], v121 offset:27936\n\t"
        "buffer_store_dwordx4 v[68:71], v213, s[40:43], 0 offen offset:3072 sc1\n\t"
        "v_mul_f32 v80, v196, v80\n\t"
        "v_mfma_f32_16x16x32_bf16 v[36:39], v[108:111], v[164:167], v[36:39]\n\t"
        "ds_read_b64_tr_b16 v[104:105], v122 offset:24832\n\t"
        "v_mul_f32 v81, v197, v81\n\t"
        "v_mul_f32 v82, v198, v82\n\t"
        "v_mul_f32 v83, v199, v83\n\t"
        "v_mfma_f32_16x16x32_bf16 v[44:47], v[108:111], v[168:171], v[44:47]\n\t"
        "ds_read_b64_tr_b16 v[106:107], v122 offset:27904\n\t"
        "v_mul_f32 v84, v196, v84\n\t"
        "v_mul_f32 v85, v197, v85\n\t"
        "s_waitcnt lgkmcnt(6)\n\t"
        "v_mfma_f32_16x16x32_bf16 v[48:51], v[112:115], v[164:167], v[48:51]\n\t"
        "ds_read_b64_tr_b16 v[108:109], v122 offset:24864\n\t"
        "v_mul_f32 v86, v198, v86\n\t"
        "v_mul_f32 v87, v199, v87\n\t"
        "v_mfma_f32_16x16x32_bf16 v[56:59], v[112:115], v[168:171], v[56:59]\n\t"
        "ds_read_b64_tr_b16 v[110:111], v122 offset:27936\n\t"
        "v_cvt_pk_bf16_f32 v80, v80, v81\n\t"
        "v_cvt_pk_bf16_f32 v81, v82, v83\n\t"
        "v_mfma_f32_16x16x32_bf16 v[52:55], v[116:119], v[164:167], v[52:55]\n\t"
        "ds_read_b64_tr_b16 v[112:113], v121 offset:37184\n\t"
        "v_cvt_pk_bf16_f32 v82, v84, v85\n\t"
        "v_cvt_pk_bf16_f32 v83, v86, v87\n\t"
        "v_mul_f32 v88, v200, v88\n\t"
        "v_mfma_f32_16x16x32_bf16 v[60:63], v[116:119], v[168:171], v[60:63]\n\t"
        "ds_read_b64_tr_b16 v[114:115], v121 offset:40256\n\t"
        "v_mul_f32 v89, v201, v89\n\t"
        "v_mul_f32 v90, v202, v90\n\t"
        "s_waitcnt lgkmcnt(6)\n\t"
        "v_mfma_f32_16x16x32_bf16 v[32:35], v[96:99], v[172:175], v[32:35]\n\t"
        "ds_read_b64_tr_b16 v[116:117], v121 offset:37216\n\t"
        "v_mul_f32 v91, v203, v91\n\t"
        "v_mul_f32 v92, v200, v92\n\t"
        "v_mfma_f32_16x16x32_bf16 v[40:43], v[96:99], v[176:179], v[40:43]\n\t"
        "ds_read_b64_tr_b16 v[118:119], v121 offset:40288\n\t"
        "v_mul_f32 v93, v201, v93\n\t"
        "v_mul_f32 v94, v202, v94\n\t"
        "v_mfma_f32_16x16x32_bf16 v[36:39], v[100:103], v[172:175], v[36:39]\n\t"
        "ds_read_b64_tr_b16 v[96:97], v122 offset:37184\n\t"
        "v_mul_f32 v95, v203, v95\n\t"
        "v_cvt_pk_bf16_f32 v84, v88, v89\n\t"
        "v_cvt_pk_bf16_f32 v85, v90, v91\n\t"
        "v_mfma_f32_16x16x32_bf16 v[44:47], v[100:103], v[176:179], v[44:47]\n\t"
        "ds_read_b64_tr_b16 v[98:99], v122 offset:40256\n\t"
        "v_cvt_pk_bf16_f32 v86, v92, v93\n\t"
        "v_cvt_pk_bf16_f32 v87, v94, v95\n\t"
        "s_waitcnt lgkmcnt(6)\n\t"
        "v_mfma_f32_16x16x32_bf16 v[48:51], v[104:107], v[172:175], v[48:51]\n\t"
        "ds_read_b64_tr_b16 v[100:101], v122 offset:37216\n\t"
        "v_permlane16_swap_b32 v80, v82\n\t"
        "v_permlane16_swap_b32 v81, v83\n\t"
        "v_mfma_f32_16x16x32_bf16 v[56:59], v[104:107], v[176:179], v[56:59]\n\t"
        "ds_read_b64_tr_b16 v[102:103], v122 offset:40288\n\t"
        "v_permlane16_swap_b32 v84, v86\n\t"
        "v_permlane16_swap_b32 v85, v87\n\t"
        "v_mfma_f32_16x16x32_bf16 v[52:55], v[108:111], v[172:175], v[52:55]\n\t"
        "ds_read_b64_tr_b16 v[104:105], v121 offset:49536\n\t"
        "buffer_store_dwordx4 v[80:83], v214, s[40:43], 0 offen offset:2048 sc1\n\t"
        "buffer_store_dwordx4 v[84:87], v214, s[40:43], 0 offen offset:3072 sc1\n\t"
        "ds_write_b128 v212, v[80:83] offset:2048\n\t"
        "v_mfma_f32_16x16x32_bf16 v[60:63], v[108:111], v[176:179], v[60:63]\n\t"
        "ds_read_b64_tr_b16 v[106:107], v121 offset:52608\n\t"
        "ds_write_b128 v212, v[84:87] offset:3072\n\t"
        "s_waitcnt lgkmcnt(8)\n\t"
        "v_mfma_f32_16x16x32_bf16 v[32:35], v[112:115], v[180:183], v[32:35]\n\t"
        "ds_read_b64_tr_b16 v[108:109], v121 offset:49568\n\t"
        "v_mfma_f32_16x16x32_bf16 v[40:43], v[112:115], v[184:187], v[40:43]\n\t"
        "ds_read_b64_tr_b16 v[110:111], v121 offset:52640\n\t"
        "v_mfma_f32_16x16x32_bf16 v[36:39], v[116:119], v[180:183], v[36:39]\n\t"
        "ds_read_b64_tr_b16 v[112:113], v122 offset:49536\n\t"
        "v_mfma_f32_16x16x32_bf16 v[44:47], v[116:119], v[184:187], v[44:47]\n\t"
        "ds_read_b64_tr_b16 v[114:115], v122 offset:52608\n\t"
        "s_waitcnt lgkmcnt(8)\n\t"
        "v_mfma_f32_16x16x32_bf16 v[48:51], v[96:99], v[180:183], v[48:51]\n\t"
        "ds_read_b64_tr_b16 v[116:117], v122 offset:49568\n\t"
        "v_mfma_f32_16x16x32_bf16 v[56:59], v[96:99], v[184:187], v[56:59]\n\t"
        "ds_read_b64_tr_b16 v[118:119], v122 offset:52640\n\t"
        "v_mfma_f32_16x16x32_bf16 v[52:55], v[100:103], v[180:183], v[52:55]\n\t"
        "ds_read_b64_tr_b16 v[96:97], v123 offset:0\n\t"
        "v_mfma_f32_16x16x32_bf16 v[60:63], v[100:103], v[184:187], v[60:63]\n\t"
        "ds_read_b64_tr_b16 v[98:99], v123 offset:3072\n\t"
        "s_waitcnt lgkmcnt(6)\n\t"
        "v_mfma_f32_16x16x32_bf16 v[32:35], v[104:107], v[188:191], v[32:35]\n\t"
        "ds_read_b64_tr_b16 v[100:101], v123 offset:32\n\t"
        "v_mfma_f32_16x16x32_bf16 v[40:43], v[104:107], v[192:195], v[40:43]\n\t"
        "ds_read_b64_tr_b16 v[102:103], v123 offset:3104\n\t"
        "v_mfma_f32_16x16x32_bf16 v[36:39], v[108:111], v[188:191], v[36:39]\n\t"
        "ds_read_b64_tr_b16 v[104:105], v124 offset:0\n\t"
        "v_mfma_f32_16x16x32_bf16 v[44:47], v[108:111], v[192:195], v[44:47]\n\t"
        "ds_read_b64_tr_b16 v[106:107], v124 offset:3072\n\t"
        "s_waitcnt lgkmcnt(6)\n\t"
        "v_mfma_f32_16x16x32_bf16 v[48:51], v[112:115], v[188:191], v[48:51]\n\t"
        "ds_read_b64_tr_b16 v[108:109], v124 offset:32\n\t"
        "v_mfma_f32_16x16x32_bf16 v[56:59], v[112:115], v[192:195], v[56:59]\n\t"
        "ds_read_b64_tr_b16 v[110:111], v124 offset:3104\n\t"
        "v_mfma_f32_16x16x32_bf16 v[52:55], v[116:119], v[188:191], v[52:55]\n\t"
        "ds_read_b64_tr_b16 v[112:113], v121 offset:0\n\t"
        "v_mfma_f32_16x16x32_bf16 v[60:63], v[116:119], v[192:195], v[60:63]\n\t"
        "ds_read_b64_tr_b16 v[114:115], v121 offset:3072\n\t"
        "s_waitcnt lgkmcnt(6)\n\t"
        "v_mfma_f32_16x16x32_bf16 v[0:3], v[96:99], v[148:151], v[0:3]\n\t"
        "ds_read_b64_tr_b16 v[116:117], v121 offset:32\n\t"
        "v_mfma_f32_16x16x32_bf16 v[8:11], v[96:99], v[152:155], v[8:11]\n\t"
        "ds_read_b64_tr_b16 v[118:119], v121 offset:3104\n\t"
        "v_mfma_f32_16x16x32_bf16 v[4:7], v[100:103], v[148:151], v[4:7]\n\t"
        "ds_read_b64_tr_b16 v[96:97], v122 offset:0\n\t"
        "v_mul_f32 v32, v210, v32\n\t"
        "v_mul_f32 v33, v210, v33\n\t"
        "v_mul_f32 v34, v210, v34\n\t"
        "v_mfma_f32_16x16x32_bf16 v[12:15], v[100:103], v[152:155], v[12:15]\n\t"
        "ds_read_b64_tr_b16 v[98:99], v122 offset:3072\n\t"
        "v_mul_f32 v35, v210, v35\n\t"
        "v_mul_f32 v36, v210, v36\n\t"
        "s_waitcnt lgkmcnt(6)\n\t"
        "v_mfma_f32_16x16x32_bf16 v[16:19], v[104:107], v[148:151], v[16:19]\n\t"
        "ds_read_b64_tr_b16 v[100:101], v122 offset:32\n\t"
        "v_mul_f32 v37, v210, v37\n\t"
        "v_mul_f32 v38, v210, v38\n\t"
        "v_mfma_f32_16x16x32_bf16 v[24:27], v[104:107], v[152:155], v[24:27]\n\t"
        "ds_read_b64_tr_b16 v[102:103], v122 offset:3104\n\t"
        "v_mul_f32 v39, v210, v39\n\t"
        "v_cvt_pk_bf16_f32 v32, v32, v33\n\t"
        "v_mfma_f32_16x16x32_bf16 v[20:23], v[108:111], v[148:151], v[20:23]\n\t"
        "ds_read_b64_tr_b16 v[104:105], v121 offset:12352\n\t"
        "v_cvt_pk_bf16_f32 v33, v34, v35\n\t"
        "v_cvt_pk_bf16_f32 v34, v36, v37\n\t"
        "v_cvt_pk_bf16_f32 v35, v38, v39\n\t"
        "v_mfma_f32_16x16x32_bf16 v[28:31], v[108:111], v[152:155], v[28:31]\n\t"
        "ds_read_b64_tr_b16 v[106:107], v121 offset:15424\n\t"
        "v_mul_f32 v40, v211, v40\n\t"
        "v_mul_f32 v41, v211, v41\n\t"
        "s_waitcnt lgkmcnt(6)\n\t"
        "v_mfma_f32_16x16x32_bf16 v[0:3], v[112:115], v[156:159], v[0:3]\n\t"
        "ds_read_b64_tr_b16 v[108:109], v121 offset:12384\n\t"
        "v_mul_f32 v42, v211, v42\n\t"
        "v_mul_f32 v43, v211, v43\n\t"
        "v_mfma_f32_16x16x32_bf16 v[8:11], v[112:115], v[160:163], v[8:11]\n\t"
        "ds_read_b64_tr_b16 v[110:111], v121 offset:15456\n\t"
        "v_mul_f32 v44, v211, v44\n\t"
        "v_mul_f32 v45, v211, v45\n\t"
        "v_mfma_f32_16x16x32_bf16 v[4:7], v[116:119], v[156:159], v[4:7]\n\t"
        "ds_read_b64_tr_b16 v[112:113], v122 offset:12352\n\t"
        "v_mul_f32 v46, v211, v46\n\t"
        "v_mul_f32 v47, v211, v47\n\t"
        "v_cvt_pk_bf16_f32 v36, v40, v41\n\t"
        "v_mfma_f32_16x16x32_bf16 v[12:15], v[116:119], v[160:163], v[12:15]\n\t"
        "ds_read_b64_tr_b16 v[114:115], v122 offset:15424\n\t"
        "v_cvt_pk_bf16_f32 v37, v42, v43\n\t"
        "v_cvt_pk_bf16_f32 v38, v44, v45\n\t"
        "s_waitcnt lgkmcnt(6)\n\t"
        "v_mfma_f32_16x16x32_bf16 v[16:19], v[96:99], v[156:159], v[16:19]\n\t"
        "ds_read_b64_tr_b16 v[116:117], v122 offset:12384\n\t"
        "v_cvt_pk_bf16_f32 v39, v46, v47\n\t"
        "v_permlane16_swap_b32 v32, v36\n\t"
        "v_mfma_f32_16x16x32_bf16 v[24:27], v[96:99], v[160:163], v[24:27]\n\t"
        "ds_read_b64_tr_b16 v[118:119], v122 offset:15456\n\t"
        "v_permlane16_swap_b32 v33, v37\n\t"
        "v_permlane16_swap_b32 v34, v38\n\t"
        "v_mfma_f32_16x16x32_bf16 v[20:23], v[100:103], v[156:159], v[20:23]\n\t"
        "ds_read_b64_tr_b16 v[96:97], v121 offset:24704\n\t"
        "v_permlane16_swap_b32 v35, v39\n\t"
        "buffer_store_dwordx4 v[32:35], v213, s[40:43], 0 offen offset:0 sc1\n\t"
        "buffer_store_dwordx4 v[36:39], v213, s[40:43], 0 offen offset:1024 sc1\n\t"
        "v_mfma_f32_16x16x32_bf16 v[28:31], v[100:103], v[160:163], v[28:31]\n\t"
        "ds_read_b64_tr_b16 v[98:99], v121 offset:27776\n\t"
        "v_mul_f32 v48, v210, v48\n\t"
        "v_mul_f32 v49, v210, v49\n\t"
        "s_waitcnt lgkmcnt(6)\n\t"
        "v_mfma_f32_16x16x32_bf16 v[0:3], v[104:107], v[164:167], v[0:3]\n\t"
        "ds_read_b64_tr_b16 v[100:101], v121 offset:24736\n\t"
        "v_mul_f32 v50, v210, v50\n\t"
        "v_mul_f32 v51, v210, v51\n\t"
        "v_mfma_f32_16x16x32_bf16 v[8:11], v[104:107], v[168:171], v[8:11]\n\t"
        "ds_read_b64_tr_b16 v[102:103], v121 offset:27808\n\t"
        "v_mul_f32 v52, v210, v52\n\t"
        "v_mul_f32 v53, v210, v53\n\t"
        "v_mfma_f32_16x16x32_bf16 v[4:7], v[108:111], v[164:167], v[4:7]\n\t"
        "ds_read_b64_tr_b16 v[104:105], v122 offset:24704\n\t"
        "v_mul_f32 v54, v210, v54\n\t"
        "v_mul_f32 v55, v210, v55\n\t"
        "v_cvt_pk_bf16_f32 v48, v48, v49\n\t"
        "v_mfma_f32_16x16x32_bf16 v[12:15], v[108:111], v[168:171], v[12:15]\n\t"
        "ds_read_b64_tr_b16 v[106:107], v122 offset:27776\n\t"
        "v_cvt_pk_bf16_f32 v49, v50, v51\n\t"
        "v_cvt_pk_bf16_f32 v50, v52, v53\n\t"
        "s_waitcnt lgkmcnt(6)\n\t"
        "v_mfma_f32_16x16x32_bf16 v[16:19], v[112:115], v[164:167], v[16:19]\n\t"
        "ds_read_b64_tr_b16 v[108:109], v122 offset:24736\n\t"
        "v_cvt_pk_bf16_f32 v51, v54, v55\n\t"
        "v_mul_f32 v56, v211, v56\n\t"
        "v_mfma_f32_16x16x32_bf16 v[24:27], v[112:115], v[168:171], v[24:27]\n\t"
        "ds_read_b64_tr_b16 v[110:111], v122 offset:27808\n\t"
        "v_mul_f32 v57, v211, v57\n\t"
        "v_mul_f32 v58, v211, v58\n\t"
        "v_mfma_f32_16x16x32_bf16 v[20:23], v[116:119], v[164:167], v[20:23]\n\t"
        "ds_read_b64_tr_b16 v[112:113], v121 offset:37056\n\t"
        "v_mul_f32 v59, v211, v59\n\t"
        "v_mul_f32 v60, v211, v60\n\t"
        "v_mul_f32 v61, v211, v61\n\t"
        "v_mfma_f32_16x16x32_bf16 v[28:31], v[116:119], v[168:171], v[28:31]\n\t"
        "ds_read_b64_tr_b16 v[114:115], v121 offset:40128\n\t"
        "v_mul_f32 v62, v211, v62\n\t"
        "v_mul_f32 v63, v211, v63\n\t"
        "s_waitcnt lgkmcnt(6)\n\t"
        "v_mfma_f32_16x16x32_bf16 v[0:3], v[96:99], v[172:175], v[0:3]\n\t"
        "ds_read_b64_tr_b16 v[116:117], v121 offset:37088\n\t"
        "v_cvt_pk_bf16_f32 v52, v56, v57\n\t"
        "v_cvt_pk_bf16_f32 v53, v58, v59\n\t"
        "v_mfma_f32_16x16x32_bf16 v[8:11], v[96:99], v[176:179], v[8:11]\n\t"
        "ds_read_b64_tr_b16 v[118:119], v121 offset:40160\n\t"
        "v_cvt_pk_bf16_f32 v54, v60, v61\n\t"
        "v_cvt_pk_bf16_f32 v55, v62, v63\n\t"
        "v_mfma_f32_16x16x32_bf16 v[4:7], v[100:103], v[172:175], v[4:7]\n\t"
        "ds_read_b64_tr_b16 v[96:97], v122 offset:37056\n\t"
        "v_permlane16_swap_b32 v48, v52\n\t"
        "v_permlane16_swap_b32 v49, v53\n\t"
        "v_permlane16_swap_b32 v50, v54\n\t"
        "v_mfma_f32_16x16x32_bf16 v[12:15], v[100:103], v[176:179], v[12:15]\n\t"
        "ds_read_b64_tr_b16 v[98:99], v122 offset:40128\n\t"
        "v_permlane16_swap_b32 v51, v55\n\t"
        "buffer_store_dwordx4 v[48:51], v214, s[40:43], 0 offen offset:0 sc1\n\t"
        "s_waitcnt lgkmcnt(6)\n\t"
        "v_mfma_f32_16x16x32_bf16 v[16:19], v[104:107], v[172:175], v[16:19]\n\t"
        "ds_read_b64_tr_b16 v[100:101], v122 offset:37088\n\t"
        "buffer_store_dwordx4 v[52:55], v214, s[40:43], 0 offen offset:1024 sc1\n\t"
        "ds_write_b128 v212, v[48:51] offset:0\n\t"
        "v_mfma_f32_16x16x32_bf16 v[24:27], v[104:107], v[176:179], v[24:27]\n\t"
        "ds_read_b64_tr_b16 v[102:103], v122 offset:40160\n\t"
        "ds_write_b128 v212, v[52:55] offset:1024\n\t"
        "v_mfma_f32_16x16x32_bf16 v[20:23], v[108:111], v[172:175], v[20:23]\n\t"
        "ds_read_b64_tr_b16 v[104:105], v121 offset:49408\n\t"
        "v_mfma_f32_16x16x32_bf16 v[28:31], v[108:111], v[176:179], v[28:31]\n\t"
        "ds_read_b64_tr_b16 v[106:107], v121 offset:52480\n\t"
        "s_waitcnt lgkmcnt(8)\n\t"
        "v_mfma_f32_16x16x32_bf16 v[0:3], v[112:115], v[180:183], v[0:3]\n\t"
        "ds_read_b64_tr_b16 v[108:109], v121 offset:49440\n\t"
        "v_mfma_f32_16x16x32_bf16 v[8:11], v[112:115], v[184:187], v[8:11]\n\t"
        "ds_read_b64_tr_b16 v[110:111], v121 offset:52512\n\t"
        "v_mfma_f32_16x16x32_bf16 v[4:7], v[116:119], v[180:183], v[4:7]\n\t"
        "ds_read_b64_tr_b16 v[112:113], v122 offset:49408\n\t"
        "v_mfma_f32_16x16x32_bf16 v[12:15], v[116:119], v[184:187], v[12:15]\n\t"
        "ds_read_b64_tr_b16 v[114:115], v122 offset:52480\n\t"
        "s_waitcnt lgkmcnt(7)\n\t"
        "v_mfma_f32_16x16x32_bf16 v[16:19], v[96:99], v[180:183], v[16:19]\n\t"
        "ds_read_b64_tr_b16 v[116:117], v122 offset:49440\n\t"
        "v_mfma_f32_16x16x32_bf16 v[24:27], v[96:99], v[184:187], v[24:27]\n\t"
        "ds_read_b64_tr_b16 v[118:119], v122 offset:52512\n\t"
        "v_mfma_f32_16x16x32_bf16 v[20:23], v[100:103], v[180:183], v[20:23]\n\t"
        "v_mfma_f32_16x16x32_bf16 v[28:31], v[100:103], v[184:187], v[28:31]\n\t"
        "s_waitcnt lgkmcnt(4)\n\t"
        "v_mfma_f32_16x16x32_bf16 v[0:3], v[104:107], v[188:191], v[0:3]\n\t"
        "v_mfma_f32_16x16x32_bf16 v[8:11], v[104:107], v[192:195], v[8:11]\n\t"
        "v_mfma_f32_16x16x32_bf16 v[4:7], v[108:111], v[188:191], v[4:7]\n\t"
        "v_mfma_f32_16x16x32_bf16 v[12:15], v[108:111], v[192:195], v[12:15]\n\t"
        "s_waitcnt lgkmcnt(0)\n\t"
        "v_mfma_f32_16x16x32_bf16 v[16:19], v[112:115], v[188:191], v[16:19]\n\t"
        "v_mfma_f32_16x16x32_bf16 v[24:27], v[112:115], v[192:195], v[24:27]\n\t"
        "v_mfma_f32_16x16x32_bf16 v[20:23], v[116:119], v[188:191], v[20:23]\n\t"
        "v_mfma_f32_16x16x32_bf16 v[28:31], v[116:119], v[192:195], v[28:31]\n\t"
        "s_nop 7\n\t"
        "s_branch 9f\n\t"
        "12:\n\t"
        "s_mov_b32 s48, 0x10000\n\t"
        "8:\n\t"
        "ds_read_b32 v126, v125\n\t"
        "s_sub_u32 s48, s48, 1\n\t"
        "s_waitcnt lgkmcnt(0)\n\t"
        "s_cbranch_scc1 13b\n\t"
        "v_cmp_gt_u32 vcc, v128, v126\n\t"
        "s_cbranch_vccnz 8b\n\t"
        "s_branch 13b\n\t"
        "14:\n\t"
        "s_mov_b32 s48, 0x10000\n\t"
        "8:\n\t"
        "ds_read_b32 v126, v125\n\t"
        "s_sub_u32 s48, s48, 1\n\t"
        "s_waitcnt lgkmcnt(0)\n\t"
        "s_cbranch_scc1 15b\n\t"
        "v_cmp_gt_u32 vcc, v128, v126\n\t"
        "s_cbranch_vccnz 8b\n\t"
        "s_branch 15b\n\t"
        "16:\n\t"
        "s_mov_b32 s48, 0x10000\n\t"
        "8:\n\t"
        "ds_read_b32 v126, v125\n\t"
        "s_sub_u32 s48, s48, 1\n\t"
        "s_waitcnt lgkmcnt(0)\n\t"
        "s_cbranch_scc1 17b\n\t"
        "v_cmp_gt_u32 vcc, v128, v126\n\t"
        "s_cbranch_vccnz 8b\n\t"
        "s_branch 17b\n\t"
        "18:\n\t"
        "s_mov_b32 s48, 0x10000\n\t"
        "8:\n\t"
        "ds_read_b32 v126, v125\n\t"
        "s_sub_u32 s48, s48, 1\n\t"
        "s_waitcnt lgkmcnt(0)\n\t"
        "s_cbranch_scc1 19b\n\t"
        "v_cmp_gt_u32 vcc, v128, v126\n\t"
        "s_cbranch_vccnz 8b\n\t"
        "s_branch 19b\n\t"
        "20:\n\t"
        "s_mov_b32 s48, 0x10000\n\t"
        "8:\n\t"
        "ds_read_b32 v126, v125\n\t"
        "s_sub_u32 s48, s48, 1\n\t"
        "s_waitcnt lgkmcnt(0)\n\t"
        "s_cbranch_scc1 21b\n\t"
        "v_cmp_gt_u32 vcc, v128, v126\n\t"
        "s_cbranch_vccnz 8b\n\t"
        "s_branch 21b\n\t"
        "38:\n\t"
        "s_mov_b32 s48, 0x10000\n\t"
        "8:\n\t"
        "ds_read_b32 v126, v125\n\t"
        "s_sub_u32 s48, s48, 1\n\t"
        "s_waitcnt lgkmcnt(0)\n\t"
        "s_cbranch_scc1 39b\n\t"
        "v_cmp_gt_u32 vcc, v128, v126\n\t"
        "s_cbranch_vccnz 8b\n\t"
        "s_branch 39b\n\t"
        "9:\n\t"
        : "+{v[0:15]}"(acc[0]), "+{v[16:31]}"(acc[1]), "+{v[32:47]}"(acc[2]), "+{v[48:63]}"(acc[3]), "+{v[64:79]}"(acc[4]), "+{v[80:95]}"(acc[5]),
          "+{v[96:111]}"(stA), "+{v[112:127]}"(stB), "+{v[128:131]}"(stC), "+{v[148:163]}"(xr1), "+{v[164:179]}"(xr2), "+{v[180:195]}"(xr3),
          "+{s44}"(soff), "+{s46}"(slot), "+{v[140:147]}"(xrb)
        : "{s[40:43]}"(srd), KSKIP, "{v213}"(ep[0]), "{v214}"(ep[1]), "{v215}"(ep[2]), "{v212}"(ep[3]), "{v[210:211]}"(al2) : "v196", "v197", "v198", "v199", "v200", "v201", "v202", "v203", "v132", "v133", "v134", "v135", "v136", "v137", "v138", "v139", "s45", "s47", "s48", "vcc", "scc", "memory");
      STAMPV(4 + 16 * L);
      PHASE_VARS;
      u32x4 kv[4][2];
#pragma unroll
      for (int j = 0; j < 4; ++j)
#pragma unroll
        for (int e = 0; e < 8; ++e) kv[j][e >> 2][e & 3] = __float_as_uint(acc[2 + j][e]);
      u32x4 qo[2][2];
      auto drained = [&]() { asm volatile("s_waitcnt vmcnt(0)\n\tds_write_b32 %0, %1" :: "v"(lbase + L_W + 80 + 4 * wid), "v"(2 * L + 1) : "memory"); };
      if (L == 0) {
#pragma unroll
        for (int e = 0; e < 16; ++e) {
          if (e == 8) { SBAR(); drained(); SBAR(); }
          const unsigned w = pack2(acc[e >> 3][2 * (e & 7)], acc[e >> 3][2 * (e & 7) + 1]);
          qo[e >> 3][(e >> 2) & 1][e & 3] = pack2(lo16(w) * QS, hi16(w) * QS);
        }
        row_swap(qo[0][0], qo[0][1]); row_swap(qo[1][0], qo[1][1]);
      } else {
        lds_wait_ge<0>(lbase + L_W + 4 + 4 * (lane & 1), 1);
        const unsigned idxv = words[3];
        const f32x2 aq = al2 * QS;
#pragma unroll
        for (int e = 0; e < 16; ++e) qo[e >> 3][(e >> 2) & 1][e & 3] = pack2(acc[e >> 3][2 * (e & 7)] * aq[(e >> 2) & 1], acc[e >> 3][2 * (e & 7) + 1] * aq[(e >> 2) & 1]);
        row_swap(qo[0][0], qo[0][1]); row_swap(qo[1][0], qo[1][1]);
        SBAR();
        const int idx = (int)__builtin_amdgcn_readfirstlane(idxv);
        if ((idx >> 5) == tile) {
          const int rel = (idx & 31) - 8 * hi, dfo = ((r32 >> 2) & 1) * 16 + ((r32 >> 3) & 1) * 8 + (r32 >> 4) * 4 + (r32 & 3);
          unsigned pv[2];
#pragma unroll
          for (int j = 0; j < 2; ++j) pv[j] = reinterpret_cast<const u16*>(smem + L_PATCH)[128 + (j ^ hh) * 32 + dfo] * 0x10001u;
          DRAIN();
          if (r32 == (idx & 31)) {
#pragma unroll
            for (int i = 0; i < 4; ++i) {
              const char* p = smem + L_PATCH + ((i >> 1) ^ hh) * 64 + (i & 1) * 32 + hi * 16;
              qo[i >> 1][i & 1] = *reinterpret_cast<const u32x4*>(p);
              kv[i >> 1][i & 1] = *reinterpret_cast<const u32x4*>(p + 128);
              x1p[i] = *reinterpret_cast<const u32x4*>(smem + L_X2P + (i * 2 + hi) * 16);
            }
            reinterpret_cast<float*>(smem + L_AB)[256 + wid * 64 + lane] = hi ? 1.0f : 0.0f;
#pragma unroll
            for (int i = 0; i < 4; ++i) stg(rs, ep[i >> 1] + (i & 1) * 1024, kv[i >> 1][i & 1]);
          }
#pragma unroll
          for (int i = 0; i < 4; ++i) {
#pragma unroll
            for (int e = 0; e < 4; ++e) {
              const int t0 = 16 * (i & 1) + 2 * e;
              const unsigned mask = rel == t0 ? 0xffffu : rel == t0 + 1 ? 0xffff0000u : 0u;
              kv[2 + (i >> 1)][i & 1][e] = (kv[2 + (i >> 1)][i & 1][e] & ~mask) | (pv[i >> 1] & mask);
            }
            stg(rs, (rel >> 3) == 2 * (i & 1) ? ep[i >> 1] + 2048 + (i & 1) * 1024 : OOB, kv[2 + (i >> 1)][i & 1]);
          }
#pragma unroll
          for (int i = 0; i < 4; ++i) *reinterpret_cast<u32x4*>(smem + L_KX + wid * 4096 + i * 1024 + lane * 16) = kv[(i & 2) + 1][i & 1];
        }
        if (8 < 16) drained();
      }
      SBAR();
#pragma unroll
      for (int j = 0; j < 2; ++j) {
        char* p = smem + L_QX + ((j ^ hh) * 4 + wid) * 2048 + lane * 16;
        *reinterpret_cast<u32x4*>(p) = qo[j][0]; *reinterpret_cast<u32x4*>(p + 1024) = qo[j][1];
      }
      if (8 >= 16) drained();
      STAMPV(41 + 4 * L);
      BAR_LGKM();
      STAMPV(43 + 4 * L);
      kva = cat4(kv[0][0], kv[0][1], kv[2][0], kv[2][1]);
      const u32x4* pk = reinterpret_cast<const u32x4*>(smem + L_KX + (wid ^ 1) * 4096 + lane * 16);
#pragma unroll
      for (int u = 0; u < 4; ++u) {
        const u32x4* p = reinterpret_cast<const u32x4*>(smem + L_QX + (hh * 4 + ((2 * kh + 2 + u) & 3)) * 2048 + lane * 16);
        q[u] = __builtin_shufflevector(p[0], p[64], 0, 1, 2, 3, 4, 5, 6, 7);
      }
      kvb = cat4(pk[0], pk[64], pk[128], pk[192]);
      SBAR();
      asm volatile(
        "v_mov_b64 v[0:1], 0\n\t"
        "v_mov_b64 v[2:3], 0\n\t"
        "v_mov_b64 v[4:5], 0\n\t"
        "v_mov_b64 v[6:7], 0\n\t"
        "v_mov_b64 v[8:9], 0\n\t"
        "v_mov_b64 v[10:11], 0\n\t"
        "v_mov_b64 v[12:13], 0\n\t"
        "v_mov_b64 v[14:15], 0\n\t"
        "v_mov_b64 v[16:17], 0\n\t"
        "v_mov_b64 v[18:19], 0\n\t"
        "v_mov_b64 v[20:21], 0\n\t"
        "v_mov_b64 v[22:23], 0\n\t"
        "v_mov_b64 v[24:25], 0\n\t"
        "v_mov_b64 v[26:27], 0\n\t"
        "v_mov_b64 v[28:29], 0\n\t"
        "v_mov_b64 v[30:31], 0\n\t"
        "v_mov_b64 v[32:33], 0\n\t"
        "v_mov_b64 v[34:35], 0\n\t"
        "v_mov_b64 v[36:37], 0\n\t"
        "v_mov_b64 v[38:39], 0\n\t"
        "v_mov_b64 v[40:41], 0\n\t"
        "v_mov_b64 v[42:43], 0\n\t"
        "v_mov_b64 v[44:45], 0\n\t"
        "v_mov_b64 v[46:47], 0\n\t"
        "v_mov_b64 v[48:49], 0\n\t"
        "v_mov_b64 v[50:51], 0\n\t"
        "v_mov_b64 v[52:53], 0\n\t"
        "v_mov_b64 v[54:55], 0\n\t"
        "v_mov_b64 v[56:57], 0\n\t"
        "v_mov_b64 v[58:59], 0\n\t"
        "v_mov_b64 v[60:61], 0\n\t"
        "v_mov_b64 v[62:63], 0\n\t"
        "v_mov_b64 v[128:129], 0\n\t"
        "v_mov_b64 v[130:131], 0\n\t"
        "v_mov_b64 v[132:133], 0\n\t"
        "v_mov_b64 v[134:135], 0\n\t"
        "v_mov_b64 v[136:137], 0\n\t"
        "v_mov_b64 v[138:139], 0\n\t"
        "v_mov_b64 v[140:141], 0\n\t"
        "v_mov_b64 v[142:143], 0\n\t"
        : "={v[0:15]}"(oz[0]), "={v[16:31]}"(oz[1]), "={v[32:47]}"(oz[2]), "={v[48:63]}"(oz[3]), "={v[128:143]}"(oz[4]));
    }
    STAMPV(5 + 16 * L);
    {
      u32x4 ones;
      unsigned kvoff;
      u32x8 fv, cv;
      asm volatile("" : "={v[184:191]}"(fv), "={v[216:223]}"(cv));
      { PHASE_VARS;
        const unsigned v = (lane & 3) == 0 ? 0x3F803F80u : 0u; const bool up = (lane & 4) != 0; ones[0] = ones[1] = up ? 0u : v; ones[2] = ones[3] = up ? v : 0u;
        kvoff = OFF_KV + L * (10 * MBY) + (tn * 2 + hh) * HS + kh * 8192 + lane * 16;
        fv[1] = lbase + L_FREE + wid * 256 + lane * 4; fv[2] = 32 * L + 32;
        cv[0] = lbase + L_XCH + wid * 8192 + lane * 16; cv[1] = lbase + L_XCH + (wid ^ 2) * 8192 + lane * 16;
        cv[2] = lbase + L_XCH + 32768 + wid * 512 + lane * 4; cv[3] = lbase + L_XCH + 32768 + (wid ^ 2) * 512 + lane * 4;
        cv[4] = lbase + L_W + 48 + wid * 4; cv[5] = lbase + L_W + 48 + (wid ^ 2) * 4; cv[6] = L + 1;
        cv[7] = ((tile * 32 + r32) * D_ + tn * 64 + hi * 8) * 2; }
      f32x16 o0 = oz[0], o1 = oz[1], o2 = oz[2], o3 = oz[3], lv = oz[4];
      unsigned soff = (tm | 7) * 16384;
      STAMP_DECL;
      asm volatile(
        "s_nop 1\n\t"
        "v_mfma_f32_32x32x16_bf16 v[96:111], v[152:155], v[64:67], 0\n\t"
        "v_mfma_f32_32x32x16_bf16 v[96:111], v[156:159], v[68:71], v[96:111]\n\t"
        "s_mov_b32 s46, 0\n\t"
        "s_mov_b64 s[54:55], s[42:43]\n\t"
        "ds_write_b32 v185, v186\n\t"
        "s_nop 8\n\t"
        "v_exp_f32 v96, v96\n\t"
        "v_exp_f32 v97, v97\n\t"
        "v_exp_f32 v98, v98\n\t"
        "v_exp_f32 v99, v99\n\t"
        "v_exp_f32 v100, v100\n\t"
        "v_exp_f32 v101, v101\n\t"
        "v_exp_f32 v102, v102\n\t"
        "v_mfma_f32_32x32x16_bf16 v[112:127], v[152:155], v[72:75], 0\n\t"
        "v_exp_f32 v103, v103\n\t"
        "v_exp_f32 v104, v104\n\t"
        "v_exp_f32 v105, v105\n\t"
        "v_mfma_f32_32x32x16_bf16 v[112:127], v[156:159], v[76:79], v[112:127]\n\t"
        "v_exp_f32 v106, v106\n\t"
        "v_exp_f32 v107, v107\n\t"
        "v_exp_f32 v108, v108\n\t"
        "v_smfmac_f32_32x32x32_bf16 v[0:15], v[160:163], v[96:103], v148\n\t"
        "v_exp_f32 v109, v109\n\t"
        "v_exp_f32 v110, v110\n\t"
        "v_exp_f32 v111, v111\n\t"
        "v_smfmac_f32_16x16x64_bf16 v[128:131], v[144:147], v[96:103], v148\n\t"
        "v_exp_f32 v112, v112\n\t"
        "v_exp_f32 v113, v113\n\t"
        "v_smfmac_f32_32x32x32_bf16 v[0:15], v[164:167], v[104:111], v148\n\t"
        "v_exp_f32 v114, v114\n\t"
        "v_exp_f32 v115, v115\n\t"
        "v_exp_f32 v116, v116\n\t"
        "v_smfmac_f32_16x16x64_bf16 v[128:131], v[144:147], v[104:111], v148\n\t"
        "v_exp_f32 v117, v117\n\t"
        "v_exp_f32 v118, v118\n\t"
        "v_mfma_f32_32x32x16_bf16 v[96:111], v[152:155], v[80:83], 0\n\t"
        "v_exp_f32 v119, v119\n\t"
        "v_exp_f32 v120, v120\n\t"
        "v_exp_f32 v121, v121\n\t"
        "v_mfma_f32_32x32x16_bf16 v[96:111], v[156:159], v[84:87], v[96:111]\n\t"
        "ds_read_b32 v151, v224\n\t"
        "v_exp_f32 v122, v122\n\t"
        "v_exp_f32 v123, v123\n\t"
        "v_exp_f32 v124, v124\n\t"
        "v_smfmac_f32_32x32x32_bf16 v[16:31], v[160:163], v[112:119], v148\n\t"
        "v_exp_f32 v125, v125\n\t"
        "v_exp_f32 v126, v126\n\t"
        "v_exp_f32 v127, v127\n\t"
        "v_smfmac_f32_16x16x64_bf16 v[132:135], v[144:147], v[112:119], v148\n\t"
        "s_cmp_lg_u32 s46, 0\n\t"
        "s_cbranch_scc1 25f\n\t"
        "s_waitcnt lgkmcnt(0)\n\t"
        "v_cmp_ne_u32 vcc, v222, v151\n\t"
        "s_cbranch_vccz 24f\n\t"
        "25:\n\t"
        "v_exp_f32 v96, v96\n\t"
        "v_exp_f32 v97, v97\n\t"
        "v_smfmac_f32_32x32x32_bf16 v[16:31], v[164:167], v[120:127], v148\n\t"
        "v_exp_f32 v98, v98\n\t"
        "v_exp_f32 v99, v99\n\t"
        "v_exp_f32 v100, v100\n\t"
        "v_smfmac_f32_16x16x64_bf16 v[132:135], v[144:147], v[120:127], v148\n\t"
        "v_exp_f32 v101, v101\n\t"
        "v_exp_f32 v102, v102\n\t"
        "v_mfma_f32_32x32x16_bf16 v[112:127], v[152:155], v[88:91], 0\n\t"
        "v_exp_f32 v103, v103\n\t"
        "v_exp_f32 v104, v104\n\t"
        "v_exp_f32 v105, v105\n\t"
        "v_mfma_f32_32x32x16_bf16 v[112:127], v[156:159], v[92:95], v[112:127]\n\t"
        "ds_read_b32 v151, v224\n\t"
        "v_exp_f32 v106, v106\n\t"
        "v_exp_f32 v107, v107\n\t"
        "v_exp_f32 v108, v108\n\t"
        "v_smfmac_f32_32x32x32_bf16 v[32:47], v[160:163], v[96:103], v148\n\t"
        "v_exp_f32 v109, v109\n\t"
        "v_exp_f32 v110, v110\n\t"
        "v_exp_f32 v111, v111\n\t"
        "v_smfmac_f32_16x16x64_bf16 v[136:139], v[144:147], v[96:103], v148\n\t"
        "s_cmp_lg_u32 s46, 0\n\t"
        "s_cbranch_scc1 27f\n\t"
        "s_waitcnt lgkmcnt(0)\n\t"
        "v_cmp_ne_u32 vcc, v222, v151\n\t"
        "s_cbranch_vccz 26f\n\t"
        "27:\n\t"
        "v_exp_f32 v112, v112\n\t"
        "v_exp_f32 v113, v113\n\t"
        "v_smfmac_f32_32x32x32_bf16 v[32:47], v[164:167], v[104:111], v148\n\t"
        "v_exp_f32 v114, v114\n\t"
        "v_exp_f32 v115, v115\n\t"
        "v_exp_f32 v116, v116\n\t"
        "v_smfmac_f32_16x16x64_bf16 v[136:139], v[144:147], v[104:111], v148\n\t"
        "v_exp_f32 v117, v117\n\t"
        "v_exp_f32 v118, v118\n\t"
        "v_mfma_f32_32x32x16_bf16 v[96:111], v[168:171], v[64:67], 0\n\t"
        "v_exp_f32 v119, v119\n\t"
        "v_exp_f32 v120, v120\n\t"
        "v_exp_f32 v121, v121\n\t"
        "v_mfma_f32_32x32x16_bf16 v[96:111], v[172:175], v[68:71], v[96:111]\n\t"
        "ds_read_b32 v151, v224\n\t"
        "v_exp_f32 v122, v122\n\t"
        "v_exp_f32 v123, v123\n\t"
        "v_exp_f32 v124, v124\n\t"
        "v_smfmac_f32_32x32x32_bf16 v[48:63], v[160:163], v[112:119], v148\n\t"
        "v_exp_f32 v125, v125\n\t"
        "v_exp_f32 v126, v126\n\t"
        "v_exp_f32 v127, v127\n\t"
        "v_smfmac_f32_16x16x64_bf16 v[140:143], v[144:147], v[112:119], v148\n\t"
        "s_cmp_lg_u32 s46, 0\n\t"
        "s_cbranch_scc1 29f\n\t"
        "s_waitcnt lgkmcnt(0)\n\t"
        "v_cmp_ne_u32 vcc, v222, v151\n\t"
        "s_cbranch_vccz 28f\n\t"
        "29:\n\t"
        "v_exp_f32 v96, v96\n\t"
        "v_exp_f32 v97, v97\n\t"
        "v_smfmac_f32_32x32x32_bf16 v[48:63], v[164:167], v[120:127], v148\n\t"
        "v_exp_f32 v98, v98\n\t"
        "v_exp_f32 v99, v99\n\t"
        "v_exp_f32 v100, v100\n\t"
        "v_smfmac_f32_16x16x64_bf16 v[140:143], v[144:147], v[120:127], v148\n\t"
        "v_exp_f32 v101, v101\n\t"
        "v_exp_f32 v102, v102\n\t"
        "v_mfma_f32_32x32x16_bf16 v[112:127], v[168:171], v[72:75], 0\n\t"
        "v_exp_f32 v103, v103\n\t"
        "v_exp_f32 v104, v104\n\t"
        "v_exp_f32 v105, v105\n\t"
        "v_mfma_f32_32x32x16_bf16 v[112:127], v[172:175], v[76:79], v[112:127]\n\t"
        "ds_read_b32 v151, v224\n\t"
        "v_exp_f32 v106, v106\n\t"
        "v_exp_f32 v107, v107\n\t"
        "v_exp_f32 v108, v108\n\t"
        "v_smfmac_f32_32x32x32_bf16 v[0:15], v[176:179], v[96:103], v148\n\t"
        "v_exp_f32 v109, v109\n\t"
        "v_exp_f32 v110, v110\n\t"
        "v_exp_f32 v111, v111\n\t"
        "v_smfmac_f32_16x16x64_bf16 v[128:131], v[144:147], v[96:103], v148\n\t"
        "s_cmp_lg_u32 s46, 0\n\t"
        "s_cbranch_scc1 31f\n\t"
        "s_waitcnt lgkmcnt(0)\n\t"
        "v_cmp_ne_u32 vcc, v222, v151\n\t"
        "s_cbranch_vccz 30f\n\t"
        "31:\n\t"
        "v_exp_f32 v112, v112\n\t"
        "v_exp_f32 v113, v113\n\t"
        "v_smfmac_f32_32x32x32_bf16 v[0:15], v[180:183], v[104:111], v148\n\t"
        "v_exp_f32 v114, v114\n\t"
        "v_exp_f32 v115, v115\n\t"
        "v_exp_f32 v116, v116\n\t"
        "v_smfmac_f32_16x16x64_bf16 v[128:131], v[144:147], v[104:111], v148\n\t"
        "v_exp_f32 v117, v117\n\t"
        "v_exp_f32 v118, v118\n\t"
        "v_mfma_f32_32x32x16_bf16 v[96:111], v[168:171], v[80:83], 0\n\t"
        "v_exp_f32 v119, v119\n\t"
        "v_exp_f32 v120, v120\n\t"
        "v_exp_f32 v121, v121\n\t"
        "v_mfma_f32_32x32x16_bf16 v[96:111], v[172:175], v[84:87], v[96:111]\n\t"
        "ds_read_b32 v151, v224\n\t"
        "v_exp_f32 v122, v122\n\t"
        "v_exp_f32 v123, v123\n\t"
        "v_exp_f32 v124, v124\n\t"
        "v_smfmac_f32_32x32x32_bf16 v[16:31], v[176:179], v[112:119], v148\n\t"
        "v_exp_f32 v125, v125\n\t"
        "v_exp_f32 v126, v126\n\t"
        "v_exp_f32 v127, v127\n\t"
        "v_smfmac_f32_16x16x64_bf16 v[132:135], v[144:147], v[112:119], v148\n\t"
        "s_cmp_lg_u32 s46, 0\n\t"
        "s_cbranch_scc1 33f\n\t"
        "s_waitcnt lgkmcnt(0)\n\t"
        "v_cmp_ne_u32 vcc, v222, v151\n\t"
        "s_cbranch_vccz 32f\n\t"
        "33:\n\t"
        "v_exp_f32 v96, v96\n\t"
        "v_exp_f32 v97, v97\n\t"
        "v_smfmac_f32_32x32x32_bf16 v[16:31], v[180:183], v[120:127], v148\n\t"
        "v_exp_f32 v98, v98\n\t"
        "v_exp_f32 v99, v99\n\t"
        "v_exp_f32 v100, v100\n\t"
        "v_smfmac_f32_16x16x64_bf16 v[132:135], v[144:147], v[120:127], v148\n\t"
        "v_exp_f32 v101, v101\n\t"
        "v_exp_f32 v102, v102\n\t"
        "v_mfma_f32_32x32x16_bf16 v[112:127], v[168:171], v[88:91], 0\n\t"
        "v_exp_f32 v103, v103\n\t"
        "v_exp_f32 v104, v104\n\t"
        "v_exp_f32 v105, v105\n\t"
        "v_mfma_f32_32x32x16_bf16 v[112:127], v[172:175], v[92:95], v[112:127]\n\t"
        "v_exp_f32 v106, v106\n\t"
        "v_exp_f32 v107, v107\n\t"
        "v_exp_f32 v108, v108\n\t"
        "v_smfmac_f32_32x32x32_bf16 v[32:47], v[176:179], v[96:103], v148\n\t"
        "v_exp_f32 v109, v109\n\t"
        "v_exp_f32 v110, v110\n\t"
        "v_exp_f32 v111, v111\n\t"
        "v_smfmac_f32_16x16x64_bf16 v[136:139], v[144:147], v[96:103], v148\n\t"
        "s_cmp_lg_u32 s46, 0\n\t"
        "s_cbranch_scc1 5f\n\t"
        "s_mov_b32 s45, 0x200000\n\t"
        "2:\n\t"
        "ds_read_b32 v151, v224\n\t"
        "s_sub_u32 s45, s45, 1\n\t"
        "s_waitcnt lgkmcnt(0)\n\t"
        "s_cbranch_scc1 3f\n\t"
        "v_cmp_ne_u32 vcc, v222, v151\n\t"
        "s_cbranch_vccnz 2b\n\t"
        "3:\n\t"
        "s_add_u32 s44, s44, 0x4000\n\t"
        "s_and_b32 s44, s44, 0x3ffff\n\t"
        "s_cmp_eq_u32 s44, s49\n\t"
        "s_cselect_b32 s44, s50, s44\n\t"
        "s_add_u32 s52, s40, s44\n\t"
        "s_addc_u32 s53, s41, 0\n\t"
        "buffer_load_dwordx4 v[184:187], v149, s[52:55], 0 offen offset:0 sc1\n\t"
        "buffer_load_dwordx4 v[188:191], v149, s[52:55], 0 offen offset:1024 sc1\n\t"
        "buffer_load_dwordx4 v[192:195], v149, s[52:55], 0 offen offset:2048 sc1\n\t"
        "buffer_load_dwordx4 v[196:199], v149, s[52:55], 0 offen offset:3072 sc1\n\t"
        "buffer_load_dwordx4 v[200:203], v150, s[52:55], 0 offen offset:0 sc1\n\t"
        "buffer_load_dwordx4 v[204:207], v150, s[52:55], 0 offen offset:1024 sc1\n\t"
        "buffer_load_dwordx4 v[208:211], v150, s[52:55], 0 offen offset:2048 sc1\n\t"
        "buffer_load_dwordx4 v[212:215], v150, s[52:55], 0 offen offset:3072 sc1\n\t"
        "5:\n\t"
        "s_add_u32 s44, s44, 0x4000\n\t"
        "s_and_b32 s44, s44, 0x3ffff\n\t"
        "s_cmp_eq_u32 s44, s49\n\t"
        "s_cselect_b32 s44, s50, s44\n\t"
        "s_add_u32 s52, s40, s44\n\t"
        "s_addc_u32 s53, s41, 0\n\t"
        "buffer_load_dwordx4 v[152:155], v149, s[52:55], 0 offen offset:0 sc1\n\t"
        "buffer_load_dwordx4 v[156:159], v149, s[52:55], 0 offen offset:1024 sc1\n\t"
        "buffer_load_dwordx4 v[160:163], v149, s[52:55], 0 offen offset:2048 sc1\n\t"
        "buffer_load_dwordx4 v[164:167], v149, s[52:55], 0 offen offset:3072 sc1\n\t"
        "s_mov_b32 s45, 6\n\t"
        "1:\n\t"
        "v_exp_f32 v112, v112\n\t"
        "v_exp_f32 v113, v113\n\t"
        "v_smfmac_f32_32x32x32_bf16 v[32:47], v[180:183], v[104:111], v148\n\t"
        "v_exp_f32 v114, v114\n\t"
        "v_exp_f32 v115, v115\n\t"
        "v_exp_f32 v116, v116\n\t"
        "v_smfmac_f32_16x16x64_bf16 v[136:139], v[144:147], v[104:111], v148\n\t"
        "s_waitcnt vmcnt(8)\n\t"
        "v_exp_f32 v117, v117\n\t"
        "v_exp_f32 v118, v118\n\t"
        "v_mfma_f32_32x32x16_bf16 v[96:111], v[184:187], v[64:67], 0\n\t"
        "v_exp_f32 v119, v119\n\t"
        "v_exp_f32 v120, v120\n\t"
        "v_exp_f32 v121, v121\n\t"
        "v_mfma_f32_32x32x16_bf16 v[96:111], v[188:191], v[68:71], v[96:111]\n\t"
        "v_exp_f32 v122, v122\n\t"
        "v_exp_f32 v123, v123\n\t"
        "v_exp_f32 v124, v124\n\t"
        "v_smfmac_f32_32x32x32_bf16 v[48:63], v[176:179], v[112:119], v148\n\t"
        "v_exp_f32 v125, v125\n\t"
        "v_exp_f32 v126, v126\n\t"
        "v_exp_f32 v127, v127\n\t"
        "v_smfmac_f32_16x16x64_bf16 v[140:143], v[144:147], v[112:119], v148\n\t"
        "buffer_load_dwordx4 v[168:171], v150, s[52:55], 0 offen offset:0 sc1\n\t"
        "v_exp_f32 v96, v96\n\t"
        "v_exp_f32 v97, v97\n\t"
        "v_smfmac_f32_32x32x32_bf16 v[48:63], v[180:183], v[120:127], v148\n\t"
        "v_exp_f32 v98, v98\n\t"
        "v_exp_f32 v99, v99\n\t"
        "v_exp_f32 v100, v100\n\t"
        "v_smfmac_f32_16x16x64_bf16 v[140:143], v[144:147], v[120:127], v148\n\t"
        "v_exp_f32 v101, v101\n\t"
        "v_exp_f32 v102, v102\n\t"
        "v_mfma_f32_32x32x16_bf16 v[112:127], v[184:187], v[72:75], 0\n\t"
        "v_exp_f32 v103, v103\n\t"
        "v_exp_f32 v104, v104\n\t"
        "v_exp_f32 v105, v105\n\t"
        "v_mfma_f32_32x32x16_bf16 v[112:127], v[188:191], v[76:79], v[112:127]\n\t"
        "v_exp_f32 v106, v106\n\t"
        "v_exp_f32 v107, v107\n\t"
        "v_exp_f32 v108, v108\n\t"
        "v_smfmac_f32_32x32x32_bf16 v[0:15], v[192:195], v[96:103], v148\n\t"
        "v_exp_f32 v109, v109\n\t"
        "v_exp_f32 v110, v110\n\t"
        "v_exp_f32 v111, v111\n\t"
        "v_smfmac_f32_16x16x64_bf16 v[128:131], v[144:147], v[96:103], v148\n\t"
        "buffer_load_dwordx4 v[172:175], v150, s[52:55], 0 offen offset:1024 sc1\n\t"
        "v_exp_f32 v112, v112\n\t"
        "v_exp_f32 v113, v113\n\t"
        "v_smfmac_f32_32x32x32_bf16 v[0:15], v[196:199], v[104:111], v148\n\t"
        "v_exp_f32 v114, v114\n\t"
        "v_exp_f32 v115, v115\n\t"
        "v_exp_f32 v116, v116\n\t"
        "v_smfmac_f32_16x16x64_bf16 v[128:131], v[144:147], v[104:111], v148\n\t"
        "v_exp_f32 v117, v117\n\t"
        "v_exp_f32 v118, v118\n\t"
        "v_mfma_f32_32x32x16_bf16 v[96:111], v[184:187], v[80:83], 0\n\t"
        "v_exp_f32 v119, v119\n\t"
        "v_exp_f32 v120, v120\n\t"
        "v_exp_f32 v121, v121\n\t"
        "v_mfma_f32_32x32x16_bf16 v[96:111], v[188:191], v[84:87], v[96:111]\n\t"
        "v_exp_f32 v122, v122\n\t"
        "v_exp_f32 v123, v123\n\t"
        "v_exp_f32 v124, v124\n\t"
        "v_smfmac_f32_32x32x32_bf16 v[16:31], v[192:195], v[112:119], v148\n\t"
        "v_exp_f32 v125, v125\n\t"
        "v_exp_f32 v126, v126\n\t"
        "v_exp_f32 v127, v127\n\t"
        "v_smfmac_f32_16x16x64_bf16 v[132:135], v[144:147], v[112:119], v148\n\t"
        "buffer_load_dwordx4 v[176:179], v150, s[52:55], 0 offen offset:2048 sc1\n\t"
        "v_exp_f32 v96, v96\n\t"
        "v_exp_f32 v97, v97\n\t"
        "v_smfmac_f32_32x32x32_bf16 v[16:31], v[196:199], v[120:127], v148\n\t"
        "v_exp_f32 v98, v98\n\t"
        "v_exp_f32 v99, v99\n\t"
        "v_exp_f32 v100, v100\n\t"
        "v_smfmac_f32_16x16x64_bf16 v[132:135], v[144:147], v[120:127], v148\n\t"
        "v_exp_f32 v101, v101\n\t"
        "v_exp_f32 v102, v102\n\t"
        "v_mfma_f32_32x32x16_bf16 v[112:127], v[184:187], v[88:91], 0\n\t"
        "v_exp_f32 v103, v103\n\t"
        "v_exp_f32 v104, v104\n\t"
        "v_exp_f32 v105, v105\n\t"
        "v_mfma_f32_32x32x16_bf16 v[112:127], v[188:191], v[92:95], v[112:127]\n\t"
        "v_exp_f32 v106, v106\n\t"
        "v_exp_f32 v107, v107\n\t"
        "v_exp_f32 v108, v108\n\t"
        "v_smfmac_f32_32x32x32_bf16 v[32:47], v[192:195], v[96:103], v148\n\t"
        "v_exp_f32 v109, v109\n\t"
        "v_exp_f32 v110, v110\n\t"
        "v_exp_f32 v111, v111\n\t"
        "v_smfmac_f32_16x16x64_bf16 v[136:139], v[144:147], v[96:103], v148\n\t"
        "buffer_load_dwordx4 v[180:183], v150, s[52:55], 0 offen offset:3072 sc1\n\t"
        "v_exp_f32 v112, v112\n\t"
        "v_exp_f32 v113, v113\n\t"
        "v_smfmac_f32_32x32x32_bf16 v[32:47], v[196:199], v[104:111], v148\n\t"
        "s_add_u32 s44, s44, 0x4000\n\t"
        "s_and_b32 s44, s44, 0x3ffff\n\t"
        "v_exp_f32 v114, v114\n\t"
        "v_exp_f32 v115, v115\n\t"
        "v_exp_f32 v116, v116\n\t"
        "v_smfmac_f32_16x16x64_bf16 v[136:139], v[144:147], v[104:111], v148\n\t"
        "s_waitcnt vmcnt(8)\n\t"
        "v_exp_f32 v117, v117\n\t"
        "v_exp_f32 v118, v118\n\t"
        "v_mfma_f32_32x32x16_bf16 v[96:111], v[200:203], v[64:67], 0\n\t"
        "s_cmp_eq_u32 s44, s49\n\t"
        "v_exp_f32 v119, v119\n\t"
        "v_exp_f32 v120, v120\n\t"
        "v_exp_f32 v121, v121\n\t"
        "v_mfma_f32_32x32x16_bf16 v[96:111], v[204:207], v[68:71], v[96:111]\n\t"
        "s_cselect_b32 s44, s50, s44\n\t"
        "v_exp_f32 v122, v122\n\t"
        "v_exp_f32 v123, v123\n\t"
        "v_exp_f32 v124, v124\n\t"
        "v_smfmac_f32_32x32x32_bf16 v[48:63], v[192:195], v[112:119], v148\n\t"
        "s_add_u32 s52, s40, s44\n\t"
        "s_addc_u32 s53, s41, 0\n\t"
        "v_exp_f32 v125, v125\n\t"
        "v_exp_f32 v126, v126\n\t"
        "v_exp_f32 v127, v127\n\t"
        "v_smfmac_f32_16x16x64_bf16 v[140:143], v[144:147], v[112:119], v148\n\t"
        "buffer_load_dwordx4 v[184:187], v149, s[52:55], 0 offen offset:0 sc1\n\t"
        "v_exp_f32 v96, v96\n\t"
        "v_exp_f32 v97, v97\n\t"
        "v_smfmac_f32_32x32x32_bf16 v[48:63], v[196:199], v[120:127], v148\n\t"
        "v_exp_f32 v98, v98\n\t"
        "v_exp_f32 v99, v99\n\t"
        "v_exp_f32 v100, v100\n\t"
        "v_smfmac_f32_16x16x64_bf16 v[140:143], v[144:147], v[120:127], v148\n\t"
        "v_exp_f32 v101, v101\n\t"
        "v_exp_f32 v102, v102\n\t"
        "v_mfma_f32_32x32x16_bf16 v[112:127], v[200:203], v[72:75], 0\n\t"
        "v_exp_f32 v103, v103\n\t"
        "v_exp_f32 v104, v104\n\t"
        "v_exp_f32 v105, v105\n\t"
        "v_mfma_f32_32x32x16_bf16 v[112:127], v[204:207], v[76:79], v[112:127]\n\t"
        "v_exp_f32 v106, v106\n\t"
        "v_exp_f32 v107, v107\n\t"
        "v_exp_f32 v108, v108\n\t"
        "v_smfmac_f32_32x32x32_bf16 v[0:15], v[208:211], v[96:103], v148\n\t"
        "v_exp_f32 v109, v109\n\t"
        "v_exp_f32 v110, v110\n\t"
        "v_exp_f32 v111, v111\n\t"
        "v_smfmac_f32_16x16x64_bf16 v[128:131], v[144:147], v[96:103], v148\n\t"
        "buffer_load_dwordx4 v[188:191], v149, s[52:55], 0 offen offset:1024 sc1\n\t"
        "v_exp_f32 v112, v112\n\t"
        "v_exp_f32 v113, v113\n\t"
        "v_smfmac_f32_32x32x32_bf16 v[0:15], v[212:215], v[104:111], v148\n\t"
        "v_exp_f32 v114, v114\n\t"
        "v_exp_f32 v115, v115\n\t"
        "v_exp_f32 v116, v116\n\t"
        "v_smfmac_f32_16x16x64_bf16 v[128:131], v[144:147], v[104:111], v148\n\t"
        "v_exp_f32 v117, v117\n\t"
        "v_exp_f32 v118, v118\n\t"
        "v_mfma_f32_32x32x16_bf16 v[96:111], v[200:203], v[80:83], 0\n\t"
        "v_exp_f32 v119, v119\n\t"
        "v_exp_f32 v120, v120\n\t"
        "v_exp_f32 v121, v121\n\t"
        "v_mfma_f32_32x32x16_bf16 v[96:111], v[204:207], v[84:87], v[96:111]\n\t"
        "v_exp_f32 v122, v122\n\t"
        "v_exp_f32 v123, v123\n\t"
        "v_exp_f32 v124, v124\n\t"
        "v_smfmac_f32_32x32x32_bf16 v[16:31], v[208:211], v[112:119], v148\n\t"
        "v_exp_f32 v125, v125\n\t"
        "v_exp_f32 v126, v126\n\t"
        "v_exp_f32 v127, v127\n\t"
        "v_smfmac_f32_16x16x64_bf16 v[132:135], v[144:147], v[112:119], v148\n\t"
        "buffer_load_dwordx4 v[192:195], v149, s[52:55], 0 offen offset:2048 sc1\n\t"
        "v_exp_f32 v96, v96\n\t"
        "v_exp_f32 v97, v97\n\t"
        "v_smfmac_f32_32x32x32_bf16 v[16:31], v[212:215], v[120:127], v148\n\t"
        "v_exp_f32 v98, v98\n\t"
        "v_exp_f32 v99, v99\n\t"
        "v_exp_f32 v100, v100\n\t"
        "v_smfmac_f32_16x16x64_bf16 v[132:135], v[144:147], v[120:127], v148\n\t"
        "v_exp_f32 v101, v101\n\t"
        "v_exp_f32 v102, v102\n\t"
        "v_mfma_f32_32x32x16_bf16 v[112:127], v[200:203], v[88:91], 0\n\t"
        "v_exp_f32 v103, v103\n\t"
        "v_exp_f32 v104, v104\n\t"
        "v_exp_f32 v105, v105\n\t"
        "v_mfma_f32_32x32x16_bf16 v[112:127], v[204:207], v[92:95], v[112:127]\n\t"
        "v_exp_f32 v106, v106\n\t"
        "v_exp_f32 v107, v107\n\t"
        "v_exp_f32 v108, v108\n\t"
        "v_smfmac_f32_32x32x32_bf16 v[32:47], v[208:211], v[96:103], v148\n\t"
        "v_exp_f32 v109, v109\n\t"
        "v_exp_f32 v110, v110\n\t"
        "v_exp_f32 v111, v111\n\t"
        "v_smfmac_f32_16x16x64_bf16 v[136:139], v[144:147], v[96:103], v148\n\t"
        "buffer_load_dwordx4 v[196:199], v149, s[52:55], 0 offen offset:3072 sc1\n\t"
        "v_exp_f32 v112, v112\n\t"
        "v_exp_f32 v113, v113\n\t"
        "v_smfmac_f32_32x32x32_bf16 v[32:47], v[212:215], v[104:111], v148\n\t"
        "v_exp_f32 v114, v114\n\t"
        "v_exp_f32 v115, v115\n\t"
        "v_exp_f32 v116, v116\n\t"
        "v_smfmac_f32_16x16x64_bf16 v[136:139], v[144:147], v[104:111], v148\n\t"
        "s_waitcnt vmcnt(8)\n\t"
        "v_exp_f32 v117, v117\n\t"
        "v_exp_f32 v118, v118\n\t"
        "v_mfma_f32_32x32x16_bf16 v[96:111], v[152:155], v[64:67], 0\n\t"
        "v_exp_f32 v119, v119\n\t"
        "v_exp_f32 v120, v120\n\t"
        "v_exp_f32 v121, v121\n\t"
        "v_mfma_f32_32x32x16_bf16 v[96:111], v[156:159], v[68:71], v[96:111]\n\t"
        "v_exp_f32 v122, v122\n\t"
        "v_exp_f32 v123, v123\n\t"
        "v_exp_f32 v124, v124\n\t"
        "v_smfmac_f32_32x32x32_bf16 v[48:63], v[208:211], v[112:119], v148\n\t"
        "v_exp_f32 v125, v125\n\t"
        "v_exp_f32 v126, v126\n\t"
        "v_exp_f32 v127, v127\n\t"
        "v_smfmac_f32_16x16x64_bf16 v[140:143], v[144:147], v[112:119], v148\n\t"
        "buffer_load_dwordx4 v[200:203], v150, s[52:55], 0 offen offset:0 sc1\n\t"
        "v_exp_f32 v96, v96\n\t"
        "v_exp_f32 v97, v97\n\t"
        "v_smfmac_f32_32x32x32_bf16 v[48:63], v[212:215], v[120:127], v148\n\t"
        "v_exp_f32 v98, v98\n\t"
        "v_exp_f32 v99, v99\n\t"
        "v_exp_f32 v100, v100\n\t"
        "v_smfmac_f32_16x16x64_bf16 v[140:143], v[144:147], v[120:127], v148\n\t"
        "v_exp_f32 v101, v101\n\t"
        "v_exp_f32 v102, v102\n\t"
        "v_mfma_f32_32x32x16_bf16 v[112:127], v[152:155], v[72:75], 0\n\t"
        "v_exp_f32 v103, v103\n\t"
        "v_exp_f32 v104, v104\n\t"
        "v_exp_f32 v105, v105\n\t"
        "v_mfma_f32_32x32x16_bf16 v[112:127], v[156:159], v[76:79], v[112:127]\n\t"
        "v_exp_f32 v106, v106\n\t"
        "v_exp_f32 v107, v107\n\t"
        "v_exp_f32 v108, v108\n\t"
        "v_smfmac_f32_32x32x32_bf16 v[0:15], v[160:163], v[96:103], v148\n\t"
        "v_exp_f32 v109, v109\n\t"
        "v_exp_f32 v110, v110\n\t"
        "v_exp_f32 v111, v111\n\t"
        "v_smfmac_f32_16x16x64_bf16 v[128:131], v[144:147], v[96:103], v148\n\t"
        "buffer_load_dwordx4 v[204:207], v150, s[52:55], 0 offen offset:1024 sc1\n\t"
        "v_exp_f32 v112, v112\n\t"
        "v_exp_f32 v113, v113\n\t"
        "v_smfmac_f32_32x32x32_bf16 v[0:15], v[164:167], v[104:111], v148\n\t"
        "v_exp_f32 v114, v114\n\t"
        "v_exp_f32 v115, v115\n\t"
        "v_exp_f32 v116, v116\n\t"
        "v_smfmac_f32_16x16x64_bf16 v[128:131], v[144:147], v[104:111], v148\n\t"
        "v_exp_f32 v117, v117\n\t"
        "v_exp_f32 v118, v118\n\t"
        "v_mfma_f32_32x32x16_bf16 v[96:111], v[152:155], v[80:83], 0\n\t"
        "v_exp_f32 v119, v119\n\t"
        "v_exp_f32 v120, v120\n\t"
        "v_exp_f32 v121, v121\n\t"
        "v_mfma_f32_32x32x16_bf16 v[96:111], v[156:159], v[84:87], v[96:111]\n\t"
        "v_exp_f32 v122, v122\n\t"
        "v_exp_f32 v123, v123\n\t"
        "v_exp_f32 v124, v124\n\t"
        "v_smfmac_f32_32x32x32_bf16 v[16:31], v[160:163], v[112:119], v148\n\t"
        "v_exp_f32 v125, v125\n\t"
        "v_exp_f32 v126, v126\n\t"
        "v_exp_f32 v127, v127\n\t"
        "v_smfmac_f32_16x16x64_bf16 v[132:135], v[144:147], v[112:119], v148\n\t"
        "buffer_load_dwordx4 v[208:211], v150, s[52:55], 0 offen offset:2048 sc1\n\t"
        "v_exp_f32 v96, v96\n\t"
        "v_exp_f32 v97, v97\n\t"
        "v_smfmac_f32_32x32x32_bf16 v[16:31], v[164:167], v[120:127], v148\n\t"
        "v_exp_f32 v98, v98\n\t"
        "v_exp_f32 v99, v99\n\t"
        "v_exp_f32 v100, v100\n\t"
        "v_smfmac_f32_16x16x64_bf16 v[132:135], v[144:147], v[120:127], v148\n\t"
        "v_exp_f32 v101, v101\n\t"
        "v_exp_f32 v102, v102\n\t"
        "v_mfma_f32_32x32x16_bf16 v[112:127], v[152:155], v[88:91], 0\n\t"
        "v_exp_f32 v103, v103\n\t"
        "v_exp_f32 v104, v104\n\t"
        "v_exp_f32 v105, v105\n\t"
        "v_mfma_f32_32x32x16_bf16 v[112:127], v[156:159], v[92:95], v[112:127]\n\t"
        "v_exp_f32 v106, v106\n\t"
        "v_exp_f32 v107, v107\n\t"
        "v_exp_f32 v108, v108\n\t"
        "v_smfmac_f32_32x32x32_bf16 v[32:47], v[160:163], v[96:103], v148\n\t"
        "v_exp_f32 v109, v109\n\t"
        "v_exp_f32 v110, v110\n\t"
        "v_exp_f32 v111, v111\n\t"
        "v_smfmac_f32_16x16x64_bf16 v[136:139], v[144:147], v[96:103], v148\n\t"
        "buffer_load_dwordx4 v[212:215], v150, s[52:55], 0 offen offset:3072 sc1\n\t"
        "v_exp_f32 v112, v112\n\t"
        "v_exp_f32 v113, v113\n\t"
        "v_smfmac_f32_32x32x32_bf16 v[32:47], v[164:167], v[104:111], v148\n\t"
        "s_add_u32 s44, s44, 0x4000\n\t"
        "s_and_b32 s44, s44, 0x3ffff\n\t"
        "v_exp_f32 v114, v114\n\t"
        "v_exp_f32 v115, v115\n\t"
        "v_exp_f32 v116, v116\n\t"
        "v_smfmac_f32_16x16x64_bf16 v[136:139], v[144:147], v[104:111], v148\n\t"
        "s_waitcnt vmcnt(8)\n\t"
        "v_exp_f32 v117, v117\n\t"
        "v_exp_f32 v118, v118\n\t"
        "v_mfma_f32_32x32x16_bf16 v[96:111], v[168:171], v[64:67], 0\n\t"
        "s_cmp_eq_u32 s44, s49\n\t"
        "v_exp_f32 v119, v119\n\t"
        "v_exp_f32 v120, v120\n\t"
        "v_exp_f32 v121, v121\n\t"
        "v_mfma_f32_32x32x16_bf16 v[96:111], v[172:175], v[68:71], v[96:111]\n\t"
        "s_cselect_b32 s44, s50, s44\n\t"
        "v_exp_f32 v122, v122\n\t"
        "v_exp_f32 v123, v123\n\t"
        "v_exp_f32 v124, v124\n\t"
        "v_smfmac_f32_32x32x32_bf16 v[48:63], v[160:163], v[112:119], v148\n\t"
        "s_add_u32 s52, s40, s44\n\t"
        "s_addc_u32 s53, s41, 0\n\t"
        "v_exp_f32 v125, v125\n\t"
        "v_exp_f32 v126, v126\n\t"
        "v_exp_f32 v127, v127\n\t"
        "v_smfmac_f32_16x16x64_bf16 v[140:143], v[144:147], v[112:119], v148\n\t"
        "buffer_load_dwordx4 v[152:155], v149, s[52:55], 0 offen offset:0 sc1\n\t"
        "v_exp_f32 v96, v96\n\t"
        "v_exp_f32 v97, v97\n\t"
        "v_smfmac_f32_32x32x32_bf16 v[48:63], v[164:167], v[120:127], v148\n\t"
        "v_exp_f32 v98, v98\n\t"
        "v_exp_f32 v99, v99\n\t"
        "v_exp_f32 v100, v100\n\t"
        "v_smfmac_f32_16x16x64_bf16 v[140:143], v[144:147], v[120:127], v148\n\t"
        "v_exp_f32 v101, v101\n\t"
        "v_exp_f32 v102, v102\n\t"
        "v_mfma_f32_32x32x16_bf16 v[112:127], v[168:171], v[72:75], 0\n\t"
        "v_exp_f32 v103, v103\n\t"
        "v_exp_f32 v104, v104\n\t"
        "v_exp_f32 v105, v105\n\t"
        "v_mfma_f32_32x32x16_bf16 v[112:127], v[172:175], v[76:79], v[112:127]\n\t"
        "v_exp_f32 v106, v106\n\t"
        "v_exp_f32 v107, v107\n\t"
        "v_exp_f32 v108, v108\n\t"
        "v_smfmac_f32_32x32x32_bf16 v[0:15], v[176:179], v[96:103], v148\n\t"
        "v_exp_f32 v109, v109\n\t"
        "v_exp_f32 v110, v110\n\t"
        "v_exp_f32 v111, v111\n\t"
        "v_smfmac_f32_16x16x64_bf16 v[128:131], v[144:147], v[96:103], v148\n\t"
        "buffer_load_dwordx4 v[156:159], v149, s[52:55], 0 offen offset:1024 sc1\n\t"
        "v_exp_f32 v112, v112\n\t"
        "v_exp_f32 v113, v113\n\t"
        "v_smfmac_f32_32x32x32_bf16 v[0:15], v[180:183], v[104:111], v148\n\t"
        "v_exp_f32 v114, v114\n\t"
        "v_exp_f32 v115, v115\n\t"
        "v_exp_f32 v116, v116\n\t"
        "v_smfmac_f32_16x16x64_bf16 v[128:131], v[144:147], v[104:111], v148\n\t"
        "v_exp_f32 v117, v117\n\t"
        "v_exp_f32 v118, v118\n\t"
        "v_mfma_f32_32x32x16_bf16 v[96:111], v[168:171], v[80:83], 0\n\t"
        "v_exp_f32 v119, v119\n\t"
        "v_exp_f32 v120, v120\n\t"
        "v_exp_f32 v121, v121\n\t"
        "v_mfma_f32_32x32x16_bf16 v[96:111], v[172:175], v[84:87], v[96:111]\n\t"
        "v_exp_f32 v122, v122\n\t"
        "v_exp_f32 v123, v123\n\t"
        "v_exp_f32 v124, v124\n\t"
        "v_smfmac_f32_32x32x32_bf16 v[16:31], v[176:179], v[112:119], v148\n\t"
        "v_exp_f32 v125, v125\n\t"
        "v_exp_f32 v126, v126\n\t"
        "v_exp_f32 v127, v127\n\t"
        "v_smfmac_f32_16x16x64_bf16 v[132:135], v[144:147], v[112:119], v148\n\t"
        "buffer_load_dwordx4 v[160:163], v149, s[52:55], 0 offen offset:2048 sc1\n\t"
        "v_exp_f32 v96, v96\n\t"
        "v_exp_f32 v97, v97\n\t"
        "v_smfmac_f32_32x32x32_bf16 v[16:31], v[180:183], v[120:127], v148\n\t"
        "v_exp_f32 v98, v98\n\t"
        "v_exp_f32 v99, v99\n\t"
        "v_exp_f32 v100, v100\n\t"
        "v_smfmac_f32_16x16x64_bf16 v[132:135], v[144:147], v[120:127], v148\n\t"
        "v_exp_f32 v101, v101\n\t"
        "v_exp_f32 v102, v102\n\t"
        "v_mfma_f32_32x32x16_bf16 v[112:127], v[168:171], v[88:91], 0\n\t"
        "v_exp_f32 v103, v103\n\t"
        "v_exp_f32 v104, v104\n\t"
        "v_exp_f32 v105, v105\n\t"
        "v_mfma_f32_32x32x16_bf16 v[112:127], v[172:175], v[92:95], v[112:127]\n\t"
        "v_exp_f32 v106, v106\n\t"
        "v_exp_f32 v107, v107\n\t"
        "v_exp_f32 v108, v108\n\t"
        "v_smfmac_f32_32x32x32_bf16 v[32:47], v[176:179], v[96:103], v148\n\t"
        "s_sub_u32 s45, s45, 1\n\t"
        "v_exp_f32 v109, v109\n\t"
        "v_exp_f32 v110, v110\n\t"
        "v_exp_f32 v111, v111\n\t"
        "v_smfmac_f32_16x16x64_bf16 v[136:139], v[144:147], v[96:103], v148\n\t"
        "buffer_load_dwordx4 v[164:167], v149, s[52:55], 0 offen offset:3072 sc1\n\t"
        "s_cbranch_scc0 1b\n\t"
        "v_exp_f32 v112, v112\n\t"
        "v_exp_f32 v113, v113\n\t"
        "v_smfmac_f32_32x32x32_bf16 v[32:47], v[180:183], v[104:111], v148\n\t"
        "v_exp_f32 v114, v114\n\t"
        "v_exp_f32 v115, v115\n\t"
        "v_exp_f32 v116, v116\n\t"
        "v_smfmac_f32_16x16x64_bf16 v[136:139], v[144:147], v[104:111], v148\n\t"
        "s_waitcnt vmcnt(8)\n\t"
        "v_exp_f32 v117, v117\n\t"
        "v_exp_f32 v118, v118\n\t"
        "v_mfma_f32_32x32x16_bf16 v[96:111], v[184:187], v[64:67], 0\n\t"
        "v_exp_f32 v119, v119\n\t"
        "v_exp_f32 v120, v120\n\t"
        "v_exp_f32 v121, v121\n\t"
        "v_mfma_f32_32x32x16_bf16 v[96:111], v[188:191], v[68:71], v[96:111]\n\t"
        "v_exp_f32 v122, v122\n\t"
        "v_exp_f32 v123, v123\n\t"
        "v_exp_f32 v124, v124\n\t"
        "v_smfmac_f32_32x32x32_bf16 v[48:63], v[176:179], v[112:119], v148\n\t"
        "v_exp_f32 v125, v125\n\t"
        "v_exp_f32 v126, v126\n\t"
        "v_exp_f32 v127, v127\n\t"
        "v_smfmac_f32_16x16x64_bf16 v[140:143], v[144:147], v[112:119], v148\n\t"
        "v_exp_f32 v96, v96\n\t"
        "v_exp_f32 v97, v97\n\t"
        "v_smfmac_f32_32x32x32_bf16 v[48:63], v[180:183], v[120:127], v148\n\t"
        "v_exp_f32 v98, v98\n\t"
        "v_exp_f32 v99, v99\n\t"
        "v_exp_f32 v100, v100\n\t"
        "v_smfmac_f32_16x16x64_bf16 v[140:143], v[144:147], v[120:127], v148\n\t"
        "v_exp_f32 v101, v101\n\t"
        "v_exp_f32 v102, v102\n\t"
        "v_mfma_f32_32x32x16_bf16 v[112:127], v[184:187], v[72:75], 0\n\t"
        "v_exp_f32 v103, v103\n\t"
        "v_exp_f32 v104, v104\n\t"
        "v_exp_f32 v105, v105\n\t"
        "v_mfma_f32_32x32x16_bf16 v[112:127], v[188:191], v[76:79], v[112:127]\n\t"
        "v_exp_f32 v106, v106\n\t"
        "v_exp_f32 v107, v107\n\t"
        "v_exp_f32 v108, v108\n\t"
        "v_smfmac_f32_32x32x32_bf16 v[0:15], v[192:195], v[96:103], v148\n\t"
        "v_exp_f32 v109, v109\n\t"
        "v_exp_f32 v110, v110\n\t"
        "v_exp_f32 v111, v111\n\t"
        "v_smfmac_f32_16x16x64_bf16 v[128:131], v[144:147], v[96:103], v148\n\t"
        "v_exp_f32 v112, v112\n\t"
        "v_exp_f32 v113, v113\n\t"
        "v_smfmac_f32_32x32x32_bf16 v[0:15], v[196:199], v[104:111], v148\n\t"
        "v_exp_f32 v114, v114\n\t"
        "v_exp_f32 v115, v115\n\t"
        "v_exp_f32 v116, v116\n\t"
        "v_smfmac_f32_16x16x64_bf16 v[128:131], v[144:147], v[104:111], v148\n\t"
        "s_waitcnt vmcnt(4)\n\t"
        "v_exp_f32 v117, v117\n\t"
        "v_exp_f32 v118, v118\n\t"
        "v_mfma_f32_32x32x16_bf16 v[96:111], v[200:203], v[64:67], 0\n\t"
        "v_exp_f32 v119, v119\n\t"
        "v_exp_f32 v120, v120\n\t"
        "v_exp_f32 v121, v121\n\t"
        "v_mfma_f32_32x32x16_bf16 v[96:111], v[204:207], v[68:71], v[96:111]\n\t"
        "v_exp_f32 v122, v122\n\t"
        "v_exp_f32 v123, v123\n\t"
        "v_exp_f32 v124, v124\n\t"
        "v_smfmac_f32_32x32x32_bf16 v[16:31], v[192:195], v[112:119], v148\n\t"
        "v_exp_f32 v125, v125\n\t"
        "v_exp_f32 v126, v126\n\t"
        "v_exp_f32 v127, v127\n\t"
        "v_smfmac_f32_16x16x64_bf16 v[132:135], v[144:147], v[112:119], v148\n\t"
        "s_cmp_lg_u32 s47, 0\n\t"
        "s_cbranch_scc1 4f\n\t"
        "global_load_dwordx4 %[r0], v223, %[xb] offset:0\n\t"
        "global_load_dwordx4 %[r1], v223, %[xb] offset:32\n\t"
        "global_load_dwordx4 %[r2], v223, %[xb] offset:64\n\t"
        "global_load_dwordx4 %[r3], v223, %[xb] offset:96\n\t"
        "4:\n\t"
        "v_exp_f32 v96, v96\n\t"
        "v_exp_f32 v97, v97\n\t"
        "v_smfmac_f32_32x32x32_bf16 v[16:31], v[196:199], v[120:127], v148\n\t"
        "v_exp_f32 v98, v98\n\t"
        "v_exp_f32 v99, v99\n\t"
        "v_exp_f32 v100, v100\n\t"
        "v_smfmac_f32_16x16x64_bf16 v[132:135], v[144:147], v[120:127], v148\n\t"
        "v_exp_f32 v101, v101\n\t"
        "v_exp_f32 v102, v102\n\t"
        "v_mfma_f32_32x32x16_bf16 v[112:127], v[200:203], v[72:75], 0\n\t"
        "v_exp_f32 v103, v103\n\t"
        "v_exp_f32 v104, v104\n\t"
        "v_exp_f32 v105, v105\n\t"
        "v_mfma_f32_32x32x16_bf16 v[112:127], v[204:207], v[76:79], v[112:127]\n\t"
        "v_exp_f32 v106, v106\n\t"
        "v_exp_f32 v107, v107\n\t"
        "v_exp_f32 v108, v108\n\t"
        "v_smfmac_f32_32x32x32_bf16 v[0:15], v[208:211], v[96:103], v148\n\t"
        "v_exp_f32 v109, v109\n\t"
        "v_exp_f32 v110, v110\n\t"
        "v_exp_f32 v111, v111\n\t"
        "v_smfmac_f32_16x16x64_bf16 v[128:131], v[144:147], v[96:103], v148\n\t"
        "v_exp_f32 v112, v112\n\t"
        "v_exp_f32 v113, v113\n\t"
        "v_smfmac_f32_32x32x32_bf16 v[0:15], v[212:215], v[104:111], v148\n\t"
        "v_exp_f32 v114, v114\n\t"
        "v_exp_f32 v115, v115\n\t"
        "v_exp_f32 v116, v116\n\t"
        "v_smfmac_f32_16x16x64_bf16 v[128:131], v[144:147], v[104:111], v148\n\t"
        "v_exp_f32 v117, v117\n\t"
        "v_exp_f32 v118, v118\n\t"
        "v_mfma_f32_32x32x16_bf16 v[96:111], v[184:187], v[80:83], 0\n\t"
        "v_exp_f32 v119, v119\n\t"
        "v_exp_f32 v120, v120\n\t"
        "v_exp_f32 v121, v121\n\t"
        "v_mfma_f32_32x32x16_bf16 v[96:111], v[188:191], v[84:87], v[96:111]\n\t"
        "ds_write_b128 v216, v[0:3] offset:0\n\t"
        "ds_write_b128 v216, v[4:7] offset:1024\n\t"
        "v_exp_f32 v122, v122\n\t"
        "v_exp_f32 v123, v123\n\t"
        "v_exp_f32 v124, v124\n\t"
        "v_smfmac_f32_32x32x32_bf16 v[16:31], v[208:211], v[112:119], v148\n\t"
        "ds_write_b128 v216, v[8:11] offset:2048\n\t"
        "ds_write_b128 v216, v[12:15] offset:3072\n\t"
        "v_exp_f32 v125, v125\n\t"
        "v_exp_f32 v126, v126\n\t"
        "v_exp_f32 v127, v127\n\t"
        "v_smfmac_f32_16x16x64_bf16 v[132:135], v[144:147], v[112:119], v148\n\t"
        "ds_write_b32 v218, v128 offset:0\n\t"
        "v_exp_f32 v96, v96\n\t"
        "v_exp_f32 v97, v97\n\t"
        "v_smfmac_f32_32x32x32_bf16 v[16:31], v[212:215], v[120:127], v148\n\t"
        "v_exp_f32 v98, v98\n\t"
        "v_exp_f32 v99, v99\n\t"
        "v_exp_f32 v100, v100\n\t"
        "v_smfmac_f32_16x16x64_bf16 v[132:135], v[144:147], v[120:127], v148\n\t"
        "v_exp_f32 v101, v101\n\t"
        "v_exp_f32 v102, v102\n\t"
        "v_mfma_f32_32x32x16_bf16 v[112:127], v[184:187], v[88:91], 0\n\t"
        "v_exp_f32 v103, v103\n\t"
        "v_exp_f32 v104, v104\n\t"
        "v_exp_f32 v105, v105\n\t"
        "v_mfma_f32_32x32x16_bf16 v[112:127], v[188:191], v[92:95], v[112:127]\n\t"
        "ds_write_b128 v216, v[16:19] offset:4096\n\t"
        "ds_write_b128 v216, v[20:23] offset:5120\n\t"
        "v_exp_f32 v106, v106\n\t"
        "v_exp_f32 v107, v107\n\t"
        "v_exp_f32 v108, v108\n\t"
        "v_smfmac_f32_32x32x32_bf16 v[32:47], v[192:195], v[96:103], v148\n\t"
        "ds_write_b128 v216, v[24:27] offset:6144\n\t"
        "ds_write_b128 v216, v[28:31] offset:7168\n\t"
        "v_exp_f32 v109, v109\n\t"
        "v_exp_f32 v110, v110\n\t"
        "v_exp_f32 v111, v111\n\t"
        "v_smfmac_f32_16x16x64_bf16 v[136:139], v[144:147], v[96:103], v148\n\t"
        "ds_write_b32 v218, v132 offset:256\n\t"
        "ds_write_b32 v220, v222\n\t"
        "v_exp_f32 v112, v112\n\t"
        "v_exp_f32 v113, v113\n\t"
        "v_smfmac_f32_32x32x32_bf16 v[32:47], v[196:199], v[104:111], v148\n\t"
        "v_exp_f32 v114, v114\n\t"
        "v_exp_f32 v115, v115\n\t"
        "v_exp_f32 v116, v116\n\t"
        "v_smfmac_f32_16x16x64_bf16 v[136:139], v[144:147], v[104:111], v148\n\t"
        "v_exp_f32 v117, v117\n\t"
        "v_exp_f32 v118, v118\n\t"
        "v_mfma_f32_32x32x16_bf16 v[96:111], v[200:203], v[80:83], 0\n\t"
        "v_exp_f32 v119, v119\n\t"
        "v_exp_f32 v120, v120\n\t"
        "v_exp_f32 v121, v121\n\t"
        "v_mfma_f32_32x32x16_bf16 v[96:111], v[204:207], v[84:87], v[96:111]\n\t"
        "v_exp_f32 v122, v122\n\t"
        "v_exp_f32 v123, v123\n\t"
        "v_exp_f32 v124, v124\n\t"
        "v_smfmac_f32_32x32x32_bf16 v[48:63], v[192:195], v[112:119], v148\n\t"
        "v_exp_f32 v125, v125\n\t"
        "v_exp_f32 v126, v126\n\t"
        "v_exp_f32 v127, v127\n\t"
        "v_smfmac_f32_16x16x64_bf16 v[140:143], v[144:147], v[112:119], v148\n\t"
        "v_exp_f32 v96, v96\n\t"
        "v_exp_f32 v97, v97\n\t"
        "v_smfmac_f32_32x32x32_bf16 v[48:63], v[196:199], v[120:127], v148\n\t"
        "v_exp_f32 v98, v98\n\t"
        "v_exp_f32 v99, v99\n\t"
        "v_exp_f32 v100, v100\n\t"
        "v_smfmac_f32_16x16x64_bf16 v[140:143], v[144:147], v[120:127], v148\n\t"
        "v_exp_f32 v101, v101\n\t"
        "v_exp_f32 v102, v102\n\t"
        "v_mfma_f32_32x32x16_bf16 v[112:127], v[200:203], v[88:91], 0\n\t"
        "v_exp_f32 v103, v103\n\t"
        "v_exp_f32 v104, v104\n\t"
        "v_exp_f32 v105, v105\n\t"
        "v_mfma_f32_32x32x16_bf16 v[112:127], v[204:207], v[92:95], v[112:127]\n\t"
        "ds_read_b32 v151, v221\n\t"
        "v_exp_f32 v106, v106\n\t"
        "v_exp_f32 v107, v107\n\t"
        "v_exp_f32 v108, v108\n\t"
        "v_smfmac_f32_32x32x32_bf16 v[32:47], v[208:211], v[96:103], v148\n\t"
        "v_exp_f32 v109, v109\n\t"
        "v_exp_f32 v110, v110\n\t"
        "v_exp_f32 v111, v111\n\t"
        "v_smfmac_f32_16x16x64_bf16 v[136:139], v[144:147], v[96:103], v148\n\t"
        "s_waitcnt lgkmcnt(0)\n\t"
        "v_cmp_ne_u32 vcc, v222, v151\n\t"
        "s_cbranch_vccnz 40f\n\t"
        "41:\n\t"
        "v_exp_f32 v112, v112\n\t"
        "v_exp_f32 v113, v113\n\t"
        "v_smfmac_f32_32x32x32_bf16 v[32:47], v[212:215], v[104:111], v148\n\t"
        "ds_read_b128 v[0:3], v217 offset:0\n\t"
        "ds_read_b128 v[4:7], v217 offset:1024\n\t"
        "ds_read_b128 v[8:11], v217 offset:2048\n\t"
        "ds_read_b128 v[12:15], v217 offset:3072\n\t"
        "v_exp_f32 v114, v114\n\t"
        "v_exp_f32 v115, v115\n\t"
        "v_exp_f32 v116, v116\n\t"
        "v_smfmac_f32_16x16x64_bf16 v[136:139], v[144:147], v[104:111], v148\n\t"
        "ds_read_b32 v128, v219 offset:0\n\t"
        "ds_read_b128 v[16:19], v217 offset:4096\n\t"
        "ds_read_b128 v[20:23], v217 offset:5120\n\t"
        "v_exp_f32 v117, v117\n\t"
        "v_exp_f32 v118, v118\n\t"
        "v_exp_f32 v119, v119\n\t"
        "v_exp_f32 v120, v120\n\t"
        "v_exp_f32 v121, v121\n\t"
        "v_exp_f32 v122, v122\n\t"
        "v_exp_f32 v123, v123\n\t"
        "v_exp_f32 v124, v124\n\t"
        "v_smfmac_f32_32x32x32_bf16 v[48:63], v[208:211], v[112:119], v148\n\t"
        "ds_read_b128 v[24:27], v217 offset:6144\n\t"
        "ds_read_b128 v[28:31], v217 offset:7168\n\t"
        "ds_read_b32 v132, v219 offset:256\n\t"
        "v_exp_f32 v125, v125\n\t"
        "v_exp_f32 v126, v126\n\t"
        "v_exp_f32 v127, v127\n\t"
        "v_smfmac_f32_16x16x64_bf16 v[140:143], v[144:147], v[112:119], v148\n\t"
        "s_nop 1\n\t"
        "v_smfmac_f32_32x32x32_bf16 v[48:63], v[212:215], v[120:127], v148\n\t"
        "v_smfmac_f32_16x16x64_bf16 v[140:143], v[144:147], v[120:127], v148\n\t"
        "s_waitcnt vmcnt(0) lgkmcnt(0)\n\t"
        "s_nop 6\n\t"
        "s_branch 9f\n\t"
        "24:\n\t"
        "s_add_u32 s44, s44, 0x4000\n\t"
        "s_and_b32 s44, s44, 0x3ffff\n\t"
        "s_cmp_eq_u32 s44, s49\n\t"
        "s_cselect_b32 s44, s50, s44\n\t"
        "s_add_u32 s52, s40, s44\n\t"
        "s_addc_u32 s53, s41, 0\n\t"
        "buffer_load_dwordx4 v[184:187], v149, s[52:55], 0 offen offset:0 sc1\n\t"
        "buffer_load_dwordx4 v[188:191], v149, s[52:55], 0 offen offset:1024 sc1\n\t"
        "buffer_load_dwordx4 v[192:195], v149, s[52:55], 0 offen offset:2048 sc1\n\t"
        "buffer_load_dwordx4 v[196:199], v149, s[52:55], 0 offen offset:3072 sc1\n\t"
        "buffer_load_dwordx4 v[200:203], v150, s[52:55], 0 offen offset:0 sc1\n\t"
        "buffer_load_dwordx4 v[204:207], v150, s[52:55], 0 offen offset:1024 sc1\n\t"
        "buffer_load_dwordx4 v[208:211], v150, s[52:55], 0 offen offset:2048 sc1\n\t"
        "buffer_load_dwordx4 v[212:215], v150, s[52:55], 0 offen offset:3072 sc1\n\t"
        "s_mov_b32 s46, 1\n\t"
        "s_branch 25b\n\t"
        "26:\n\t"
        "s_add_u32 s44, s44, 0x4000\n\t"
        "s_and_b32 s44, s44, 0x3ffff\n\t"
        "s_cmp_eq_u32 s44, s49\n\t"
        "s_cselect_b32 s44, s50, s44\n\t"
        "s_add_u32 s52, s40, s44\n\t"
        "s_addc_u32 s53, s41, 0\n\t"
        "buffer_load_dwordx4 v[184:187], v149, s[52:55], 0 offen offset:0 sc1\n\t"
        "buffer_load_dwordx4 v[188:191], v149, s[52:55], 0 offen offset:1024 sc1\n\t"
        "buffer_load_dwordx4 v[192:195], v149, s[52:55], 0 offen offset:2048 sc1\n\t"
        "buffer_load_dwordx4 v[196:199], v149, s[52:55], 0 offen offset:3072 sc1\n\t"
        "buffer_load_dwordx4 v[200:203], v150, s[52:55], 0 offen offset:0 sc1\n\t"
        "buffer_load_dwordx4 v[204:207], v150, s[52:55], 0 offen offset:1024 sc1\n\t"
        "buffer_load_dwordx4 v[208:211], v150, s[52:55], 0 offen offset:2048 sc1\n\t"
        "buffer_load_dwordx4 v[212:215], v150, s[52:55], 0 offen offset:3072 sc1\n\t"
        "s_mov_b32 s46, 1\n\t"
        "s_branch 27b\n\t"
        "28:\n\t"
        "s_add_u32 s44, s44, 0x4000\n\t"
        "s_and_b32 s44, s44, 0x3ffff\n\t"
        "s_cmp_eq_u32 s44, s49\n\t"
        "s_cselect_b32 s44, s50, s44\n\t"
        "s_add_u32 s52, s40, s44\n\t"
        "s_addc_u32 s53, s41, 0\n\t"
        "buffer_load_dwordx4 v[184:187], v149, s[52:55], 0 offen offset:0 sc1\n\t"
        "buffer_load_dwordx4 v[188:191], v149, s[52:55], 0 offen offset:1024 sc1\n\t"
        "buffer_load_dwordx4 v[192:195], v149, s[52:55], 0 offen offset:2048 sc1\n\t"
        "buffer_load_dwordx4 v[196:199], v149, s[52:55], 0 offen offset:3072 sc1\n\t"
        "buffer_load_dwordx4 v[200:203], v150, s[52:55], 0 offen offset:0 sc1\n\t"
        "buffer_load_dwordx4 v[204:207], v150, s[52:55], 0 offen offset:1024 sc1\n\t"
        "buffer_load_dwordx4 v[208:211], v150, s[52:55], 0 offen offset:2048 sc1\n\t"
        "buffer_load_dwordx4 v[212:215], v150, s[52:55], 0 offen offset:3072 sc1\n\t"
        "s_mov_b32 s46, 1\n\t"
        "s_branch 29b\n\t"
        "30:\n\t"
        "s_add_u32 s44, s44, 0x4000\n\t"
        "s_and_b32 s44, s44, 0x3ffff\n\t"
        "s_cmp_eq_u32 s44, s49\n\t"
        "s_cselect_b32 s44, s50, s44\n\t"
        "s_add_u32 s52, s40, s44\n\t"
        "s_addc_u32 s53, s41, 0\n\t"
        "buffer_load_dwordx4 v[184:187], v149, s[52:55], 0 offen offset:0 sc1\n\t"
        "buffer_load_dwordx4 v[188:191], v149, s[52:55], 0 offen offset:1024 sc1\n\t"
        "buffer_load_dwordx4 v[192:195], v149, s[52:55], 0 offen offset:2048 sc1\n\t"
        "buffer_load_dwordx4 v[196:199], v149, s[52:55], 0 offen offset:3072 sc1\n\t"
        "buffer_load_dwordx4 v[200:203], v150, s[52:55], 0 offen offset:0 sc1\n\t"
        "buffer_load_dwordx4 v[204:207], v150, s[52:55], 0 offen offset:1024 sc1\n\t"
        "buffer_load_dwordx4 v[208:211], v150, s[52:55], 0 offen offset:2048 sc1\n\t"
        "buffer_load_dwordx4 v[212:215], v150, s[52:55], 0 offen offset:3072 sc1\n\t"
        "s_mov_b32 s46, 1\n\t"
        "s_branch 31b\n\t"
        "32:\n\t"
        "s_add_u32 s44, s44, 0x4000\n\t"
        "s_and_b32 s44, s44, 0x3ffff\n\t"
        "s_cmp_eq_u32 s44, s49\n\t"
        "s_cselect_b32 s44, s50, s44\n\t"
        "s_add_u32 s52, s40, s44\n\t"
        "s_addc_u32 s53, s41, 0\n\t"
        "buffer_load_dwordx4 v[184:187], v149, s[52:55], 0 offen offset:0 sc1\n\t"
        "buffer_load_dwordx4 v[188:191], v149, s[52:55], 0 offen offset:1024 sc1\n\t"
        "buffer_load_dwordx4 v[192:195], v149, s[52:55], 0 offen offset:2048 sc1\n\t"
        "buffer_load_dwordx4 v[196:199], v149, s[52:55], 0 offen offset:3072 sc1\n\t"
        "buffer_load_dwordx4 v[200:203], v150, s[52:55], 0 offen offset:0 sc1\n\t"
        "buffer_load_dwordx4 v[204:207], v150, s[52:55], 0 offen offset:1024 sc1\n\t"
        "buffer_load_dwordx4 v[208:211], v150, s[52:55], 0 offen offset:2048 sc1\n\t"
        "buffer_load_dwordx4 v[212:215], v150, s[52:55], 0 offen offset:3072 sc1\n\t"
        "s_mov_b32 s46, 1\n\t"
        "s_branch 33b\n\t"
        "40:\n\t"
        "s_mov_b32 s45, 0x100000\n\t"
        "42:\n\t"
        "ds_read_b32 v151, v221\n\t"
        "s_sub_u32 s45, s45, 1\n\t"
        "s_waitcnt lgkmcnt(0)\n\t"
        "s_cbranch_scc1 41b\n\t"
        "v_cmp_ne_u32 vcc, v222, v151\n\t"
        "s_cbranch_vccnz 42b\n\t"
        "s_branch 41b\n\t"
        "9:\n\t"
        : "+{v[0:15]}"(o0), "+{v[16:31]}"(o1), "+{v[32:47]}"(o2), "+{v[48:63]}"(o3), "+{v[128:143]}"(lv), "+{s44}"(soff), "+{v[152:167]}"(kva), "+{v[168:183]}"(kvb), "+{v[184:191]}"(fv),
          [r0] "+v"(x1p[0]), [r1] "+v"(x1p[1]), [r2] "+v"(x1p[2]), [r3] "+v"(x1p[3]) STAMP_OUTS
        : "{v[64:71]}"(q[0]), "{v[72:79]}"(q[1]), "{v[80:87]}"(q[2]), "{v[88:95]}"(q[3]), "{v[144:147]}"(ones), "{v148}"(0xddddu), "{v149}"(kvoff), "{v150}"(kvoff + 4096),
          "{v[216:223]}"(cv), "{v224}"(lbase + L_W + 16), "{s[40:43]}"(srd), "{s47}"(L), "{s49}"(tm * 16384), "{s50}"(((tm + 1) & 15) * 16384), [xb] "s"(x0)
        : "v96", "v97", "v98", "v99", "v100", "v101", "v102", "v103", "v104", "v105", "v106", "v107", "v108", "v109", "v110", "v111", "v112", "v113", "v114", "v115", "v116", "v117", "v118", "v119", "v120", "v121", "v122", "v123", "v124", "v125", "v126", "v127", "v151", "v192", "v193", "v194", "v195", "v196", "v197", "v198", "v199", "v200", "v201", "v202", "v203", "v204", "v205", "v206", "v207", "v208", "v209", "v210", "v211", "v212", "v213", "v214", "v215", "s45", "s46", "s52", "s53", "s54", "s55", "vcc", "scc", "memory");
      STAMP_PUT(6 + 16 * L);
      STAMPV(9 + 16 * L);
      PHASE_VARS;
      const unsigned dst = OFF_OF + L * (5 * MBY) + (tm * 4 + 2 * kh) * TS + (tn * 2 + hh) * 2048 + lane * 16;
      auto put = [&](int u, const f32x16& o) {
#pragma unroll
        for (int c = 0; c < 2; ++c) {
          u32x4 w;
#pragma unroll
          for (int e = 0; e < 4; ++e) w[e] = pack2(o[8 * c + 2 * e], o[8 * c + 2 * e + 1]);
          stg(rs, dst + u * TS + c * 1024, w);
        }
      };
      const float la = lv[8] + lv[0], lb = lv[12] + lv[4];
      o2 = (o2 + o0) * RCP(la); put(0, o2);
      SBAR();
      o3 = (o3 + o1) * RCP(lb); put(1, o3);
      SBAR();
      {
        const f32x16 t = o2 + o3;
        float sum = 0.f;
#pragma unroll
        for (int e = 0; e < 16; e += 4) sum += (t[e] + t[e + 1]) + (t[e + 2] + t[e + 3]);
        if (__any(!(fabsf(sum) < 1e37f) | !(fmaxf(la, lb) < 1e27f) | !(fminf(la, lb) > 1e-9f) | !(la + lb == la + lb))) {
          DRAIN();
          attn_safe(rs, kvoff - kh * 8192, q[2], ones, o2); put(0, o2);
          attn_safe(rs, kvoff - kh * 8192, q[3], ones, o3); put(1, o3);
        }
      }
    }
    STAMPV(10 + 16 * L);
    {
      f32x16 acc[8];
      u32x16 wrc;
      u32x16 fl0, fl1, fl2;
      STAMP_DECL;
      { PHASE_VARS;
      f32x2 r0 = {0.f, 0.f}, r1 = {1.f, 1.f};
      if (L == 0) {
#pragma unroll
        for (int i = 0; i < 4; ++i) x1p[i] = fo_swap(x1p[i]);
      } else {
        const float* p = reinterpret_cast<const float*>(smem + L_AB) + 256 + wid * 64 + (lane & 15);
        r0 = f32x2{p[0], p[16]}; r1 = f32x2{p[32], p[48]};
      }
      row_swap(x1p[0], x1p[1]); row_swap(x1p[2], x1p[3]);
#pragma unroll
      for (int e = 0; e < 16; ++e) {
        acc[e >> 3][2 * (e & 7)] = lo16(x1p[e >> 2][e & 3]) * r1[(e >> 2) & 1] + r0[(e >> 2) & 1];
        acc[e >> 3][2 * (e & 7) + 1] = hi16(x1p[e >> 2][e & 3]) * r1[(e >> 2) & 1] + r0[(e >> 2) & 1];
      }
      PIN(acc[0]); PIN(acc[1]);
      asm volatile("s_waitcnt vmcnt(0)\n\tds_write_b32 %0, %1" :: "v"(lbase + L_W + 80 + 4 * wid), "v"(2 * L + 2) : "memory");
      STAMPV(11 + 16 * L);
      u32x8 wa;
#pragma unroll
      for (int f = 0; f < 8; ++f) wa[f] = lbase + (f >> 2) * 65536 + (16 * tblk + wrow) * 128 + (((4 * ((f >> 1) & 1) + 2 * (tp & 1) + (f & 1)) ^ wsw ^ tblk) << 4) + 8 * (tp >> 1);
      asm volatile(
        "s_mov_b32 s46, 0\n\t"
        "ostart_%=:\n\t"
        "s_mov_b32 s48, 0x200000\n\t"
        "2:\n\t"
        "ds_read_b32 v239, v238\n\t"
        "s_sub_u32 s48, s48, 1\n\t"
        "s_waitcnt lgkmcnt(0)\n\t"
        "s_cselect_b32 s47, 15, 0\n\t"
        "v_readfirstlane_b32 s51, v239\n\t"
        "s_nop 3\n\t"
        "s_or_b32 s51, s51, s47\n\t"
        "s_andn2_b32 s47, s51, s46\n\t"
        "s_cbranch_scc0 2b\n\t"
        "s_andn2_b32 s51, s51, s46\n\t"
        "s_ff1_i32_b32 s47, s51\n\t"
        "s_add_u32 s45, s47, s49\n\t"
        "s_and_b32 s45, s45, 3\n\t"
        "s_lshl_b32 s45, s45, 14\n\t"
        "s_lshr_b32 s51, s47, 31\n\t"
        "s_lshl_b32 s51, s51, 31\n\t"
        "v_or_b32 v225, s51, v224\n\t"
        "s_and_b32 s51, s47, 3\n\t"
        "s_lshl_b32 s51, s51, 15\n\t"
        "v_add_u32 v234, s51, v226\n\t"
        "v_add_u32 v235, s51, v227\n\t"
        "v_add_u32 v236, s51, v228\n\t"
        "v_add_u32 v237, s51, v229\n\t"
        "buffer_load_dwordx4 v[160:163], v225, s[40:43], s45 offen offset:0 sc1\n\t"
        "buffer_load_dwordx4 v[164:167], v225, s[40:43], s45 offen offset:256 sc1\n\t"
        "buffer_load_dwordx4 v[168:171], v225, s[40:43], s45 offen offset:2048 sc1\n\t"
        "buffer_load_dwordx4 v[172:175], v225, s[40:43], s45 offen offset:2304 sc1\n\t"
        "s_addk_i32 s45, 0x1000\n\t"
        "buffer_load_dwordx4 v[176:179], v225, s[40:43], s45 offen offset:0 sc1\n\t"
        "buffer_load_dwordx4 v[180:183], v225, s[40:43], s45 offen offset:256 sc1\n\t"
        "buffer_load_dwordx4 v[184:187], v225, s[40:43], s45 offen offset:2048 sc1\n\t"
        "buffer_load_dwordx4 v[188:191], v225, s[40:43], s45 offen offset:2304 sc1\n\t"
        "s_addk_i32 s45, 0x1000\n\t"
        "buffer_load_dwordx4 v[192:195], v225, s[40:43], s45 offen offset:0 sc1\n\t"
        "buffer_load_dwordx4 v[196:199], v225, s[40:43], s45 offen offset:256 sc1\n\t"
        "buffer_load_dwordx4 v[200:203], v225, s[40:43], s45 offen offset:2048 sc1\n\t"
        "buffer_load_dwordx4 v[204:207], v225, s[40:43], s45 offen offset:2304 sc1\n\t"
        "ds_read_b64_tr_b16 v[128:129], v234 offset:0\n\t"
        "ds_read_b64_tr_b16 v[130:131], v234 offset:1024\n\t"
        "ds_read_b64_tr_b16 v[132:133], v235 offset:0\n\t"
        "ds_read_b64_tr_b16 v[134:135], v235 offset:1024\n\t"
        "ds_read_b64_tr_b16 v[136:137], v236 offset:0\n\t"
        "ds_read_b64_tr_b16 v[138:139], v236 offset:1024\n\t"
        "ds_read_b64_tr_b16 v[140:141], v237 offset:0\n\t"
        "ds_read_b64_tr_b16 v[142:143], v237 offset:1024\n\t"
        "ds_read_b64_tr_b16 v[144:145], v234 offset:4096\n\t"
        "ds_read_b64_tr_b16 v[146:147], v234 offset:5120\n\t"
        "ds_read_b64_tr_b16 v[148:149], v235 offset:4096\n\t"
        "ds_read_b64_tr_b16 v[150:151], v235 offset:5120\n\t"
        "ds_read_b64_tr_b16 v[152:153], v236 offset:4096\n\t"
        "ds_read_b64_tr_b16 v[154:155], v236 offset:5120\n\t"
        "s_mov_b32 s44, s45\n\t"
        "s_cmp_eq_u32 s47, 0\n\t"
        "s_cbranch_scc1 oseg0_%=\n\t"
        "s_cmp_eq_u32 s47, 1\n\t"
        "s_cbranch_scc1 oseg1_%=\n\t"
        "s_cmp_eq_u32 s47, 2\n\t"
        "s_cbranch_scc1 oseg2_%=\n\t"
        "s_cmp_eq_u32 s47, 3\n\t"
        "s_cbranch_scc1 oseg3_%=\n\t"
        "oseg0_%=:\n\t"
        "s_or_b32 s46, s46, 1\n\t"
        "s_waitcnt vmcnt(10) lgkmcnt(10)\n\t"
        "v_mfma_f32_16x16x32_bf16 v[0:3], v[128:131], v[160:163], v[0:3]\n\t"
        "ds_read_b64_tr_b16 v[156:157], v229 offset:4096\n\t"
        "ds_read_b32 v239, v238\n\t"
        "v_mfma_f32_16x16x32_bf16 v[8:11], v[128:131], v[164:167], v[8:11]\n\t"
        "ds_read_b64_tr_b16 v[158:159], v229 offset:5120\n\t"
        "v_mfma_f32_16x16x32_bf16 v[4:7], v[132:135], v[160:163], v[4:7]\n\t"
        "ds_read_b64_tr_b16 v[128:129], v226 offset:8192\n\t"
        "s_addk_i32 s44, 0x1000\n\t"
        "buffer_load_dwordx4 v[208:211], v224, s[40:43], s44 offen offset:0 sc1\n\t"
        "v_mfma_f32_16x16x32_bf16 v[12:15], v[132:135], v[164:167], v[12:15]\n\t"
        "ds_read_b64_tr_b16 v[130:131], v226 offset:9216\n\t"
        "s_waitcnt lgkmcnt(3)\n\t"
        "v_mfma_f32_16x16x32_bf16 v[16:19], v[136:139], v[160:163], v[16:19]\n\t"
        "ds_read_b64_tr_b16 v[132:133], v227 offset:8192\n\t"
        "v_readfirstlane_b32 s51, v239\n\t"
        "v_mfma_f32_16x16x32_bf16 v[24:27], v[136:139], v[164:167], v[24:27]\n\t"
        "ds_read_b64_tr_b16 v[134:135], v227 offset:9216\n\t"
        "v_mfma_f32_16x16x32_bf16 v[20:23], v[140:143], v[160:163], v[20:23]\n\t"
        "ds_read_b64_tr_b16 v[136:137], v228 offset:8192\n\t"
        "buffer_load_dwordx4 v[212:215], v224, s[40:43], s44 offen offset:256 sc1\n\t"
        "v_mfma_f32_16x16x32_bf16 v[28:31], v[140:143], v[164:167], v[28:31]\n\t"
        "ds_read_b64_tr_b16 v[138:139], v228 offset:9216\n\t"
        "s_andn2_b32 s51, s51, s46\n\t"
        "s_waitcnt vmcnt(10) lgkmcnt(11)\n\t"
        "v_mfma_f32_16x16x32_bf16 v[0:3], v[144:147], v[168:171], v[0:3]\n\t"
        "ds_read_b64_tr_b16 v[140:141], v229 offset:8192\n\t"
        "s_ff1_i32_b32 s47, s51\n\t"
        "v_mfma_f32_16x16x32_bf16 v[8:11], v[144:147], v[172:175], v[8:11]\n\t"
        "ds_read_b64_tr_b16 v[142:143], v229 offset:9216\n\t"
        "s_add_u32 s45, s47, s49\n\t"
        "v_mfma_f32_16x16x32_bf16 v[4:7], v[148:151], v[168:171], v[4:7]\n\t"
        "ds_read_b64_tr_b16 v[144:145], v226 offset:12288\n\t"
        "buffer_load_dwordx4 v[216:219], v224, s[40:43], s44 offen offset:2048 sc1\n\t"
        "v_mfma_f32_16x16x32_bf16 v[12:15], v[148:151], v[172:175], v[12:15]\n\t"
        "ds_read_b64_tr_b16 v[146:147], v226 offset:13312\n\t"
        "s_and_b32 s45, s45, 3\n\t"
        "s_waitcnt lgkmcnt(10)\n\t"
        "v_mfma_f32_16x16x32_bf16 v[16:19], v[152:155], v[168:171], v[16:19]\n\t"
        "ds_read_b64_tr_b16 v[148:149], v227 offset:12288\n\t"
        "s_lshl_b32 s45, s45, 14\n\t"
        "v_mfma_f32_16x16x32_bf16 v[24:27], v[152:155], v[172:175], v[24:27]\n\t"
        "ds_read_b64_tr_b16 v[150:151], v227 offset:13312\n\t"
        "s_lshr_b32 s51, s47, 31\n\t"
        "v_mfma_f32_16x16x32_bf16 v[20:23], v[156:159], v[168:171], v[20:23]\n\t"
        "ds_read_b64_tr_b16 v[152:153], v228 offset:12288\n\t"
        "buffer_load_dwordx4 v[220:223], v224, s[40:43], s44 offen offset:2304 sc1\n\t"
        "v_mfma_f32_16x16x32_bf16 v[28:31], v[156:159], v[172:175], v[28:31]\n\t"
        "ds_read_b64_tr_b16 v[154:155], v228 offset:13312\n\t"
        "s_lshl_b32 s51, s51, 31\n\t"
        "s_waitcnt vmcnt(10) lgkmcnt(10)\n\t"
        "v_mfma_f32_16x16x32_bf16 v[0:3], v[128:131], v[176:179], v[0:3]\n\t"
        "ds_read_b64_tr_b16 v[156:157], v229 offset:12288\n\t"
        "v_or_b32 v225, s51, v224\n\t"
        "v_mfma_f32_16x16x32_bf16 v[8:11], v[128:131], v[180:183], v[8:11]\n\t"
        "ds_read_b64_tr_b16 v[158:159], v229 offset:13312\n\t"
        "s_and_b32 s51, s47, 3\n\t"
        "v_mfma_f32_16x16x32_bf16 v[4:7], v[132:135], v[176:179], v[4:7]\n\t"
        "ds_read_b64_tr_b16 v[128:129], v226 offset:16384\n\t"
        "buffer_load_dwordx4 v[160:163], v225, s[40:43], s45 offen offset:0 sc1\n\t"
        "v_mfma_f32_16x16x32_bf16 v[12:15], v[132:135], v[180:183], v[12:15]\n\t"
        "ds_read_b64_tr_b16 v[130:131], v226 offset:17408\n\t"
        "s_lshl_b32 s51, s51, 15\n\t"
        "s_waitcnt lgkmcnt(10)\n\t"
        "v_mfma_f32_16x16x32_bf16 v[16:19], v[136:139], v[176:179], v[16:19]\n\t"
        "ds_read_b64_tr_b16 v[132:133], v227 offset:16384\n\t"
        "v_add_u32 v234, s51, v226\n\t"
        "v_mfma_f32_16x16x32_bf16 v[24:27], v[136:139], v[180:183], v[24:27]\n\t"
        "ds_read_b64_tr_b16 v[134:135], v227 offset:17408\n\t"
        "v_add_u32 v235, s51, v227\n\t"
        "v_mfma_f32_16x16x32_bf16 v[20:23], v[140:143], v[176:179], v[20:23]\n\t"
        "ds_read_b64_tr_b16 v[136:137], v228 offset:16384\n\t"
        "buffer_load_dwordx4 v[164:167], v225, s[40:43], s45 offen offset:256 sc1\n\t"
        "v_mfma_f32_16x16x32_bf16 v[28:31], v[140:143], v[180:183], v[28:31]\n\t"
        "ds_read_b64_tr_b16 v[138:139], v228 offset:17408\n\t"
        "v_add_u32 v236, s51, v228\n\t"
        "s_waitcnt vmcnt(10) lgkmcnt(10)\n\t"
        "v_mfma_f32_16x16x32_bf16 v[0:3], v[144:147], v[184:187], v[0:3]\n\t"
        "ds_read_b64_tr_b16 v[140:141], v229 offset:16384\n\t"
        "v_add_u32 v237, s51, v229\n\t"
        "v_mfma_f32_16x16x32_bf16 v[8:11], v[144:147], v[188:191], v[8:11]\n\t"
        "ds_read_b64_tr_b16 v[142:143], v229 offset:17408\n\t"
        "v_mfma_f32_16x16x32_bf16 v[4:7], v[148:151], v[184:187], v[4:7]\n\t"
        "ds_read_b64_tr_b16 v[144:145], v226 offset:20480\n\t"
        "buffer_load_dwordx4 v[168:171], v225, s[40:43], s45 offen offset:2048 sc1\n\t"
        "v_mfma_f32_16x16x32_bf16 v[12:15], v[148:151], v[188:191], v[12:15]\n\t"
        "ds_read_b64_tr_b16 v[146:147], v226 offset:21504\n\t"
        "s_waitcnt lgkmcnt(10)\n\t"
        "v_mfma_f32_16x16x32_bf16 v[16:19], v[152:155], v[184:187], v[16:19]\n\t"
        "ds_read_b64_tr_b16 v[148:149], v227 offset:20480\n\t"
        "v_mfma_f32_16x16x32_bf16 v[24:27], v[152:155], v[188:191], v[24:27]\n\t"
        "ds_read_b64_tr_b16 v[150:151], v227 offset:21504\n\t"
        "v_mfma_f32_16x16x32_bf16 v[20:23], v[156:159], v[184:187], v[20:23]\n\t"
        "ds_read_b64_tr_b16 v[152:153], v228 offset:20480\n\t"
        "buffer_load_dwordx4 v[172:175], v225, s[40:43], s45 offen offset:2304 sc1\n\t"
        "v_mfma_f32_16x16x32_bf16 v[28:31], v[156:159], v[188:191], v[28:31]\n\t"
        "ds_read_b64_tr_b16 v[154:155], v228 offset:21504\n\t"
        "s_waitcnt vmcnt(10) lgkmcnt(10)\n\t"
        "v_mfma_f32_16x16x32_bf16 v[0:3], v[128:131], v[192:195], v[0:3]\n\t"
        "ds_read_b64_tr_b16 v[156:157], v229 offset:20480\n\t"
        "v_mfma_f32_16x16x32_bf16 v[8:11], v[128:131], v[196:199], v[8:11]\n\t"
        "ds_read_b64_tr_b16 v[158:159], v229 offset:21504\n\t"
        "v_mfma_f32_16x16x32_bf16 v[4:7], v[132:135], v[192:195], v[4:7]\n\t"
        "ds_read_b64_tr_b16 v[128:129], v226 offset:24576\n\t"
        "s_addk_i32 s45, 0x1000\n\t"
        "buffer_load_dwordx4 v[176:179], v225, s[40:43], s45 offen offset:0 sc1\n\t"
        "v_mfma_f32_16x16x32_bf16 v[12:15], v[132:135], v[196:199], v[12:15]\n\t"
        "ds_read_b64_tr_b16 v[130:131], v226 offset:25600\n\t"
        "s_waitcnt lgkmcnt(10)\n\t"
        "v_mfma_f32_16x16x32_bf16 v[16:19], v[136:139], v[192:195], v[16:19]\n\t"
        "ds_read_b64_tr_b16 v[132:133], v227 offset:24576\n\t"
        "v_mfma_f32_16x16x32_bf16 v[24:27], v[136:139], v[196:199], v[24:27]\n\t"
        "ds_read_b64_tr_b16 v[134:135], v227 offset:25600\n\t"
        "v_mfma_f32_16x16x32_bf16 v[20:23], v[140:143], v[192:195], v[20:23]\n\t"
        "ds_read_b64_tr_b16 v[136:137], v228 offset:24576\n\t"
        "buffer_load_dwordx4 v[180:183], v225, s[40:43], s45 offen offset:256 sc1\n\t"
        "v_mfma_f32_16x16x32_bf16 v[28:31], v[140:143], v[196:199], v[28:31]\n\t"
        "ds_read_b64_tr_b16 v[138:139], v228 offset:25600\n\t"
        "s_waitcnt vmcnt(10) lgkmcnt(10)\n\t"
        "v_mfma_f32_16x16x32_bf16 v[0:3], v[144:147], v[200:203], v[0:3]\n\t"
        "ds_read_b64_tr_b16 v[140:141], v229 offset:24576\n\t"
        "v_mfma_f32_16x16x32_bf16 v[8:11], v[144:147], v[204:207], v[8:11]\n\t"
        "ds_read_b64_tr_b16 v[142:143], v229 offset:25600\n\t"
        "v_mfma_f32_16x16x32_bf16 v[4:7], v[148:151], v[200:203], v[4:7]\n\t"
        "ds_read_b64_tr_b16 v[144:145], v226 offset:28672\n\t"
        "buffer_load_dwordx4 v[184:187], v225, s[40:43], s45 offen offset:2048 sc1\n\t"
        "v_mfma_f32_16x16x32_bf16 v[12:15], v[148:151], v[204:207], v[12:15]\n\t"
        "ds_read_b64_tr_b16 v[146:147], v226 offset:29696\n\t"
        "s_waitcnt lgkmcnt(10)\n\t"
        "v_mfma_f32_16x16x32_bf16 v[16:19], v[152:155], v[200:203], v[16:19]\n\t"
        "ds_read_b64_tr_b16 v[148:149], v227 offset:28672\n\t"
        "v_mfma_f32_16x16x32_bf16 v[24:27], v[152:155], v[204:207], v[24:27]\n\t"
        "ds_read_b64_tr_b16 v[150:151], v227 offset:29696\n\t"
        "v_mfma_f32_16x16x32_bf16 v[20:23], v[156:159], v[200:203], v[20:23]\n\t"
        "ds_read_b64_tr_b16 v[152:153], v228 offset:28672\n\t"
        "buffer_load_dwordx4 v[188:191], v225, s[40:43], s45 offen offset:2304 sc1\n\t"
        "v_mfma_f32_16x16x32_bf16 v[28:31], v[156:159], v[204:207], v[28:31]\n\t"
        "ds_read_b64_tr_b16 v[154:155], v228 offset:29696\n\t"
        "s_waitcnt vmcnt(10) lgkmcnt(10)\n\t"
        "v_mfma_f32_16x16x32_bf16 v[0:3], v[128:131], v[208:211], v[0:3]\n\t"
        "ds_read_b64_tr_b16 v[156:157], v229 offset:28672\n\t"
        "v_mfma_f32_16x16x32_bf16 v[8:11], v[128:131], v[212:215], v[8:11]\n\t"
        "ds_read_b64_tr_b16 v[158:159], v229 offset:29696\n\t"
        "v_mfma_f32_16x16x32_bf16 v[4:7], v[132:135], v[208:211], v[4:7]\n\t"
        "ds_read_b64_tr_b16 v[128:129], v234 offset:0\n\t"
        "s_addk_i32 s45, 0x1000\n\t"
        "buffer_load_dwordx4 v[192:195], v225, s[40:43], s45 offen offset:0 sc1\n\t"
        "v_mfma_f32_16x16x32_bf16 v[12:15], v[132:135], v[212:215], v[12:15]\n\t"
        "ds_read_b64_tr_b16 v[130:131], v234 offset:1024\n\t"
        "s_waitcnt lgkmcnt(10)\n\t"
        "v_mfma_f32_16x16x32_bf16 v[16:19], v[136:139], v[208:211], v[16:19]\n\t"
        "ds_read_b64_tr_b16 v[132:133], v235 offset:0\n\t"
        "v_mfma_f32_16x16x32_bf16 v[24:27], v[136:139], v[212:215], v[24:27]\n\t"
        "ds_read_b64_tr_b16 v[134:135], v235 offset:1024\n\t"
        "v_mfma_f32_16x16x32_bf16 v[20:23], v[140:143], v[208:211], v[20:23]\n\t"
        "ds_read_b64_tr_b16 v[136:137], v236 offset:0\n\t"
        "buffer_load_dwordx4 v[196:199], v225, s[40:43], s45 offen offset:256 sc1\n\t"
        "v_mfma_f32_16x16x32_bf16 v[28:31], v[140:143], v[212:215], v[28:31]\n\t"
        "ds_read_b64_tr_b16 v[138:139], v236 offset:1024\n\t"
        "s_waitcnt vmcnt(10) lgkmcnt(10)\n\t"
        "v_mfma_f32_16x16x32_bf16 v[0:3], v[144:147], v[216:219], v[0:3]\n\t"
        "ds_read_b64_tr_b16 v[140:141], v237 offset:0\n\t"
        "v_mfma_f32_16x16x32_bf16 v[8:11], v[144:147], v[220:223], v[8:11]\n\t"
        "ds_read_b64_tr_b16 v[142:143], v237 offset:1024\n\t"
        "v_mfma_f32_16x16x32_bf16 v[4:7], v[148:151], v[216:219], v[4:7]\n\t"
        "ds_read_b64_tr_b16 v[144:145], v234 offset:4096\n\t"
        "buffer_load_dwordx4 v[200:203], v225, s[40:43], s45 offen offset:2048 sc1\n\t"
        "v_mfma_f32_16x16x32_bf16 v[12:15], v[148:151], v[220:223], v[12:15]\n\t"
        "ds_read_b64_tr_b16 v[146:147], v234 offset:5120\n\t"
        "s_waitcnt lgkmcnt(10)\n\t"
        "v_mfma_f32_16x16x32_bf16 v[16:19], v[152:155], v[216:219], v[16:19]\n\t"
        "ds_read_b64_tr_b16 v[148:149], v235 offset:4096\n\t"
        "v_mfma_f32_16x16x32_bf16 v[24:27], v[152:155], v[220:223], v[24:27]\n\t"
        "ds_read_b64_tr_b16 v[150:151], v235 offset:5120\n\t"
        "v_mfma_f32_16x16x32_bf16 v[20:23], v[156:159], v[216:219], v[20:23]\n\t"
        "ds_read_b64_tr_b16 v[152:153], v236 offset:4096\n\t"
        "buffer_load_dwordx4 v[204:207], v225, s[40:43], s45 offen offset:2304 sc1\n\t"
        "v_mfma_f32_16x16x32_bf16 v[28:31], v[156:159], v[220:223], v[28:31]\n\t"
        "ds_read_b64_tr_b16 v[154:155], v236 offset:5120\n\t"
        "s_mov_b32 s44, s45\n\t"
        "s_cmp_eq_u32 s47, 0\n\t"
        "s_cbranch_scc1 oseg0_%=\n\t"
        "s_cmp_eq_u32 s47, 1\n\t"
        "s_cbranch_scc1 oseg1_%=\n\t"
        "s_cmp_eq_u32 s47, 2\n\t"
        "s_cbranch_scc1 oseg2_%=\n\t"
        "s_cmp_eq_u32 s47, 3\n\t"
        "s_cbranch_scc1 oseg3_%=\n\t"
        "s_cmp_lg_u32 s46, 15\n\t"
        "s_cbranch_scc1 ostart_%=\n\t"
        "s_branch oend_%=\n\t"
        "oseg1_%=:\n\t"
        "s_or_b32 s46, s46, 2\n\t"
        "s_waitcnt vmcnt(10) lgkmcnt(10)\n\t"
        "v_mfma_f32_16x16x32_bf16 v[32:35], v[128:131], v[160:163], 0\n\t"
        "ds_read_b64_tr_b16 v[156:157], v229 offset:36864\n\t"
        "ds_read_b32 v239, v238\n\t"
        "v_mfma_f32_16x16x32_bf16 v[40:43], v[128:131], v[164:167], 0\n\t"
        "ds_read_b64_tr_b16 v[158:159], v229 offset:37888\n\t"
        "v_mfma_f32_16x16x32_bf16 v[36:39], v[132:135], v[160:163], 0\n\t"
        "ds_read_b64_tr_b16 v[128:129], v226 offset:40960\n\t"
        "s_addk_i32 s44, 0x1000\n\t"
        "buffer_load_dwordx4 v[208:211], v224, s[40:43], s44 offen offset:0 sc1\n\t"
        "v_mfma_f32_16x16x32_bf16 v[44:47], v[132:135], v[164:167], 0\n\t"
        "ds_read_b64_tr_b16 v[130:131], v226 offset:41984\n\t"
        "s_waitcnt lgkmcnt(3)\n\t"
        "v_mfma_f32_16x16x32_bf16 v[48:51], v[136:139], v[160:163], 0\n\t"
        "ds_read_b64_tr_b16 v[132:133], v227 offset:40960\n\t"
        "v_readfirstlane_b32 s51, v239\n\t"
        "v_mfma_f32_16x16x32_bf16 v[56:59], v[136:139], v[164:167], 0\n\t"
        "ds_read_b64_tr_b16 v[134:135], v227 offset:41984\n\t"
        "v_mfma_f32_16x16x32_bf16 v[52:55], v[140:143], v[160:163], 0\n\t"
        "ds_read_b64_tr_b16 v[136:137], v228 offset:40960\n\t"
        "buffer_load_dwordx4 v[212:215], v224, s[40:43], s44 offen offset:256 sc1\n\t"
        "v_mfma_f32_16x16x32_bf16 v[60:63], v[140:143], v[164:167], 0\n\t"
        "ds_read_b64_tr_b16 v[138:139], v228 offset:41984\n\t"
        "s_andn2_b32 s51, s51, s46\n\t"
        "s_waitcnt vmcnt(10) lgkmcnt(11)\n\t"
        "v_mfma_f32_16x16x32_bf16 v[32:35], v[144:147], v[168:171], v[32:35]\n\t"
        "ds_read_b64_tr_b16 v[140:141], v229 offset:40960\n\t"
        "s_ff1_i32_b32 s47, s51\n\t"
        "v_mfma_f32_16x16x32_bf16 v[40:43], v[144:147], v[172:175], v[40:43]\n\t"
        "ds_read_b64_tr_b16 v[142:143], v229 offset:41984\n\t"
        "s_add_u32 s45, s47, s49\n\t"
        "v_mfma_f32_16x16x32_bf16 v[36:39], v[148:151], v[168:171], v[36:39]\n\t"
        "ds_read_b64_tr_b16 v[144:145], v226 offset:45056\n\t"
        "buffer_load_dwordx4 v[216:219], v224, s[40:43], s44 offen offset:2048 sc1\n\t"
        "v_mfma_f32_16x16x32_bf16 v[44:47], v[148:151], v[172:175], v[44:47]\n\t"
        "ds_read_b64_tr_b16 v[146:147], v226 offset:46080\n\t"
        "s_and_b32 s45, s45, 3\n\t"
        "s_waitcnt lgkmcnt(10)\n\t"
        "v_mfma_f32_16x16x32_bf16 v[48:51], v[152:155], v[168:171], v[48:51]\n\t"
        "ds_read_b64_tr_b16 v[148:149], v227 offset:45056\n\t"
        "s_lshl_b32 s45, s45, 14\n\t"
        "v_mfma_f32_16x16x32_bf16 v[56:59], v[152:155], v[172:175], v[56:59]\n\t"
        "ds_read_b64_tr_b16 v[150:151], v227 offset:46080\n\t"
        "s_lshr_b32 s51, s47, 31\n\t"
        "v_mfma_f32_16x16x32_bf16 v[52:55], v[156:159], v[168:171], v[52:55]\n\t"
        "ds_read_b64_tr_b16 v[152:153], v228 offset:45056\n\t"
        "buffer_load_dwordx4 v[220:223], v224, s[40:43], s44 offen offset:2304 sc1\n\t"
        "v_mfma_f32_16x16x32_bf16 v[60:63], v[156:159], v[172:175], v[60:63]\n\t"
        "ds_read_b64_tr_b16 v[154:155], v228 offset:46080\n\t"
        "s_lshl_b32 s51, s51, 31\n\t"
        "s_waitcnt vmcnt(10) lgkmcnt(10)\n\t"
        "v_mfma_f32_16x16x32_bf16 v[32:35], v[128:131], v[176:179], v[32:35]\n\t"
        "ds_read_b64_tr_b16 v[156:157], v229 offset:45056\n\t"
        "v_or_b32 v225, s51, v224\n\t"
        "v_mfma_f32_16x16x32_bf16 v[40:43], v[128:131], v[180:183], v[40:43]\n\t"
        "ds_read_b64_tr_b16 v[158:159], v229 offset:46080\n\t"
        "s_and_b32 s51, s47, 3\n\t"
        "v_mfma_f32_16x16x32_bf16 v[36:39], v[132:135], v[176:179], v[36:39]\n\t"
        "ds_read_b64_tr_b16 v[128:129], v226 offset:49152\n\t"
        "buffer_load_dwordx4 v[160:163], v225, s[40:43], s45 offen offset:0 sc1\n\t"
        "v_mfma_f32_16x16x32_bf16 v[44:47], v[132:135], v[180:183], v[44:47]\n\t"
        "ds_read_b64_tr_b16 v[130:131], v226 offset:50176\n\t"
        "s_lshl_b32 s51, s51, 15\n\t"
        "s_waitcnt lgkmcnt(10)\n\t"
        "v_mfma_f32_16x16x32_bf16 v[48:51], v[136:139], v[176:179], v[48:51]\n\t"
        "ds_read_b64_tr_b16 v[132:133], v227 offset:49152\n\t"
        "v_add_u32 v234, s51, v226\n\t"
        "v_mfma_f32_16x16x32_bf16 v[56:59], v[136:139], v[180:183], v[56:59]\n\t"
        "ds_read_b64_tr_b16 v[134:135], v227 offset:50176\n\t"
        "v_add_u32 v235, s51, v227\n\t"
        "v_mfma_f32_16x16x32_bf16 v[52:55], v[140:143], v[176:179], v[52:55]\n\t"
        "ds_read_b64_tr_b16 v[136:137], v228 offset:49152\n\t"
        "buffer_load_dwordx4 v[164:167], v225, s[40:43], s45 offen offset:256 sc1\n\t"
        "v_mfma_f32_16x16x32_bf16 v[60:63], v[140:143], v[180:183], v[60:63]\n\t"
        "ds_read_b64_tr_b16 v[138:139], v228 offset:50176\n\t"
        "v_add_u32 v236, s51, v228\n\t"
        "s_waitcnt vmcnt(10) lgkmcnt(10)\n\t"
        "v_mfma_f32_16x16x32_bf16 v[32:35], v[144:147], v[184:187], v[32:35]\n\t"
        "ds_read_b64_tr_b16 v[140:141], v229 offset:49152\n\t"
        "v_add_u32 v237, s51, v229\n\t"
        "v_mfma_f32_16x16x32_bf16 v[40:43], v[144:147], v[188:191], v[40:43]\n\t"
        "ds_read_b64_tr_b16 v[142:143], v229 offset:50176\n\t"
        "v_mfma_f32_16x16x32_bf16 v[36:39], v[148:151], v[184:187], v[36:39]\n\t"
        "ds_read_b64_tr_b16 v[144:145], v226 offset:53248\n\t"
        "buffer_load_dwordx4 v[168:171], v225, s[40:43], s45 offen offset:2048 sc1\n\t"
        "v_mfma_f32_16x16x32_bf16 v[44:47], v[148:151], v[188:191], v[44:47]\n\t"
        "ds_read_b64_tr_b16 v[146:147], v226 offset:54272\n\t"
        "s_waitcnt lgkmcnt(10)\n\t"
        "v_mfma_f32_16x16x32_bf16 v[48:51], v[152:155], v[184:187], v[48:51]\n\t"
        "ds_read_b64_tr_b16 v[148:149], v227 offset:53248\n\t"
        "v_mfma_f32_16x16x32_bf16 v[56:59], v[152:155], v[188:191], v[56:59]\n\t"
        "ds_read_b64_tr_b16 v[150:151], v227 offset:54272\n\t"
        "v_mfma_f32_16x16x32_bf16 v[52:55], v[156:159], v[184:187], v[52:55]\n\t"
        "ds_read_b64_tr_b16 v[152:153], v228 offset:53248\n\t"
        "buffer_load_dwordx4 v[172:175], v225, s[40:43], s45 offen offset:2304 sc1\n\t"
        "v_mfma_f32_16x16x32_bf16 v[60:63], v[156:159], v[188:191], v[60:63]\n\t"
        "ds_read_b64_tr_b16 v[154:155], v228 offset:54272\n\t"
        "s_waitcnt vmcnt(10) lgkmcnt(10)\n\t"
        "v_mfma_f32_16x16x32_bf16 v[32:35], v[128:131], v[192:195], v[32:35]\n\t"
        "ds_read_b64_tr_b16 v[156:157], v229 offset:53248\n\t"
        "v_mfma_f32_16x16x32_bf16 v[40:43], v[128:131], v[196:199], v[40:43]\n\t"
        "ds_read_b64_tr_b16 v[158:159], v229 offset:54272\n\t"
        "v_mfma_f32_16x16x32_bf16 v[36:39], v[132:135], v[192:195], v[36:39]\n\t"
        "ds_read_b64_tr_b16 v[128:129], v226 offset:57344\n\t"
        "s_addk_i32 s45, 0x1000\n\t"
        "buffer_load_dwordx4 v[176:179], v225, s[40:43], s45 offen offset:0 sc1\n\t"
        "v_mfma_f32_16x16x32_bf16 v[44:47], v[132:135], v[196:199], v[44:47]\n\t"
        "ds_read_b64_tr_b16 v[130:131], v226 offset:58368\n\t"
        "s_waitcnt lgkmcnt(10)\n\t"
        "v_mfma_f32_16x16x32_bf16 v[48:51], v[136:139], v[192:195], v[48:51]\n\t"
        "ds_read_b64_tr_b16 v[132:133], v227 offset:57344\n\t"
        "v_mfma_f32_16x16x32_bf16 v[56:59], v[136:139], v[196:199], v[56:59]\n\t"
        "ds_read_b64_tr_b16 v[134:135], v227 offset:58368\n\t"
        "v_mfma_f32_16x16x32_bf16 v[52:55], v[140:143], v[192:195], v[52:55]\n\t"
        "ds_read_b64_tr_b16 v[136:137], v228 offset:57344\n\t"
        "buffer_load_dwordx4 v[180:183], v225, s[40:43], s45 offen offset:256 sc1\n\t"
        "v_mfma_f32_16x16x32_bf16 v[60:63], v[140:143], v[196:199], v[60:63]\n\t"
        "ds_read_b64_tr_b16 v[138:139], v228 offset:58368\n\t"
        "s_waitcnt vmcnt(10) lgkmcnt(10)\n\t"
        "v_mfma_f32_16x16x32_bf16 v[32:35], v[144:147], v[200:203], v[32:35]\n\t"
        "ds_read_b64_tr_b16 v[140:141], v229 offset:57344\n\t"
        "v_mfma_f32_16x16x32_bf16 v[40:43], v[144:147], v[204:207], v[40:43]\n\t"
        "ds_read_b64_tr_b16 v[142:143], v229 offset:58368\n\t"
        "v_mfma_f32_16x16x32_bf16 v[36:39], v[148:151], v[200:203], v[36:39]\n\t"
        "ds_read_b64_tr_b16 v[144:145], v226 offset:61440\n\t"
        "buffer_load_dwordx4 v[184:187], v225, s[40:43], s45 offen offset:2048 sc1\n\t"
        "v_mfma_f32_16x16x32_bf16 v[44:47], v[148:151], v[204:207], v[44:47]\n\t"
        "ds_read_b64_tr_b16 v[146:147], v226 offset:62464\n\t"
        "s_waitcnt lgkmcnt(10)\n\t"
        "v_mfma_f32_16x16x32_bf16 v[48:51], v[152:155], v[200:203], v[48:51]\n\t"
        "ds_read_b64_tr_b16 v[148:149], v227 offset:61440\n\t"
        "v_mfma_f32_16x16x32_bf16 v[56:59], v[152:155], v[204:207], v[56:59]\n\t"
        "ds_read_b64_tr_b16 v[150:151], v227 offset:62464\n\t"
        "v_mfma_f32_16x16x32_bf16 v[52:55], v[156:159], v[200:203], v[52:55]\n\t"
        "ds_read_b64_tr_b16 v[152:153], v228 offset:61440\n\t"
        "buffer_load_dwordx4 v[188:191], v225, s[40:43], s45 offen offset:2304 sc1\n\t"
        "v_mfma_f32_16x16x32_bf16 v[60:63], v[156:159], v[204:207], v[60:63]\n\t"
        "ds_read_b64_tr_b16 v[154:155], v228 offset:62464\n\t"
        "s_waitcnt vmcnt(10) lgkmcnt(10)\n\t"
        "v_mfma_f32_16x16x32_bf16 v[32:35], v[128:131], v[208:211], v[32:35]\n\t"
        "ds_read_b64_tr_b16 v[156:157], v229 offset:61440\n\t"
        "v_mfma_f32_16x16x32_bf16 v[40:43], v[128:131], v[212:215], v[40:43]\n\t"
        "ds_read_b64_tr_b16 v[158:159], v229 offset:62464\n\t"
        "v_mfma_f32_16x16x32_bf16 v[36:39], v[132:135], v[208:211], v[36:39]\n\t"
        "ds_read_b64_tr_b16 v[128:129], v234 offset:0\n\t"
        "s_addk_i32 s45, 0x1000\n\t"
        "buffer_load_dwordx4 v[192:195], v225, s[40:43], s45 offen offset:0 sc1\n\t"
        "v_mfma_f32_16x16x32_bf16 v[44:47], v[132:135], v[212:215], v[44:47]\n\t"
        "ds_read_b64_tr_b16 v[130:131], v234 offset:1024\n\t"
        "s_waitcnt lgkmcnt(10)\n\t"
        "v_mfma_f32_16x16x32_bf16 v[48:51], v[136:139], v[208:211], v[48:51]\n\t"
        "ds_read_b64_tr_b16 v[132:133], v235 offset:0\n\t"
        "v_mfma_f32_16x16x32_bf16 v[56:59], v[136:139], v[212:215], v[56:59]\n\t"
        "ds_read_b64_tr_b16 v[134:135], v235 offset:1024\n\t"
        "v_mfma_f32_16x16x32_bf16 v[52:55], v[140:143], v[208:211], v[52:55]\n\t"
        "ds_read_b64_tr_b16 v[136:137], v236 offset:0\n\t"
        "buffer_load_dwordx4 v[196:199], v225, s[40:43], s45 offen offset:256 sc1\n\t"
        "v_mfma_f32_16x16x32_bf16 v[60:63], v[140:143], v[212:215], v[60:63]\n\t"
        "ds_read_b64_tr_b16 v[138:139], v236 offset:1024\n\t"
        "s_waitcnt vmcnt(10) lgkmcnt(10)\n\t"
        "v_mfma_f32_16x16x32_bf16 v[32:35], v[144:147], v[216:219], v[32:35]\n\t"
        "ds_read_b64_tr_b16 v[140:141], v237 offset:0\n\t"
        "v_mfma_f32_16x16x32_bf16 v[40:43], v[144:147], v[220:223], v[40:43]\n\t"
        "ds_read_b64_tr_b16 v[142:143], v237 offset:1024\n\t"
        "v_mfma_f32_16x16x32_bf16 v[36:39], v[148:151], v[216:219], v[36:39]\n\t"
        "ds_read_b64_tr_b16 v[144:145], v234 offset:4096\n\t"
        "buffer_load_dwordx4 v[200:203], v225, s[40:43], s45 offen offset:2048 sc1\n\t"
        "v_mfma_f32_16x16x32_bf16 v[44:47], v[148:151], v[220:223], v[44:47]\n\t"
        "ds_read_b64_tr_b16 v[146:147], v234 offset:5120\n\t"
        "s_waitcnt lgkmcnt(10)\n\t"
        "v_mfma_f32_16x16x32_bf16 v[48:51], v[152:155], v[216:219], v[48:51]\n\t"
        "ds_read_b64_tr_b16 v[148:149], v235 offset:4096\n\t"
        "v_mfma_f32_16x16x32_bf16 v[56:59], v[152:155], v[220:223], v[56:59]\n\t"
        "ds_read_b64_tr_b16 v[150:151], v235 offset:5120\n\t"
        "v_mfma_f32_16x16x32_bf16 v[52:55], v[156:159], v[216:219], v[52:55]\n\t"
        "ds_read_b64_tr_b16 v[152:153], v236 offset:4096\n\t"
        "buffer_load_dwordx4 v[204:207], v225, s[40:43], s45 offen offset:2304 sc1\n\t"
        "v_mfma_f32_16x16x32_bf16 v[60:63], v[156:159], v[220:223], v[60:63]\n\t"
        "ds_read_b64_tr_b16 v[154:155], v236 offset:5120\n\t"
        "s_mov_b32 s44, s45\n\t"
        "s_cmp_eq_u32 s47, 0\n\t"
        "s_cbranch_scc1 oseg0_%=\n\t"
        "s_cmp_eq_u32 s47, 1\n\t"
        "s_cbranch_scc1 oseg1_%=\n\t"
        "s_cmp_eq_u32 s47, 2\n\t"
        "s_cbranch_scc1 oseg2_%=\n\t"
        "s_cmp_eq_u32 s47, 3\n\t"
        "s_cbranch_scc1 oseg3_%=\n\t"
        "s_cmp_lg_u32 s46, 15\n\t"
        "s_cbranch_scc1 ostart_%=\n\t"
        "s_branch oend_%=\n\t"
        "oseg2_%=:\n\t"
        "s_or_b32 s46, s46, 4\n\t"
        "s_waitcnt vmcnt(10) lgkmcnt(10)\n\t"
        "v_mfma_f32_16x16x32_bf16 v[64:67], v[128:131], v[160:163], 0\n\t"
        "ds_read_b64_tr_b16 v[156:157], v233 offset:4096\n\t"
        "ds_read_b32 v239, v238\n\t"
        "v_mfma_f32_16x16x32_bf16 v[72:75], v[128:131], v[164:167], 0\n\t"
        "ds_read_b64_tr_b16 v[158:159], v233 offset:5120\n\t"
        "v_mfma_f32_16x16x32_bf16 v[68:71], v[132:135], v[160:163], 0\n\t"
        "ds_read_b64_tr_b16 v[128:129], v230 offset:8192\n\t"
        "s_addk_i32 s44, 0x1000\n\t"
        "buffer_load_dwordx4 v[208:211], v224, s[40:43], s44 offen offset:0 sc1\n\t"
        "v_mfma_f32_16x16x32_bf16 v[76:79], v[132:135], v[164:167], 0\n\t"
        "ds_read_b64_tr_b16 v[130:131], v230 offset:9216\n\t"
        "s_waitcnt lgkmcnt(3)\n\t"
        "v_mfma_f32_16x16x32_bf16 v[80:83], v[136:139], v[160:163], 0\n\t"
        "ds_read_b64_tr_b16 v[132:133], v231 offset:8192\n\t"
        "v_readfirstlane_b32 s51, v239\n\t"
        "v_mfma_f32_16x16x32_bf16 v[88:91], v[136:139], v[164:167], 0\n\t"
        "ds_read_b64_tr_b16 v[134:135], v231 offset:9216\n\t"
        "v_mfma_f32_16x16x32_bf16 v[84:87], v[140:143], v[160:163], 0\n\t"
        "ds_read_b64_tr_b16 v[136:137], v232 offset:8192\n\t"
        "buffer_load_dwordx4 v[212:215], v224, s[40:43], s44 offen offset:256 sc1\n\t"
        "v_mfma_f32_16x16x32_bf16 v[92:95], v[140:143], v[164:167], 0\n\t"
        "ds_read_b64_tr_b16 v[138:139], v232 offset:9216\n\t"
        "s_andn2_b32 s51, s51, s46\n\t"
        "s_waitcnt vmcnt(10) lgkmcnt(11)\n\t"
        "v_mfma_f32_16x16x32_bf16 v[64:67], v[144:147], v[168:171], v[64:67]\n\t"
        "ds_read_b64_tr_b16 v[140:141], v233 offset:8192\n\t"
        "s_ff1_i32_b32 s47, s51\n\t"
        "v_mfma_f32_16x16x32_bf16 v[72:75], v[144:147], v[172:175], v[72:75]\n\t"
        "ds_read_b64_tr_b16 v[142:143], v233 offset:9216\n\t"
        "s_add_u32 s45, s47, s49\n\t"
        "v_mfma_f32_16x16x32_bf16 v[68:71], v[148:151], v[168:171], v[68:71]\n\t"
        "ds_read_b64_tr_b16 v[144:145], v230 offset:12288\n\t"
        "buffer_load_dwordx4 v[216:219], v224, s[40:43], s44 offen offset:2048 sc1\n\t"
        "v_mfma_f32_16x16x32_bf16 v[76:79], v[148:151], v[172:175], v[76:79]\n\t"
        "ds_read_b64_tr_b16 v[146:147], v230 offset:13312\n\t"
        "s_and_b32 s45, s45, 3\n\t"
        "s_waitcnt lgkmcnt(10)\n\t"
        "v_mfma_f32_16x16x32_bf16 v[80:83], v[152:155], v[168:171], v[80:83]\n\t"
        "ds_read_b64_tr_b16 v[148:149], v231 offset:12288\n\t"
        "s_lshl_b32 s45, s45, 14\n\t"
        "v_mfma_f32_16x16x32_bf16 v[88:91], v[152:155], v[172:175], v[88:91]\n\t"
        "ds_read_b64_tr_b16 v[150:151], v231 offset:13312\n\t"
        "s_lshr_b32 s51, s47, 31\n\t"
        "v_mfma_f32_16x16x32_bf16 v[84:87], v[156:159], v[168:171], v[84:87]\n\t"
        "ds_read_b64_tr_b16 v[152:153], v232 offset:12288\n\t"
        "buffer_load_dwordx4 v[220:223], v224, s[40:43], s44 offen offset:2304 sc1\n\t"
        "v_mfma_f32_16x16x32_bf16 v[92:95], v[156:159], v[172:175], v[92:95]\n\t"
        "ds_read_b64_tr_b16 v[154:155], v232 offset:13312\n\t"
        "s_lshl_b32 s51, s51, 31\n\t"
        "s_waitcnt vmcnt(10) lgkmcnt(10)\n\t"
        "v_mfma_f32_16x16x32_bf16 v[64:67], v[128:131], v[176:179], v[64:67]\n\t"
        "ds_read_b64_tr_b16 v[156:157], v233 offset:12288\n\t"
        "v_or_b32 v225, s51, v224\n\t"
        "v_mfma_f32_16x16x32_bf16 v[72:75], v[128:131], v[180:183], v[72:75]\n\t"
        "ds_read_b64_tr_b16 v[158:159], v233 offset:13312\n\t"
        "s_and_b32 s51, s47, 3\n\t"
        "v_mfma_f32_16x16x32_bf16 v[68:71], v[132:135], v[176:179], v[68:71]\n\t"
        "ds_read_b64_tr_b16 v[128:129], v230 offset:16384\n\t"
        "buffer_load_dwordx4 v[160:163], v225, s[40:43], s45 offen offset:0 sc1\n\t"
        "v_mfma_f32_16x16x32_bf16 v[76:79], v[132:135], v[180:183], v[76:79]\n\t"
        "ds_read_b64_tr_b16 v[130:131], v230 offset:17408\n\t"
        "s_lshl_b32 s51, s51, 15\n\t"
        "s_waitcnt lgkmcnt(10)\n\t"
        "v_mfma_f32_16x16x32_bf16 v[80:83], v[136:139], v[176:179], v[80:83]\n\t"
        "ds_read_b64_tr_b16 v[132:133], v231 offset:16384\n\t"
        "v_add_u32 v234, s51, v226\n\t"
        "v_mfma_f32_16x16x32_bf16 v[88:91], v[136:139], v[180:183], v[88:91]\n\t"
        "ds_read_b64_tr_b16 v[134:135], v231 offset:17408\n\t"
        "v_add_u32 v235, s51, v227\n\t"
        "v_mfma_f32_16x16x32_bf16 v[84:87], v[140:143], v[176:179], v[84:87]\n\t"
        "ds_read_b64_tr_b16 v[136:137], v232 offset:16384\n\t"
        "buffer_load_dwordx4 v[164:167], v225, s[40:43], s45 offen offset:256 sc1\n\t"
        "v_mfma_f32_16x16x32_bf16 v[92:95], v[140:143], v[180:183], v[92:95]\n\t"
        "ds_read_b64_tr_b16 v[138:139], v232 offset:17408\n\t"
        "v_add_u32 v236, s51, v228\n\t"
        "s_waitcnt vmcnt(10) lgkmcnt(10)\n\t"
        "v_mfma_f32_16x16x32_bf16 v[64:67], v[144:147], v[184:187], v[64:67]\n\t"
        "ds_read_b64_tr_b16 v[140:141], v233 offset:16384\n\t"
        "v_add_u32 v237, s51, v229\n\t"
        "v_mfma_f32_16x16x32_bf16 v[72:75], v[144:147], v[188:191], v[72:75]\n\t"
        "ds_read_b64_tr_b16 v[142:143], v233 offset:17408\n\t"
        "v_mfma_f32_16x16x32_bf16 v[68:71], v[148:151], v[184:187], v[68:71]\n\t"
        "ds_read_b64_tr_b16 v[144:145], v230 offset:20480\n\t"
        "buffer_load_dwordx4 v[168:171], v225, s[40:43], s45 offen offset:2048 sc1\n\t"
        "v_mfma_f32_16x16x32_bf16 v[76:79], v[148:151], v[188:191], v[76:79]\n\t"
        "ds_read_b64_tr_b16 v[146:147], v230 offset:21504\n\t"
        "s_waitcnt lgkmcnt(10)\n\t"
        "v_mfma_f32_16x16x32_bf16 v[80:83], v[152:155], v[184:187], v[80:83]\n\t"
        "ds_read_b64_tr_b16 v[148:149], v231 offset:20480\n\t"
        "v_mfma_f32_16x16x32_bf16 v[88:91], v[152:155], v[188:191], v[88:91]\n\t"
        "ds_read_b64_tr_b16 v[150:151], v231 offset:21504\n\t"
        "v_mfma_f32_16x16x32_bf16 v[84:87], v[156:159], v[184:187], v[84:87]\n\t"
        "ds_read_b64_tr_b16 v[152:153], v232 offset:20480\n\t"
        "buffer_load_dwordx4 v[172:175], v225, s[40:43], s45 offen offset:2304 sc1\n\t"
        "v_mfma_f32_16x16x32_bf16 v[92:95], v[156:159], v[188:191], v[92:95]\n\t"
        "ds_read_b64_tr_b16 v[154:155], v232 offset:21504\n\t"
        "s_waitcnt vmcnt(10) lgkmcnt(10)\n\t"
        "v_mfma_f32_16x16x32_bf16 v[64:67], v[128:131], v[192:195], v[64:67]\n\t"
        "ds_read_b64_tr_b16 v[156:157], v233 offset:20480\n\t"
        "v_mfma_f32_16x16x32_bf16 v[72:75], v[128:131], v[196:199], v[72:75]\n\t"
        "ds_read_b64_tr_b16 v[158:159], v233 offset:21504\n\t"
        "v_mfma_f32_16x16x32_bf16 v[68:71], v[132:135], v[192:195], v[68:71]\n\t"
        "ds_read_b64_tr_b16 v[128:129], v230 offset:24576\n\t"
        "s_addk_i32 s45, 0x1000\n\t"
        "buffer_load_dwordx4 v[176:179], v225, s[40:43], s45 offen offset:0 sc1\n\t"
        "v_mfma_f32_16x16x32_bf16 v[76:79], v[132:135], v[196:199], v[76:79]\n\t"
        "ds_read_b64_tr_b16 v[130:131], v230 offset:25600\n\t"
        "s_waitcnt lgkmcnt(10)\n\t"
        "v_mfma_f32_16x16x32_bf16 v[80:83], v[136:139], v[192:195], v[80:83]\n\t"
        "ds_read_b64_tr_b16 v[132:133], v231 offset:24576\n\t"
        "v_mfma_f32_16x16x32_bf16 v[88:91], v[136:139], v[196:199], v[88:91]\n\t"
        "ds_read_b64_tr_b16 v[134:135], v231 offset:25600\n\t"
        "v_mfma_f32_16x16x32_bf16 v[84:87], v[140:143], v[192:195], v[84:87]\n\t"
        "ds_read_b64_tr_b16 v[136:137], v232 offset:24576\n\t"
        "buffer_load_dwordx4 v[180:183], v225, s[40:43], s45 offen offset:256 sc1\n\t"
        "v_mfma_f32_16x16x32_bf16 v[92:95], v[140:143], v[196:199], v[92:95]\n\t"
        "ds_read_b64_tr_b16 v[138:139], v232 offset:25600\n\t"
        "s_waitcnt vmcnt(10) lgkmcnt(10)\n\t"
        "v_mfma_f32_16x16x32_bf16 v[64:67], v[144:147], v[200:203], v[64:67]\n\t"
        "ds_read_b64_tr_b16 v[140:141], v233 offset:24576\n\t"
        "v_mfma_f32_16x16x32_bf16 v[72:75], v[144:147], v[204:207], v[72:75]\n\t"
        "ds_read_b64_tr_b16 v[142:143], v233 offset:25600\n\t"
        "v_mfma_f32_16x16x32_bf16 v[68:71], v[148:151], v[200:203], v[68:71]\n\t"
        "ds_read_b64_tr_b16 v[144:145], v230 offset:28672\n\t"
        "buffer_load_dwordx4 v[184:187], v225, s[40:43], s45 offen offset:2048 sc1\n\t"
        "v_mfma_f32_16x16x32_bf16 v[76:79], v[148:151], v[204:207], v[76:79]\n\t"
        "ds_read_b64_tr_b16 v[146:147], v230 offset:29696\n\t"
        "s_waitcnt lgkmcnt(10)\n\t"
        "v_mfma_f32_16x16x32_bf16 v[80:83], v[152:155], v[200:203], v[80:83]\n\t"
        "ds_read_b64_tr_b16 v[148:149], v231 offset:28672\n\t"
        "v_mfma_f32_16x16x32_bf16 v[88:91], v[152:155], v[204:207], v[88:91]\n\t"
        "ds_read_b64_tr_b16 v[150:151], v231 offset:29696\n\t"
        "v_mfma_f32_16x16x32_bf16 v[84:87], v[156:159], v[200:203], v[84:87]\n\t"
        "ds_read_b64_tr_b16 v[152:153], v232 offset:28672\n\t"
        "buffer_load_dwordx4 v[188:191], v225, s[40:43], s45 offen offset:2304 sc1\n\t"
        "v_mfma_f32_16x16x32_bf16 v[92:95], v[156:159], v[204:207], v[92:95]\n\t"
        "ds_read_b64_tr_b16 v[154:155], v232 offset:29696\n\t"
        "s_waitcnt vmcnt(10) lgkmcnt(10)\n\t"
        "v_mfma_f32_16x16x32_bf16 v[64:67], v[128:131], v[208:211], v[64:67]\n\t"
        "ds_read_b64_tr_b16 v[156:157], v233 offset:28672\n\t"
        "v_mfma_f32_16x16x32_bf16 v[72:75], v[128:131], v[212:215], v[72:75]\n\t"
        "ds_read_b64_tr_b16 v[158:159], v233 offset:29696\n\t"
        "v_mfma_f32_16x16x32_bf16 v[68:71], v[132:135], v[208:211], v[68:71]\n\t"
        "ds_read_b64_tr_b16 v[128:129], v234 offset:0\n\t"
        "s_addk_i32 s45, 0x1000\n\t"
        "buffer_load_dwordx4 v[192:195], v225, s[40:43], s45 offen offset:0 sc1\n\t"
        "v_mfma_f32_16x16x32_bf16 v[76:79], v[132:135], v[212:215], v[76:79]\n\t"
        "ds_read_b64_tr_b16 v[130:131], v234 offset:1024\n\t"
        "s_waitcnt lgkmcnt(10)\n\t"
        "v_mfma_f32_16x16x32_bf16 v[80:83], v[136:139], v[208:211], v[80:83]\n\t"
        "ds_read_b64_tr_b16 v[132:133], v235 offset:0\n\t"
        "v_mfma_f32_16x16x32_bf16 v[88:91], v[136:139], v[212:215], v[88:91]\n\t"
        "ds_read_b64_tr_b16 v[134:135], v235 offset:1024\n\t"
        "v_mfma_f32_16x16x32_bf16 v[84:87], v[140:143], v[208:211], v[84:87]\n\t"
        "ds_read_b64_tr_b16 v[136:137], v236 offset:0\n\t"
        "buffer_load_dwordx4 v[196:199], v225, s[40:43], s45 offen offset:256 sc1\n\t"
        "v_mfma_f32_16x16x32_bf16 v[92:95], v[140:143], v[212:215], v[92:95]\n\t"
        "ds_read_b64_tr_b16 v[138:139], v236 offset:1024\n\t"
        "s_waitcnt vmcnt(10) lgkmcnt(10)\n\t"
        "v_mfma_f32_16x16x32_bf16 v[64:67], v[144:147], v[216:219], v[64:67]\n\t"
        "ds_read_b64_tr_b16 v[140:141], v237 offset:0\n\t"
        "v_mfma_f32_16x16x32_bf16 v[72:75], v[144:147], v[220:223], v[72:75]\n\t"
        "ds_read_b64_tr_b16 v[142:143], v237 offset:1024\n\t"
        "v_mfma_f32_16x16x32_bf16 v[68:71], v[148:151], v[216:219], v[68:71]\n\t"
        "ds_read_b64_tr_b16 v[144:145], v234 offset:4096\n\t"
        "buffer_load_dwordx4 v[200:203], v225, s[40:43], s45 offen offset:2048 sc1\n\t"
        "v_mfma_f32_16x16x32_bf16 v[76:79], v[148:151], v[220:223], v[76:79]\n\t"
        "ds_read_b64_tr_b16 v[146:147], v234 offset:5120\n\t"
        "s_waitcnt lgkmcnt(10)\n\t"
        "v_mfma_f32_16x16x32_bf16 v[80:83], v[152:155], v[216:219], v[80:83]\n\t"
        "ds_read_b64_tr_b16 v[148:149], v235 offset:4096\n\t"
        "v_mfma_f32_16x16x32_bf16 v[88:91], v[152:155], v[220:223], v[88:91]\n\t"
        "ds_read_b64_tr_b16 v[150:151], v235 offset:5120\n\t"
        "v_mfma_f32_16x16x32_bf16 v[84:87], v[156:159], v[216:219], v[84:87]\n\t"
        "ds_read_b64_tr_b16 v[152:153], v236 offset:4096\n\t"
        "buffer_load_dwordx4 v[204:207], v225, s[40:43], s45 offen offset:2304 sc1\n\t"
        "v_mfma_f32_16x16x32_bf16 v[92:95], v[156:159], v[220:223], v[92:95]\n\t"
        "ds_read_b64_tr_b16 v[154:155], v236 offset:5120\n\t"
        "s_mov_b32 s44, s45\n\t"
        "s_cmp_eq_u32 s47, 0\n\t"
        "s_cbranch_scc1 oseg0_%=\n\t"
        "s_cmp_eq_u32 s47, 1\n\t"
        "s_cbranch_scc1 oseg1_%=\n\t"
        "s_cmp_eq_u32 s47, 2\n\t"
        "s_cbranch_scc1 oseg2_%=\n\t"
        "s_cmp_eq_u32 s47, 3\n\t"
        "s_cbranch_scc1 oseg3_%=\n\t"
        "s_cmp_lg_u32 s46, 15\n\t"
        "s_cbranch_scc1 ostart_%=\n\t"
        "s_branch oend_%=\n\t"
        "oseg3_%=:\n\t"
        "s_or_b32 s46, s46, 8\n\t"
        "s_waitcnt vmcnt(10) lgkmcnt(10)\n\t"
        "v_mfma_f32_16x16x32_bf16 v[96:99], v[128:131], v[160:163], 0\n\t"
        "ds_read_b64_tr_b16 v[156:157], v233 offset:36864\n\t"
        "ds_read_b32 v239, v238\n\t"
        "v_mfma_f32_16x16x32_bf16 v[104:107], v[128:131], v[164:167], 0\n\t"
        "ds_read_b64_tr_b16 v[158:159], v233 offset:37888\n\t"
        "v_mfma_f32_16x16x32_bf16 v[100:103], v[132:135], v[160:163], 0\n\t"
        "ds_read_b64_tr_b16 v[128:129], v230 offset:40960\n\t"
        "s_addk_i32 s44, 0x1000\n\t"
        "buffer_load_dwordx4 v[208:211], v224, s[40:43], s44 offen offset:0 sc1\n\t"
        "v_mfma_f32_16x16x32_bf16 v[108:111], v[132:135], v[164:167], 0\n\t"
        "ds_read_b64_tr_b16 v[130:131], v230 offset:41984\n\t"
        "s_waitcnt lgkmcnt(3)\n\t"
        "v_mfma_f32_16x16x32_bf16 v[112:115], v[136:139], v[160:163], 0\n\t"
        "ds_read_b64_tr_b16 v[132:133], v231 offset:40960\n\t"
        "v_readfirstlane_b32 s51, v239\n\t"
        "v_mfma_f32_16x16x32_bf16 v[120:123], v[136:139], v[164:167], 0\n\t"
        "ds_read_b64_tr_b16 v[134:135], v231 offset:41984\n\t"
        "v_mfma_f32_16x16x32_bf16 v[116:119], v[140:143], v[160:163], 0\n\t"
        "ds_read_b64_tr_b16 v[136:137], v232 offset:40960\n\t"
        "buffer_load_dwordx4 v[212:215], v224, s[40:43], s44 offen offset:256 sc1\n\t"
        "v_mfma_f32_16x16x32_bf16 v[124:127], v[140:143], v[164:167], 0\n\t"
        "ds_read_b64_tr_b16 v[138:139], v232 offset:41984\n\t"
        "s_andn2_b32 s51, s51, s46\n\t"
        "s_waitcnt vmcnt(10) lgkmcnt(11)\n\t"
        "v_mfma_f32_16x16x32_bf16 v[96:99], v[144:147], v[168:171], v[96:99]\n\t"
        "ds_read_b64_tr_b16 v[140:141], v233 offset:40960\n\t"
        "s_ff1_i32_b32 s47, s51\n\t"
        "v_mfma_f32_16x16x32_bf16 v[104:107], v[144:147], v[172:175], v[104:107]\n\t"
        "ds_read_b64_tr_b16 v[142:143], v233 offset:41984\n\t"
        "s_add_u32 s45, s47, s49\n\t"
        "v_mfma_f32_16x16x32_bf16 v[100:103], v[148:151], v[168:171], v[100:103]\n\t"
        "ds_read_b64_tr_b16 v[144:145], v230 offset:45056\n\t"
        "buffer_load_dwordx4 v[216:219], v224, s[40:43], s44 offen offset:2048 sc1\n\t"
        "v_mfma_f32_16x16x32_bf16 v[108:111], v[148:151], v[172:175], v[108:111]\n\t"
        "ds_read_b64_tr_b16 v[146:147], v230 offset:46080\n\t"
        "s_and_b32 s45, s45, 3\n\t"
        "s_waitcnt lgkmcnt(10)\n\t"
        "v_mfma_f32_16x16x32_bf16 v[112:115], v[152:155], v[168:171], v[112:115]\n\t"
        "ds_read_b64_tr_b16 v[148:149], v231 offset:45056\n\t"
        "s_lshl_b32 s45, s45, 14\n\t"
        "v_mfma_f32_16x16x32_bf16 v[120:123], v[152:155], v[172:175], v[120:123]\n\t"
        "ds_read_b64_tr_b16 v[150:151], v231 offset:46080\n\t"
        "s_lshr_b32 s51, s47, 31\n\t"
        "v_mfma_f32_16x16x32_bf16 v[116:119], v[156:159], v[168:171], v[116:119]\n\t"
        "ds_read_b64_tr_b16 v[152:153], v232 offset:45056\n\t"
        "buffer_load_dwordx4 v[220:223], v224, s[40:43], s44 offen offset:2304 sc1\n\t"
        "v_mfma_f32_16x16x32_bf16 v[124:127], v[156:159], v[172:175], v[124:127]\n\t"
        "ds_read_b64_tr_b16 v[154:155], v232 offset:46080\n\t"
        "s_lshl_b32 s51, s51, 31\n\t"
        "s_waitcnt vmcnt(10) lgkmcnt(10)\n\t"
        "v_mfma_f32_16x16x32_bf16 v[96:99], v[128:131], v[176:179], v[96:99]\n\t"
        "ds_read_b64_tr_b16 v[156:157], v233 offset:45056\n\t"
        "v_or_b32 v225, s51, v224\n\t"
        "v_mfma_f32_16x16x32_bf16 v[104:107], v[128:131], v[180:183], v[104:107]\n\t"
        "ds_read_b64_tr_b16 v[158:159], v233 offset:46080\n\t"
        "s_and_b32 s51, s47, 3\n\t"
        "v_mfma_f32_16x16x32_bf16 v[100:103], v[132:135], v[176:179], v[100:103]\n\t"
        "ds_read_b64_tr_b16 v[128:129], v230 offset:49152\n\t"
        "buffer_load_dwordx4 v[160:163], v225, s[40:43], s45 offen offset:0 sc1\n\t"
        "v_mfma_f32_16x16x32_bf16 v[108:111], v[132:135], v[180:183], v[108:111]\n\t"
        "ds_read_b64_tr_b16 v[130:131], v230 offset:50176\n\t"
        "s_lshl_b32 s51, s51, 15\n\t"
        "s_waitcnt lgkmcnt(10)\n\t"
        "v_mfma_f32_16x16x32_bf16 v[112:115], v[136:139], v[176:179], v[112:115]\n\t"
        "ds_read_b64_tr_b16 v[132:133], v231 offset:49152\n\t"
        "v_add_u32 v234, s51, v226\n\t"
        "v_mfma_f32_16x16x32_bf16 v[120:123], v[136:139], v[180:183], v[120:123]\n\t"
        "ds_read_b64_tr_b16 v[134:135], v231 offset:50176\n\t"
        "v_add_u32 v235, s51, v227\n\t"
        "v_mfma_f32_16x16x32_bf16 v[116:119], v[140:143], v[176:179], v[116:119]\n\t"
        "ds_read_b64_tr_b16 v[136:137], v232 offset:49152\n\t"
        "buffer_load_dwordx4 v[164:167], v225, s[40:43], s45 offen offset:256 sc1\n\t"
        "v_mfma_f32_16x16x32_bf16 v[124:127], v[140:143], v[180:183], v[124:127]\n\t"
        "ds_read_b64_tr_b16 v[138:139], v232 offset:50176\n\t"
        "v_add_u32 v236, s51, v228\n\t"
        "s_waitcnt vmcnt(10) lgkmcnt(10)\n\t"
        "v_mfma_f32_16x16x32_bf16 v[96:99], v[144:147], v[184:187], v[96:99]\n\t"
        "ds_read_b64_tr_b16 v[140:141], v233 offset:49152\n\t"
        "v_add_u32 v237, s51, v229\n\t"
        "v_mfma_f32_16x16x32_bf16 v[104:107], v[144:147], v[188:191], v[104:107]\n\t"
        "ds_read_b64_tr_b16 v[142:143], v233 offset:50176\n\t"
        "v_mfma_f32_16x16x32_bf16 v[100:103], v[148:151], v[184:187], v[100:103]\n\t"
        "ds_read_b64_tr_b16 v[144:145], v230 offset:53248\n\t"
        "buffer_load_dwordx4 v[168:171], v225, s[40:43], s45 offen offset:2048 sc1\n\t"
        "v_mfma_f32_16x16x32_bf16 v[108:111], v[148:151], v[188:191], v[108:111]\n\t"
        "ds_read_b64_tr_b16 v[146:147], v230 offset:54272\n\t"
        "s_waitcnt lgkmcnt(10)\n\t"
        "v_mfma_f32_16x16x32_bf16 v[112:115], v[152:155], v[184:187], v[112:115]\n\t"
        "ds_read_b64_tr_b16 v[148:149], v231 offset:53248\n\t"
        "v_mfma_f32_16x16x32_bf16 v[120:123], v[152:155], v[188:191], v[120:123]\n\t"
        "ds_read_b64_tr_b16 v[150:151], v231 offset:54272\n\t"
        "v_mfma_f32_16x16x32_bf16 v[116:119], v[156:159], v[184:187], v[116:119]\n\t"
        "ds_read_b64_tr_b16 v[152:153], v232 offset:53248\n\t"
        "buffer_load_dwordx4 v[172:175], v225, s[40:43], s45 offen offset:2304 sc1\n\t"
        "v_mfma_f32_16x16x32_bf16 v[124:127], v[156:159], v[188:191], v[124:127]\n\t"
        "ds_read_b64_tr_b16 v[154:155], v232 offset:54272\n\t"
        "s_waitcnt vmcnt(10) lgkmcnt(10)\n\t"
        "v_mfma_f32_16x16x32_bf16 v[96:99], v[128:131], v[192:195], v[96:99]\n\t"
        "ds_read_b64_tr_b16 v[156:157], v233 offset:53248\n\t"
        "v_mfma_f32_16x16x32_bf16 v[104:107], v[128:131], v[196:199], v[104:107]\n\t"
        "ds_read_b64_tr_b16 v[158:159], v233 offset:54272\n\t"
        "v_mfma_f32_16x16x32_bf16 v[100:103], v[132:135], v[192:195], v[100:103]\n\t"
        "ds_read_b64_tr_b16 v[128:129], v230 offset:57344\n\t"
        "s_addk_i32 s45, 0x1000\n\t"
        "buffer_load_dwordx4 v[176:179], v225, s[40:43], s45 offen offset:0 sc1\n\t"
        "v_mfma_f32_16x16x32_bf16 v[108:111], v[132:135], v[196:199], v[108:111]\n\t"
        "ds_read_b64_tr_b16 v[130:131], v230 offset:58368\n\t"
        "s_waitcnt lgkmcnt(10)\n\t"
        "v_mfma_f32_16x16x32_bf16 v[112:115], v[136:139], v[192:195], v[112:115]\n\t"
        "ds_read_b64_tr_b16 v[132:133], v231 offset:57344\n\t"
        "v_mfma_f32_16x16x32_bf16 v[120:123], v[136:139], v[196:199], v[120:123]\n\t"
        "ds_read_b64_tr_b16 v[134:135], v231 offset:58368\n\t"
        "v_mfma_f32_16x16x32_bf16 v[116:119], v[140:143], v[192:195], v[116:119]\n\t"
        "ds_read_b64_tr_b16 v[136:137], v232 offset:57344\n\t"
        "buffer_load_dwordx4 v[180:183], v225, s[40:43], s45 offen offset:256 sc1\n\t"
        "v_mfma_f32_16x16x32_bf16 v[124:127], v[140:143], v[196:199], v[124:127]\n\t"
        "ds_read_b64_tr_b16 v[138:139], v232 offset:58368\n\t"
        "s_waitcnt vmcnt(10) lgkmcnt(10)\n\t"
        "v_mfma_f32_16x16x32_bf16 v[96:99], v[144:147], v[200:203], v[96:99]\n\t"
        "ds_read_b64_tr_b16 v[140:141], v233 offset:57344\n\t"
        "v_mfma_f32_16x16x32_bf16 v[104:107], v[144:147], v[204:207], v[104:107]\n\t"
        "ds_read_b64_tr_b16 v[142:143], v233 offset:58368\n\t"
        "v_mfma_f32_16x16x32_bf16 v[100:103], v[148:151], v[200:203], v[100:103]\n\t"
        "ds_read_b64_tr_b16 v[144:145], v230 offset:61440\n\t"
        "buffer_load_dwordx4 v[184:187], v225, s[40:43], s45 offen offset:2048 sc1\n\t"
        "v_mfma_f32_16x16x32_bf16 v[108:111], v[148:151], v[204:207], v[108:111]\n\t"
        "ds_read_b64_tr_b16 v[146:147], v230 offset:62464\n\t"
        "s_waitcnt lgkmcnt(10)\n\t"
        "v_mfma_f32_16x16x32_bf16 v[112:115], v[152:155], v[200:203], v[112:115]\n\t"
        "ds_read_b64_tr_b16 v[148:149], v231 offset:61440\n\t"
        "v_mfma_f32_16x16x32_bf16 v[120:123], v[152:155], v[204:207], v[120:123]\n\t"
        "ds_read_b64_tr_b16 v[150:151], v231 offset:62464\n\t"
        "v_mfma_f32_16x16x32_bf16 v[116:119], v[156:159], v[200:203], v[116:119]\n\t"
        "ds_read_b64_tr_b16 v[152:153], v232 offset:61440\n\t"
        "buffer_load_dwordx4 v[188:191], v225, s[40:43], s45 offen offset:2304 sc1\n\t"
        "v_mfma_f32_16x16x32_bf16 v[124:127], v[156:159], v[204:207], v[124:127]\n\t"
        "ds_read_b64_tr_b16 v[154:155], v232 offset:62464\n\t"
        "s_waitcnt vmcnt(10) lgkmcnt(10)\n\t"
        "v_mfma_f32_16x16x32_bf16 v[96:99], v[128:131], v[208:211], v[96:99]\n\t"
        "ds_read_b64_tr_b16 v[156:157], v233 offset:61440\n\t"
        "v_mfma_f32_16x16x32_bf16 v[104:107], v[128:131], v[212:215], v[104:107]\n\t"
        "ds_read_b64_tr_b16 v[158:159], v233 offset:62464\n\t"
        "v_mfma_f32_16x16x32_bf16 v[100:103], v[132:135], v[208:211], v[100:103]\n\t"
        "ds_read_b64_tr_b16 v[128:129], v234 offset:0\n\t"
        "s_addk_i32 s45, 0x1000\n\t"
        "buffer_load_dwordx4 v[192:195], v225, s[40:43], s45 offen offset:0 sc1\n\t"
        "v_mfma_f32_16x16x32_bf16 v[108:111], v[132:135], v[212:215], v[108:111]\n\t"
        "ds_read_b64_tr_b16 v[130:131], v234 offset:1024\n\t"
        "s_waitcnt lgkmcnt(10)\n\t"
        "v_mfma_f32_16x16x32_bf16 v[112:115], v[136:139], v[208:211], v[112:115]\n\t"
        "ds_read_b64_tr_b16 v[132:133], v235 offset:0\n\t"
        "v_mfma_f32_16x16x32_bf16 v[120:123], v[136:139], v[212:215], v[120:123]\n\t"
        "ds_read_b64_tr_b16 v[134:135], v235 offset:1024\n\t"
        "v_mfma_f32_16x16x32_bf16 v[116:119], v[140:143], v[208:211], v[116:119]\n\t"
        "ds_read_b64_tr_b16 v[136:137], v236 offset:0\n\t"
        "buffer_load_dwordx4 v[196:199], v225, s[40:43], s45 offen offset:256 sc1\n\t"
        "v_mfma_f32_16x16x32_bf16 v[124:127], v[140:143], v[212:215], v[124:127]\n\t"
        "ds_read_b64_tr_b16 v[138:139], v236 offset:1024\n\t"
        "s_waitcnt vmcnt(10) lgkmcnt(10)\n\t"
        "v_mfma_f32_16x16x32_bf16 v[96:99], v[144:147], v[216:219], v[96:99]\n\t"
        "ds_read_b64_tr_b16 v[140:141], v237 offset:0\n\t"
        "v_mfma_f32_16x16x32_bf16 v[104:107], v[144:147], v[220:223], v[104:107]\n\t"
        "ds_read_b64_tr_b16 v[142:143], v237 offset:1024\n\t"
        "v_mfma_f32_16x16x32_bf16 v[100:103], v[148:151], v[216:219], v[100:103]\n\t"
        "ds_read_b64_tr_b16 v[144:145], v234 offset:4096\n\t"
        "buffer_load_dwordx4 v[200:203], v225, s[40:43], s45 offen offset:2048 sc1\n\t"
        "v_mfma_f32_16x16x32_bf16 v[108:111], v[148:151], v[220:223], v[108:111]\n\t"
        "ds_read_b64_tr_b16 v[146:147], v234 offset:5120\n\t"
        "s_waitcnt lgkmcnt(10)\n\t"
        "v_mfma_f32_16x16x32_bf16 v[112:115], v[152:155], v[216:219], v[112:115]\n\t"
        "ds_read_b64_tr_b16 v[148:149], v235 offset:4096\n\t"
        "v_mfma_f32_16x16x32_bf16 v[120:123], v[152:155], v[220:223], v[120:123]\n\t"
        "ds_read_b64_tr_b16 v[150:151], v235 offset:5120\n\t"
        "v_mfma_f32_16x16x32_bf16 v[116:119], v[156:159], v[216:219], v[116:119]\n\t"
        "ds_read_b64_tr_b16 v[152:153], v236 offset:4096\n\t"
        "buffer_load_dwordx4 v[204:207], v225, s[40:43], s45 offen offset:2304 sc1\n\t"
        "v_mfma_f32_16x16x32_bf16 v[124:127], v[156:159], v[220:223], v[124:127]\n\t"
        "ds_read_b64_tr_b16 v[154:155], v236 offset:5120\n\t"
        "s_mov_b32 s44, s45\n\t"
        "s_cmp_eq_u32 s47, 0\n\t"
        "s_cbranch_scc1 oseg0_%=\n\t"
        "s_cmp_eq_u32 s47, 1\n\t"
        "s_cbranch_scc1 oseg1_%=\n\t"
        "s_cmp_eq_u32 s47, 2\n\t"
        "s_cbranch_scc1 oseg2_%=\n\t"
        "s_cmp_eq_u32 s47, 3\n\t"
        "s_cbranch_scc1 oseg3_%=\n\t"
        "s_cmp_lg_u32 s46, 15\n\t"
        "s_cbranch_scc1 ostart_%=\n\t"
        "s_branch oend_%=\n\t"
        "oend_%=:\n\t"
        "ds_read_b64 v[128:129], v240 offset:0\n\t"
        "ds_read_b64 v[130:131], v240 offset:16\n\t"
        "ds_read_b64 v[132:133], v240 offset:32\n\t"
        "ds_read_b64 v[134:135], v240 offset:48\n\t"
        "ds_read_b64 v[136:137], v240 offset:64\n\t"
        "ds_read_b64 v[138:139], v240 offset:80\n\t"
        "ds_read_b64 v[140:141], v240 offset:96\n\t"
        "ds_read_b64 v[142:143], v240 offset:112\n\t"
        "s_waitcnt lgkmcnt(0)\n\t"
        "s_barrier\n\t"
        : "+{v[0:15]}"(acc[0]), "+{v[16:31]}"(acc[1]), "={v[32:47]}"(acc[2]), "={v[48:63]}"(acc[3]), "={v[64:79]}"(acc[4]), "={v[80:95]}"(acc[5]),
          "={v[96:111]}"(acc[6]), "={v[112:127]}"(acc[7]), "={v[128:143]}"(wrc),
          "={v[160:175]}"(fl0), "={v[176:191]}"(fl1), "={v[192:207]}"(fl2) STAMP_OUTS
        : "{v224}"(OFF_OF + L * (5 * MBY) + tile * TS + tblk * 1024 + (hi * 32 + (lane & 15)) * 16), "{v[226:233]}"(wa),
          "{v238}"(lbase + L_W + 20 + 8 * L), "{v240}"(lbase + L_WR + hi * 8), "{s49}"((tn >> 2) + 1), "{s[40:43]}"(srd)
        : "v144", "v145", "v146", "v147", "v148", "v149", "v150", "v151", "v152", "v153", "v154", "v155", "v156", "v157", "v158", "v159", "v208", "v209", "v210", "v211", "v212", "v213", "v214", "v215", "v216", "v217", "v218", "v219", "v220", "v221", "v222", "v223", "v225", "v234", "v235", "v236", "v237", "v239", "s44", "s45", "s46", "s47", "s48", "s51", "vcc", "scc", "memory");
      }
      PHASE_VARS;
      STAMP_PUT(12 + 16 * L);
      STAMPV(15 + 16 * L);
      float s = 0.f, sq = 0.f, d = 0.f;
      const unsigned xdst = OFF_X1 + tile * TS + tn * 4096 + lane * 16;
#pragma unroll
      for (int i = 0; i < 4; ++i) {
#pragma unroll
        for (int e = 0; e < 4; ++e) {
          const int j = i >> 1, k = 8 * (i & 1) + 2 * e;
          x1p[i][e] = pack2((acc[j][k] + acc[2 + j][k]) + (acc[4 + j][k] + acc[6 + j][k]), (acc[j][k + 1] + acc[2 + j][k + 1]) + (acc[4 + j][k + 1] + acc[6 + j][k + 1]));
        }
        if (i & 1) {
          row_swap(x1p[i - 1], x1p[i]);
          if (L == 0) { stg(rs, xdst + (i - 1) * 1024, x1p[i - 1]); stg(rs, xdst + i * 1024, x1p[i]); }
          SBAR();
        }
      }
#pragma unroll
      for (int g = 0; g < 8; ++g)
#pragma unroll
        for (int e = 0; e < 2; ++e) {
          const unsigned o = x1p[g >> 1][2 * (g & 1) + e];
          s = dot2(o, 0x3f803f80u, s); sq = dot2(o, o, sq); d = dot2(o, wrc[2 * g + e], d);
        }
      const u32x4 pst = {nzf(half_swap_sum(s)), nzf(half_swap_sum(sq)), nzf(half_swap_sum(d)), 0u};
      asm volatile("buffer_store_dwordx4 %0, %1, %2, 0 offen sc1\n\ts_waitcnt vmcnt(1)\n\tds_write_b32 %3, %4"
                   :: "v"(pst), "v"(stats_dst(sx, tile, tn, lane)), "s"(srd), "v"(lbase + L_W + 64 + 4 * wid), "v"(3),
                      "{v[160:175]}"(fl0), "{v[176:191]}"(fl1), "{v[192:207]}"(fl2) : "memory");
    }
    STAMPV(16 + 16 * L);
  }
  {
    PHASE_VARS;
    float S, Q, Dd, mean, var, mb1, rd1;
    __builtin_amdgcn_s_sleep(13);
    row_stats_sum(rs, OFF_SX + SX_LAYER, tile, lane, S, Q, Dd);
    STAMPV(33);
    row_stats_fin(rs, OFF_SX + SX_LAYER + SX_CAND + (tn * 16 + tm) * 128 + wid * 4, smem, 2, tile, lane, S, Q, Dd, U2F(words[0]), x1p, mean, var, mb1, rd1);
    STAMPV(34);
    u32x4 xo[4];
#pragma unroll
    for (int i = 0; i < 4; ++i) {
#pragma unroll
      for (int e = 0; e < 4; ++e) {
        const unsigned h2 = ln_pair(x1p[i][e], mb1, rd1);
        xo[i][e] = pack2(lo16(x1p[i][e]) + lo16(h2), hi16(x1p[i][e]) + hi16(h2));
      }
      *reinterpret_cast<u32x4*>(smem + wid * 4096 + r32 * 128 + ((2 * i + hi) ^ (r32 & 7)) * 16) = fo_swap(xo[i]);
    }
    asm volatile("s_waitcnt lgkmcnt(0)" ::: "memory");
#pragma unroll
    for (int i = 0; i < 4; ++i) { xo[i] = *reinterpret_cast<const u32x4*>(smem + wid * 4096 + i * 1024 + (lane >> 3) * 128 + ((lane ^ (lane >> 3)) & 7) * 16); PIN(xo[i]); }
    u16* dst = out + (size_t)(tile * 32 + (lane >> 3)) * D_ + tn * 64 + (lane & 7) * 8;
    lds_wait_ge(lbase + L_W + 4 + 4 * (lane & 1), 2);
    const int rel = (int)words[3] - tile * 32 - (lane >> 3);
#pragma unroll
    for (int i = 0; i < 4; ++i)
      if (rel != 8 * i) st16(dst + (size_t)i * 8 * D_, xo[i]);
    STAMPV(35);
    STAMP_END;
  }
}

extern "C" __global__ void end_marker() {}

extern "C" void kernel_launch(void* const* d_in, const int* in_sizes, int n_in, void* d_out, int out_size,
                              void* d_ws, size_t ws_size, hipStream_t stream) {
  char* ws = (char*)d_ws;
  mega_kernel<<<256, 512, 0, stream>>>((const u16*)d_in[0], (const u16*)d_in[1], (const u16*)d_in[2], (const u16*)d_in[4], (const u16*)d_in[5],
                                       (const u16*)d_in[6], (const u16*)d_in[7], ws, (u16*)d_out);
}
```
